```python
import torch

def _softmax(x, axis=-1):
    return torch.softmax(x, dim=axis)

def _gelu(x):
    return torch.nn.functional.gelu(x, approximate='tanh')

def _silu(x):
    return torch.nn.functional.silu(x)

def _sigmoid(x):
    return torch.sigmoid(x)

def _layernorm(x, axis=-1, eps=1e-05):
    m = x.mean(dim=axis, keepdim=True)
    v = x.var(dim=axis, keepdim=True, unbiased=False)
    return (x - m) / torch.sqrt(v + eps)

def _scatter_add(tgt, idx, src, axis=0):
    return tgt.clone().index_add_(axis, idx, src)

def _segment_sum(src, seg, n):
    out = torch.zeros((n,) + src.shape[1:], dtype=src.dtype)
    return out.index_add_(0, seg, src)

def _pad(x, pw):
    flat = tuple((v for pair in reversed(pw) for v in pair))
    return torch.nn.functional.pad(x, flat)
kernel_name = 'interleave_layers__memory_aug_recurrent_mem_tr__glu_gated_swiglu__prompt'

def setup_inputs():

    _w = lambda a: (lambda t: t.to(torch.bfloat16) if t.is_floating_point() else t)(torch.as_tensor(a))

    def _A_setup():
        B, S, Dm = (1, 2048, 576)
        H, Dh = (32, 128)
        M, seg = (8, 32)
        seg = min(seg, S)
        S = S // seg * seg
        return {'x': _w((lambda *s: torch.randn(*s, dtype=torch.bfloat16) * 0.05)(B, S, Dm)), 'mem0': _w((lambda *s: torch.randn(*s, dtype=torch.bfloat16) * 0.2)(B, M, Dm)), 'Wq': _w((lambda *s: torch.randn(*s, dtype=torch.bfloat16) * 1.6)(Dm, H, Dh) * Dm ** (-0.5)), 'Wk': _w((lambda *s: torch.randn(*s, dtype=torch.bfloat16) * 1.6)(Dm, H, Dh) * Dm ** (-0.5)), 'Wv': _w((lambda *s: torch.randn(*s, dtype=torch.bfloat16) * 0.2)(Dm, H, Dh) * Dm ** (-0.5)), 'Wo': _w((lambda *s: torch.randn(*s, dtype=torch.bfloat16) * 0.2)(H, Dh, Dm) * (H * Dh) ** (-0.5)), 'seg_len': seg}
    _A_in = _A_setup()

    def _B_setup():
        B, S, D, F = (1, 2048, 576, 4608)
        return {'x': _w((lambda *s: torch.randn(*s, dtype=torch.bfloat16) * 0.05)(B, S, D)), 'w1': _w((lambda *s: torch.randn(*s, dtype=torch.bfloat16) * 0.2)(D, F) * (2.0 / D) ** 0.5), 'w3': _w((lambda *s: torch.randn(*s, dtype=torch.bfloat16) * 0.2)(D, F) * (2.0 / D) ** 0.5), 'w2': _w((lambda *s: torch.randn(*s, dtype=torch.bfloat16) * 0.2)(F, D) * (2.0 / F) ** 0.5)}
    _B_in = _B_setup()
    return {'A_x': _A_in['x'], 'A_mem0': _A_in['mem0'], 'A_Wq': _A_in['Wq'], 'A_Wk': _A_in['Wk'], 'A_Wv': _A_in['Wv'], 'A_Wo': _A_in['Wo'], 'A_seg_len': _A_in['seg_len'], 'B_x': _B_in['x'], 'B_w1': _B_in['w1'], 'B_w3': _B_in['w3'], 'B_w2': _B_in['w2']}

def reference(A_x, A_mem0, A_Wq, A_Wk, A_Wv, A_Wo, A_seg_len, B_x, B_w1, B_w3, B_w2):

    def _A_ref(x, mem0, Wq, Wk, Wv, Wo, seg_len):
        B, S, Dm = x.shape
        H, Dh = (Wq.shape[1], Wq.shape[2])
        M = mem0.shape[1]
        scale = Dh ** (-0.5)
        n_seg = S // seg_len
        L = M + seg_len
        idx = torch.arange(L)
        mask = (idx[:, None] < M) | (idx[:, None] >= idx[None, :])
        mem = mem0
        outs = []
        for s in range(n_seg):
            xs = x[:, s * seg_len:(s + 1) * seg_len, :].clone()
            h = torch.concatenate([mem, xs], axis=1)
            q = torch.einsum('btd,dhe->bhte', h, Wq)
            k = torch.einsum('btd,dhe->bhte', h, Wk)
            v = torch.einsum('btd,dhe->bhte', h, Wv)
            scores = torch.einsum('bhqe,bhke->bhqk', q, k) * scale
            scores = torch.where(mask[None, None], scores, -1000000000.0)
            p = _softmax(scores, axis=-1)
            o = torch.einsum('bhqk,bhke->bhqe', p, v)
            o = torch.einsum('bhte,hed->btd', o, Wo)
            mem = o[:, :M, :].clone()
            outs.append(o[:, M:, :])
        return torch.concatenate(outs, axis=1)

    def _B_ref(x, w1, w3, w2):
        gate = torch.einsum('bsd,df->bsf', x, w1)
        up = torch.einsum('bsd,df->bsf', x, w3)
        h = _silu(gate) * up
        out = torch.einsum('bsf,fd->bsd', h, w2)
        return out
    x = A_x
    x = x + _A_ref(_layernorm(x, axis=-1), A_mem0, A_Wq, A_Wk, A_Wv, A_Wo, A_seg_len)
    x = x + _B_ref(_layernorm(x, axis=-1), B_w1, B_w3, B_w2)
    x = x + _A_ref(_layernorm(x, axis=-1), A_mem0, A_Wq, A_Wk, A_Wv, A_Wo, A_seg_len)
    x = x + _B_ref(_layernorm(x, axis=-1), B_w1, B_w3, B_w2)
    return x
```

```cpp
#include <hip/hip_runtime.h>
#include <stdint.h>

typedef unsigned short u16;
typedef unsigned long long u64;
typedef unsigned v4u __attribute__((ext_vector_type(4)));
typedef unsigned v2u __attribute__((ext_vector_type(2)));
typedef short bf16x8 __attribute__((ext_vector_type(8)));
typedef float f32x4 __attribute__((ext_vector_type(4)));
typedef __bf16 bf2_t __attribute__((ext_vector_type(2)));
typedef __amdgpu_buffer_rsrc_t rsrc_t;

#define XF2L 0
#define XF2S 0
#define XF2M 24
#define XFN 1
#define RSLEEP 1
namespace {

constexpr unsigned A_K = 262144u;
constexpr unsigned A_KM = 786432u;
constexpr unsigned A_PROBE = 1044480u;
constexpr unsigned A_Q = 0u;
constexpr unsigned A_ST = 1036288u;
constexpr unsigned A_V = 524288u;
constexpr unsigned A_X1 = 999424u;
constexpr unsigned A_XN = 917504u;
constexpr unsigned A_XR = 958464u;
constexpr unsigned B_H = 0u;
constexpr unsigned B_P3 = 294912u;
constexpr unsigned B_XN1 = 442368u;
constexpr unsigned C_P5 = 0u;
constexpr unsigned DBG = 236978176u;
constexpr unsigned DBG2 = 238026752u;
constexpr unsigned EXB = 294912u;
constexpr unsigned EXL = 245760u;
constexpr unsigned FLAG_BYTES = 491520u;
constexpr unsigned F_AT = 640u;
constexpr unsigned F_CH = 512u;
constexpr unsigned F_F1 = 832u;
constexpr unsigned F_F2 = 1472u;
constexpr unsigned F_LINES = 1728u;
constexpr unsigned F_OP = 1216u;
constexpr unsigned F_R3 = 128u;
constexpr unsigned F_S1 = 256u;
constexpr unsigned F_XN = 0u;
constexpr unsigned G_ABORT = 320u;
constexpr unsigned G_CI = 128u;
constexpr unsigned G_LINES = 384u;
constexpr unsigned G_PAIR = 0u;
constexpr unsigned G_PG = 192u;
constexpr unsigned K1 = 1024u;
constexpr unsigned LF0 = 24576u;
constexpr unsigned MEMB = 40960u;
constexpr unsigned MEML = 835584u;
constexpr unsigned NPAGES = 113u;
constexpr unsigned OOB = 2147483648u;
constexpr unsigned PAGE = 2097152u;
constexpr unsigned SPIN_CAP = 1048576u;
constexpr unsigned WS_USED = 236978176u;
constexpr unsigned CH_WB0 = 0u;
constexpr unsigned CH_WB1 = 40960u;
constexpr unsigned CH_WB2 = 81920u;
constexpr unsigned CH_WB3 = 116736u;
constexpr unsigned CH_IMG = 151552u;
constexpr unsigned CH_SO = 156672u;
constexpr unsigned CH_SELT = 160768u;
constexpr unsigned NPROBE = 6u;
constexpr unsigned DL0 = 407162880u;
constexpr unsigned DL1 = 170434560u;
constexpr unsigned D1 = 673447980u;
constexpr unsigned D2 = 1010302976u;
constexpr unsigned D3 = 0u;
constexpr unsigned D4 = 1579038u;
constexpr int LDS_BYTES = 160 * 1024;
constexpr unsigned spread(unsigned o) { return (o >> 13) * 16384u + (o & 8191u); }

struct Params {
    const u16* x; const u16* mem0; const u16* Wq; const u16* Wk; const u16* Wv; const u16* Wo;
    const u16* w1; const u16* w3; const u16* w2; u16* out; char* ws;
    u64 tune0, tune1, tune2;
};

__device__ __forceinline__ v4u ld16(rsrc_t rs, unsigned voff, unsigned soff) { return __builtin_amdgcn_raw_buffer_load_b128(rs, voff, soff, 16); }
__device__ __forceinline__ int ld4(rsrc_t rs, unsigned off) { return (int)__builtin_amdgcn_raw_buffer_load_b32(rs, off, 0, 16); }
__device__ __forceinline__ void st16(rsrc_t rs, unsigned voff, unsigned soff, v4u v) { __builtin_amdgcn_raw_buffer_store_b128(v, rs, voff, soff, 16); }
__device__ __forceinline__ void st4(rsrc_t rs, unsigned off, int v) { __builtin_amdgcn_raw_buffer_store_b32((unsigned)v, rs, off, 0, 16); }
#define DRAIN() asm volatile("s_waitcnt vmcnt(0)" ::: "memory")
#define BAR() do { asm volatile("s_waitcnt lgkmcnt(0)" ::: "memory"); __builtin_amdgcn_s_barrier(); asm volatile("" ::: "memory"); } while (0)
__device__ __forceinline__ int rfl(int v) { return __builtin_amdgcn_readfirstlane(v); }
__device__ __forceinline__ u64 pair(unsigned lo, unsigned hi) { return (u64)lo | (u64)hi << 32; }
__device__ __forceinline__ int my_half() {
    unsigned id;
    asm volatile("s_getreg_b32 %0, hwreg(HW_REG_XCC_ID)" : "=s"(id));
    return (id >> 2) & 1;
}

__device__ __forceinline__ void startup(rsrc_t rs, int hw) {
    const int lane = threadIdx.x & 63;
    const v4u ones = {~0u, ~0u, ~0u, ~0u};
    const unsigned o = (unsigned)(hw < 96 ? hw + 1 : (hw == 96 ? 0 : hw)) * PAGE + spread(A_PROBE);
    unsigned best[2] = {~0u, ~0u};
    if (hw < NPAGES) {
#pragma unroll 1
        for (int r = 0; r < 2 * NPROBE; ++r) {
            const unsigned t0 = (unsigned)__builtin_amdgcn_s_memtime();
            st16(rs, lane < 8 ? lane * 16 : OOB, o + (r & 1) * 8192, ones);
            DRAIN();
            const unsigned t = (unsigned)__builtin_amdgcn_s_memtime() - t0;
            if (r & 1) best[1] = min(best[1], t); else best[0] = min(best[0], t);
        }
    }
#pragma unroll
    for (int q = 0; q < 12; ++q) st16(rs, lane * 16, 2 * EXL + (hw * 12 + q) * 1024, ones);
    static_assert(128 * 12 * 1024 >= 2 * (2 * EXB + 4 * MEMB) && 2 * EXL + 128 * 12 * 1024 <= 2 * A_PROBE, "exchange area");
#pragma unroll
    for (int q = 0; q < 4; ++q) st16(rs, lane * 16, (unsigned)(1 + (hw >> 1)) * PAGE + spread(A_ST + q * 1024) + (hw & 1) * 8192, ones);
    DRAIN();
    if (lane == 0) st4(rs, (G_PG + hw) * 128, (best[1] < best[0] ? 1 : 0) ^ my_half());
}

__device__ __forceinline__ void warm(const u16* W, unsigned bytes, unsigned first) {
    const rsrc_t r = __builtin_amdgcn_make_buffer_rsrc(const_cast<u16*>(W), 0, bytes, 0x00020000);
    const unsigned vo = (threadIdx.x & 63) * 16;
#pragma unroll 1
    for (unsigned o = first; o < bytes; o += 448 * 1024)
        asm volatile("s_mov_b32 m0, 0\n\ts_nop 0\n\tbuffer_load_dwordx4 %0, %1, %2 offen lds\n\ts_waitcnt vmcnt(40)" :: "v"(vo), "s"(r), "s"(o) : "memory");
}

__device__ __forceinline__ void role_chain(const Params& p, int h, int x4, int pr, char* smem) {
    const unsigned tid = threadIdx.x, lane = tid & 63, c = lane & 15, g = lane >> 4;
    const int w = rfl(tid >> 6);
    const unsigned wb = w == 0 ? CH_WB0 : (w == 1 ? CH_WB1 : (w == 2 ? CH_WB2 : CH_WB3));
    const unsigned slot = (g * 8 + (c & 7)) * 16;
    const unsigned spr = CH_IMG + (c >> 3) * 512 + slot;
    const unsigned gam = lane >> 4, nh = (lane >> 3) & 1, ksl = (lane >> 2) & 1, tau = ((lane ^ gam) >> 1) & 1, rho = lane & 1;
    auto img = [&](unsigned r) -> unsigned { return (2 * (r & 3) + (g & 1)) * 128 + (((g ^ r) & 2) + (r >> 2)) * 16; };
    if (w >= 2) {
        v4u sel;
#pragma unroll
        for (int e = 0; e < 4; ++e) sel[e] = ((c & 7) == 2 * e) ? 0x3f80u : (((c & 7) == 2 * e + 1) ? 0x3f800000u : 0u);
        const unsigned g3 = ksl * 512 + (2 * (gam & 1) + nh) * 128 + (4 * (gam >> 1) + 2 * tau + rho) * 16;
        *reinterpret_cast<v4u*>(smem + wb + 32768 + lane * 16) = sel;
        *reinterpret_cast<v4u*>(smem + wb + 32768 + 1024 + lane * 16) =
            v4u{lane < 32 ? (lane >> 3) * 1024 + (lane & 7) * 16 : OOB, g3, lane < 32 ? (lane >> 3) * 16384 + ((lane & 7) ^ ((h >> 1) & 2)) * 16 : OOB, 0u};
    } else {
        const bool on = w == 0 ? g == (c & 3) : (g == (c >> 2) && (c & 3) < 2);
        const bool up = w == 0 ? (c & 4) != 0 : (c & 1) != 0;
        *reinterpret_cast<v4u*>(smem + spr + CH_SELT - CH_IMG + w * 1024) = v4u{on ? (up ? 0x3f800000u : 0x3f80u) : 0u, 0u, 0u, on ? (up ? 0x3f80u : 0x3f800000u) : 0u};
    }
    if (w == 0) *reinterpret_cast<unsigned*>(smem + spr + CH_SELT - CH_IMG + 2048) = ~0u;
    BAR();
    const unsigned idx = (lane & 2) ? 0xeeeeu : 0x4444u;
    const unsigned va = CH_IMG + img((c >> 2) + 4 * ((((c & 3) + 1) >> 1) & 1));
    const unsigned pub = (lane >> 4) * 16384 + ((lane >> 3) & 1) * 4096 + (lane & 7) * 16;
    const unsigned kmo = (c & 3) < 2 ? (g * 8 + 2 * (c >> 2) + (c & 3)) * 16 : OOB;
    const unsigned vmo = (g & 1) ? OOB : (g >> 1) * 256 + c * 16;
    const unsigned pp = lane >> 2, row = (pp & 9) | ((pp & 2) << 1) | ((pp & 4) >> 1);
    const unsigned qo = row * 8192 + (128 * h + 32 * w) * 2 + (lane & 3) * 16;
    const unsigned oo = (128 * h + 16 * ((row >> 2) & 1) + 4 * (row >> 3) + (row & 3)) * 1152 + w * 128 + (lane & 3) * 16;
    const unsigned q = c >> 2;
    const unsigned tr = wb + ((q & 1) + 2 * (g & 1) + 8 * (g >> 1)) * 64 + (q >> 1) * 32 + (lane & 3) * 8;
    const unsigned m0 = (gam + 4 * rho) * 1152 + (4 * ksl + 2 * tau + nh) * 16;
    asm volatile(
        "s_nop 4\n"
        "s_mov_b64 s[40:41], %0\n"
        "s_and_b32 s41, s41, 0xffff\n"
        "s_mov_b32 s42, 236978176\n"
        "s_mov_b32 s43, 0x20000\n"
        "s_mov_b64 s[48:49], %6\n"
        "s_mov_b64 s[56:57], %7\n"
        "s_mov_b64 s[60:61], %8\n"
        "s_mov_b64 s[62:63], %9\n"
        "v_mov_b64 v[0:1], %10\n"
        "v_mov_b64 v[2:3], %11\n"
        "v_mov_b64 v[4:5], %12\n"
        "v_mov_b64 v[6:7], %13\n"
        "v_mov_b64 v[8:9], %14\n"
        "v_mov_b64 v[10:11], %15\n"
        "v_mov_b64 v[12:13], %16\n"
        "v_mov_b64 v[14:15], %17\n"
        "s_mov_b32 s47, 0x20000\n"
        "v_readfirstlane_b32 s82, v2\n"
        "s_lshr_b32 s65, s56, 8\n"
        "s_and_b32 s56, s56, 1\n"
        ".Lch_start_%=:\n"
        "s_sub_u32 s65, s65, 1\n"
        "s_cbranch_scc1 .Lch_start2_%=\n"
        "s_sleep 16\n"
        "s_branch .Lch_start_%=\n"
        ".Lch_start2_%=:\n"
        "s_mov_b64 s[44:45], %5\n"
        "s_and_b32 s45, s45, 0xffff\n"
        "s_mov_b32 s46, 9216\n"
        "s_lshl_b32 s65, s48, 10\n"
        "s_add_u32 m0, s65, 151552\n"
        "s_lshl_b32 s65, s48, 7\n"
        "s_add_u32 s65, s65, 0\n"
        "buffer_load_dwordx4 v15, s[44:47], s65 offen lds\n"
        "s_lshl_b32 s65, s48, 10\n"
        "s_add_u32 m0, s65, 155648\n"
        "s_lshl_b32 s65, s48, 7\n"
        "s_add_u32 s65, s65, 512\n"
        "buffer_load_dwordx4 v15, s[44:47], s65 offen lds\n"
        "s_mov_b32 m0, 159744\n"
        "s_mov_b32 s65, 1024\n"
        "buffer_load_dwordx4 v15, s[44:47], s65 offen lds\n"
        "s_mov_b64 s[44:45], %1\n"
        "s_and_b32 s45, s45, 0xffff\n"
        "s_mov_b32 s46, 4718592\n"
        "s_add_u32 m0, s82, 0\n"
        "s_mov_b32 s65, 0\n"
        "buffer_load_dwordx4 v12, s[44:47], s65 offen lds\n"
        "s_mov_b32 s65, 130048\n"
        "buffer_load_dwordx4 v12, s[44:47], s65 offen offset:1024 lds\n"
        "s_add_u32 m0, s82, 2048\n"
        "s_mov_b32 s65, 262144\n"
        "buffer_load_dwordx4 v12, s[44:47], s65 offen lds\n"
        "s_mov_b32 s65, 392192\n"
        "buffer_load_dwordx4 v12, s[44:47], s65 offen offset:1024 lds\n"
        "s_add_u32 m0, s82, 4096\n"
        "s_mov_b32 s65, 524288\n"
        "buffer_load_dwordx4 v12, s[44:47], s65 offen lds\n"
        "s_mov_b32 s65, 654336\n"
        "buffer_load_dwordx4 v12, s[44:47], s65 offen offset:1024 lds\n"
        "s_add_u32 m0, s82, 6144\n"
        "s_mov_b32 s65, 786432\n"
        "buffer_load_dwordx4 v12, s[44:47], s65 offen lds\n"
        "s_mov_b32 s65, 916480\n"
        "buffer_load_dwordx4 v12, s[44:47], s65 offen offset:1024 lds\n"
        "s_add_u32 m0, s82, 8192\n"
        "s_mov_b32 s65, 1048576\n"
        "buffer_load_dwordx4 v12, s[44:47], s65 offen lds\n"
        "s_mov_b32 s65, 1178624\n"
        "buffer_load_dwordx4 v12, s[44:47], s65 offen offset:1024 lds\n"
        "s_add_u32 m0, s82, 10240\n"
        "s_mov_b32 s65, 1310720\n"
        "buffer_load_dwordx4 v12, s[44:47], s65 offen lds\n"
        "s_mov_b32 s65, 1440768\n"
        "buffer_load_dwordx4 v12, s[44:47], s65 offen offset:1024 lds\n"
        "s_add_u32 m0, s82, 12288\n"
        "s_mov_b32 s65, 1572864\n"
        "buffer_load_dwordx4 v12, s[44:47], s65 offen lds\n"
        "s_mov_b32 s65, 1702912\n"
        "buffer_load_dwordx4 v12, s[44:47], s65 offen offset:1024 lds\n"
        "s_add_u32 m0, s82, 14336\n"
        "s_mov_b32 s65, 1835008\n"
        "buffer_load_dwordx4 v12, s[44:47], s65 offen lds\n"
        "s_mov_b32 s65, 1965056\n"
        "buffer_load_dwordx4 v12, s[44:47], s65 offen offset:1024 lds\n"
        "s_add_u32 m0, s82, 16384\n"
        "s_mov_b32 s65, 2097152\n"
        "buffer_load_dwordx4 v12, s[44:47], s65 offen lds\n"
        "s_mov_b32 s65, 2227200\n"
        "buffer_load_dwordx4 v12, s[44:47], s65 offen offset:1024 lds\n"
        "s_add_u32 m0, s82, 18432\n"
        "s_mov_b32 s65, 2359296\n"
        "buffer_load_dwordx4 v12, s[44:47], s65 offen lds\n"
        "s_mov_b32 s65, 2489344\n"
        "buffer_load_dwordx4 v12, s[44:47], s65 offen offset:1024 lds\n"
        "s_add_u32 m0, s82, 20480\n"
        "s_mov_b32 s65, 2621440\n"
        "buffer_load_dwordx4 v12, s[44:47], s65 offen lds\n"
        "s_mov_b32 s65, 2751488\n"
        "buffer_load_dwordx4 v12, s[44:47], s65 offen offset:1024 lds\n"
        "s_add_u32 m0, s82, 22528\n"
        "s_mov_b32 s65, 2883584\n"
        "buffer_load_dwordx4 v12, s[44:47], s65 offen lds\n"
        "s_mov_b32 s65, 3013632\n"
        "buffer_load_dwordx4 v12, s[44:47], s65 offen offset:1024 lds\n"
        "s_add_u32 m0, s82, 24576\n"
        "s_mov_b32 s65, 3145728\n"
        "buffer_load_dwordx4 v12, s[44:47], s65 offen lds\n"
        "s_mov_b32 s65, 3275776\n"
        "buffer_load_dwordx4 v12, s[44:47], s65 offen offset:1024 lds\n"
        "s_add_u32 m0, s82, 26624\n"
        "s_mov_b32 s65, 3407872\n"
        "buffer_load_dwordx4 v12, s[44:47], s65 offen lds\n"
        "s_mov_b32 s65, 3537920\n"
        "buffer_load_dwordx4 v12, s[44:47], s65 offen offset:1024 lds\n"
        "s_add_u32 m0, s82, 28672\n"
        "s_mov_b32 s65, 3670016\n"
        "buffer_load_dwordx4 v12, s[44:47], s65 offen lds\n"
        "s_mov_b32 s65, 3800064\n"
        "buffer_load_dwordx4 v12, s[44:47], s65 offen offset:1024 lds\n"
        "s_add_u32 m0, s82, 30720\n"
        "s_mov_b32 s65, 3932160\n"
        "buffer_load_dwordx4 v12, s[44:47], s65 offen lds\n"
        "s_mov_b32 s65, 4062208\n"
        "buffer_load_dwordx4 v12, s[44:47], s65 offen offset:1024 lds\n"
        "s_waitcnt vmcnt(30)\n"
        "ds_read_b64_tr_b16 a[0:1], v14 offset:0\n"
        "ds_read_b64_tr_b16 a[2:3], v14 offset:256\n"
        "ds_read_b64_tr_b16 a[4:5], v14 offset:1024\n"
        "ds_read_b64_tr_b16 a[6:7], v14 offset:1280\n"
        "s_waitcnt vmcnt(28)\n"
        "ds_read_b64_tr_b16 a[8:9], v14 offset:2048\n"
        "ds_read_b64_tr_b16 a[10:11], v14 offset:2304\n"
        "ds_read_b64_tr_b16 a[12:13], v14 offset:3072\n"
        "ds_read_b64_tr_b16 a[14:15], v14 offset:3328\n"
        "s_waitcnt lgkmcnt(4)\n"
        "s_add_u32 m0, s82, 0\n"
        "s_mov_b32 s65, 4194304\n"
        "buffer_load_dwordx4 v12, s[44:47], s65 offen lds\n"
        "s_mov_b32 s65, 4324352\n"
        "buffer_load_dwordx4 v12, s[44:47], s65 offen offset:1024 lds\n"
        "s_waitcnt vmcnt(28)\n"
        "ds_read_b64_tr_b16 a[16:17], v14 offset:4096\n"
        "ds_read_b64_tr_b16 a[18:19], v14 offset:4352\n"
        "ds_read_b64_tr_b16 a[20:21], v14 offset:5120\n"
        "ds_read_b64_tr_b16 a[22:23], v14 offset:5376\n"
        "s_waitcnt lgkmcnt(4)\n"
        "s_add_u32 m0, s82, 2048\n"
        "s_mov_b32 s65, 4456448\n"
        "buffer_load_dwordx4 v12, s[44:47], s65 offen lds\n"
        "s_mov_b32 s65, 4586496\n"
        "buffer_load_dwordx4 v12, s[44:47], s65 offen offset:1024 lds\n"
        "s_waitcnt vmcnt(28)\n"
        "ds_read_b64_tr_b16 a[24:25], v14 offset:6144\n"
        "ds_read_b64_tr_b16 a[26:27], v14 offset:6400\n"
        "ds_read_b64_tr_b16 a[28:29], v14 offset:7168\n"
        "ds_read_b64_tr_b16 a[30:31], v14 offset:7424\n"
        "s_waitcnt lgkmcnt(4)\n"
        "s_mov_b64 s[44:45], %2\n"
        "s_and_b32 s45, s45, 0xffff\n"
        "s_mov_b32 s46, 4718592\n"
        "s_add_u32 m0, s82, 4096\n"
        "s_mov_b32 s65, 0\n"
        "buffer_load_dwordx4 v12, s[44:47], s65 offen lds\n"
        "s_mov_b32 s65, 130048\n"
        "buffer_load_dwordx4 v12, s[44:47], s65 offen offset:1024 lds\n"
        "s_waitcnt vmcnt(28)\n"
        "ds_read_b64_tr_b16 a[32:33], v14 offset:8192\n"
        "ds_read_b64_tr_b16 a[34:35], v14 offset:8448\n"
        "ds_read_b64_tr_b16 a[36:37], v14 offset:9216\n"
        "ds_read_b64_tr_b16 a[38:39], v14 offset:9472\n"
        "s_waitcnt lgkmcnt(4)\n"
        "s_add_u32 m0, s82, 6144\n"
        "s_mov_b32 s65, 262144\n"
        "buffer_load_dwordx4 v12, s[44:47], s65 offen lds\n"
        "s_mov_b32 s65, 392192\n"
        "buffer_load_dwordx4 v12, s[44:47], s65 offen offset:1024 lds\n"
        "s_waitcnt vmcnt(28)\n"
        "ds_read_b64_tr_b16 a[40:41], v14 offset:10240\n"
        "ds_read_b64_tr_b16 a[42:43], v14 offset:10496\n"
        "ds_read_b64_tr_b16 a[44:45], v14 offset:11264\n"
        "ds_read_b64_tr_b16 a[46:47], v14 offset:11520\n"
        "s_waitcnt lgkmcnt(4)\n"
        "s_add_u32 m0, s82, 8192\n"
        "s_mov_b32 s65, 524288\n"
        "buffer_load_dwordx4 v12, s[44:47], s65 offen lds\n"
        "s_mov_b32 s65, 654336\n"
        "buffer_load_dwordx4 v12, s[44:47], s65 offen offset:1024 lds\n"
        "s_waitcnt vmcnt(28)\n"
        "ds_read_b64_tr_b16 a[48:49], v14 offset:12288\n"
        "ds_read_b64_tr_b16 a[50:51], v14 offset:12544\n"
        "ds_read_b64_tr_b16 a[52:53], v14 offset:13312\n"
        "ds_read_b64_tr_b16 a[54:55], v14 offset:13568\n"
        "s_waitcnt lgkmcnt(4)\n"
        "s_add_u32 m0, s82, 10240\n"
        "s_mov_b32 s65, 786432\n"
        "buffer_load_dwordx4 v12, s[44:47], s65 offen lds\n"
        "s_mov_b32 s65, 916480\n"
        "buffer_load_dwordx4 v12, s[44:47], s65 offen offset:1024 lds\n"
        "s_waitcnt vmcnt(28)\n"
        "ds_read_b64_tr_b16 a[56:57], v14 offset:14336\n"
        "ds_read_b64_tr_b16 a[58:59], v14 offset:14592\n"
        "ds_read_b64_tr_b16 a[60:61], v14 offset:15360\n"
        "ds_read_b64_tr_b16 a[62:63], v14 offset:15616\n"
        "s_waitcnt lgkmcnt(4)\n"
        "s_add_u32 m0, s82, 12288\n"
        "s_mov_b32 s65, 1048576\n"
        "buffer_load_dwordx4 v12, s[44:47], s65 offen lds\n"
        "s_mov_b32 s65, 1178624\n"
        "buffer_load_dwordx4 v12, s[44:47], s65 offen offset:1024 lds\n"
        "s_waitcnt vmcnt(28)\n"
        "ds_read_b64_tr_b16 a[64:65], v14 offset:16384\n"
        "ds_read_b64_tr_b16 a[66:67], v14 offset:16640\n"
        "ds_read_b64_tr_b16 a[68:69], v14 offset:17408\n"
        "ds_read_b64_tr_b16 a[70:71], v14 offset:17664\n"
        "s_waitcnt lgkmcnt(4)\n"
        "s_add_u32 m0, s82, 14336\n"
        "s_mov_b32 s65, 1310720\n"
        "buffer_load_dwordx4 v12, s[44:47], s65 offen lds\n"
        "s_mov_b32 s65, 1440768\n"
        "buffer_load_dwordx4 v12, s[44:47], s65 offen offset:1024 lds\n"
        "s_waitcnt vmcnt(28)\n"
        "ds_read_b64_tr_b16 a[72:73], v14 offset:18432\n"
        "ds_read_b64_tr_b16 a[74:75], v14 offset:18688\n"
        "ds_read_b64_tr_b16 a[76:77], v14 offset:19456\n"
        "ds_read_b64_tr_b16 a[78:79], v14 offset:19712\n"
        "s_waitcnt lgkmcnt(4)\n"
        "s_add_u32 m0, s82, 16384\n"
        "s_mov_b32 s65, 1572864\n"
        "buffer_load_dwordx4 v12, s[44:47], s65 offen lds\n"
        "s_mov_b32 s65, 1702912\n"
        "buffer_load_dwordx4 v12, s[44:47], s65 offen offset:1024 lds\n"
        "s_waitcnt vmcnt(28)\n"
        "ds_read_b64_tr_b16 a[80:81], v14 offset:20480\n"
        "ds_read_b64_tr_b16 a[82:83], v14 offset:20736\n"
        "ds_read_b64_tr_b16 a[84:85], v14 offset:21504\n"
        "ds_read_b64_tr_b16 a[86:87], v14 offset:21760\n"
        "s_waitcnt lgkmcnt(4)\n"
        "s_add_u32 m0, s82, 18432\n"
        "s_mov_b32 s65, 1835008\n"
        "buffer_load_dwordx4 v12, s[44:47], s65 offen lds\n"
        "s_mov_b32 s65, 1965056\n"
        "buffer_load_dwordx4 v12, s[44:47], s65 offen offset:1024 lds\n"
        "s_waitcnt vmcnt(28)\n"
        "ds_read_b64_tr_b16 a[88:89], v14 offset:22528\n"
        "ds_read_b64_tr_b16 a[90:91], v14 offset:22784\n"
        "ds_read_b64_tr_b16 a[92:93], v14 offset:23552\n"
        "ds_read_b64_tr_b16 a[94:95], v14 offset:23808\n"
        "s_waitcnt lgkmcnt(4)\n"
        "s_add_u32 m0, s82, 20480\n"
        "s_mov_b32 s65, 2097152\n"
        "buffer_load_dwordx4 v12, s[44:47], s65 offen lds\n"
        "s_mov_b32 s65, 2227200\n"
        "buffer_load_dwordx4 v12, s[44:47], s65 offen offset:1024 lds\n"
        "s_waitcnt vmcnt(28)\n"
        "ds_read_b64_tr_b16 a[96:97], v14 offset:24576\n"
        "ds_read_b64_tr_b16 a[98:99], v14 offset:24832\n"
        "ds_read_b64_tr_b16 a[100:101], v14 offset:25600\n"
        "ds_read_b64_tr_b16 a[102:103], v14 offset:25856\n"
        "s_waitcnt lgkmcnt(4)\n"
        "s_add_u32 m0, s82, 22528\n"
        "s_mov_b32 s65, 2359296\n"
        "buffer_load_dwordx4 v12, s[44:47], s65 offen lds\n"
        "s_mov_b32 s65, 2489344\n"
        "buffer_load_dwordx4 v12, s[44:47], s65 offen offset:1024 lds\n"
        "s_waitcnt vmcnt(28)\n"
        "ds_read_b64_tr_b16 a[104:105], v14 offset:26624\n"
        "ds_read_b64_tr_b16 a[106:107], v14 offset:26880\n"
        "ds_read_b64_tr_b16 a[108:109], v14 offset:27648\n"
        "ds_read_b64_tr_b16 a[110:111], v14 offset:27904\n"
        "s_waitcnt lgkmcnt(4)\n"
        "s_add_u32 m0, s82, 24576\n"
        "s_mov_b32 s65, 2621440\n"
        "buffer_load_dwordx4 v12, s[44:47], s65 offen lds\n"
        "s_mov_b32 s65, 2751488\n"
        "buffer_load_dwordx4 v12, s[44:47], s65 offen offset:1024 lds\n"
        "s_waitcnt vmcnt(28)\n"
        "ds_read_b64_tr_b16 a[112:113], v14 offset:28672\n"
        "ds_read_b64_tr_b16 a[114:115], v14 offset:28928\n"
        "ds_read_b64_tr_b16 a[116:117], v14 offset:29696\n"
        "ds_read_b64_tr_b16 a[118:119], v14 offset:29952\n"
        "s_waitcnt lgkmcnt(4)\n"
        "s_add_u32 m0, s82, 26624\n"
        "s_mov_b32 s65, 2883584\n"
        "buffer_load_dwordx4 v12, s[44:47], s65 offen lds\n"
        "s_mov_b32 s65, 3013632\n"
        "buffer_load_dwordx4 v12, s[44:47], s65 offen offset:1024 lds\n"
        "s_waitcnt vmcnt(28)\n"
        "ds_read_b64_tr_b16 a[120:121], v14 offset:30720\n"
        "ds_read_b64_tr_b16 a[122:123], v14 offset:30976\n"
        "ds_read_b64_tr_b16 a[124:125], v14 offset:31744\n"
        "ds_read_b64_tr_b16 a[126:127], v14 offset:32000\n"
        "s_waitcnt lgkmcnt(4)\n"
        "s_add_u32 m0, s82, 28672\n"
        "s_mov_b32 s65, 3145728\n"
        "buffer_load_dwordx4 v12, s[44:47], s65 offen lds\n"
        "s_mov_b32 s65, 3275776\n"
        "buffer_load_dwordx4 v12, s[44:47], s65 offen offset:1024 lds\n"
        "s_waitcnt vmcnt(28)\n"
        "ds_read_b64_tr_b16 a[128:129], v14 offset:0\n"
        "ds_read_b64_tr_b16 a[130:131], v14 offset:256\n"
        "ds_read_b64_tr_b16 a[132:133], v14 offset:1024\n"
        "ds_read_b64_tr_b16 a[134:135], v14 offset:1280\n"
        "s_waitcnt lgkmcnt(4)\n"
        "s_add_u32 m0, s82, 30720\n"
        "s_mov_b32 s65, 3407872\n"
        "buffer_load_dwordx4 v12, s[44:47], s65 offen lds\n"
        "s_mov_b32 s65, 3537920\n"
        "buffer_load_dwordx4 v12, s[44:47], s65 offen offset:1024 lds\n"
        "s_waitcnt vmcnt(28)\n"
        "ds_read_b64_tr_b16 a[136:137], v14 offset:2048\n"
        "ds_read_b64_tr_b16 a[138:139], v14 offset:2304\n"
        "ds_read_b64_tr_b16 a[140:141], v14 offset:3072\n"
        "ds_read_b64_tr_b16 a[142:143], v14 offset:3328\n"
        "s_waitcnt lgkmcnt(4)\n"
        "s_add_u32 m0, s82, 0\n"
        "s_mov_b32 s65, 3670016\n"
        "buffer_load_dwordx4 v12, s[44:47], s65 offen lds\n"
        "s_mov_b32 s65, 3800064\n"
        "buffer_load_dwordx4 v12, s[44:47], s65 offen offset:1024 lds\n"
        "s_waitcnt vmcnt(28)\n"
        "ds_read_b64_tr_b16 a[144:145], v14 offset:4096\n"
        "ds_read_b64_tr_b16 a[146:147], v14 offset:4352\n"
        "ds_read_b64_tr_b16 a[148:149], v14 offset:5120\n"
        "ds_read_b64_tr_b16 a[150:151], v14 offset:5376\n"
        "s_waitcnt lgkmcnt(4)\n"
        "s_add_u32 m0, s82, 2048\n"
        "s_mov_b32 s65, 3932160\n"
        "buffer_load_dwordx4 v12, s[44:47], s65 offen lds\n"
        "s_mov_b32 s65, 4062208\n"
        "buffer_load_dwordx4 v12, s[44:47], s65 offen offset:1024 lds\n"
        "s_waitcnt vmcnt(28)\n"
        "ds_read_b64_tr_b16 a[152:153], v14 offset:6144\n"
        "ds_read_b64_tr_b16 a[154:155], v14 offset:6400\n"
        "ds_read_b64_tr_b16 a[156:157], v14 offset:7168\n"
        "ds_read_b64_tr_b16 a[158:159], v14 offset:7424\n"
        "s_waitcnt lgkmcnt(4)\n"
        "s_add_u32 m0, s82, 4096\n"
        "s_mov_b32 s65, 4194304\n"
        "buffer_load_dwordx4 v12, s[44:47], s65 offen lds\n"
        "s_mov_b32 s65, 4324352\n"
        "buffer_load_dwordx4 v12, s[44:47], s65 offen offset:1024 lds\n"
        "s_waitcnt vmcnt(28)\n"
        "ds_read_b64_tr_b16 a[160:161], v14 offset:8192\n"
        "ds_read_b64_tr_b16 a[162:163], v14 offset:8448\n"
        "ds_read_b64_tr_b16 a[164:165], v14 offset:9216\n"
        "ds_read_b64_tr_b16 a[166:167], v14 offset:9472\n"
        "s_waitcnt lgkmcnt(4)\n"
        "s_add_u32 m0, s82, 6144\n"
        "s_mov_b32 s65, 4456448\n"
        "buffer_load_dwordx4 v12, s[44:47], s65 offen lds\n"
        "s_mov_b32 s65, 4586496\n"
        "buffer_load_dwordx4 v12, s[44:47], s65 offen offset:1024 lds\n"
        "s_waitcnt vmcnt(28)\n"
        "ds_read_b64_tr_b16 a[168:169], v14 offset:10240\n"
        "ds_read_b64_tr_b16 a[170:171], v14 offset:10496\n"
        "ds_read_b64_tr_b16 a[172:173], v14 offset:11264\n"
        "ds_read_b64_tr_b16 a[174:175], v14 offset:11520\n"
        "s_waitcnt lgkmcnt(4)\n"
        "s_mov_b64 s[44:45], %4\n"
        "s_and_b32 s45, s45, 0xffff\n"
        "s_mov_b32 s46, 4718592\n"
        "s_add_u32 m0, s82, 8192\n"
        "s_mov_b32 s65, 0\n"
        "buffer_load_dwordx4 v13, s[44:47], s65 offen lds\n"
        "s_mov_b32 s65, 8192\n"
        "buffer_load_dwordx4 v13, s[44:47], s65 offen offset:1024 lds\n"
        "s_waitcnt vmcnt(28)\n"
        "ds_read_b64_tr_b16 a[176:177], v14 offset:12288\n"
        "ds_read_b64_tr_b16 a[178:179], v14 offset:12544\n"
        "ds_read_b64_tr_b16 a[180:181], v14 offset:13312\n"
        "ds_read_b64_tr_b16 a[182:183], v14 offset:13568\n"
        "s_waitcnt lgkmcnt(4)\n"
        "s_add_u32 m0, s82, 10240\n"
        "s_mov_b32 s65, 36864\n"
        "buffer_load_dwordx4 v13, s[44:47], s65 offen lds\n"
        "s_mov_b32 s65, 45056\n"
        "buffer_load_dwordx4 v13, s[44:47], s65 offen offset:1024 lds\n"
        "s_waitcnt vmcnt(28)\n"
        "ds_read_b64_tr_b16 a[184:185], v14 offset:14336\n"
        "ds_read_b64_tr_b16 a[186:187], v14 offset:14592\n"
        "ds_read_b64_tr_b16 a[188:189], v14 offset:15360\n"
        "ds_read_b64_tr_b16 a[190:191], v14 offset:15616\n"
        "s_waitcnt lgkmcnt(4)\n"
        "s_add_u32 m0, s82, 12288\n"
        "s_mov_b32 s65, 73728\n"
        "buffer_load_dwordx4 v13, s[44:47], s65 offen lds\n"
        "s_mov_b32 s65, 81920\n"
        "buffer_load_dwordx4 v13, s[44:47], s65 offen offset:1024 lds\n"
        "s_waitcnt vmcnt(28)\n"
        "ds_read_b64_tr_b16 a[192:193], v14 offset:16384\n"
        "ds_read_b64_tr_b16 a[194:195], v14 offset:16640\n"
        "ds_read_b64_tr_b16 a[196:197], v14 offset:17408\n"
        "ds_read_b64_tr_b16 a[198:199], v14 offset:17664\n"
        "s_waitcnt lgkmcnt(4)\n"
        "s_add_u32 m0, s82, 14336\n"
        "s_mov_b32 s65, 110592\n"
        "buffer_load_dwordx4 v13, s[44:47], s65 offen lds\n"
        "s_mov_b32 s65, 118784\n"
        "buffer_load_dwordx4 v13, s[44:47], s65 offen offset:1024 lds\n"
        "s_waitcnt vmcnt(28)\n"
        "ds_read_b64_tr_b16 a[200:201], v14 offset:18432\n"
        "ds_read_b64_tr_b16 a[202:203], v14 offset:18688\n"
        "ds_read_b64_tr_b16 a[204:205], v14 offset:19456\n"
        "ds_read_b64_tr_b16 a[206:207], v14 offset:19712\n"
        "s_waitcnt lgkmcnt(4)\n"
        "s_add_u32 m0, s82, 16384\n"
        "s_mov_b32 s65, 64\n"
        "buffer_load_dwordx4 v13, s[44:47], s65 offen lds\n"
        "s_mov_b32 s65, 8256\n"
        "buffer_load_dwordx4 v13, s[44:47], s65 offen offset:1024 lds\n"
        "s_waitcnt vmcnt(28)\n"
        "ds_read_b64_tr_b16 a[208:209], v14 offset:20480\n"
        "ds_read_b64_tr_b16 a[210:211], v14 offset:20736\n"
        "ds_read_b64_tr_b16 a[212:213], v14 offset:21504\n"
        "ds_read_b64_tr_b16 a[214:215], v14 offset:21760\n"
        "s_waitcnt lgkmcnt(4)\n"
        "s_add_u32 m0, s82, 18432\n"
        "s_mov_b32 s65, 36928\n"
        "buffer_load_dwordx4 v13, s[44:47], s65 offen lds\n"
        "s_mov_b32 s65, 45120\n"
        "buffer_load_dwordx4 v13, s[44:47], s65 offen offset:1024 lds\n"
        "s_waitcnt vmcnt(28)\n"
        "ds_read_b64_tr_b16 a[216:217], v14 offset:22528\n"
        "ds_read_b64_tr_b16 a[218:219], v14 offset:22784\n"
        "ds_read_b64_tr_b16 a[220:221], v14 offset:23552\n"
        "ds_read_b64_tr_b16 a[222:223], v14 offset:23808\n"
        "s_waitcnt lgkmcnt(4)\n"
        "s_add_u32 m0, s82, 20480\n"
        "s_mov_b32 s65, 73792\n"
        "buffer_load_dwordx4 v13, s[44:47], s65 offen lds\n"
        "s_mov_b32 s65, 81984\n"
        "buffer_load_dwordx4 v13, s[44:47], s65 offen offset:1024 lds\n"
        "s_waitcnt vmcnt(28)\n"
        "ds_read_b64_tr_b16 a[224:225], v14 offset:24576\n"
        "ds_read_b64_tr_b16 a[226:227], v14 offset:24832\n"
        "ds_read_b64_tr_b16 a[228:229], v14 offset:25600\n"
        "ds_read_b64_tr_b16 a[230:231], v14 offset:25856\n"
        "s_waitcnt lgkmcnt(4)\n"
        "s_add_u32 m0, s82, 22528\n"
        "s_mov_b32 s65, 110656\n"
        "buffer_load_dwordx4 v13, s[44:47], s65 offen lds\n"
        "s_mov_b32 s65, 118848\n"
        "buffer_load_dwordx4 v13, s[44:47], s65 offen offset:1024 lds\n"
        "s_waitcnt vmcnt(28)\n"
        "ds_read_b64_tr_b16 a[232:233], v14 offset:26624\n"
        "ds_read_b64_tr_b16 a[234:235], v14 offset:26880\n"
        "ds_read_b64_tr_b16 a[236:237], v14 offset:27648\n"
        "ds_read_b64_tr_b16 a[238:239], v14 offset:27904\n"
        "s_waitcnt lgkmcnt(4)\n"
        "s_add_u32 m0, s82, 24576\n"
        "s_mov_b32 s65, 512\n"
        "buffer_load_dwordx4 v13, s[44:47], s65 offen lds\n"
        "s_mov_b32 s65, 8704\n"
        "buffer_load_dwordx4 v13, s[44:47], s65 offen offset:1024 lds\n"
        "s_waitcnt vmcnt(28)\n"
        "ds_read_b64_tr_b16 a[240:241], v14 offset:28672\n"
        "ds_read_b64_tr_b16 a[242:243], v14 offset:28928\n"
        "ds_read_b64_tr_b16 a[244:245], v14 offset:29696\n"
        "ds_read_b64_tr_b16 a[246:247], v14 offset:29952\n"
        "s_waitcnt lgkmcnt(4)\n"
        "s_add_u32 m0, s82, 26624\n"
        "s_mov_b32 s65, 37376\n"
        "buffer_load_dwordx4 v13, s[44:47], s65 offen lds\n"
        "s_mov_b32 s65, 45568\n"
        "buffer_load_dwordx4 v13, s[44:47], s65 offen offset:1024 lds\n"
        "s_waitcnt vmcnt(28)\n"
        "ds_read_b64_tr_b16 a[248:249], v14 offset:30720\n"
        "ds_read_b64_tr_b16 a[250:251], v14 offset:30976\n"
        "ds_read_b64_tr_b16 a[252:253], v14 offset:31744\n"
        "ds_read_b64_tr_b16 a[254:255], v14 offset:32000\n"
        "s_waitcnt lgkmcnt(4)\n"
        "s_add_u32 m0, s82, 28672\n"
        "s_mov_b32 s65, 74240\n"
        "buffer_load_dwordx4 v13, s[44:47], s65 offen lds\n"
        "s_mov_b32 s65, 82432\n"
        "buffer_load_dwordx4 v13, s[44:47], s65 offen offset:1024 lds\n"
        "s_waitcnt vmcnt(28)\n"
        "ds_read_b64_tr_b16 v[80:81], v14 offset:0\n"
        "ds_read_b64_tr_b16 v[82:83], v14 offset:256\n"
        "ds_read_b64_tr_b16 v[84:85], v14 offset:1024\n"
        "ds_read_b64_tr_b16 v[86:87], v14 offset:1280\n"
        "s_waitcnt lgkmcnt(4)\n"
        "s_add_u32 m0, s82, 30720\n"
        "s_mov_b32 s65, 111104\n"
        "buffer_load_dwordx4 v13, s[44:47], s65 offen lds\n"
        "s_mov_b32 s65, 119296\n"
        "buffer_load_dwordx4 v13, s[44:47], s65 offen offset:1024 lds\n"
        "s_waitcnt vmcnt(28)\n"
        "ds_read_b64_tr_b16 v[88:89], v14 offset:2048\n"
        "ds_read_b64_tr_b16 v[90:91], v14 offset:2304\n"
        "ds_read_b64_tr_b16 v[92:93], v14 offset:3072\n"
        "ds_read_b64_tr_b16 v[94:95], v14 offset:3328\n"
        "s_waitcnt lgkmcnt(4)\n"
        "s_add_u32 m0, s82, 0\n"
        "s_mov_b32 s65, 576\n"
        "buffer_load_dwordx4 v13, s[44:47], s65 offen lds\n"
        "s_mov_b32 s65, 8768\n"
        "buffer_load_dwordx4 v13, s[44:47], s65 offen offset:1024 lds\n"
        "s_waitcnt vmcnt(28)\n"
        "ds_read_b64_tr_b16 v[96:97], v14 offset:4096\n"
        "ds_read_b64_tr_b16 v[98:99], v14 offset:4352\n"
        "ds_read_b64_tr_b16 v[100:101], v14 offset:5120\n"
        "ds_read_b64_tr_b16 v[102:103], v14 offset:5376\n"
        "s_waitcnt lgkmcnt(4)\n"
        "s_add_u32 m0, s82, 2048\n"
        "s_mov_b32 s65, 37440\n"
        "buffer_load_dwordx4 v13, s[44:47], s65 offen lds\n"
        "s_mov_b32 s65, 45632\n"
        "buffer_load_dwordx4 v13, s[44:47], s65 offen offset:1024 lds\n"
        "s_waitcnt vmcnt(28)\n"
        "ds_read_b64_tr_b16 v[104:105], v14 offset:6144\n"
        "ds_read_b64_tr_b16 v[106:107], v14 offset:6400\n"
        "ds_read_b64_tr_b16 v[108:109], v14 offset:7168\n"
        "ds_read_b64_tr_b16 v[110:111], v14 offset:7424\n"
        "s_waitcnt lgkmcnt(4)\n"
        "s_add_u32 m0, s82, 4096\n"
        "s_mov_b32 s65, 74304\n"
        "buffer_load_dwordx4 v13, s[44:47], s65 offen lds\n"
        "s_mov_b32 s65, 82496\n"
        "buffer_load_dwordx4 v13, s[44:47], s65 offen offset:1024 lds\n"
        "s_waitcnt vmcnt(28)\n"
        "ds_read_b64_tr_b16 v[112:113], v14 offset:8192\n"
        "ds_read_b64_tr_b16 v[114:115], v14 offset:8448\n"
        "ds_read_b64_tr_b16 v[116:117], v14 offset:9216\n"
        "ds_read_b64_tr_b16 v[118:119], v14 offset:9472\n"
        "s_waitcnt lgkmcnt(4)\n"
        "s_add_u32 m0, s82, 6144\n"
        "s_mov_b32 s65, 111168\n"
        "buffer_load_dwordx4 v13, s[44:47], s65 offen lds\n"
        "s_mov_b32 s65, 119360\n"
        "buffer_load_dwordx4 v13, s[44:47], s65 offen offset:1024 lds\n"
        "s_cmp_lt_u32 s48, 2\n"
        "s_cbranch_scc0 .Lch_noex_%=\n"
        "s_lshl_b32 s66, s48, 6\n"
        "s_add_u32 m0, s82, 32768\n"
        "s_sub_u32 s65, 1024, s66\n"
        "buffer_load_dwordx4 v13, s[44:47], s65 offen lds\n"
        "s_sub_u32 s65, 9216, s66\n"
        "buffer_load_dwordx4 v13, s[44:47], s65 offen offset:1024 lds\n"
        "s_add_u32 m0, s82, 34816\n"
        "s_sub_u32 s65, 37888, s66\n"
        "buffer_load_dwordx4 v13, s[44:47], s65 offen lds\n"
        "s_sub_u32 s65, 46080, s66\n"
        "buffer_load_dwordx4 v13, s[44:47], s65 offen offset:1024 lds\n"
        "s_add_u32 m0, s82, 36864\n"
        "s_sub_u32 s65, 74752, s66\n"
        "buffer_load_dwordx4 v13, s[44:47], s65 offen lds\n"
        "s_sub_u32 s65, 82944, s66\n"
        "buffer_load_dwordx4 v13, s[44:47], s65 offen offset:1024 lds\n"
        "s_add_u32 m0, s82, 38912\n"
        "s_sub_u32 s65, 111616, s66\n"
        "buffer_load_dwordx4 v13, s[44:47], s65 offen lds\n"
        "s_sub_u32 s65, 119808, s66\n"
        "buffer_load_dwordx4 v13, s[44:47], s65 offen offset:1024 lds\n"
        ".Lch_noex_%=:\n"
        "s_waitcnt vmcnt(28)\n"
        "ds_read_b64_tr_b16 v[120:121], v14 offset:10240\n"
        "ds_read_b64_tr_b16 v[122:123], v14 offset:10496\n"
        "ds_read_b64_tr_b16 v[124:125], v14 offset:11264\n"
        "ds_read_b64_tr_b16 v[126:127], v14 offset:11520\n"
        "s_waitcnt lgkmcnt(4)\n"
        "s_mov_b64 s[44:45], %3\n"
        "s_and_b32 s45, s45, 0xffff\n"
        "s_mov_b32 s46, 4718592\n"
        "s_add_u32 m0, s82, 8192\n"
        "s_mov_b32 s65, 4194304\n"
        "buffer_load_dwordx4 v12, s[44:47], s65 offen lds\n"
        "s_mov_b32 s65, 4324352\n"
        "buffer_load_dwordx4 v12, s[44:47], s65 offen offset:1024 lds\n"
        "s_waitcnt vmcnt(28)\n"
        "ds_read_b64_tr_b16 v[128:129], v14 offset:12288\n"
        "ds_read_b64_tr_b16 v[130:131], v14 offset:12544\n"
        "ds_read_b64_tr_b16 v[132:133], v14 offset:13312\n"
        "ds_read_b64_tr_b16 v[134:135], v14 offset:13568\n"
        "s_waitcnt lgkmcnt(4)\n"
        "s_add_u32 m0, s82, 10240\n"
        "s_mov_b32 s65, 4456448\n"
        "buffer_load_dwordx4 v12, s[44:47], s65 offen lds\n"
        "s_mov_b32 s65, 4586496\n"
        "buffer_load_dwordx4 v12, s[44:47], s65 offen offset:1024 lds\n"
        "s_waitcnt vmcnt(28)\n"
        "ds_read_b64_tr_b16 v[136:137], v14 offset:14336\n"
        "ds_read_b64_tr_b16 v[138:139], v14 offset:14592\n"
        "ds_read_b64_tr_b16 v[140:141], v14 offset:15360\n"
        "ds_read_b64_tr_b16 v[142:143], v14 offset:15616\n"
        "s_waitcnt lgkmcnt(4)\n"
        "s_add_u32 m0, s82, 12288\n"
        "s_mov_b32 s65, 1572864\n"
        "buffer_load_dwordx4 v12, s[44:47], s65 offen lds\n"
        "s_mov_b32 s65, 1702912\n"
        "buffer_load_dwordx4 v12, s[44:47], s65 offen offset:1024 lds\n"
        "s_waitcnt vmcnt(28)\n"
        "ds_read_b64_tr_b16 v[144:145], v14 offset:16384\n"
        "ds_read_b64_tr_b16 v[146:147], v14 offset:16640\n"
        "ds_read_b64_tr_b16 v[148:149], v14 offset:17408\n"
        "ds_read_b64_tr_b16 v[150:151], v14 offset:17664\n"
        "s_waitcnt lgkmcnt(4)\n"
        "s_add_u32 m0, s82, 14336\n"
        "s_mov_b32 s65, 1835008\n"
        "buffer_load_dwordx4 v12, s[44:47], s65 offen lds\n"
        "s_mov_b32 s65, 1965056\n"
        "buffer_load_dwordx4 v12, s[44:47], s65 offen offset:1024 lds\n"
        "s_waitcnt vmcnt(28)\n"
        "ds_read_b64_tr_b16 v[152:153], v14 offset:18432\n"
        "ds_read_b64_tr_b16 v[154:155], v14 offset:18688\n"
        "ds_read_b64_tr_b16 v[156:157], v14 offset:19456\n"
        "ds_read_b64_tr_b16 v[158:159], v14 offset:19712\n"
        "s_waitcnt lgkmcnt(4)\n"
        "s_add_u32 m0, s82, 16384\n"
        "s_mov_b32 s65, 2097152\n"
        "buffer_load_dwordx4 v12, s[44:47], s65 offen lds\n"
        "s_mov_b32 s65, 2227200\n"
        "buffer_load_dwordx4 v12, s[44:47], s65 offen offset:1024 lds\n"
        "s_waitcnt vmcnt(28)\n"
        "ds_read_b64_tr_b16 v[160:161], v14 offset:20480\n"
        "ds_read_b64_tr_b16 v[162:163], v14 offset:20736\n"
        "ds_read_b64_tr_b16 v[164:165], v14 offset:21504\n"
        "ds_read_b64_tr_b16 v[166:167], v14 offset:21760\n"
        "s_waitcnt lgkmcnt(4)\n"
        "s_add_u32 m0, s82, 18432\n"
        "s_mov_b32 s65, 2359296\n"
        "buffer_load_dwordx4 v12, s[44:47], s65 offen lds\n"
        "s_mov_b32 s65, 2489344\n"
        "buffer_load_dwordx4 v12, s[44:47], s65 offen offset:1024 lds\n"
        "s_waitcnt vmcnt(28)\n"
        "ds_read_b64_tr_b16 v[168:169], v14 offset:22528\n"
        "ds_read_b64_tr_b16 v[170:171], v14 offset:22784\n"
        "ds_read_b64_tr_b16 v[172:173], v14 offset:23552\n"
        "ds_read_b64_tr_b16 v[174:175], v14 offset:23808\n"
        "s_waitcnt lgkmcnt(4)\n"
        "s_add_u32 m0, s82, 20480\n"
        "s_mov_b32 s65, 2621440\n"
        "buffer_load_dwordx4 v12, s[44:47], s65 offen lds\n"
        "s_mov_b32 s65, 2751488\n"
        "buffer_load_dwordx4 v12, s[44:47], s65 offen offset:1024 lds\n"
        "s_waitcnt vmcnt(28)\n"
        "ds_read_b64_tr_b16 v[176:177], v14 offset:24576\n"
        "ds_read_b64_tr_b16 v[178:179], v14 offset:24832\n"
        "ds_read_b64_tr_b16 v[180:181], v14 offset:25600\n"
        "ds_read_b64_tr_b16 v[182:183], v14 offset:25856\n"
        "s_waitcnt lgkmcnt(4)\n"
        "s_add_u32 m0, s82, 22528\n"
        "s_mov_b32 s65, 2883584\n"
        "buffer_load_dwordx4 v12, s[44:47], s65 offen lds\n"
        "s_mov_b32 s65, 3013632\n"
        "buffer_load_dwordx4 v12, s[44:47], s65 offen offset:1024 lds\n"
        "s_waitcnt vmcnt(28)\n"
        "ds_read_b64_tr_b16 v[184:185], v14 offset:26624\n"
        "ds_read_b64_tr_b16 v[186:187], v14 offset:26880\n"
        "ds_read_b64_tr_b16 v[188:189], v14 offset:27648\n"
        "ds_read_b64_tr_b16 v[190:191], v14 offset:27904\n"
        "s_waitcnt lgkmcnt(4)\n"
        "s_add_u32 m0, s82, 24576\n"
        "s_mov_b32 s65, 3145728\n"
        "buffer_load_dwordx4 v12, s[44:47], s65 offen lds\n"
        "s_mov_b32 s65, 3275776\n"
        "buffer_load_dwordx4 v12, s[44:47], s65 offen offset:1024 lds\n"
        "s_waitcnt vmcnt(28)\n"
        "ds_read_b64_tr_b16 v[192:193], v14 offset:28672\n"
        "ds_read_b64_tr_b16 v[194:195], v14 offset:28928\n"
        "ds_read_b64_tr_b16 v[196:197], v14 offset:29696\n"
        "ds_read_b64_tr_b16 v[198:199], v14 offset:29952\n"
        "s_waitcnt lgkmcnt(4)\n"
        "s_add_u32 m0, s82, 26624\n"
        "s_mov_b32 s65, 3407872\n"
        "buffer_load_dwordx4 v12, s[44:47], s65 offen lds\n"
        "s_mov_b32 s65, 3537920\n"
        "buffer_load_dwordx4 v12, s[44:47], s65 offen offset:1024 lds\n"
        "s_waitcnt vmcnt(28)\n"
        "ds_read_b64_tr_b16 v[200:201], v14 offset:30720\n"
        "ds_read_b64_tr_b16 v[202:203], v14 offset:30976\n"
        "ds_read_b64_tr_b16 v[204:205], v14 offset:31744\n"
        "ds_read_b64_tr_b16 v[206:207], v14 offset:32000\n"
        "s_waitcnt lgkmcnt(4)\n"
        "s_add_u32 m0, s82, 28672\n"
        "s_mov_b32 s65, 3670016\n"
        "buffer_load_dwordx4 v12, s[44:47], s65 offen lds\n"
        "s_mov_b32 s65, 3800064\n"
        "buffer_load_dwordx4 v12, s[44:47], s65 offen offset:1024 lds\n"
        "s_waitcnt vmcnt(28)\n"
        "ds_read_b64_tr_b16 v[208:209], v14 offset:0\n"
        "ds_read_b64_tr_b16 v[210:211], v14 offset:256\n"
        "ds_read_b64_tr_b16 v[212:213], v14 offset:1024\n"
        "ds_read_b64_tr_b16 v[214:215], v14 offset:1280\n"
        "s_waitcnt lgkmcnt(4)\n"
        "s_add_u32 m0, s82, 30720\n"
        "s_mov_b32 s65, 3932160\n"
        "buffer_load_dwordx4 v12, s[44:47], s65 offen lds\n"
        "s_mov_b32 s65, 4062208\n"
        "buffer_load_dwordx4 v12, s[44:47], s65 offen offset:1024 lds\n"
        "s_waitcnt vmcnt(28)\n"
        "ds_read_b64_tr_b16 v[216:217], v14 offset:2048\n"
        "ds_read_b64_tr_b16 v[218:219], v14 offset:2304\n"
        "ds_read_b64_tr_b16 v[220:221], v14 offset:3072\n"
        "ds_read_b64_tr_b16 v[222:223], v14 offset:3328\n"
        "s_waitcnt lgkmcnt(4)\n"
        "s_add_u32 m0, s82, 0\n"
        "s_mov_b32 s65, 0\n"
        "buffer_load_dwordx4 v12, s[44:47], s65 offen lds\n"
        "s_mov_b32 s65, 130048\n"
        "buffer_load_dwordx4 v12, s[44:47], s65 offen offset:1024 lds\n"
        "s_waitcnt vmcnt(28)\n"
        "ds_read_b64_tr_b16 v[224:225], v14 offset:4096\n"
        "ds_read_b64_tr_b16 v[226:227], v14 offset:4352\n"
        "ds_read_b64_tr_b16 v[228:229], v14 offset:5120\n"
        "ds_read_b64_tr_b16 v[230:231], v14 offset:5376\n"
        "s_waitcnt lgkmcnt(4)\n"
        "s_add_u32 m0, s82, 2048\n"
        "s_mov_b32 s65, 262144\n"
        "buffer_load_dwordx4 v12, s[44:47], s65 offen lds\n"
        "s_mov_b32 s65, 392192\n"
        "buffer_load_dwordx4 v12, s[44:47], s65 offen offset:1024 lds\n"
        "s_waitcnt vmcnt(28)\n"
        "ds_read_b64_tr_b16 v[232:233], v14 offset:6144\n"
        "ds_read_b64_tr_b16 v[234:235], v14 offset:6400\n"
        "ds_read_b64_tr_b16 v[236:237], v14 offset:7168\n"
        "ds_read_b64_tr_b16 v[238:239], v14 offset:7424\n"
        "s_waitcnt lgkmcnt(4)\n"
        "s_add_u32 m0, s82, 4096\n"
        "s_mov_b32 s65, 524288\n"
        "buffer_load_dwordx4 v12, s[44:47], s65 offen lds\n"
        "s_mov_b32 s65, 654336\n"
        "buffer_load_dwordx4 v12, s[44:47], s65 offen offset:1024 lds\n"
        "s_waitcnt vmcnt(28)\n"
        "ds_read_b64_tr_b16 v[240:241], v14 offset:8192\n"
        "ds_read_b64_tr_b16 v[242:243], v14 offset:8448\n"
        "ds_read_b64_tr_b16 v[244:245], v14 offset:9216\n"
        "ds_read_b64_tr_b16 v[246:247], v14 offset:9472\n"
        "s_waitcnt lgkmcnt(4)\n"
        "s_add_u32 m0, s82, 6144\n"
        "s_mov_b32 s65, 786432\n"
        "buffer_load_dwordx4 v12, s[44:47], s65 offen lds\n"
        "s_mov_b32 s65, 916480\n"
        "buffer_load_dwordx4 v12, s[44:47], s65 offen offset:1024 lds\n"
        "s_waitcnt vmcnt(28)\n"
        "ds_read_b64_tr_b16 v[248:249], v14 offset:10240\n"
        "ds_read_b64_tr_b16 v[250:251], v14 offset:10496\n"
        "ds_read_b64_tr_b16 v[252:253], v14 offset:11264\n"
        "ds_read_b64_tr_b16 v[254:255], v14 offset:11520\n"
        "s_waitcnt lgkmcnt(4)\n"
        "s_add_u32 m0, s82, 8192\n"
        "s_mov_b32 s65, 1048576\n"
        "buffer_load_dwordx4 v12, s[44:47], s65 offen lds\n"
        "s_mov_b32 s65, 1178624\n"
        "buffer_load_dwordx4 v12, s[44:47], s65 offen offset:1024 lds\n"
        "s_waitcnt vmcnt(28)\n"
        "ds_read_b64_tr_b16 v[24:25], v14 offset:12288\n"
        "ds_read_b64_tr_b16 v[26:27], v14 offset:12544\n"
        "ds_read_b64_tr_b16 v[28:29], v14 offset:13312\n"
        "ds_read_b64_tr_b16 v[30:31], v14 offset:13568\n"
        "s_waitcnt lgkmcnt(4)\n"
        "s_add_u32 m0, s82, 10240\n"
        "s_mov_b32 s65, 1310720\n"
        "buffer_load_dwordx4 v12, s[44:47], s65 offen lds\n"
        "s_mov_b32 s65, 1440768\n"
        "buffer_load_dwordx4 v12, s[44:47], s65 offen offset:1024 lds\n"
        "s_waitcnt vmcnt(28)\n"
        "ds_read_b64_tr_b16 v[32:33], v14 offset:14336\n"
        "ds_read_b64_tr_b16 v[34:35], v14 offset:14592\n"
        "ds_read_b64_tr_b16 v[36:37], v14 offset:15360\n"
        "ds_read_b64_tr_b16 v[38:39], v14 offset:15616\n"
        "s_waitcnt lgkmcnt(4)\n"
        "ds_write_b128 v2, v[24:27] offset:12288\n"
        "ds_write_b128 v2, v[28:31] offset:13312\n"
        "s_waitcnt vmcnt(26)\n"
        "ds_read_b64_tr_b16 v[24:25], v14 offset:16384\n"
        "ds_read_b64_tr_b16 v[26:27], v14 offset:16640\n"
        "ds_read_b64_tr_b16 v[28:29], v14 offset:17408\n"
        "ds_read_b64_tr_b16 v[30:31], v14 offset:17664\n"
        "s_waitcnt lgkmcnt(4)\n"
        "ds_write_b128 v2, v[32:35] offset:14336\n"
        "ds_write_b128 v2, v[36:39] offset:15360\n"
        "s_waitcnt vmcnt(24)\n"
        "ds_read_b64_tr_b16 v[32:33], v14 offset:18432\n"
        "ds_read_b64_tr_b16 v[34:35], v14 offset:18688\n"
        "ds_read_b64_tr_b16 v[36:37], v14 offset:19456\n"
        "ds_read_b64_tr_b16 v[38:39], v14 offset:19712\n"
        "s_waitcnt lgkmcnt(4)\n"
        "ds_write_b128 v2, v[24:27] offset:16384\n"
        "ds_write_b128 v2, v[28:31] offset:17408\n"
        "s_waitcnt vmcnt(22)\n"
        "ds_read_b64_tr_b16 v[24:25], v14 offset:20480\n"
        "ds_read_b64_tr_b16 v[26:27], v14 offset:20736\n"
        "ds_read_b64_tr_b16 v[28:29], v14 offset:21504\n"
        "ds_read_b64_tr_b16 v[30:31], v14 offset:21760\n"
        "s_waitcnt lgkmcnt(4)\n"
        "ds_write_b128 v2, v[32:35] offset:18432\n"
        "ds_write_b128 v2, v[36:39] offset:19456\n"
        "s_waitcnt vmcnt(20)\n"
        "ds_read_b64_tr_b16 v[32:33], v14 offset:22528\n"
        "ds_read_b64_tr_b16 v[34:35], v14 offset:22784\n"
        "ds_read_b64_tr_b16 v[36:37], v14 offset:23552\n"
        "ds_read_b64_tr_b16 v[38:39], v14 offset:23808\n"
        "s_waitcnt lgkmcnt(4)\n"
        "ds_write_b128 v2, v[24:27] offset:20480\n"
        "ds_write_b128 v2, v[28:31] offset:21504\n"
        "s_waitcnt vmcnt(18)\n"
        "ds_read_b64_tr_b16 v[24:25], v14 offset:24576\n"
        "ds_read_b64_tr_b16 v[26:27], v14 offset:24832\n"
        "ds_read_b64_tr_b16 v[28:29], v14 offset:25600\n"
        "ds_read_b64_tr_b16 v[30:31], v14 offset:25856\n"
        "s_waitcnt lgkmcnt(4)\n"
        "ds_write_b128 v2, v[32:35] offset:22528\n"
        "ds_write_b128 v2, v[36:39] offset:23552\n"
        "s_waitcnt vmcnt(16)\n"
        "ds_read_b64_tr_b16 v[32:33], v14 offset:26624\n"
        "ds_read_b64_tr_b16 v[34:35], v14 offset:26880\n"
        "ds_read_b64_tr_b16 v[36:37], v14 offset:27648\n"
        "ds_read_b64_tr_b16 v[38:39], v14 offset:27904\n"
        "s_waitcnt lgkmcnt(4)\n"
        "ds_write_b128 v2, v[24:27] offset:24576\n"
        "ds_write_b128 v2, v[28:31] offset:25600\n"
        "s_waitcnt vmcnt(14)\n"
        "ds_read_b64_tr_b16 v[24:25], v14 offset:28672\n"
        "ds_read_b64_tr_b16 v[26:27], v14 offset:28928\n"
        "ds_read_b64_tr_b16 v[28:29], v14 offset:29696\n"
        "ds_read_b64_tr_b16 v[30:31], v14 offset:29952\n"
        "s_waitcnt lgkmcnt(4)\n"
        "ds_write_b128 v2, v[32:35] offset:26624\n"
        "ds_write_b128 v2, v[36:39] offset:27648\n"
        "s_waitcnt vmcnt(12)\n"
        "ds_read_b64_tr_b16 v[32:33], v14 offset:30720\n"
        "ds_read_b64_tr_b16 v[34:35], v14 offset:30976\n"
        "ds_read_b64_tr_b16 v[36:37], v14 offset:31744\n"
        "ds_read_b64_tr_b16 v[38:39], v14 offset:32000\n"
        "s_waitcnt lgkmcnt(4)\n"
        "ds_write_b128 v2, v[24:27] offset:28672\n"
        "ds_write_b128 v2, v[28:31] offset:29696\n"
        "s_waitcnt vmcnt(10)\n"
        "ds_read_b64_tr_b16 v[24:25], v14 offset:0\n"
        "ds_read_b64_tr_b16 v[26:27], v14 offset:256\n"
        "ds_read_b64_tr_b16 v[28:29], v14 offset:1024\n"
        "ds_read_b64_tr_b16 v[30:31], v14 offset:1280\n"
        "s_waitcnt lgkmcnt(4)\n"
        "ds_write_b128 v2, v[32:35] offset:30720\n"
        "ds_write_b128 v2, v[36:39] offset:31744\n"
        "s_waitcnt vmcnt(8)\n"
        "ds_read_b64_tr_b16 v[32:33], v14 offset:2048\n"
        "ds_read_b64_tr_b16 v[34:35], v14 offset:2304\n"
        "ds_read_b64_tr_b16 v[36:37], v14 offset:3072\n"
        "ds_read_b64_tr_b16 v[38:39], v14 offset:3328\n"
        "s_waitcnt lgkmcnt(4)\n"
        "ds_write_b128 v2, v[24:27] offset:0\n"
        "ds_write_b128 v2, v[28:31] offset:1024\n"
        "s_waitcnt vmcnt(6)\n"
        "ds_read_b64_tr_b16 v[24:25], v14 offset:4096\n"
        "ds_read_b64_tr_b16 v[26:27], v14 offset:4352\n"
        "ds_read_b64_tr_b16 v[28:29], v14 offset:5120\n"
        "ds_read_b64_tr_b16 v[30:31], v14 offset:5376\n"
        "s_waitcnt lgkmcnt(4)\n"
        "ds_write_b128 v2, v[32:35] offset:2048\n"
        "ds_write_b128 v2, v[36:39] offset:3072\n"
        "s_waitcnt vmcnt(4)\n"
        "ds_read_b64_tr_b16 v[32:33], v14 offset:6144\n"
        "ds_read_b64_tr_b16 v[34:35], v14 offset:6400\n"
        "ds_read_b64_tr_b16 v[36:37], v14 offset:7168\n"
        "ds_read_b64_tr_b16 v[38:39], v14 offset:7424\n"
        "s_waitcnt lgkmcnt(4)\n"
        "ds_write_b128 v2, v[24:27] offset:4096\n"
        "ds_write_b128 v2, v[28:31] offset:5120\n"
        "s_waitcnt vmcnt(2)\n"
        "ds_read_b64_tr_b16 v[24:25], v14 offset:8192\n"
        "ds_read_b64_tr_b16 v[26:27], v14 offset:8448\n"
        "ds_read_b64_tr_b16 v[28:29], v14 offset:9216\n"
        "ds_read_b64_tr_b16 v[30:31], v14 offset:9472\n"
        "s_waitcnt lgkmcnt(4)\n"
        "ds_write_b128 v2, v[32:35] offset:6144\n"
        "ds_write_b128 v2, v[36:39] offset:7168\n"
        "s_waitcnt vmcnt(0)\n"
        "ds_read_b64_tr_b16 v[32:33], v14 offset:10240\n"
        "ds_read_b64_tr_b16 v[34:35], v14 offset:10496\n"
        "ds_read_b64_tr_b16 v[36:37], v14 offset:11264\n"
        "ds_read_b64_tr_b16 v[38:39], v14 offset:11520\n"
        "s_waitcnt lgkmcnt(4)\n"
        "ds_write_b128 v2, v[24:27] offset:8192\n"
        "ds_write_b128 v2, v[28:31] offset:9216\n"
        "s_waitcnt lgkmcnt(0)\n"
        "ds_write_b128 v2, v[32:35] offset:10240\n"
        "ds_write_b128 v2, v[36:39] offset:11264\n"
        "s_cmp_lt_u32 s48, 2\n"
        "s_cbranch_scc0 .Lch_noex2_%=\n"
        "ds_read_b64_tr_b16 v[40:41], v14 offset:32768\n"
        "ds_read_b64_tr_b16 v[42:43], v14 offset:33024\n"
        "ds_read_b64_tr_b16 v[44:45], v14 offset:33792\n"
        "ds_read_b64_tr_b16 v[46:47], v14 offset:34048\n"
        "ds_read_b64_tr_b16 v[48:49], v14 offset:34816\n"
        "ds_read_b64_tr_b16 v[50:51], v14 offset:35072\n"
        "ds_read_b64_tr_b16 v[52:53], v14 offset:35840\n"
        "ds_read_b64_tr_b16 v[54:55], v14 offset:36096\n"
        "ds_read_b64_tr_b16 v[56:57], v14 offset:36864\n"
        "ds_read_b64_tr_b16 v[58:59], v14 offset:37120\n"
        "ds_read_b64_tr_b16 v[60:61], v14 offset:37888\n"
        "ds_read_b64_tr_b16 v[62:63], v14 offset:38144\n"
        "ds_read_b64_tr_b16 v[64:65], v14 offset:38912\n"
        "ds_read_b64_tr_b16 v[66:67], v14 offset:39168\n"
        "ds_read_b64_tr_b16 v[68:69], v14 offset:39936\n"
        "ds_read_b64_tr_b16 v[70:71], v14 offset:40192\n"
        "s_waitcnt lgkmcnt(0)\n"
        "ds_write_b128 v2, v[40:43] offset:32768\n"
        "ds_write_b128 v2, v[44:47] offset:33792\n"
        "ds_write_b128 v2, v[48:51] offset:34816\n"
        "ds_write_b128 v2, v[52:55] offset:35840\n"
        "ds_write_b128 v2, v[56:59] offset:36864\n"
        "ds_write_b128 v2, v[60:63] offset:37888\n"
        "ds_write_b128 v2, v[64:67] offset:38912\n"
        "ds_write_b128 v2, v[68:71] offset:39936\n"
        ".Lch_noex2_%=:\n"
        "s_waitcnt lgkmcnt(0)\n"
        "v_lshrrev_b32 v12, 4, v3\n"
        "s_lshl_b32 s66, s57, 7\n"
        "s_mov_b32 s64, 0\n"
        ".Lch_pair_top_%=:\n"
        "s_mov_b64 exec, 1\n"
        "buffer_load_dword v13, off, s[40:43], s66 sc1\n"
        "s_mov_b64 exec, -1\n"
        "s_waitcnt vmcnt(0)\n"
        "v_cmp_gt_i32 vcc, 0, v13\n"
        "s_nop 0\n"
        "s_and_b64 vcc, vcc, 1\n"
        "s_cbranch_vccz .Lch_pair_ok_%=\n"
        "s_add_u32 s64, s64, 1\n"
        "s_and_b32 s65, s64, 255\n"
        "s_cbranch_scc1 .Lch_pair_go_%=\n"
        "s_mov_b32 s65, 40960\n"
        "s_cmp_lt_u32 s64, 1048576\n"
        "s_cbranch_scc1 .Lch_pair_chk_%=\n"
        "v_mov_b32 v13, 1\n"
        "buffer_store_dword v13, off, s[40:43], s65 sc1\n"
        "s_waitcnt vmcnt(0)\n"
        ".Lch_pair_chk_%=:\n"
        "buffer_load_dword v13, off, s[40:43], s65 sc1\n"
        "s_waitcnt vmcnt(0)\n"
        "v_readfirstlane_b32 s65, v13\n"
        "s_nop 0\n"
        "s_cmp_lg_u32 s65, 1\n"
        "s_cbranch_scc1 .Lch_pair_go_%=\n"
        "s_endpgm\n"
        ".Lch_pair_go_%=:\n"
        "s_sleep 2\n"
        "s_branch .Lch_pair_top_%=\n"
        ".Lch_pair_ok_%=:\n"
        "v_readfirstlane_b32 s50, v13\n"
        "s_nop 0\n"
        "s_xor_b32 s50, s50, s56\n"
        "v_lshlrev_b32 v14, 7, v12\n"
        "s_sub_u32 s66, 0, s50\n"
        "s_mov_b32 s52, 24576\n"
        "s_mov_b32 s64, 0\n"
        ".Lch_pg0_top_%=:\n"
        "buffer_load_dword v13, v14, s[40:43], s52 offen sc1\n"
        "s_waitcnt vmcnt(0)\n"
        "v_cmp_gt_i32 vcc, 0, v13\n"
        "s_cbranch_vccz .Lch_pg0_ok_%=\n"
        "s_add_u32 s64, s64, 1\n"
        "s_and_b32 s65, s64, 255\n"
        "s_cbranch_scc1 .Lch_pg0_go_%=\n"
        "s_mov_b32 s65, 40960\n"
        "s_cmp_lt_u32 s64, 1048576\n"
        "s_cbranch_scc1 .Lch_pg0_chk_%=\n"
        "v_mov_b32 v13, 1\n"
        "buffer_store_dword v13, off, s[40:43], s65 sc1\n"
        "s_waitcnt vmcnt(0)\n"
        ".Lch_pg0_chk_%=:\n"
        "buffer_load_dword v13, off, s[40:43], s65 sc1\n"
        "s_waitcnt vmcnt(0)\n"
        "v_readfirstlane_b32 s65, v13\n"
        "s_nop 0\n"
        "s_cmp_lg_u32 s65, 1\n"
        "s_cbranch_scc1 .Lch_pg0_go_%=\n"
        "s_endpgm\n"
        ".Lch_pg0_go_%=:\n"
        "s_sleep 2\n"
        "s_branch .Lch_pg0_top_%=\n"
        ".Lch_pg0_ok_%=:\n"
        "v_cmp_eq_u32 s[52:53], 1, v13\n"
        "s_nop 1\n"
        "s_mov_b32 s54, 32768\n"
        "s_mov_b32 s64, 0\n"
        ".Lch_pg1_top_%=:\n"
        "buffer_load_dword v13, v14, s[40:43], s54 offen sc1\n"
        "s_waitcnt vmcnt(0)\n"
        "v_cmp_gt_i32 vcc, 0, v13\n"
        "s_cbranch_vccz .Lch_pg1_ok_%=\n"
        "s_add_u32 s64, s64, 1\n"
        "s_and_b32 s65, s64, 255\n"
        "s_cbranch_scc1 .Lch_pg1_go_%=\n"
        "s_mov_b32 s65, 40960\n"
        "s_cmp_lt_u32 s64, 1048576\n"
        "s_cbranch_scc1 .Lch_pg1_chk_%=\n"
        "v_mov_b32 v13, 1\n"
        "buffer_store_dword v13, off, s[40:43], s65 sc1\n"
        "s_waitcnt vmcnt(0)\n"
        ".Lch_pg1_chk_%=:\n"
        "buffer_load_dword v13, off, s[40:43], s65 sc1\n"
        "s_waitcnt vmcnt(0)\n"
        "v_readfirstlane_b32 s65, v13\n"
        "s_nop 0\n"
        "s_cmp_lg_u32 s65, 1\n"
        "s_cbranch_scc1 .Lch_pg1_go_%=\n"
        "s_endpgm\n"
        ".Lch_pg1_go_%=:\n"
        "s_sleep 2\n"
        "s_branch .Lch_pg1_top_%=\n"
        ".Lch_pg1_ok_%=:\n"
        "v_cmp_eq_u32 s[54:55], 1, v13\n"
        "s_nop 1\n"
        "s_mov_b32 vcc_lo, 0x55555555\n"
        "s_mov_b32 vcc_hi, 0x55555555\n"
        "s_xor_b64 vcc, vcc, s[52:53]\n"
        "s_bcnt1_i32_b64 s65, vcc\n"
        "s_sub_u32 s64, s65, 23\n"
        "s_cmp_lt_u32 s64, 19\n"
        "s_cbranch_scc1 .Lch_rawmap_%=\n"
        "s_cmp_lt_u32 s65, 32\n"
        "s_cselect_b32 s65, 0, -1\n"
        "s_xor_b32 s52, s65, 0x55555555\n"
        "s_xor_b32 s53, s65, 0x55555555\n"
        "s_xor_b32 s54, s65, 0x55555555\n"
        "s_xor_b32 s55, s65, 0xaaaaaaaa\n"
        ".Lch_rawmap_%=:\n"
        "s_xor_b32 s52, s52, s66\n"
        "s_xor_b32 s53, s53, s66\n"
        "s_xor_b32 s54, s54, s66\n"
        "s_xor_b32 s55, s55, s66\n"
        "s_mov_b32 s58, 0xff00ff00\n"
        "s_mov_b32 s59, 0xff00ff00\n"
        "s_and_b32 s67, s55, 1\n"
        "s_lshl_b32 s67, s67, 13\n"
        "s_lshl_b32 s65, s49, 2\n"
        "s_add_u32 s65, s65, s48\n"
        "s_lshl_b32 s65, s65, 7\n"
        "s_lshr_b32 s66, s65, 13\n"
        "s_lshl_b32 s66, s66, 13\n"
        "s_add_u32 s66, s66, s65\n"
        "s_add_u32 s66, s66, s67\n"
        "s_add_u32 s78, s66, 114688\n"
        "s_add_u32 s79, s66, 180224\n"
        "s_lshl_b32 s83, s49, 14\n"
        "s_lshl_b32 s65, s48, 11\n"
        "s_add_u32 s83, s83, s65\n"
        "s_add_u32 s83, s83, 524288\n"
        "s_lshl_b32 s65, s49, 12\n"
        "s_lshr_b32 s84, s65, 13\n"
        "s_lshl_b32 s84, s84, 13\n"
        "s_add_u32 s84, s84, s65\n"
        "s_lshl_b32 s65, s48, 9\n"
        "s_add_u32 s84, s84, s65\n"
        "s_add_u32 s84, s84, 1572864\n"
        "s_lshr_b32 s65, s49, 2\n"
        "s_add_u32 s65, s65, 64\n"
        "s_cmp_eq_u32 s48, 2\n"
        "s_cselect_b32 s65, s49, s65\n"
        "s_add_u32 s66, s49, 32\n"
        "s_cmp_eq_u32 s48, 3\n"
        "s_cselect_b32 s65, s66, s65\n"
        "s_lshl_b32 s85, s65, 7\n"
        "s_cmp_eq_u32 s48, 0\n"
        "s_cselect_b32 s66, 57344, 0\n"
        "s_add_u32 s85, s85, s66\n"
        "s_add_u32 s85, s85, s67\n"
        "s_add_u32 s85, s85, 1671168\n"
        "s_lshr_b32 s66, s49, 3\n"
        "s_lshl_b32 s54, s66, 14\n"
        "s_lshl_b32 s55, s66, 10\n"
        "s_and_b32 s66, s48, 1\n"
        "s_lshl_b32 s66, s66, 12\n"
        "s_add_u32 s54, s54, s66\n"
        "s_add_u32 s66, s67, 1671168\n"
        "s_add_u32 s54, s54, s66\n"
        "s_add_u32 s55, s55, s66\n"
        "s_add_u32 s55, s55, 65536\n"
        "s_add_u32 s72, s85, 245760\n"
        "s_lshl_b32 s66, s65, 12\n"
        "s_lshr_b32 s87, s66, 13\n"
        "s_lshl_b32 s87, s87, 13\n"
        "s_add_u32 s87, s87, s66\n"
        "s_and_b32 s65, s49, 3\n"
        "s_or_b32 s88, s65, s48\n"
        "s_lshl_b32 s86, s48, 16\n"
        "s_lshl_b32 s65, s49, 7\n"
        "s_add_u32 s86, s86, s65\n"
        "s_add_u32 s67, s67, 491520\n"
        "s_add_u32 s86, s86, s67\n"
        "s_add_u32 s87, s87, s67\n"
        "s_lshl_b32 s65, s48, 3\n"
        "s_lshr_b32 s60, s60, s65\n"
        "s_and_b32 s60, s60, 255\n"
        "s_sub_u32 s60, s60, 32\n"
        "s_lshr_b32 s34, s61, s65\n"
        "s_and_b32 s34, s34, 255\n"
        "s_lshr_b32 s61, s62, s65\n"
        "s_and_b32 s61, s61, 255\n"
        "s_mov_b32 s35, 80\n"
        "s_mov_b32 s51, -1\n"
        "s_mov_b32 s80, -2\n"
        "s_lshl_b32 s62, s49, 7\n"
        "s_cmp_eq_u32 s48, 1\n"
        "s_cselect_b32 s62, s62, 2147483648\n"
        "s_bfe_u32 s56, s63, 0x80008\n"
        "s_lshr_b32 s57, s63, 16\n"
        "s_and_b32 s63, s63, 255\n"
        "s_mov_b32 s44, 0x3f803f80\n"
        "s_mov_b32 s45, 0x3f803f80\n"
        "s_branch .Lch_idle2_%=\n"
        ".Lch_Q_%=:\n"
        "ds_read_b128 v[24:27], v1 offset:0\n"
        "ds_read_b128 v[28:31], v1 offset:64\n"
        "ds_read_b128 v[32:35], v1 offset:1024\n"
        "s_waitcnt lgkmcnt(2)\n"
        "v_smfmac_f32_16x16x64_bf16 v[12:15], v[24:27], a[0:7], v0\n"
        "ds_read_b128 v[36:39], v1 offset:1088\n"
        "v_max3_u32 v4, v4, v24, v25\n"
        "v_smfmac_f32_16x16x64_bf16 v[16:19], v[24:27], a[144:151], v0\n"
        "s_waitcnt lgkmcnt(2)\n"
        "v_smfmac_f32_16x16x64_bf16 v[20:23], v[24:27], v[40:47], v0\n"
        "ds_read_b128 v[40:43], v2 offset:6144\n"
        "v_max3_u32 v4, v4, v26, v27\n"
        "v_smfmac_f32_16x16x64_bf16 v[12:15], v[28:31], a[8:15], v0\n"
        "ds_read_b128 v[24:27], v1 offset:2048\n"
        "v_max3_u32 v4, v4, v28, v29\n"
        "v_smfmac_f32_16x16x64_bf16 v[16:19], v[28:31], a[152:159], v0\n"
        "ds_read_b128 v[44:47], v2 offset:7168\n"
        "s_waitcnt lgkmcnt(4)\n"
        "v_smfmac_f32_16x16x64_bf16 v[20:23], v[28:31], v[48:55], v0\n"
        "ds_read_b128 v[48:51], v2 offset:8192\n"
        "v_max3_u32 v4, v4, v30, v31\n"
        "v_smfmac_f32_16x16x64_bf16 v[12:15], v[32:35], a[16:23], v0\n"
        "ds_read_b128 v[28:31], v1 offset:2112\n"
        "v_max3_u32 v4, v4, v32, v33\n"
        "v_smfmac_f32_16x16x64_bf16 v[16:19], v[32:35], a[160:167], v0\n"
        "ds_read_b128 v[52:55], v2 offset:9216\n"
        "s_waitcnt lgkmcnt(6)\n"
        "v_smfmac_f32_16x16x64_bf16 v[20:23], v[32:35], v[56:63], v0\n"
        "ds_read_b128 v[56:59], v2 offset:10240\n"
        "v_max3_u32 v4, v4, v34, v35\n"
        "v_smfmac_f32_16x16x64_bf16 v[12:15], v[36:39], a[24:31], v0\n"
        "ds_read_b128 v[32:35], v1 offset:3072\n"
        "v_max3_u32 v4, v4, v36, v37\n"
        "v_smfmac_f32_16x16x64_bf16 v[16:19], v[36:39], a[168:175], v0\n"
        "ds_read_b128 v[60:63], v2 offset:11264\n"
        "s_waitcnt lgkmcnt(6)\n"
        "v_smfmac_f32_16x16x64_bf16 v[20:23], v[36:39], v[40:47], v0\n"
        "ds_read_b128 v[40:43], v2 offset:12288\n"
        "v_max3_u32 v4, v4, v38, v39\n"
        "v_smfmac_f32_16x16x64_bf16 v[12:15], v[24:27], a[32:39], v0\n"
        "ds_read_b128 v[36:39], v1 offset:3136\n"
        "v_max3_u32 v4, v4, v24, v25\n"
        "v_smfmac_f32_16x16x64_bf16 v[16:19], v[24:27], a[176:183], v0\n"
        "ds_read_b128 v[44:47], v2 offset:13312\n"
        "s_waitcnt lgkmcnt(6)\n"
        "v_smfmac_f32_16x16x64_bf16 v[20:23], v[24:27], v[48:55], v0\n"
        "ds_read_b128 v[48:51], v2 offset:14336\n"
        "v_max3_u32 v4, v4, v26, v27\n"
        "v_smfmac_f32_16x16x64_bf16 v[12:15], v[28:31], a[40:47], v0\n"
        "s_waitcnt vmcnt(10)\n"
        "v_cmp_eq_u32 vcc, -1, v4\n"
        "s_cbranch_vccnz .Lch_redo_%=\n"
        "s_barrier\n"
        "ds_read_b128 v[24:27], v1 offset:4096\n"
        "v_max3_u32 v4, v4, v28, v29\n"
        "v_smfmac_f32_16x16x64_bf16 v[16:19], v[28:31], a[184:191], v0\n"
        "ds_read_b128 v[52:55], v2 offset:15360\n"
        "s_waitcnt lgkmcnt(6)\n"
        "v_smfmac_f32_16x16x64_bf16 v[20:23], v[28:31], v[56:63], v0\n"
        "ds_read_b128 v[56:59], v2 offset:16384\n"
        "v_max3_u32 v4, v4, v30, v31\n"
        "v_smfmac_f32_16x16x64_bf16 v[12:15], v[32:35], a[48:55], v0\n"
        "ds_read_b128 v[28:31], v1 offset:4160\n"
        "v_max3_u32 v4, v4, v32, v33\n"
        "v_smfmac_f32_16x16x64_bf16 v[16:19], v[32:35], a[192:199], v0\n"
        "ds_read_b128 v[60:63], v2 offset:17408\n"
        "s_waitcnt lgkmcnt(6)\n"
        "v_smfmac_f32_16x16x64_bf16 v[20:23], v[32:35], v[40:47], v0\n"
        "ds_read_b128 v[40:43], v2 offset:18432\n"
        "v_max3_u32 v4, v4, v34, v35\n"
        "v_smfmac_f32_16x16x64_bf16 v[12:15], v[36:39], a[56:63], v0\n"
        "ds_read_b128 v[32:35], v1 offset:5120\n"
        "v_max3_u32 v4, v4, v36, v37\n"
        "v_smfmac_f32_16x16x64_bf16 v[16:19], v[36:39], a[200:207], v0\n"
        "ds_read_b128 v[44:47], v2 offset:19456\n"
        "s_waitcnt lgkmcnt(6)\n"
        "v_smfmac_f32_16x16x64_bf16 v[20:23], v[36:39], v[48:55], v0\n"
        "ds_read_b128 v[48:51], v2 offset:20480\n"
        "v_max3_u32 v4, v4, v38, v39\n"
        "v_smfmac_f32_16x16x64_bf16 v[12:15], v[24:27], a[64:71], v0\n"
        "ds_read_b128 v[36:39], v1 offset:5184\n"
        "v_max3_u32 v4, v4, v24, v25\n"
        "v_smfmac_f32_16x16x64_bf16 v[16:19], v[24:27], a[208:215], v0\n"
        "ds_read_b128 v[52:55], v2 offset:21504\n"
        "s_waitcnt lgkmcnt(6)\n"
        "v_smfmac_f32_16x16x64_bf16 v[20:23], v[24:27], v[56:63], v0\n"
        "ds_read_b128 v[56:59], v2 offset:22528\n"
        "v_max3_u32 v4, v4, v26, v27\n"
        "v_smfmac_f32_16x16x64_bf16 v[12:15], v[28:31], a[72:79], v0\n"
        "ds_read_b128 v[24:27], v1 offset:6144\n"
        "v_max3_u32 v4, v4, v28, v29\n"
        "v_smfmac_f32_16x16x64_bf16 v[16:19], v[28:31], a[216:223], v0\n"
        "ds_read_b128 v[60:63], v2 offset:23552\n"
        "s_waitcnt lgkmcnt(6)\n"
        "v_smfmac_f32_16x16x64_bf16 v[20:23], v[28:31], v[40:47], v0\n"
        "ds_read_b128 v[40:43], v2 offset:24576\n"
        "v_max3_u32 v4, v4, v30, v31\n"
        "v_smfmac_f32_16x16x64_bf16 v[12:15], v[32:35], a[80:87], v0\n"
        "ds_read_b128 v[28:31], v1 offset:6208\n"
        "v_max3_u32 v4, v4, v32, v33\n"
        "v_smfmac_f32_16x16x64_bf16 v[16:19], v[32:35], a[224:231], v0\n"
        "ds_read_b128 v[44:47], v2 offset:25600\n"
        "s_waitcnt lgkmcnt(6)\n"
        "v_smfmac_f32_16x16x64_bf16 v[20:23], v[32:35], v[48:55], v0\n"
        "ds_read_b128 v[48:51], v2 offset:26624\n"
        "v_max3_u32 v4, v4, v34, v35\n"
        "v_smfmac_f32_16x16x64_bf16 v[12:15], v[36:39], a[88:95], v0\n"
        "ds_read_b128 v[32:35], v1 offset:7168\n"
        "v_max3_u32 v4, v4, v36, v37\n"
        "v_smfmac_f32_16x16x64_bf16 v[16:19], v[36:39], a[232:239], v0\n"
        "ds_read_b128 v[52:55], v2 offset:27648\n"
        "s_waitcnt lgkmcnt(6)\n"
        "v_smfmac_f32_16x16x64_bf16 v[20:23], v[36:39], v[56:63], v0\n"
        "ds_read_b128 v[56:59], v2 offset:28672\n"
        "v_max3_u32 v4, v4, v38, v39\n"
        "v_smfmac_f32_16x16x64_bf16 v[12:15], v[24:27], a[96:103], v0\n"
        "ds_read_b128 v[36:39], v1 offset:7232\n"
        "v_max3_u32 v4, v4, v24, v25\n"
        "v_smfmac_f32_16x16x64_bf16 v[16:19], v[24:27], a[240:247], v0\n"
        "ds_read_b128 v[60:63], v2 offset:29696\n"
        "s_waitcnt lgkmcnt(6)\n"
        "v_smfmac_f32_16x16x64_bf16 v[20:23], v[24:27], v[40:47], v0\n"
        "ds_read_b128 v[40:43], v2 offset:30720\n"
        "v_max3_u32 v4, v4, v26, v27\n"
        "v_smfmac_f32_16x16x64_bf16 v[12:15], v[28:31], a[104:111], v0\n"
        "s_waitcnt vmcnt(9)\n"
        "v_cmp_eq_u32 vcc, -1, v4\n"
        "s_cbranch_vccnz .Lch_redo_%=\n"
        "s_barrier\n"
        "ds_read_b128 v[24:27], v1 offset:8192\n"
        "v_max3_u32 v4, v4, v28, v29\n"
        "v_smfmac_f32_16x16x64_bf16 v[16:19], v[28:31], a[248:255], v0\n"
        "ds_read_b128 v[44:47], v2 offset:31744\n"
        "s_waitcnt lgkmcnt(6)\n"
        "v_smfmac_f32_16x16x64_bf16 v[20:23], v[28:31], v[48:55], v0\n"
        "v_max3_u32 v4, v4, v30, v31\n"
        "v_smfmac_f32_16x16x64_bf16 v[12:15], v[32:35], a[112:119], v0\n"
        "ds_read_b128 v[28:31], v1 offset:8256\n"
        "v_max3_u32 v4, v4, v32, v33\n"
        "v_smfmac_f32_16x16x64_bf16 v[16:19], v[32:35], v[80:87], v0\n"
        "ds_read_b128 v[48:51], v6 offset:9216\n"
        "s_waitcnt lgkmcnt(5)\n"
        "v_smfmac_f32_16x16x64_bf16 v[20:23], v[32:35], v[56:63], v0\n"
        "ds_read_b128 v[52:55], v6 offset:10240\n"
        "v_max3_u32 v4, v4, v34, v35\n"
        "v_smfmac_f32_16x16x64_bf16 v[12:15], v[36:39], a[120:127], v0\n"
        "s_waitcnt lgkmcnt(4)\n"
        "v_max3_u32 v4, v4, v36, v37\n"
        "v_smfmac_f32_16x16x64_bf16 v[12:15], v[24:27], a[128:135], v0\n"
        "s_waitcnt lgkmcnt(2)\n"
        "v_max3_u32 v4, v4, v38, v39\n"
        "v_smfmac_f32_16x16x64_bf16 v[12:15], v[28:31], a[136:143], v0\n"
        "ds_read_b32 v56, v6 offset:11264\n"
        "v_max3_u32 v4, v4, v24, v25\n"
        "v_smfmac_f32_16x16x64_bf16 v[16:19], v[36:39], v[88:95], v0\n"
        "s_memrealtime s[36:37]\n"
        "v_max3_u32 v4, v4, v26, v27\n"
        "v_smfmac_f32_16x16x64_bf16 v[16:19], v[24:27], v[96:103], v0\n"
        "v_cvt_pk_bf16_f32 v32, v12, v13\n"
        "v_cvt_pk_bf16_f32 v33, v14, v15\n"
        "v_smfmac_f32_16x16x64_bf16 v[16:19], v[28:31], v[104:111], v0\n"
        "s_waitcnt lgkmcnt(0)\n"
        "v_readfirstlane_b32 s81, v56\n"
        "v_mfma_f32_16x16x16_bf16 v[56:59], v[32:33], v[48:49], 0\n"
        "v_max3_u32 v4, v4, v28, v29\n"
        "v_max3_u32 v4, v4, v30, v31\n"
        "v_mfma_f32_16x16x16_bf16 v[60:63], v[32:33], v[50:51], 0\n"
        "v_cvt_pk_bf16_f32 v34, v16, v17\n"
        "v_cvt_pk_bf16_f32 v35, v18, v19\n"
        "v_cmp_eq_u32 vcc, -1, v4\n"
        "s_cbranch_vccnz .Lch_redo_%=\n"
        "v_mfma_f32_16x16x16_bf16 v[12:15], v[34:35], v[52:53], 0\n"
        "v_cvt_pk_bf16_f32 v48, v56, v57\n"
        "v_cvt_pk_bf16_f32 v49, v58, v59\n"
        "s_cmp_eq_u32 s81, s51\n"
        "v_mfma_f32_16x16x16_bf16 v[16:19], v[34:35], v[54:55], 0\n"
        "v_cvt_pk_bf16_f32 v50, v60, v61\n"
        "v_cvt_pk_bf16_f32 v51, v62, v63\n"
        "s_cbranch_scc1 .Lch_slow_%=\n"
        ".Lch_back_%=:\n"
        "s_waitcnt vmcnt(0)\n"
        "v_mfma_f32_16x16x32_bf16 v[56:59], v[64:67], v[48:51], 0\n"
        "v_cvt_pk_bf16_f32 v52, v12, v13\n"
        "v_cvt_pk_bf16_f32 v53, v14, v15\n"
        "s_add_u32 s37, s36, s35\n"
        "v_mfma_f32_16x16x32_bf16 v[60:63], v[68:71], v[48:51], 0\n"
        "v_cvt_pk_bf16_f32 v54, v16, v17\n"
        "v_cvt_pk_bf16_f32 v55, v18, v19\n"
        "v_and_b32 v4, -2, s37\n"
        "s_mov_b64 exec, 1\n"
        "buffer_store_dword v4, off, s[40:43], s46 sc1\n"
        "s_mov_b64 exec, -1\n"
        "v_mfma_f32_16x16x32_bf16 v[12:15], v[52:55], v[48:51], 0\n"
        "v_mov_b64 v[64:65], 0\n"
        "v_mov_b32_dpp v56, v60 quad_perm:[0,1,2,3] row_mask:0xf bank_mask:0xc\n"
        "v_mov_b32_dpp v57, v61 quad_perm:[0,1,2,3] row_mask:0xf bank_mask:0xc\n"
        "v_mov_b32_dpp v58, v62 quad_perm:[0,1,2,3] row_mask:0xf bank_mask:0xc\n"
        "v_mov_b32_dpp v59, v63 quad_perm:[0,1,2,3] row_mask:0xf bank_mask:0xc\n"
        "ds_write_b128 v5, v[56:59]\n"
        "s_nop 1\n"
        "v_mov_b32_dpp v12, v13 quad_perm:[0,1,2,3] row_mask:0xf bank_mask:0xc\n"
        "ds_write_b32 v7, v12\n"
        "v_smfmac_f32_16x16x64_bf16 v[20:23], v[36:39], v[40:47], v0\n"
        "v_mov_b64 v[66:67], 0\n"
        "v_smfmac_f32_16x16x64_bf16 v[20:23], v[24:27], v[240:247], v0\n"
        "v_mov_b64 v[68:69], 0\n"
        "v_smfmac_f32_16x16x64_bf16 v[20:23], v[28:31], v[248:255], v0\n"
        "v_mov_b64 v[70:71], 0\n"
        "s_waitcnt lgkmcnt(0)\n"
        "s_barrier\n"
        "ds_read_b128 v[24:27], v6\n"
        "ds_read_b128 v[28:31], v6 offset:1024\n"
        "ds_read_b128 v[36:39], v6 offset:2048\n"
        "ds_read_b128 v[56:59], v6 offset:3072\n"
        "ds_read_b128 v[16:19], v6 offset:4096\n"
        "buffer_store_dwordx4 v[52:55], v10, s[40:43], s70 offen sc1\n"
        "v_cvt_pk_bf16_f32 v32, v20, v21\n"
        "v_cvt_pk_bf16_f32 v34, v23, v22\n"
        "v_cvt_pk_bf16_f32 v20, v20, v21\n"
        "v_cvt_pk_bf16_f32 v21, v22, v23\n"
        "v_permlane16_swap_b32 v32, v33\n"
        "v_permlane16_swap_b32 v34, v35\n"
        "v_mov_b64 v[48:49], s[44:45]\n"
        "v_mov_b64 v[50:51], s[44:45]\n"
        "buffer_store_dwordx4 v[32:35], v11, s[40:43], s70 offen offset:2048 sc1\n"
        "s_waitcnt lgkmcnt(3)\n"
        "v_pk_add_f32 v[24:25], v[24:25], v[28:29]\n"
        "v_pk_add_f32 v[26:27], v[26:27], v[30:31]\n"
        "s_waitcnt lgkmcnt(1)\n"
        "v_pk_add_f32 v[36:37], v[36:37], v[56:57]\n"
        "v_pk_add_f32 v[38:39], v[38:39], v[58:59]\n"
        "v_pk_add_f32 v[24:25], v[24:25], v[36:37]\n"
        "v_pk_add_f32 v[26:27], v[26:27], v[38:39]\n"
        "s_waitcnt lgkmcnt(0)\n"
        "v_pk_add_f32 v[16:17], v[16:17], v[18:19]\n"
        "v_max3_f32 v36, v24, v25, v26\n"
        "v_add_f32 v16, v16, v17\n"
        "v_max3_f32 v36, v36, v27, v16\n"
        "v_mul_f32 v16, 0x3e0293ee, v16\n"
        "v_mul_f32 v24, 0x3e0293ee, v24\n"
        "v_max_f32_dpp v37, v36, v36 row_ror:8 row_mask:0xf bank_mask:0xf\n"
        "v_max_f32_dpp v38, v36, v36 row_ror:8 row_mask:0xf bank_mask:0xf\n"
        "v_mul_f32 v25, 0x3e0293ee, v25\n"
        "v_mul_f32 v26, 0x3e0293ee, v26\n"
        "v_permlane16_swap_b32 v37, v38\n"
        "v_max_f32 v36, v37, v38\n"
        "v_max_f32 v39, v37, v38\n"
        "v_mul_f32 v27, 0x3e0293ee, v27\n"
        "v_lshlrev_b32 v4, 3, v3\n"
        "v_permlane32_swap_b32 v36, v39\n"
        "v_max_f32 v36, v36, v39\n"
        "v_fmamk_f32 v16, v36, 0xbe0293ee, v16\n"
        "v_exp_f32 v16, v16\n"
        "v_fmamk_f32 v24, v36, 0xbe0293ee, v24\n"
        "v_fmamk_f32 v25, v36, 0xbe0293ee, v25\n"
        "v_mov_b32_dpp v17, v16 row_ror:8 row_mask:0xf bank_mask:0xf\n"
        "v_exp_f32 v24, v24\n"
        "v_cvt_pk_bf16_f32 v17, v16, v17\n"
        "v_exp_f32 v25, v25\n"
        "v_cndmask_b32 v32, v17, 0, s[58:59]\n"
        "v_cndmask_b32 v33, 0, v17, s[58:59]\n"
        "v_fmamk_f32 v26, v36, 0xbe0293ee, v26\n"
        "v_fmamk_f32 v27, v36, 0xbe0293ee, v27\n"
        "v_mfma_f32_16x16x16_bf16 v[12:15], v[48:49], v[32:33], 0\n"
        "v_exp_f32 v26, v26\n"
        "v_exp_f32 v27, v27\n"
        "v_cvt_pk_bf16_f32 v28, v24, v25\n"
        "v_mfma_f32_16x16x16_bf16 v[52:55], v[20:21], v[32:33], 0\n"
        "v_cvt_pk_bf16_f32 v29, v26, v27\n"
        "v_mov_b32_dpp v30, v28 row_ror:8 row_mask:0xf bank_mask:0xf\n"
        "v_and_b32 v4, 0xf80, v4\n"
        "v_mov_b32_dpp v31, v29 row_ror:8 row_mask:0xf bank_mask:0xf\n"
        "v_swap_b32 v76, v78\n"
        "v_swap_b32 v77, v79\n"
        "v_mfma_f32_16x16x32_bf16 v[12:15], v[48:51], v[28:31], v[12:15]\n"
        "v_mfma_f32_16x16x32_bf16 v[56:59], v[72:75], v[28:31], v[52:55]\n"
        "buffer_load_dword v4, v4, s[40:43], s47 offen sc1\n"
        "v_mfma_f32_16x16x32_bf16 v[60:63], v[76:79], v[28:31], v[52:55]\n"
        "v_rcp_f32 v36, v12\n"
        "v_mov_b64 v[16:17], 0\n"
        "v_mov_b64 v[18:19], 0\n"
        "v_mov_b64 v[20:21], 0\n"
        "v_mul_f32 v56, v56, v36\n"
        "v_mul_f32 v57, v57, v36\n"
        "v_mul_f32 v58, v58, v36\n"
        "v_mul_f32 v59, v59, v36\n"
        "v_mul_f32_dpp v56, v60, v36 quad_perm:[0,1,2,3] row_mask:0xf bank_mask:0xc\n"
        "v_mul_f32_dpp v57, v61, v36 quad_perm:[0,1,2,3] row_mask:0xf bank_mask:0xc\n"
        "v_mul_f32_dpp v58, v62, v36 quad_perm:[0,1,2,3] row_mask:0xf bank_mask:0xc\n"
        "v_mul_f32_dpp v59, v63, v36 quad_perm:[0,1,2,3] row_mask:0xf bank_mask:0xc\n"
        "v_cvt_pk_bf16_f32 v56, v56, v57\n"
        "v_cvt_pk_bf16_f32 v57, v58, v59\n"
        "ds_write_b64 v8, v[56:57]\n"
        "v_mov_b64 v[12:13], 0\n"
        "v_mov_b64 v[14:15], 0\n"
        "v_mov_b64 v[22:23], 0\n"
        "s_waitcnt lgkmcnt(0)\n"
        "s_barrier\n"
        "ds_read_b128 v[24:27], v1 offset:5120\n"
        "ds_read_b128 v[28:31], v1 offset:5184\n"
        "ds_read_b128 v[32:35], v1 offset:6144\n"
        "ds_read_b128 v[36:39], v1 offset:6208\n"
        "s_waitcnt lgkmcnt(3)\n"
        "v_smfmac_f32_16x16x64_bf16 v[12:15], v[24:27], v[112:119], v0\n"
        "s_waitcnt lgkmcnt(2)\n"
        "v_smfmac_f32_16x16x64_bf16 v[12:15], v[28:31], v[120:127], v0\n"
        "s_waitcnt lgkmcnt(1)\n"
        "v_smfmac_f32_16x16x64_bf16 v[12:15], v[32:35], v[128:135], v0\n"
        "s_waitcnt lgkmcnt(0)\n"
        "v_smfmac_f32_16x16x64_bf16 v[12:15], v[36:39], v[136:143], v0\n"
        "s_barrier\n"
        "v_smfmac_f32_16x16x64_bf16 v[16:19], v[24:27], v[144:151], v0\n"
        "ds_read_b128 v[40:43], v2 offset:32768\n"
        "v_smfmac_f32_16x16x64_bf16 v[16:19], v[28:31], v[152:159], v0\n"
        "ds_read_b128 v[44:47], v2 offset:33792\n"
        "v_cvt_pk_bf16_f32 v12, v12, v13\n"
        "v_smfmac_f32_16x16x64_bf16 v[16:19], v[32:35], v[160:167], v0\n"
        "ds_read_b128 v[48:51], v2 offset:34816\n"
        "v_cvt_pk_bf16_f32 v13, v15, v14\n"
        "v_smfmac_f32_16x16x64_bf16 v[16:19], v[36:39], v[168:175], v0\n"
        "ds_read_b128 v[52:55], v2 offset:35840\n"
        "v_smfmac_f32_16x16x64_bf16 v[20:23], v[24:27], v[176:183], v0\n"
        "ds_read_b128 v[56:59], v2 offset:36864\n"
        "v_smfmac_f32_16x16x64_bf16 v[20:23], v[28:31], v[184:191], v0\n"
        "v_cvt_pk_bf16_f32 v14, v16, v17\n"
        "v_cvt_pk_bf16_f32 v15, v19, v18\n"
        "buffer_store_dwordx4 v[12:15], v9, s[40:43], s73 offen sc1\n"
        "v_smfmac_f32_16x16x64_bf16 v[20:23], v[32:35], v[192:199], v0\n"
        "ds_read_b128 v[60:63], v2 offset:37888\n"
        "v_smfmac_f32_16x16x64_bf16 v[20:23], v[36:39], v[200:207], v0\n"
        "ds_read_b128 v[72:75], v2 offset:38912\n"
        "v_smfmac_f32_16x16x64_bf16 v[64:67], v[24:27], v[208:215], v0\n"
        "ds_read_b128 v[76:79], v2 offset:39936\n"
        "v_smfmac_f32_16x16x64_bf16 v[64:67], v[28:31], v[216:223], v0\n"
        "v_cvt_pk_bf16_f32 v20, v20, v21\n"
        "v_smfmac_f32_16x16x64_bf16 v[64:67], v[32:35], v[224:231], v0\n"
        "v_cvt_pk_bf16_f32 v21, v23, v22\n"
        "s_cmp_lt_u32 s48, 2\n"
        "v_smfmac_f32_16x16x64_bf16 v[64:67], v[36:39], v[232:239], v0\n"
        "s_cbranch_scc0 .Lch_R23_%=\n"
        "s_waitcnt lgkmcnt(0)\n"
        "v_smfmac_f32_16x16x64_bf16 v[68:71], v[24:27], v[40:47], v0\n"
        "v_smfmac_f32_16x16x64_bf16 v[68:71], v[28:31], v[48:55], v0\n"
        "v_cvt_pk_bf16_f32 v22, v64, v65\n"
        "v_cvt_pk_bf16_f32 v23, v67, v66\n"
        "buffer_store_dwordx4 v[20:23], v9, s[40:43], s74 offen sc1\n"
        "v_smfmac_f32_16x16x64_bf16 v[68:71], v[32:35], v[56:63], v0\n"
        "v_smfmac_f32_16x16x64_bf16 v[68:71], v[36:39], v[72:79], v0\n"
        "v_add_u32 v24, 114688, v3\n"
        "s_nop 6\n"
        "v_cvt_pk_bf16_f32 v68, v68, v69\n"
        "v_cvt_pk_bf16_f32 v69, v71, v70\n"
        "s_nop 0\n"
        "v_permlane32_swap_b32 v68, v70\n"
        "v_permlane32_swap_b32 v69, v71\n"
        "s_mov_b32 exec_hi, 0\n"
        "buffer_store_dwordx4 v[68:71], v9, s[40:43], s75 offen sc1\n"
        "s_memrealtime s[66:67]\n"
        "s_mov_b32 exec_hi, -1\n"
        "ds_read_b128 v[40:43], v24\n"
        "ds_read_b32 v46, v24 offset:1024\n"
        "s_waitcnt vmcnt(3)\n"
        "v_mov_b32 v16, s51\n"
        "s_mov_b64 exec, 1\n"
        "buffer_store_dword v16, off, s[40:43], s79 sc1\n"
        "s_mov_b64 exec, -1\n"
        "s_cmp_eq_u32 s88, 0\n"
        "s_cbranch_scc1 .Lch_R_%=\n"
        "s_waitcnt lgkmcnt(0)\n"
        "s_sub_u32 s65, s66, s36\n"
        "s_min_u32 s65, s65, 100\n"
        "s_lshl_b32 s65, s65, 3\n"
        "s_cmp_eq_u32 s51, 0\n"
        "s_cselect_b32 s38, s65, s38\n"
        "s_sub_u32 s65, s65, s38\n"
        "s_ashr_i32 s65, s65, 2\n"
        "s_add_u32 s38, s38, s65\n"
        "s_lshr_b32 s35, s38, 3\n"
        "s_mov_b32 s65, s63\n"
        ".Lch_d4_%=:\n"
        "s_sub_u32 s65, s65, 1\n"
        "s_cbranch_scc0 .Lch_d4_%=\n"
        ".Lch_idle2_%=:\n"
        "s_mov_b64 exec, 1\n"
        "buffer_load_dword v16, off, s[40:43], s78 sc1\n"
        "s_mov_b64 exec, -1\n"
        "s_add_u32 s51, s51, 1\n"
        "s_add_u32 s80, s80, 3\n"
        "s_bitcmp1_b64 s[52:53], s51\n"
        "s_cselect_b32 s65, 8192, 0\n"
        "s_lshl_b32 s68, s51, 21\n"
        "s_add_u32 s68, s68, s65\n"
        "s_add_u32 s68, s68, 2097152\n"
        "s_add_u32 s70, s68, s84\n"
        "s_add_u32 s69, s68, s83\n"
        "s_add_u32 s47, s68, 2068480\n"
        "s_add_u32 s46, s47, s62\n"
        "s_bitcmp1_b32 s51, 0\n"
        "s_cselect_b32 s77, 589824, 0\n"
        "s_add_u32 s73, s77, s86\n"
        "s_add_u32 s74, s73, 262144\n"
        "s_lshl_b32 s65, s48, 15\n"
        "s_sub_u32 s75, s73, s65\n"
        "s_add_u32 s75, s75, 524288\n"
        "s_waitcnt vmcnt(0) lgkmcnt(0)\n"
        "s_branch .Lch_kv_%=\n"
        ".Lch_R23_%=:\n"
        "s_nop 6\n"
        "v_cvt_pk_bf16_f32 v22, v64, v65\n"
        "v_cvt_pk_bf16_f32 v23, v67, v66\n"
        "buffer_store_dwordx4 v[20:23], v9, s[40:43], s74 offen sc1\n"
        "s_waitcnt vmcnt(2)\n"
        "v_mov_b32 v16, s51\n"
        "s_mov_b64 exec, 1\n"
        "buffer_store_dword v16, off, s[40:43], s79 sc1\n"
        "s_mov_b64 exec, -1\n"
        ".Lch_R_%=:\n"
        "s_add_u32 s76, s77, s87\n"
        "s_mov_b32 s39, s72\n"
        "s_and_b32 s71, s51, 3\n"
        "s_mul_i32 s71, s71, 81920\n"
        "s_add_u32 s72, s71, s85\n"
        "s_mov_b32 s64, 0\n"
        ".Lch_st1_%=:\n"
        "v_cmp_eq_u32 vcc, -1, v4\n"
        "s_cbranch_vccnz .Lch_stwait_%=\n"
        ".Lch_st2_%=:\n"
        "s_memrealtime s[90:91]\n"
        "s_waitcnt lgkmcnt(0)\n"
        "s_sub_u32 s65, s90, s60\n"
        "v_sub_u32 v12, s65, v4\n"
        "v_cmp_gt_i32 vcc, 0, v12\n"
        "s_cbranch_vccnz .Lch_st2_%=\n"
        ".Lch_rl_%=:\n"
        "buffer_load_dwordx4 v[24:27], v3, s[40:43], s76 offen offset:0 sc1\n"
        "buffer_load_dwordx4 v[28:31], v3, s[40:43], s76 offen offset:1024 sc1\n"
        "buffer_load_dwordx4 v[32:35], v3, s[40:43], s76 offen offset:2048 sc1\n"
        "buffer_load_dwordx4 v[36:39], v3, s[40:43], s76 offen offset:3072 sc1\n"
        "v_subrev_u32 v13, s36, v4\n"
        "v_subrev_u32 v14, s36, v4\n"
        "s_nop 0\n"
        "v_max_i32_dpp v13, v13, v13 row_ror:8 row_mask:0xf bank_mask:0xf\n"
        "v_min_i32_dpp v14, v14, v14 row_ror:8 row_mask:0xf bank_mask:0xf\n"
        "s_nop 0\n"
        "v_max_i32_dpp v13, v13, v13 row_ror:4 row_mask:0xf bank_mask:0xf\n"
        "v_min_i32_dpp v14, v14, v14 row_ror:4 row_mask:0xf bank_mask:0xf\n"
        "s_nop 0\n"
        "v_max_i32_dpp v13, v13, v13 row_ror:2 row_mask:0xf bank_mask:0xf\n"
        "v_min_i32_dpp v14, v14, v14 row_ror:2 row_mask:0xf bank_mask:0xf\n"
        "s_nop 0\n"
        "v_max_i32_dpp v13, v13, v13 row_ror:1 row_mask:0xf bank_mask:0xf\n"
        "v_min_i32_dpp v14, v14, v14 row_ror:1 row_mask:0xf bank_mask:0xf\n"
        "v_readlane_b32 s65, v13, 0\n"
        "v_readlane_b32 s66, v13, 16\n"
        "v_readlane_b32 s90, v14, 0\n"
        "v_readlane_b32 s91, v14, 16\n"
        "s_max_i32 s65, s65, s66\n"
        "s_min_i32 s90, s90, s91\n"
        "s_sub_u32 s90, s65, s90\n"
        "s_add_u32 s65, s65, s36\n"
        "s_add_u32 s65, s65, s34\n"
        "s_cmp_gt_u32 s90, s56\n"
        "s_cselect_b32 s90, s57, 0\n"
        "s_add_u32 s82, s65, s90\n"
        "s_waitcnt vmcnt(3)\n"
        "v_mfma_f32_16x16x32_bf16 v[16:19], v[24:27], v[40:43], 0\n"
        "v_max_u32 v12, v24, v25\n"
        "v_max3_u32 v12, v12, v26, v27\n"
        "s_waitcnt vmcnt(2)\n"
        "v_mfma_f32_16x16x32_bf16 v[16:19], v[28:31], v[40:43], v[16:19]\n"
        "v_max3_u32 v12, v12, v28, v29\n"
        "v_max3_u32 v12, v12, v30, v31\n"
        "s_waitcnt vmcnt(1)\n"
        "v_mfma_f32_16x16x32_bf16 v[16:19], v[32:35], v[40:43], v[16:19]\n"
        "v_max3_u32 v12, v12, v32, v33\n"
        "v_max3_u32 v12, v12, v34, v35\n"
        "s_waitcnt vmcnt(0)\n"
        "v_mfma_f32_16x16x32_bf16 v[16:19], v[36:39], v[40:43], v[16:19]\n"
        "v_max3_u32 v12, v12, v36, v37\n"
        "v_max3_u32 v12, v12, v38, v39\n"
        "v_cmp_eq_u32 vcc, -1, v12\n"
        "s_cbranch_vccnz .Lch_rretry_%=\n"
        "s_nop 3\n"
        "v_permlane32_swap_b32 v16, v18\n"
        "v_permlane32_swap_b32 v17, v19\n"
        "v_add_f32 v16, v16, v18\n"
        "v_add_f32 v17, v17, v19\n"
        "v_cvt_pk_bf16_f32 v20, v16, v17\n"
        "v_cvt_pk_bf16_f32 v22, v16, v17\n"
        "s_nop 1\n"
        "v_permlane16_swap_b32 v20, v22\n"
        "s_nop 1\n"
        "v_permlane32_swap_b32 v20, v21\n"
        "v_permlane32_swap_b32 v22, v23\n"
        "buffer_store_dwordx4 v[20:23], v46, s[40:43], s72 offen sc1\n"
        "s_mov_b64 exec, 1\n"
        "buffer_load_dword v16, off, s[40:43], s78 sc1\n"
        "s_mov_b64 exec, -1\n"
        "v_mov_b64 v[12:13], -1\n"
        "v_mov_b64 v[14:15], -1\n"
        "s_add_u32 s51, s51, 1\n"
        "s_add_u32 s80, s80, 3\n"
        "s_bitcmp1_b64 s[52:53], s51\n"
        "s_cselect_b32 s65, 8192, 0\n"
        "s_lshl_b32 s68, s51, 21\n"
        "s_add_u32 s68, s68, s65\n"
        "s_add_u32 s68, s68, 2097152\n"
        "s_add_u32 s70, s68, s84\n"
        "s_add_u32 s69, s68, s83\n"
        "s_add_u32 s47, s68, 2068480\n"
        "s_add_u32 s46, s47, s62\n"
        "s_bitcmp1_b32 s51, 0\n"
        "s_cselect_b32 s77, 589824, 0\n"
        "s_add_u32 s73, s77, s86\n"
        "s_add_u32 s74, s73, 262144\n"
        "s_lshl_b32 s65, s48, 15\n"
        "s_sub_u32 s75, s73, s65\n"
        "s_add_u32 s75, s75, 524288\n"
        "s_cmp_eq_u32 s48, 0\n"
        "s_cbranch_scc1 .Lch_nog_%=\n"
        "s_and_b32 s66, s48, 1\n"
        "s_lshl_b32 s66, s66, 12\n"
        "s_add_u32 m0, s66, 151552\n"
        "s_add_u32 s66, s54, s71\n"
        ".Lch_gw_%=:\n"
        "s_memrealtime s[90:91]\n"
        "s_waitcnt lgkmcnt(0)\n"
        "s_sub_u32 s65, s90, s82\n"
        "s_cmp_lt_i32 s65, 0\n"
        "s_cbranch_scc1 .Lch_gw_%=\n"
        "s_mov_b32 s65, s61\n"
        ".Lch_d2_%=:\n"
        "s_sub_u32 s65, s65, 1\n"
        "s_cbranch_scc0 .Lch_d2_%=\n"
        "buffer_load_dwordx4 v3, s[40:43], s66 offen offset:0 sc1 lds\n"
        "buffer_load_dwordx4 v3, s[40:43], s66 offen offset:1024 sc1 lds\n"
        "buffer_load_dwordx4 v3, s[40:43], s66 offen offset:2048 sc1 lds\n"
        "buffer_load_dwordx4 v3, s[40:43], s66 offen offset:3072 sc1 lds\n"
        "s_cmp_eq_u32 s48, 2\n"
        "s_cbranch_scc1 .Lch_nog3a_%=\n"
        "s_mov_b32 m0, 159744\n"
        "s_add_u32 s66, s71, s55\n"
        "buffer_load_dwordx4 v45, s[40:43], s66 offen sc1 lds\n"
        ".Lch_nog3a_%=:\n"
        "buffer_store_dwordx4 v[12:15], v3, s[40:43], s76 offen offset:0 sc1\n"
        "buffer_store_dwordx4 v[12:15], v3, s[40:43], s76 offen offset:1024 sc1\n"
        "buffer_store_dwordx4 v[12:15], v3, s[40:43], s76 offen offset:2048 sc1\n"
        "buffer_store_dwordx4 v[12:15], v3, s[40:43], s76 offen offset:3072 sc1\n"
        "buffer_store_dwordx4 v[12:15], v46, s[40:43], s39 offen sc1\n"
        "s_waitcnt vmcnt(10)\n"
        "s_cmp_eq_u32 s48, 3\n"
        "s_cbranch_scc1 .Lch_kv_%=\n"
        "s_waitcnt vmcnt(9)\n"
        "s_branch .Lch_kv_%=\n"
        ".Lch_nog_%=:\n"
        "buffer_store_dwordx4 v[12:15], v3, s[40:43], s76 offen offset:0 sc1\n"
        "buffer_store_dwordx4 v[12:15], v3, s[40:43], s76 offen offset:1024 sc1\n"
        "buffer_store_dwordx4 v[12:15], v3, s[40:43], s76 offen offset:2048 sc1\n"
        "buffer_store_dwordx4 v[12:15], v3, s[40:43], s76 offen offset:3072 sc1\n"
        "buffer_store_dwordx4 v[12:15], v46, s[40:43], s39 offen sc1\n"
        "s_waitcnt vmcnt(5)\n"
        ".Lch_kv_%=:\n"
        "v_readfirstlane_b32 s65, v16\n"
        "s_nop 0\n"
        "s_cmp_ge_i32 s65, s80\n"
        "s_cselect_b32 s81, 0, 1\n"
        "s_cmp_eq_u32 s51, 63\n"
        "s_cselect_b32 s81, 1, s81\n"
        "s_cmp_eq_u32 s81, 0\n"
        "s_cselect_b32 s66, s69, 2147483648\n"
        "s_cbranch_scc1 .Lch_fast_%=\n"
        "v_mov_b32 v16, s51\n"
        "ds_write_b32 v6, v16 offset:11264\n"
        ".Lch_fast_%=:\n"
        "s_add_u32 s89, s66, 524288\n"
        "buffer_load_dwordx4 v[64:67], v3, s[40:43], s66 offen sc1\n"
        "buffer_load_dwordx4 v[68:71], v3, s[40:43], s66 offen offset:1024 sc1\n"
        "buffer_load_dwordx4 v[72:75], v3, s[40:43], s89 offen sc1\n"
        "buffer_load_dwordx4 v[76:79], v3, s[40:43], s89 offen offset:1024 sc1\n"
        "ds_read_b128 v[40:43], v2 offset:0\n"
        "ds_read_b128 v[44:47], v2 offset:1024\n"
        "ds_read_b128 v[48:51], v2 offset:2048\n"
        "ds_read_b128 v[52:55], v2 offset:3072\n"
        "ds_read_b128 v[56:59], v2 offset:4096\n"
        "ds_read_b128 v[60:63], v2 offset:5120\n"
        "v_mov_b64 v[12:13], 0\n"
        "v_mov_b64 v[14:15], 0\n"
        "v_mov_b64 v[16:17], 0\n"
        "v_mov_b64 v[18:19], 0\n"
        "v_mov_b64 v[20:21], 0\n"
        "v_mov_b64 v[22:23], 0\n"
        "v_mov_b32 v4, 0\n"
        "s_cmp_eq_u32 s48, 3\n"
        "s_cbranch_scc1 .Lch_A_%=\n"
        "s_waitcnt vmcnt(9)\n"
        ".Lch_A_%=:\n"
        "s_waitcnt lgkmcnt(0)\n"
        "s_barrier\n"
        "s_branch .Lch_Q_%=\n"
        ".Lch_rretry_%=:\n"
        "s_add_u32 s64, s64, 1\n"
        "s_and_b32 s65, s64, 255\n"
        "s_cbranch_scc1 .Lch_rr_go_%=\n"
        "s_mov_b32 s65, 40960\n"
        "s_cmp_lt_u32 s64, 1048576\n"
        "s_cbranch_scc1 .Lch_rr_chk_%=\n"
        "v_mov_b32 v12, 1\n"
        "buffer_store_dword v12, off, s[40:43], s65 sc1\n"
        "s_waitcnt vmcnt(0)\n"
        ".Lch_rr_chk_%=:\n"
        "buffer_load_dword v12, off, s[40:43], s65 sc1\n"
        "s_waitcnt vmcnt(0)\n"
        "v_readfirstlane_b32 s65, v12\n"
        "s_nop 0\n"
        "s_cmp_lg_u32 s65, 1\n"
        "s_cbranch_scc1 .Lch_rr_go_%=\n"
        "s_endpgm\n"
        ".Lch_rr_go_%=:\n"
        "s_branch .Lch_rl_%=\n"
        ".Lch_stwait_%=:\n"
        "s_add_u32 s64, s64, 1\n"
        "s_and_b32 s65, s64, 255\n"
        "s_cbranch_scc1 .Lch_sw_go_%=\n"
        "s_mov_b32 s65, 40960\n"
        "s_cmp_lt_u32 s64, 1048576\n"
        "s_cbranch_scc1 .Lch_sw_chk_%=\n"
        "v_mov_b32 v12, 1\n"
        "buffer_store_dword v12, off, s[40:43], s65 sc1\n"
        "s_waitcnt vmcnt(0)\n"
        ".Lch_sw_chk_%=:\n"
        "buffer_load_dword v12, off, s[40:43], s65 sc1\n"
        "s_waitcnt vmcnt(0)\n"
        "v_readfirstlane_b32 s65, v12\n"
        "s_nop 0\n"
        "s_cmp_lg_u32 s65, 1\n"
        "s_cbranch_scc1 .Lch_sw_go_%=\n"
        "s_endpgm\n"
        ".Lch_sw_go_%=:\n"
        "v_lshlrev_b32 v4, 3, v3\n"
        "v_and_b32 v4, 0xf80, v4\n"
        "buffer_load_dword v4, v4, s[40:43], s47 offen sc1\n"
        "s_waitcnt vmcnt(0)\n"
        "s_branch .Lch_st1_%=\n"
        ".Lch_redo_%=:\n"
        "s_waitcnt vmcnt(0) lgkmcnt(0)\n"
        "s_barrier\n"
        "s_add_u32 s64, s64, 1\n"
        "s_and_b32 s65, s64, 255\n"
        "s_cbranch_scc1 .Lch_rd_go_%=\n"
        "s_mov_b32 s65, 40960\n"
        "s_cmp_lt_u32 s64, 1048576\n"
        "s_cbranch_scc1 .Lch_rd_chk_%=\n"
        "v_mov_b32 v24, 1\n"
        "buffer_store_dword v24, off, s[40:43], s65 sc1\n"
        "s_waitcnt vmcnt(0)\n"
        ".Lch_rd_chk_%=:\n"
        "buffer_load_dword v24, off, s[40:43], s65 sc1\n"
        "s_waitcnt vmcnt(0)\n"
        "v_readfirstlane_b32 s65, v24\n"
        "s_nop 0\n"
        "s_cmp_lg_u32 s65, 1\n"
        "s_cbranch_scc1 .Lch_rd_go_%=\n"
        "s_endpgm\n"
        ".Lch_rd_go_%=:\n"
        "s_cmp_lt_u32 s48, 2\n"
        "s_cbranch_scc1 .Lch_rd2_%=\n"
        "ds_read_b128 v[44:47], v2 offset:33792\n"
        "s_waitcnt lgkmcnt(0)\n"
        "s_and_b32 s66, s48, 1\n"
        "s_lshl_b32 s66, s66, 12\n"
        "s_add_u32 m0, s66, 151552\n"
        "s_add_u32 s66, s54, s71\n"
        "buffer_load_dwordx4 v3, s[40:43], s66 offen offset:0 sc1 lds\n"
        "buffer_load_dwordx4 v3, s[40:43], s66 offen offset:1024 sc1 lds\n"
        "buffer_load_dwordx4 v3, s[40:43], s66 offen offset:2048 sc1 lds\n"
        "buffer_load_dwordx4 v3, s[40:43], s66 offen offset:3072 sc1 lds\n"
        "s_cmp_eq_u32 s48, 2\n"
        "s_cbranch_scc1 .Lch_nog3b_%=\n"
        "s_mov_b32 m0, 159744\n"
        "s_add_u32 s66, s71, s55\n"
        "buffer_load_dwordx4 v45, s[40:43], s66 offen sc1 lds\n"
        ".Lch_nog3b_%=:\n"
        ".Lch_rd2_%=:\n"
        "s_nop 7\n"
        "ds_read_b128 v[40:43], v2 offset:0\n"
        "ds_read_b128 v[44:47], v2 offset:1024\n"
        "ds_read_b128 v[48:51], v2 offset:2048\n"
        "ds_read_b128 v[52:55], v2 offset:3072\n"
        "ds_read_b128 v[56:59], v2 offset:4096\n"
        "ds_read_b128 v[60:63], v2 offset:5120\n"
        "v_mov_b64 v[12:13], 0\n"
        "v_mov_b64 v[14:15], 0\n"
        "v_mov_b64 v[16:17], 0\n"
        "v_mov_b64 v[18:19], 0\n"
        "v_mov_b64 v[20:21], 0\n"
        "v_mov_b64 v[22:23], 0\n"
        "v_mov_b32 v4, 0\n"
        "s_waitcnt vmcnt(0)\n"
        "s_branch .Lch_A_%=\n"
        ".Lch_slow_%=:\n"
        "s_nop 7\n"
        "v_cvt_pk_bf16_f32 v52, v12, v13\n"
        "v_cvt_pk_bf16_f32 v53, v14, v15\n"
        "v_cvt_pk_bf16_f32 v54, v16, v17\n"
        "v_cvt_pk_bf16_f32 v55, v18, v19\n"
        "v_mov_b64 v[56:57], v[20:21]\n"
        "v_mov_b64 v[58:59], v[22:23]\n"
        "buffer_store_dwordx4 v[52:55], v10, s[40:43], s70 offen sc1\n"
        "s_nop 0\n"
        "v_smfmac_f32_16x16x64_bf16 v[56:59], v[36:39], v[40:47], v0\n"
        "v_smfmac_f32_16x16x64_bf16 v[56:59], v[24:27], v[240:247], v0\n"
        "v_smfmac_f32_16x16x64_bf16 v[56:59], v[28:31], v[248:255], v0\n"
        "s_nop 7\n"
        "v_cvt_pk_bf16_f32 v32, v56, v57\n"
        "v_cvt_pk_bf16_f32 v34, v59, v58\n"
        "s_nop 1\n"
        "v_permlane16_swap_b32 v32, v33\n"
        "v_permlane16_swap_b32 v34, v35\n"
        "buffer_store_dwordx4 v[32:35], v11, s[40:43], s70 offen offset:2048 sc1\n"
        "s_waitcnt vmcnt(0)\n"
        "v_mov_b32 v60, s51\n"
        "s_mov_b64 exec, 1\n"
        "buffer_store_dword v60, off, s[40:43], s79 sc1\n"
        "s_mov_b64 exec, -1\n"
        "s_cmp_eq_u32 s51, 63\n"
        "s_cbranch_scc0 .Lch_slow2_%=\n"
        "s_endpgm\n"
        ".Lch_slow2_%=:\n"
        "s_mov_b32 s64, 0\n"
        ".Lch_kvw_top_%=:\n"
        "s_mov_b64 exec, 1\n"
        "buffer_load_dword v60, off, s[40:43], s78 sc1\n"
        "s_mov_b64 exec, -1\n"
        "s_waitcnt vmcnt(0)\n"
        "v_cmp_gt_i32 vcc, s80, v60\n"
        "s_nop 0\n"
        "s_and_b64 vcc, vcc, 1\n"
        "s_cbranch_vccz .Lch_kvw_ok_%=\n"
        "s_add_u32 s64, s64, 1\n"
        "s_and_b32 s65, s64, 255\n"
        "s_cbranch_scc1 .Lch_kvw_go_%=\n"
        "s_mov_b32 s65, 40960\n"
        "s_cmp_lt_u32 s64, 1048576\n"
        "s_cbranch_scc1 .Lch_kvw_chk_%=\n"
        "v_mov_b32 v60, 1\n"
        "buffer_store_dword v60, off, s[40:43], s65 sc1\n"
        "s_waitcnt vmcnt(0)\n"
        ".Lch_kvw_chk_%=:\n"
        "buffer_load_dword v60, off, s[40:43], s65 sc1\n"
        "s_waitcnt vmcnt(0)\n"
        "v_readfirstlane_b32 s65, v60\n"
        "s_nop 0\n"
        "s_cmp_lg_u32 s65, 1\n"
        "s_cbranch_scc1 .Lch_kvw_go_%=\n"
        "s_endpgm\n"
        ".Lch_kvw_go_%=:\n"
        "s_sleep 1\n"
        "s_branch .Lch_kvw_top_%=\n"
        ".Lch_kvw_ok_%=:\n"
        "s_add_u32 s89, s69, 524288\n"
        "buffer_load_dwordx4 v[64:67], v3, s[40:43], s69 offen sc1\n"
        "buffer_load_dwordx4 v[68:71], v3, s[40:43], s69 offen offset:1024 sc1\n"
        "buffer_load_dwordx4 v[72:75], v3, s[40:43], s89 offen sc1\n"
        "buffer_load_dwordx4 v[76:79], v3, s[40:43], s89 offen offset:1024 sc1\n"
        "s_waitcnt vmcnt(0)\n"
        "s_barrier\n"
        "s_memrealtime s[36:37]\n"
        "s_waitcnt lgkmcnt(0)\n"
        "s_branch .Lch_back_%=\n"
        :: "s"(p.ws), "s"(p.Wq), "s"(p.Wk), "s"(p.Wv), "s"(p.Wo), "s"(p.mem0), "s"(pair(w, h)), "s"(pair(x4, pr)), "s"(p.tune0), "s"(p.tune1),
           "v"(pair(idx, va)), "v"(pair(wb + lane * 16, lane * 16)), "v"(pair(0, spr + w * 1024)), "v"(pair(spr, spr + 4096 + 4 * w)),
           "v"(pair(CH_SO + (w >> 1) * 1024 + (w & 1) * 64 + img(c & 7) + (c >> 3) * 8, pub)), "v"(pair(kmo, vmo)), "v"(pair(qo, oo)), "v"(pair(tr, m0))
        : "v0", "v1", "v2", "v3", "v4", "v5", "v6", "v7", "v8", "v9", "v10", "v11", "v12", "v13", "v14", "v15", "v16", "v17", "v18", "v19", "v20", "v21", "v22", "v23", "v24", "v25", "v26", "v27", "v28", "v29", "v30", "v31", "v32", "v33", "v34", "v35", "v36", "v37", "v38", "v39", "v40", "v41", "v42", "v43", "v44", "v45", "v46", "v47", "v48", "v49", "v50", "v51", "v52", "v53", "v54", "v55", "v56", "v57", "v58", "v59", "v60", "v61", "v62", "v63", "v64", "v65", "v66", "v67", "v68", "v69", "v70", "v71", "v72", "v73", "v74", "v75", "v76", "v77", "v78", "v79", "v255", "a255", "s34", "s35", "s36", "s37", "s38", "s39", "s40", "s41", "s42", "s43", "s44", "s45", "s46", "s47", "s48", "s49", "s50", "s51", "s52", "s53", "s54", "s55", "s56", "s57", "s58", "s59", "s60", "s61", "s62", "s63", "s64", "s65", "s66", "s67", "s68", "s69", "s70", "s71", "s72", "s73", "s74", "s75", "s76", "s77", "s78", "s79", "s80", "s81", "s82", "s83", "s84", "s85", "s86", "s87", "s88", "s89", "s90", "s91", "s92", "s93", "s94", "s95", "s96", "s97", "s98", "s99", "vcc", "scc", "memory");
    __builtin_unreachable();
}

constexpr unsigned flag(unsigned i) { return spread(LF0 + i * 128); }
#define GEMM_HEAD const unsigned lane = threadIdx.x & 63, i = lane & 15, g = lane >> 4, q = i >> 2, pp = lane & 3; const int w = rfl(threadIdx.x >> 6)
__device__ __forceinline__ void role_s1(const Params& p, int h, int x4, int pr, int delay) {
    GEMM_HEAD;
    const unsigned vo = (8 * g + q) * 8192 + (128 * h + 32 * w) * 2 + (pp ^ (2 * (g & 1))) * 16;
    const unsigned tr = w * 40960 + (4 * g + q) * 64 + pp * 8;
    asm volatile(
        "s_nop 4\n"
        "s_mov_b64 s[40:41], %0\n"
        "s_and_b32 s41, s41, 0xffff\n"
        "s_mov_b32 s42, 236978176\n"
        "s_mov_b32 s43, 0x20000\n"
        "s_mov_b64 s[48:49], %4\n"
        "s_mov_b64 s[56:57], %5\n"
        "s_mov_b64 s[58:59], %6\n"
        "s_mov_b64 s[60:61], %7\n"
        "v_mov_b64 v[0:1], %8\n"
        "v_mov_b64 v[2:3], %9\n"
        "v_mov_b64 v[4:5], %10\n"
        "v_mov_b64 v[6:7], %11\n"
        "v_mov_b64 v[8:9], %12\n"
        "s_mov_b32 s47, 0x20000\n"
        "s_mul_i32 s79, s48, 40960\n"
        ".Ls1_start_%=:\n"
        "s_sub_u32 s49, s49, 1\n"
        "s_cbranch_scc1 .Ls1_start2_%=\n"
        "s_sleep 16\n"
        "s_branch .Ls1_start_%=\n"
        ".Ls1_start2_%=:\n"
        "s_mov_b64 s[44:45], %3\n"
        "s_and_b32 s45, s45, 0xffff\n"
        "s_mov_b32 s46, 4718592\n"
        "s_add_u32 m0, s79, 0\n"
        "s_mov_b32 s65, 0\n"
        "buffer_load_dwordx4 v6, s[44:47], s65 offen lds\n"
        "s_mov_b32 s65, 31744\n"
        "buffer_load_dwordx4 v6, s[44:47], s65 offen offset:1024 lds\n"
        "s_add_u32 m0, s79, 2048\n"
        "s_mov_b32 s65, 262144\n"
        "buffer_load_dwordx4 v6, s[44:47], s65 offen lds\n"
        "s_mov_b32 s65, 293888\n"
        "buffer_load_dwordx4 v6, s[44:47], s65 offen offset:1024 lds\n"
        "s_add_u32 m0, s79, 4096\n"
        "s_mov_b32 s65, 524288\n"
        "buffer_load_dwordx4 v6, s[44:47], s65 offen lds\n"
        "s_mov_b32 s65, 556032\n"
        "buffer_load_dwordx4 v6, s[44:47], s65 offen offset:1024 lds\n"
        "s_add_u32 m0, s79, 6144\n"
        "s_mov_b32 s65, 786432\n"
        "buffer_load_dwordx4 v6, s[44:47], s65 offen lds\n"
        "s_mov_b32 s65, 818176\n"
        "buffer_load_dwordx4 v6, s[44:47], s65 offen offset:1024 lds\n"
        "s_add_u32 m0, s79, 8192\n"
        "s_mov_b32 s65, 1048576\n"
        "buffer_load_dwordx4 v6, s[44:47], s65 offen lds\n"
        "s_mov_b32 s65, 1080320\n"
        "buffer_load_dwordx4 v6, s[44:47], s65 offen offset:1024 lds\n"
        "s_add_u32 m0, s79, 10240\n"
        "s_mov_b32 s65, 1310720\n"
        "buffer_load_dwordx4 v6, s[44:47], s65 offen lds\n"
        "s_mov_b32 s65, 1342464\n"
        "buffer_load_dwordx4 v6, s[44:47], s65 offen offset:1024 lds\n"
        "s_add_u32 m0, s79, 12288\n"
        "s_mov_b32 s65, 1572864\n"
        "buffer_load_dwordx4 v6, s[44:47], s65 offen lds\n"
        "s_mov_b32 s65, 1604608\n"
        "buffer_load_dwordx4 v6, s[44:47], s65 offen offset:1024 lds\n"
        "s_add_u32 m0, s79, 14336\n"
        "s_mov_b32 s65, 1835008\n"
        "buffer_load_dwordx4 v6, s[44:47], s65 offen lds\n"
        "s_mov_b32 s65, 1866752\n"
        "buffer_load_dwordx4 v6, s[44:47], s65 offen offset:1024 lds\n"
        "s_add_u32 m0, s79, 16384\n"
        "s_mov_b32 s65, 2097152\n"
        "buffer_load_dwordx4 v6, s[44:47], s65 offen lds\n"
        "s_mov_b32 s65, 2128896\n"
        "buffer_load_dwordx4 v6, s[44:47], s65 offen offset:1024 lds\n"
        "s_add_u32 m0, s79, 18432\n"
        "s_mov_b32 s65, 2359296\n"
        "buffer_load_dwordx4 v6, s[44:47], s65 offen lds\n"
        "s_mov_b32 s65, 2391040\n"
        "buffer_load_dwordx4 v6, s[44:47], s65 offen offset:1024 lds\n"
        "s_add_u32 m0, s79, 20480\n"
        "s_mov_b32 s65, 2621440\n"
        "buffer_load_dwordx4 v6, s[44:47], s65 offen lds\n"
        "s_mov_b32 s65, 2653184\n"
        "buffer_load_dwordx4 v6, s[44:47], s65 offen offset:1024 lds\n"
        "s_add_u32 m0, s79, 22528\n"
        "s_mov_b32 s65, 2883584\n"
        "buffer_load_dwordx4 v6, s[44:47], s65 offen lds\n"
        "s_mov_b32 s65, 2915328\n"
        "buffer_load_dwordx4 v6, s[44:47], s65 offen offset:1024 lds\n"
        "s_add_u32 m0, s79, 24576\n"
        "s_mov_b32 s65, 3145728\n"
        "buffer_load_dwordx4 v6, s[44:47], s65 offen lds\n"
        "s_mov_b32 s65, 3177472\n"
        "buffer_load_dwordx4 v6, s[44:47], s65 offen offset:1024 lds\n"
        "s_add_u32 m0, s79, 26624\n"
        "s_mov_b32 s65, 3407872\n"
        "buffer_load_dwordx4 v6, s[44:47], s65 offen lds\n"
        "s_mov_b32 s65, 3439616\n"
        "buffer_load_dwordx4 v6, s[44:47], s65 offen offset:1024 lds\n"
        "s_add_u32 m0, s79, 28672\n"
        "s_mov_b32 s65, 3670016\n"
        "buffer_load_dwordx4 v6, s[44:47], s65 offen lds\n"
        "s_mov_b32 s65, 3701760\n"
        "buffer_load_dwordx4 v6, s[44:47], s65 offen offset:1024 lds\n"
        "s_add_u32 m0, s79, 30720\n"
        "s_mov_b32 s65, 3932160\n"
        "buffer_load_dwordx4 v6, s[44:47], s65 offen lds\n"
        "s_mov_b32 s65, 3963904\n"
        "buffer_load_dwordx4 v6, s[44:47], s65 offen offset:1024 lds\n"
        "s_waitcnt vmcnt(30)\n"
        "ds_read_b64_tr_b16 v[112:113], v8 offset:0\n"
        "ds_read_b64_tr_b16 v[114:115], v8 offset:1024\n"
        "ds_read_b64_tr_b16 v[116:117], v9 offset:0\n"
        "ds_read_b64_tr_b16 v[118:119], v9 offset:1024\n"
        "s_waitcnt vmcnt(28)\n"
        "ds_read_b64_tr_b16 v[120:121], v8 offset:2048\n"
        "ds_read_b64_tr_b16 v[122:123], v8 offset:3072\n"
        "ds_read_b64_tr_b16 v[124:125], v9 offset:2048\n"
        "ds_read_b64_tr_b16 v[126:127], v9 offset:3072\n"
        "s_waitcnt lgkmcnt(4)\n"
        "s_add_u32 m0, s79, 0\n"
        "s_mov_b32 s65, 4194304\n"
        "buffer_load_dwordx4 v6, s[44:47], s65 offen lds\n"
        "s_mov_b32 s65, 4226048\n"
        "buffer_load_dwordx4 v6, s[44:47], s65 offen offset:1024 lds\n"
        "s_waitcnt vmcnt(28)\n"
        "ds_read_b64_tr_b16 v[128:129], v8 offset:4096\n"
        "ds_read_b64_tr_b16 v[130:131], v8 offset:5120\n"
        "ds_read_b64_tr_b16 v[132:133], v9 offset:4096\n"
        "ds_read_b64_tr_b16 v[134:135], v9 offset:5120\n"
        "s_waitcnt lgkmcnt(4)\n"
        "s_add_u32 m0, s79, 2048\n"
        "s_mov_b32 s65, 4456448\n"
        "buffer_load_dwordx4 v6, s[44:47], s65 offen lds\n"
        "s_mov_b32 s65, 4488192\n"
        "buffer_load_dwordx4 v6, s[44:47], s65 offen offset:1024 lds\n"
        "s_waitcnt vmcnt(28)\n"
        "ds_read_b64_tr_b16 v[136:137], v8 offset:6144\n"
        "ds_read_b64_tr_b16 v[138:139], v8 offset:7168\n"
        "ds_read_b64_tr_b16 v[140:141], v9 offset:6144\n"
        "ds_read_b64_tr_b16 v[142:143], v9 offset:7168\n"
        "s_waitcnt lgkmcnt(4)\n"
        "s_mov_b64 s[44:45], %1\n"
        "s_and_b32 s45, s45, 0xffff\n"
        "s_mov_b32 s46, 4718592\n"
        "s_add_u32 m0, s79, 4096\n"
        "s_mov_b32 s65, 0\n"
        "buffer_load_dwordx4 v6, s[44:47], s65 offen lds\n"
        "s_mov_b32 s65, 31744\n"
        "buffer_load_dwordx4 v6, s[44:47], s65 offen offset:1024 lds\n"
        "s_waitcnt vmcnt(28)\n"
        "ds_read_b64_tr_b16 v[144:145], v8 offset:8192\n"
        "ds_read_b64_tr_b16 v[146:147], v8 offset:9216\n"
        "ds_read_b64_tr_b16 v[148:149], v9 offset:8192\n"
        "ds_read_b64_tr_b16 v[150:151], v9 offset:9216\n"
        "s_waitcnt lgkmcnt(4)\n"
        "s_add_u32 m0, s79, 6144\n"
        "s_mov_b32 s65, 262144\n"
        "buffer_load_dwordx4 v6, s[44:47], s65 offen lds\n"
        "s_mov_b32 s65, 293888\n"
        "buffer_load_dwordx4 v6, s[44:47], s65 offen offset:1024 lds\n"
        "s_waitcnt vmcnt(28)\n"
        "ds_read_b64_tr_b16 v[152:153], v8 offset:10240\n"
        "ds_read_b64_tr_b16 v[154:155], v8 offset:11264\n"
        "ds_read_b64_tr_b16 v[156:157], v9 offset:10240\n"
        "ds_read_b64_tr_b16 v[158:159], v9 offset:11264\n"
        "s_waitcnt lgkmcnt(4)\n"
        "s_add_u32 m0, s79, 8192\n"
        "s_mov_b32 s65, 524288\n"
        "buffer_load_dwordx4 v6, s[44:47], s65 offen lds\n"
        "s_mov_b32 s65, 556032\n"
        "buffer_load_dwordx4 v6, s[44:47], s65 offen offset:1024 lds\n"
        "s_waitcnt vmcnt(28)\n"
        "ds_read_b64_tr_b16 v[160:161], v8 offset:12288\n"
        "ds_read_b64_tr_b16 v[162:163], v8 offset:13312\n"
        "ds_read_b64_tr_b16 v[164:165], v9 offset:12288\n"
        "ds_read_b64_tr_b16 v[166:167], v9 offset:13312\n"
        "s_waitcnt lgkmcnt(4)\n"
        "s_add_u32 m0, s79, 10240\n"
        "s_mov_b32 s65, 786432\n"
        "buffer_load_dwordx4 v6, s[44:47], s65 offen lds\n"
        "s_mov_b32 s65, 818176\n"
        "buffer_load_dwordx4 v6, s[44:47], s65 offen offset:1024 lds\n"
        "s_waitcnt vmcnt(28)\n"
        "ds_read_b64_tr_b16 v[168:169], v8 offset:14336\n"
        "ds_read_b64_tr_b16 v[170:171], v8 offset:15360\n"
        "ds_read_b64_tr_b16 v[172:173], v9 offset:14336\n"
        "ds_read_b64_tr_b16 v[174:175], v9 offset:15360\n"
        "s_waitcnt lgkmcnt(4)\n"
        "s_add_u32 m0, s79, 12288\n"
        "s_mov_b32 s65, 1048576\n"
        "buffer_load_dwordx4 v6, s[44:47], s65 offen lds\n"
        "s_mov_b32 s65, 1080320\n"
        "buffer_load_dwordx4 v6, s[44:47], s65 offen offset:1024 lds\n"
        "s_waitcnt vmcnt(28)\n"
        "ds_read_b64_tr_b16 v[176:177], v8 offset:16384\n"
        "ds_read_b64_tr_b16 v[178:179], v8 offset:17408\n"
        "ds_read_b64_tr_b16 v[180:181], v9 offset:16384\n"
        "ds_read_b64_tr_b16 v[182:183], v9 offset:17408\n"
        "s_waitcnt lgkmcnt(4)\n"
        "s_add_u32 m0, s79, 14336\n"
        "s_mov_b32 s65, 1310720\n"
        "buffer_load_dwordx4 v6, s[44:47], s65 offen lds\n"
        "s_mov_b32 s65, 1342464\n"
        "buffer_load_dwordx4 v6, s[44:47], s65 offen offset:1024 lds\n"
        "s_waitcnt vmcnt(28)\n"
        "ds_read_b64_tr_b16 v[184:185], v8 offset:18432\n"
        "ds_read_b64_tr_b16 v[186:187], v8 offset:19456\n"
        "ds_read_b64_tr_b16 v[188:189], v9 offset:18432\n"
        "ds_read_b64_tr_b16 v[190:191], v9 offset:19456\n"
        "s_waitcnt lgkmcnt(4)\n"
        "s_add_u32 m0, s79, 16384\n"
        "s_mov_b32 s65, 1572864\n"
        "buffer_load_dwordx4 v6, s[44:47], s65 offen lds\n"
        "s_mov_b32 s65, 1604608\n"
        "buffer_load_dwordx4 v6, s[44:47], s65 offen offset:1024 lds\n"
        "s_waitcnt vmcnt(28)\n"
        "ds_read_b64_tr_b16 v[192:193], v8 offset:20480\n"
        "ds_read_b64_tr_b16 v[194:195], v8 offset:21504\n"
        "ds_read_b64_tr_b16 v[196:197], v9 offset:20480\n"
        "ds_read_b64_tr_b16 v[198:199], v9 offset:21504\n"
        "s_waitcnt lgkmcnt(4)\n"
        "s_add_u32 m0, s79, 18432\n"
        "s_mov_b32 s65, 1835008\n"
        "buffer_load_dwordx4 v6, s[44:47], s65 offen lds\n"
        "s_mov_b32 s65, 1866752\n"
        "buffer_load_dwordx4 v6, s[44:47], s65 offen offset:1024 lds\n"
        "s_waitcnt vmcnt(28)\n"
        "ds_read_b64_tr_b16 v[200:201], v8 offset:22528\n"
        "ds_read_b64_tr_b16 v[202:203], v8 offset:23552\n"
        "ds_read_b64_tr_b16 v[204:205], v9 offset:22528\n"
        "ds_read_b64_tr_b16 v[206:207], v9 offset:23552\n"
        "s_waitcnt lgkmcnt(4)\n"
        "s_add_u32 m0, s79, 20480\n"
        "s_mov_b32 s65, 2097152\n"
        "buffer_load_dwordx4 v6, s[44:47], s65 offen lds\n"
        "s_mov_b32 s65, 2128896\n"
        "buffer_load_dwordx4 v6, s[44:47], s65 offen offset:1024 lds\n"
        "s_waitcnt vmcnt(28)\n"
        "ds_read_b64_tr_b16 v[208:209], v8 offset:24576\n"
        "ds_read_b64_tr_b16 v[210:211], v8 offset:25600\n"
        "ds_read_b64_tr_b16 v[212:213], v9 offset:24576\n"
        "ds_read_b64_tr_b16 v[214:215], v9 offset:25600\n"
        "s_waitcnt lgkmcnt(4)\n"
        "s_add_u32 m0, s79, 22528\n"
        "s_mov_b32 s65, 2359296\n"
        "buffer_load_dwordx4 v6, s[44:47], s65 offen lds\n"
        "s_mov_b32 s65, 2391040\n"
        "buffer_load_dwordx4 v6, s[44:47], s65 offen offset:1024 lds\n"
        "s_waitcnt vmcnt(28)\n"
        "ds_read_b64_tr_b16 v[216:217], v8 offset:26624\n"
        "ds_read_b64_tr_b16 v[218:219], v8 offset:27648\n"
        "ds_read_b64_tr_b16 v[220:221], v9 offset:26624\n"
        "ds_read_b64_tr_b16 v[222:223], v9 offset:27648\n"
        "s_waitcnt lgkmcnt(4)\n"
        "s_add_u32 m0, s79, 24576\n"
        "s_mov_b32 s65, 2621440\n"
        "buffer_load_dwordx4 v6, s[44:47], s65 offen lds\n"
        "s_mov_b32 s65, 2653184\n"
        "buffer_load_dwordx4 v6, s[44:47], s65 offen offset:1024 lds\n"
        "s_waitcnt vmcnt(28)\n"
        "ds_read_b64_tr_b16 v[224:225], v8 offset:28672\n"
        "ds_read_b64_tr_b16 v[226:227], v8 offset:29696\n"
        "ds_read_b64_tr_b16 v[228:229], v9 offset:28672\n"
        "ds_read_b64_tr_b16 v[230:231], v9 offset:29696\n"
        "s_waitcnt lgkmcnt(4)\n"
        "s_add_u32 m0, s79, 26624\n"
        "s_mov_b32 s65, 2883584\n"
        "buffer_load_dwordx4 v6, s[44:47], s65 offen lds\n"
        "s_mov_b32 s65, 2915328\n"
        "buffer_load_dwordx4 v6, s[44:47], s65 offen offset:1024 lds\n"
        "s_waitcnt vmcnt(28)\n"
        "ds_read_b64_tr_b16 v[232:233], v8 offset:30720\n"
        "ds_read_b64_tr_b16 v[234:235], v8 offset:31744\n"
        "ds_read_b64_tr_b16 v[236:237], v9 offset:30720\n"
        "ds_read_b64_tr_b16 v[238:239], v9 offset:31744\n"
        "s_waitcnt lgkmcnt(4)\n"
        "s_add_u32 m0, s79, 28672\n"
        "s_mov_b32 s65, 3145728\n"
        "buffer_load_dwordx4 v6, s[44:47], s65 offen lds\n"
        "s_mov_b32 s65, 3177472\n"
        "buffer_load_dwordx4 v6, s[44:47], s65 offen offset:1024 lds\n"
        "s_waitcnt vmcnt(28)\n"
        "ds_read_b64_tr_b16 v[240:241], v8 offset:0\n"
        "ds_read_b64_tr_b16 v[242:243], v8 offset:1024\n"
        "ds_read_b64_tr_b16 v[244:245], v9 offset:0\n"
        "ds_read_b64_tr_b16 v[246:247], v9 offset:1024\n"
        "s_waitcnt lgkmcnt(4)\n"
        "s_add_u32 m0, s79, 30720\n"
        "s_mov_b32 s65, 3407872\n"
        "buffer_load_dwordx4 v6, s[44:47], s65 offen lds\n"
        "s_mov_b32 s65, 3439616\n"
        "buffer_load_dwordx4 v6, s[44:47], s65 offen offset:1024 lds\n"
        "s_waitcnt vmcnt(28)\n"
        "ds_read_b64_tr_b16 v[248:249], v8 offset:2048\n"
        "ds_read_b64_tr_b16 v[250:251], v8 offset:3072\n"
        "ds_read_b64_tr_b16 v[252:253], v9 offset:2048\n"
        "ds_read_b64_tr_b16 v[254:255], v9 offset:3072\n"
        "s_waitcnt lgkmcnt(4)\n"
        "s_add_u32 m0, s79, 0\n"
        "s_mov_b32 s65, 3670016\n"
        "buffer_load_dwordx4 v6, s[44:47], s65 offen lds\n"
        "s_mov_b32 s65, 3701760\n"
        "buffer_load_dwordx4 v6, s[44:47], s65 offen offset:1024 lds\n"
        "s_waitcnt vmcnt(28)\n"
        "ds_read_b64_tr_b16 a[0:1], v8 offset:4096\n"
        "ds_read_b64_tr_b16 a[2:3], v8 offset:5120\n"
        "ds_read_b64_tr_b16 a[4:5], v9 offset:4096\n"
        "ds_read_b64_tr_b16 a[6:7], v9 offset:5120\n"
        "s_waitcnt lgkmcnt(4)\n"
        "s_add_u32 m0, s79, 2048\n"
        "s_mov_b32 s65, 3932160\n"
        "buffer_load_dwordx4 v6, s[44:47], s65 offen lds\n"
        "s_mov_b32 s65, 3963904\n"
        "buffer_load_dwordx4 v6, s[44:47], s65 offen offset:1024 lds\n"
        "s_waitcnt vmcnt(28)\n"
        "ds_read_b64_tr_b16 a[8:9], v8 offset:6144\n"
        "ds_read_b64_tr_b16 a[10:11], v8 offset:7168\n"
        "ds_read_b64_tr_b16 a[12:13], v9 offset:6144\n"
        "ds_read_b64_tr_b16 a[14:15], v9 offset:7168\n"
        "s_waitcnt lgkmcnt(4)\n"
        "s_add_u32 m0, s79, 4096\n"
        "s_mov_b32 s65, 4194304\n"
        "buffer_load_dwordx4 v6, s[44:47], s65 offen lds\n"
        "s_mov_b32 s65, 4226048\n"
        "buffer_load_dwordx4 v6, s[44:47], s65 offen offset:1024 lds\n"
        "s_waitcnt vmcnt(28)\n"
        "ds_read_b64_tr_b16 a[16:17], v8 offset:8192\n"
        "ds_read_b64_tr_b16 a[18:19], v8 offset:9216\n"
        "ds_read_b64_tr_b16 a[20:21], v9 offset:8192\n"
        "ds_read_b64_tr_b16 a[22:23], v9 offset:9216\n"
        "s_waitcnt lgkmcnt(4)\n"
        "s_add_u32 m0, s79, 6144\n"
        "s_mov_b32 s65, 4456448\n"
        "buffer_load_dwordx4 v6, s[44:47], s65 offen lds\n"
        "s_mov_b32 s65, 4488192\n"
        "buffer_load_dwordx4 v6, s[44:47], s65 offen offset:1024 lds\n"
        "s_waitcnt vmcnt(28)\n"
        "ds_read_b64_tr_b16 a[24:25], v8 offset:10240\n"
        "ds_read_b64_tr_b16 a[26:27], v8 offset:11264\n"
        "ds_read_b64_tr_b16 a[28:29], v9 offset:10240\n"
        "ds_read_b64_tr_b16 a[30:31], v9 offset:11264\n"
        "s_waitcnt lgkmcnt(4)\n"
        "s_mov_b64 s[44:45], %2\n"
        "s_and_b32 s45, s45, 0xffff\n"
        "s_mov_b32 s46, 4718592\n"
        "s_add_u32 m0, s79, 8192\n"
        "s_mov_b32 s65, 0\n"
        "buffer_load_dwordx4 v6, s[44:47], s65 offen lds\n"
        "s_mov_b32 s65, 31744\n"
        "buffer_load_dwordx4 v6, s[44:47], s65 offen offset:1024 lds\n"
        "s_waitcnt vmcnt(28)\n"
        "ds_read_b64_tr_b16 a[32:33], v8 offset:12288\n"
        "ds_read_b64_tr_b16 a[34:35], v8 offset:13312\n"
        "ds_read_b64_tr_b16 a[36:37], v9 offset:12288\n"
        "ds_read_b64_tr_b16 a[38:39], v9 offset:13312\n"
        "s_waitcnt lgkmcnt(4)\n"
        "s_add_u32 m0, s79, 10240\n"
        "s_mov_b32 s65, 262144\n"
        "buffer_load_dwordx4 v6, s[44:47], s65 offen lds\n"
        "s_mov_b32 s65, 293888\n"
        "buffer_load_dwordx4 v6, s[44:47], s65 offen offset:1024 lds\n"
        "s_waitcnt vmcnt(28)\n"
        "ds_read_b64_tr_b16 a[40:41], v8 offset:14336\n"
        "ds_read_b64_tr_b16 a[42:43], v8 offset:15360\n"
        "ds_read_b64_tr_b16 a[44:45], v9 offset:14336\n"
        "ds_read_b64_tr_b16 a[46:47], v9 offset:15360\n"
        "s_waitcnt lgkmcnt(4)\n"
        "s_add_u32 m0, s79, 12288\n"
        "s_mov_b32 s65, 524288\n"
        "buffer_load_dwordx4 v6, s[44:47], s65 offen lds\n"
        "s_mov_b32 s65, 556032\n"
        "buffer_load_dwordx4 v6, s[44:47], s65 offen offset:1024 lds\n"
        "s_waitcnt vmcnt(28)\n"
        "ds_read_b64_tr_b16 a[48:49], v8 offset:16384\n"
        "ds_read_b64_tr_b16 a[50:51], v8 offset:17408\n"
        "ds_read_b64_tr_b16 a[52:53], v9 offset:16384\n"
        "ds_read_b64_tr_b16 a[54:55], v9 offset:17408\n"
        "s_waitcnt lgkmcnt(4)\n"
        "s_add_u32 m0, s79, 14336\n"
        "s_mov_b32 s65, 786432\n"
        "buffer_load_dwordx4 v6, s[44:47], s65 offen lds\n"
        "s_mov_b32 s65, 818176\n"
        "buffer_load_dwordx4 v6, s[44:47], s65 offen offset:1024 lds\n"
        "s_waitcnt vmcnt(28)\n"
        "ds_read_b64_tr_b16 a[56:57], v8 offset:18432\n"
        "ds_read_b64_tr_b16 a[58:59], v8 offset:19456\n"
        "ds_read_b64_tr_b16 a[60:61], v9 offset:18432\n"
        "ds_read_b64_tr_b16 a[62:63], v9 offset:19456\n"
        "s_waitcnt lgkmcnt(4)\n"
        "s_add_u32 m0, s79, 16384\n"
        "s_mov_b32 s65, 1048576\n"
        "buffer_load_dwordx4 v6, s[44:47], s65 offen lds\n"
        "s_mov_b32 s65, 1080320\n"
        "buffer_load_dwordx4 v6, s[44:47], s65 offen offset:1024 lds\n"
        "s_waitcnt vmcnt(28)\n"
        "ds_read_b64_tr_b16 a[64:65], v8 offset:20480\n"
        "ds_read_b64_tr_b16 a[66:67], v8 offset:21504\n"
        "ds_read_b64_tr_b16 a[68:69], v9 offset:20480\n"
        "ds_read_b64_tr_b16 a[70:71], v9 offset:21504\n"
        "s_waitcnt lgkmcnt(4)\n"
        "s_add_u32 m0, s79, 18432\n"
        "s_mov_b32 s65, 1310720\n"
        "buffer_load_dwordx4 v6, s[44:47], s65 offen lds\n"
        "s_mov_b32 s65, 1342464\n"
        "buffer_load_dwordx4 v6, s[44:47], s65 offen offset:1024 lds\n"
        "s_waitcnt vmcnt(28)\n"
        "ds_read_b64_tr_b16 a[72:73], v8 offset:22528\n"
        "ds_read_b64_tr_b16 a[74:75], v8 offset:23552\n"
        "ds_read_b64_tr_b16 a[76:77], v9 offset:22528\n"
        "ds_read_b64_tr_b16 a[78:79], v9 offset:23552\n"
        "s_waitcnt lgkmcnt(4)\n"
        "s_add_u32 m0, s79, 20480\n"
        "s_mov_b32 s65, 1572864\n"
        "buffer_load_dwordx4 v6, s[44:47], s65 offen lds\n"
        "s_mov_b32 s65, 1604608\n"
        "buffer_load_dwordx4 v6, s[44:47], s65 offen offset:1024 lds\n"
        "s_waitcnt vmcnt(28)\n"
        "ds_read_b64_tr_b16 a[80:81], v8 offset:24576\n"
        "ds_read_b64_tr_b16 a[82:83], v8 offset:25600\n"
        "ds_read_b64_tr_b16 a[84:85], v9 offset:24576\n"
        "ds_read_b64_tr_b16 a[86:87], v9 offset:25600\n"
        "s_waitcnt lgkmcnt(4)\n"
        "s_add_u32 m0, s79, 22528\n"
        "s_mov_b32 s65, 1835008\n"
        "buffer_load_dwordx4 v6, s[44:47], s65 offen lds\n"
        "s_mov_b32 s65, 1866752\n"
        "buffer_load_dwordx4 v6, s[44:47], s65 offen offset:1024 lds\n"
        "s_waitcnt vmcnt(28)\n"
        "ds_read_b64_tr_b16 a[88:89], v8 offset:26624\n"
        "ds_read_b64_tr_b16 a[90:91], v8 offset:27648\n"
        "ds_read_b64_tr_b16 a[92:93], v9 offset:26624\n"
        "ds_read_b64_tr_b16 a[94:95], v9 offset:27648\n"
        "s_waitcnt lgkmcnt(4)\n"
        "s_add_u32 m0, s79, 24576\n"
        "s_mov_b32 s65, 2097152\n"
        "buffer_load_dwordx4 v6, s[44:47], s65 offen lds\n"
        "s_mov_b32 s65, 2128896\n"
        "buffer_load_dwordx4 v6, s[44:47], s65 offen offset:1024 lds\n"
        "s_waitcnt vmcnt(28)\n"
        "ds_read_b64_tr_b16 a[96:97], v8 offset:28672\n"
        "ds_read_b64_tr_b16 a[98:99], v8 offset:29696\n"
        "ds_read_b64_tr_b16 a[100:101], v9 offset:28672\n"
        "ds_read_b64_tr_b16 a[102:103], v9 offset:29696\n"
        "s_waitcnt lgkmcnt(4)\n"
        "s_add_u32 m0, s79, 26624\n"
        "s_mov_b32 s65, 2359296\n"
        "buffer_load_dwordx4 v6, s[44:47], s65 offen lds\n"
        "s_mov_b32 s65, 2391040\n"
        "buffer_load_dwordx4 v6, s[44:47], s65 offen offset:1024 lds\n"
        "s_waitcnt vmcnt(28)\n"
        "ds_read_b64_tr_b16 a[104:105], v8 offset:30720\n"
        "ds_read_b64_tr_b16 a[106:107], v8 offset:31744\n"
        "ds_read_b64_tr_b16 a[108:109], v9 offset:30720\n"
        "ds_read_b64_tr_b16 a[110:111], v9 offset:31744\n"
        "s_waitcnt lgkmcnt(4)\n"
        "s_add_u32 m0, s79, 28672\n"
        "s_mov_b32 s65, 2621440\n"
        "buffer_load_dwordx4 v6, s[44:47], s65 offen lds\n"
        "s_mov_b32 s65, 2653184\n"
        "buffer_load_dwordx4 v6, s[44:47], s65 offen offset:1024 lds\n"
        "s_waitcnt vmcnt(28)\n"
        "ds_read_b64_tr_b16 a[112:113], v8 offset:0\n"
        "ds_read_b64_tr_b16 a[114:115], v8 offset:1024\n"
        "ds_read_b64_tr_b16 a[116:117], v9 offset:0\n"
        "ds_read_b64_tr_b16 a[118:119], v9 offset:1024\n"
        "s_waitcnt lgkmcnt(4)\n"
        "s_add_u32 m0, s79, 30720\n"
        "s_mov_b32 s65, 2883584\n"
        "buffer_load_dwordx4 v6, s[44:47], s65 offen lds\n"
        "s_mov_b32 s65, 2915328\n"
        "buffer_load_dwordx4 v6, s[44:47], s65 offen offset:1024 lds\n"
        "s_waitcnt vmcnt(28)\n"
        "ds_read_b64_tr_b16 a[120:121], v8 offset:2048\n"
        "ds_read_b64_tr_b16 a[122:123], v8 offset:3072\n"
        "ds_read_b64_tr_b16 a[124:125], v9 offset:2048\n"
        "ds_read_b64_tr_b16 a[126:127], v9 offset:3072\n"
        "s_waitcnt lgkmcnt(4)\n"
        "s_add_u32 m0, s79, 0\n"
        "s_mov_b32 s65, 3145728\n"
        "buffer_load_dwordx4 v6, s[44:47], s65 offen lds\n"
        "s_mov_b32 s65, 3177472\n"
        "buffer_load_dwordx4 v6, s[44:47], s65 offen offset:1024 lds\n"
        "s_waitcnt vmcnt(28)\n"
        "ds_read_b64_tr_b16 a[128:129], v8 offset:4096\n"
        "ds_read_b64_tr_b16 a[130:131], v8 offset:5120\n"
        "ds_read_b64_tr_b16 a[132:133], v9 offset:4096\n"
        "ds_read_b64_tr_b16 a[134:135], v9 offset:5120\n"
        "s_waitcnt lgkmcnt(4)\n"
        "s_add_u32 m0, s79, 2048\n"
        "s_mov_b32 s65, 3407872\n"
        "buffer_load_dwordx4 v6, s[44:47], s65 offen lds\n"
        "s_mov_b32 s65, 3439616\n"
        "buffer_load_dwordx4 v6, s[44:47], s65 offen offset:1024 lds\n"
        "s_waitcnt vmcnt(28)\n"
        "ds_read_b64_tr_b16 a[136:137], v8 offset:6144\n"
        "ds_read_b64_tr_b16 a[138:139], v8 offset:7168\n"
        "ds_read_b64_tr_b16 a[140:141], v9 offset:6144\n"
        "ds_read_b64_tr_b16 a[142:143], v9 offset:7168\n"
        "s_waitcnt lgkmcnt(4)\n"
        "s_add_u32 m0, s79, 4096\n"
        "s_mov_b32 s65, 3670016\n"
        "buffer_load_dwordx4 v6, s[44:47], s65 offen lds\n"
        "s_mov_b32 s65, 3701760\n"
        "buffer_load_dwordx4 v6, s[44:47], s65 offen offset:1024 lds\n"
        "s_waitcnt vmcnt(28)\n"
        "ds_read_b64_tr_b16 a[144:145], v8 offset:8192\n"
        "ds_read_b64_tr_b16 a[146:147], v8 offset:9216\n"
        "ds_read_b64_tr_b16 a[148:149], v9 offset:8192\n"
        "ds_read_b64_tr_b16 a[150:151], v9 offset:9216\n"
        "s_waitcnt lgkmcnt(4)\n"
        "s_add_u32 m0, s79, 6144\n"
        "s_mov_b32 s65, 3932160\n"
        "buffer_load_dwordx4 v6, s[44:47], s65 offen lds\n"
        "s_mov_b32 s65, 3963904\n"
        "buffer_load_dwordx4 v6, s[44:47], s65 offen offset:1024 lds\n"
        "s_waitcnt vmcnt(28)\n"
        "ds_read_b64_tr_b16 a[152:153], v8 offset:10240\n"
        "ds_read_b64_tr_b16 a[154:155], v8 offset:11264\n"
        "ds_read_b64_tr_b16 a[156:157], v9 offset:10240\n"
        "ds_read_b64_tr_b16 a[158:159], v9 offset:11264\n"
        "s_waitcnt lgkmcnt(4)\n"
        "s_add_u32 m0, s79, 8192\n"
        "s_mov_b32 s65, 4194304\n"
        "buffer_load_dwordx4 v6, s[44:47], s65 offen lds\n"
        "s_mov_b32 s65, 4226048\n"
        "buffer_load_dwordx4 v6, s[44:47], s65 offen offset:1024 lds\n"
        "s_waitcnt vmcnt(28)\n"
        "ds_read_b64_tr_b16 a[160:161], v8 offset:12288\n"
        "ds_read_b64_tr_b16 a[162:163], v8 offset:13312\n"
        "ds_read_b64_tr_b16 a[164:165], v9 offset:12288\n"
        "ds_read_b64_tr_b16 a[166:167], v9 offset:13312\n"
        "s_waitcnt lgkmcnt(4)\n"
        "s_add_u32 m0, s79, 10240\n"
        "s_mov_b32 s65, 4456448\n"
        "buffer_load_dwordx4 v6, s[44:47], s65 offen lds\n"
        "s_mov_b32 s65, 4488192\n"
        "buffer_load_dwordx4 v6, s[44:47], s65 offen offset:1024 lds\n"
        "s_waitcnt vmcnt(28)\n"
        "ds_read_b64_tr_b16 a[168:169], v8 offset:14336\n"
        "ds_read_b64_tr_b16 a[170:171], v8 offset:15360\n"
        "ds_read_b64_tr_b16 a[172:173], v9 offset:14336\n"
        "ds_read_b64_tr_b16 a[174:175], v9 offset:15360\n"
        "s_waitcnt vmcnt(26)\n"
        "ds_read_b64_tr_b16 a[176:177], v8 offset:16384\n"
        "ds_read_b64_tr_b16 a[178:179], v8 offset:17408\n"
        "ds_read_b64_tr_b16 a[180:181], v9 offset:16384\n"
        "ds_read_b64_tr_b16 a[182:183], v9 offset:17408\n"
        "s_waitcnt vmcnt(24)\n"
        "ds_read_b64_tr_b16 a[184:185], v8 offset:18432\n"
        "ds_read_b64_tr_b16 a[186:187], v8 offset:19456\n"
        "ds_read_b64_tr_b16 a[188:189], v9 offset:18432\n"
        "ds_read_b64_tr_b16 a[190:191], v9 offset:19456\n"
        "s_waitcnt vmcnt(22)\n"
        "ds_read_b64_tr_b16 a[192:193], v8 offset:20480\n"
        "ds_read_b64_tr_b16 a[194:195], v8 offset:21504\n"
        "ds_read_b64_tr_b16 a[196:197], v9 offset:20480\n"
        "ds_read_b64_tr_b16 a[198:199], v9 offset:21504\n"
        "s_waitcnt vmcnt(20)\n"
        "ds_read_b64_tr_b16 a[200:201], v8 offset:22528\n"
        "ds_read_b64_tr_b16 a[202:203], v8 offset:23552\n"
        "ds_read_b64_tr_b16 a[204:205], v9 offset:22528\n"
        "ds_read_b64_tr_b16 a[206:207], v9 offset:23552\n"
        "s_waitcnt vmcnt(18)\n"
        "ds_read_b64_tr_b16 a[208:209], v8 offset:24576\n"
        "ds_read_b64_tr_b16 a[210:211], v8 offset:25600\n"
        "ds_read_b64_tr_b16 a[212:213], v9 offset:24576\n"
        "ds_read_b64_tr_b16 a[214:215], v9 offset:25600\n"
        "s_waitcnt vmcnt(16)\n"
        "ds_read_b64_tr_b16 a[216:217], v8 offset:26624\n"
        "ds_read_b64_tr_b16 a[218:219], v8 offset:27648\n"
        "ds_read_b64_tr_b16 a[220:221], v9 offset:26624\n"
        "ds_read_b64_tr_b16 a[222:223], v9 offset:27648\n"
        "s_waitcnt vmcnt(14)\n"
        "ds_read_b64_tr_b16 a[224:225], v8 offset:28672\n"
        "ds_read_b64_tr_b16 a[226:227], v8 offset:29696\n"
        "ds_read_b64_tr_b16 a[228:229], v9 offset:28672\n"
        "ds_read_b64_tr_b16 a[230:231], v9 offset:29696\n"
        "s_waitcnt vmcnt(12)\n"
        "ds_read_b64_tr_b16 a[232:233], v8 offset:30720\n"
        "ds_read_b64_tr_b16 a[234:235], v8 offset:31744\n"
        "ds_read_b64_tr_b16 a[236:237], v9 offset:30720\n"
        "ds_read_b64_tr_b16 a[238:239], v9 offset:31744\n"
        "s_waitcnt vmcnt(10)\n"
        "ds_read_b64_tr_b16 a[240:241], v8 offset:0\n"
        "ds_read_b64_tr_b16 a[242:243], v8 offset:1024\n"
        "ds_read_b64_tr_b16 a[244:245], v9 offset:0\n"
        "ds_read_b64_tr_b16 a[246:247], v9 offset:1024\n"
        "s_waitcnt vmcnt(8)\n"
        "ds_read_b64_tr_b16 a[248:249], v8 offset:2048\n"
        "ds_read_b64_tr_b16 a[250:251], v8 offset:3072\n"
        "ds_read_b64_tr_b16 a[252:253], v9 offset:2048\n"
        "ds_read_b64_tr_b16 a[254:255], v9 offset:3072\n"
        "s_waitcnt vmcnt(6)\n"
        "ds_read_b64_tr_b16 v[80:81], v8 offset:4096\n"
        "ds_read_b64_tr_b16 v[82:83], v8 offset:5120\n"
        "ds_read_b64_tr_b16 v[84:85], v9 offset:4096\n"
        "ds_read_b64_tr_b16 v[86:87], v9 offset:5120\n"
        "s_waitcnt vmcnt(4)\n"
        "ds_read_b64_tr_b16 v[88:89], v8 offset:6144\n"
        "ds_read_b64_tr_b16 v[90:91], v8 offset:7168\n"
        "ds_read_b64_tr_b16 v[92:93], v9 offset:6144\n"
        "ds_read_b64_tr_b16 v[94:95], v9 offset:7168\n"
        "s_waitcnt vmcnt(2)\n"
        "ds_read_b64_tr_b16 v[96:97], v8 offset:8192\n"
        "ds_read_b64_tr_b16 v[98:99], v8 offset:9216\n"
        "ds_read_b64_tr_b16 v[100:101], v9 offset:8192\n"
        "ds_read_b64_tr_b16 v[102:103], v9 offset:9216\n"
        "s_waitcnt vmcnt(0)\n"
        "ds_read_b64_tr_b16 v[104:105], v8 offset:10240\n"
        "ds_read_b64_tr_b16 v[106:107], v8 offset:11264\n"
        "ds_read_b64_tr_b16 v[108:109], v9 offset:10240\n"
        "ds_read_b64_tr_b16 v[110:111], v9 offset:11264\n"
        "s_waitcnt lgkmcnt(0)\n"
        "v_lshrrev_b32 v12, 4, v1\n"
        "s_lshl_b32 s66, s57, 7\n"
        "s_mov_b32 s64, 0\n"
        ".Ls1_pair_top_%=:\n"
        "s_mov_b64 exec, 1\n"
        "buffer_load_dword v13, off, s[40:43], s66 sc1\n"
        "s_mov_b64 exec, -1\n"
        "s_waitcnt vmcnt(0)\n"
        "v_cmp_gt_i32 vcc, 0, v13\n"
        "s_nop 0\n"
        "s_and_b64 vcc, vcc, 1\n"
        "s_cbranch_vccz .Ls1_pair_ok_%=\n"
        "s_add_u32 s64, s64, 1\n"
        "s_and_b32 s65, s64, 255\n"
        "s_cbranch_scc1 .Ls1_pair_go_%=\n"
        "s_mov_b32 s65, 40960\n"
        "s_cmp_lt_u32 s64, 1048576\n"
        "s_cbranch_scc1 .Ls1_pair_chk_%=\n"
        "v_mov_b32 v13, 1\n"
        "buffer_store_dword v13, off, s[40:43], s65 sc1\n"
        "s_waitcnt vmcnt(0)\n"
        ".Ls1_pair_chk_%=:\n"
        "buffer_load_dword v13, off, s[40:43], s65 sc1\n"
        "s_waitcnt vmcnt(0)\n"
        "v_readfirstlane_b32 s65, v13\n"
        "s_nop 0\n"
        "s_cmp_lg_u32 s65, 1\n"
        "s_cbranch_scc1 .Ls1_pair_go_%=\n"
        "s_endpgm\n"
        ".Ls1_pair_go_%=:\n"
        "s_sleep 2\n"
        "s_branch .Ls1_pair_top_%=\n"
        ".Ls1_pair_ok_%=:\n"
        "v_readfirstlane_b32 s50, v13\n"
        "s_nop 0\n"
        "s_xor_b32 s50, s50, s56\n"
        "v_lshlrev_b32 v14, 7, v12\n"
        "s_sub_u32 s66, 0, s50\n"
        "s_mov_b32 s52, 24576\n"
        "s_mov_b32 s64, 0\n"
        ".Ls1_pg0_top_%=:\n"
        "buffer_load_dword v13, v14, s[40:43], s52 offen sc1\n"
        "s_waitcnt vmcnt(0)\n"
        "v_cmp_gt_i32 vcc, 0, v13\n"
        "s_cbranch_vccz .Ls1_pg0_ok_%=\n"
        "s_add_u32 s64, s64, 1\n"
        "s_and_b32 s65, s64, 255\n"
        "s_cbranch_scc1 .Ls1_pg0_go_%=\n"
        "s_mov_b32 s65, 40960\n"
        "s_cmp_lt_u32 s64, 1048576\n"
        "s_cbranch_scc1 .Ls1_pg0_chk_%=\n"
        "v_mov_b32 v13, 1\n"
        "buffer_store_dword v13, off, s[40:43], s65 sc1\n"
        "s_waitcnt vmcnt(0)\n"
        ".Ls1_pg0_chk_%=:\n"
        "buffer_load_dword v13, off, s[40:43], s65 sc1\n"
        "s_waitcnt vmcnt(0)\n"
        "v_readfirstlane_b32 s65, v13\n"
        "s_nop 0\n"
        "s_cmp_lg_u32 s65, 1\n"
        "s_cbranch_scc1 .Ls1_pg0_go_%=\n"
        "s_endpgm\n"
        ".Ls1_pg0_go_%=:\n"
        "s_sleep 2\n"
        "s_branch .Ls1_pg0_top_%=\n"
        ".Ls1_pg0_ok_%=:\n"
        "v_cmp_eq_u32 s[52:53], 1, v13\n"
        "s_nop 1\n"
        "s_mov_b32 s54, 32768\n"
        "s_mov_b32 s64, 0\n"
        ".Ls1_pg1_top_%=:\n"
        "buffer_load_dword v13, v14, s[40:43], s54 offen sc1\n"
        "s_waitcnt vmcnt(0)\n"
        "v_cmp_gt_i32 vcc, 0, v13\n"
        "s_cbranch_vccz .Ls1_pg1_ok_%=\n"
        "s_add_u32 s64, s64, 1\n"
        "s_and_b32 s65, s64, 255\n"
        "s_cbranch_scc1 .Ls1_pg1_go_%=\n"
        "s_mov_b32 s65, 40960\n"
        "s_cmp_lt_u32 s64, 1048576\n"
        "s_cbranch_scc1 .Ls1_pg1_chk_%=\n"
        "v_mov_b32 v13, 1\n"
        "buffer_store_dword v13, off, s[40:43], s65 sc1\n"
        "s_waitcnt vmcnt(0)\n"
        ".Ls1_pg1_chk_%=:\n"
        "buffer_load_dword v13, off, s[40:43], s65 sc1\n"
        "s_waitcnt vmcnt(0)\n"
        "v_readfirstlane_b32 s65, v13\n"
        "s_nop 0\n"
        "s_cmp_lg_u32 s65, 1\n"
        "s_cbranch_scc1 .Ls1_pg1_go_%=\n"
        "s_endpgm\n"
        ".Ls1_pg1_go_%=:\n"
        "s_sleep 2\n"
        "s_branch .Ls1_pg1_top_%=\n"
        ".Ls1_pg1_ok_%=:\n"
        "v_cmp_eq_u32 s[54:55], 1, v13\n"
        "s_nop 1\n"
        "s_mov_b32 vcc_lo, 0x55555555\n"
        "s_mov_b32 vcc_hi, 0x55555555\n"
        "s_xor_b64 vcc, vcc, s[52:53]\n"
        "s_bcnt1_i32_b64 s65, vcc\n"
        "s_sub_u32 s64, s65, 23\n"
        "s_cmp_lt_u32 s64, 19\n"
        "s_cbranch_scc1 .Ls1_rawmap_%=\n"
        "s_cmp_lt_u32 s65, 32\n"
        "s_cselect_b32 s65, 0, -1\n"
        "s_xor_b32 s52, s65, 0x55555555\n"
        "s_xor_b32 s53, s65, 0x55555555\n"
        "s_xor_b32 s54, s65, 0x55555555\n"
        "s_xor_b32 s55, s65, 0xaaaaaaaa\n"
        ".Ls1_rawmap_%=:\n"
        "s_xor_b32 s52, s52, s66\n"
        "s_xor_b32 s53, s53, s66\n"
        "s_xor_b32 s54, s54, s66\n"
        "s_xor_b32 s55, s55, s66\n"
        "s_and_b32 s76, s55, 1\n"
        "s_lshl_b32 s76, s76, 13\n"
        "s_add_u32 s60, s60, s76\n"
        "s_barrier\n"
        "s_mov_b32 s68, -1\n"
        "s_mov_b32 s69, 0\n"
        "s_mov_b32 s77, 0\n"
        "s_mov_b32 s78, 0\n"
        "v_mov_b32 v6, v0\n"
        "s_mov_b32 s80, 0xbfb8aa3b\n"
        "s_branch .Ls1_wait_%=\n"
        ".Ls1_p00_%=:\n"
        "v_mfma_f32_16x16x32_bf16 v[32:35], a[0:3], v[8:11], 0\n"
        "v_mfma_f32_16x16x32_bf16 v[36:39], a[4:7], v[8:11], 0\n"
        "ds_read_b128 v[8:11], v0 offset:1536\n"
        "v_mfma_f32_16x16x32_bf16 v[40:43], a[0:3], v[12:15], 0\n"
        "v_cvt_pk_bf16_f32 v64, v64, v65\n"
        "v_cvt_pk_bf16_f32 v65, v66, v67\n"
        "v_mfma_f32_16x16x32_bf16 v[44:47], a[4:7], v[12:15], 0\n"
        "ds_read_b128 v[12:15], v0 offset:19968\n"
        "s_waitcnt lgkmcnt(4)\n"
        "v_mfma_f32_16x16x32_bf16 v[32:35], a[8:11], v[16:19], v[32:35]\n"
        "v_cvt_pk_bf16_f32 v66, v68, v69\n"
        "v_cvt_pk_bf16_f32 v67, v70, v71\n"
        "v_mfma_f32_16x16x32_bf16 v[36:39], a[12:15], v[16:19], v[36:39]\n"
        "ds_read_b128 v[16:19], v0 offset:2048\n"
        "v_mfma_f32_16x16x32_bf16 v[40:43], a[8:11], v[20:23], v[40:43]\n"
        "buffer_store_dwordx4 v[64:67], v1, s[40:43], s72 offen offset:0 sc1\n"
        "v_mfma_f32_16x16x32_bf16 v[44:47], a[12:15], v[20:23], v[44:47]\n"
        "ds_read_b128 v[20:23], v0 offset:20480\n"
        "s_waitcnt lgkmcnt(4)\n"
        "v_mfma_f32_16x16x32_bf16 v[32:35], a[16:19], v[24:27], v[32:35]\n"
        "v_cvt_pk_bf16_f32 v72, v72, v73\n"
        "v_cvt_pk_bf16_f32 v73, v74, v75\n"
        "v_mfma_f32_16x16x32_bf16 v[36:39], a[20:23], v[24:27], v[36:39]\n"
        "ds_read_b128 v[24:27], v0 offset:2560\n"
        "v_cvt_pk_bf16_f32 v74, v76, v77\n"
        "v_mfma_f32_16x16x32_bf16 v[40:43], a[16:19], v[28:31], v[40:43]\n"
        "v_cvt_pk_bf16_f32 v75, v78, v79\n"
        "v_mfma_f32_16x16x32_bf16 v[44:47], a[20:23], v[28:31], v[44:47]\n"
        "ds_read_b128 v[28:31], v0 offset:20992\n"
        "s_waitcnt lgkmcnt(4)\n"
        "v_mfma_f32_16x16x32_bf16 v[32:35], a[24:27], v[8:11], v[32:35]\n"
        "v_mfma_f32_16x16x32_bf16 v[36:39], a[28:31], v[8:11], v[36:39]\n"
        "ds_read_b128 v[8:11], v0 offset:3072\n"
        "v_mfma_f32_16x16x32_bf16 v[40:43], a[24:27], v[12:15], v[40:43]\n"
        "buffer_store_dwordx4 v[72:75], v1, s[40:43], s72 offen offset:1024 sc1\n"
        "v_mfma_f32_16x16x32_bf16 v[44:47], a[28:31], v[12:15], v[44:47]\n"
        "ds_read_b128 v[12:15], v0 offset:21504\n"
        "s_waitcnt lgkmcnt(4)\n"
        "v_mfma_f32_16x16x32_bf16 v[32:35], a[32:35], v[16:19], v[32:35]\n"
        "v_mfma_f32_16x16x32_bf16 v[36:39], a[36:39], v[16:19], v[36:39]\n"
        "ds_read_b128 v[16:19], v0 offset:3584\n"
        "v_mfma_f32_16x16x32_bf16 v[40:43], a[32:35], v[20:23], v[40:43]\n"
        "v_mfma_f32_16x16x32_bf16 v[44:47], a[36:39], v[20:23], v[44:47]\n"
        "ds_read_b128 v[20:23], v0 offset:22016\n"
        "s_waitcnt lgkmcnt(4)\n"
        "v_mfma_f32_16x16x32_bf16 v[32:35], a[40:43], v[24:27], v[32:35]\n"
        "s_waitcnt vmcnt(2)\n"
        "s_barrier\n"
        "v_mfma_f32_16x16x32_bf16 v[36:39], a[44:47], v[24:27], v[36:39]\n"
        "ds_read_b128 v[24:27], v0 offset:4096\n"
        "v_mfma_f32_16x16x32_bf16 v[40:43], a[40:43], v[28:31], v[40:43]\n"
        "v_mfma_f32_16x16x32_bf16 v[44:47], a[44:47], v[28:31], v[44:47]\n"
        "ds_read_b128 v[28:31], v0 offset:22528\n"
        "s_waitcnt lgkmcnt(4)\n"
        "v_mfma_f32_16x16x32_bf16 v[32:35], a[48:51], v[8:11], v[32:35]\n"
        "v_mfma_f32_16x16x32_bf16 v[36:39], a[52:55], v[8:11], v[36:39]\n"
        "ds_read_b128 v[8:11], v0 offset:4608\n"
        "v_mfma_f32_16x16x32_bf16 v[40:43], a[48:51], v[12:15], v[40:43]\n"
        "v_mfma_f32_16x16x32_bf16 v[44:47], a[52:55], v[12:15], v[44:47]\n"
        "ds_read_b128 v[12:15], v0 offset:23040\n"
        "s_waitcnt lgkmcnt(4)\n"
        "v_mfma_f32_16x16x32_bf16 v[32:35], a[56:59], v[16:19], v[32:35]\n"
        "v_mfma_f32_16x16x32_bf16 v[36:39], a[60:63], v[16:19], v[36:39]\n"
        "ds_read_b128 v[16:19], v0 offset:5120\n"
        "v_mfma_f32_16x16x32_bf16 v[40:43], a[56:59], v[20:23], v[40:43]\n"
        "v_mfma_f32_16x16x32_bf16 v[44:47], a[60:63], v[20:23], v[44:47]\n"
        "ds_read_b128 v[20:23], v0 offset:23552\n"
        "s_waitcnt lgkmcnt(4)\n"
        "v_mfma_f32_16x16x32_bf16 v[32:35], a[64:67], v[24:27], v[32:35]\n"
        "buffer_load_dword v4, v2, s[40:43], s76 offen sc1\n"
        "v_mfma_f32_16x16x32_bf16 v[36:39], a[68:71], v[24:27], v[36:39]\n"
        "ds_read_b128 v[24:27], v0 offset:5632\n"
        "v_mfma_f32_16x16x32_bf16 v[40:43], a[64:67], v[28:31], v[40:43]\n"
        "v_mfma_f32_16x16x32_bf16 v[44:47], a[68:71], v[28:31], v[44:47]\n"
        "ds_read_b128 v[28:31], v0 offset:24064\n"
        "s_waitcnt lgkmcnt(4)\n"
        "v_mfma_f32_16x16x32_bf16 v[32:35], a[72:75], v[8:11], v[32:35]\n"
        "v_mfma_f32_16x16x32_bf16 v[36:39], a[76:79], v[8:11], v[36:39]\n"
        "ds_read_b128 v[8:11], v0 offset:6144\n"
        "v_mfma_f32_16x16x32_bf16 v[40:43], a[72:75], v[12:15], v[40:43]\n"
        "v_mfma_f32_16x16x32_bf16 v[44:47], a[76:79], v[12:15], v[44:47]\n"
        "ds_read_b128 v[12:15], v0 offset:24576\n"
        "s_waitcnt lgkmcnt(4)\n"
        "v_mfma_f32_16x16x32_bf16 v[32:35], a[80:83], v[16:19], v[32:35]\n"
        "v_mfma_f32_16x16x32_bf16 v[36:39], a[84:87], v[16:19], v[36:39]\n"
        "ds_read_b128 v[16:19], v0 offset:6656\n"
        "v_mfma_f32_16x16x32_bf16 v[40:43], a[80:83], v[20:23], v[40:43]\n"
        "v_mfma_f32_16x16x32_bf16 v[44:47], a[84:87], v[20:23], v[44:47]\n"
        "ds_read_b128 v[20:23], v0 offset:25088\n"
        "s_waitcnt lgkmcnt(4)\n"
        "v_mfma_f32_16x16x32_bf16 v[32:35], a[88:91], v[24:27], v[32:35]\n"
        "s_waitcnt vmcnt(1)\n"
        "v_mov_b32 v5, s74\n"
        "v_mfma_f32_16x16x32_bf16 v[36:39], a[92:95], v[24:27], v[36:39]\n"
        "ds_read_b128 v[24:27], v0 offset:7168\n"
        "buffer_store_dword v5, v3, s[40:43], s73 offen sc1\n"
        "v_mfma_f32_16x16x32_bf16 v[40:43], a[88:91], v[28:31], v[40:43]\n"
        "s_add_u32 s68, s68, 1\n"
        "v_mfma_f32_16x16x32_bf16 v[44:47], a[92:95], v[28:31], v[44:47]\n"
        "ds_read_b128 v[28:31], v0 offset:25600\n"
        "s_waitcnt lgkmcnt(4)\n"
        "v_mfma_f32_16x16x32_bf16 v[32:35], a[96:99], v[8:11], v[32:35]\n"
        "s_bitcmp1_b64 s[52:53], s68\n"
        "s_cselect_b32 s66, 8192, 0\n"
        "v_mfma_f32_16x16x32_bf16 v[36:39], a[100:103], v[8:11], v[36:39]\n"
        "ds_read_b128 v[8:11], v0 offset:7680\n"
        "s_lshl_b32 s75, s68, 21\n"
        "v_mfma_f32_16x16x32_bf16 v[40:43], a[96:99], v[12:15], v[40:43]\n"
        "s_add_u32 s75, s75, s66\n"
        "s_add_u32 s75, s75, s59\n"
        "v_mfma_f32_16x16x32_bf16 v[44:47], a[100:103], v[12:15], v[44:47]\n"
        "ds_read_b128 v[12:15], v0 offset:26112\n"
        "s_waitcnt lgkmcnt(4)\n"
        "v_mfma_f32_16x16x32_bf16 v[32:35], a[104:107], v[16:19], v[32:35]\n"
        "s_mul_i32 s74, s68, 3\n"
        "s_mov_b32 s73, s60\n"
        "v_mfma_f32_16x16x32_bf16 v[36:39], a[108:111], v[16:19], v[36:39]\n"
        "ds_read_b128 v[16:19], v0 offset:8192\n"
        "s_add_u32 s72, s75, 524288\n"
        "v_mfma_f32_16x16x32_bf16 v[40:43], a[104:107], v[20:23], v[40:43]\n"
        "v_mfma_f32_16x16x32_bf16 v[44:47], a[108:111], v[20:23], v[44:47]\n"
        "ds_read_b128 v[20:23], v0 offset:26624\n"
        "s_waitcnt lgkmcnt(4)\n"
        "v_mfma_f32_16x16x32_bf16 v[32:35], a[112:115], v[24:27], v[32:35]\n"
        "v_mfma_f32_16x16x32_bf16 v[36:39], a[116:119], v[24:27], v[36:39]\n"
        "ds_read_b128 v[24:27], v0 offset:8704\n"
        "v_mfma_f32_16x16x32_bf16 v[40:43], a[112:115], v[28:31], v[40:43]\n"
        "v_mfma_f32_16x16x32_bf16 v[44:47], a[116:119], v[28:31], v[44:47]\n"
        "ds_read_b128 v[28:31], v0 offset:27136\n"
        "s_waitcnt lgkmcnt(4)\n"
        "v_mfma_f32_16x16x32_bf16 v[32:35], a[120:123], v[8:11], v[32:35]\n"
        "v_mfma_f32_16x16x32_bf16 v[36:39], a[124:127], v[8:11], v[36:39]\n"
        "ds_read_b128 v[8:11], v0 offset:0\n"
        "v_mfma_f32_16x16x32_bf16 v[40:43], a[120:123], v[12:15], v[40:43]\n"
        "v_mfma_f32_16x16x32_bf16 v[44:47], a[124:127], v[12:15], v[44:47]\n"
        "ds_read_b128 v[12:15], v0 offset:18432\n"
        "s_waitcnt lgkmcnt(4)\n"
        "v_mfma_f32_16x16x32_bf16 v[32:35], a[128:131], v[16:19], v[32:35]\n"
        "v_mfma_f32_16x16x32_bf16 v[36:39], a[132:135], v[16:19], v[36:39]\n"
        "ds_read_b128 v[16:19], v0 offset:512\n"
        "v_mfma_f32_16x16x32_bf16 v[40:43], a[128:131], v[20:23], v[40:43]\n"
        "v_mfma_f32_16x16x32_bf16 v[44:47], a[132:135], v[20:23], v[44:47]\n"
        "ds_read_b128 v[20:23], v0 offset:18944\n"
        "s_waitcnt lgkmcnt(4)\n"
        "v_mfma_f32_16x16x32_bf16 v[32:35], a[136:139], v[24:27], v[32:35]\n"
        "s_waitcnt vmcnt(1)\n"
        "v_cmp_gt_i32 vcc, s70, v4\n"
        "v_mfma_f32_16x16x32_bf16 v[36:39], a[140:143], v[24:27], v[36:39]\n"
        "ds_read_b128 v[24:27], v0 offset:1024\n"
        "v_mfma_f32_16x16x32_bf16 v[40:43], a[136:139], v[28:31], v[40:43]\n"
        "s_and_b64 vcc, vcc, 1\n"
        "v_mfma_f32_16x16x32_bf16 v[44:47], a[140:143], v[28:31], v[44:47]\n"
        "ds_read_b128 v[28:31], v0 offset:19456\n"
        "s_waitcnt lgkmcnt(4)\n"
        "s_cbranch_vccz .Ls1_p11_%=\n"
        ".Ls1_p10_%=:\n"
        "v_mfma_f32_16x16x32_bf16 v[48:51], v[8:11], a[144:147], 0\n"
        "v_mfma_f32_16x16x32_bf16 v[52:55], v[8:11], a[148:151], 0\n"
        "ds_read_b128 v[8:11], v0 offset:1536\n"
        "v_mfma_f32_16x16x32_bf16 v[56:59], v[12:15], a[144:147], 0\n"
        "v_cvt_pk_bf16_f32 v32, v32, v33\n"
        "v_cvt_pk_bf16_f32 v33, v34, v35\n"
        "v_mfma_f32_16x16x32_bf16 v[60:63], v[12:15], a[148:151], 0\n"
        "ds_read_b128 v[12:15], v0 offset:19968\n"
        "s_waitcnt lgkmcnt(4)\n"
        "v_mfma_f32_16x16x32_bf16 v[48:51], v[16:19], a[152:155], v[48:51]\n"
        "v_cvt_pk_bf16_f32 v34, v36, v37\n"
        "v_cvt_pk_bf16_f32 v35, v38, v39\n"
        "v_mfma_f32_16x16x32_bf16 v[52:55], v[16:19], a[156:159], v[52:55]\n"
        "ds_read_b128 v[16:19], v0 offset:2048\n"
        "v_mfma_f32_16x16x32_bf16 v[56:59], v[20:23], a[152:155], v[56:59]\n"
        "buffer_store_dwordx4 v[32:35], v1, s[40:43], s72 offen offset:0 sc1\n"
        "v_mfma_f32_16x16x32_bf16 v[60:63], v[20:23], a[156:159], v[60:63]\n"
        "ds_read_b128 v[20:23], v0 offset:20480\n"
        "s_waitcnt lgkmcnt(4)\n"
        "v_mfma_f32_16x16x32_bf16 v[48:51], v[24:27], a[160:163], v[48:51]\n"
        "v_cvt_pk_bf16_f32 v40, v40, v41\n"
        "v_cvt_pk_bf16_f32 v41, v42, v43\n"
        "v_mfma_f32_16x16x32_bf16 v[52:55], v[24:27], a[164:167], v[52:55]\n"
        "ds_read_b128 v[24:27], v0 offset:2560\n"
        "v_cvt_pk_bf16_f32 v42, v44, v45\n"
        "v_mfma_f32_16x16x32_bf16 v[56:59], v[28:31], a[160:163], v[56:59]\n"
        "v_cvt_pk_bf16_f32 v43, v46, v47\n"
        "v_mfma_f32_16x16x32_bf16 v[60:63], v[28:31], a[164:167], v[60:63]\n"
        "ds_read_b128 v[28:31], v0 offset:20992\n"
        "s_waitcnt lgkmcnt(4)\n"
        "v_mfma_f32_16x16x32_bf16 v[48:51], v[8:11], a[168:171], v[48:51]\n"
        "v_mfma_f32_16x16x32_bf16 v[52:55], v[8:11], a[172:175], v[52:55]\n"
        "ds_read_b128 v[8:11], v0 offset:3072\n"
        "v_mfma_f32_16x16x32_bf16 v[56:59], v[12:15], a[168:171], v[56:59]\n"
        "buffer_store_dwordx4 v[40:43], v1, s[40:43], s72 offen offset:1024 sc1\n"
        "v_mfma_f32_16x16x32_bf16 v[60:63], v[12:15], a[172:175], v[60:63]\n"
        "ds_read_b128 v[12:15], v0 offset:21504\n"
        "s_waitcnt lgkmcnt(4)\n"
        "v_mfma_f32_16x16x32_bf16 v[48:51], v[16:19], a[176:179], v[48:51]\n"
        "v_mfma_f32_16x16x32_bf16 v[52:55], v[16:19], a[180:183], v[52:55]\n"
        "ds_read_b128 v[16:19], v0 offset:3584\n"
        "v_mfma_f32_16x16x32_bf16 v[56:59], v[20:23], a[176:179], v[56:59]\n"
        "v_mfma_f32_16x16x32_bf16 v[60:63], v[20:23], a[180:183], v[60:63]\n"
        "ds_read_b128 v[20:23], v0 offset:22016\n"
        "s_waitcnt lgkmcnt(4)\n"
        "v_mfma_f32_16x16x32_bf16 v[48:51], v[24:27], a[184:187], v[48:51]\n"
        "v_mfma_f32_16x16x32_bf16 v[52:55], v[24:27], a[188:191], v[52:55]\n"
        "ds_read_b128 v[24:27], v0 offset:4096\n"
        "v_mfma_f32_16x16x32_bf16 v[56:59], v[28:31], a[184:187], v[56:59]\n"
        "v_mfma_f32_16x16x32_bf16 v[60:63], v[28:31], a[188:191], v[60:63]\n"
        "ds_read_b128 v[28:31], v0 offset:22528\n"
        "s_waitcnt lgkmcnt(4)\n"
        "v_mfma_f32_16x16x32_bf16 v[48:51], v[8:11], a[192:195], v[48:51]\n"
        "v_mfma_f32_16x16x32_bf16 v[52:55], v[8:11], a[196:199], v[52:55]\n"
        "ds_read_b128 v[8:11], v0 offset:4608\n"
        "v_mfma_f32_16x16x32_bf16 v[56:59], v[12:15], a[192:195], v[56:59]\n"
        "v_mfma_f32_16x16x32_bf16 v[60:63], v[12:15], a[196:199], v[60:63]\n"
        "ds_read_b128 v[12:15], v0 offset:23040\n"
        "s_waitcnt lgkmcnt(4)\n"
        "v_mfma_f32_16x16x32_bf16 v[48:51], v[16:19], a[200:203], v[48:51]\n"
        "v_mfma_f32_16x16x32_bf16 v[52:55], v[16:19], a[204:207], v[52:55]\n"
        "ds_read_b128 v[16:19], v0 offset:5120\n"
        "v_mfma_f32_16x16x32_bf16 v[56:59], v[20:23], a[200:203], v[56:59]\n"
        "v_mfma_f32_16x16x32_bf16 v[60:63], v[20:23], a[204:207], v[60:63]\n"
        "ds_read_b128 v[20:23], v0 offset:23552\n"
        "s_waitcnt lgkmcnt(4)\n"
        "v_mfma_f32_16x16x32_bf16 v[48:51], v[24:27], a[208:211], v[48:51]\n"
        "buffer_load_dword v4, v2, s[40:43], s76 offen sc1\n"
        "v_mfma_f32_16x16x32_bf16 v[52:55], v[24:27], a[212:215], v[52:55]\n"
        "ds_read_b128 v[24:27], v0 offset:5632\n"
        "v_mfma_f32_16x16x32_bf16 v[56:59], v[28:31], a[208:211], v[56:59]\n"
        "v_mfma_f32_16x16x32_bf16 v[60:63], v[28:31], a[212:215], v[60:63]\n"
        "ds_read_b128 v[28:31], v0 offset:24064\n"
        "s_waitcnt lgkmcnt(4)\n"
        "v_mfma_f32_16x16x32_bf16 v[48:51], v[8:11], a[216:219], v[48:51]\n"
        "v_mfma_f32_16x16x32_bf16 v[52:55], v[8:11], a[220:223], v[52:55]\n"
        "ds_read_b128 v[8:11], v0 offset:6144\n"
        "v_mfma_f32_16x16x32_bf16 v[56:59], v[12:15], a[216:219], v[56:59]\n"
        "v_mfma_f32_16x16x32_bf16 v[60:63], v[12:15], a[220:223], v[60:63]\n"
        "ds_read_b128 v[12:15], v0 offset:24576\n"
        "s_waitcnt lgkmcnt(4)\n"
        "v_mfma_f32_16x16x32_bf16 v[48:51], v[16:19], a[224:227], v[48:51]\n"
        "v_mfma_f32_16x16x32_bf16 v[52:55], v[16:19], a[228:231], v[52:55]\n"
        "ds_read_b128 v[16:19], v0 offset:6656\n"
        "v_mfma_f32_16x16x32_bf16 v[56:59], v[20:23], a[224:227], v[56:59]\n"
        "v_mfma_f32_16x16x32_bf16 v[60:63], v[20:23], a[228:231], v[60:63]\n"
        "ds_read_b128 v[20:23], v0 offset:25088\n"
        "s_waitcnt lgkmcnt(4)\n"
        "v_mfma_f32_16x16x32_bf16 v[48:51], v[24:27], a[232:235], v[48:51]\n"
        "s_waitcnt vmcnt(1)\n"
        "v_mov_b32 v5, s74\n"
        "v_mfma_f32_16x16x32_bf16 v[52:55], v[24:27], a[236:239], v[52:55]\n"
        "ds_read_b128 v[24:27], v0 offset:7168\n"
        "buffer_store_dword v5, v3, s[40:43], s73 offen sc1\n"
        "v_mfma_f32_16x16x32_bf16 v[56:59], v[28:31], a[232:235], v[56:59]\n"
        "s_add_u32 s74, s74, 1\n"
        "s_add_u32 s72, s75, 1048576\n"
        "v_mfma_f32_16x16x32_bf16 v[60:63], v[28:31], a[236:239], v[60:63]\n"
        "ds_read_b128 v[28:31], v0 offset:25600\n"
        "s_waitcnt lgkmcnt(4)\n"
        "v_mfma_f32_16x16x32_bf16 v[48:51], v[8:11], a[240:243], v[48:51]\n"
        "v_mfma_f32_16x16x32_bf16 v[52:55], v[8:11], a[244:247], v[52:55]\n"
        "ds_read_b128 v[8:11], v0 offset:7680\n"
        "v_mfma_f32_16x16x32_bf16 v[56:59], v[12:15], a[240:243], v[56:59]\n"
        "v_mfma_f32_16x16x32_bf16 v[60:63], v[12:15], a[244:247], v[60:63]\n"
        "ds_read_b128 v[12:15], v0 offset:26112\n"
        "s_waitcnt lgkmcnt(4)\n"
        "v_mfma_f32_16x16x32_bf16 v[48:51], v[16:19], a[248:251], v[48:51]\n"
        "v_mfma_f32_16x16x32_bf16 v[52:55], v[16:19], a[252:255], v[52:55]\n"
        "ds_read_b128 v[16:19], v0 offset:8192\n"
        "v_mfma_f32_16x16x32_bf16 v[56:59], v[20:23], a[248:251], v[56:59]\n"
        "v_mfma_f32_16x16x32_bf16 v[60:63], v[20:23], a[252:255], v[60:63]\n"
        "ds_read_b128 v[20:23], v0 offset:26624\n"
        "s_waitcnt lgkmcnt(4)\n"
        "v_mfma_f32_16x16x32_bf16 v[48:51], v[24:27], v[80:83], v[48:51]\n"
        "v_mfma_f32_16x16x32_bf16 v[52:55], v[24:27], v[84:87], v[52:55]\n"
        "ds_read_b128 v[24:27], v0 offset:8704\n"
        "v_mfma_f32_16x16x32_bf16 v[56:59], v[28:31], v[80:83], v[56:59]\n"
        "v_mfma_f32_16x16x32_bf16 v[60:63], v[28:31], v[84:87], v[60:63]\n"
        "ds_read_b128 v[28:31], v0 offset:27136\n"
        "s_waitcnt lgkmcnt(4)\n"
        "v_mfma_f32_16x16x32_bf16 v[48:51], v[8:11], v[88:91], v[48:51]\n"
        "v_mfma_f32_16x16x32_bf16 v[52:55], v[8:11], v[92:95], v[52:55]\n"
        "ds_read_b128 v[8:11], v0 offset:0\n"
        "v_mfma_f32_16x16x32_bf16 v[56:59], v[12:15], v[88:91], v[56:59]\n"
        "v_mfma_f32_16x16x32_bf16 v[60:63], v[12:15], v[92:95], v[60:63]\n"
        "ds_read_b128 v[12:15], v0 offset:18432\n"
        "s_waitcnt lgkmcnt(4)\n"
        "v_mfma_f32_16x16x32_bf16 v[48:51], v[16:19], v[96:99], v[48:51]\n"
        "v_mfma_f32_16x16x32_bf16 v[52:55], v[16:19], v[100:103], v[52:55]\n"
        "ds_read_b128 v[16:19], v0 offset:512\n"
        "v_mfma_f32_16x16x32_bf16 v[56:59], v[20:23], v[96:99], v[56:59]\n"
        "v_mfma_f32_16x16x32_bf16 v[60:63], v[20:23], v[100:103], v[60:63]\n"
        "ds_read_b128 v[20:23], v0 offset:18944\n"
        "s_waitcnt lgkmcnt(4)\n"
        "v_mfma_f32_16x16x32_bf16 v[48:51], v[24:27], v[104:107], v[48:51]\n"
        "s_waitcnt vmcnt(1)\n"
        "v_cmp_gt_i32 vcc, s70, v4\n"
        "v_mfma_f32_16x16x32_bf16 v[52:55], v[24:27], v[108:111], v[52:55]\n"
        "ds_read_b128 v[24:27], v0 offset:1024\n"
        "v_mfma_f32_16x16x32_bf16 v[56:59], v[28:31], v[104:107], v[56:59]\n"
        "s_and_b64 vcc, vcc, 1\n"
        "v_mfma_f32_16x16x32_bf16 v[60:63], v[28:31], v[108:111], v[60:63]\n"
        "ds_read_b128 v[28:31], v0 offset:19456\n"
        "s_waitcnt lgkmcnt(4)\n"
        "s_cbranch_vccz .Ls1_p21_%=\n"
        ".Ls1_p20_%=:\n"
        "v_mfma_f32_16x16x32_bf16 v[64:67], v[112:115], v[8:11], 0\n"
        "v_mfma_f32_16x16x32_bf16 v[68:71], v[116:119], v[8:11], 0\n"
        "ds_read_b128 v[8:11], v0 offset:1536\n"
        "v_mfma_f32_16x16x32_bf16 v[72:75], v[112:115], v[12:15], 0\n"
        "v_cvt_pk_bf16_f32 v48, v48, v49\n"
        "v_cvt_pk_bf16_f32 v49, v50, v51\n"
        "v_mfma_f32_16x16x32_bf16 v[76:79], v[116:119], v[12:15], 0\n"
        "ds_read_b128 v[12:15], v0 offset:19968\n"
        "s_waitcnt lgkmcnt(4)\n"
        "v_mfma_f32_16x16x32_bf16 v[64:67], v[120:123], v[16:19], v[64:67]\n"
        "v_cvt_pk_bf16_f32 v50, v56, v57\n"
        "v_cvt_pk_bf16_f32 v51, v58, v59\n"
        "v_mfma_f32_16x16x32_bf16 v[68:71], v[124:127], v[16:19], v[68:71]\n"
        "ds_read_b128 v[16:19], v0 offset:2048\n"
        "v_mfma_f32_16x16x32_bf16 v[72:75], v[120:123], v[20:23], v[72:75]\n"
        "buffer_store_dwordx4 v[48:51], v1, s[40:43], s72 offen offset:0 sc1\n"
        "v_mfma_f32_16x16x32_bf16 v[76:79], v[124:127], v[20:23], v[76:79]\n"
        "ds_read_b128 v[20:23], v0 offset:20480\n"
        "s_waitcnt lgkmcnt(4)\n"
        "v_mfma_f32_16x16x32_bf16 v[64:67], v[128:131], v[24:27], v[64:67]\n"
        "v_cvt_pk_bf16_f32 v52, v52, v53\n"
        "v_cvt_pk_bf16_f32 v53, v54, v55\n"
        "v_mfma_f32_16x16x32_bf16 v[68:71], v[132:135], v[24:27], v[68:71]\n"
        "ds_read_b128 v[24:27], v0 offset:2560\n"
        "v_cvt_pk_bf16_f32 v54, v60, v61\n"
        "v_mfma_f32_16x16x32_bf16 v[72:75], v[128:131], v[28:31], v[72:75]\n"
        "v_cvt_pk_bf16_f32 v55, v62, v63\n"
        "v_mfma_f32_16x16x32_bf16 v[76:79], v[132:135], v[28:31], v[76:79]\n"
        "ds_read_b128 v[28:31], v0 offset:20992\n"
        "s_waitcnt lgkmcnt(4)\n"
        "v_mfma_f32_16x16x32_bf16 v[64:67], v[136:139], v[8:11], v[64:67]\n"
        "v_mfma_f32_16x16x32_bf16 v[68:71], v[140:143], v[8:11], v[68:71]\n"
        "ds_read_b128 v[8:11], v0 offset:3072\n"
        "v_mfma_f32_16x16x32_bf16 v[72:75], v[136:139], v[12:15], v[72:75]\n"
        "buffer_store_dwordx4 v[52:55], v1, s[40:43], s72 offen offset:1024 sc1\n"
        "v_mfma_f32_16x16x32_bf16 v[76:79], v[140:143], v[12:15], v[76:79]\n"
        "ds_read_b128 v[12:15], v0 offset:21504\n"
        "s_waitcnt lgkmcnt(4)\n"
        "v_mfma_f32_16x16x32_bf16 v[64:67], v[144:147], v[16:19], v[64:67]\n"
        "v_mfma_f32_16x16x32_bf16 v[68:71], v[148:151], v[16:19], v[68:71]\n"
        "ds_read_b128 v[16:19], v0 offset:3584\n"
        "v_mfma_f32_16x16x32_bf16 v[72:75], v[144:147], v[20:23], v[72:75]\n"
        "v_mfma_f32_16x16x32_bf16 v[76:79], v[148:151], v[20:23], v[76:79]\n"
        "ds_read_b128 v[20:23], v0 offset:22016\n"
        "s_waitcnt lgkmcnt(4)\n"
        "v_mfma_f32_16x16x32_bf16 v[64:67], v[152:155], v[24:27], v[64:67]\n"
        "v_mfma_f32_16x16x32_bf16 v[68:71], v[156:159], v[24:27], v[68:71]\n"
        "ds_read_b128 v[24:27], v0 offset:4096\n"
        "v_mfma_f32_16x16x32_bf16 v[72:75], v[152:155], v[28:31], v[72:75]\n"
        "v_mfma_f32_16x16x32_bf16 v[76:79], v[156:159], v[28:31], v[76:79]\n"
        "ds_read_b128 v[28:31], v0 offset:22528\n"
        "s_waitcnt lgkmcnt(4)\n"
        "v_mfma_f32_16x16x32_bf16 v[64:67], v[160:163], v[8:11], v[64:67]\n"
        "v_mfma_f32_16x16x32_bf16 v[68:71], v[164:167], v[8:11], v[68:71]\n"
        "ds_read_b128 v[8:11], v0 offset:4608\n"
        "v_mfma_f32_16x16x32_bf16 v[72:75], v[160:163], v[12:15], v[72:75]\n"
        "v_mfma_f32_16x16x32_bf16 v[76:79], v[164:167], v[12:15], v[76:79]\n"
        "ds_read_b128 v[12:15], v0 offset:23040\n"
        "s_waitcnt lgkmcnt(4)\n"
        "v_mfma_f32_16x16x32_bf16 v[64:67], v[168:171], v[16:19], v[64:67]\n"
        "v_mfma_f32_16x16x32_bf16 v[68:71], v[172:175], v[16:19], v[68:71]\n"
        "ds_read_b128 v[16:19], v0 offset:5120\n"
        "v_mfma_f32_16x16x32_bf16 v[72:75], v[168:171], v[20:23], v[72:75]\n"
        "v_mfma_f32_16x16x32_bf16 v[76:79], v[172:175], v[20:23], v[76:79]\n"
        "ds_read_b128 v[20:23], v0 offset:23552\n"
        "s_waitcnt lgkmcnt(4)\n"
        "v_mfma_f32_16x16x32_bf16 v[64:67], v[176:179], v[24:27], v[64:67]\n"
        "buffer_load_dword v4, v2, s[40:43], s76 offen sc1\n"
        "v_mfma_f32_16x16x32_bf16 v[68:71], v[180:183], v[24:27], v[68:71]\n"
        "ds_read_b128 v[24:27], v0 offset:5632\n"
        "v_mfma_f32_16x16x32_bf16 v[72:75], v[176:179], v[28:31], v[72:75]\n"
        "v_mfma_f32_16x16x32_bf16 v[76:79], v[180:183], v[28:31], v[76:79]\n"
        "ds_read_b128 v[28:31], v0 offset:24064\n"
        "s_waitcnt lgkmcnt(4)\n"
        "v_mfma_f32_16x16x32_bf16 v[64:67], v[184:187], v[8:11], v[64:67]\n"
        "v_mfma_f32_16x16x32_bf16 v[68:71], v[188:191], v[8:11], v[68:71]\n"
        "ds_read_b128 v[8:11], v0 offset:6144\n"
        "v_mfma_f32_16x16x32_bf16 v[72:75], v[184:187], v[12:15], v[72:75]\n"
        "v_mfma_f32_16x16x32_bf16 v[76:79], v[188:191], v[12:15], v[76:79]\n"
        "ds_read_b128 v[12:15], v0 offset:24576\n"
        "s_waitcnt lgkmcnt(4)\n"
        "v_mfma_f32_16x16x32_bf16 v[64:67], v[192:195], v[16:19], v[64:67]\n"
        "s_add_u32 s78, s78, 36864\n"
        "v_mfma_f32_16x16x32_bf16 v[68:71], v[196:199], v[16:19], v[68:71]\n"
        "ds_read_b128 v[16:19], v0 offset:6656\n"
        "v_mfma_f32_16x16x32_bf16 v[72:75], v[192:195], v[20:23], v[72:75]\n"
        "s_cmp_eq_u32 s78, 110592\n"
        "s_cselect_b32 s78, 0, s78\n"
        "v_mfma_f32_16x16x32_bf16 v[76:79], v[196:199], v[20:23], v[76:79]\n"
        "ds_read_b128 v[20:23], v0 offset:25088\n"
        "s_waitcnt lgkmcnt(4)\n"
        "v_mfma_f32_16x16x32_bf16 v[64:67], v[200:203], v[24:27], v[64:67]\n"
        "s_waitcnt vmcnt(1)\n"
        "v_mov_b32 v5, s74\n"
        "v_mfma_f32_16x16x32_bf16 v[68:71], v[204:207], v[24:27], v[68:71]\n"
        "ds_read_b128 v[24:27], v0 offset:7168\n"
        "buffer_store_dword v5, v3, s[40:43], s73 offen sc1\n"
        "v_mfma_f32_16x16x32_bf16 v[72:75], v[200:203], v[28:31], v[72:75]\n"
        "s_add_u32 s74, s74, 1\n"
        "s_add_u32 s72, s75, 0\n"
        "v_mfma_f32_16x16x32_bf16 v[76:79], v[204:207], v[28:31], v[76:79]\n"
        "ds_read_b128 v[28:31], v0 offset:25600\n"
        "s_waitcnt lgkmcnt(4)\n"
        "v_mfma_f32_16x16x32_bf16 v[64:67], v[208:211], v[8:11], v[64:67]\n"
        "v_mfma_f32_16x16x32_bf16 v[68:71], v[212:215], v[8:11], v[68:71]\n"
        "ds_read_b128 v[8:11], v0 offset:7680\n"
        "v_mfma_f32_16x16x32_bf16 v[72:75], v[208:211], v[12:15], v[72:75]\n"
        "v_mfma_f32_16x16x32_bf16 v[76:79], v[212:215], v[12:15], v[76:79]\n"
        "ds_read_b128 v[12:15], v0 offset:26112\n"
        "s_waitcnt lgkmcnt(4)\n"
        "v_mfma_f32_16x16x32_bf16 v[64:67], v[216:219], v[16:19], v[64:67]\n"
        "v_mfma_f32_16x16x32_bf16 v[68:71], v[220:223], v[16:19], v[68:71]\n"
        "ds_read_b128 v[16:19], v0 offset:8192\n"
        "v_mfma_f32_16x16x32_bf16 v[72:75], v[216:219], v[20:23], v[72:75]\n"
        "v_mfma_f32_16x16x32_bf16 v[76:79], v[220:223], v[20:23], v[76:79]\n"
        "ds_read_b128 v[20:23], v0 offset:26624\n"
        "s_waitcnt lgkmcnt(4)\n"
        "v_mfma_f32_16x16x32_bf16 v[64:67], v[224:227], v[24:27], v[64:67]\n"
        "v_mfma_f32_16x16x32_bf16 v[68:71], v[228:231], v[24:27], v[68:71]\n"
        "ds_read_b128 v[24:27], v0 offset:8704\n"
        "v_mfma_f32_16x16x32_bf16 v[72:75], v[224:227], v[28:31], v[72:75]\n"
        "v_mfma_f32_16x16x32_bf16 v[76:79], v[228:231], v[28:31], v[76:79]\n"
        "ds_read_b128 v[28:31], v0 offset:27136\n"
        "s_waitcnt lgkmcnt(4)\n"
        "v_mfma_f32_16x16x32_bf16 v[64:67], v[232:235], v[8:11], v[64:67]\n"
        "s_sub_u32 s65, s69, s68\n"
        "s_cmp_eq_u32 s65, 2\n"
        "s_cbranch_scc0 .Ls1_tail_%=\n"
        "s_barrier\n"
        "v_add_u32 v0, s78, v6\n"
        "v_mfma_f32_16x16x32_bf16 v[68:71], v[236:239], v[8:11], v[68:71]\n"
        "ds_read_b128 v[8:11], v0 offset:0\n"
        "v_mfma_f32_16x16x32_bf16 v[72:75], v[232:235], v[12:15], v[72:75]\n"
        "s_cmp_lt_u32 s69, 64\n"
        "s_cselect_b32 s70, s69, 2147483647\n"
        "v_mfma_f32_16x16x32_bf16 v[76:79], v[236:239], v[12:15], v[76:79]\n"
        "ds_read_b128 v[12:15], v0 offset:18432\n"
        "s_waitcnt lgkmcnt(4)\n"
        "v_mfma_f32_16x16x32_bf16 v[64:67], v[240:243], v[16:19], v[64:67]\n"
        "v_mfma_f32_16x16x32_bf16 v[68:71], v[244:247], v[16:19], v[68:71]\n"
        "ds_read_b128 v[16:19], v0 offset:512\n"
        "v_mfma_f32_16x16x32_bf16 v[72:75], v[240:243], v[20:23], v[72:75]\n"
        "v_mfma_f32_16x16x32_bf16 v[76:79], v[244:247], v[20:23], v[76:79]\n"
        "ds_read_b128 v[20:23], v0 offset:18944\n"
        "s_waitcnt lgkmcnt(4)\n"
        "v_mfma_f32_16x16x32_bf16 v[64:67], v[248:251], v[24:27], v[64:67]\n"
        "s_waitcnt vmcnt(1)\n"
        "v_cmp_gt_i32 vcc, s70, v4\n"
        "v_mfma_f32_16x16x32_bf16 v[68:71], v[252:255], v[24:27], v[68:71]\n"
        "ds_read_b128 v[24:27], v0 offset:1024\n"
        "v_mfma_f32_16x16x32_bf16 v[72:75], v[248:251], v[28:31], v[72:75]\n"
        "s_and_b64 vcc, vcc, 1\n"
        "v_mfma_f32_16x16x32_bf16 v[76:79], v[252:255], v[28:31], v[76:79]\n"
        "ds_read_b128 v[28:31], v0 offset:19456\n"
        "s_waitcnt lgkmcnt(4)\n"
        "s_cbranch_vccz .Ls1_p01_%=\n"
        "s_branch .Ls1_p00_%=\n"
        ".Ls1_p01_%=:\n"
        "v_mfma_f32_16x16x32_bf16 v[32:35], a[0:3], v[8:11], 0\n"
        "s_add_u32 m0, s77, s61\n"
        "v_mfma_f32_16x16x32_bf16 v[36:39], a[4:7], v[8:11], 0\n"
        "ds_read_b128 v[8:11], v0 offset:1536\n"
        "v_mfma_f32_16x16x32_bf16 v[40:43], a[0:3], v[12:15], 0\n"
        "v_cvt_pk_bf16_f32 v64, v64, v65\n"
        "v_cvt_pk_bf16_f32 v65, v66, v67\n"
        "v_mfma_f32_16x16x32_bf16 v[44:47], a[4:7], v[12:15], 0\n"
        "ds_read_b128 v[12:15], v0 offset:19968\n"
        "s_waitcnt lgkmcnt(4)\n"
        "v_mfma_f32_16x16x32_bf16 v[32:35], a[8:11], v[16:19], v[32:35]\n"
        "v_cvt_pk_bf16_f32 v66, v68, v69\n"
        "v_cvt_pk_bf16_f32 v67, v70, v71\n"
        "v_mfma_f32_16x16x32_bf16 v[36:39], a[12:15], v[16:19], v[36:39]\n"
        "ds_read_b128 v[16:19], v0 offset:2048\n"
        "v_mfma_f32_16x16x32_bf16 v[40:43], a[8:11], v[20:23], v[40:43]\n"
        "buffer_store_dwordx4 v[64:67], v1, s[40:43], s72 offen offset:0 sc1\n"
        "v_mfma_f32_16x16x32_bf16 v[44:47], a[12:15], v[20:23], v[44:47]\n"
        "ds_read_b128 v[20:23], v0 offset:20480\n"
        "s_waitcnt lgkmcnt(4)\n"
        "v_mfma_f32_16x16x32_bf16 v[32:35], a[16:19], v[24:27], v[32:35]\n"
        "v_cvt_pk_bf16_f32 v72, v72, v73\n"
        "v_cvt_pk_bf16_f32 v73, v74, v75\n"
        "v_mfma_f32_16x16x32_bf16 v[36:39], a[20:23], v[24:27], v[36:39]\n"
        "ds_read_b128 v[24:27], v0 offset:2560\n"
        "v_cvt_pk_bf16_f32 v74, v76, v77\n"
        "v_mfma_f32_16x16x32_bf16 v[40:43], a[16:19], v[28:31], v[40:43]\n"
        "v_cvt_pk_bf16_f32 v75, v78, v79\n"
        "v_mfma_f32_16x16x32_bf16 v[44:47], a[20:23], v[28:31], v[44:47]\n"
        "ds_read_b128 v[28:31], v0 offset:20992\n"
        "s_waitcnt lgkmcnt(4)\n"
        "v_mfma_f32_16x16x32_bf16 v[32:35], a[24:27], v[8:11], v[32:35]\n"
        "s_bitcmp1_b64 s[52:53], s69\n"
        "s_cselect_b32 s65, 8192, 0\n"
        "v_mfma_f32_16x16x32_bf16 v[36:39], a[28:31], v[8:11], v[36:39]\n"
        "ds_read_b128 v[8:11], v0 offset:3072\n"
        "s_lshl_b32 s71, s69, 21\n"
        "v_mfma_f32_16x16x32_bf16 v[40:43], a[24:27], v[12:15], v[40:43]\n"
        "buffer_store_dwordx4 v[72:75], v1, s[40:43], s72 offen offset:1024 sc1\n"
        "v_mfma_f32_16x16x32_bf16 v[44:47], a[28:31], v[12:15], v[44:47]\n"
        "ds_read_b128 v[12:15], v0 offset:21504\n"
        "s_waitcnt lgkmcnt(4)\n"
        "v_mfma_f32_16x16x32_bf16 v[32:35], a[32:35], v[16:19], v[32:35]\n"
        "s_add_u32 s71, s71, s65\n"
        "s_add_u32 s71, s71, s58\n"
        "v_mfma_f32_16x16x32_bf16 v[36:39], a[36:39], v[16:19], v[36:39]\n"
        "ds_read_b128 v[16:19], v0 offset:3584\n"
        "buffer_load_dwordx4 v1, s[40:43], s71 offen offset:0 sc1 lds\n"
        "v_mfma_f32_16x16x32_bf16 v[40:43], a[32:35], v[20:23], v[40:43]\n"
        "s_add_u32 s69, s69, 1\n"
        "s_add_u32 s77, s77, 36864\n"
        "v_mfma_f32_16x16x32_bf16 v[44:47], a[36:39], v[20:23], v[44:47]\n"
        "ds_read_b128 v[20:23], v0 offset:22016\n"
        "s_waitcnt lgkmcnt(4)\n"
        "v_mfma_f32_16x16x32_bf16 v[32:35], a[40:43], v[24:27], v[32:35]\n"
        "s_waitcnt vmcnt(3)\n"
        "s_barrier\n"
        "v_mfma_f32_16x16x32_bf16 v[36:39], a[44:47], v[24:27], v[36:39]\n"
        "ds_read_b128 v[24:27], v0 offset:4096\n"
        "v_mfma_f32_16x16x32_bf16 v[40:43], a[40:43], v[28:31], v[40:43]\n"
        "buffer_load_dwordx4 v1, s[40:43], s71 offen offset:1024 sc1 lds\n"
        "v_mfma_f32_16x16x32_bf16 v[44:47], a[44:47], v[28:31], v[44:47]\n"
        "ds_read_b128 v[28:31], v0 offset:22528\n"
        "s_waitcnt lgkmcnt(4)\n"
        "v_mfma_f32_16x16x32_bf16 v[32:35], a[48:51], v[8:11], v[32:35]\n"
        "s_cmp_eq_u32 s77, 110592\n"
        "s_cselect_b32 s77, 0, s77\n"
        "v_mfma_f32_16x16x32_bf16 v[36:39], a[52:55], v[8:11], v[36:39]\n"
        "ds_read_b128 v[8:11], v0 offset:4608\n"
        "s_mov_b32 s70, 2147483647\n"
        "v_mfma_f32_16x16x32_bf16 v[40:43], a[48:51], v[12:15], v[40:43]\n"
        "v_mfma_f32_16x16x32_bf16 v[44:47], a[52:55], v[12:15], v[44:47]\n"
        "ds_read_b128 v[12:15], v0 offset:23040\n"
        "s_waitcnt lgkmcnt(4)\n"
        "v_mfma_f32_16x16x32_bf16 v[32:35], a[56:59], v[16:19], v[32:35]\n"
        "buffer_load_dwordx4 v1, s[40:43], s71 offen offset:2048 sc1 lds\n"
        "v_mfma_f32_16x16x32_bf16 v[36:39], a[60:63], v[16:19], v[36:39]\n"
        "ds_read_b128 v[16:19], v0 offset:5120\n"
        "v_mfma_f32_16x16x32_bf16 v[40:43], a[56:59], v[20:23], v[40:43]\n"
        "v_mfma_f32_16x16x32_bf16 v[44:47], a[60:63], v[20:23], v[44:47]\n"
        "ds_read_b128 v[20:23], v0 offset:23552\n"
        "s_waitcnt lgkmcnt(4)\n"
        "v_mfma_f32_16x16x32_bf16 v[32:35], a[64:67], v[24:27], v[32:35]\n"
        "v_mfma_f32_16x16x32_bf16 v[36:39], a[68:71], v[24:27], v[36:39]\n"
        "ds_read_b128 v[24:27], v0 offset:5632\n"
        "v_mfma_f32_16x16x32_bf16 v[40:43], a[64:67], v[28:31], v[40:43]\n"
        "buffer_load_dwordx4 v1, s[40:43], s71 offen offset:3072 sc1 lds\n"
        "s_add_u32 m0, m0, 4096\n"
        "v_mfma_f32_16x16x32_bf16 v[44:47], a[68:71], v[28:31], v[44:47]\n"
        "ds_read_b128 v[28:31], v0 offset:24064\n"
        "s_waitcnt lgkmcnt(4)\n"
        "v_mfma_f32_16x16x32_bf16 v[32:35], a[72:75], v[8:11], v[32:35]\n"
        "s_add_u32 s71, s71, 4096\n"
        "s_mul_i32 s65, s48, -15360\n"
        "v_mfma_f32_16x16x32_bf16 v[36:39], a[76:79], v[8:11], v[36:39]\n"
        "ds_read_b128 v[8:11], v0 offset:6144\n"
        "v_mfma_f32_16x16x32_bf16 v[40:43], a[72:75], v[12:15], v[40:43]\n"
        "v_mfma_f32_16x16x32_bf16 v[44:47], a[76:79], v[12:15], v[44:47]\n"
        "ds_read_b128 v[12:15], v0 offset:24576\n"
        "s_waitcnt lgkmcnt(4)\n"
        "v_mfma_f32_16x16x32_bf16 v[32:35], a[80:83], v[16:19], v[32:35]\n"
        "buffer_load_dwordx4 v1, s[40:43], s71 offen offset:0 sc1 lds\n"
        "v_mfma_f32_16x16x32_bf16 v[36:39], a[84:87], v[16:19], v[36:39]\n"
        "ds_read_b128 v[16:19], v0 offset:6656\n"
        "v_mfma_f32_16x16x32_bf16 v[40:43], a[80:83], v[20:23], v[40:43]\n"
        "v_mfma_f32_16x16x32_bf16 v[44:47], a[84:87], v[20:23], v[44:47]\n"
        "ds_read_b128 v[20:23], v0 offset:25088\n"
        "s_waitcnt lgkmcnt(4)\n"
        "v_mfma_f32_16x16x32_bf16 v[32:35], a[88:91], v[24:27], v[32:35]\n"
        "s_waitcnt vmcnt(5)\n"
        "v_mov_b32 v5, s74\n"
        "v_mfma_f32_16x16x32_bf16 v[36:39], a[92:95], v[24:27], v[36:39]\n"
        "ds_read_b128 v[24:27], v0 offset:7168\n"
        "buffer_store_dword v5, v3, s[40:43], s73 offen sc1\n"
        "v_mfma_f32_16x16x32_bf16 v[40:43], a[88:91], v[28:31], v[40:43]\n"
        "buffer_load_dwordx4 v1, s[40:43], s71 offen offset:1024 sc1 lds\n"
        "s_add_u32 s68, s68, 1\n"
        "v_mfma_f32_16x16x32_bf16 v[44:47], a[92:95], v[28:31], v[44:47]\n"
        "ds_read_b128 v[28:31], v0 offset:25600\n"
        "s_waitcnt lgkmcnt(4)\n"
        "v_mfma_f32_16x16x32_bf16 v[32:35], a[96:99], v[8:11], v[32:35]\n"
        "s_bitcmp1_b64 s[52:53], s68\n"
        "s_cselect_b32 s66, 8192, 0\n"
        "v_mfma_f32_16x16x32_bf16 v[36:39], a[100:103], v[8:11], v[36:39]\n"
        "ds_read_b128 v[8:11], v0 offset:7680\n"
        "s_lshl_b32 s75, s68, 21\n"
        "v_mfma_f32_16x16x32_bf16 v[40:43], a[96:99], v[12:15], v[40:43]\n"
        "s_add_u32 s75, s75, s66\n"
        "s_add_u32 s75, s75, s59\n"
        "v_mfma_f32_16x16x32_bf16 v[44:47], a[100:103], v[12:15], v[44:47]\n"
        "ds_read_b128 v[12:15], v0 offset:26112\n"
        "s_waitcnt lgkmcnt(4)\n"
        "v_mfma_f32_16x16x32_bf16 v[32:35], a[104:107], v[16:19], v[32:35]\n"
        "buffer_load_dwordx4 v1, s[40:43], s71 offen offset:2048 sc1 lds\n"
        "s_mul_i32 s74, s68, 3\n"
        "v_mfma_f32_16x16x32_bf16 v[36:39], a[108:111], v[16:19], v[36:39]\n"
        "ds_read_b128 v[16:19], v0 offset:8192\n"
        "s_mov_b32 s73, s60\n"
        "v_mfma_f32_16x16x32_bf16 v[40:43], a[104:107], v[20:23], v[40:43]\n"
        "s_add_u32 s72, s75, 524288\n"
        "v_mfma_f32_16x16x32_bf16 v[44:47], a[108:111], v[20:23], v[44:47]\n"
        "ds_read_b128 v[20:23], v0 offset:26624\n"
        "s_waitcnt lgkmcnt(4)\n"
        "v_mfma_f32_16x16x32_bf16 v[32:35], a[112:115], v[24:27], v[32:35]\n"
        "v_mfma_f32_16x16x32_bf16 v[36:39], a[116:119], v[24:27], v[36:39]\n"
        "ds_read_b128 v[24:27], v0 offset:8704\n"
        "v_mfma_f32_16x16x32_bf16 v[40:43], a[112:115], v[28:31], v[40:43]\n"
        "buffer_load_dwordx4 v1, s[40:43], s71 offen offset:3072 sc1 lds\n"
        "s_add_u32 m0, m0, 4096\n"
        "v_mfma_f32_16x16x32_bf16 v[44:47], a[116:119], v[28:31], v[44:47]\n"
        "ds_read_b128 v[28:31], v0 offset:27136\n"
        "s_waitcnt lgkmcnt(4)\n"
        "v_mfma_f32_16x16x32_bf16 v[32:35], a[120:123], v[8:11], v[32:35]\n"
        "s_add_u32 s71, s71, s65\n"
        "s_add_u32 s71, s71, 61440\n"
        "v_mfma_f32_16x16x32_bf16 v[36:39], a[124:127], v[8:11], v[36:39]\n"
        "ds_read_b128 v[8:11], v0 offset:0\n"
        "v_mfma_f32_16x16x32_bf16 v[40:43], a[120:123], v[12:15], v[40:43]\n"
        "v_mfma_f32_16x16x32_bf16 v[44:47], a[124:127], v[12:15], v[44:47]\n"
        "ds_read_b128 v[12:15], v0 offset:18432\n"
        "s_waitcnt lgkmcnt(4)\n"
        "v_mfma_f32_16x16x32_bf16 v[32:35], a[128:131], v[16:19], v[32:35]\n"
        "buffer_load_dwordx4 v1, s[40:43], s71 offen offset:0 sc1 lds\n"
        "v_mfma_f32_16x16x32_bf16 v[36:39], a[132:135], v[16:19], v[36:39]\n"
        "ds_read_b128 v[16:19], v0 offset:512\n"
        "v_mfma_f32_16x16x32_bf16 v[40:43], a[128:131], v[20:23], v[40:43]\n"
        "v_mfma_f32_16x16x32_bf16 v[44:47], a[132:135], v[20:23], v[44:47]\n"
        "ds_read_b128 v[20:23], v0 offset:18944\n"
        "s_waitcnt lgkmcnt(4)\n"
        "v_mfma_f32_16x16x32_bf16 v[32:35], a[136:139], v[24:27], v[32:35]\n"
        "v_mfma_f32_16x16x32_bf16 v[36:39], a[140:143], v[24:27], v[36:39]\n"
        "ds_read_b128 v[24:27], v0 offset:1024\n"
        "v_mfma_f32_16x16x32_bf16 v[40:43], a[136:139], v[28:31], v[40:43]\n"
        "v_mfma_f32_16x16x32_bf16 v[44:47], a[140:143], v[28:31], v[44:47]\n"
        "ds_read_b128 v[28:31], v0 offset:19456\n"
        "s_waitcnt lgkmcnt(4)\n"
        "s_branch .Ls1_p10_%=\n"
        ".Ls1_p11_%=:\n"
        "v_mfma_f32_16x16x32_bf16 v[48:51], v[8:11], a[144:147], 0\n"
        "s_add_u32 m0, s77, s61\n"
        "v_mfma_f32_16x16x32_bf16 v[52:55], v[8:11], a[148:151], 0\n"
        "ds_read_b128 v[8:11], v0 offset:1536\n"
        "v_mfma_f32_16x16x32_bf16 v[56:59], v[12:15], a[144:147], 0\n"
        "v_cvt_pk_bf16_f32 v32, v32, v33\n"
        "v_cvt_pk_bf16_f32 v33, v34, v35\n"
        "v_mfma_f32_16x16x32_bf16 v[60:63], v[12:15], a[148:151], 0\n"
        "ds_read_b128 v[12:15], v0 offset:19968\n"
        "s_waitcnt lgkmcnt(4)\n"
        "v_mfma_f32_16x16x32_bf16 v[48:51], v[16:19], a[152:155], v[48:51]\n"
        "v_cvt_pk_bf16_f32 v34, v36, v37\n"
        "v_cvt_pk_bf16_f32 v35, v38, v39\n"
        "v_mfma_f32_16x16x32_bf16 v[52:55], v[16:19], a[156:159], v[52:55]\n"
        "ds_read_b128 v[16:19], v0 offset:2048\n"
        "v_mfma_f32_16x16x32_bf16 v[56:59], v[20:23], a[152:155], v[56:59]\n"
        "buffer_store_dwordx4 v[32:35], v1, s[40:43], s72 offen offset:0 sc1\n"
        "v_mfma_f32_16x16x32_bf16 v[60:63], v[20:23], a[156:159], v[60:63]\n"
        "ds_read_b128 v[20:23], v0 offset:20480\n"
        "s_waitcnt lgkmcnt(4)\n"
        "v_mfma_f32_16x16x32_bf16 v[48:51], v[24:27], a[160:163], v[48:51]\n"
        "v_cvt_pk_bf16_f32 v40, v40, v41\n"
        "v_cvt_pk_bf16_f32 v41, v42, v43\n"
        "v_mfma_f32_16x16x32_bf16 v[52:55], v[24:27], a[164:167], v[52:55]\n"
        "ds_read_b128 v[24:27], v0 offset:2560\n"
        "v_cvt_pk_bf16_f32 v42, v44, v45\n"
        "v_mfma_f32_16x16x32_bf16 v[56:59], v[28:31], a[160:163], v[56:59]\n"
        "v_cvt_pk_bf16_f32 v43, v46, v47\n"
        "v_mfma_f32_16x16x32_bf16 v[60:63], v[28:31], a[164:167], v[60:63]\n"
        "ds_read_b128 v[28:31], v0 offset:20992\n"
        "s_waitcnt lgkmcnt(4)\n"
        "v_mfma_f32_16x16x32_bf16 v[48:51], v[8:11], a[168:171], v[48:51]\n"
        "s_bitcmp1_b64 s[52:53], s69\n"
        "s_cselect_b32 s65, 8192, 0\n"
        "v_mfma_f32_16x16x32_bf16 v[52:55], v[8:11], a[172:175], v[52:55]\n"
        "ds_read_b128 v[8:11], v0 offset:3072\n"
        "s_lshl_b32 s71, s69, 21\n"
        "v_mfma_f32_16x16x32_bf16 v[56:59], v[12:15], a[168:171], v[56:59]\n"
        "buffer_store_dwordx4 v[40:43], v1, s[40:43], s72 offen offset:1024 sc1\n"
        "v_mfma_f32_16x16x32_bf16 v[60:63], v[12:15], a[172:175], v[60:63]\n"
        "ds_read_b128 v[12:15], v0 offset:21504\n"
        "s_waitcnt lgkmcnt(4)\n"
        "v_mfma_f32_16x16x32_bf16 v[48:51], v[16:19], a[176:179], v[48:51]\n"
        "s_add_u32 s71, s71, s65\n"
        "s_add_u32 s71, s71, s58\n"
        "v_mfma_f32_16x16x32_bf16 v[52:55], v[16:19], a[180:183], v[52:55]\n"
        "ds_read_b128 v[16:19], v0 offset:3584\n"
        "buffer_load_dwordx4 v1, s[40:43], s71 offen offset:0 sc1 lds\n"
        "v_mfma_f32_16x16x32_bf16 v[56:59], v[20:23], a[176:179], v[56:59]\n"
        "s_add_u32 s69, s69, 1\n"
        "s_add_u32 s77, s77, 36864\n"
        "v_mfma_f32_16x16x32_bf16 v[60:63], v[20:23], a[180:183], v[60:63]\n"
        "ds_read_b128 v[20:23], v0 offset:22016\n"
        "s_waitcnt lgkmcnt(4)\n"
        "v_mfma_f32_16x16x32_bf16 v[48:51], v[24:27], a[184:187], v[48:51]\n"
        "s_cmp_eq_u32 s77, 110592\n"
        "s_cselect_b32 s77, 0, s77\n"
        "v_mfma_f32_16x16x32_bf16 v[52:55], v[24:27], a[188:191], v[52:55]\n"
        "ds_read_b128 v[24:27], v0 offset:4096\n"
        "s_mov_b32 s70, 2147483647\n"
        "v_mfma_f32_16x16x32_bf16 v[56:59], v[28:31], a[184:187], v[56:59]\n"
        "buffer_load_dwordx4 v1, s[40:43], s71 offen offset:1024 sc1 lds\n"
        "v_mfma_f32_16x16x32_bf16 v[60:63], v[28:31], a[188:191], v[60:63]\n"
        "ds_read_b128 v[28:31], v0 offset:22528\n"
        "s_waitcnt lgkmcnt(4)\n"
        "v_mfma_f32_16x16x32_bf16 v[48:51], v[8:11], a[192:195], v[48:51]\n"
        "v_mfma_f32_16x16x32_bf16 v[52:55], v[8:11], a[196:199], v[52:55]\n"
        "ds_read_b128 v[8:11], v0 offset:4608\n"
        "v_mfma_f32_16x16x32_bf16 v[56:59], v[12:15], a[192:195], v[56:59]\n"
        "v_mfma_f32_16x16x32_bf16 v[60:63], v[12:15], a[196:199], v[60:63]\n"
        "ds_read_b128 v[12:15], v0 offset:23040\n"
        "s_waitcnt lgkmcnt(4)\n"
        "v_mfma_f32_16x16x32_bf16 v[48:51], v[16:19], a[200:203], v[48:51]\n"
        "buffer_load_dwordx4 v1, s[40:43], s71 offen offset:2048 sc1 lds\n"
        "v_mfma_f32_16x16x32_bf16 v[52:55], v[16:19], a[204:207], v[52:55]\n"
        "ds_read_b128 v[16:19], v0 offset:5120\n"
        "v_mfma_f32_16x16x32_bf16 v[56:59], v[20:23], a[200:203], v[56:59]\n"
        "v_mfma_f32_16x16x32_bf16 v[60:63], v[20:23], a[204:207], v[60:63]\n"
        "ds_read_b128 v[20:23], v0 offset:23552\n"
        "s_waitcnt lgkmcnt(4)\n"
        "v_mfma_f32_16x16x32_bf16 v[48:51], v[24:27], a[208:211], v[48:51]\n"
        "v_mfma_f32_16x16x32_bf16 v[52:55], v[24:27], a[212:215], v[52:55]\n"
        "ds_read_b128 v[24:27], v0 offset:5632\n"
        "v_mfma_f32_16x16x32_bf16 v[56:59], v[28:31], a[208:211], v[56:59]\n"
        "buffer_load_dwordx4 v1, s[40:43], s71 offen offset:3072 sc1 lds\n"
        "s_add_u32 m0, m0, 4096\n"
        "v_mfma_f32_16x16x32_bf16 v[60:63], v[28:31], a[212:215], v[60:63]\n"
        "ds_read_b128 v[28:31], v0 offset:24064\n"
        "s_waitcnt lgkmcnt(4)\n"
        "v_mfma_f32_16x16x32_bf16 v[48:51], v[8:11], a[216:219], v[48:51]\n"
        "s_add_u32 s71, s71, 4096\n"
        "s_mul_i32 s65, s48, -15360\n"
        "v_mfma_f32_16x16x32_bf16 v[52:55], v[8:11], a[220:223], v[52:55]\n"
        "ds_read_b128 v[8:11], v0 offset:6144\n"
        "v_mfma_f32_16x16x32_bf16 v[56:59], v[12:15], a[216:219], v[56:59]\n"
        "v_mfma_f32_16x16x32_bf16 v[60:63], v[12:15], a[220:223], v[60:63]\n"
        "ds_read_b128 v[12:15], v0 offset:24576\n"
        "s_waitcnt lgkmcnt(4)\n"
        "v_mfma_f32_16x16x32_bf16 v[48:51], v[16:19], a[224:227], v[48:51]\n"
        "buffer_load_dwordx4 v1, s[40:43], s71 offen offset:0 sc1 lds\n"
        "v_mfma_f32_16x16x32_bf16 v[52:55], v[16:19], a[228:231], v[52:55]\n"
        "ds_read_b128 v[16:19], v0 offset:6656\n"
        "v_mfma_f32_16x16x32_bf16 v[56:59], v[20:23], a[224:227], v[56:59]\n"
        "v_mfma_f32_16x16x32_bf16 v[60:63], v[20:23], a[228:231], v[60:63]\n"
        "ds_read_b128 v[20:23], v0 offset:25088\n"
        "s_waitcnt lgkmcnt(4)\n"
        "v_mfma_f32_16x16x32_bf16 v[48:51], v[24:27], a[232:235], v[48:51]\n"
        "s_waitcnt vmcnt(5)\n"
        "v_mov_b32 v5, s74\n"
        "v_mfma_f32_16x16x32_bf16 v[52:55], v[24:27], a[236:239], v[52:55]\n"
        "ds_read_b128 v[24:27], v0 offset:7168\n"
        "buffer_store_dword v5, v3, s[40:43], s73 offen sc1\n"
        "v_mfma_f32_16x16x32_bf16 v[56:59], v[28:31], a[232:235], v[56:59]\n"
        "buffer_load_dwordx4 v1, s[40:43], s71 offen offset:1024 sc1 lds\n"
        "s_add_u32 s74, s74, 1\n"
        "v_mfma_f32_16x16x32_bf16 v[60:63], v[28:31], a[236:239], v[60:63]\n"
        "ds_read_b128 v[28:31], v0 offset:25600\n"
        "s_waitcnt lgkmcnt(4)\n"
        "v_mfma_f32_16x16x32_bf16 v[48:51], v[8:11], a[240:243], v[48:51]\n"
        "s_add_u32 s72, s75, 1048576\n"
        "v_mfma_f32_16x16x32_bf16 v[52:55], v[8:11], a[244:247], v[52:55]\n"
        "ds_read_b128 v[8:11], v0 offset:7680\n"
        "v_mfma_f32_16x16x32_bf16 v[56:59], v[12:15], a[240:243], v[56:59]\n"
        "v_mfma_f32_16x16x32_bf16 v[60:63], v[12:15], a[244:247], v[60:63]\n"
        "ds_read_b128 v[12:15], v0 offset:26112\n"
        "s_waitcnt lgkmcnt(4)\n"
        "v_mfma_f32_16x16x32_bf16 v[48:51], v[16:19], a[248:251], v[48:51]\n"
        "buffer_load_dwordx4 v1, s[40:43], s71 offen offset:2048 sc1 lds\n"
        "v_mfma_f32_16x16x32_bf16 v[52:55], v[16:19], a[252:255], v[52:55]\n"
        "ds_read_b128 v[16:19], v0 offset:8192\n"
        "v_mfma_f32_16x16x32_bf16 v[56:59], v[20:23], a[248:251], v[56:59]\n"
        "v_mfma_f32_16x16x32_bf16 v[60:63], v[20:23], a[252:255], v[60:63]\n"
        "ds_read_b128 v[20:23], v0 offset:26624\n"
        "s_waitcnt lgkmcnt(4)\n"
        "v_mfma_f32_16x16x32_bf16 v[48:51], v[24:27], v[80:83], v[48:51]\n"
        "v_mfma_f32_16x16x32_bf16 v[52:55], v[24:27], v[84:87], v[52:55]\n"
        "ds_read_b128 v[24:27], v0 offset:8704\n"
        "v_mfma_f32_16x16x32_bf16 v[56:59], v[28:31], v[80:83], v[56:59]\n"
        "buffer_load_dwordx4 v1, s[40:43], s71 offen offset:3072 sc1 lds\n"
        "s_add_u32 m0, m0, 4096\n"
        "v_mfma_f32_16x16x32_bf16 v[60:63], v[28:31], v[84:87], v[60:63]\n"
        "ds_read_b128 v[28:31], v0 offset:27136\n"
        "s_waitcnt lgkmcnt(4)\n"
        "v_mfma_f32_16x16x32_bf16 v[48:51], v[8:11], v[88:91], v[48:51]\n"
        "s_add_u32 s71, s71, s65\n"
        "s_add_u32 s71, s71, 61440\n"
        "v_mfma_f32_16x16x32_bf16 v[52:55], v[8:11], v[92:95], v[52:55]\n"
        "ds_read_b128 v[8:11], v0 offset:0\n"
        "v_mfma_f32_16x16x32_bf16 v[56:59], v[12:15], v[88:91], v[56:59]\n"
        "v_mfma_f32_16x16x32_bf16 v[60:63], v[12:15], v[92:95], v[60:63]\n"
        "ds_read_b128 v[12:15], v0 offset:18432\n"
        "s_waitcnt lgkmcnt(4)\n"
        "v_mfma_f32_16x16x32_bf16 v[48:51], v[16:19], v[96:99], v[48:51]\n"
        "buffer_load_dwordx4 v1, s[40:43], s71 offen offset:0 sc1 lds\n"
        "v_mfma_f32_16x16x32_bf16 v[52:55], v[16:19], v[100:103], v[52:55]\n"
        "ds_read_b128 v[16:19], v0 offset:512\n"
        "v_mfma_f32_16x16x32_bf16 v[56:59], v[20:23], v[96:99], v[56:59]\n"
        "v_mfma_f32_16x16x32_bf16 v[60:63], v[20:23], v[100:103], v[60:63]\n"
        "ds_read_b128 v[20:23], v0 offset:18944\n"
        "s_waitcnt lgkmcnt(4)\n"
        "v_mfma_f32_16x16x32_bf16 v[48:51], v[24:27], v[104:107], v[48:51]\n"
        "v_mfma_f32_16x16x32_bf16 v[52:55], v[24:27], v[108:111], v[52:55]\n"
        "ds_read_b128 v[24:27], v0 offset:1024\n"
        "v_mfma_f32_16x16x32_bf16 v[56:59], v[28:31], v[104:107], v[56:59]\n"
        "v_mfma_f32_16x16x32_bf16 v[60:63], v[28:31], v[108:111], v[60:63]\n"
        "ds_read_b128 v[28:31], v0 offset:19456\n"
        "s_waitcnt lgkmcnt(4)\n"
        "s_branch .Ls1_p20_%=\n"
        ".Ls1_p21_%=:\n"
        "v_mfma_f32_16x16x32_bf16 v[64:67], v[112:115], v[8:11], 0\n"
        "s_add_u32 m0, s77, s61\n"
        "v_mfma_f32_16x16x32_bf16 v[68:71], v[116:119], v[8:11], 0\n"
        "ds_read_b128 v[8:11], v0 offset:1536\n"
        "v_mfma_f32_16x16x32_bf16 v[72:75], v[112:115], v[12:15], 0\n"
        "v_cvt_pk_bf16_f32 v48, v48, v49\n"
        "v_cvt_pk_bf16_f32 v49, v50, v51\n"
        "v_mfma_f32_16x16x32_bf16 v[76:79], v[116:119], v[12:15], 0\n"
        "ds_read_b128 v[12:15], v0 offset:19968\n"
        "s_waitcnt lgkmcnt(4)\n"
        "v_mfma_f32_16x16x32_bf16 v[64:67], v[120:123], v[16:19], v[64:67]\n"
        "v_cvt_pk_bf16_f32 v50, v56, v57\n"
        "v_cvt_pk_bf16_f32 v51, v58, v59\n"
        "v_mfma_f32_16x16x32_bf16 v[68:71], v[124:127], v[16:19], v[68:71]\n"
        "ds_read_b128 v[16:19], v0 offset:2048\n"
        "v_mfma_f32_16x16x32_bf16 v[72:75], v[120:123], v[20:23], v[72:75]\n"
        "buffer_store_dwordx4 v[48:51], v1, s[40:43], s72 offen offset:0 sc1\n"
        "v_mfma_f32_16x16x32_bf16 v[76:79], v[124:127], v[20:23], v[76:79]\n"
        "ds_read_b128 v[20:23], v0 offset:20480\n"
        "s_waitcnt lgkmcnt(4)\n"
        "v_mfma_f32_16x16x32_bf16 v[64:67], v[128:131], v[24:27], v[64:67]\n"
        "v_cvt_pk_bf16_f32 v52, v52, v53\n"
        "v_cvt_pk_bf16_f32 v53, v54, v55\n"
        "v_mfma_f32_16x16x32_bf16 v[68:71], v[132:135], v[24:27], v[68:71]\n"
        "ds_read_b128 v[24:27], v0 offset:2560\n"
        "v_cvt_pk_bf16_f32 v54, v60, v61\n"
        "v_mfma_f32_16x16x32_bf16 v[72:75], v[128:131], v[28:31], v[72:75]\n"
        "v_cvt_pk_bf16_f32 v55, v62, v63\n"
        "v_mfma_f32_16x16x32_bf16 v[76:79], v[132:135], v[28:31], v[76:79]\n"
        "ds_read_b128 v[28:31], v0 offset:20992\n"
        "s_waitcnt lgkmcnt(4)\n"
        "v_mfma_f32_16x16x32_bf16 v[64:67], v[136:139], v[8:11], v[64:67]\n"
        "s_bitcmp1_b64 s[52:53], s69\n"
        "s_cselect_b32 s65, 8192, 0\n"
        "v_mfma_f32_16x16x32_bf16 v[68:71], v[140:143], v[8:11], v[68:71]\n"
        "ds_read_b128 v[8:11], v0 offset:3072\n"
        "s_lshl_b32 s71, s69, 21\n"
        "v_mfma_f32_16x16x32_bf16 v[72:75], v[136:139], v[12:15], v[72:75]\n"
        "buffer_store_dwordx4 v[52:55], v1, s[40:43], s72 offen offset:1024 sc1\n"
        "v_mfma_f32_16x16x32_bf16 v[76:79], v[140:143], v[12:15], v[76:79]\n"
        "ds_read_b128 v[12:15], v0 offset:21504\n"
        "s_waitcnt lgkmcnt(4)\n"
        "v_mfma_f32_16x16x32_bf16 v[64:67], v[144:147], v[16:19], v[64:67]\n"
        "s_add_u32 s71, s71, s65\n"
        "s_add_u32 s71, s71, s58\n"
        "v_mfma_f32_16x16x32_bf16 v[68:71], v[148:151], v[16:19], v[68:71]\n"
        "ds_read_b128 v[16:19], v0 offset:3584\n"
        "buffer_load_dwordx4 v1, s[40:43], s71 offen offset:0 sc1 lds\n"
        "v_mfma_f32_16x16x32_bf16 v[72:75], v[144:147], v[20:23], v[72:75]\n"
        "s_add_u32 s69, s69, 1\n"
        "s_add_u32 s77, s77, 36864\n"
        "v_mfma_f32_16x16x32_bf16 v[76:79], v[148:151], v[20:23], v[76:79]\n"
        "ds_read_b128 v[20:23], v0 offset:22016\n"
        "s_waitcnt lgkmcnt(4)\n"
        "v_mfma_f32_16x16x32_bf16 v[64:67], v[152:155], v[24:27], v[64:67]\n"
        "buffer_load_dwordx4 v1, s[40:43], s71 offen offset:1024 sc1 lds\n"
        "v_mfma_f32_16x16x32_bf16 v[68:71], v[156:159], v[24:27], v[68:71]\n"
        "ds_read_b128 v[24:27], v0 offset:4096\n"
        "v_mfma_f32_16x16x32_bf16 v[72:75], v[152:155], v[28:31], v[72:75]\n"
        "s_cmp_eq_u32 s77, 110592\n"
        "s_cselect_b32 s77, 0, s77\n"
        "v_mfma_f32_16x16x32_bf16 v[76:79], v[156:159], v[28:31], v[76:79]\n"
        "ds_read_b128 v[28:31], v0 offset:22528\n"
        "s_waitcnt lgkmcnt(4)\n"
        "v_mfma_f32_16x16x32_bf16 v[64:67], v[160:163], v[8:11], v[64:67]\n"
        "buffer_load_dwordx4 v1, s[40:43], s71 offen offset:2048 sc1 lds\n"
        "s_mov_b32 s70, 2147483647\n"
        "v_mfma_f32_16x16x32_bf16 v[68:71], v[164:167], v[8:11], v[68:71]\n"
        "ds_read_b128 v[8:11], v0 offset:4608\n"
        "v_mfma_f32_16x16x32_bf16 v[72:75], v[160:163], v[12:15], v[72:75]\n"
        "v_mfma_f32_16x16x32_bf16 v[76:79], v[164:167], v[12:15], v[76:79]\n"
        "ds_read_b128 v[12:15], v0 offset:23040\n"
        "s_waitcnt lgkmcnt(4)\n"
        "v_mfma_f32_16x16x32_bf16 v[64:67], v[168:171], v[16:19], v[64:67]\n"
        "buffer_load_dwordx4 v1, s[40:43], s71 offen offset:3072 sc1 lds\n"
        "s_add_u32 m0, m0, 4096\n"
        "v_mfma_f32_16x16x32_bf16 v[68:71], v[172:175], v[16:19], v[68:71]\n"
        "ds_read_b128 v[16:19], v0 offset:5120\n"
        "s_add_u32 s71, s71, 4096\n"
        "v_mfma_f32_16x16x32_bf16 v[72:75], v[168:171], v[20:23], v[72:75]\n"
        "s_mul_i32 s65, s48, -15360\n"
        "v_mfma_f32_16x16x32_bf16 v[76:79], v[172:175], v[20:23], v[76:79]\n"
        "ds_read_b128 v[20:23], v0 offset:23552\n"
        "s_waitcnt lgkmcnt(4)\n"
        "v_mfma_f32_16x16x32_bf16 v[64:67], v[176:179], v[24:27], v[64:67]\n"
        "buffer_load_dword v4, v2, s[40:43], s76 offen sc1\n"
        "v_mfma_f32_16x16x32_bf16 v[68:71], v[180:183], v[24:27], v[68:71]\n"
        "ds_read_b128 v[24:27], v0 offset:5632\n"
        "v_mfma_f32_16x16x32_bf16 v[72:75], v[176:179], v[28:31], v[72:75]\n"
        "buffer_load_dwordx4 v1, s[40:43], s71 offen offset:0 sc1 lds\n"
        "v_mfma_f32_16x16x32_bf16 v[76:79], v[180:183], v[28:31], v[76:79]\n"
        "ds_read_b128 v[28:31], v0 offset:24064\n"
        "s_waitcnt lgkmcnt(4)\n"
        "v_mfma_f32_16x16x32_bf16 v[64:67], v[184:187], v[8:11], v[64:67]\n"
        "v_mfma_f32_16x16x32_bf16 v[68:71], v[188:191], v[8:11], v[68:71]\n"
        "ds_read_b128 v[8:11], v0 offset:6144\n"
        "v_mfma_f32_16x16x32_bf16 v[72:75], v[184:187], v[12:15], v[72:75]\n"
        "v_mfma_f32_16x16x32_bf16 v[76:79], v[188:191], v[12:15], v[76:79]\n"
        "ds_read_b128 v[12:15], v0 offset:24576\n"
        "s_waitcnt lgkmcnt(4)\n"
        "v_mfma_f32_16x16x32_bf16 v[64:67], v[192:195], v[16:19], v[64:67]\n"
        "buffer_load_dwordx4 v1, s[40:43], s71 offen offset:1024 sc1 lds\n"
        "s_add_u32 s78, s78, 36864\n"
        "v_mfma_f32_16x16x32_bf16 v[68:71], v[196:199], v[16:19], v[68:71]\n"
        "ds_read_b128 v[16:19], v0 offset:6656\n"
        "v_mfma_f32_16x16x32_bf16 v[72:75], v[192:195], v[20:23], v[72:75]\n"
        "s_cmp_eq_u32 s78, 110592\n"
        "s_cselect_b32 s78, 0, s78\n"
        "v_mfma_f32_16x16x32_bf16 v[76:79], v[196:199], v[20:23], v[76:79]\n"
        "ds_read_b128 v[20:23], v0 offset:25088\n"
        "s_waitcnt lgkmcnt(4)\n"
        "v_mfma_f32_16x16x32_bf16 v[64:67], v[200:203], v[24:27], v[64:67]\n"
        "s_waitcnt vmcnt(7)\n"
        "v_mov_b32 v5, s74\n"
        "v_mfma_f32_16x16x32_bf16 v[68:71], v[204:207], v[24:27], v[68:71]\n"
        "ds_read_b128 v[24:27], v0 offset:7168\n"
        "buffer_store_dword v5, v3, s[40:43], s73 offen sc1\n"
        "v_mfma_f32_16x16x32_bf16 v[72:75], v[200:203], v[28:31], v[72:75]\n"
        "buffer_load_dwordx4 v1, s[40:43], s71 offen offset:2048 sc1 lds\n"
        "s_add_u32 s74, s74, 1\n"
        "v_mfma_f32_16x16x32_bf16 v[76:79], v[204:207], v[28:31], v[76:79]\n"
        "ds_read_b128 v[28:31], v0 offset:25600\n"
        "s_waitcnt lgkmcnt(4)\n"
        "v_mfma_f32_16x16x32_bf16 v[64:67], v[208:211], v[8:11], v[64:67]\n"
        "s_add_u32 s72, s75, 0\n"
        "v_mfma_f32_16x16x32_bf16 v[68:71], v[212:215], v[8:11], v[68:71]\n"
        "ds_read_b128 v[8:11], v0 offset:7680\n"
        "v_mfma_f32_16x16x32_bf16 v[72:75], v[208:211], v[12:15], v[72:75]\n"
        "buffer_load_dwordx4 v1, s[40:43], s71 offen offset:3072 sc1 lds\n"
        "s_add_u32 m0, m0, 4096\n"
        "v_mfma_f32_16x16x32_bf16 v[76:79], v[212:215], v[12:15], v[76:79]\n"
        "ds_read_b128 v[12:15], v0 offset:26112\n"
        "s_waitcnt lgkmcnt(4)\n"
        "v_mfma_f32_16x16x32_bf16 v[64:67], v[216:219], v[16:19], v[64:67]\n"
        "s_add_u32 s71, s71, s65\n"
        "s_add_u32 s71, s71, 61440\n"
        "v_mfma_f32_16x16x32_bf16 v[68:71], v[220:223], v[16:19], v[68:71]\n"
        "ds_read_b128 v[16:19], v0 offset:8192\n"
        "v_mfma_f32_16x16x32_bf16 v[72:75], v[216:219], v[20:23], v[72:75]\n"
        "v_mfma_f32_16x16x32_bf16 v[76:79], v[220:223], v[20:23], v[76:79]\n"
        "ds_read_b128 v[20:23], v0 offset:26624\n"
        "s_waitcnt lgkmcnt(4)\n"
        "v_mfma_f32_16x16x32_bf16 v[64:67], v[224:227], v[24:27], v[64:67]\n"
        "buffer_load_dwordx4 v1, s[40:43], s71 offen offset:0 sc1 lds\n"
        "v_mfma_f32_16x16x32_bf16 v[68:71], v[228:231], v[24:27], v[68:71]\n"
        "ds_read_b128 v[24:27], v0 offset:8704\n"
        "v_mfma_f32_16x16x32_bf16 v[72:75], v[224:227], v[28:31], v[72:75]\n"
        "v_mfma_f32_16x16x32_bf16 v[76:79], v[228:231], v[28:31], v[76:79]\n"
        "ds_read_b128 v[28:31], v0 offset:27136\n"
        "s_waitcnt lgkmcnt(4)\n"
        "v_mfma_f32_16x16x32_bf16 v[64:67], v[232:235], v[8:11], v[64:67]\n"
        "s_waitcnt vmcnt(7)\n"
        "s_barrier\n"
        "v_add_u32 v0, s78, v6\n"
        "v_mfma_f32_16x16x32_bf16 v[68:71], v[236:239], v[8:11], v[68:71]\n"
        "ds_read_b128 v[8:11], v0 offset:0\n"
        "v_mfma_f32_16x16x32_bf16 v[72:75], v[232:235], v[12:15], v[72:75]\n"
        "s_cmp_lt_u32 s69, 64\n"
        "s_cselect_b32 s70, s69, 2147483647\n"
        "v_mfma_f32_16x16x32_bf16 v[76:79], v[236:239], v[12:15], v[76:79]\n"
        "ds_read_b128 v[12:15], v0 offset:18432\n"
        "s_waitcnt lgkmcnt(4)\n"
        "v_mfma_f32_16x16x32_bf16 v[64:67], v[240:243], v[16:19], v[64:67]\n"
        "v_mfma_f32_16x16x32_bf16 v[68:71], v[244:247], v[16:19], v[68:71]\n"
        "ds_read_b128 v[16:19], v0 offset:512\n"
        "v_mfma_f32_16x16x32_bf16 v[72:75], v[240:243], v[20:23], v[72:75]\n"
        "v_mfma_f32_16x16x32_bf16 v[76:79], v[244:247], v[20:23], v[76:79]\n"
        "ds_read_b128 v[20:23], v0 offset:18944\n"
        "s_waitcnt lgkmcnt(4)\n"
        "v_mfma_f32_16x16x32_bf16 v[64:67], v[248:251], v[24:27], v[64:67]\n"
        "s_waitcnt vmcnt(6)\n"
        "v_cmp_gt_i32 vcc, s70, v4\n"
        "v_mfma_f32_16x16x32_bf16 v[68:71], v[252:255], v[24:27], v[68:71]\n"
        "ds_read_b128 v[24:27], v0 offset:1024\n"
        "v_mfma_f32_16x16x32_bf16 v[72:75], v[248:251], v[28:31], v[72:75]\n"
        "s_and_b64 vcc, vcc, 1\n"
        "v_mfma_f32_16x16x32_bf16 v[76:79], v[252:255], v[28:31], v[76:79]\n"
        "ds_read_b128 v[28:31], v0 offset:19456\n"
        "s_waitcnt lgkmcnt(4)\n"
        "s_cbranch_vccz .Ls1_p01_%=\n"
        "s_branch .Ls1_p00_%=\n"
        ".Ls1_tail_%=:\n"
        "v_mfma_f32_16x16x32_bf16 v[68:71], v[236:239], v[8:11], v[68:71]\n"
        "v_mfma_f32_16x16x32_bf16 v[72:75], v[232:235], v[12:15], v[72:75]\n"
        "v_mfma_f32_16x16x32_bf16 v[76:79], v[236:239], v[12:15], v[76:79]\n"
        "s_waitcnt lgkmcnt(2)\n"
        "v_mfma_f32_16x16x32_bf16 v[64:67], v[240:243], v[16:19], v[64:67]\n"
        "v_mfma_f32_16x16x32_bf16 v[68:71], v[244:247], v[16:19], v[68:71]\n"
        "v_mfma_f32_16x16x32_bf16 v[72:75], v[240:243], v[20:23], v[72:75]\n"
        "v_mfma_f32_16x16x32_bf16 v[76:79], v[244:247], v[20:23], v[76:79]\n"
        "s_waitcnt lgkmcnt(0)\n"
        "v_mfma_f32_16x16x32_bf16 v[64:67], v[248:251], v[24:27], v[64:67]\n"
        "v_mfma_f32_16x16x32_bf16 v[68:71], v[252:255], v[24:27], v[68:71]\n"
        "v_mfma_f32_16x16x32_bf16 v[72:75], v[248:251], v[28:31], v[72:75]\n"
        "v_mfma_f32_16x16x32_bf16 v[76:79], v[252:255], v[28:31], v[76:79]\n"
        "s_waitcnt vmcnt(1)\n"
        "buffer_load_dword v4, v2, s[40:43], s76 offen sc1\n"
        "s_nop 5\n"
        "v_cvt_pk_bf16_f32 v64, v64, v65\n"
        "v_cvt_pk_bf16_f32 v65, v66, v67\n"
        "v_cvt_pk_bf16_f32 v66, v68, v69\n"
        "v_cvt_pk_bf16_f32 v67, v70, v71\n"
        "buffer_store_dwordx4 v[64:67], v1, s[40:43], s72 offen offset:0 sc1\n"
        "v_cvt_pk_bf16_f32 v72, v72, v73\n"
        "v_cvt_pk_bf16_f32 v73, v74, v75\n"
        "v_cvt_pk_bf16_f32 v74, v76, v77\n"
        "v_cvt_pk_bf16_f32 v75, v78, v79\n"
        "buffer_store_dwordx4 v[72:75], v1, s[40:43], s72 offen offset:1024 sc1\n"
        "s_waitcnt vmcnt(2)\n"
        "v_cmp_gt_i32 vcc, s70, v4\n"
        "s_nop 0\n"
        "s_and_b64 vcc, vcc, 1\n"
        "s_cbranch_vccnz .Ls1_late_%=\n"
        "s_add_u32 m0, s77, s61\n"
        "s_bitcmp1_b64 s[52:53], s69\n"
        "s_cselect_b32 s65, 8192, 0\n"
        "s_lshl_b32 s71, s69, 21\n"
        "s_add_u32 s71, s71, s65\n"
        "s_add_u32 s71, s71, s58\n"
        "buffer_load_dwordx4 v1, s[40:43], s71 offen offset:0 sc1 lds\n"
        "buffer_load_dwordx4 v1, s[40:43], s71 offen offset:1024 sc1 lds\n"
        "buffer_load_dwordx4 v1, s[40:43], s71 offen offset:2048 sc1 lds\n"
        "buffer_load_dwordx4 v1, s[40:43], s71 offen offset:3072 sc1 lds\n"
        "s_add_u32 m0, m0, 4096\n"
        "s_add_u32 s71, s71, 4096\n"
        "s_mul_i32 s65, s48, -15360\n"
        "buffer_load_dwordx4 v1, s[40:43], s71 offen offset:0 sc1 lds\n"
        "buffer_load_dwordx4 v1, s[40:43], s71 offen offset:1024 sc1 lds\n"
        "buffer_load_dwordx4 v1, s[40:43], s71 offen offset:2048 sc1 lds\n"
        "buffer_load_dwordx4 v1, s[40:43], s71 offen offset:3072 sc1 lds\n"
        "s_add_u32 m0, m0, 4096\n"
        "s_add_u32 s71, s71, s65\n"
        "s_add_u32 s71, s71, 61440\n"
        "buffer_load_dwordx4 v1, s[40:43], s71 offen offset:0 sc1 lds\n"
        "s_add_u32 s69, s69, 1\n"
        "s_add_u32 s77, s77, 36864\n"
        "s_cmp_eq_u32 s77, 110592\n"
        "s_cselect_b32 s77, 0, s77\n"
        "s_mov_b32 s70, 2147483647\n"
        "s_waitcnt vmcnt(9)\n"
        "v_mov_b32 v5, s74\n"
        "buffer_store_dword v5, v3, s[40:43], s73 offen sc1\n"
        "s_waitcnt vmcnt(6)\n"
        "s_branch .Ls1_go_%=\n"
        ".Ls1_late_%=:\n"
        "s_waitcnt vmcnt(0)\n"
        "v_mov_b32 v5, s74\n"
        "buffer_store_dword v5, v3, s[40:43], s73 offen sc1\n"
        "s_cmp_eq_u32 s68, 63\n"
        "s_cbranch_scc0 .Ls1_wait_%=\n"
        "s_endpgm\n"
        ".Ls1_wait_%=:\n"
        "s_mov_b32 s64, 0\n"
        ".Ls1_in_top_%=:\n"
        "buffer_load_dword v4, v2, s[40:43], s76 offen sc1\n"
        "s_waitcnt vmcnt(0)\n"
        "v_cmp_gt_i32 vcc, s69, v4\n"
        "s_nop 0\n"
        "s_and_b64 vcc, vcc, 1\n"
        "s_cbranch_vccz .Ls1_in_ok_%=\n"
        "s_add_u32 s64, s64, 1\n"
        "s_and_b32 s65, s64, 255\n"
        "s_cbranch_scc1 .Ls1_in_go_%=\n"
        "s_mov_b32 s65, 40960\n"
        "s_cmp_lt_u32 s64, 1048576\n"
        "s_cbranch_scc1 .Ls1_in_chk_%=\n"
        "v_mov_b32 v4, 1\n"
        "buffer_store_dword v4, off, s[40:43], s65 sc1\n"
        "s_waitcnt vmcnt(0)\n"
        ".Ls1_in_chk_%=:\n"
        "buffer_load_dword v4, off, s[40:43], s65 sc1\n"
        "s_waitcnt vmcnt(0)\n"
        "v_readfirstlane_b32 s65, v4\n"
        "s_nop 0\n"
        "s_cmp_lg_u32 s65, 1\n"
        "s_cbranch_scc1 .Ls1_in_go_%=\n"
        "s_endpgm\n"
        ".Ls1_in_go_%=:\n"
        "s_sleep 1\n"
        "s_branch .Ls1_in_top_%=\n"
        ".Ls1_in_ok_%=:\n"
        "s_add_u32 m0, s77, s61\n"
        "s_bitcmp1_b64 s[52:53], s69\n"
        "s_cselect_b32 s65, 8192, 0\n"
        "s_lshl_b32 s71, s69, 21\n"
        "s_add_u32 s71, s71, s65\n"
        "s_add_u32 s71, s71, s58\n"
        "buffer_load_dwordx4 v1, s[40:43], s71 offen offset:0 sc1 lds\n"
        "buffer_load_dwordx4 v1, s[40:43], s71 offen offset:1024 sc1 lds\n"
        "buffer_load_dwordx4 v1, s[40:43], s71 offen offset:2048 sc1 lds\n"
        "buffer_load_dwordx4 v1, s[40:43], s71 offen offset:3072 sc1 lds\n"
        "s_add_u32 m0, m0, 4096\n"
        "s_add_u32 s71, s71, 4096\n"
        "s_mul_i32 s65, s48, -15360\n"
        "buffer_load_dwordx4 v1, s[40:43], s71 offen offset:0 sc1 lds\n"
        "buffer_load_dwordx4 v1, s[40:43], s71 offen offset:1024 sc1 lds\n"
        "buffer_load_dwordx4 v1, s[40:43], s71 offen offset:2048 sc1 lds\n"
        "buffer_load_dwordx4 v1, s[40:43], s71 offen offset:3072 sc1 lds\n"
        "s_add_u32 m0, m0, 4096\n"
        "s_add_u32 s71, s71, s65\n"
        "s_add_u32 s71, s71, 61440\n"
        "buffer_load_dwordx4 v1, s[40:43], s71 offen offset:0 sc1 lds\n"
        "s_add_u32 s69, s69, 1\n"
        "s_add_u32 s77, s77, 36864\n"
        "s_cmp_eq_u32 s77, 110592\n"
        "s_cselect_b32 s77, 0, s77\n"
        "s_mov_b32 s70, 2147483647\n"
        "s_waitcnt vmcnt(5)\n"
        ".Ls1_go_%=:\n"
        "s_mov_b32 s72, 241172480\n"
        "s_mov_b32 s73, 241172480\n"
        "s_cmp_lt_u32 s69, 64\n"
        "s_cselect_b32 s70, s69, 2147483647\n"
        "v_add_u32 v0, s78, v6\n"
        "s_barrier\n"
        "ds_read_b128 v[8:11], v0 offset:0\n"
        "ds_read_b128 v[12:15], v0 offset:18432\n"
        "ds_read_b128 v[16:19], v0 offset:512\n"
        "ds_read_b128 v[20:23], v0 offset:18944\n"
        "ds_read_b128 v[24:27], v0 offset:1024\n"
        "ds_read_b128 v[28:31], v0 offset:19456\n"
        "v_cmp_gt_i32 vcc, s70, v4\n"
        "s_nop 0\n"
        "s_and_b64 vcc, vcc, 1\n"
        "s_waitcnt lgkmcnt(4)\n"
        "s_cbranch_vccz .Ls1_p01_%=\n"
        "s_branch .Ls1_p00_%=\n"
        :: "s"(p.ws), "s"(p.Wk), "s"(p.Wv), "s"(p.Wq), "s"(pair(w, delay)), "s"(pair(x4, pr)),
           "s"(pair(PAGE + spread(A_XN) + w * 16384, PAGE + h * 16384 + w * 2048)), "s"(pair(flag(F_S1 + 4 * h + w), w * 9216)),
           "v"(pair((i >> 3) * 9216 + g * 128 + (i & 7) * 16, lane * 16)), "v"(pair(lane == 0 ? flag(F_XN + 4 * h + w) : OOB, lane < 2 ? lane * 32768 : OOB)), "v"(pair(0, 0)),
           "v"(pair(vo, 0)), "v"(pair(tr + (g & 1) * 32, tr + (1 - (g & 1)) * 32))
        : "v0", "v1", "v2", "v3", "v4", "v5", "v6", "v7", "v8", "v9", "v10", "v11", "v12", "v13", "v14", "v15", "v16", "v17", "v18", "v19", "v20", "v21", "v22", "v23", "v24", "v25", "v26", "v27", "v28", "v29", "v30", "v31", "v32", "v33", "v34", "v35", "v36", "v37", "v38", "v39", "v40", "v41", "v42", "v43", "v44", "v45", "v46", "v47", "v48", "v49", "v50", "v51", "v52", "v53", "v54", "v55", "v56", "v57", "v58", "v59", "v60", "v61", "v62", "v63", "v64", "v65", "v66", "v67", "v68", "v69", "v70", "v71", "v72", "v73", "v74", "v75", "v76", "v77", "v78", "v79", "v255", "a255", "s34", "s35", "s36", "s37", "s38", "s39", "s40", "s41", "s42", "s43", "s44", "s45", "s46", "s47", "s48", "s49", "s50", "s51", "s52", "s53", "s54", "s55", "s56", "s57", "s58", "s59", "s60", "s61", "s62", "s63", "s64", "s65", "s66", "s67", "s68", "s69", "s70", "s71", "s72", "s73", "s74", "s75", "s76", "s77", "s78", "s79", "s80", "s81", "s82", "s83", "s84", "s85", "s86", "s87", "s88", "s89", "s90", "s91", "s92", "s93", "s94", "s95", "s96", "s97", "s98", "s99", "vcc", "scc", "memory");
    __builtin_unreachable();
}
__device__ __forceinline__ void role_f1(const Params& p, int kap, int jj, int x4, int pr, int delay) {
    GEMM_HEAD;
    const unsigned u = jj * 4 + w;
    unsigned vo[3];
#pragma unroll
    for (int n = 0; n < 3; ++n) vo[n] = ((64 * n + lane) / 6) * 9216 + (1152 * kap + 48 * u) * 2 + ((64 * n + lane) % 6) * 16 - 1024 * n;
    const unsigned tr = w * 40960 + (pp >> 1) * 3072 + (8 * (g >> 1) + q) * 96 + 16 * (pp & 1) + 8 * (g & 1);
    asm volatile(
        "s_nop 4\n"
        "s_mov_b64 s[40:41], %0\n"
        "s_and_b32 s41, s41, 0xffff\n"
        "s_mov_b32 s42, 236978176\n"
        "s_mov_b32 s43, 0x20000\n"
        "s_mov_b64 s[48:49], %4\n"
        "s_mov_b64 s[56:57], %5\n"
        "s_mov_b64 s[58:59], %6\n"
        "s_mov_b64 s[60:61], %7\n"
        "v_mov_b64 v[0:1], %8\n"
        "v_mov_b64 v[2:3], %9\n"
        "v_mov_b64 v[4:5], %10\n"
        "v_mov_b64 v[6:7], %11\n"
        "v_mov_b64 v[8:9], %12\n"
        "s_mov_b32 s47, 0x20000\n"
        "s_mul_i32 s79, s48, 40960\n"
        ".Lf1_start_%=:\n"
        "s_sub_u32 s49, s49, 1\n"
        "s_cbranch_scc1 .Lf1_start2_%=\n"
        "s_sleep 16\n"
        "s_branch .Lf1_start_%=\n"
        ".Lf1_start2_%=:\n"
        "s_mov_b64 s[44:45], %1\n"
        "s_and_b32 s45, s45, 0xffff\n"
        "s_mov_b32 s46, 5308416\n"
        "s_add_u32 m0, s79, 0\n"
        "s_mov_b32 s65, 0\n"
        "buffer_load_dwordx4 v6, s[44:47], s65 offen lds\n"
        "buffer_load_dwordx4 v7, s[44:47], s65 offen offset:1024 lds\n"
        "buffer_load_dwordx4 v8, s[44:47], s65 offen offset:2048 lds\n"
        "s_mov_b64 s[44:45], %2\n"
        "s_and_b32 s45, s45, 0xffff\n"
        "s_mov_b32 s46, 5308416\n"
        "s_add_u32 m0, s79, 3072\n"
        "s_mov_b32 s65, 0\n"
        "buffer_load_dwordx4 v6, s[44:47], s65 offen lds\n"
        "buffer_load_dwordx4 v7, s[44:47], s65 offen offset:1024 lds\n"
        "buffer_load_dwordx4 v8, s[44:47], s65 offen offset:2048 lds\n"
        "s_mov_b64 s[44:45], %1\n"
        "s_and_b32 s45, s45, 0xffff\n"
        "s_mov_b32 s46, 5308416\n"
        "s_add_u32 m0, s79, 6144\n"
        "s_mov_b32 s65, 294912\n"
        "buffer_load_dwordx4 v6, s[44:47], s65 offen lds\n"
        "buffer_load_dwordx4 v7, s[44:47], s65 offen offset:1024 lds\n"
        "buffer_load_dwordx4 v8, s[44:47], s65 offen offset:2048 lds\n"
        "s_mov_b64 s[44:45], %2\n"
        "s_and_b32 s45, s45, 0xffff\n"
        "s_mov_b32 s46, 5308416\n"
        "s_add_u32 m0, s79, 9216\n"
        "s_mov_b32 s65, 294912\n"
        "buffer_load_dwordx4 v6, s[44:47], s65 offen lds\n"
        "buffer_load_dwordx4 v7, s[44:47], s65 offen offset:1024 lds\n"
        "buffer_load_dwordx4 v8, s[44:47], s65 offen offset:2048 lds\n"
        "s_mov_b64 s[44:45], %1\n"
        "s_and_b32 s45, s45, 0xffff\n"
        "s_mov_b32 s46, 5308416\n"
        "s_add_u32 m0, s79, 12288\n"
        "s_mov_b32 s65, 589824\n"
        "buffer_load_dwordx4 v6, s[44:47], s65 offen lds\n"
        "buffer_load_dwordx4 v7, s[44:47], s65 offen offset:1024 lds\n"
        "buffer_load_dwordx4 v8, s[44:47], s65 offen offset:2048 lds\n"
        "s_mov_b64 s[44:45], %2\n"
        "s_and_b32 s45, s45, 0xffff\n"
        "s_mov_b32 s46, 5308416\n"
        "s_add_u32 m0, s79, 15360\n"
        "s_mov_b32 s65, 589824\n"
        "buffer_load_dwordx4 v6, s[44:47], s65 offen lds\n"
        "buffer_load_dwordx4 v7, s[44:47], s65 offen offset:1024 lds\n"
        "buffer_load_dwordx4 v8, s[44:47], s65 offen offset:2048 lds\n"
        "s_mov_b64 s[44:45], %1\n"
        "s_and_b32 s45, s45, 0xffff\n"
        "s_mov_b32 s46, 5308416\n"
        "s_add_u32 m0, s79, 18432\n"
        "s_mov_b32 s65, 884736\n"
        "buffer_load_dwordx4 v6, s[44:47], s65 offen lds\n"
        "buffer_load_dwordx4 v7, s[44:47], s65 offen offset:1024 lds\n"
        "buffer_load_dwordx4 v8, s[44:47], s65 offen offset:2048 lds\n"
        "s_mov_b64 s[44:45], %2\n"
        "s_and_b32 s45, s45, 0xffff\n"
        "s_mov_b32 s46, 5308416\n"
        "s_add_u32 m0, s79, 21504\n"
        "s_mov_b32 s65, 884736\n"
        "buffer_load_dwordx4 v6, s[44:47], s65 offen lds\n"
        "buffer_load_dwordx4 v7, s[44:47], s65 offen offset:1024 lds\n"
        "buffer_load_dwordx4 v8, s[44:47], s65 offen offset:2048 lds\n"
        "s_mov_b64 s[44:45], %1\n"
        "s_and_b32 s45, s45, 0xffff\n"
        "s_mov_b32 s46, 5308416\n"
        "s_add_u32 m0, s79, 24576\n"
        "s_mov_b32 s65, 1179648\n"
        "buffer_load_dwordx4 v6, s[44:47], s65 offen lds\n"
        "buffer_load_dwordx4 v7, s[44:47], s65 offen offset:1024 lds\n"
        "buffer_load_dwordx4 v8, s[44:47], s65 offen offset:2048 lds\n"
        "s_mov_b64 s[44:45], %2\n"
        "s_and_b32 s45, s45, 0xffff\n"
        "s_mov_b32 s46, 5308416\n"
        "s_add_u32 m0, s79, 27648\n"
        "s_mov_b32 s65, 1179648\n"
        "buffer_load_dwordx4 v6, s[44:47], s65 offen lds\n"
        "buffer_load_dwordx4 v7, s[44:47], s65 offen offset:1024 lds\n"
        "buffer_load_dwordx4 v8, s[44:47], s65 offen offset:2048 lds\n"
        "s_mov_b64 s[44:45], %1\n"
        "s_and_b32 s45, s45, 0xffff\n"
        "s_mov_b32 s46, 5308416\n"
        "s_add_u32 m0, s79, 30720\n"
        "s_mov_b32 s65, 1474560\n"
        "buffer_load_dwordx4 v6, s[44:47], s65 offen lds\n"
        "buffer_load_dwordx4 v7, s[44:47], s65 offen offset:1024 lds\n"
        "buffer_load_dwordx4 v8, s[44:47], s65 offen offset:2048 lds\n"
        "s_mov_b64 s[44:45], %2\n"
        "s_and_b32 s45, s45, 0xffff\n"
        "s_mov_b32 s46, 5308416\n"
        "s_add_u32 m0, s79, 33792\n"
        "s_mov_b32 s65, 1474560\n"
        "buffer_load_dwordx4 v6, s[44:47], s65 offen lds\n"
        "buffer_load_dwordx4 v7, s[44:47], s65 offen offset:1024 lds\n"
        "buffer_load_dwordx4 v8, s[44:47], s65 offen offset:2048 lds\n"
        "s_waitcnt vmcnt(30)\n"
        "ds_read_b64_tr_b16 a[0:1], v9 offset:0\n"
        "ds_read_b64_tr_b16 a[2:3], v9 offset:384\n"
        "ds_read_b64_tr_b16 a[4:5], v9 offset:1536\n"
        "ds_read_b64_tr_b16 a[6:7], v9 offset:1920\n"
        "ds_read_b64_tr_b16 a[144:145], v9 offset:32\n"
        "ds_read_b64_tr_b16 a[146:147], v9 offset:416\n"
        "ds_read_b64_tr_b16 a[148:149], v9 offset:1568\n"
        "ds_read_b64_tr_b16 a[150:151], v9 offset:1952\n"
        "ds_read_b64_tr_b16 v[112:113], v9 offset:64\n"
        "ds_read_b64_tr_b16 v[114:115], v9 offset:448\n"
        "ds_read_b64_tr_b16 v[116:117], v9 offset:1600\n"
        "ds_read_b64_tr_b16 v[118:119], v9 offset:1984\n"
        "s_waitcnt vmcnt(24)\n"
        "ds_read_b64_tr_b16 a[8:9], v9 offset:6144\n"
        "ds_read_b64_tr_b16 a[10:11], v9 offset:6528\n"
        "ds_read_b64_tr_b16 a[12:13], v9 offset:7680\n"
        "ds_read_b64_tr_b16 a[14:15], v9 offset:8064\n"
        "ds_read_b64_tr_b16 a[152:153], v9 offset:6176\n"
        "ds_read_b64_tr_b16 a[154:155], v9 offset:6560\n"
        "ds_read_b64_tr_b16 a[156:157], v9 offset:7712\n"
        "ds_read_b64_tr_b16 a[158:159], v9 offset:8096\n"
        "ds_read_b64_tr_b16 v[120:121], v9 offset:6208\n"
        "ds_read_b64_tr_b16 v[122:123], v9 offset:6592\n"
        "ds_read_b64_tr_b16 v[124:125], v9 offset:7744\n"
        "ds_read_b64_tr_b16 v[126:127], v9 offset:8128\n"
        "s_waitcnt lgkmcnt(12)\n"
        "s_mov_b64 s[44:45], %1\n"
        "s_and_b32 s45, s45, 0xffff\n"
        "s_mov_b32 s46, 5308416\n"
        "s_add_u32 m0, s79, 0\n"
        "s_mov_b32 s65, 1769472\n"
        "buffer_load_dwordx4 v6, s[44:47], s65 offen lds\n"
        "buffer_load_dwordx4 v7, s[44:47], s65 offen offset:1024 lds\n"
        "buffer_load_dwordx4 v8, s[44:47], s65 offen offset:2048 lds\n"
        "s_mov_b64 s[44:45], %2\n"
        "s_and_b32 s45, s45, 0xffff\n"
        "s_mov_b32 s46, 5308416\n"
        "s_add_u32 m0, s79, 3072\n"
        "s_mov_b32 s65, 1769472\n"
        "buffer_load_dwordx4 v6, s[44:47], s65 offen lds\n"
        "buffer_load_dwordx4 v7, s[44:47], s65 offen offset:1024 lds\n"
        "buffer_load_dwordx4 v8, s[44:47], s65 offen offset:2048 lds\n"
        "s_waitcnt vmcnt(24)\n"
        "ds_read_b64_tr_b16 a[16:17], v9 offset:12288\n"
        "ds_read_b64_tr_b16 a[18:19], v9 offset:12672\n"
        "ds_read_b64_tr_b16 a[20:21], v9 offset:13824\n"
        "ds_read_b64_tr_b16 a[22:23], v9 offset:14208\n"
        "ds_read_b64_tr_b16 a[160:161], v9 offset:12320\n"
        "ds_read_b64_tr_b16 a[162:163], v9 offset:12704\n"
        "ds_read_b64_tr_b16 a[164:165], v9 offset:13856\n"
        "ds_read_b64_tr_b16 a[166:167], v9 offset:14240\n"
        "ds_read_b64_tr_b16 v[128:129], v9 offset:12352\n"
        "ds_read_b64_tr_b16 v[130:131], v9 offset:12736\n"
        "ds_read_b64_tr_b16 v[132:133], v9 offset:13888\n"
        "ds_read_b64_tr_b16 v[134:135], v9 offset:14272\n"
        "s_waitcnt lgkmcnt(12)\n"
        "s_mov_b64 s[44:45], %1\n"
        "s_and_b32 s45, s45, 0xffff\n"
        "s_mov_b32 s46, 5308416\n"
        "s_add_u32 m0, s79, 6144\n"
        "s_mov_b32 s65, 2064384\n"
        "buffer_load_dwordx4 v6, s[44:47], s65 offen lds\n"
        "buffer_load_dwordx4 v7, s[44:47], s65 offen offset:1024 lds\n"
        "buffer_load_dwordx4 v8, s[44:47], s65 offen offset:2048 lds\n"
        "s_mov_b64 s[44:45], %2\n"
        "s_and_b32 s45, s45, 0xffff\n"
        "s_mov_b32 s46, 5308416\n"
        "s_add_u32 m0, s79, 9216\n"
        "s_mov_b32 s65, 2064384\n"
        "buffer_load_dwordx4 v6, s[44:47], s65 offen lds\n"
        "buffer_load_dwordx4 v7, s[44:47], s65 offen offset:1024 lds\n"
        "buffer_load_dwordx4 v8, s[44:47], s65 offen offset:2048 lds\n"
        "s_waitcnt vmcnt(24)\n"
        "ds_read_b64_tr_b16 a[24:25], v9 offset:18432\n"
        "ds_read_b64_tr_b16 a[26:27], v9 offset:18816\n"
        "ds_read_b64_tr_b16 a[28:29], v9 offset:19968\n"
        "ds_read_b64_tr_b16 a[30:31], v9 offset:20352\n"
        "ds_read_b64_tr_b16 a[168:169], v9 offset:18464\n"
        "ds_read_b64_tr_b16 a[170:171], v9 offset:18848\n"
        "ds_read_b64_tr_b16 a[172:173], v9 offset:20000\n"
        "ds_read_b64_tr_b16 a[174:175], v9 offset:20384\n"
        "ds_read_b64_tr_b16 v[136:137], v9 offset:18496\n"
        "ds_read_b64_tr_b16 v[138:139], v9 offset:18880\n"
        "ds_read_b64_tr_b16 v[140:141], v9 offset:20032\n"
        "ds_read_b64_tr_b16 v[142:143], v9 offset:20416\n"
        "s_waitcnt lgkmcnt(12)\n"
        "s_mov_b64 s[44:45], %1\n"
        "s_and_b32 s45, s45, 0xffff\n"
        "s_mov_b32 s46, 5308416\n"
        "s_add_u32 m0, s79, 12288\n"
        "s_mov_b32 s65, 2359296\n"
        "buffer_load_dwordx4 v6, s[44:47], s65 offen lds\n"
        "buffer_load_dwordx4 v7, s[44:47], s65 offen offset:1024 lds\n"
        "buffer_load_dwordx4 v8, s[44:47], s65 offen offset:2048 lds\n"
        "s_mov_b64 s[44:45], %2\n"
        "s_and_b32 s45, s45, 0xffff\n"
        "s_mov_b32 s46, 5308416\n"
        "s_add_u32 m0, s79, 15360\n"
        "s_mov_b32 s65, 2359296\n"
        "buffer_load_dwordx4 v6, s[44:47], s65 offen lds\n"
        "buffer_load_dwordx4 v7, s[44:47], s65 offen offset:1024 lds\n"
        "buffer_load_dwordx4 v8, s[44:47], s65 offen offset:2048 lds\n"
        "s_waitcnt vmcnt(24)\n"
        "ds_read_b64_tr_b16 a[32:33], v9 offset:24576\n"
        "ds_read_b64_tr_b16 a[34:35], v9 offset:24960\n"
        "ds_read_b64_tr_b16 a[36:37], v9 offset:26112\n"
        "ds_read_b64_tr_b16 a[38:39], v9 offset:26496\n"
        "ds_read_b64_tr_b16 a[176:177], v9 offset:24608\n"
        "ds_read_b64_tr_b16 a[178:179], v9 offset:24992\n"
        "ds_read_b64_tr_b16 a[180:181], v9 offset:26144\n"
        "ds_read_b64_tr_b16 a[182:183], v9 offset:26528\n"
        "ds_read_b64_tr_b16 v[144:145], v9 offset:24640\n"
        "ds_read_b64_tr_b16 v[146:147], v9 offset:25024\n"
        "ds_read_b64_tr_b16 v[148:149], v9 offset:26176\n"
        "ds_read_b64_tr_b16 v[150:151], v9 offset:26560\n"
        "s_waitcnt lgkmcnt(12)\n"
        "s_mov_b64 s[44:45], %1\n"
        "s_and_b32 s45, s45, 0xffff\n"
        "s_mov_b32 s46, 5308416\n"
        "s_add_u32 m0, s79, 18432\n"
        "s_mov_b32 s65, 2654208\n"
        "buffer_load_dwordx4 v6, s[44:47], s65 offen lds\n"
        "buffer_load_dwordx4 v7, s[44:47], s65 offen offset:1024 lds\n"
        "buffer_load_dwordx4 v8, s[44:47], s65 offen offset:2048 lds\n"
        "s_mov_b64 s[44:45], %2\n"
        "s_and_b32 s45, s45, 0xffff\n"
        "s_mov_b32 s46, 5308416\n"
        "s_add_u32 m0, s79, 21504\n"
        "s_mov_b32 s65, 2654208\n"
        "buffer_load_dwordx4 v6, s[44:47], s65 offen lds\n"
        "buffer_load_dwordx4 v7, s[44:47], s65 offen offset:1024 lds\n"
        "buffer_load_dwordx4 v8, s[44:47], s65 offen offset:2048 lds\n"
        "s_waitcnt vmcnt(24)\n"
        "ds_read_b64_tr_b16 a[40:41], v9 offset:30720\n"
        "ds_read_b64_tr_b16 a[42:43], v9 offset:31104\n"
        "ds_read_b64_tr_b16 a[44:45], v9 offset:32256\n"
        "ds_read_b64_tr_b16 a[46:47], v9 offset:32640\n"
        "ds_read_b64_tr_b16 a[184:185], v9 offset:30752\n"
        "ds_read_b64_tr_b16 a[186:187], v9 offset:31136\n"
        "ds_read_b64_tr_b16 a[188:189], v9 offset:32288\n"
        "ds_read_b64_tr_b16 a[190:191], v9 offset:32672\n"
        "ds_read_b64_tr_b16 v[152:153], v9 offset:30784\n"
        "ds_read_b64_tr_b16 v[154:155], v9 offset:31168\n"
        "ds_read_b64_tr_b16 v[156:157], v9 offset:32320\n"
        "ds_read_b64_tr_b16 v[158:159], v9 offset:32704\n"
        "s_waitcnt lgkmcnt(12)\n"
        "s_mov_b64 s[44:45], %1\n"
        "s_and_b32 s45, s45, 0xffff\n"
        "s_mov_b32 s46, 5308416\n"
        "s_add_u32 m0, s79, 24576\n"
        "s_mov_b32 s65, 2949120\n"
        "buffer_load_dwordx4 v6, s[44:47], s65 offen lds\n"
        "buffer_load_dwordx4 v7, s[44:47], s65 offen offset:1024 lds\n"
        "buffer_load_dwordx4 v8, s[44:47], s65 offen offset:2048 lds\n"
        "s_mov_b64 s[44:45], %2\n"
        "s_and_b32 s45, s45, 0xffff\n"
        "s_mov_b32 s46, 5308416\n"
        "s_add_u32 m0, s79, 27648\n"
        "s_mov_b32 s65, 2949120\n"
        "buffer_load_dwordx4 v6, s[44:47], s65 offen lds\n"
        "buffer_load_dwordx4 v7, s[44:47], s65 offen offset:1024 lds\n"
        "buffer_load_dwordx4 v8, s[44:47], s65 offen offset:2048 lds\n"
        "s_waitcnt vmcnt(24)\n"
        "ds_read_b64_tr_b16 a[48:49], v9 offset:0\n"
        "ds_read_b64_tr_b16 a[50:51], v9 offset:384\n"
        "ds_read_b64_tr_b16 a[52:53], v9 offset:1536\n"
        "ds_read_b64_tr_b16 a[54:55], v9 offset:1920\n"
        "ds_read_b64_tr_b16 a[192:193], v9 offset:32\n"
        "ds_read_b64_tr_b16 a[194:195], v9 offset:416\n"
        "ds_read_b64_tr_b16 a[196:197], v9 offset:1568\n"
        "ds_read_b64_tr_b16 a[198:199], v9 offset:1952\n"
        "ds_read_b64_tr_b16 v[160:161], v9 offset:64\n"
        "ds_read_b64_tr_b16 v[162:163], v9 offset:448\n"
        "ds_read_b64_tr_b16 v[164:165], v9 offset:1600\n"
        "ds_read_b64_tr_b16 v[166:167], v9 offset:1984\n"
        "s_waitcnt lgkmcnt(12)\n"
        "s_mov_b64 s[44:45], %1\n"
        "s_and_b32 s45, s45, 0xffff\n"
        "s_mov_b32 s46, 5308416\n"
        "s_add_u32 m0, s79, 30720\n"
        "s_mov_b32 s65, 3244032\n"
        "buffer_load_dwordx4 v6, s[44:47], s65 offen lds\n"
        "buffer_load_dwordx4 v7, s[44:47], s65 offen offset:1024 lds\n"
        "buffer_load_dwordx4 v8, s[44:47], s65 offen offset:2048 lds\n"
        "s_mov_b64 s[44:45], %2\n"
        "s_and_b32 s45, s45, 0xffff\n"
        "s_mov_b32 s46, 5308416\n"
        "s_add_u32 m0, s79, 33792\n"
        "s_mov_b32 s65, 3244032\n"
        "buffer_load_dwordx4 v6, s[44:47], s65 offen lds\n"
        "buffer_load_dwordx4 v7, s[44:47], s65 offen offset:1024 lds\n"
        "buffer_load_dwordx4 v8, s[44:47], s65 offen offset:2048 lds\n"
        "s_waitcnt vmcnt(24)\n"
        "ds_read_b64_tr_b16 a[56:57], v9 offset:6144\n"
        "ds_read_b64_tr_b16 a[58:59], v9 offset:6528\n"
        "ds_read_b64_tr_b16 a[60:61], v9 offset:7680\n"
        "ds_read_b64_tr_b16 a[62:63], v9 offset:8064\n"
        "ds_read_b64_tr_b16 a[200:201], v9 offset:6176\n"
        "ds_read_b64_tr_b16 a[202:203], v9 offset:6560\n"
        "ds_read_b64_tr_b16 a[204:205], v9 offset:7712\n"
        "ds_read_b64_tr_b16 a[206:207], v9 offset:8096\n"
        "ds_read_b64_tr_b16 v[168:169], v9 offset:6208\n"
        "ds_read_b64_tr_b16 v[170:171], v9 offset:6592\n"
        "ds_read_b64_tr_b16 v[172:173], v9 offset:7744\n"
        "ds_read_b64_tr_b16 v[174:175], v9 offset:8128\n"
        "s_waitcnt lgkmcnt(12)\n"
        "s_mov_b64 s[44:45], %1\n"
        "s_and_b32 s45, s45, 0xffff\n"
        "s_mov_b32 s46, 5308416\n"
        "s_add_u32 m0, s79, 0\n"
        "s_mov_b32 s65, 3538944\n"
        "buffer_load_dwordx4 v6, s[44:47], s65 offen lds\n"
        "buffer_load_dwordx4 v7, s[44:47], s65 offen offset:1024 lds\n"
        "buffer_load_dwordx4 v8, s[44:47], s65 offen offset:2048 lds\n"
        "s_mov_b64 s[44:45], %2\n"
        "s_and_b32 s45, s45, 0xffff\n"
        "s_mov_b32 s46, 5308416\n"
        "s_add_u32 m0, s79, 3072\n"
        "s_mov_b32 s65, 3538944\n"
        "buffer_load_dwordx4 v6, s[44:47], s65 offen lds\n"
        "buffer_load_dwordx4 v7, s[44:47], s65 offen offset:1024 lds\n"
        "buffer_load_dwordx4 v8, s[44:47], s65 offen offset:2048 lds\n"
        "s_waitcnt vmcnt(24)\n"
        "ds_read_b64_tr_b16 a[64:65], v9 offset:12288\n"
        "ds_read_b64_tr_b16 a[66:67], v9 offset:12672\n"
        "ds_read_b64_tr_b16 a[68:69], v9 offset:13824\n"
        "ds_read_b64_tr_b16 a[70:71], v9 offset:14208\n"
        "ds_read_b64_tr_b16 a[208:209], v9 offset:12320\n"
        "ds_read_b64_tr_b16 a[210:211], v9 offset:12704\n"
        "ds_read_b64_tr_b16 a[212:213], v9 offset:13856\n"
        "ds_read_b64_tr_b16 a[214:215], v9 offset:14240\n"
        "ds_read_b64_tr_b16 v[176:177], v9 offset:12352\n"
        "ds_read_b64_tr_b16 v[178:179], v9 offset:12736\n"
        "ds_read_b64_tr_b16 v[180:181], v9 offset:13888\n"
        "ds_read_b64_tr_b16 v[182:183], v9 offset:14272\n"
        "s_waitcnt lgkmcnt(12)\n"
        "s_mov_b64 s[44:45], %1\n"
        "s_and_b32 s45, s45, 0xffff\n"
        "s_mov_b32 s46, 5308416\n"
        "s_add_u32 m0, s79, 6144\n"
        "s_mov_b32 s65, 3833856\n"
        "buffer_load_dwordx4 v6, s[44:47], s65 offen lds\n"
        "buffer_load_dwordx4 v7, s[44:47], s65 offen offset:1024 lds\n"
        "buffer_load_dwordx4 v8, s[44:47], s65 offen offset:2048 lds\n"
        "s_mov_b64 s[44:45], %2\n"
        "s_and_b32 s45, s45, 0xffff\n"
        "s_mov_b32 s46, 5308416\n"
        "s_add_u32 m0, s79, 9216\n"
        "s_mov_b32 s65, 3833856\n"
        "buffer_load_dwordx4 v6, s[44:47], s65 offen lds\n"
        "buffer_load_dwordx4 v7, s[44:47], s65 offen offset:1024 lds\n"
        "buffer_load_dwordx4 v8, s[44:47], s65 offen offset:2048 lds\n"
        "s_waitcnt vmcnt(24)\n"
        "ds_read_b64_tr_b16 a[72:73], v9 offset:18432\n"
        "ds_read_b64_tr_b16 a[74:75], v9 offset:18816\n"
        "ds_read_b64_tr_b16 a[76:77], v9 offset:19968\n"
        "ds_read_b64_tr_b16 a[78:79], v9 offset:20352\n"
        "ds_read_b64_tr_b16 a[216:217], v9 offset:18464\n"
        "ds_read_b64_tr_b16 a[218:219], v9 offset:18848\n"
        "ds_read_b64_tr_b16 a[220:221], v9 offset:20000\n"
        "ds_read_b64_tr_b16 a[222:223], v9 offset:20384\n"
        "ds_read_b64_tr_b16 v[184:185], v9 offset:18496\n"
        "ds_read_b64_tr_b16 v[186:187], v9 offset:18880\n"
        "ds_read_b64_tr_b16 v[188:189], v9 offset:20032\n"
        "ds_read_b64_tr_b16 v[190:191], v9 offset:20416\n"
        "s_waitcnt lgkmcnt(12)\n"
        "s_mov_b64 s[44:45], %1\n"
        "s_and_b32 s45, s45, 0xffff\n"
        "s_mov_b32 s46, 5308416\n"
        "s_add_u32 m0, s79, 12288\n"
        "s_mov_b32 s65, 4128768\n"
        "buffer_load_dwordx4 v6, s[44:47], s65 offen lds\n"
        "buffer_load_dwordx4 v7, s[44:47], s65 offen offset:1024 lds\n"
        "buffer_load_dwordx4 v8, s[44:47], s65 offen offset:2048 lds\n"
        "s_mov_b64 s[44:45], %2\n"
        "s_and_b32 s45, s45, 0xffff\n"
        "s_mov_b32 s46, 5308416\n"
        "s_add_u32 m0, s79, 15360\n"
        "s_mov_b32 s65, 4128768\n"
        "buffer_load_dwordx4 v6, s[44:47], s65 offen lds\n"
        "buffer_load_dwordx4 v7, s[44:47], s65 offen offset:1024 lds\n"
        "buffer_load_dwordx4 v8, s[44:47], s65 offen offset:2048 lds\n"
        "s_waitcnt vmcnt(24)\n"
        "ds_read_b64_tr_b16 a[80:81], v9 offset:24576\n"
        "ds_read_b64_tr_b16 a[82:83], v9 offset:24960\n"
        "ds_read_b64_tr_b16 a[84:85], v9 offset:26112\n"
        "ds_read_b64_tr_b16 a[86:87], v9 offset:26496\n"
        "ds_read_b64_tr_b16 a[224:225], v9 offset:24608\n"
        "ds_read_b64_tr_b16 a[226:227], v9 offset:24992\n"
        "ds_read_b64_tr_b16 a[228:229], v9 offset:26144\n"
        "ds_read_b64_tr_b16 a[230:231], v9 offset:26528\n"
        "ds_read_b64_tr_b16 v[192:193], v9 offset:24640\n"
        "ds_read_b64_tr_b16 v[194:195], v9 offset:25024\n"
        "ds_read_b64_tr_b16 v[196:197], v9 offset:26176\n"
        "ds_read_b64_tr_b16 v[198:199], v9 offset:26560\n"
        "s_waitcnt lgkmcnt(12)\n"
        "s_mov_b64 s[44:45], %1\n"
        "s_and_b32 s45, s45, 0xffff\n"
        "s_mov_b32 s46, 5308416\n"
        "s_add_u32 m0, s79, 18432\n"
        "s_mov_b32 s65, 4423680\n"
        "buffer_load_dwordx4 v6, s[44:47], s65 offen lds\n"
        "buffer_load_dwordx4 v7, s[44:47], s65 offen offset:1024 lds\n"
        "buffer_load_dwordx4 v8, s[44:47], s65 offen offset:2048 lds\n"
        "s_mov_b64 s[44:45], %2\n"
        "s_and_b32 s45, s45, 0xffff\n"
        "s_mov_b32 s46, 5308416\n"
        "s_add_u32 m0, s79, 21504\n"
        "s_mov_b32 s65, 4423680\n"
        "buffer_load_dwordx4 v6, s[44:47], s65 offen lds\n"
        "buffer_load_dwordx4 v7, s[44:47], s65 offen offset:1024 lds\n"
        "buffer_load_dwordx4 v8, s[44:47], s65 offen offset:2048 lds\n"
        "s_waitcnt vmcnt(24)\n"
        "ds_read_b64_tr_b16 a[88:89], v9 offset:30720\n"
        "ds_read_b64_tr_b16 a[90:91], v9 offset:31104\n"
        "ds_read_b64_tr_b16 a[92:93], v9 offset:32256\n"
        "ds_read_b64_tr_b16 a[94:95], v9 offset:32640\n"
        "ds_read_b64_tr_b16 a[232:233], v9 offset:30752\n"
        "ds_read_b64_tr_b16 a[234:235], v9 offset:31136\n"
        "ds_read_b64_tr_b16 a[236:237], v9 offset:32288\n"
        "ds_read_b64_tr_b16 a[238:239], v9 offset:32672\n"
        "ds_read_b64_tr_b16 v[200:201], v9 offset:30784\n"
        "ds_read_b64_tr_b16 v[202:203], v9 offset:31168\n"
        "ds_read_b64_tr_b16 v[204:205], v9 offset:32320\n"
        "ds_read_b64_tr_b16 v[206:207], v9 offset:32704\n"
        "s_waitcnt lgkmcnt(12)\n"
        "s_mov_b64 s[44:45], %1\n"
        "s_and_b32 s45, s45, 0xffff\n"
        "s_mov_b32 s46, 5308416\n"
        "s_add_u32 m0, s79, 24576\n"
        "s_mov_b32 s65, 4718592\n"
        "buffer_load_dwordx4 v6, s[44:47], s65 offen lds\n"
        "buffer_load_dwordx4 v7, s[44:47], s65 offen offset:1024 lds\n"
        "buffer_load_dwordx4 v8, s[44:47], s65 offen offset:2048 lds\n"
        "s_mov_b64 s[44:45], %2\n"
        "s_and_b32 s45, s45, 0xffff\n"
        "s_mov_b32 s46, 5308416\n"
        "s_add_u32 m0, s79, 27648\n"
        "s_mov_b32 s65, 4718592\n"
        "buffer_load_dwordx4 v6, s[44:47], s65 offen lds\n"
        "buffer_load_dwordx4 v7, s[44:47], s65 offen offset:1024 lds\n"
        "buffer_load_dwordx4 v8, s[44:47], s65 offen offset:2048 lds\n"
        "s_waitcnt vmcnt(24)\n"
        "ds_read_b64_tr_b16 a[96:97], v9 offset:0\n"
        "ds_read_b64_tr_b16 a[98:99], v9 offset:384\n"
        "ds_read_b64_tr_b16 a[100:101], v9 offset:1536\n"
        "ds_read_b64_tr_b16 a[102:103], v9 offset:1920\n"
        "ds_read_b64_tr_b16 a[240:241], v9 offset:32\n"
        "ds_read_b64_tr_b16 a[242:243], v9 offset:416\n"
        "ds_read_b64_tr_b16 a[244:245], v9 offset:1568\n"
        "ds_read_b64_tr_b16 a[246:247], v9 offset:1952\n"
        "ds_read_b64_tr_b16 v[208:209], v9 offset:64\n"
        "ds_read_b64_tr_b16 v[210:211], v9 offset:448\n"
        "ds_read_b64_tr_b16 v[212:213], v9 offset:1600\n"
        "ds_read_b64_tr_b16 v[214:215], v9 offset:1984\n"
        "s_waitcnt lgkmcnt(12)\n"
        "s_mov_b64 s[44:45], %1\n"
        "s_and_b32 s45, s45, 0xffff\n"
        "s_mov_b32 s46, 5308416\n"
        "s_add_u32 m0, s79, 30720\n"
        "s_mov_b32 s65, 5013504\n"
        "buffer_load_dwordx4 v6, s[44:47], s65 offen lds\n"
        "buffer_load_dwordx4 v7, s[44:47], s65 offen offset:1024 lds\n"
        "buffer_load_dwordx4 v8, s[44:47], s65 offen offset:2048 lds\n"
        "s_mov_b64 s[44:45], %2\n"
        "s_and_b32 s45, s45, 0xffff\n"
        "s_mov_b32 s46, 5308416\n"
        "s_add_u32 m0, s79, 33792\n"
        "s_mov_b32 s65, 5013504\n"
        "buffer_load_dwordx4 v6, s[44:47], s65 offen lds\n"
        "buffer_load_dwordx4 v7, s[44:47], s65 offen offset:1024 lds\n"
        "buffer_load_dwordx4 v8, s[44:47], s65 offen offset:2048 lds\n"
        "s_waitcnt vmcnt(24)\n"
        "ds_read_b64_tr_b16 a[104:105], v9 offset:6144\n"
        "ds_read_b64_tr_b16 a[106:107], v9 offset:6528\n"
        "ds_read_b64_tr_b16 a[108:109], v9 offset:7680\n"
        "ds_read_b64_tr_b16 a[110:111], v9 offset:8064\n"
        "ds_read_b64_tr_b16 a[248:249], v9 offset:6176\n"
        "ds_read_b64_tr_b16 a[250:251], v9 offset:6560\n"
        "ds_read_b64_tr_b16 a[252:253], v9 offset:7712\n"
        "ds_read_b64_tr_b16 a[254:255], v9 offset:8096\n"
        "ds_read_b64_tr_b16 v[216:217], v9 offset:6208\n"
        "ds_read_b64_tr_b16 v[218:219], v9 offset:6592\n"
        "ds_read_b64_tr_b16 v[220:221], v9 offset:7744\n"
        "ds_read_b64_tr_b16 v[222:223], v9 offset:8128\n"
        "s_waitcnt vmcnt(18)\n"
        "ds_read_b64_tr_b16 a[112:113], v9 offset:12288\n"
        "ds_read_b64_tr_b16 a[114:115], v9 offset:12672\n"
        "ds_read_b64_tr_b16 a[116:117], v9 offset:13824\n"
        "ds_read_b64_tr_b16 a[118:119], v9 offset:14208\n"
        "ds_read_b64_tr_b16 v[80:81], v9 offset:12320\n"
        "ds_read_b64_tr_b16 v[82:83], v9 offset:12704\n"
        "ds_read_b64_tr_b16 v[84:85], v9 offset:13856\n"
        "ds_read_b64_tr_b16 v[86:87], v9 offset:14240\n"
        "ds_read_b64_tr_b16 v[224:225], v9 offset:12352\n"
        "ds_read_b64_tr_b16 v[226:227], v9 offset:12736\n"
        "ds_read_b64_tr_b16 v[228:229], v9 offset:13888\n"
        "ds_read_b64_tr_b16 v[230:231], v9 offset:14272\n"
        "s_waitcnt vmcnt(12)\n"
        "ds_read_b64_tr_b16 a[120:121], v9 offset:18432\n"
        "ds_read_b64_tr_b16 a[122:123], v9 offset:18816\n"
        "ds_read_b64_tr_b16 a[124:125], v9 offset:19968\n"
        "ds_read_b64_tr_b16 a[126:127], v9 offset:20352\n"
        "ds_read_b64_tr_b16 v[88:89], v9 offset:18464\n"
        "ds_read_b64_tr_b16 v[90:91], v9 offset:18848\n"
        "ds_read_b64_tr_b16 v[92:93], v9 offset:20000\n"
        "ds_read_b64_tr_b16 v[94:95], v9 offset:20384\n"
        "ds_read_b64_tr_b16 v[232:233], v9 offset:18496\n"
        "ds_read_b64_tr_b16 v[234:235], v9 offset:18880\n"
        "ds_read_b64_tr_b16 v[236:237], v9 offset:20032\n"
        "ds_read_b64_tr_b16 v[238:239], v9 offset:20416\n"
        "s_waitcnt vmcnt(6)\n"
        "ds_read_b64_tr_b16 a[128:129], v9 offset:24576\n"
        "ds_read_b64_tr_b16 a[130:131], v9 offset:24960\n"
        "ds_read_b64_tr_b16 a[132:133], v9 offset:26112\n"
        "ds_read_b64_tr_b16 a[134:135], v9 offset:26496\n"
        "ds_read_b64_tr_b16 v[96:97], v9 offset:24608\n"
        "ds_read_b64_tr_b16 v[98:99], v9 offset:24992\n"
        "ds_read_b64_tr_b16 v[100:101], v9 offset:26144\n"
        "ds_read_b64_tr_b16 v[102:103], v9 offset:26528\n"
        "ds_read_b64_tr_b16 v[240:241], v9 offset:24640\n"
        "ds_read_b64_tr_b16 v[242:243], v9 offset:25024\n"
        "ds_read_b64_tr_b16 v[244:245], v9 offset:26176\n"
        "ds_read_b64_tr_b16 v[246:247], v9 offset:26560\n"
        "s_waitcnt vmcnt(0)\n"
        "ds_read_b64_tr_b16 a[136:137], v9 offset:30720\n"
        "ds_read_b64_tr_b16 a[138:139], v9 offset:31104\n"
        "ds_read_b64_tr_b16 a[140:141], v9 offset:32256\n"
        "ds_read_b64_tr_b16 a[142:143], v9 offset:32640\n"
        "ds_read_b64_tr_b16 v[104:105], v9 offset:30752\n"
        "ds_read_b64_tr_b16 v[106:107], v9 offset:31136\n"
        "ds_read_b64_tr_b16 v[108:109], v9 offset:32288\n"
        "ds_read_b64_tr_b16 v[110:111], v9 offset:32672\n"
        "ds_read_b64_tr_b16 v[248:249], v9 offset:30784\n"
        "ds_read_b64_tr_b16 v[250:251], v9 offset:31168\n"
        "ds_read_b64_tr_b16 v[252:253], v9 offset:32320\n"
        "ds_read_b64_tr_b16 v[254:255], v9 offset:32704\n"
        "s_waitcnt lgkmcnt(0)\n"
        "v_lshrrev_b32 v12, 4, v1\n"
        "s_lshl_b32 s66, s57, 7\n"
        "s_mov_b32 s64, 0\n"
        ".Lf1_pair_top_%=:\n"
        "s_mov_b64 exec, 1\n"
        "buffer_load_dword v13, off, s[40:43], s66 sc1\n"
        "s_mov_b64 exec, -1\n"
        "s_waitcnt vmcnt(0)\n"
        "v_cmp_gt_i32 vcc, 0, v13\n"
        "s_nop 0\n"
        "s_and_b64 vcc, vcc, 1\n"
        "s_cbranch_vccz .Lf1_pair_ok_%=\n"
        "s_add_u32 s64, s64, 1\n"
        "s_and_b32 s65, s64, 255\n"
        "s_cbranch_scc1 .Lf1_pair_go_%=\n"
        "s_mov_b32 s65, 40960\n"
        "s_cmp_lt_u32 s64, 1048576\n"
        "s_cbranch_scc1 .Lf1_pair_chk_%=\n"
        "v_mov_b32 v13, 1\n"
        "buffer_store_dword v13, off, s[40:43], s65 sc1\n"
        "s_waitcnt vmcnt(0)\n"
        ".Lf1_pair_chk_%=:\n"
        "buffer_load_dword v13, off, s[40:43], s65 sc1\n"
        "s_waitcnt vmcnt(0)\n"
        "v_readfirstlane_b32 s65, v13\n"
        "s_nop 0\n"
        "s_cmp_lg_u32 s65, 1\n"
        "s_cbranch_scc1 .Lf1_pair_go_%=\n"
        "s_endpgm\n"
        ".Lf1_pair_go_%=:\n"
        "s_sleep 2\n"
        "s_branch .Lf1_pair_top_%=\n"
        ".Lf1_pair_ok_%=:\n"
        "v_readfirstlane_b32 s50, v13\n"
        "s_nop 0\n"
        "s_xor_b32 s50, s50, s56\n"
        "v_lshlrev_b32 v14, 7, v12\n"
        "s_sub_u32 s66, 0, s50\n"
        "s_mov_b32 s52, 24576\n"
        "s_mov_b32 s64, 0\n"
        ".Lf1_pg0_top_%=:\n"
        "buffer_load_dword v13, v14, s[40:43], s52 offen sc1\n"
        "s_waitcnt vmcnt(0)\n"
        "v_cmp_gt_i32 vcc, 0, v13\n"
        "s_cbranch_vccz .Lf1_pg0_ok_%=\n"
        "s_add_u32 s64, s64, 1\n"
        "s_and_b32 s65, s64, 255\n"
        "s_cbranch_scc1 .Lf1_pg0_go_%=\n"
        "s_mov_b32 s65, 40960\n"
        "s_cmp_lt_u32 s64, 1048576\n"
        "s_cbranch_scc1 .Lf1_pg0_chk_%=\n"
        "v_mov_b32 v13, 1\n"
        "buffer_store_dword v13, off, s[40:43], s65 sc1\n"
        "s_waitcnt vmcnt(0)\n"
        ".Lf1_pg0_chk_%=:\n"
        "buffer_load_dword v13, off, s[40:43], s65 sc1\n"
        "s_waitcnt vmcnt(0)\n"
        "v_readfirstlane_b32 s65, v13\n"
        "s_nop 0\n"
        "s_cmp_lg_u32 s65, 1\n"
        "s_cbranch_scc1 .Lf1_pg0_go_%=\n"
        "s_endpgm\n"
        ".Lf1_pg0_go_%=:\n"
        "s_sleep 2\n"
        "s_branch .Lf1_pg0_top_%=\n"
        ".Lf1_pg0_ok_%=:\n"
        "v_cmp_eq_u32 s[52:53], 1, v13\n"
        "s_nop 1\n"
        "s_mov_b32 s54, 32768\n"
        "s_mov_b32 s64, 0\n"
        ".Lf1_pg1_top_%=:\n"
        "buffer_load_dword v13, v14, s[40:43], s54 offen sc1\n"
        "s_waitcnt vmcnt(0)\n"
        "v_cmp_gt_i32 vcc, 0, v13\n"
        "s_cbranch_vccz .Lf1_pg1_ok_%=\n"
        "s_add_u32 s64, s64, 1\n"
        "s_and_b32 s65, s64, 255\n"
        "s_cbranch_scc1 .Lf1_pg1_go_%=\n"
        "s_mov_b32 s65, 40960\n"
        "s_cmp_lt_u32 s64, 1048576\n"
        "s_cbranch_scc1 .Lf1_pg1_chk_%=\n"
        "v_mov_b32 v13, 1\n"
        "buffer_store_dword v13, off, s[40:43], s65 sc1\n"
        "s_waitcnt vmcnt(0)\n"
        ".Lf1_pg1_chk_%=:\n"
        "buffer_load_dword v13, off, s[40:43], s65 sc1\n"
        "s_waitcnt vmcnt(0)\n"
        "v_readfirstlane_b32 s65, v13\n"
        "s_nop 0\n"
        "s_cmp_lg_u32 s65, 1\n"
        "s_cbranch_scc1 .Lf1_pg1_go_%=\n"
        "s_endpgm\n"
        ".Lf1_pg1_go_%=:\n"
        "s_sleep 2\n"
        "s_branch .Lf1_pg1_top_%=\n"
        ".Lf1_pg1_ok_%=:\n"
        "v_cmp_eq_u32 s[54:55], 1, v13\n"
        "s_nop 1\n"
        "s_mov_b32 vcc_lo, 0x55555555\n"
        "s_mov_b32 vcc_hi, 0x55555555\n"
        "s_xor_b64 vcc, vcc, s[52:53]\n"
        "s_bcnt1_i32_b64 s65, vcc\n"
        "s_sub_u32 s64, s65, 23\n"
        "s_cmp_lt_u32 s64, 19\n"
        "s_cbranch_scc1 .Lf1_rawmap_%=\n"
        "s_cmp_lt_u32 s65, 32\n"
        "s_cselect_b32 s65, 0, -1\n"
        "s_xor_b32 s52, s65, 0x55555555\n"
        "s_xor_b32 s53, s65, 0x55555555\n"
        "s_xor_b32 s54, s65, 0x55555555\n"
        "s_xor_b32 s55, s65, 0xaaaaaaaa\n"
        ".Lf1_rawmap_%=:\n"
        "s_xor_b32 s52, s52, s66\n"
        "s_xor_b32 s53, s53, s66\n"
        "s_xor_b32 s54, s54, s66\n"
        "s_xor_b32 s55, s55, s66\n"
        "s_and_b32 s76, s55, 1\n"
        "s_lshl_b32 s76, s76, 13\n"
        "s_add_u32 s60, s60, s76\n"
        "s_barrier\n"
        "s_mov_b32 s68, -1\n"
        "s_mov_b32 s69, 0\n"
        "s_mov_b32 s77, 0\n"
        "s_mov_b32 s78, 0\n"
        "v_mov_b32 v6, v0\n"
        "s_mov_b32 s80, 0xbfb8aa3b\n"
        "s_branch .Lf1_wait_%=\n"
        ".Lf1_p00_%=:\n"
        "v_mfma_f32_32x32x16_bf16 v[32:47], a[0:3], v[8:11], 0\n"
        "ds_read_b128 v[8:11], v0 offset:1536\n"
        "v_mfma_f32_32x32x16_bf16 v[32:47], a[4:7], v[12:15], v[32:47]\n"
        "ds_read_b128 v[12:15], v0 offset:1792\n"
        "s_waitcnt lgkmcnt(4)\n"
        "v_mul_f32 v48, s80, v64\n"
        "v_mul_f32 v49, s80, v65\n"
        "v_mul_f32 v50, s80, v66\n"
        "v_mul_f32 v51, s80, v67\n"
        "v_mfma_f32_32x32x16_bf16 v[32:47], a[8:11], v[16:19], v[32:47]\n"
        "ds_read_b128 v[16:19], v0 offset:2048\n"
        "v_mul_f32 v52, s80, v72\n"
        "v_mul_f32 v53, s80, v73\n"
        "v_mul_f32 v54, s80, v74\n"
        "v_mul_f32 v55, s80, v75\n"
        "v_mfma_f32_32x32x16_bf16 v[32:47], a[12:15], v[20:23], v[32:47]\n"
        "ds_read_b128 v[20:23], v0 offset:2304\n"
        "s_waitcnt lgkmcnt(4)\n"
        "v_exp_f32 v48, v48\n"
        "v_mul_f32 v64, v64, v68\n"
        "v_mfma_f32_32x32x16_bf16 v[32:47], a[16:19], v[24:27], v[32:47]\n"
        "ds_read_b128 v[24:27], v0 offset:2560\n"
        "v_exp_f32 v49, v49\n"
        "v_mul_f32 v65, v65, v69\n"
        "v_exp_f32 v50, v50\n"
        "v_mfma_f32_32x32x16_bf16 v[32:47], a[20:23], v[28:31], v[32:47]\n"
        "ds_read_b128 v[28:31], v0 offset:2816\n"
        "s_waitcnt lgkmcnt(4)\n"
        "v_mul_f32 v66, v66, v70\n"
        "v_exp_f32 v51, v51\n"
        "v_mul_f32 v67, v67, v71\n"
        "v_mfma_f32_32x32x16_bf16 v[32:47], a[24:27], v[8:11], v[32:47]\n"
        "ds_read_b128 v[8:11], v0 offset:3072\n"
        "v_exp_f32 v52, v52\n"
        "v_mul_f32 v72, v72, v76\n"
        "v_exp_f32 v53, v53\n"
        "v_mfma_f32_32x32x16_bf16 v[32:47], a[28:31], v[12:15], v[32:47]\n"
        "ds_read_b128 v[12:15], v0 offset:3328\n"
        "s_waitcnt lgkmcnt(4)\n"
        "v_mul_f32 v73, v73, v77\n"
        "v_exp_f32 v54, v54\n"
        "v_mul_f32 v74, v74, v78\n"
        "v_mfma_f32_32x32x16_bf16 v[32:47], a[32:35], v[16:19], v[32:47]\n"
        "ds_read_b128 v[16:19], v0 offset:3584\n"
        "v_exp_f32 v55, v55\n"
        "v_mul_f32 v75, v75, v79\n"
        "v_add_f32 v48, 1.0, v48\n"
        "v_add_f32 v49, 1.0, v49\n"
        "v_mfma_f32_32x32x16_bf16 v[32:47], a[36:39], v[20:23], v[32:47]\n"
        "s_waitcnt vmcnt(0)\n"
        "s_barrier\n"
        "ds_read_b128 v[20:23], v0 offset:3840\n"
        "s_waitcnt lgkmcnt(4)\n"
        "v_rcp_f32 v48, v48\n"
        "v_mfma_f32_32x32x16_bf16 v[32:47], a[40:43], v[24:27], v[32:47]\n"
        "ds_read_b128 v[24:27], v0 offset:4096\n"
        "v_add_f32 v50, 1.0, v50\n"
        "v_rcp_f32 v49, v49\n"
        "v_add_f32 v51, 1.0, v51\n"
        "v_mfma_f32_32x32x16_bf16 v[32:47], a[44:47], v[28:31], v[32:47]\n"
        "ds_read_b128 v[28:31], v0 offset:4352\n"
        "s_waitcnt lgkmcnt(4)\n"
        "v_rcp_f32 v50, v50\n"
        "v_add_f32 v52, 1.0, v52\n"
        "v_mfma_f32_32x32x16_bf16 v[32:47], a[48:51], v[8:11], v[32:47]\n"
        "ds_read_b128 v[8:11], v0 offset:4608\n"
        "v_rcp_f32 v51, v51\n"
        "v_add_f32 v53, 1.0, v53\n"
        "v_rcp_f32 v52, v52\n"
        "v_mfma_f32_32x32x16_bf16 v[32:47], a[52:55], v[12:15], v[32:47]\n"
        "ds_read_b128 v[12:15], v0 offset:4864\n"
        "s_waitcnt lgkmcnt(4)\n"
        "v_add_f32 v54, 1.0, v54\n"
        "v_rcp_f32 v53, v53\n"
        "v_add_f32 v55, 1.0, v55\n"
        "v_mfma_f32_32x32x16_bf16 v[32:47], a[56:59], v[16:19], v[32:47]\n"
        "ds_read_b128 v[16:19], v0 offset:5120\n"
        "v_rcp_f32 v54, v54\n"
        "v_rcp_f32 v55, v55\n"
        "v_mul_f32 v64, v64, v48\n"
        "v_mfma_f32_32x32x16_bf16 v[32:47], a[60:63], v[20:23], v[32:47]\n"
        "ds_read_b128 v[20:23], v0 offset:5376\n"
        "s_waitcnt lgkmcnt(4)\n"
        "v_mul_f32 v65, v65, v49\n"
        "v_mul_f32 v66, v66, v50\n"
        "v_mul_f32 v67, v67, v51\n"
        "v_mul_f32 v72, v72, v52\n"
        "v_mfma_f32_32x32x16_bf16 v[32:47], a[64:67], v[24:27], v[32:47]\n"
        "ds_read_b128 v[24:27], v0 offset:5632\n"
        "v_mul_f32 v73, v73, v53\n"
        "v_mul_f32 v74, v74, v54\n"
        "v_mul_f32 v75, v75, v55\n"
        "v_cvt_pk_bf16_f32 v56, v64, v65\n"
        "v_cvt_pk_bf16_f32 v57, v66, v67\n"
        "v_mfma_f32_32x32x16_bf16 v[32:47], a[68:71], v[28:31], v[32:47]\n"
        "ds_read_b128 v[28:31], v0 offset:5888\n"
        "s_waitcnt lgkmcnt(4)\n"
        "v_cvt_pk_bf16_f32 v58, v72, v73\n"
        "v_cvt_pk_bf16_f32 v59, v74, v75\n"
        "buffer_load_dword v4, v2, s[40:43], s76 offen sc1\n"
        "v_mfma_f32_32x32x16_bf16 v[32:47], a[72:75], v[8:11], v[32:47]\n"
        "ds_read_b128 v[8:11], v0 offset:6144\n"
        "buffer_store_dwordx4 v[56:59], v1, s[40:43], s72 offen sc1\n"
        "v_mfma_f32_32x32x16_bf16 v[32:47], a[76:79], v[12:15], v[32:47]\n"
        "ds_read_b128 v[12:15], v0 offset:6400\n"
        "s_waitcnt lgkmcnt(4)\n"
        "v_mfma_f32_32x32x16_bf16 v[32:47], a[80:83], v[16:19], v[32:47]\n"
        "ds_read_b128 v[16:19], v0 offset:6656\n"
        "v_mfma_f32_32x32x16_bf16 v[32:47], a[84:87], v[20:23], v[32:47]\n"
        "ds_read_b128 v[20:23], v0 offset:6912\n"
        "s_waitcnt lgkmcnt(4)\n"
        "v_mfma_f32_32x32x16_bf16 v[32:47], a[88:91], v[24:27], v[32:47]\n"
        "ds_read_b128 v[24:27], v0 offset:7168\n"
        "v_mfma_f32_32x32x16_bf16 v[32:47], a[92:95], v[28:31], v[32:47]\n"
        "ds_read_b128 v[28:31], v0 offset:7424\n"
        "s_waitcnt lgkmcnt(4)\n"
        "v_mfma_f32_32x32x16_bf16 v[32:47], a[96:99], v[8:11], v[32:47]\n"
        "ds_read_b128 v[8:11], v0 offset:7680\n"
        "v_mfma_f32_32x32x16_bf16 v[32:47], a[100:103], v[12:15], v[32:47]\n"
        "ds_read_b128 v[12:15], v0 offset:7936\n"
        "s_waitcnt lgkmcnt(4)\n"
        "v_mfma_f32_32x32x16_bf16 v[32:47], a[104:107], v[16:19], v[32:47]\n"
        "ds_read_b128 v[16:19], v0 offset:8192\n"
        "v_mfma_f32_32x32x16_bf16 v[32:47], a[108:111], v[20:23], v[32:47]\n"
        "ds_read_b128 v[20:23], v0 offset:8448\n"
        "s_waitcnt lgkmcnt(4)\n"
        "v_mfma_f32_32x32x16_bf16 v[32:47], a[112:115], v[24:27], v[32:47]\n"
        "ds_read_b128 v[24:27], v0 offset:8704\n"
        "v_mfma_f32_32x32x16_bf16 v[32:47], a[116:119], v[28:31], v[32:47]\n"
        "ds_read_b128 v[28:31], v0 offset:8960\n"
        "s_waitcnt lgkmcnt(4)\n"
        "v_mfma_f32_32x32x16_bf16 v[32:47], a[120:123], v[8:11], v[32:47]\n"
        "ds_read_b128 v[8:11], v0 offset:0\n"
        "v_mfma_f32_32x32x16_bf16 v[32:47], a[124:127], v[12:15], v[32:47]\n"
        "ds_read_b128 v[12:15], v0 offset:256\n"
        "s_waitcnt lgkmcnt(4)\n"
        "v_mfma_f32_32x32x16_bf16 v[32:47], a[128:131], v[16:19], v[32:47]\n"
        "ds_read_b128 v[16:19], v0 offset:512\n"
        "s_waitcnt vmcnt(0)\n"
        "v_mov_b32 v5, s74\n"
        "buffer_store_dword v5, v3, s[40:43], s73 offen sc1\n"
        "s_add_u32 s68, s68, 1\n"
        "s_lshr_b32 s66, s68, 1\n"
        "v_mfma_f32_32x32x16_bf16 v[32:47], a[132:135], v[20:23], v[32:47]\n"
        "ds_read_b128 v[20:23], v0 offset:768\n"
        "s_waitcnt lgkmcnt(4)\n"
        "s_bitcmp1_b32 s54, s66\n"
        "s_cselect_b32 s66, 8192, 0\n"
        "s_lshl_b32 s75, s68, 20\n"
        "s_add_u32 s75, s75, s66\n"
        "v_mfma_f32_32x32x16_bf16 v[32:47], a[136:139], v[24:27], v[32:47]\n"
        "ds_read_b128 v[24:27], v0 offset:1024\n"
        "s_add_u32 s75, s75, s59\n"
        "s_mul_i32 s74, s68, 3\n"
        "s_mov_b32 s73, s60\n"
        "s_add_u32 s72, s75, 0\n"
        "v_cmp_gt_i32 vcc, s70, v4\n"
        "v_mfma_f32_32x32x16_bf16 v[32:47], a[140:143], v[28:31], v[32:47]\n"
        "ds_read_b128 v[28:31], v0 offset:1280\n"
        "s_waitcnt lgkmcnt(4)\n"
        "s_and_b64 vcc, vcc, 1\n"
        "s_cbranch_vccz .Lf1_p11_%=\n"
        ".Lf1_p10_%=:\n"
        "v_mfma_f32_32x32x16_bf16 v[48:63], a[144:147], v[8:11], 0\n"
        "ds_read_b128 v[8:11], v0 offset:1536\n"
        "v_mfma_f32_32x32x16_bf16 v[48:63], a[148:151], v[12:15], v[48:63]\n"
        "ds_read_b128 v[12:15], v0 offset:1792\n"
        "s_waitcnt lgkmcnt(4)\n"
        "v_mul_f32 v64, s80, v32\n"
        "v_mul_f32 v65, s80, v33\n"
        "v_mul_f32 v66, s80, v34\n"
        "v_mul_f32 v67, s80, v35\n"
        "v_mfma_f32_32x32x16_bf16 v[48:63], a[152:155], v[16:19], v[48:63]\n"
        "ds_read_b128 v[16:19], v0 offset:2048\n"
        "v_mul_f32 v68, s80, v40\n"
        "v_mul_f32 v69, s80, v41\n"
        "v_mul_f32 v70, s80, v42\n"
        "v_mul_f32 v71, s80, v43\n"
        "v_mfma_f32_32x32x16_bf16 v[48:63], a[156:159], v[20:23], v[48:63]\n"
        "ds_read_b128 v[20:23], v0 offset:2304\n"
        "s_waitcnt lgkmcnt(4)\n"
        "v_exp_f32 v64, v64\n"
        "v_mul_f32 v32, v32, v36\n"
        "v_mfma_f32_32x32x16_bf16 v[48:63], a[160:163], v[24:27], v[48:63]\n"
        "ds_read_b128 v[24:27], v0 offset:2560\n"
        "v_exp_f32 v65, v65\n"
        "v_mul_f32 v33, v33, v37\n"
        "v_exp_f32 v66, v66\n"
        "v_mfma_f32_32x32x16_bf16 v[48:63], a[164:167], v[28:31], v[48:63]\n"
        "ds_read_b128 v[28:31], v0 offset:2816\n"
        "s_waitcnt lgkmcnt(4)\n"
        "v_mul_f32 v34, v34, v38\n"
        "v_exp_f32 v67, v67\n"
        "v_mul_f32 v35, v35, v39\n"
        "v_mfma_f32_32x32x16_bf16 v[48:63], a[168:171], v[8:11], v[48:63]\n"
        "ds_read_b128 v[8:11], v0 offset:3072\n"
        "v_exp_f32 v68, v68\n"
        "v_mul_f32 v40, v40, v44\n"
        "v_exp_f32 v69, v69\n"
        "v_mfma_f32_32x32x16_bf16 v[48:63], a[172:175], v[12:15], v[48:63]\n"
        "ds_read_b128 v[12:15], v0 offset:3328\n"
        "s_waitcnt lgkmcnt(4)\n"
        "v_mul_f32 v41, v41, v45\n"
        "v_exp_f32 v70, v70\n"
        "v_mul_f32 v42, v42, v46\n"
        "v_mfma_f32_32x32x16_bf16 v[48:63], a[176:179], v[16:19], v[48:63]\n"
        "ds_read_b128 v[16:19], v0 offset:3584\n"
        "v_exp_f32 v71, v71\n"
        "v_mul_f32 v43, v43, v47\n"
        "v_add_f32 v64, 1.0, v64\n"
        "v_add_f32 v65, 1.0, v65\n"
        "v_mfma_f32_32x32x16_bf16 v[48:63], a[180:183], v[20:23], v[48:63]\n"
        "ds_read_b128 v[20:23], v0 offset:3840\n"
        "s_waitcnt lgkmcnt(4)\n"
        "v_rcp_f32 v64, v64\n"
        "v_add_f32 v66, 1.0, v66\n"
        "v_mfma_f32_32x32x16_bf16 v[48:63], a[184:187], v[24:27], v[48:63]\n"
        "ds_read_b128 v[24:27], v0 offset:4096\n"
        "v_rcp_f32 v65, v65\n"
        "v_add_f32 v67, 1.0, v67\n"
        "v_rcp_f32 v66, v66\n"
        "v_mfma_f32_32x32x16_bf16 v[48:63], a[188:191], v[28:31], v[48:63]\n"
        "ds_read_b128 v[28:31], v0 offset:4352\n"
        "s_waitcnt lgkmcnt(4)\n"
        "v_add_f32 v68, 1.0, v68\n"
        "v_rcp_f32 v67, v67\n"
        "v_add_f32 v69, 1.0, v69\n"
        "v_mfma_f32_32x32x16_bf16 v[48:63], a[192:195], v[8:11], v[48:63]\n"
        "ds_read_b128 v[8:11], v0 offset:4608\n"
        "v_rcp_f32 v68, v68\n"
        "v_add_f32 v70, 1.0, v70\n"
        "v_rcp_f32 v69, v69\n"
        "v_mfma_f32_32x32x16_bf16 v[48:63], a[196:199], v[12:15], v[48:63]\n"
        "ds_read_b128 v[12:15], v0 offset:4864\n"
        "s_waitcnt lgkmcnt(4)\n"
        "v_add_f32 v71, 1.0, v71\n"
        "v_rcp_f32 v70, v70\n"
        "v_mfma_f32_32x32x16_bf16 v[48:63], a[200:203], v[16:19], v[48:63]\n"
        "ds_read_b128 v[16:19], v0 offset:5120\n"
        "v_rcp_f32 v71, v71\n"
        "v_mul_f32 v32, v32, v64\n"
        "v_mul_f32 v33, v33, v65\n"
        "v_mul_f32 v34, v34, v66\n"
        "v_mfma_f32_32x32x16_bf16 v[48:63], a[204:207], v[20:23], v[48:63]\n"
        "ds_read_b128 v[20:23], v0 offset:5376\n"
        "s_waitcnt lgkmcnt(4)\n"
        "v_mul_f32 v35, v35, v67\n"
        "v_mul_f32 v40, v40, v68\n"
        "v_mul_f32 v41, v41, v69\n"
        "v_mul_f32 v42, v42, v70\n"
        "v_mfma_f32_32x32x16_bf16 v[48:63], a[208:211], v[24:27], v[48:63]\n"
        "ds_read_b128 v[24:27], v0 offset:5632\n"
        "v_mul_f32 v43, v43, v71\n"
        "v_cvt_pk_bf16_f32 v72, v32, v33\n"
        "v_cvt_pk_bf16_f32 v73, v34, v35\n"
        "v_cvt_pk_bf16_f32 v74, v40, v41\n"
        "v_cvt_pk_bf16_f32 v75, v42, v43\n"
        "v_mfma_f32_32x32x16_bf16 v[48:63], a[212:215], v[28:31], v[48:63]\n"
        "ds_read_b128 v[28:31], v0 offset:5888\n"
        "s_waitcnt lgkmcnt(4)\n"
        "buffer_store_dwordx4 v[72:75], v1, s[40:43], s72 offen sc1\n"
        "v_mfma_f32_32x32x16_bf16 v[48:63], a[216:219], v[8:11], v[48:63]\n"
        "ds_read_b128 v[8:11], v0 offset:6144\n"
        "buffer_load_dword v4, v2, s[40:43], s76 offen sc1\n"
        "v_mfma_f32_32x32x16_bf16 v[48:63], a[220:223], v[12:15], v[48:63]\n"
        "ds_read_b128 v[12:15], v0 offset:6400\n"
        "s_waitcnt lgkmcnt(4)\n"
        "v_mfma_f32_32x32x16_bf16 v[48:63], a[224:227], v[16:19], v[48:63]\n"
        "ds_read_b128 v[16:19], v0 offset:6656\n"
        "v_mfma_f32_32x32x16_bf16 v[48:63], a[228:231], v[20:23], v[48:63]\n"
        "ds_read_b128 v[20:23], v0 offset:6912\n"
        "s_waitcnt lgkmcnt(4)\n"
        "v_mfma_f32_32x32x16_bf16 v[48:63], a[232:235], v[24:27], v[48:63]\n"
        "ds_read_b128 v[24:27], v0 offset:7168\n"
        "v_mfma_f32_32x32x16_bf16 v[48:63], a[236:239], v[28:31], v[48:63]\n"
        "ds_read_b128 v[28:31], v0 offset:7424\n"
        "s_waitcnt lgkmcnt(4)\n"
        "v_mfma_f32_32x32x16_bf16 v[48:63], a[240:243], v[8:11], v[48:63]\n"
        "ds_read_b128 v[8:11], v0 offset:7680\n"
        "v_mfma_f32_32x32x16_bf16 v[48:63], a[244:247], v[12:15], v[48:63]\n"
        "ds_read_b128 v[12:15], v0 offset:7936\n"
        "s_waitcnt lgkmcnt(4)\n"
        "v_mfma_f32_32x32x16_bf16 v[48:63], a[248:251], v[16:19], v[48:63]\n"
        "ds_read_b128 v[16:19], v0 offset:8192\n"
        "v_mfma_f32_32x32x16_bf16 v[48:63], a[252:255], v[20:23], v[48:63]\n"
        "ds_read_b128 v[20:23], v0 offset:8448\n"
        "s_waitcnt lgkmcnt(4)\n"
        "v_mfma_f32_32x32x16_bf16 v[48:63], v[80:83], v[24:27], v[48:63]\n"
        "ds_read_b128 v[24:27], v0 offset:8704\n"
        "v_mfma_f32_32x32x16_bf16 v[48:63], v[84:87], v[28:31], v[48:63]\n"
        "ds_read_b128 v[28:31], v0 offset:8960\n"
        "s_waitcnt lgkmcnt(4)\n"
        "v_mfma_f32_32x32x16_bf16 v[48:63], v[88:91], v[8:11], v[48:63]\n"
        "ds_read_b128 v[8:11], v0 offset:0\n"
        "v_mfma_f32_32x32x16_bf16 v[48:63], v[92:95], v[12:15], v[48:63]\n"
        "ds_read_b128 v[12:15], v0 offset:256\n"
        "s_waitcnt lgkmcnt(4)\n"
        "v_mfma_f32_32x32x16_bf16 v[48:63], v[96:99], v[16:19], v[48:63]\n"
        "ds_read_b128 v[16:19], v0 offset:512\n"
        "s_waitcnt vmcnt(1)\n"
        "v_mov_b32 v5, s74\n"
        "buffer_store_dword v5, v3, s[40:43], s73 offen sc1\n"
        "s_add_u32 s74, s74, 1\n"
        "s_add_u32 s72, s75, 49152\n"
        "v_mfma_f32_32x32x16_bf16 v[48:63], v[100:103], v[20:23], v[48:63]\n"
        "ds_read_b128 v[20:23], v0 offset:768\n"
        "s_waitcnt lgkmcnt(4)\n"
        "v_mfma_f32_32x32x16_bf16 v[48:63], v[104:107], v[24:27], v[48:63]\n"
        "ds_read_b128 v[24:27], v0 offset:1024\n"
        "s_waitcnt vmcnt(1)\n"
        "v_cmp_gt_i32 vcc, s70, v4\n"
        "v_mfma_f32_32x32x16_bf16 v[48:63], v[108:111], v[28:31], v[48:63]\n"
        "ds_read_b128 v[28:31], v0 offset:1280\n"
        "s_waitcnt lgkmcnt(4)\n"
        "s_and_b64 vcc, vcc, 1\n"
        "s_cbranch_vccz .Lf1_p21_%=\n"
        ".Lf1_p20_%=:\n"
        "v_mfma_f32_32x32x16_bf16 v[64:79], v[112:115], v[8:11], 0\n"
        "ds_read_b128 v[8:11], v0 offset:1536\n"
        "v_mfma_f32_32x32x16_bf16 v[64:79], v[116:119], v[12:15], v[64:79]\n"
        "ds_read_b128 v[12:15], v0 offset:1792\n"
        "s_waitcnt lgkmcnt(4)\n"
        "v_mul_f32 v32, s80, v48\n"
        "v_mul_f32 v33, s80, v49\n"
        "v_mul_f32 v34, s80, v50\n"
        "v_mul_f32 v35, s80, v51\n"
        "v_mfma_f32_32x32x16_bf16 v[64:79], v[120:123], v[16:19], v[64:79]\n"
        "ds_read_b128 v[16:19], v0 offset:2048\n"
        "v_mul_f32 v36, s80, v56\n"
        "v_mul_f32 v37, s80, v57\n"
        "v_mul_f32 v38, s80, v58\n"
        "v_mul_f32 v39, s80, v59\n"
        "v_mfma_f32_32x32x16_bf16 v[64:79], v[124:127], v[20:23], v[64:79]\n"
        "ds_read_b128 v[20:23], v0 offset:2304\n"
        "s_waitcnt lgkmcnt(4)\n"
        "v_exp_f32 v32, v32\n"
        "v_mul_f32 v48, v48, v52\n"
        "v_mfma_f32_32x32x16_bf16 v[64:79], v[128:131], v[24:27], v[64:79]\n"
        "ds_read_b128 v[24:27], v0 offset:2560\n"
        "v_exp_f32 v33, v33\n"
        "v_mul_f32 v49, v49, v53\n"
        "v_exp_f32 v34, v34\n"
        "v_mfma_f32_32x32x16_bf16 v[64:79], v[132:135], v[28:31], v[64:79]\n"
        "ds_read_b128 v[28:31], v0 offset:2816\n"
        "s_waitcnt lgkmcnt(4)\n"
        "v_mul_f32 v50, v50, v54\n"
        "v_exp_f32 v35, v35\n"
        "v_mul_f32 v51, v51, v55\n"
        "v_mfma_f32_32x32x16_bf16 v[64:79], v[136:139], v[8:11], v[64:79]\n"
        "ds_read_b128 v[8:11], v0 offset:3072\n"
        "v_exp_f32 v36, v36\n"
        "v_mul_f32 v56, v56, v60\n"
        "v_exp_f32 v37, v37\n"
        "v_mfma_f32_32x32x16_bf16 v[64:79], v[140:143], v[12:15], v[64:79]\n"
        "ds_read_b128 v[12:15], v0 offset:3328\n"
        "s_waitcnt lgkmcnt(4)\n"
        "v_mul_f32 v57, v57, v61\n"
        "v_exp_f32 v38, v38\n"
        "v_mul_f32 v58, v58, v62\n"
        "v_mfma_f32_32x32x16_bf16 v[64:79], v[144:147], v[16:19], v[64:79]\n"
        "ds_read_b128 v[16:19], v0 offset:3584\n"
        "v_exp_f32 v39, v39\n"
        "v_mul_f32 v59, v59, v63\n"
        "v_add_f32 v32, 1.0, v32\n"
        "v_add_f32 v33, 1.0, v33\n"
        "v_mfma_f32_32x32x16_bf16 v[64:79], v[148:151], v[20:23], v[64:79]\n"
        "ds_read_b128 v[20:23], v0 offset:3840\n"
        "s_waitcnt lgkmcnt(4)\n"
        "v_rcp_f32 v32, v32\n"
        "v_add_f32 v34, 1.0, v34\n"
        "v_mfma_f32_32x32x16_bf16 v[64:79], v[152:155], v[24:27], v[64:79]\n"
        "ds_read_b128 v[24:27], v0 offset:4096\n"
        "v_rcp_f32 v33, v33\n"
        "v_add_f32 v35, 1.0, v35\n"
        "v_rcp_f32 v34, v34\n"
        "v_mfma_f32_32x32x16_bf16 v[64:79], v[156:159], v[28:31], v[64:79]\n"
        "ds_read_b128 v[28:31], v0 offset:4352\n"
        "s_waitcnt lgkmcnt(4)\n"
        "v_add_f32 v36, 1.0, v36\n"
        "v_rcp_f32 v35, v35\n"
        "v_add_f32 v37, 1.0, v37\n"
        "v_mfma_f32_32x32x16_bf16 v[64:79], v[160:163], v[8:11], v[64:79]\n"
        "ds_read_b128 v[8:11], v0 offset:4608\n"
        "v_rcp_f32 v36, v36\n"
        "v_add_f32 v38, 1.0, v38\n"
        "v_rcp_f32 v37, v37\n"
        "v_mfma_f32_32x32x16_bf16 v[64:79], v[164:167], v[12:15], v[64:79]\n"
        "ds_read_b128 v[12:15], v0 offset:4864\n"
        "s_waitcnt lgkmcnt(4)\n"
        "v_add_f32 v39, 1.0, v39\n"
        "v_rcp_f32 v38, v38\n"
        "v_mfma_f32_32x32x16_bf16 v[64:79], v[168:171], v[16:19], v[64:79]\n"
        "ds_read_b128 v[16:19], v0 offset:5120\n"
        "v_rcp_f32 v39, v39\n"
        "v_mul_f32 v48, v48, v32\n"
        "v_mul_f32 v49, v49, v33\n"
        "v_mul_f32 v50, v50, v34\n"
        "v_mfma_f32_32x32x16_bf16 v[64:79], v[172:175], v[20:23], v[64:79]\n"
        "ds_read_b128 v[20:23], v0 offset:5376\n"
        "s_waitcnt lgkmcnt(4)\n"
        "v_mul_f32 v51, v51, v35\n"
        "v_mul_f32 v56, v56, v36\n"
        "v_mul_f32 v57, v57, v37\n"
        "v_mul_f32 v58, v58, v38\n"
        "v_mfma_f32_32x32x16_bf16 v[64:79], v[176:179], v[24:27], v[64:79]\n"
        "ds_read_b128 v[24:27], v0 offset:5632\n"
        "v_mul_f32 v59, v59, v39\n"
        "v_cvt_pk_bf16_f32 v40, v48, v49\n"
        "v_cvt_pk_bf16_f32 v41, v50, v51\n"
        "v_cvt_pk_bf16_f32 v42, v56, v57\n"
        "v_cvt_pk_bf16_f32 v43, v58, v59\n"
        "v_mfma_f32_32x32x16_bf16 v[64:79], v[180:183], v[28:31], v[64:79]\n"
        "ds_read_b128 v[28:31], v0 offset:5888\n"
        "s_waitcnt lgkmcnt(4)\n"
        "buffer_store_dwordx4 v[40:43], v1, s[40:43], s72 offen sc1\n"
        "v_mfma_f32_32x32x16_bf16 v[64:79], v[184:187], v[8:11], v[64:79]\n"
        "ds_read_b128 v[8:11], v0 offset:6144\n"
        "buffer_load_dword v4, v2, s[40:43], s76 offen sc1\n"
        "v_mfma_f32_32x32x16_bf16 v[64:79], v[188:191], v[12:15], v[64:79]\n"
        "ds_read_b128 v[12:15], v0 offset:6400\n"
        "s_waitcnt lgkmcnt(4)\n"
        "v_mfma_f32_32x32x16_bf16 v[64:79], v[192:195], v[16:19], v[64:79]\n"
        "ds_read_b128 v[16:19], v0 offset:6656\n"
        "s_add_u32 s78, s78, 37376\n"
        "s_cmp_eq_u32 s78, 112128\n"
        "s_cselect_b32 s78, 0, s78\n"
        "v_mfma_f32_32x32x16_bf16 v[64:79], v[196:199], v[20:23], v[64:79]\n"
        "ds_read_b128 v[20:23], v0 offset:6912\n"
        "s_waitcnt lgkmcnt(4)\n"
        "v_mfma_f32_32x32x16_bf16 v[64:79], v[200:203], v[24:27], v[64:79]\n"
        "ds_read_b128 v[24:27], v0 offset:7168\n"
        "v_mfma_f32_32x32x16_bf16 v[64:79], v[204:207], v[28:31], v[64:79]\n"
        "ds_read_b128 v[28:31], v0 offset:7424\n"
        "s_waitcnt lgkmcnt(4)\n"
        "v_mfma_f32_32x32x16_bf16 v[64:79], v[208:211], v[8:11], v[64:79]\n"
        "ds_read_b128 v[8:11], v0 offset:7680\n"
        "v_mfma_f32_32x32x16_bf16 v[64:79], v[212:215], v[12:15], v[64:79]\n"
        "ds_read_b128 v[12:15], v0 offset:7936\n"
        "s_waitcnt lgkmcnt(4)\n"
        "v_mfma_f32_32x32x16_bf16 v[64:79], v[216:219], v[16:19], v[64:79]\n"
        "ds_read_b128 v[16:19], v0 offset:8192\n"
        "v_mfma_f32_32x32x16_bf16 v[64:79], v[220:223], v[20:23], v[64:79]\n"
        "ds_read_b128 v[20:23], v0 offset:8448\n"
        "s_waitcnt lgkmcnt(4)\n"
        "v_mfma_f32_32x32x16_bf16 v[64:79], v[224:227], v[24:27], v[64:79]\n"
        "ds_read_b128 v[24:27], v0 offset:8704\n"
        "v_mfma_f32_32x32x16_bf16 v[64:79], v[228:231], v[28:31], v[64:79]\n"
        "ds_read_b128 v[28:31], v0 offset:8960\n"
        "s_waitcnt lgkmcnt(4)\n"
        "s_sub_u32 s65, s69, s68\n"
        "s_cmp_eq_u32 s65, 2\n"
        "s_cbranch_scc0 .Lf1_tail_%=\n"
        "s_waitcnt vmcnt(2)\n"
        "s_barrier\n"
        "v_add_u32 v0, s78, v6\n"
        "v_mfma_f32_32x32x16_bf16 v[64:79], v[232:235], v[8:11], v[64:79]\n"
        "ds_read_b128 v[8:11], v0 offset:0\n"
        "s_cmp_lt_u32 s69, 64\n"
        "s_cselect_b32 s70, s69, 2147483647\n"
        "v_mfma_f32_32x32x16_bf16 v[64:79], v[236:239], v[12:15], v[64:79]\n"
        "ds_read_b128 v[12:15], v0 offset:256\n"
        "s_waitcnt lgkmcnt(4)\n"
        "v_mfma_f32_32x32x16_bf16 v[64:79], v[240:243], v[16:19], v[64:79]\n"
        "ds_read_b128 v[16:19], v0 offset:512\n"
        "s_waitcnt vmcnt(1)\n"
        "v_mov_b32 v5, s74\n"
        "buffer_store_dword v5, v3, s[40:43], s73 offen sc1\n"
        "s_add_u32 s74, s74, 1\n"
        "s_add_u32 s72, s75, 98304\n"
        "v_mfma_f32_32x32x16_bf16 v[64:79], v[244:247], v[20:23], v[64:79]\n"
        "ds_read_b128 v[20:23], v0 offset:768\n"
        "s_waitcnt lgkmcnt(4)\n"
        "v_mfma_f32_32x32x16_bf16 v[64:79], v[248:251], v[24:27], v[64:79]\n"
        "ds_read_b128 v[24:27], v0 offset:1024\n"
        "s_waitcnt vmcnt(1)\n"
        "v_cmp_gt_i32 vcc, s70, v4\n"
        "v_mfma_f32_32x32x16_bf16 v[64:79], v[252:255], v[28:31], v[64:79]\n"
        "ds_read_b128 v[28:31], v0 offset:1280\n"
        "s_waitcnt lgkmcnt(4)\n"
        "s_and_b64 vcc, vcc, 1\n"
        "s_cbranch_vccz .Lf1_p01_%=\n"
        "s_branch .Lf1_p00_%=\n"
        ".Lf1_p01_%=:\n"
        "v_mfma_f32_32x32x16_bf16 v[32:47], a[0:3], v[8:11], 0\n"
        "ds_read_b128 v[8:11], v0 offset:1536\n"
        "s_add_u32 m0, s77, s61\n"
        "s_lshr_b32 s65, s69, 1\n"
        "s_bitcmp1_b32 s54, s65\n"
        "s_cselect_b32 s65, 8192, 0\n"
        "s_lshl_b32 s71, s69, 20\n"
        "v_mfma_f32_32x32x16_bf16 v[32:47], a[4:7], v[12:15], v[32:47]\n"
        "ds_read_b128 v[12:15], v0 offset:1792\n"
        "s_waitcnt lgkmcnt(4)\n"
        "s_add_u32 s71, s71, s65\n"
        "s_add_u32 s71, s71, s58\n"
        "v_mul_f32 v48, s80, v64\n"
        "v_mul_f32 v49, s80, v65\n"
        "v_mfma_f32_32x32x16_bf16 v[32:47], a[8:11], v[16:19], v[32:47]\n"
        "ds_read_b128 v[16:19], v0 offset:2048\n"
        "buffer_load_dwordx4 v1, s[40:43], s71 offen offset:0 sc1 lds\n"
        "v_mul_f32 v50, s80, v66\n"
        "v_mul_f32 v51, s80, v67\n"
        "v_mul_f32 v52, s80, v72\n"
        "v_mul_f32 v53, s80, v73\n"
        "v_mfma_f32_32x32x16_bf16 v[32:47], a[12:15], v[20:23], v[32:47]\n"
        "ds_read_b128 v[20:23], v0 offset:2304\n"
        "s_waitcnt lgkmcnt(4)\n"
        "v_mul_f32 v54, s80, v74\n"
        "v_mul_f32 v55, s80, v75\n"
        "v_exp_f32 v48, v48\n"
        "v_mfma_f32_32x32x16_bf16 v[32:47], a[16:19], v[24:27], v[32:47]\n"
        "ds_read_b128 v[24:27], v0 offset:2560\n"
        "buffer_load_dwordx4 v1, s[40:43], s71 offen offset:1024 sc1 lds\n"
        "v_mul_f32 v64, v64, v68\n"
        "v_exp_f32 v49, v49\n"
        "v_mul_f32 v65, v65, v69\n"
        "v_mfma_f32_32x32x16_bf16 v[32:47], a[20:23], v[28:31], v[32:47]\n"
        "ds_read_b128 v[28:31], v0 offset:2816\n"
        "s_waitcnt lgkmcnt(4)\n"
        "v_exp_f32 v50, v50\n"
        "v_mul_f32 v66, v66, v70\n"
        "v_mfma_f32_32x32x16_bf16 v[32:47], a[24:27], v[8:11], v[32:47]\n"
        "ds_read_b128 v[8:11], v0 offset:3072\n"
        "buffer_load_dwordx4 v1, s[40:43], s71 offen offset:2048 sc1 lds\n"
        "v_exp_f32 v51, v51\n"
        "v_mul_f32 v67, v67, v71\n"
        "v_mfma_f32_32x32x16_bf16 v[32:47], a[28:31], v[12:15], v[32:47]\n"
        "ds_read_b128 v[12:15], v0 offset:3328\n"
        "s_waitcnt lgkmcnt(4)\n"
        "v_exp_f32 v52, v52\n"
        "v_mul_f32 v72, v72, v76\n"
        "v_mfma_f32_32x32x16_bf16 v[32:47], a[32:35], v[16:19], v[32:47]\n"
        "ds_read_b128 v[16:19], v0 offset:3584\n"
        "buffer_load_dwordx4 v1, s[40:43], s71 offen offset:3072 sc1 lds\n"
        "s_add_u32 m0, m0, 4096\n"
        "s_add_u32 s71, s71, 4096\n"
        "s_mul_i32 s65, s48, -15360\n"
        "v_mfma_f32_32x32x16_bf16 v[32:47], a[36:39], v[20:23], v[32:47]\n"
        "s_waitcnt vmcnt(4)\n"
        "s_barrier\n"
        "ds_read_b128 v[20:23], v0 offset:3840\n"
        "s_waitcnt lgkmcnt(4)\n"
        "v_exp_f32 v53, v53\n"
        "v_mfma_f32_32x32x16_bf16 v[32:47], a[40:43], v[24:27], v[32:47]\n"
        "ds_read_b128 v[24:27], v0 offset:4096\n"
        "buffer_load_dwordx4 v1, s[40:43], s71 offen offset:0 sc1 lds\n"
        "v_mul_f32 v73, v73, v77\n"
        "v_exp_f32 v54, v54\n"
        "v_mul_f32 v74, v74, v78\n"
        "v_mfma_f32_32x32x16_bf16 v[32:47], a[44:47], v[28:31], v[32:47]\n"
        "ds_read_b128 v[28:31], v0 offset:4352\n"
        "s_waitcnt lgkmcnt(4)\n"
        "v_exp_f32 v55, v55\n"
        "v_mul_f32 v75, v75, v79\n"
        "v_add_f32 v48, 1.0, v48\n"
        "v_mfma_f32_32x32x16_bf16 v[32:47], a[48:51], v[8:11], v[32:47]\n"
        "ds_read_b128 v[8:11], v0 offset:4608\n"
        "buffer_load_dwordx4 v1, s[40:43], s71 offen offset:1024 sc1 lds\n"
        "v_add_f32 v49, 1.0, v49\n"
        "v_rcp_f32 v48, v48\n"
        "v_add_f32 v50, 1.0, v50\n"
        "v_mfma_f32_32x32x16_bf16 v[32:47], a[52:55], v[12:15], v[32:47]\n"
        "ds_read_b128 v[12:15], v0 offset:4864\n"
        "s_waitcnt lgkmcnt(4)\n"
        "v_rcp_f32 v49, v49\n"
        "v_add_f32 v51, 1.0, v51\n"
        "v_mfma_f32_32x32x16_bf16 v[32:47], a[56:59], v[16:19], v[32:47]\n"
        "ds_read_b128 v[16:19], v0 offset:5120\n"
        "buffer_load_dwordx4 v1, s[40:43], s71 offen offset:2048 sc1 lds\n"
        "v_rcp_f32 v50, v50\n"
        "v_add_f32 v52, 1.0, v52\n"
        "v_mfma_f32_32x32x16_bf16 v[32:47], a[60:63], v[20:23], v[32:47]\n"
        "ds_read_b128 v[20:23], v0 offset:5376\n"
        "s_waitcnt lgkmcnt(4)\n"
        "v_rcp_f32 v51, v51\n"
        "v_add_f32 v53, 1.0, v53\n"
        "v_mfma_f32_32x32x16_bf16 v[32:47], a[64:67], v[24:27], v[32:47]\n"
        "ds_read_b128 v[24:27], v0 offset:5632\n"
        "buffer_load_dwordx4 v1, s[40:43], s71 offen offset:3072 sc1 lds\n"
        "s_add_u32 m0, m0, 4096\n"
        "s_add_u32 s71, s71, s65\n"
        "s_add_u32 s71, s71, 61440\n"
        "v_mfma_f32_32x32x16_bf16 v[32:47], a[68:71], v[28:31], v[32:47]\n"
        "ds_read_b128 v[28:31], v0 offset:5888\n"
        "s_waitcnt lgkmcnt(4)\n"
        "v_rcp_f32 v52, v52\n"
        "v_add_f32 v54, 1.0, v54\n"
        "v_mfma_f32_32x32x16_bf16 v[32:47], a[72:75], v[8:11], v[32:47]\n"
        "ds_read_b128 v[8:11], v0 offset:6144\n"
        "buffer_load_dwordx4 v1, s[40:43], s71 offen offset:0 sc1 lds\n"
        "s_add_u32 s69, s69, 1\n"
        "s_add_u32 s77, s77, 37376\n"
        "s_cmp_eq_u32 s77, 112128\n"
        "s_cselect_b32 s77, 0, s77\n"
        "v_mfma_f32_32x32x16_bf16 v[32:47], a[76:79], v[12:15], v[32:47]\n"
        "ds_read_b128 v[12:15], v0 offset:6400\n"
        "s_waitcnt lgkmcnt(4)\n"
        "s_mov_b32 s70, 2147483647\n"
        "v_rcp_f32 v53, v53\n"
        "v_add_f32 v55, 1.0, v55\n"
        "v_mfma_f32_32x32x16_bf16 v[32:47], a[80:83], v[16:19], v[32:47]\n"
        "ds_read_b128 v[16:19], v0 offset:6656\n"
        "v_rcp_f32 v54, v54\n"
        "v_rcp_f32 v55, v55\n"
        "v_mul_f32 v64, v64, v48\n"
        "v_mfma_f32_32x32x16_bf16 v[32:47], a[84:87], v[20:23], v[32:47]\n"
        "ds_read_b128 v[20:23], v0 offset:6912\n"
        "s_waitcnt lgkmcnt(4)\n"
        "v_mul_f32 v65, v65, v49\n"
        "v_mul_f32 v66, v66, v50\n"
        "v_mul_f32 v67, v67, v51\n"
        "v_mul_f32 v72, v72, v52\n"
        "v_mfma_f32_32x32x16_bf16 v[32:47], a[88:91], v[24:27], v[32:47]\n"
        "ds_read_b128 v[24:27], v0 offset:7168\n"
        "v_mul_f32 v73, v73, v53\n"
        "v_mul_f32 v74, v74, v54\n"
        "v_mul_f32 v75, v75, v55\n"
        "v_cvt_pk_bf16_f32 v56, v64, v65\n"
        "v_cvt_pk_bf16_f32 v57, v66, v67\n"
        "v_mfma_f32_32x32x16_bf16 v[32:47], a[92:95], v[28:31], v[32:47]\n"
        "ds_read_b128 v[28:31], v0 offset:7424\n"
        "s_waitcnt lgkmcnt(4)\n"
        "v_cvt_pk_bf16_f32 v58, v72, v73\n"
        "v_cvt_pk_bf16_f32 v59, v74, v75\n"
        "v_mfma_f32_32x32x16_bf16 v[32:47], a[96:99], v[8:11], v[32:47]\n"
        "ds_read_b128 v[8:11], v0 offset:7680\n"
        "buffer_store_dwordx4 v[56:59], v1, s[40:43], s72 offen sc1\n"
        "v_mfma_f32_32x32x16_bf16 v[32:47], a[100:103], v[12:15], v[32:47]\n"
        "ds_read_b128 v[12:15], v0 offset:7936\n"
        "s_waitcnt lgkmcnt(4)\n"
        "v_mfma_f32_32x32x16_bf16 v[32:47], a[104:107], v[16:19], v[32:47]\n"
        "ds_read_b128 v[16:19], v0 offset:8192\n"
        "v_mfma_f32_32x32x16_bf16 v[32:47], a[108:111], v[20:23], v[32:47]\n"
        "ds_read_b128 v[20:23], v0 offset:8448\n"
        "s_waitcnt lgkmcnt(4)\n"
        "v_mfma_f32_32x32x16_bf16 v[32:47], a[112:115], v[24:27], v[32:47]\n"
        "ds_read_b128 v[24:27], v0 offset:8704\n"
        "v_mfma_f32_32x32x16_bf16 v[32:47], a[116:119], v[28:31], v[32:47]\n"
        "ds_read_b128 v[28:31], v0 offset:8960\n"
        "s_waitcnt lgkmcnt(4)\n"
        "v_mfma_f32_32x32x16_bf16 v[32:47], a[120:123], v[8:11], v[32:47]\n"
        "ds_read_b128 v[8:11], v0 offset:0\n"
        "v_mfma_f32_32x32x16_bf16 v[32:47], a[124:127], v[12:15], v[32:47]\n"
        "ds_read_b128 v[12:15], v0 offset:256\n"
        "s_waitcnt lgkmcnt(4)\n"
        "v_mfma_f32_32x32x16_bf16 v[32:47], a[128:131], v[16:19], v[32:47]\n"
        "ds_read_b128 v[16:19], v0 offset:512\n"
        "s_waitcnt vmcnt(0)\n"
        "v_mov_b32 v5, s74\n"
        "buffer_store_dword v5, v3, s[40:43], s73 offen sc1\n"
        "s_add_u32 s68, s68, 1\n"
        "s_lshr_b32 s66, s68, 1\n"
        "v_mfma_f32_32x32x16_bf16 v[32:47], a[132:135], v[20:23], v[32:47]\n"
        "ds_read_b128 v[20:23], v0 offset:768\n"
        "s_waitcnt lgkmcnt(4)\n"
        "s_bitcmp1_b32 s54, s66\n"
        "s_cselect_b32 s66, 8192, 0\n"
        "s_lshl_b32 s75, s68, 20\n"
        "s_add_u32 s75, s75, s66\n"
        "v_mfma_f32_32x32x16_bf16 v[32:47], a[136:139], v[24:27], v[32:47]\n"
        "ds_read_b128 v[24:27], v0 offset:1024\n"
        "s_add_u32 s75, s75, s59\n"
        "s_mul_i32 s74, s68, 3\n"
        "s_mov_b32 s73, s60\n"
        "s_add_u32 s72, s75, 0\n"
        "v_mfma_f32_32x32x16_bf16 v[32:47], a[140:143], v[28:31], v[32:47]\n"
        "ds_read_b128 v[28:31], v0 offset:1280\n"
        "s_waitcnt lgkmcnt(4)\n"
        "s_branch .Lf1_p10_%=\n"
        ".Lf1_p11_%=:\n"
        "v_mfma_f32_32x32x16_bf16 v[48:63], a[144:147], v[8:11], 0\n"
        "ds_read_b128 v[8:11], v0 offset:1536\n"
        "s_add_u32 m0, s77, s61\n"
        "s_lshr_b32 s65, s69, 1\n"
        "s_bitcmp1_b32 s54, s65\n"
        "s_cselect_b32 s65, 8192, 0\n"
        "s_lshl_b32 s71, s69, 20\n"
        "v_mfma_f32_32x32x16_bf16 v[48:63], a[148:151], v[12:15], v[48:63]\n"
        "ds_read_b128 v[12:15], v0 offset:1792\n"
        "s_waitcnt lgkmcnt(4)\n"
        "s_add_u32 s71, s71, s65\n"
        "s_add_u32 s71, s71, s58\n"
        "v_mul_f32 v64, s80, v32\n"
        "v_mul_f32 v65, s80, v33\n"
        "v_mfma_f32_32x32x16_bf16 v[48:63], a[152:155], v[16:19], v[48:63]\n"
        "ds_read_b128 v[16:19], v0 offset:2048\n"
        "buffer_load_dwordx4 v1, s[40:43], s71 offen offset:0 sc1 lds\n"
        "v_mul_f32 v66, s80, v34\n"
        "v_mul_f32 v67, s80, v35\n"
        "v_mul_f32 v68, s80, v40\n"
        "v_mul_f32 v69, s80, v41\n"
        "v_mfma_f32_32x32x16_bf16 v[48:63], a[156:159], v[20:23], v[48:63]\n"
        "ds_read_b128 v[20:23], v0 offset:2304\n"
        "s_waitcnt lgkmcnt(4)\n"
        "v_mul_f32 v70, s80, v42\n"
        "v_mul_f32 v71, s80, v43\n"
        "v_exp_f32 v64, v64\n"
        "v_mfma_f32_32x32x16_bf16 v[48:63], a[160:163], v[24:27], v[48:63]\n"
        "ds_read_b128 v[24:27], v0 offset:2560\n"
        "buffer_load_dwordx4 v1, s[40:43], s71 offen offset:1024 sc1 lds\n"
        "v_mul_f32 v32, v32, v36\n"
        "v_exp_f32 v65, v65\n"
        "v_mul_f32 v33, v33, v37\n"
        "v_mfma_f32_32x32x16_bf16 v[48:63], a[164:167], v[28:31], v[48:63]\n"
        "ds_read_b128 v[28:31], v0 offset:2816\n"
        "s_waitcnt lgkmcnt(4)\n"
        "v_exp_f32 v66, v66\n"
        "v_mul_f32 v34, v34, v38\n"
        "v_mfma_f32_32x32x16_bf16 v[48:63], a[168:171], v[8:11], v[48:63]\n"
        "ds_read_b128 v[8:11], v0 offset:3072\n"
        "buffer_load_dwordx4 v1, s[40:43], s71 offen offset:2048 sc1 lds\n"
        "v_exp_f32 v67, v67\n"
        "v_mul_f32 v35, v35, v39\n"
        "v_mfma_f32_32x32x16_bf16 v[48:63], a[172:175], v[12:15], v[48:63]\n"
        "ds_read_b128 v[12:15], v0 offset:3328\n"
        "s_waitcnt lgkmcnt(4)\n"
        "v_exp_f32 v68, v68\n"
        "v_mul_f32 v40, v40, v44\n"
        "v_mfma_f32_32x32x16_bf16 v[48:63], a[176:179], v[16:19], v[48:63]\n"
        "ds_read_b128 v[16:19], v0 offset:3584\n"
        "buffer_load_dwordx4 v1, s[40:43], s71 offen offset:3072 sc1 lds\n"
        "s_add_u32 m0, m0, 4096\n"
        "s_add_u32 s71, s71, 4096\n"
        "s_mul_i32 s65, s48, -15360\n"
        "v_mfma_f32_32x32x16_bf16 v[48:63], a[180:183], v[20:23], v[48:63]\n"
        "ds_read_b128 v[20:23], v0 offset:3840\n"
        "s_waitcnt lgkmcnt(4)\n"
        "v_exp_f32 v69, v69\n"
        "v_mul_f32 v41, v41, v45\n"
        "v_mfma_f32_32x32x16_bf16 v[48:63], a[184:187], v[24:27], v[48:63]\n"
        "ds_read_b128 v[24:27], v0 offset:4096\n"
        "buffer_load_dwordx4 v1, s[40:43], s71 offen offset:0 sc1 lds\n"
        "v_exp_f32 v70, v70\n"
        "v_mul_f32 v42, v42, v46\n"
        "v_mfma_f32_32x32x16_bf16 v[48:63], a[188:191], v[28:31], v[48:63]\n"
        "ds_read_b128 v[28:31], v0 offset:4352\n"
        "s_waitcnt lgkmcnt(4)\n"
        "v_exp_f32 v71, v71\n"
        "v_mul_f32 v43, v43, v47\n"
        "v_add_f32 v64, 1.0, v64\n"
        "v_mfma_f32_32x32x16_bf16 v[48:63], a[192:195], v[8:11], v[48:63]\n"
        "ds_read_b128 v[8:11], v0 offset:4608\n"
        "buffer_load_dwordx4 v1, s[40:43], s71 offen offset:1024 sc1 lds\n"
        "v_add_f32 v65, 1.0, v65\n"
        "v_rcp_f32 v64, v64\n"
        "v_add_f32 v66, 1.0, v66\n"
        "v_mfma_f32_32x32x16_bf16 v[48:63], a[196:199], v[12:15], v[48:63]\n"
        "ds_read_b128 v[12:15], v0 offset:4864\n"
        "s_waitcnt lgkmcnt(4)\n"
        "v_rcp_f32 v65, v65\n"
        "v_add_f32 v67, 1.0, v67\n"
        "v_mfma_f32_32x32x16_bf16 v[48:63], a[200:203], v[16:19], v[48:63]\n"
        "ds_read_b128 v[16:19], v0 offset:5120\n"
        "buffer_load_dwordx4 v1, s[40:43], s71 offen offset:2048 sc1 lds\n"
        "v_rcp_f32 v66, v66\n"
        "v_add_f32 v68, 1.0, v68\n"
        "v_mfma_f32_32x32x16_bf16 v[48:63], a[204:207], v[20:23], v[48:63]\n"
        "ds_read_b128 v[20:23], v0 offset:5376\n"
        "s_waitcnt lgkmcnt(4)\n"
        "v_rcp_f32 v67, v67\n"
        "v_add_f32 v69, 1.0, v69\n"
        "v_mfma_f32_32x32x16_bf16 v[48:63], a[208:211], v[24:27], v[48:63]\n"
        "ds_read_b128 v[24:27], v0 offset:5632\n"
        "buffer_load_dwordx4 v1, s[40:43], s71 offen offset:3072 sc1 lds\n"
        "s_add_u32 m0, m0, 4096\n"
        "s_add_u32 s71, s71, s65\n"
        "s_add_u32 s71, s71, 61440\n"
        "v_mfma_f32_32x32x16_bf16 v[48:63], a[212:215], v[28:31], v[48:63]\n"
        "ds_read_b128 v[28:31], v0 offset:5888\n"
        "s_waitcnt lgkmcnt(4)\n"
        "v_rcp_f32 v68, v68\n"
        "v_add_f32 v70, 1.0, v70\n"
        "v_mfma_f32_32x32x16_bf16 v[48:63], a[216:219], v[8:11], v[48:63]\n"
        "ds_read_b128 v[8:11], v0 offset:6144\n"
        "buffer_load_dwordx4 v1, s[40:43], s71 offen offset:0 sc1 lds\n"
        "s_add_u32 s69, s69, 1\n"
        "s_add_u32 s77, s77, 37376\n"
        "s_cmp_eq_u32 s77, 112128\n"
        "s_cselect_b32 s77, 0, s77\n"
        "v_mfma_f32_32x32x16_bf16 v[48:63], a[220:223], v[12:15], v[48:63]\n"
        "ds_read_b128 v[12:15], v0 offset:6400\n"
        "s_waitcnt lgkmcnt(4)\n"
        "s_mov_b32 s70, 2147483647\n"
        "v_rcp_f32 v69, v69\n"
        "v_add_f32 v71, 1.0, v71\n"
        "v_mfma_f32_32x32x16_bf16 v[48:63], a[224:227], v[16:19], v[48:63]\n"
        "ds_read_b128 v[16:19], v0 offset:6656\n"
        "v_rcp_f32 v70, v70\n"
        "v_rcp_f32 v71, v71\n"
        "v_mul_f32 v32, v32, v64\n"
        "v_mfma_f32_32x32x16_bf16 v[48:63], a[228:231], v[20:23], v[48:63]\n"
        "ds_read_b128 v[20:23], v0 offset:6912\n"
        "s_waitcnt lgkmcnt(4)\n"
        "v_mul_f32 v33, v33, v65\n"
        "v_mul_f32 v34, v34, v66\n"
        "v_mul_f32 v35, v35, v67\n"
        "v_mul_f32 v40, v40, v68\n"
        "v_mfma_f32_32x32x16_bf16 v[48:63], a[232:235], v[24:27], v[48:63]\n"
        "ds_read_b128 v[24:27], v0 offset:7168\n"
        "v_mul_f32 v41, v41, v69\n"
        "v_mul_f32 v42, v42, v70\n"
        "v_mul_f32 v43, v43, v71\n"
        "v_cvt_pk_bf16_f32 v72, v32, v33\n"
        "v_cvt_pk_bf16_f32 v73, v34, v35\n"
        "v_mfma_f32_32x32x16_bf16 v[48:63], a[236:239], v[28:31], v[48:63]\n"
        "ds_read_b128 v[28:31], v0 offset:7424\n"
        "s_waitcnt lgkmcnt(4)\n"
        "v_cvt_pk_bf16_f32 v74, v40, v41\n"
        "v_cvt_pk_bf16_f32 v75, v42, v43\n"
        "v_mfma_f32_32x32x16_bf16 v[48:63], a[240:243], v[8:11], v[48:63]\n"
        "ds_read_b128 v[8:11], v0 offset:7680\n"
        "buffer_store_dwordx4 v[72:75], v1, s[40:43], s72 offen sc1\n"
        "v_mfma_f32_32x32x16_bf16 v[48:63], a[244:247], v[12:15], v[48:63]\n"
        "ds_read_b128 v[12:15], v0 offset:7936\n"
        "s_waitcnt lgkmcnt(4)\n"
        "v_mfma_f32_32x32x16_bf16 v[48:63], a[248:251], v[16:19], v[48:63]\n"
        "ds_read_b128 v[16:19], v0 offset:8192\n"
        "v_mfma_f32_32x32x16_bf16 v[48:63], a[252:255], v[20:23], v[48:63]\n"
        "ds_read_b128 v[20:23], v0 offset:8448\n"
        "s_waitcnt lgkmcnt(4)\n"
        "v_mfma_f32_32x32x16_bf16 v[48:63], v[80:83], v[24:27], v[48:63]\n"
        "ds_read_b128 v[24:27], v0 offset:8704\n"
        "v_mfma_f32_32x32x16_bf16 v[48:63], v[84:87], v[28:31], v[48:63]\n"
        "ds_read_b128 v[28:31], v0 offset:8960\n"
        "s_waitcnt lgkmcnt(4)\n"
        "v_mfma_f32_32x32x16_bf16 v[48:63], v[88:91], v[8:11], v[48:63]\n"
        "ds_read_b128 v[8:11], v0 offset:0\n"
        "v_mfma_f32_32x32x16_bf16 v[48:63], v[92:95], v[12:15], v[48:63]\n"
        "ds_read_b128 v[12:15], v0 offset:256\n"
        "s_waitcnt lgkmcnt(4)\n"
        "v_mfma_f32_32x32x16_bf16 v[48:63], v[96:99], v[16:19], v[48:63]\n"
        "ds_read_b128 v[16:19], v0 offset:512\n"
        "s_waitcnt vmcnt(0)\n"
        "v_mov_b32 v5, s74\n"
        "buffer_store_dword v5, v3, s[40:43], s73 offen sc1\n"
        "s_add_u32 s74, s74, 1\n"
        "s_add_u32 s72, s75, 49152\n"
        "v_mfma_f32_32x32x16_bf16 v[48:63], v[100:103], v[20:23], v[48:63]\n"
        "ds_read_b128 v[20:23], v0 offset:768\n"
        "s_waitcnt lgkmcnt(4)\n"
        "v_mfma_f32_32x32x16_bf16 v[48:63], v[104:107], v[24:27], v[48:63]\n"
        "ds_read_b128 v[24:27], v0 offset:1024\n"
        "v_mfma_f32_32x32x16_bf16 v[48:63], v[108:111], v[28:31], v[48:63]\n"
        "ds_read_b128 v[28:31], v0 offset:1280\n"
        "s_waitcnt lgkmcnt(4)\n"
        "s_branch .Lf1_p20_%=\n"
        ".Lf1_p21_%=:\n"
        "v_mfma_f32_32x32x16_bf16 v[64:79], v[112:115], v[8:11], 0\n"
        "ds_read_b128 v[8:11], v0 offset:1536\n"
        "s_add_u32 m0, s77, s61\n"
        "s_lshr_b32 s65, s69, 1\n"
        "s_bitcmp1_b32 s54, s65\n"
        "s_cselect_b32 s65, 8192, 0\n"
        "s_lshl_b32 s71, s69, 20\n"
        "v_mfma_f32_32x32x16_bf16 v[64:79], v[116:119], v[12:15], v[64:79]\n"
        "ds_read_b128 v[12:15], v0 offset:1792\n"
        "s_waitcnt lgkmcnt(4)\n"
        "s_add_u32 s71, s71, s65\n"
        "s_add_u32 s71, s71, s58\n"
        "v_mul_f32 v32, s80, v48\n"
        "v_mul_f32 v33, s80, v49\n"
        "v_mfma_f32_32x32x16_bf16 v[64:79], v[120:123], v[16:19], v[64:79]\n"
        "ds_read_b128 v[16:19], v0 offset:2048\n"
        "buffer_load_dwordx4 v1, s[40:43], s71 offen offset:0 sc1 lds\n"
        "v_mul_f32 v34, s80, v50\n"
        "v_mul_f32 v35, s80, v51\n"
        "v_mul_f32 v36, s80, v56\n"
        "v_mul_f32 v37, s80, v57\n"
        "v_mfma_f32_32x32x16_bf16 v[64:79], v[124:127], v[20:23], v[64:79]\n"
        "ds_read_b128 v[20:23], v0 offset:2304\n"
        "s_waitcnt lgkmcnt(4)\n"
        "v_mul_f32 v38, s80, v58\n"
        "v_mul_f32 v39, s80, v59\n"
        "v_exp_f32 v32, v32\n"
        "v_mfma_f32_32x32x16_bf16 v[64:79], v[128:131], v[24:27], v[64:79]\n"
        "ds_read_b128 v[24:27], v0 offset:2560\n"
        "buffer_load_dwordx4 v1, s[40:43], s71 offen offset:1024 sc1 lds\n"
        "v_mul_f32 v48, v48, v52\n"
        "v_exp_f32 v33, v33\n"
        "v_mul_f32 v49, v49, v53\n"
        "v_mfma_f32_32x32x16_bf16 v[64:79], v[132:135], v[28:31], v[64:79]\n"
        "ds_read_b128 v[28:31], v0 offset:2816\n"
        "s_waitcnt lgkmcnt(4)\n"
        "v_exp_f32 v34, v34\n"
        "v_mul_f32 v50, v50, v54\n"
        "v_mfma_f32_32x32x16_bf16 v[64:79], v[136:139], v[8:11], v[64:79]\n"
        "ds_read_b128 v[8:11], v0 offset:3072\n"
        "buffer_load_dwordx4 v1, s[40:43], s71 offen offset:2048 sc1 lds\n"
        "v_exp_f32 v35, v35\n"
        "v_mul_f32 v51, v51, v55\n"
        "v_mfma_f32_32x32x16_bf16 v[64:79], v[140:143], v[12:15], v[64:79]\n"
        "ds_read_b128 v[12:15], v0 offset:3328\n"
        "s_waitcnt lgkmcnt(4)\n"
        "v_exp_f32 v36, v36\n"
        "v_mul_f32 v56, v56, v60\n"
        "v_mfma_f32_32x32x16_bf16 v[64:79], v[144:147], v[16:19], v[64:79]\n"
        "ds_read_b128 v[16:19], v0 offset:3584\n"
        "buffer_load_dwordx4 v1, s[40:43], s71 offen offset:3072 sc1 lds\n"
        "s_add_u32 m0, m0, 4096\n"
        "s_add_u32 s71, s71, 4096\n"
        "s_mul_i32 s65, s48, -15360\n"
        "v_mfma_f32_32x32x16_bf16 v[64:79], v[148:151], v[20:23], v[64:79]\n"
        "ds_read_b128 v[20:23], v0 offset:3840\n"
        "s_waitcnt lgkmcnt(4)\n"
        "v_exp_f32 v37, v37\n"
        "v_mul_f32 v57, v57, v61\n"
        "v_mfma_f32_32x32x16_bf16 v[64:79], v[152:155], v[24:27], v[64:79]\n"
        "ds_read_b128 v[24:27], v0 offset:4096\n"
        "buffer_load_dwordx4 v1, s[40:43], s71 offen offset:0 sc1 lds\n"
        "v_exp_f32 v38, v38\n"
        "v_mul_f32 v58, v58, v62\n"
        "v_mfma_f32_32x32x16_bf16 v[64:79], v[156:159], v[28:31], v[64:79]\n"
        "ds_read_b128 v[28:31], v0 offset:4352\n"
        "s_waitcnt lgkmcnt(4)\n"
        "v_exp_f32 v39, v39\n"
        "v_mul_f32 v59, v59, v63\n"
        "v_add_f32 v32, 1.0, v32\n"
        "v_mfma_f32_32x32x16_bf16 v[64:79], v[160:163], v[8:11], v[64:79]\n"
        "ds_read_b128 v[8:11], v0 offset:4608\n"
        "buffer_load_dwordx4 v1, s[40:43], s71 offen offset:1024 sc1 lds\n"
        "v_add_f32 v33, 1.0, v33\n"
        "v_rcp_f32 v32, v32\n"
        "v_add_f32 v34, 1.0, v34\n"
        "v_mfma_f32_32x32x16_bf16 v[64:79], v[164:167], v[12:15], v[64:79]\n"
        "ds_read_b128 v[12:15], v0 offset:4864\n"
        "s_waitcnt lgkmcnt(4)\n"
        "v_rcp_f32 v33, v33\n"
        "v_add_f32 v35, 1.0, v35\n"
        "v_mfma_f32_32x32x16_bf16 v[64:79], v[168:171], v[16:19], v[64:79]\n"
        "ds_read_b128 v[16:19], v0 offset:5120\n"
        "buffer_load_dwordx4 v1, s[40:43], s71 offen offset:2048 sc1 lds\n"
        "v_rcp_f32 v34, v34\n"
        "v_add_f32 v36, 1.0, v36\n"
        "v_mfma_f32_32x32x16_bf16 v[64:79], v[172:175], v[20:23], v[64:79]\n"
        "ds_read_b128 v[20:23], v0 offset:5376\n"
        "s_waitcnt lgkmcnt(4)\n"
        "v_rcp_f32 v35, v35\n"
        "v_add_f32 v37, 1.0, v37\n"
        "v_mfma_f32_32x32x16_bf16 v[64:79], v[176:179], v[24:27], v[64:79]\n"
        "ds_read_b128 v[24:27], v0 offset:5632\n"
        "buffer_load_dwordx4 v1, s[40:43], s71 offen offset:3072 sc1 lds\n"
        "s_add_u32 m0, m0, 4096\n"
        "s_add_u32 s71, s71, s65\n"
        "s_add_u32 s71, s71, 61440\n"
        "buffer_load_dword v4, v2, s[40:43], s76 offen sc1\n"
        "v_mfma_f32_32x32x16_bf16 v[64:79], v[180:183], v[28:31], v[64:79]\n"
        "ds_read_b128 v[28:31], v0 offset:5888\n"
        "s_waitcnt lgkmcnt(4)\n"
        "v_rcp_f32 v36, v36\n"
        "v_add_f32 v38, 1.0, v38\n"
        "v_mfma_f32_32x32x16_bf16 v[64:79], v[184:187], v[8:11], v[64:79]\n"
        "ds_read_b128 v[8:11], v0 offset:6144\n"
        "buffer_load_dwordx4 v1, s[40:43], s71 offen offset:0 sc1 lds\n"
        "s_add_u32 s69, s69, 1\n"
        "s_add_u32 s77, s77, 37376\n"
        "s_cmp_eq_u32 s77, 112128\n"
        "s_cselect_b32 s77, 0, s77\n"
        "v_mfma_f32_32x32x16_bf16 v[64:79], v[188:191], v[12:15], v[64:79]\n"
        "ds_read_b128 v[12:15], v0 offset:6400\n"
        "s_waitcnt lgkmcnt(4)\n"
        "s_mov_b32 s70, 2147483647\n"
        "v_rcp_f32 v37, v37\n"
        "v_add_f32 v39, 1.0, v39\n"
        "v_mfma_f32_32x32x16_bf16 v[64:79], v[192:195], v[16:19], v[64:79]\n"
        "ds_read_b128 v[16:19], v0 offset:6656\n"
        "v_rcp_f32 v38, v38\n"
        "v_rcp_f32 v39, v39\n"
        "v_mul_f32 v48, v48, v32\n"
        "v_mfma_f32_32x32x16_bf16 v[64:79], v[196:199], v[20:23], v[64:79]\n"
        "ds_read_b128 v[20:23], v0 offset:6912\n"
        "s_waitcnt lgkmcnt(4)\n"
        "v_mul_f32 v49, v49, v33\n"
        "v_mul_f32 v50, v50, v34\n"
        "v_mul_f32 v51, v51, v35\n"
        "v_mul_f32 v56, v56, v36\n"
        "v_mfma_f32_32x32x16_bf16 v[64:79], v[200:203], v[24:27], v[64:79]\n"
        "ds_read_b128 v[24:27], v0 offset:7168\n"
        "v_mul_f32 v57, v57, v37\n"
        "v_mul_f32 v58, v58, v38\n"
        "v_mul_f32 v59, v59, v39\n"
        "v_cvt_pk_bf16_f32 v40, v48, v49\n"
        "v_cvt_pk_bf16_f32 v41, v50, v51\n"
        "v_mfma_f32_32x32x16_bf16 v[64:79], v[204:207], v[28:31], v[64:79]\n"
        "ds_read_b128 v[28:31], v0 offset:7424\n"
        "s_waitcnt lgkmcnt(4)\n"
        "v_cvt_pk_bf16_f32 v42, v56, v57\n"
        "v_cvt_pk_bf16_f32 v43, v58, v59\n"
        "s_add_u32 s78, s78, 37376\n"
        "v_mfma_f32_32x32x16_bf16 v[64:79], v[208:211], v[8:11], v[64:79]\n"
        "ds_read_b128 v[8:11], v0 offset:7680\n"
        "buffer_store_dwordx4 v[40:43], v1, s[40:43], s72 offen sc1\n"
        "v_mfma_f32_32x32x16_bf16 v[64:79], v[212:215], v[12:15], v[64:79]\n"
        "ds_read_b128 v[12:15], v0 offset:7936\n"
        "s_waitcnt lgkmcnt(4)\n"
        "s_cmp_eq_u32 s78, 112128\n"
        "s_cselect_b32 s78, 0, s78\n"
        "v_mfma_f32_32x32x16_bf16 v[64:79], v[216:219], v[16:19], v[64:79]\n"
        "ds_read_b128 v[16:19], v0 offset:8192\n"
        "v_mfma_f32_32x32x16_bf16 v[64:79], v[220:223], v[20:23], v[64:79]\n"
        "ds_read_b128 v[20:23], v0 offset:8448\n"
        "s_waitcnt lgkmcnt(4)\n"
        "v_mfma_f32_32x32x16_bf16 v[64:79], v[224:227], v[24:27], v[64:79]\n"
        "ds_read_b128 v[24:27], v0 offset:8704\n"
        "v_mfma_f32_32x32x16_bf16 v[64:79], v[228:231], v[28:31], v[64:79]\n"
        "ds_read_b128 v[28:31], v0 offset:8960\n"
        "s_waitcnt lgkmcnt(4)\n"
        "s_waitcnt vmcnt(7)\n"
        "s_barrier\n"
        "v_add_u32 v0, s78, v6\n"
        "v_mfma_f32_32x32x16_bf16 v[64:79], v[232:235], v[8:11], v[64:79]\n"
        "ds_read_b128 v[8:11], v0 offset:0\n"
        "s_cmp_lt_u32 s69, 64\n"
        "s_cselect_b32 s70, s69, 2147483647\n"
        "v_mfma_f32_32x32x16_bf16 v[64:79], v[236:239], v[12:15], v[64:79]\n"
        "ds_read_b128 v[12:15], v0 offset:256\n"
        "s_waitcnt lgkmcnt(4)\n"
        "v_mfma_f32_32x32x16_bf16 v[64:79], v[240:243], v[16:19], v[64:79]\n"
        "ds_read_b128 v[16:19], v0 offset:512\n"
        "s_waitcnt vmcnt(0)\n"
        "v_mov_b32 v5, s74\n"
        "buffer_store_dword v5, v3, s[40:43], s73 offen sc1\n"
        "s_add_u32 s74, s74, 1\n"
        "s_add_u32 s72, s75, 98304\n"
        "v_mfma_f32_32x32x16_bf16 v[64:79], v[244:247], v[20:23], v[64:79]\n"
        "ds_read_b128 v[20:23], v0 offset:768\n"
        "s_waitcnt lgkmcnt(4)\n"
        "v_mfma_f32_32x32x16_bf16 v[64:79], v[248:251], v[24:27], v[64:79]\n"
        "ds_read_b128 v[24:27], v0 offset:1024\n"
        "v_cmp_gt_i32 vcc, s70, v4\n"
        "v_mfma_f32_32x32x16_bf16 v[64:79], v[252:255], v[28:31], v[64:79]\n"
        "ds_read_b128 v[28:31], v0 offset:1280\n"
        "s_waitcnt lgkmcnt(4)\n"
        "s_and_b64 vcc, vcc, 1\n"
        "s_cbranch_vccz .Lf1_p01_%=\n"
        "s_branch .Lf1_p00_%=\n"
        ".Lf1_tail_%=:\n"
        "v_mfma_f32_32x32x16_bf16 v[64:79], v[232:235], v[8:11], v[64:79]\n"
        "v_mfma_f32_32x32x16_bf16 v[64:79], v[236:239], v[12:15], v[64:79]\n"
        "s_waitcnt lgkmcnt(2)\n"
        "v_mfma_f32_32x32x16_bf16 v[64:79], v[240:243], v[16:19], v[64:79]\n"
        "s_waitcnt vmcnt(1)\n"
        "v_mov_b32 v5, s74\n"
        "buffer_store_dword v5, v3, s[40:43], s73 offen sc1\n"
        "s_add_u32 s74, s74, 1\n"
        "s_add_u32 s72, s75, 98304\n"
        "v_mfma_f32_32x32x16_bf16 v[64:79], v[244:247], v[20:23], v[64:79]\n"
        "s_waitcnt lgkmcnt(0)\n"
        "v_mfma_f32_32x32x16_bf16 v[64:79], v[248:251], v[24:27], v[64:79]\n"
        "v_mfma_f32_32x32x16_bf16 v[64:79], v[252:255], v[28:31], v[64:79]\n"
        "s_waitcnt vmcnt(1)\n"
        "buffer_load_dword v4, v2, s[40:43], s76 offen sc1\n"
        "s_nop 10\n"
        "v_mul_f32 v48, s80, v64\n"
        "v_mul_f32 v49, s80, v65\n"
        "v_mul_f32 v50, s80, v66\n"
        "v_mul_f32 v51, s80, v67\n"
        "v_mul_f32 v52, s80, v72\n"
        "v_mul_f32 v53, s80, v73\n"
        "v_mul_f32 v54, s80, v74\n"
        "v_mul_f32 v55, s80, v75\n"
        "v_exp_f32 v48, v48\n"
        "v_mul_f32 v64, v64, v68\n"
        "v_exp_f32 v49, v49\n"
        "v_mul_f32 v65, v65, v69\n"
        "v_exp_f32 v50, v50\n"
        "v_mul_f32 v66, v66, v70\n"
        "v_exp_f32 v51, v51\n"
        "v_mul_f32 v67, v67, v71\n"
        "v_exp_f32 v52, v52\n"
        "v_mul_f32 v72, v72, v76\n"
        "v_exp_f32 v53, v53\n"
        "v_mul_f32 v73, v73, v77\n"
        "v_exp_f32 v54, v54\n"
        "v_mul_f32 v74, v74, v78\n"
        "v_exp_f32 v55, v55\n"
        "v_mul_f32 v75, v75, v79\n"
        "v_add_f32 v48, 1.0, v48\n"
        "v_add_f32 v49, 1.0, v49\n"
        "v_rcp_f32 v48, v48\n"
        "v_add_f32 v50, 1.0, v50\n"
        "v_rcp_f32 v49, v49\n"
        "v_add_f32 v51, 1.0, v51\n"
        "v_rcp_f32 v50, v50\n"
        "v_add_f32 v52, 1.0, v52\n"
        "v_rcp_f32 v51, v51\n"
        "v_add_f32 v53, 1.0, v53\n"
        "v_rcp_f32 v52, v52\n"
        "v_add_f32 v54, 1.0, v54\n"
        "v_rcp_f32 v53, v53\n"
        "v_add_f32 v55, 1.0, v55\n"
        "v_rcp_f32 v54, v54\n"
        "v_rcp_f32 v55, v55\n"
        "v_mul_f32 v64, v64, v48\n"
        "v_mul_f32 v65, v65, v49\n"
        "v_mul_f32 v66, v66, v50\n"
        "v_mul_f32 v67, v67, v51\n"
        "v_mul_f32 v72, v72, v52\n"
        "v_mul_f32 v73, v73, v53\n"
        "v_mul_f32 v74, v74, v54\n"
        "v_mul_f32 v75, v75, v55\n"
        "v_cvt_pk_bf16_f32 v56, v64, v65\n"
        "v_cvt_pk_bf16_f32 v57, v66, v67\n"
        "v_cvt_pk_bf16_f32 v58, v72, v73\n"
        "v_cvt_pk_bf16_f32 v59, v74, v75\n"
        "buffer_store_dwordx4 v[56:59], v1, s[40:43], s72 offen sc1\n"
        "s_waitcnt vmcnt(1)\n"
        "v_cmp_gt_i32 vcc, s70, v4\n"
        "s_nop 0\n"
        "s_and_b64 vcc, vcc, 1\n"
        "s_cbranch_vccnz .Lf1_late_%=\n"
        "s_add_u32 m0, s77, s61\n"
        "s_lshr_b32 s65, s69, 1\n"
        "s_bitcmp1_b32 s54, s65\n"
        "s_cselect_b32 s65, 8192, 0\n"
        "s_lshl_b32 s71, s69, 20\n"
        "s_add_u32 s71, s71, s65\n"
        "s_add_u32 s71, s71, s58\n"
        "buffer_load_dwordx4 v1, s[40:43], s71 offen offset:0 sc1 lds\n"
        "buffer_load_dwordx4 v1, s[40:43], s71 offen offset:1024 sc1 lds\n"
        "buffer_load_dwordx4 v1, s[40:43], s71 offen offset:2048 sc1 lds\n"
        "buffer_load_dwordx4 v1, s[40:43], s71 offen offset:3072 sc1 lds\n"
        "s_add_u32 m0, m0, 4096\n"
        "s_add_u32 s71, s71, 4096\n"
        "s_mul_i32 s65, s48, -15360\n"
        "buffer_load_dwordx4 v1, s[40:43], s71 offen offset:0 sc1 lds\n"
        "buffer_load_dwordx4 v1, s[40:43], s71 offen offset:1024 sc1 lds\n"
        "buffer_load_dwordx4 v1, s[40:43], s71 offen offset:2048 sc1 lds\n"
        "buffer_load_dwordx4 v1, s[40:43], s71 offen offset:3072 sc1 lds\n"
        "s_add_u32 m0, m0, 4096\n"
        "s_add_u32 s71, s71, s65\n"
        "s_add_u32 s71, s71, 61440\n"
        "buffer_load_dwordx4 v1, s[40:43], s71 offen offset:0 sc1 lds\n"
        "s_add_u32 s69, s69, 1\n"
        "s_add_u32 s77, s77, 37376\n"
        "s_cmp_eq_u32 s77, 112128\n"
        "s_cselect_b32 s77, 0, s77\n"
        "s_mov_b32 s70, 2147483647\n"
        "s_waitcnt vmcnt(9)\n"
        "v_mov_b32 v5, s74\n"
        "buffer_store_dword v5, v3, s[40:43], s73 offen sc1\n"
        "s_waitcnt vmcnt(6)\n"
        "s_branch .Lf1_go_%=\n"
        ".Lf1_late_%=:\n"
        "s_waitcnt vmcnt(0)\n"
        "v_mov_b32 v5, s74\n"
        "buffer_store_dword v5, v3, s[40:43], s73 offen sc1\n"
        "s_cmp_eq_u32 s68, 63\n"
        "s_cbranch_scc0 .Lf1_wait_%=\n"
        "s_endpgm\n"
        ".Lf1_wait_%=:\n"
        "s_mov_b32 s64, 0\n"
        ".Lf1_in_top_%=:\n"
        "buffer_load_dword v4, v2, s[40:43], s76 offen sc1\n"
        "s_waitcnt vmcnt(0)\n"
        "v_cmp_gt_i32 vcc, s69, v4\n"
        "s_nop 0\n"
        "s_and_b64 vcc, vcc, 1\n"
        "s_cbranch_vccz .Lf1_in_ok_%=\n"
        "s_add_u32 s64, s64, 1\n"
        "s_and_b32 s65, s64, 255\n"
        "s_cbranch_scc1 .Lf1_in_go_%=\n"
        "s_mov_b32 s65, 40960\n"
        "s_cmp_lt_u32 s64, 1048576\n"
        "s_cbranch_scc1 .Lf1_in_chk_%=\n"
        "v_mov_b32 v4, 1\n"
        "buffer_store_dword v4, off, s[40:43], s65 sc1\n"
        "s_waitcnt vmcnt(0)\n"
        ".Lf1_in_chk_%=:\n"
        "buffer_load_dword v4, off, s[40:43], s65 sc1\n"
        "s_waitcnt vmcnt(0)\n"
        "v_readfirstlane_b32 s65, v4\n"
        "s_nop 0\n"
        "s_cmp_lg_u32 s65, 1\n"
        "s_cbranch_scc1 .Lf1_in_go_%=\n"
        "s_endpgm\n"
        ".Lf1_in_go_%=:\n"
        "s_sleep 1\n"
        "s_branch .Lf1_in_top_%=\n"
        ".Lf1_in_ok_%=:\n"
        "s_add_u32 m0, s77, s61\n"
        "s_lshr_b32 s65, s69, 1\n"
        "s_bitcmp1_b32 s54, s65\n"
        "s_cselect_b32 s65, 8192, 0\n"
        "s_lshl_b32 s71, s69, 20\n"
        "s_add_u32 s71, s71, s65\n"
        "s_add_u32 s71, s71, s58\n"
        "buffer_load_dwordx4 v1, s[40:43], s71 offen offset:0 sc1 lds\n"
        "buffer_load_dwordx4 v1, s[40:43], s71 offen offset:1024 sc1 lds\n"
        "buffer_load_dwordx4 v1, s[40:43], s71 offen offset:2048 sc1 lds\n"
        "buffer_load_dwordx4 v1, s[40:43], s71 offen offset:3072 sc1 lds\n"
        "s_add_u32 m0, m0, 4096\n"
        "s_add_u32 s71, s71, 4096\n"
        "s_mul_i32 s65, s48, -15360\n"
        "buffer_load_dwordx4 v1, s[40:43], s71 offen offset:0 sc1 lds\n"
        "buffer_load_dwordx4 v1, s[40:43], s71 offen offset:1024 sc1 lds\n"
        "buffer_load_dwordx4 v1, s[40:43], s71 offen offset:2048 sc1 lds\n"
        "buffer_load_dwordx4 v1, s[40:43], s71 offen offset:3072 sc1 lds\n"
        "s_add_u32 m0, m0, 4096\n"
        "s_add_u32 s71, s71, s65\n"
        "s_add_u32 s71, s71, 61440\n"
        "buffer_load_dwordx4 v1, s[40:43], s71 offen offset:0 sc1 lds\n"
        "s_add_u32 s69, s69, 1\n"
        "s_add_u32 s77, s77, 37376\n"
        "s_cmp_eq_u32 s77, 112128\n"
        "s_cselect_b32 s77, 0, s77\n"
        "s_mov_b32 s70, 2147483647\n"
        "s_waitcnt vmcnt(5)\n"
        ".Lf1_go_%=:\n"
        "s_mov_b32 s72, 241172480\n"
        "s_mov_b32 s73, 241172480\n"
        "s_cmp_lt_u32 s69, 64\n"
        "s_cselect_b32 s70, s69, 2147483647\n"
        "v_add_u32 v0, s78, v6\n"
        "s_barrier\n"
        "ds_read_b128 v[8:11], v0 offset:0\n"
        "ds_read_b128 v[12:15], v0 offset:256\n"
        "ds_read_b128 v[16:19], v0 offset:512\n"
        "ds_read_b128 v[20:23], v0 offset:768\n"
        "ds_read_b128 v[24:27], v0 offset:1024\n"
        "ds_read_b128 v[28:31], v0 offset:1280\n"
        "v_cmp_gt_i32 vcc, s70, v4\n"
        "s_nop 0\n"
        "s_and_b64 vcc, vcc, 1\n"
        "s_waitcnt lgkmcnt(4)\n"
        "s_cbranch_vccz .Lf1_p01_%=\n"
        "s_branch .Lf1_p00_%=\n"
        :: "s"(p.ws), "s"(p.w1), "s"(p.w3), "s"(p.w3), "s"(pair(w, delay)), "s"(pair(x4, pr)),
           "s"(pair(65 * PAGE + spread(B_XN1) + w * 16384, 65 * PAGE + spread(kap * 73728 + u * 1024))), "s"(pair(flag(F_F1 + 24 * kap + u), w * 9344)),
           "v"(pair(((lane & 31) >> 3) * 9344 + (lane >> 5) * 128 + (lane & 7) * 16, lane * 16)), "v"(pair(lane == 0 ? flag(F_R3 + 24 * kap + u) : OOB, lane < 3 ? lane * 32768 : OOB)), "v"(pair(0, 0)),
           "v"(pair(vo[0], vo[1])), "v"(pair(vo[2], tr))
        : "v0", "v1", "v2", "v3", "v4", "v5", "v6", "v7", "v8", "v9", "v10", "v11", "v12", "v13", "v14", "v15", "v16", "v17", "v18", "v19", "v20", "v21", "v22", "v23", "v24", "v25", "v26", "v27", "v28", "v29", "v30", "v31", "v32", "v33", "v34", "v35", "v36", "v37", "v38", "v39", "v40", "v41", "v42", "v43", "v44", "v45", "v46", "v47", "v48", "v49", "v50", "v51", "v52", "v53", "v54", "v55", "v56", "v57", "v58", "v59", "v60", "v61", "v62", "v63", "v64", "v65", "v66", "v67", "v68", "v69", "v70", "v71", "v72", "v73", "v74", "v75", "v76", "v77", "v78", "v79", "v255", "a255", "s34", "s35", "s36", "s37", "s38", "s39", "s40", "s41", "s42", "s43", "s44", "s45", "s46", "s47", "s48", "s49", "s50", "s51", "s52", "s53", "s54", "s55", "s56", "s57", "s58", "s59", "s60", "s61", "s62", "s63", "s64", "s65", "s66", "s67", "s68", "s69", "s70", "s71", "s72", "s73", "s74", "s75", "s76", "s77", "s78", "s79", "s80", "s81", "s82", "s83", "s84", "s85", "s86", "s87", "s88", "s89", "s90", "s91", "s92", "s93", "s94", "s95", "s96", "s97", "s98", "s99", "vcc", "scc", "memory");
    __builtin_unreachable();
}
__device__ __forceinline__ unsigned part_off(unsigned a, unsigned kap, unsigned chunk) {
    const unsigned b = (a & 1) * 4 + kap;
    return (a >> 1) * (9 * 16384) + (chunk < 64 ? b * 16384 + chunk * 128 : 8 * 16384 + b * 1024 + (chunk - 64) * 128);
}
template <bool OP>
__device__ __forceinline__ void role_k(const Params& p, int kap, int nu, int x4, int pr, int delay) {
    GEMM_HEAD;
    unsigned vo[3];
#pragma unroll
    for (int n = 0; n < 3; ++n) {
        const unsigned ps = (64 * n + lane) / 6, gg = (ps >> 2) & 3;
        const unsigned row = OP ? 1024 * kap + ps : 1152 * kap + 48 * (gg >> 1) + 8 * (gg & 1) + 4 * (ps >> 4) + (ps & 3);
        vo[n] = row * 1152 + (192 * nu + 48 * w) * 2 + ((64 * n + lane) % 6) * 16 - 1024 * n;
    }
    const unsigned tr = w * 40960 + (4 * g + q) * 96;
    const unsigned c0 = 24 * nu + 6 * w;
    const unsigned s01 = part_off(i >> 3, kap, c0 + g) + (i & 7) * 16, s2 = part_off(2 * (g & 1) + (i >> 3), kap, c0 + 4 + (g >> 1)) + (i & 7) * 16;
    const unsigned fl = nu * 16 + kap * 4 + w, fo4 = lane < 4 ? lane * 16384 : OOB;
    if constexpr (OP) {
        asm volatile(
        "s_nop 4\n"
        "s_mov_b64 s[40:41], %0\n"
        "s_and_b32 s41, s41, 0xffff\n"
        "s_mov_b32 s42, 236978176\n"
        "s_mov_b32 s43, 0x20000\n"
        "s_mov_b64 s[48:49], %4\n"
        "s_mov_b64 s[56:57], %5\n"
        "s_mov_b64 s[58:59], %6\n"
        "s_mov_b64 s[60:61], %7\n"
        "v_mov_b64 v[0:1], %8\n"
        "v_mov_b64 v[2:3], %9\n"
        "v_mov_b64 v[4:5], %10\n"
        "v_mov_b64 v[6:7], %11\n"
        "v_mov_b64 v[8:9], %12\n"
        "v_mov_b64 v[10:11], %13\n"
        "s_mov_b32 s47, 0x20000\n"
        "s_mul_i32 s79, s48, 40960\n"
        ".Lop_start_%=:\n"
        "s_sub_u32 s49, s49, 1\n"
        "s_cbranch_scc1 .Lop_start2_%=\n"
        "s_sleep 16\n"
        "s_branch .Lop_start_%=\n"
        ".Lop_start2_%=:\n"
        "s_mov_b64 s[44:45], %1\n"
        "s_and_b32 s45, s45, 0xffff\n"
        "s_mov_b32 s46, 4718592\n"
        "s_add_u32 m0, s79, 0\n"
        "s_mov_b32 s65, 0\n"
        "buffer_load_dwordx4 v8, s[44:47], s65 offen lds\n"
        "buffer_load_dwordx4 v9, s[44:47], s65 offen offset:1024 lds\n"
        "buffer_load_dwordx4 v10, s[44:47], s65 offen offset:2048 lds\n"
        "s_add_u32 m0, s79, 3072\n"
        "s_mov_b32 s65, 36864\n"
        "buffer_load_dwordx4 v8, s[44:47], s65 offen lds\n"
        "buffer_load_dwordx4 v9, s[44:47], s65 offen offset:1024 lds\n"
        "buffer_load_dwordx4 v10, s[44:47], s65 offen offset:2048 lds\n"
        "s_add_u32 m0, s79, 6144\n"
        "s_mov_b32 s65, 73728\n"
        "buffer_load_dwordx4 v8, s[44:47], s65 offen lds\n"
        "buffer_load_dwordx4 v9, s[44:47], s65 offen offset:1024 lds\n"
        "buffer_load_dwordx4 v10, s[44:47], s65 offen offset:2048 lds\n"
        "s_add_u32 m0, s79, 9216\n"
        "s_mov_b32 s65, 110592\n"
        "buffer_load_dwordx4 v8, s[44:47], s65 offen lds\n"
        "buffer_load_dwordx4 v9, s[44:47], s65 offen offset:1024 lds\n"
        "buffer_load_dwordx4 v10, s[44:47], s65 offen offset:2048 lds\n"
        "s_add_u32 m0, s79, 12288\n"
        "s_mov_b32 s65, 147456\n"
        "buffer_load_dwordx4 v8, s[44:47], s65 offen lds\n"
        "buffer_load_dwordx4 v9, s[44:47], s65 offen offset:1024 lds\n"
        "buffer_load_dwordx4 v10, s[44:47], s65 offen offset:2048 lds\n"
        "s_add_u32 m0, s79, 15360\n"
        "s_mov_b32 s65, 184320\n"
        "buffer_load_dwordx4 v8, s[44:47], s65 offen lds\n"
        "buffer_load_dwordx4 v9, s[44:47], s65 offen offset:1024 lds\n"
        "buffer_load_dwordx4 v10, s[44:47], s65 offen offset:2048 lds\n"
        "s_add_u32 m0, s79, 18432\n"
        "s_mov_b32 s65, 221184\n"
        "buffer_load_dwordx4 v8, s[44:47], s65 offen lds\n"
        "buffer_load_dwordx4 v9, s[44:47], s65 offen offset:1024 lds\n"
        "buffer_load_dwordx4 v10, s[44:47], s65 offen offset:2048 lds\n"
        "s_add_u32 m0, s79, 21504\n"
        "s_mov_b32 s65, 258048\n"
        "buffer_load_dwordx4 v8, s[44:47], s65 offen lds\n"
        "buffer_load_dwordx4 v9, s[44:47], s65 offen offset:1024 lds\n"
        "buffer_load_dwordx4 v10, s[44:47], s65 offen offset:2048 lds\n"
        "s_add_u32 m0, s79, 24576\n"
        "s_mov_b32 s65, 294912\n"
        "buffer_load_dwordx4 v8, s[44:47], s65 offen lds\n"
        "buffer_load_dwordx4 v9, s[44:47], s65 offen offset:1024 lds\n"
        "buffer_load_dwordx4 v10, s[44:47], s65 offen offset:2048 lds\n"
        "s_add_u32 m0, s79, 27648\n"
        "s_mov_b32 s65, 331776\n"
        "buffer_load_dwordx4 v8, s[44:47], s65 offen lds\n"
        "buffer_load_dwordx4 v9, s[44:47], s65 offen offset:1024 lds\n"
        "buffer_load_dwordx4 v10, s[44:47], s65 offen offset:2048 lds\n"
        "s_add_u32 m0, s79, 30720\n"
        "s_mov_b32 s65, 368640\n"
        "buffer_load_dwordx4 v8, s[44:47], s65 offen lds\n"
        "buffer_load_dwordx4 v9, s[44:47], s65 offen offset:1024 lds\n"
        "buffer_load_dwordx4 v10, s[44:47], s65 offen offset:2048 lds\n"
        "s_add_u32 m0, s79, 33792\n"
        "s_mov_b32 s65, 405504\n"
        "buffer_load_dwordx4 v8, s[44:47], s65 offen lds\n"
        "buffer_load_dwordx4 v9, s[44:47], s65 offen offset:1024 lds\n"
        "buffer_load_dwordx4 v10, s[44:47], s65 offen offset:2048 lds\n"
        "s_waitcnt vmcnt(33)\n"
        "ds_read_b64_tr_b16 a[0:1], v11 offset:0\n"
        "ds_read_b64_tr_b16 a[2:3], v11 offset:1536\n"
        "ds_read_b64_tr_b16 a[4:5], v11 offset:8\n"
        "ds_read_b64_tr_b16 a[6:7], v11 offset:1544\n"
        "ds_read_b64_tr_b16 a[8:9], v4 offset:0\n"
        "ds_read_b64_tr_b16 a[10:11], v4 offset:1536\n"
        "s_waitcnt vmcnt(30)\n"
        "ds_read_b64_tr_b16 a[12:13], v11 offset:3072\n"
        "ds_read_b64_tr_b16 a[14:15], v11 offset:4608\n"
        "ds_read_b64_tr_b16 a[16:17], v11 offset:3080\n"
        "ds_read_b64_tr_b16 a[18:19], v11 offset:4616\n"
        "ds_read_b64_tr_b16 a[20:21], v4 offset:3072\n"
        "ds_read_b64_tr_b16 a[22:23], v4 offset:4608\n"
        "s_waitcnt lgkmcnt(6)\n"
        "s_add_u32 m0, s79, 0\n"
        "s_mov_b32 s65, 442368\n"
        "buffer_load_dwordx4 v8, s[44:47], s65 offen lds\n"
        "buffer_load_dwordx4 v9, s[44:47], s65 offen offset:1024 lds\n"
        "buffer_load_dwordx4 v10, s[44:47], s65 offen offset:2048 lds\n"
        "s_waitcnt vmcnt(30)\n"
        "ds_read_b64_tr_b16 a[24:25], v11 offset:6144\n"
        "ds_read_b64_tr_b16 a[26:27], v11 offset:7680\n"
        "ds_read_b64_tr_b16 a[28:29], v11 offset:6152\n"
        "ds_read_b64_tr_b16 a[30:31], v11 offset:7688\n"
        "ds_read_b64_tr_b16 a[32:33], v4 offset:6144\n"
        "ds_read_b64_tr_b16 a[34:35], v4 offset:7680\n"
        "s_waitcnt lgkmcnt(6)\n"
        "s_add_u32 m0, s79, 3072\n"
        "s_mov_b32 s65, 479232\n"
        "buffer_load_dwordx4 v8, s[44:47], s65 offen lds\n"
        "buffer_load_dwordx4 v9, s[44:47], s65 offen offset:1024 lds\n"
        "buffer_load_dwordx4 v10, s[44:47], s65 offen offset:2048 lds\n"
        "s_waitcnt vmcnt(30)\n"
        "ds_read_b64_tr_b16 a[36:37], v11 offset:9216\n"
        "ds_read_b64_tr_b16 a[38:39], v11 offset:10752\n"
        "ds_read_b64_tr_b16 a[40:41], v11 offset:9224\n"
        "ds_read_b64_tr_b16 a[42:43], v11 offset:10760\n"
        "ds_read_b64_tr_b16 a[44:45], v4 offset:9216\n"
        "ds_read_b64_tr_b16 a[46:47], v4 offset:10752\n"
        "s_waitcnt lgkmcnt(6)\n"
        "s_add_u32 m0, s79, 6144\n"
        "s_mov_b32 s65, 516096\n"
        "buffer_load_dwordx4 v8, s[44:47], s65 offen lds\n"
        "buffer_load_dwordx4 v9, s[44:47], s65 offen offset:1024 lds\n"
        "buffer_load_dwordx4 v10, s[44:47], s65 offen offset:2048 lds\n"
        "s_waitcnt vmcnt(30)\n"
        "ds_read_b64_tr_b16 a[48:49], v11 offset:12288\n"
        "ds_read_b64_tr_b16 a[50:51], v11 offset:13824\n"
        "ds_read_b64_tr_b16 a[52:53], v11 offset:12296\n"
        "ds_read_b64_tr_b16 a[54:55], v11 offset:13832\n"
        "ds_read_b64_tr_b16 a[56:57], v4 offset:12288\n"
        "ds_read_b64_tr_b16 a[58:59], v4 offset:13824\n"
        "s_waitcnt lgkmcnt(6)\n"
        "s_add_u32 m0, s79, 9216\n"
        "s_mov_b32 s65, 552960\n"
        "buffer_load_dwordx4 v8, s[44:47], s65 offen lds\n"
        "buffer_load_dwordx4 v9, s[44:47], s65 offen offset:1024 lds\n"
        "buffer_load_dwordx4 v10, s[44:47], s65 offen offset:2048 lds\n"
        "s_waitcnt vmcnt(30)\n"
        "ds_read_b64_tr_b16 a[60:61], v11 offset:15360\n"
        "ds_read_b64_tr_b16 a[62:63], v11 offset:16896\n"
        "ds_read_b64_tr_b16 a[64:65], v11 offset:15368\n"
        "ds_read_b64_tr_b16 a[66:67], v11 offset:16904\n"
        "ds_read_b64_tr_b16 a[68:69], v4 offset:15360\n"
        "ds_read_b64_tr_b16 a[70:71], v4 offset:16896\n"
        "s_waitcnt lgkmcnt(6)\n"
        "s_add_u32 m0, s79, 12288\n"
        "s_mov_b32 s65, 589824\n"
        "buffer_load_dwordx4 v8, s[44:47], s65 offen lds\n"
        "buffer_load_dwordx4 v9, s[44:47], s65 offen offset:1024 lds\n"
        "buffer_load_dwordx4 v10, s[44:47], s65 offen offset:2048 lds\n"
        "s_waitcnt vmcnt(30)\n"
        "ds_read_b64_tr_b16 a[72:73], v11 offset:18432\n"
        "ds_read_b64_tr_b16 a[74:75], v11 offset:19968\n"
        "ds_read_b64_tr_b16 a[76:77], v11 offset:18440\n"
        "ds_read_b64_tr_b16 a[78:79], v11 offset:19976\n"
        "ds_read_b64_tr_b16 a[80:81], v4 offset:18432\n"
        "ds_read_b64_tr_b16 a[82:83], v4 offset:19968\n"
        "s_waitcnt lgkmcnt(6)\n"
        "s_add_u32 m0, s79, 15360\n"
        "s_mov_b32 s65, 626688\n"
        "buffer_load_dwordx4 v8, s[44:47], s65 offen lds\n"
        "buffer_load_dwordx4 v9, s[44:47], s65 offen offset:1024 lds\n"
        "buffer_load_dwordx4 v10, s[44:47], s65 offen offset:2048 lds\n"
        "s_waitcnt vmcnt(30)\n"
        "ds_read_b64_tr_b16 a[84:85], v11 offset:21504\n"
        "ds_read_b64_tr_b16 a[86:87], v11 offset:23040\n"
        "ds_read_b64_tr_b16 a[88:89], v11 offset:21512\n"
        "ds_read_b64_tr_b16 a[90:91], v11 offset:23048\n"
        "ds_read_b64_tr_b16 a[92:93], v4 offset:21504\n"
        "ds_read_b64_tr_b16 a[94:95], v4 offset:23040\n"
        "s_waitcnt lgkmcnt(6)\n"
        "s_add_u32 m0, s79, 18432\n"
        "s_mov_b32 s65, 663552\n"
        "buffer_load_dwordx4 v8, s[44:47], s65 offen lds\n"
        "buffer_load_dwordx4 v9, s[44:47], s65 offen offset:1024 lds\n"
        "buffer_load_dwordx4 v10, s[44:47], s65 offen offset:2048 lds\n"
        "s_waitcnt vmcnt(30)\n"
        "ds_read_b64_tr_b16 a[96:97], v11 offset:24576\n"
        "ds_read_b64_tr_b16 a[98:99], v11 offset:26112\n"
        "ds_read_b64_tr_b16 a[100:101], v11 offset:24584\n"
        "ds_read_b64_tr_b16 a[102:103], v11 offset:26120\n"
        "ds_read_b64_tr_b16 a[104:105], v4 offset:24576\n"
        "ds_read_b64_tr_b16 a[106:107], v4 offset:26112\n"
        "s_waitcnt lgkmcnt(6)\n"
        "s_add_u32 m0, s79, 21504\n"
        "s_mov_b32 s65, 700416\n"
        "buffer_load_dwordx4 v8, s[44:47], s65 offen lds\n"
        "buffer_load_dwordx4 v9, s[44:47], s65 offen offset:1024 lds\n"
        "buffer_load_dwordx4 v10, s[44:47], s65 offen offset:2048 lds\n"
        "s_waitcnt vmcnt(30)\n"
        "ds_read_b64_tr_b16 a[108:109], v11 offset:27648\n"
        "ds_read_b64_tr_b16 a[110:111], v11 offset:29184\n"
        "ds_read_b64_tr_b16 a[112:113], v11 offset:27656\n"
        "ds_read_b64_tr_b16 a[114:115], v11 offset:29192\n"
        "ds_read_b64_tr_b16 a[116:117], v4 offset:27648\n"
        "ds_read_b64_tr_b16 a[118:119], v4 offset:29184\n"
        "s_waitcnt lgkmcnt(6)\n"
        "s_add_u32 m0, s79, 24576\n"
        "s_mov_b32 s65, 737280\n"
        "buffer_load_dwordx4 v8, s[44:47], s65 offen lds\n"
        "buffer_load_dwordx4 v9, s[44:47], s65 offen offset:1024 lds\n"
        "buffer_load_dwordx4 v10, s[44:47], s65 offen offset:2048 lds\n"
        "s_waitcnt vmcnt(30)\n"
        "ds_read_b64_tr_b16 a[120:121], v11 offset:30720\n"
        "ds_read_b64_tr_b16 a[122:123], v11 offset:32256\n"
        "ds_read_b64_tr_b16 a[124:125], v11 offset:30728\n"
        "ds_read_b64_tr_b16 a[126:127], v11 offset:32264\n"
        "ds_read_b64_tr_b16 a[128:129], v4 offset:30720\n"
        "ds_read_b64_tr_b16 a[130:131], v4 offset:32256\n"
        "s_waitcnt lgkmcnt(6)\n"
        "s_add_u32 m0, s79, 27648\n"
        "s_mov_b32 s65, 774144\n"
        "buffer_load_dwordx4 v8, s[44:47], s65 offen lds\n"
        "buffer_load_dwordx4 v9, s[44:47], s65 offen offset:1024 lds\n"
        "buffer_load_dwordx4 v10, s[44:47], s65 offen offset:2048 lds\n"
        "s_waitcnt vmcnt(30)\n"
        "ds_read_b64_tr_b16 a[132:133], v11 offset:33792\n"
        "ds_read_b64_tr_b16 a[134:135], v11 offset:35328\n"
        "ds_read_b64_tr_b16 a[136:137], v11 offset:33800\n"
        "ds_read_b64_tr_b16 a[138:139], v11 offset:35336\n"
        "ds_read_b64_tr_b16 a[140:141], v4 offset:33792\n"
        "ds_read_b64_tr_b16 a[142:143], v4 offset:35328\n"
        "s_waitcnt lgkmcnt(6)\n"
        "s_add_u32 m0, s79, 30720\n"
        "s_mov_b32 s65, 811008\n"
        "buffer_load_dwordx4 v8, s[44:47], s65 offen lds\n"
        "buffer_load_dwordx4 v9, s[44:47], s65 offen offset:1024 lds\n"
        "buffer_load_dwordx4 v10, s[44:47], s65 offen offset:2048 lds\n"
        "s_waitcnt vmcnt(30)\n"
        "ds_read_b64_tr_b16 a[144:145], v11 offset:0\n"
        "ds_read_b64_tr_b16 a[146:147], v11 offset:1536\n"
        "ds_read_b64_tr_b16 a[148:149], v11 offset:8\n"
        "ds_read_b64_tr_b16 a[150:151], v11 offset:1544\n"
        "ds_read_b64_tr_b16 a[152:153], v4 offset:0\n"
        "ds_read_b64_tr_b16 a[154:155], v4 offset:1536\n"
        "s_waitcnt lgkmcnt(6)\n"
        "s_add_u32 m0, s79, 33792\n"
        "s_mov_b32 s65, 847872\n"
        "buffer_load_dwordx4 v8, s[44:47], s65 offen lds\n"
        "buffer_load_dwordx4 v9, s[44:47], s65 offen offset:1024 lds\n"
        "buffer_load_dwordx4 v10, s[44:47], s65 offen offset:2048 lds\n"
        "s_waitcnt vmcnt(30)\n"
        "ds_read_b64_tr_b16 a[156:157], v11 offset:3072\n"
        "ds_read_b64_tr_b16 a[158:159], v11 offset:4608\n"
        "ds_read_b64_tr_b16 a[160:161], v11 offset:3080\n"
        "ds_read_b64_tr_b16 a[162:163], v11 offset:4616\n"
        "ds_read_b64_tr_b16 a[164:165], v4 offset:3072\n"
        "ds_read_b64_tr_b16 a[166:167], v4 offset:4608\n"
        "s_waitcnt lgkmcnt(6)\n"
        "s_add_u32 m0, s79, 0\n"
        "s_mov_b32 s65, 884736\n"
        "buffer_load_dwordx4 v8, s[44:47], s65 offen lds\n"
        "buffer_load_dwordx4 v9, s[44:47], s65 offen offset:1024 lds\n"
        "buffer_load_dwordx4 v10, s[44:47], s65 offen offset:2048 lds\n"
        "s_waitcnt vmcnt(30)\n"
        "ds_read_b64_tr_b16 a[168:169], v11 offset:6144\n"
        "ds_read_b64_tr_b16 a[170:171], v11 offset:7680\n"
        "ds_read_b64_tr_b16 a[172:173], v11 offset:6152\n"
        "ds_read_b64_tr_b16 a[174:175], v11 offset:7688\n"
        "ds_read_b64_tr_b16 a[176:177], v4 offset:6144\n"
        "ds_read_b64_tr_b16 a[178:179], v4 offset:7680\n"
        "s_waitcnt lgkmcnt(6)\n"
        "s_add_u32 m0, s79, 3072\n"
        "s_mov_b32 s65, 921600\n"
        "buffer_load_dwordx4 v8, s[44:47], s65 offen lds\n"
        "buffer_load_dwordx4 v9, s[44:47], s65 offen offset:1024 lds\n"
        "buffer_load_dwordx4 v10, s[44:47], s65 offen offset:2048 lds\n"
        "s_waitcnt vmcnt(30)\n"
        "ds_read_b64_tr_b16 a[180:181], v11 offset:9216\n"
        "ds_read_b64_tr_b16 a[182:183], v11 offset:10752\n"
        "ds_read_b64_tr_b16 a[184:185], v11 offset:9224\n"
        "ds_read_b64_tr_b16 a[186:187], v11 offset:10760\n"
        "ds_read_b64_tr_b16 a[188:189], v4 offset:9216\n"
        "ds_read_b64_tr_b16 a[190:191], v4 offset:10752\n"
        "s_waitcnt lgkmcnt(6)\n"
        "s_add_u32 m0, s79, 6144\n"
        "s_mov_b32 s65, 958464\n"
        "buffer_load_dwordx4 v8, s[44:47], s65 offen lds\n"
        "buffer_load_dwordx4 v9, s[44:47], s65 offen offset:1024 lds\n"
        "buffer_load_dwordx4 v10, s[44:47], s65 offen offset:2048 lds\n"
        "s_waitcnt vmcnt(30)\n"
        "ds_read_b64_tr_b16 a[192:193], v11 offset:12288\n"
        "ds_read_b64_tr_b16 a[194:195], v11 offset:13824\n"
        "ds_read_b64_tr_b16 a[196:197], v11 offset:12296\n"
        "ds_read_b64_tr_b16 a[198:199], v11 offset:13832\n"
        "ds_read_b64_tr_b16 a[200:201], v4 offset:12288\n"
        "ds_read_b64_tr_b16 a[202:203], v4 offset:13824\n"
        "s_waitcnt lgkmcnt(6)\n"
        "s_add_u32 m0, s79, 9216\n"
        "s_mov_b32 s65, 995328\n"
        "buffer_load_dwordx4 v8, s[44:47], s65 offen lds\n"
        "buffer_load_dwordx4 v9, s[44:47], s65 offen offset:1024 lds\n"
        "buffer_load_dwordx4 v10, s[44:47], s65 offen offset:2048 lds\n"
        "s_waitcnt vmcnt(30)\n"
        "ds_read_b64_tr_b16 a[204:205], v11 offset:15360\n"
        "ds_read_b64_tr_b16 a[206:207], v11 offset:16896\n"
        "ds_read_b64_tr_b16 a[208:209], v11 offset:15368\n"
        "ds_read_b64_tr_b16 a[210:211], v11 offset:16904\n"
        "ds_read_b64_tr_b16 a[212:213], v4 offset:15360\n"
        "ds_read_b64_tr_b16 a[214:215], v4 offset:16896\n"
        "s_waitcnt lgkmcnt(6)\n"
        "s_add_u32 m0, s79, 12288\n"
        "s_mov_b32 s65, 1032192\n"
        "buffer_load_dwordx4 v8, s[44:47], s65 offen lds\n"
        "buffer_load_dwordx4 v9, s[44:47], s65 offen offset:1024 lds\n"
        "buffer_load_dwordx4 v10, s[44:47], s65 offen offset:2048 lds\n"
        "s_waitcnt vmcnt(30)\n"
        "ds_read_b64_tr_b16 a[216:217], v11 offset:18432\n"
        "ds_read_b64_tr_b16 a[218:219], v11 offset:19968\n"
        "ds_read_b64_tr_b16 a[220:221], v11 offset:18440\n"
        "ds_read_b64_tr_b16 a[222:223], v11 offset:19976\n"
        "ds_read_b64_tr_b16 a[224:225], v4 offset:18432\n"
        "ds_read_b64_tr_b16 a[226:227], v4 offset:19968\n"
        "s_waitcnt lgkmcnt(6)\n"
        "s_add_u32 m0, s79, 15360\n"
        "s_mov_b32 s65, 1069056\n"
        "buffer_load_dwordx4 v8, s[44:47], s65 offen lds\n"
        "buffer_load_dwordx4 v9, s[44:47], s65 offen offset:1024 lds\n"
        "buffer_load_dwordx4 v10, s[44:47], s65 offen offset:2048 lds\n"
        "s_waitcnt vmcnt(30)\n"
        "ds_read_b64_tr_b16 a[228:229], v11 offset:21504\n"
        "ds_read_b64_tr_b16 a[230:231], v11 offset:23040\n"
        "ds_read_b64_tr_b16 a[232:233], v11 offset:21512\n"
        "ds_read_b64_tr_b16 a[234:235], v11 offset:23048\n"
        "ds_read_b64_tr_b16 a[236:237], v4 offset:21504\n"
        "ds_read_b64_tr_b16 a[238:239], v4 offset:23040\n"
        "s_waitcnt lgkmcnt(6)\n"
        "s_add_u32 m0, s79, 18432\n"
        "s_mov_b32 s65, 1105920\n"
        "buffer_load_dwordx4 v8, s[44:47], s65 offen lds\n"
        "buffer_load_dwordx4 v9, s[44:47], s65 offen offset:1024 lds\n"
        "buffer_load_dwordx4 v10, s[44:47], s65 offen offset:2048 lds\n"
        "s_waitcnt vmcnt(30)\n"
        "ds_read_b64_tr_b16 a[240:241], v11 offset:24576\n"
        "ds_read_b64_tr_b16 a[242:243], v11 offset:26112\n"
        "ds_read_b64_tr_b16 a[244:245], v11 offset:24584\n"
        "ds_read_b64_tr_b16 a[246:247], v11 offset:26120\n"
        "ds_read_b64_tr_b16 a[248:249], v4 offset:24576\n"
        "ds_read_b64_tr_b16 a[250:251], v4 offset:26112\n"
        "s_waitcnt lgkmcnt(6)\n"
        "s_add_u32 m0, s79, 21504\n"
        "s_mov_b32 s65, 1142784\n"
        "buffer_load_dwordx4 v8, s[44:47], s65 offen lds\n"
        "buffer_load_dwordx4 v9, s[44:47], s65 offen offset:1024 lds\n"
        "buffer_load_dwordx4 v10, s[44:47], s65 offen offset:2048 lds\n"
        "s_waitcnt vmcnt(30)\n"
        "ds_read_b64_tr_b16 a[252:253], v11 offset:27648\n"
        "ds_read_b64_tr_b16 a[254:255], v11 offset:29184\n"
        "ds_read_b64_tr_b16 v[80:81], v11 offset:27656\n"
        "ds_read_b64_tr_b16 v[82:83], v11 offset:29192\n"
        "ds_read_b64_tr_b16 v[84:85], v4 offset:27648\n"
        "ds_read_b64_tr_b16 v[86:87], v4 offset:29184\n"
        "s_waitcnt vmcnt(27)\n"
        "ds_read_b64_tr_b16 v[88:89], v11 offset:30720\n"
        "ds_read_b64_tr_b16 v[90:91], v11 offset:32256\n"
        "ds_read_b64_tr_b16 v[92:93], v11 offset:30728\n"
        "ds_read_b64_tr_b16 v[94:95], v11 offset:32264\n"
        "ds_read_b64_tr_b16 v[96:97], v4 offset:30720\n"
        "ds_read_b64_tr_b16 v[98:99], v4 offset:32256\n"
        "s_waitcnt vmcnt(24)\n"
        "ds_read_b64_tr_b16 v[100:101], v11 offset:33792\n"
        "ds_read_b64_tr_b16 v[102:103], v11 offset:35328\n"
        "ds_read_b64_tr_b16 v[104:105], v11 offset:33800\n"
        "ds_read_b64_tr_b16 v[106:107], v11 offset:35336\n"
        "ds_read_b64_tr_b16 v[108:109], v4 offset:33792\n"
        "ds_read_b64_tr_b16 v[110:111], v4 offset:35328\n"
        "s_waitcnt vmcnt(21)\n"
        "ds_read_b64_tr_b16 v[112:113], v11 offset:0\n"
        "ds_read_b64_tr_b16 v[114:115], v11 offset:1536\n"
        "ds_read_b64_tr_b16 v[116:117], v11 offset:8\n"
        "ds_read_b64_tr_b16 v[118:119], v11 offset:1544\n"
        "ds_read_b64_tr_b16 v[120:121], v4 offset:0\n"
        "ds_read_b64_tr_b16 v[122:123], v4 offset:1536\n"
        "s_waitcnt vmcnt(18)\n"
        "ds_read_b64_tr_b16 v[124:125], v11 offset:3072\n"
        "ds_read_b64_tr_b16 v[126:127], v11 offset:4608\n"
        "ds_read_b64_tr_b16 v[128:129], v11 offset:3080\n"
        "ds_read_b64_tr_b16 v[130:131], v11 offset:4616\n"
        "ds_read_b64_tr_b16 v[132:133], v4 offset:3072\n"
        "ds_read_b64_tr_b16 v[134:135], v4 offset:4608\n"
        "s_waitcnt vmcnt(15)\n"
        "ds_read_b64_tr_b16 v[136:137], v11 offset:6144\n"
        "ds_read_b64_tr_b16 v[138:139], v11 offset:7680\n"
        "ds_read_b64_tr_b16 v[140:141], v11 offset:6152\n"
        "ds_read_b64_tr_b16 v[142:143], v11 offset:7688\n"
        "ds_read_b64_tr_b16 v[144:145], v4 offset:6144\n"
        "ds_read_b64_tr_b16 v[146:147], v4 offset:7680\n"
        "s_waitcnt vmcnt(12)\n"
        "ds_read_b64_tr_b16 v[148:149], v11 offset:9216\n"
        "ds_read_b64_tr_b16 v[150:151], v11 offset:10752\n"
        "ds_read_b64_tr_b16 v[152:153], v11 offset:9224\n"
        "ds_read_b64_tr_b16 v[154:155], v11 offset:10760\n"
        "ds_read_b64_tr_b16 v[156:157], v4 offset:9216\n"
        "ds_read_b64_tr_b16 v[158:159], v4 offset:10752\n"
        "s_waitcnt vmcnt(9)\n"
        "ds_read_b64_tr_b16 v[160:161], v11 offset:12288\n"
        "ds_read_b64_tr_b16 v[162:163], v11 offset:13824\n"
        "ds_read_b64_tr_b16 v[164:165], v11 offset:12296\n"
        "ds_read_b64_tr_b16 v[166:167], v11 offset:13832\n"
        "ds_read_b64_tr_b16 v[168:169], v4 offset:12288\n"
        "ds_read_b64_tr_b16 v[170:171], v4 offset:13824\n"
        "s_waitcnt vmcnt(6)\n"
        "ds_read_b64_tr_b16 v[172:173], v11 offset:15360\n"
        "ds_read_b64_tr_b16 v[174:175], v11 offset:16896\n"
        "ds_read_b64_tr_b16 v[176:177], v11 offset:15368\n"
        "ds_read_b64_tr_b16 v[178:179], v11 offset:16904\n"
        "ds_read_b64_tr_b16 v[180:181], v4 offset:15360\n"
        "ds_read_b64_tr_b16 v[182:183], v4 offset:16896\n"
        "s_waitcnt vmcnt(3)\n"
        "ds_read_b64_tr_b16 v[184:185], v11 offset:18432\n"
        "ds_read_b64_tr_b16 v[186:187], v11 offset:19968\n"
        "ds_read_b64_tr_b16 v[188:189], v11 offset:18440\n"
        "ds_read_b64_tr_b16 v[190:191], v11 offset:19976\n"
        "ds_read_b64_tr_b16 v[192:193], v4 offset:18432\n"
        "ds_read_b64_tr_b16 v[194:195], v4 offset:19968\n"
        "s_waitcnt vmcnt(0)\n"
        "ds_read_b64_tr_b16 v[196:197], v11 offset:21504\n"
        "ds_read_b64_tr_b16 v[198:199], v11 offset:23040\n"
        "ds_read_b64_tr_b16 v[200:201], v11 offset:21512\n"
        "ds_read_b64_tr_b16 v[202:203], v11 offset:23048\n"
        "ds_read_b64_tr_b16 v[204:205], v4 offset:21504\n"
        "ds_read_b64_tr_b16 v[206:207], v4 offset:23040\n"
        "s_waitcnt lgkmcnt(0)\n"
        "v_lshrrev_b32 v12, 4, v1\n"
        "s_lshl_b32 s66, s57, 7\n"
        "s_mov_b32 s64, 0\n"
        ".Lop_pair_top_%=:\n"
        "s_mov_b64 exec, 1\n"
        "buffer_load_dword v13, off, s[40:43], s66 sc1\n"
        "s_mov_b64 exec, -1\n"
        "s_waitcnt vmcnt(0)\n"
        "v_cmp_gt_i32 vcc, 0, v13\n"
        "s_nop 0\n"
        "s_and_b64 vcc, vcc, 1\n"
        "s_cbranch_vccz .Lop_pair_ok_%=\n"
        "s_add_u32 s64, s64, 1\n"
        "s_and_b32 s65, s64, 255\n"
        "s_cbranch_scc1 .Lop_pair_go_%=\n"
        "s_mov_b32 s65, 40960\n"
        "s_cmp_lt_u32 s64, 1048576\n"
        "s_cbranch_scc1 .Lop_pair_chk_%=\n"
        "v_mov_b32 v13, 1\n"
        "buffer_store_dword v13, off, s[40:43], s65 sc1\n"
        "s_waitcnt vmcnt(0)\n"
        ".Lop_pair_chk_%=:\n"
        "buffer_load_dword v13, off, s[40:43], s65 sc1\n"
        "s_waitcnt vmcnt(0)\n"
        "v_readfirstlane_b32 s65, v13\n"
        "s_nop 0\n"
        "s_cmp_lg_u32 s65, 1\n"
        "s_cbranch_scc1 .Lop_pair_go_%=\n"
        "s_endpgm\n"
        ".Lop_pair_go_%=:\n"
        "s_sleep 2\n"
        "s_branch .Lop_pair_top_%=\n"
        ".Lop_pair_ok_%=:\n"
        "v_readfirstlane_b32 s50, v13\n"
        "s_nop 0\n"
        "s_xor_b32 s50, s50, s56\n"
        "v_lshlrev_b32 v14, 7, v12\n"
        "s_sub_u32 s66, 0, s50\n"
        "s_mov_b32 s52, 24576\n"
        "s_mov_b32 s64, 0\n"
        ".Lop_pg0_top_%=:\n"
        "buffer_load_dword v13, v14, s[40:43], s52 offen sc1\n"
        "s_waitcnt vmcnt(0)\n"
        "v_cmp_gt_i32 vcc, 0, v13\n"
        "s_cbranch_vccz .Lop_pg0_ok_%=\n"
        "s_add_u32 s64, s64, 1\n"
        "s_and_b32 s65, s64, 255\n"
        "s_cbranch_scc1 .Lop_pg0_go_%=\n"
        "s_mov_b32 s65, 40960\n"
        "s_cmp_lt_u32 s64, 1048576\n"
        "s_cbranch_scc1 .Lop_pg0_chk_%=\n"
        "v_mov_b32 v13, 1\n"
        "buffer_store_dword v13, off, s[40:43], s65 sc1\n"
        "s_waitcnt vmcnt(0)\n"
        ".Lop_pg0_chk_%=:\n"
        "buffer_load_dword v13, off, s[40:43], s65 sc1\n"
        "s_waitcnt vmcnt(0)\n"
        "v_readfirstlane_b32 s65, v13\n"
        "s_nop 0\n"
        "s_cmp_lg_u32 s65, 1\n"
        "s_cbranch_scc1 .Lop_pg0_go_%=\n"
        "s_endpgm\n"
        ".Lop_pg0_go_%=:\n"
        "s_sleep 2\n"
        "s_branch .Lop_pg0_top_%=\n"
        ".Lop_pg0_ok_%=:\n"
        "v_cmp_eq_u32 s[52:53], 1, v13\n"
        "s_nop 1\n"
        "s_mov_b32 s54, 32768\n"
        "s_mov_b32 s64, 0\n"
        ".Lop_pg1_top_%=:\n"
        "buffer_load_dword v13, v14, s[40:43], s54 offen sc1\n"
        "s_waitcnt vmcnt(0)\n"
        "v_cmp_gt_i32 vcc, 0, v13\n"
        "s_cbranch_vccz .Lop_pg1_ok_%=\n"
        "s_add_u32 s64, s64, 1\n"
        "s_and_b32 s65, s64, 255\n"
        "s_cbranch_scc1 .Lop_pg1_go_%=\n"
        "s_mov_b32 s65, 40960\n"
        "s_cmp_lt_u32 s64, 1048576\n"
        "s_cbranch_scc1 .Lop_pg1_chk_%=\n"
        "v_mov_b32 v13, 1\n"
        "buffer_store_dword v13, off, s[40:43], s65 sc1\n"
        "s_waitcnt vmcnt(0)\n"
        ".Lop_pg1_chk_%=:\n"
        "buffer_load_dword v13, off, s[40:43], s65 sc1\n"
        "s_waitcnt vmcnt(0)\n"
        "v_readfirstlane_b32 s65, v13\n"
        "s_nop 0\n"
        "s_cmp_lg_u32 s65, 1\n"
        "s_cbranch_scc1 .Lop_pg1_go_%=\n"
        "s_endpgm\n"
        ".Lop_pg1_go_%=:\n"
        "s_sleep 2\n"
        "s_branch .Lop_pg1_top_%=\n"
        ".Lop_pg1_ok_%=:\n"
        "v_cmp_eq_u32 s[54:55], 1, v13\n"
        "s_nop 1\n"
        "s_mov_b32 vcc_lo, 0x55555555\n"
        "s_mov_b32 vcc_hi, 0x55555555\n"
        "s_xor_b64 vcc, vcc, s[52:53]\n"
        "s_bcnt1_i32_b64 s65, vcc\n"
        "s_sub_u32 s64, s65, 23\n"
        "s_cmp_lt_u32 s64, 19\n"
        "s_cbranch_scc1 .Lop_rawmap_%=\n"
        "s_cmp_lt_u32 s65, 32\n"
        "s_cselect_b32 s65, 0, -1\n"
        "s_xor_b32 s52, s65, 0x55555555\n"
        "s_xor_b32 s53, s65, 0x55555555\n"
        "s_xor_b32 s54, s65, 0x55555555\n"
        "s_xor_b32 s55, s65, 0xaaaaaaaa\n"
        ".Lop_rawmap_%=:\n"
        "s_xor_b32 s52, s52, s66\n"
        "s_xor_b32 s53, s53, s66\n"
        "s_xor_b32 s54, s54, s66\n"
        "s_xor_b32 s55, s55, s66\n"
        "s_and_b32 s76, s55, 1\n"
        "s_lshl_b32 s76, s76, 13\n"
        "s_add_u32 s60, s60, s76\n"
        "s_barrier\n"
        "s_mov_b32 s68, -1\n"
        "s_mov_b32 s69, 0\n"
        "v_add_u32 v8, 32768, v0\n"
        "s_mov_b32 s72, 241172480\n"
        "s_mov_b32 s73, 241172480\n"
        "s_branch .Lop_wait3_%=\n"
        ".Lop_c01_%=:\n"
        "v_mfma_f32_16x16x32_bf16 v[44:47], a[0:3], v[12:15], 0\n"
        "v_cvt_pk_bf16_f32 v74, v60, v61\n"
        "v_cvt_pk_bf16_f32 v75, v62, v63\n"
        "v_mfma_f32_16x16x32_bf16 v[48:51], a[4:7], v[12:15], 0\n"
        "v_cvt_pk_bf16_f32 v78, v64, v65\n"
        "v_cvt_pk_bf16_f32 v79, v66, v67\n"
        "v_mfma_f32_16x16x32_bf16 v[52:55], a[8:11], v[12:15], 0\n"
        "ds_read_b128 v[12:15], v0 offset:4096\n"
        "s_add_u32 m0, s61, 16384\n"
        "v_mfma_f32_16x16x32_bf16 v[56:59], a[0:3], v[16:19], 0\n"
        "s_add_u32 s71, s71, 32768\n"
        "buffer_load_dwordx4 v1, s[40:43], s71 offen offset:0 sc1 lds\n"
        "v_mfma_f32_16x16x32_bf16 v[60:63], a[4:7], v[16:19], 0\n"
        "buffer_load_dwordx4 v1, s[40:43], s71 offen offset:1024 sc1 lds\n"
        "v_mfma_f32_16x16x32_bf16 v[64:67], a[8:11], v[16:19], 0\n"
        "ds_read_b128 v[16:19], v0 offset:5120\n"
        "s_waitcnt lgkmcnt(2)\n"
        "v_mfma_f32_16x16x32_bf16 v[44:47], a[12:15], v[20:23], v[44:47]\n"
        "buffer_store_dwordx4 v[68:71], v6, s[40:43], s72 offen sc1\n"
        "v_mfma_f32_16x16x32_bf16 v[48:51], a[16:19], v[20:23], v[48:51]\n"
        "buffer_load_dwordx4 v1, s[40:43], s71 offen offset:2048 sc1 lds\n"
        "s_add_u32 s66, s72, 147456\n"
        "v_mfma_f32_16x16x32_bf16 v[52:55], a[20:23], v[20:23], v[52:55]\n"
        "ds_read_b128 v[20:23], v0 offset:6144\n"
        "v_mfma_f32_16x16x32_bf16 v[56:59], a[12:15], v[24:27], v[56:59]\n"
        "buffer_store_dwordx4 v[72:75], v6, s[40:43], s66 offen sc1\n"
        "v_mfma_f32_16x16x32_bf16 v[60:63], a[16:19], v[24:27], v[60:63]\n"
        "buffer_load_dwordx4 v1, s[40:43], s71 offen offset:3072 sc1 lds\n"
        "v_mfma_f32_16x16x32_bf16 v[64:67], a[20:23], v[24:27], v[64:67]\n"
        "ds_read_b128 v[24:27], v0 offset:7168\n"
        "s_waitcnt lgkmcnt(2)\n"
        "v_mfma_f32_16x16x32_bf16 v[44:47], a[24:27], v[12:15], v[44:47]\n"
        "v_permlane16_swap_b32 v76, v78\n"
        "v_mfma_f32_16x16x32_bf16 v[48:51], a[28:31], v[12:15], v[48:51]\n"
        "v_permlane16_swap_b32 v77, v79\n"
        "v_mfma_f32_16x16x32_bf16 v[52:55], a[32:35], v[12:15], v[52:55]\n"
        "ds_read_b128 v[12:15], v0 offset:8192\n"
        "buffer_load_dword v4, v2, s[40:43], s76 offen sc1\n"
        "v_mfma_f32_16x16x32_bf16 v[56:59], a[24:27], v[16:19], v[56:59]\n"
        "buffer_store_dwordx4 v[76:79], v7, s[40:43], s72 offen sc1\n"
        "v_mfma_f32_16x16x32_bf16 v[60:63], a[28:31], v[16:19], v[60:63]\n"
        "v_mfma_f32_16x16x32_bf16 v[64:67], a[32:35], v[16:19], v[64:67]\n"
        "ds_read_b128 v[16:19], v0 offset:9216\n"
        "s_waitcnt lgkmcnt(2)\n"
        "v_mfma_f32_16x16x32_bf16 v[44:47], a[36:39], v[20:23], v[44:47]\n"
        "v_mfma_f32_16x16x32_bf16 v[48:51], a[40:43], v[20:23], v[48:51]\n"
        "v_mfma_f32_16x16x32_bf16 v[52:55], a[44:47], v[20:23], v[52:55]\n"
        "ds_read_b128 v[20:23], v0 offset:10240\n"
        "v_mfma_f32_16x16x32_bf16 v[56:59], a[36:39], v[24:27], v[56:59]\n"
        "v_mfma_f32_16x16x32_bf16 v[60:63], a[40:43], v[24:27], v[60:63]\n"
        "v_mfma_f32_16x16x32_bf16 v[64:67], a[44:47], v[24:27], v[64:67]\n"
        "ds_read_b128 v[24:27], v0 offset:11264\n"
        "s_waitcnt lgkmcnt(2)\n"
        "v_mfma_f32_16x16x32_bf16 v[44:47], a[48:51], v[12:15], v[44:47]\n"
        "v_mfma_f32_16x16x32_bf16 v[48:51], a[52:55], v[12:15], v[48:51]\n"
        "v_mfma_f32_16x16x32_bf16 v[52:55], a[56:59], v[12:15], v[52:55]\n"
        "ds_read_b128 v[12:15], v0 offset:12288\n"
        "v_mfma_f32_16x16x32_bf16 v[56:59], a[48:51], v[16:19], v[56:59]\n"
        "v_mfma_f32_16x16x32_bf16 v[60:63], a[52:55], v[16:19], v[60:63]\n"
        "v_mfma_f32_16x16x32_bf16 v[64:67], a[56:59], v[16:19], v[64:67]\n"
        "ds_read_b128 v[16:19], v0 offset:13312\n"
        "s_waitcnt lgkmcnt(2)\n"
        "v_mfma_f32_16x16x32_bf16 v[44:47], a[60:63], v[20:23], v[44:47]\n"
        "v_mfma_f32_16x16x32_bf16 v[48:51], a[64:67], v[20:23], v[48:51]\n"
        "v_mfma_f32_16x16x32_bf16 v[52:55], a[68:71], v[20:23], v[52:55]\n"
        "ds_read_b128 v[20:23], v0 offset:14336\n"
        "v_mfma_f32_16x16x32_bf16 v[56:59], a[60:63], v[24:27], v[56:59]\n"
        "v_mfma_f32_16x16x32_bf16 v[60:63], a[64:67], v[24:27], v[60:63]\n"
        "v_mfma_f32_16x16x32_bf16 v[64:67], a[68:71], v[24:27], v[64:67]\n"
        "ds_read_b128 v[24:27], v0 offset:15360\n"
        "s_waitcnt lgkmcnt(2)\n"
        "v_mfma_f32_16x16x32_bf16 v[44:47], a[72:75], v[12:15], v[44:47]\n"
        "v_mfma_f32_16x16x32_bf16 v[48:51], a[76:79], v[12:15], v[48:51]\n"
        "s_waitcnt vmcnt(2)\n"
        "s_barrier\n"
        "v_mfma_f32_16x16x32_bf16 v[52:55], a[80:83], v[12:15], v[52:55]\n"
        "ds_read_b128 v[12:15], v0 offset:16384\n"
        "v_mfma_f32_16x16x32_bf16 v[56:59], a[72:75], v[16:19], v[56:59]\n"
        "v_mfma_f32_16x16x32_bf16 v[60:63], a[76:79], v[16:19], v[60:63]\n"
        "s_waitcnt vmcnt(0)\n"
        "v_mov_b32 v5, s74\n"
        "v_mfma_f32_16x16x32_bf16 v[64:67], a[80:83], v[16:19], v[64:67]\n"
        "ds_read_b128 v[16:19], v0 offset:17408\n"
        "s_waitcnt lgkmcnt(2)\n"
        "v_mfma_f32_16x16x32_bf16 v[44:47], a[84:87], v[20:23], v[44:47]\n"
        "buffer_store_dword v5, v3, s[40:43], s73 offen sc1\n"
        "s_add_u32 s68, s68, 1\n"
        "v_cmp_gt_i32 vcc, s70, v4\n"
        "v_mfma_f32_16x16x32_bf16 v[48:51], a[88:91], v[20:23], v[48:51]\n"
        "s_lshr_b32 s66, s68, 1\n"
        "s_and_b64 vcc, vcc, 15\n"
        "v_mfma_f32_16x16x32_bf16 v[52:55], a[92:95], v[20:23], v[52:55]\n"
        "ds_read_b128 v[20:23], v0 offset:18432\n"
        "v_mfma_f32_16x16x32_bf16 v[56:59], a[84:87], v[24:27], v[56:59]\n"
        "s_bitcmp1_b32 s54, s66\n"
        "s_cselect_b32 s66, 8192, 0\n"
        "v_mfma_f32_16x16x32_bf16 v[60:63], a[88:91], v[24:27], v[60:63]\n"
        "s_lshl_b32 s72, s68, 20\n"
        "s_add_u32 s72, s72, s66\n"
        "v_mfma_f32_16x16x32_bf16 v[64:67], a[92:95], v[24:27], v[64:67]\n"
        "ds_read_b128 v[24:27], v0 offset:19456\n"
        "s_waitcnt lgkmcnt(2)\n"
        "s_add_u32 s72, s72, s59\n"
        "s_mov_b32 s74, s68\n"
        "s_mov_b32 s73, s60\n"
        "s_cbranch_vccnz .Lop_c10_%=\n"
        "s_branch .Lop_c11_%=\n"
        ".Lop_c00_%=:\n"
        "v_mfma_f32_16x16x32_bf16 v[44:47], a[0:3], v[12:15], 0\n"
        "v_cvt_pk_bf16_f32 v74, v60, v61\n"
        "v_cvt_pk_bf16_f32 v75, v62, v63\n"
        "v_mfma_f32_16x16x32_bf16 v[48:51], a[4:7], v[12:15], 0\n"
        "v_cvt_pk_bf16_f32 v78, v64, v65\n"
        "v_cvt_pk_bf16_f32 v79, v66, v67\n"
        "v_mfma_f32_16x16x32_bf16 v[52:55], a[8:11], v[12:15], 0\n"
        "ds_read_b128 v[12:15], v0 offset:4096\n"
        "v_mfma_f32_16x16x32_bf16 v[56:59], a[0:3], v[16:19], 0\n"
        "buffer_store_dwordx4 v[68:71], v6, s[40:43], s72 offen sc1\n"
        "v_mfma_f32_16x16x32_bf16 v[60:63], a[4:7], v[16:19], 0\n"
        "s_add_u32 s66, s72, 147456\n"
        "v_mfma_f32_16x16x32_bf16 v[64:67], a[8:11], v[16:19], 0\n"
        "ds_read_b128 v[16:19], v0 offset:5120\n"
        "s_waitcnt lgkmcnt(2)\n"
        "v_mfma_f32_16x16x32_bf16 v[44:47], a[12:15], v[20:23], v[44:47]\n"
        "buffer_store_dwordx4 v[72:75], v6, s[40:43], s66 offen sc1\n"
        "v_mfma_f32_16x16x32_bf16 v[48:51], a[16:19], v[20:23], v[48:51]\n"
        "v_permlane16_swap_b32 v76, v78\n"
        "v_mfma_f32_16x16x32_bf16 v[52:55], a[20:23], v[20:23], v[52:55]\n"
        "ds_read_b128 v[20:23], v0 offset:6144\n"
        "v_mfma_f32_16x16x32_bf16 v[56:59], a[12:15], v[24:27], v[56:59]\n"
        "v_permlane16_swap_b32 v77, v79\n"
        "v_mfma_f32_16x16x32_bf16 v[60:63], a[16:19], v[24:27], v[60:63]\n"
        "buffer_store_dwordx4 v[76:79], v7, s[40:43], s72 offen sc1\n"
        "v_mfma_f32_16x16x32_bf16 v[64:67], a[20:23], v[24:27], v[64:67]\n"
        "ds_read_b128 v[24:27], v0 offset:7168\n"
        "s_waitcnt lgkmcnt(2)\n"
        "v_mfma_f32_16x16x32_bf16 v[44:47], a[24:27], v[12:15], v[44:47]\n"
        "buffer_load_dword v4, v2, s[40:43], s76 offen sc1\n"
        "v_mfma_f32_16x16x32_bf16 v[48:51], a[28:31], v[12:15], v[48:51]\n"
        "v_mfma_f32_16x16x32_bf16 v[52:55], a[32:35], v[12:15], v[52:55]\n"
        "ds_read_b128 v[12:15], v0 offset:8192\n"
        "v_mfma_f32_16x16x32_bf16 v[56:59], a[24:27], v[16:19], v[56:59]\n"
        "v_mfma_f32_16x16x32_bf16 v[60:63], a[28:31], v[16:19], v[60:63]\n"
        "v_mfma_f32_16x16x32_bf16 v[64:67], a[32:35], v[16:19], v[64:67]\n"
        "ds_read_b128 v[16:19], v0 offset:9216\n"
        "s_waitcnt lgkmcnt(2)\n"
        "v_mfma_f32_16x16x32_bf16 v[44:47], a[36:39], v[20:23], v[44:47]\n"
        "v_mfma_f32_16x16x32_bf16 v[48:51], a[40:43], v[20:23], v[48:51]\n"
        "v_mfma_f32_16x16x32_bf16 v[52:55], a[44:47], v[20:23], v[52:55]\n"
        "ds_read_b128 v[20:23], v0 offset:10240\n"
        "v_mfma_f32_16x16x32_bf16 v[56:59], a[36:39], v[24:27], v[56:59]\n"
        "v_mfma_f32_16x16x32_bf16 v[60:63], a[40:43], v[24:27], v[60:63]\n"
        "v_mfma_f32_16x16x32_bf16 v[64:67], a[44:47], v[24:27], v[64:67]\n"
        "ds_read_b128 v[24:27], v0 offset:11264\n"
        "s_waitcnt lgkmcnt(2)\n"
        "v_mfma_f32_16x16x32_bf16 v[44:47], a[48:51], v[12:15], v[44:47]\n"
        "v_mfma_f32_16x16x32_bf16 v[48:51], a[52:55], v[12:15], v[48:51]\n"
        "v_mfma_f32_16x16x32_bf16 v[52:55], a[56:59], v[12:15], v[52:55]\n"
        "ds_read_b128 v[12:15], v0 offset:12288\n"
        "v_mfma_f32_16x16x32_bf16 v[56:59], a[48:51], v[16:19], v[56:59]\n"
        "v_mfma_f32_16x16x32_bf16 v[60:63], a[52:55], v[16:19], v[60:63]\n"
        "v_mfma_f32_16x16x32_bf16 v[64:67], a[56:59], v[16:19], v[64:67]\n"
        "ds_read_b128 v[16:19], v0 offset:13312\n"
        "s_waitcnt lgkmcnt(2)\n"
        "v_mfma_f32_16x16x32_bf16 v[44:47], a[60:63], v[20:23], v[44:47]\n"
        "v_mfma_f32_16x16x32_bf16 v[48:51], a[64:67], v[20:23], v[48:51]\n"
        "v_mfma_f32_16x16x32_bf16 v[52:55], a[68:71], v[20:23], v[52:55]\n"
        "ds_read_b128 v[20:23], v0 offset:14336\n"
        "v_mfma_f32_16x16x32_bf16 v[56:59], a[60:63], v[24:27], v[56:59]\n"
        "v_mfma_f32_16x16x32_bf16 v[60:63], a[64:67], v[24:27], v[60:63]\n"
        "v_mfma_f32_16x16x32_bf16 v[64:67], a[68:71], v[24:27], v[64:67]\n"
        "ds_read_b128 v[24:27], v0 offset:15360\n"
        "s_waitcnt lgkmcnt(2)\n"
        "v_mfma_f32_16x16x32_bf16 v[44:47], a[72:75], v[12:15], v[44:47]\n"
        "v_mfma_f32_16x16x32_bf16 v[48:51], a[76:79], v[12:15], v[48:51]\n"
        "v_mfma_f32_16x16x32_bf16 v[52:55], a[80:83], v[12:15], v[52:55]\n"
        "v_mfma_f32_16x16x32_bf16 v[56:59], a[72:75], v[16:19], v[56:59]\n"
        "v_mfma_f32_16x16x32_bf16 v[60:63], a[76:79], v[16:19], v[60:63]\n"
        "s_waitcnt vmcnt(1)\n"
        "v_mov_b32 v5, s74\n"
        "v_mfma_f32_16x16x32_bf16 v[64:67], a[80:83], v[16:19], v[64:67]\n"
        "s_waitcnt lgkmcnt(0)\n"
        "buffer_store_dword v5, v3, s[40:43], s73 offen sc1\n"
        "v_mfma_f32_16x16x32_bf16 v[44:47], a[84:87], v[20:23], v[44:47]\n"
        "s_add_u32 s68, s68, 1\n"
        "s_lshr_b32 s66, s68, 1\n"
        "v_mfma_f32_16x16x32_bf16 v[48:51], a[88:91], v[20:23], v[48:51]\n"
        "s_bitcmp1_b32 s54, s66\n"
        "s_cselect_b32 s66, 8192, 0\n"
        "v_mfma_f32_16x16x32_bf16 v[52:55], a[92:95], v[20:23], v[52:55]\n"
        "s_lshl_b32 s72, s68, 20\n"
        "s_add_u32 s72, s72, s66\n"
        "v_mfma_f32_16x16x32_bf16 v[56:59], a[84:87], v[24:27], v[56:59]\n"
        "s_add_u32 s72, s72, s59\n"
        "s_mov_b32 s74, s68\n"
        "v_mfma_f32_16x16x32_bf16 v[60:63], a[88:91], v[24:27], v[60:63]\n"
        "s_mov_b32 s73, s60\n"
        "v_mfma_f32_16x16x32_bf16 v[64:67], a[92:95], v[24:27], v[64:67]\n"
        "s_waitcnt vmcnt(1)\n"
        "v_cmp_gt_i32 vcc, s70, v4\n"
        "s_nop 0\n"
        "s_and_b64 vcc, vcc, 15\n"
        "s_cbranch_vccnz .Lop_late0_%=\n"
        "s_add_u32 m0, s61, 16384\n"
        "s_add_u32 s71, s71, 32768\n"
        "buffer_load_dwordx4 v1, s[40:43], s71 offen offset:0 sc1 lds\n"
        "buffer_load_dwordx4 v1, s[40:43], s71 offen offset:1024 sc1 lds\n"
        "buffer_load_dwordx4 v1, s[40:43], s71 offen offset:2048 sc1 lds\n"
        "buffer_load_dwordx4 v1, s[40:43], s71 offen offset:3072 sc1 lds\n"
        "s_branch .Lop_go0_%=\n"
        ".Lop_late0_%=:\n"
        "s_mov_b32 s64, 0\n"
        ".Lop_in0_top_%=:\n"
        "buffer_load_dword v4, v2, s[40:43], s76 offen sc1\n"
        "s_waitcnt vmcnt(0)\n"
        "v_cmp_gt_i32 vcc, s70, v4\n"
        "s_nop 0\n"
        "s_and_b64 vcc, vcc, 15\n"
        "s_cbranch_vccz .Lop_in0_ok_%=\n"
        "s_add_u32 s64, s64, 1\n"
        "s_and_b32 s65, s64, 255\n"
        "s_cbranch_scc1 .Lop_in0_go_%=\n"
        "s_mov_b32 s65, 40960\n"
        "s_cmp_lt_u32 s64, 1048576\n"
        "s_cbranch_scc1 .Lop_in0_chk_%=\n"
        "v_mov_b32 v4, 1\n"
        "buffer_store_dword v4, off, s[40:43], s65 sc1\n"
        "s_waitcnt vmcnt(0)\n"
        ".Lop_in0_chk_%=:\n"
        "buffer_load_dword v4, off, s[40:43], s65 sc1\n"
        "s_waitcnt vmcnt(0)\n"
        "v_readfirstlane_b32 s65, v4\n"
        "s_nop 0\n"
        "s_cmp_lg_u32 s65, 1\n"
        "s_cbranch_scc1 .Lop_in0_go_%=\n"
        "s_endpgm\n"
        ".Lop_in0_go_%=:\n"
        "s_sleep 1\n"
        "s_branch .Lop_in0_top_%=\n"
        ".Lop_in0_ok_%=:\n"
        "s_add_u32 m0, s61, 16384\n"
        "s_add_u32 s71, s71, 32768\n"
        "buffer_load_dwordx4 v1, s[40:43], s71 offen offset:0 sc1 lds\n"
        "buffer_load_dwordx4 v1, s[40:43], s71 offen offset:1024 sc1 lds\n"
        "buffer_load_dwordx4 v1, s[40:43], s71 offen offset:2048 sc1 lds\n"
        "buffer_load_dwordx4 v1, s[40:43], s71 offen offset:3072 sc1 lds\n"
        ".Lop_go0_%=:\n"
        "buffer_load_dword v4, v2, s[40:43], s76 offen sc1\n"
        "s_waitcnt vmcnt(0)\n"
        "s_barrier\n"
        "ds_read_b128 v[12:15], v0 offset:16384\n"
        "ds_read_b128 v[16:19], v0 offset:17408\n"
        "ds_read_b128 v[20:23], v0 offset:18432\n"
        "ds_read_b128 v[24:27], v0 offset:19456\n"
        "v_cmp_gt_i32 vcc, s70, v4\n"
        "s_nop 0\n"
        "s_and_b64 vcc, vcc, 15\n"
        "s_waitcnt lgkmcnt(2)\n"
        "s_cbranch_vccnz .Lop_c10_%=\n"
        "s_branch .Lop_c11_%=\n"
        ".Lop_c11_%=:\n"
        "v_mfma_f32_16x16x32_bf16 v[44:47], a[96:99], v[12:15], v[44:47]\n"
        "s_add_u32 m0, s61, 32768\n"
        "s_add_u32 s71, s71, 32768\n"
        "v_mfma_f32_16x16x32_bf16 v[48:51], a[100:103], v[12:15], v[48:51]\n"
        "buffer_load_dwordx4 v1, s[40:43], s71 offen offset:0 sc1 lds\n"
        "v_mfma_f32_16x16x32_bf16 v[52:55], a[104:107], v[12:15], v[52:55]\n"
        "ds_read_b128 v[12:15], v0 offset:20480\n"
        "v_mfma_f32_16x16x32_bf16 v[56:59], a[96:99], v[16:19], v[56:59]\n"
        "v_mfma_f32_16x16x32_bf16 v[60:63], a[100:103], v[16:19], v[60:63]\n"
        "buffer_load_dwordx4 v1, s[40:43], s71 offen offset:1024 sc1 lds\n"
        "v_mfma_f32_16x16x32_bf16 v[64:67], a[104:107], v[16:19], v[64:67]\n"
        "ds_read_b128 v[16:19], v0 offset:21504\n"
        "s_waitcnt lgkmcnt(2)\n"
        "v_mfma_f32_16x16x32_bf16 v[44:47], a[108:111], v[20:23], v[44:47]\n"
        "v_mfma_f32_16x16x32_bf16 v[48:51], a[112:115], v[20:23], v[48:51]\n"
        "buffer_load_dwordx4 v1, s[40:43], s71 offen offset:2048 sc1 lds\n"
        "v_mfma_f32_16x16x32_bf16 v[52:55], a[116:119], v[20:23], v[52:55]\n"
        "ds_read_b128 v[20:23], v0 offset:22528\n"
        "v_mfma_f32_16x16x32_bf16 v[56:59], a[108:111], v[24:27], v[56:59]\n"
        "v_mfma_f32_16x16x32_bf16 v[60:63], a[112:115], v[24:27], v[60:63]\n"
        "buffer_load_dwordx4 v1, s[40:43], s71 offen offset:3072 sc1 lds\n"
        "v_mfma_f32_16x16x32_bf16 v[64:67], a[116:119], v[24:27], v[64:67]\n"
        "ds_read_b128 v[24:27], v0 offset:23552\n"
        "s_waitcnt lgkmcnt(2)\n"
        "v_mfma_f32_16x16x32_bf16 v[44:47], a[120:123], v[12:15], v[44:47]\n"
        "buffer_load_dword v4, v2, s[40:43], s76 offen sc1\n"
        "v_mfma_f32_16x16x32_bf16 v[48:51], a[124:127], v[12:15], v[48:51]\n"
        "v_mfma_f32_16x16x32_bf16 v[52:55], a[128:131], v[12:15], v[52:55]\n"
        "ds_read_b128 v[12:15], v0 offset:24576\n"
        "v_mfma_f32_16x16x32_bf16 v[56:59], a[120:123], v[16:19], v[56:59]\n"
        "v_mfma_f32_16x16x32_bf16 v[60:63], a[124:127], v[16:19], v[60:63]\n"
        "v_mfma_f32_16x16x32_bf16 v[64:67], a[128:131], v[16:19], v[64:67]\n"
        "ds_read_b128 v[16:19], v0 offset:25600\n"
        "s_waitcnt lgkmcnt(2)\n"
        "v_mfma_f32_16x16x32_bf16 v[44:47], a[132:135], v[20:23], v[44:47]\n"
        "v_mfma_f32_16x16x32_bf16 v[48:51], a[136:139], v[20:23], v[48:51]\n"
        "v_mfma_f32_16x16x32_bf16 v[52:55], a[140:143], v[20:23], v[52:55]\n"
        "ds_read_b128 v[20:23], v0 offset:26624\n"
        "v_mfma_f32_16x16x32_bf16 v[56:59], a[132:135], v[24:27], v[56:59]\n"
        "v_mfma_f32_16x16x32_bf16 v[60:63], a[136:139], v[24:27], v[60:63]\n"
        "v_mfma_f32_16x16x32_bf16 v[64:67], a[140:143], v[24:27], v[64:67]\n"
        "ds_read_b128 v[24:27], v0 offset:27648\n"
        "s_waitcnt lgkmcnt(2)\n"
        "v_mfma_f32_16x16x32_bf16 v[44:47], a[144:147], v[12:15], v[44:47]\n"
        "v_mfma_f32_16x16x32_bf16 v[48:51], a[148:151], v[12:15], v[48:51]\n"
        "v_mfma_f32_16x16x32_bf16 v[52:55], a[152:155], v[12:15], v[52:55]\n"
        "ds_read_b128 v[12:15], v0 offset:28672\n"
        "v_mfma_f32_16x16x32_bf16 v[56:59], a[144:147], v[16:19], v[56:59]\n"
        "v_mfma_f32_16x16x32_bf16 v[60:63], a[148:151], v[16:19], v[60:63]\n"
        "v_mfma_f32_16x16x32_bf16 v[64:67], a[152:155], v[16:19], v[64:67]\n"
        "ds_read_b128 v[16:19], v0 offset:29696\n"
        "s_waitcnt lgkmcnt(2)\n"
        "v_mfma_f32_16x16x32_bf16 v[44:47], a[156:159], v[20:23], v[44:47]\n"
        "v_mfma_f32_16x16x32_bf16 v[48:51], a[160:163], v[20:23], v[48:51]\n"
        "v_mfma_f32_16x16x32_bf16 v[52:55], a[164:167], v[20:23], v[52:55]\n"
        "ds_read_b128 v[20:23], v0 offset:30720\n"
        "v_mfma_f32_16x16x32_bf16 v[56:59], a[156:159], v[24:27], v[56:59]\n"
        "v_mfma_f32_16x16x32_bf16 v[60:63], a[160:163], v[24:27], v[60:63]\n"
        "v_mfma_f32_16x16x32_bf16 v[64:67], a[164:167], v[24:27], v[64:67]\n"
        "ds_read_b128 v[24:27], v0 offset:31744\n"
        "s_waitcnt lgkmcnt(2)\n"
        "v_mfma_f32_16x16x32_bf16 v[44:47], a[168:171], v[12:15], v[44:47]\n"
        "v_mfma_f32_16x16x32_bf16 v[48:51], a[172:175], v[12:15], v[48:51]\n"
        "s_waitcnt vmcnt(1)\n"
        "s_barrier\n"
        "v_mfma_f32_16x16x32_bf16 v[52:55], a[176:179], v[12:15], v[52:55]\n"
        "ds_read_b128 v[12:15], v8 offset:0\n"
        "v_mfma_f32_16x16x32_bf16 v[56:59], a[168:171], v[16:19], v[56:59]\n"
        "v_mfma_f32_16x16x32_bf16 v[60:63], a[172:175], v[16:19], v[60:63]\n"
        "v_mfma_f32_16x16x32_bf16 v[64:67], a[176:179], v[16:19], v[64:67]\n"
        "ds_read_b128 v[16:19], v8 offset:1024\n"
        "s_waitcnt lgkmcnt(2)\n"
        "v_mfma_f32_16x16x32_bf16 v[44:47], a[180:183], v[20:23], v[44:47]\n"
        "s_waitcnt vmcnt(0)\n"
        "v_cmp_gt_i32 vcc, s70, v4\n"
        "v_mfma_f32_16x16x32_bf16 v[48:51], a[184:187], v[20:23], v[48:51]\n"
        "s_and_b64 vcc, vcc, 15\n"
        "v_mfma_f32_16x16x32_bf16 v[52:55], a[188:191], v[20:23], v[52:55]\n"
        "ds_read_b128 v[20:23], v8 offset:2048\n"
        "v_mfma_f32_16x16x32_bf16 v[56:59], a[180:183], v[24:27], v[56:59]\n"
        "v_mfma_f32_16x16x32_bf16 v[60:63], a[184:187], v[24:27], v[60:63]\n"
        "v_mfma_f32_16x16x32_bf16 v[64:67], a[188:191], v[24:27], v[64:67]\n"
        "ds_read_b128 v[24:27], v8 offset:3072\n"
        "s_waitcnt lgkmcnt(2)\n"
        "s_cbranch_vccnz .Lop_c20_%=\n"
        "s_branch .Lop_c21_%=\n"
        ".Lop_c10_%=:\n"
        "v_mfma_f32_16x16x32_bf16 v[44:47], a[96:99], v[12:15], v[44:47]\n"
        "v_mfma_f32_16x16x32_bf16 v[48:51], a[100:103], v[12:15], v[48:51]\n"
        "v_mfma_f32_16x16x32_bf16 v[52:55], a[104:107], v[12:15], v[52:55]\n"
        "ds_read_b128 v[12:15], v0 offset:20480\n"
        "v_mfma_f32_16x16x32_bf16 v[56:59], a[96:99], v[16:19], v[56:59]\n"
        "v_mfma_f32_16x16x32_bf16 v[60:63], a[100:103], v[16:19], v[60:63]\n"
        "v_mfma_f32_16x16x32_bf16 v[64:67], a[104:107], v[16:19], v[64:67]\n"
        "ds_read_b128 v[16:19], v0 offset:21504\n"
        "s_waitcnt lgkmcnt(2)\n"
        "v_mfma_f32_16x16x32_bf16 v[44:47], a[108:111], v[20:23], v[44:47]\n"
        "v_mfma_f32_16x16x32_bf16 v[48:51], a[112:115], v[20:23], v[48:51]\n"
        "v_mfma_f32_16x16x32_bf16 v[52:55], a[116:119], v[20:23], v[52:55]\n"
        "ds_read_b128 v[20:23], v0 offset:22528\n"
        "v_mfma_f32_16x16x32_bf16 v[56:59], a[108:111], v[24:27], v[56:59]\n"
        "v_mfma_f32_16x16x32_bf16 v[60:63], a[112:115], v[24:27], v[60:63]\n"
        "v_mfma_f32_16x16x32_bf16 v[64:67], a[116:119], v[24:27], v[64:67]\n"
        "ds_read_b128 v[24:27], v0 offset:23552\n"
        "s_waitcnt lgkmcnt(2)\n"
        "v_mfma_f32_16x16x32_bf16 v[44:47], a[120:123], v[12:15], v[44:47]\n"
        "buffer_load_dword v4, v2, s[40:43], s76 offen sc1\n"
        "v_mfma_f32_16x16x32_bf16 v[48:51], a[124:127], v[12:15], v[48:51]\n"
        "v_mfma_f32_16x16x32_bf16 v[52:55], a[128:131], v[12:15], v[52:55]\n"
        "ds_read_b128 v[12:15], v0 offset:24576\n"
        "v_mfma_f32_16x16x32_bf16 v[56:59], a[120:123], v[16:19], v[56:59]\n"
        "v_mfma_f32_16x16x32_bf16 v[60:63], a[124:127], v[16:19], v[60:63]\n"
        "v_mfma_f32_16x16x32_bf16 v[64:67], a[128:131], v[16:19], v[64:67]\n"
        "ds_read_b128 v[16:19], v0 offset:25600\n"
        "s_waitcnt lgkmcnt(2)\n"
        "v_mfma_f32_16x16x32_bf16 v[44:47], a[132:135], v[20:23], v[44:47]\n"
        "v_mfma_f32_16x16x32_bf16 v[48:51], a[136:139], v[20:23], v[48:51]\n"
        "v_mfma_f32_16x16x32_bf16 v[52:55], a[140:143], v[20:23], v[52:55]\n"
        "ds_read_b128 v[20:23], v0 offset:26624\n"
        "v_mfma_f32_16x16x32_bf16 v[56:59], a[132:135], v[24:27], v[56:59]\n"
        "v_mfma_f32_16x16x32_bf16 v[60:63], a[136:139], v[24:27], v[60:63]\n"
        "v_mfma_f32_16x16x32_bf16 v[64:67], a[140:143], v[24:27], v[64:67]\n"
        "ds_read_b128 v[24:27], v0 offset:27648\n"
        "s_waitcnt lgkmcnt(2)\n"
        "v_mfma_f32_16x16x32_bf16 v[44:47], a[144:147], v[12:15], v[44:47]\n"
        "v_mfma_f32_16x16x32_bf16 v[48:51], a[148:151], v[12:15], v[48:51]\n"
        "v_mfma_f32_16x16x32_bf16 v[52:55], a[152:155], v[12:15], v[52:55]\n"
        "ds_read_b128 v[12:15], v0 offset:28672\n"
        "v_mfma_f32_16x16x32_bf16 v[56:59], a[144:147], v[16:19], v[56:59]\n"
        "v_mfma_f32_16x16x32_bf16 v[60:63], a[148:151], v[16:19], v[60:63]\n"
        "v_mfma_f32_16x16x32_bf16 v[64:67], a[152:155], v[16:19], v[64:67]\n"
        "ds_read_b128 v[16:19], v0 offset:29696\n"
        "s_waitcnt lgkmcnt(2)\n"
        "v_mfma_f32_16x16x32_bf16 v[44:47], a[156:159], v[20:23], v[44:47]\n"
        "v_mfma_f32_16x16x32_bf16 v[48:51], a[160:163], v[20:23], v[48:51]\n"
        "v_mfma_f32_16x16x32_bf16 v[52:55], a[164:167], v[20:23], v[52:55]\n"
        "ds_read_b128 v[20:23], v0 offset:30720\n"
        "v_mfma_f32_16x16x32_bf16 v[56:59], a[156:159], v[24:27], v[56:59]\n"
        "v_mfma_f32_16x16x32_bf16 v[60:63], a[160:163], v[24:27], v[60:63]\n"
        "v_mfma_f32_16x16x32_bf16 v[64:67], a[164:167], v[24:27], v[64:67]\n"
        "ds_read_b128 v[24:27], v0 offset:31744\n"
        "s_waitcnt lgkmcnt(2)\n"
        "v_mfma_f32_16x16x32_bf16 v[44:47], a[168:171], v[12:15], v[44:47]\n"
        "v_mfma_f32_16x16x32_bf16 v[48:51], a[172:175], v[12:15], v[48:51]\n"
        "v_mfma_f32_16x16x32_bf16 v[52:55], a[176:179], v[12:15], v[52:55]\n"
        "v_mfma_f32_16x16x32_bf16 v[56:59], a[168:171], v[16:19], v[56:59]\n"
        "v_mfma_f32_16x16x32_bf16 v[60:63], a[172:175], v[16:19], v[60:63]\n"
        "v_mfma_f32_16x16x32_bf16 v[64:67], a[176:179], v[16:19], v[64:67]\n"
        "s_waitcnt lgkmcnt(0)\n"
        "v_mfma_f32_16x16x32_bf16 v[44:47], a[180:183], v[20:23], v[44:47]\n"
        "v_mfma_f32_16x16x32_bf16 v[48:51], a[184:187], v[20:23], v[48:51]\n"
        "v_mfma_f32_16x16x32_bf16 v[52:55], a[188:191], v[20:23], v[52:55]\n"
        "v_mfma_f32_16x16x32_bf16 v[56:59], a[180:183], v[24:27], v[56:59]\n"
        "v_mfma_f32_16x16x32_bf16 v[60:63], a[184:187], v[24:27], v[60:63]\n"
        "v_mfma_f32_16x16x32_bf16 v[64:67], a[188:191], v[24:27], v[64:67]\n"
        "s_waitcnt vmcnt(0)\n"
        "v_cmp_gt_i32 vcc, s70, v4\n"
        "s_nop 0\n"
        "s_and_b64 vcc, vcc, 15\n"
        "s_cbranch_vccnz .Lop_late1_%=\n"
        "s_add_u32 m0, s61, 32768\n"
        "s_add_u32 s71, s71, 32768\n"
        "buffer_load_dwordx4 v1, s[40:43], s71 offen offset:0 sc1 lds\n"
        "buffer_load_dwordx4 v1, s[40:43], s71 offen offset:1024 sc1 lds\n"
        "buffer_load_dwordx4 v1, s[40:43], s71 offen offset:2048 sc1 lds\n"
        "buffer_load_dwordx4 v1, s[40:43], s71 offen offset:3072 sc1 lds\n"
        "s_branch .Lop_go1_%=\n"
        ".Lop_late1_%=:\n"
        "s_mov_b32 s64, 0\n"
        ".Lop_in1_top_%=:\n"
        "buffer_load_dword v4, v2, s[40:43], s76 offen sc1\n"
        "s_waitcnt vmcnt(0)\n"
        "v_cmp_gt_i32 vcc, s70, v4\n"
        "s_nop 0\n"
        "s_and_b64 vcc, vcc, 15\n"
        "s_cbranch_vccz .Lop_in1_ok_%=\n"
        "s_add_u32 s64, s64, 1\n"
        "s_and_b32 s65, s64, 255\n"
        "s_cbranch_scc1 .Lop_in1_go_%=\n"
        "s_mov_b32 s65, 40960\n"
        "s_cmp_lt_u32 s64, 1048576\n"
        "s_cbranch_scc1 .Lop_in1_chk_%=\n"
        "v_mov_b32 v4, 1\n"
        "buffer_store_dword v4, off, s[40:43], s65 sc1\n"
        "s_waitcnt vmcnt(0)\n"
        ".Lop_in1_chk_%=:\n"
        "buffer_load_dword v4, off, s[40:43], s65 sc1\n"
        "s_waitcnt vmcnt(0)\n"
        "v_readfirstlane_b32 s65, v4\n"
        "s_nop 0\n"
        "s_cmp_lg_u32 s65, 1\n"
        "s_cbranch_scc1 .Lop_in1_go_%=\n"
        "s_endpgm\n"
        ".Lop_in1_go_%=:\n"
        "s_sleep 1\n"
        "s_branch .Lop_in1_top_%=\n"
        ".Lop_in1_ok_%=:\n"
        "s_add_u32 m0, s61, 32768\n"
        "s_add_u32 s71, s71, 32768\n"
        "buffer_load_dwordx4 v1, s[40:43], s71 offen offset:0 sc1 lds\n"
        "buffer_load_dwordx4 v1, s[40:43], s71 offen offset:1024 sc1 lds\n"
        "buffer_load_dwordx4 v1, s[40:43], s71 offen offset:2048 sc1 lds\n"
        "buffer_load_dwordx4 v1, s[40:43], s71 offen offset:3072 sc1 lds\n"
        ".Lop_go1_%=:\n"
        "buffer_load_dword v4, v2, s[40:43], s76 offen sc1\n"
        "s_waitcnt vmcnt(0)\n"
        "s_barrier\n"
        "ds_read_b128 v[12:15], v8 offset:0\n"
        "ds_read_b128 v[16:19], v8 offset:1024\n"
        "ds_read_b128 v[20:23], v8 offset:2048\n"
        "ds_read_b128 v[24:27], v8 offset:3072\n"
        "v_cmp_gt_i32 vcc, s70, v4\n"
        "s_nop 0\n"
        "s_and_b64 vcc, vcc, 15\n"
        "s_waitcnt lgkmcnt(2)\n"
        "s_cbranch_vccnz .Lop_c20_%=\n"
        "s_branch .Lop_c21_%=\n"
        ".Lop_c21_%=:\n"
        "v_mfma_f32_16x16x32_bf16 v[44:47], a[192:195], v[12:15], v[44:47]\n"
        "s_add_u32 m0, s61, 49152\n"
        "s_add_u32 s71, s71, 32768\n"
        "v_mfma_f32_16x16x32_bf16 v[48:51], a[196:199], v[12:15], v[48:51]\n"
        "buffer_load_dwordx4 v1, s[40:43], s71 offen offset:0 sc1 lds\n"
        "v_mfma_f32_16x16x32_bf16 v[52:55], a[200:203], v[12:15], v[52:55]\n"
        "ds_read_b128 v[12:15], v8 offset:4096\n"
        "v_mfma_f32_16x16x32_bf16 v[56:59], a[192:195], v[16:19], v[56:59]\n"
        "v_mfma_f32_16x16x32_bf16 v[60:63], a[196:199], v[16:19], v[60:63]\n"
        "buffer_load_dwordx4 v1, s[40:43], s71 offen offset:1024 sc1 lds\n"
        "v_mfma_f32_16x16x32_bf16 v[64:67], a[200:203], v[16:19], v[64:67]\n"
        "ds_read_b128 v[16:19], v8 offset:5120\n"
        "s_waitcnt lgkmcnt(2)\n"
        "v_mfma_f32_16x16x32_bf16 v[44:47], a[204:207], v[20:23], v[44:47]\n"
        "v_mfma_f32_16x16x32_bf16 v[48:51], a[208:211], v[20:23], v[48:51]\n"
        "buffer_load_dwordx4 v1, s[40:43], s71 offen offset:2048 sc1 lds\n"
        "v_mfma_f32_16x16x32_bf16 v[52:55], a[212:215], v[20:23], v[52:55]\n"
        "ds_read_b128 v[20:23], v8 offset:6144\n"
        "v_mfma_f32_16x16x32_bf16 v[56:59], a[204:207], v[24:27], v[56:59]\n"
        "v_mfma_f32_16x16x32_bf16 v[60:63], a[208:211], v[24:27], v[60:63]\n"
        "buffer_load_dwordx4 v1, s[40:43], s71 offen offset:3072 sc1 lds\n"
        "s_add_u32 s69, s69, 1\n"
        "v_mfma_f32_16x16x32_bf16 v[64:67], a[212:215], v[24:27], v[64:67]\n"
        "ds_read_b128 v[24:27], v8 offset:7168\n"
        "s_waitcnt lgkmcnt(2)\n"
        "v_mfma_f32_16x16x32_bf16 v[44:47], a[216:219], v[12:15], v[44:47]\n"
        "s_cmp_lt_u32 s69, 64\n"
        "s_cselect_b32 s70, s69, 2147483647\n"
        "v_mfma_f32_16x16x32_bf16 v[48:51], a[220:223], v[12:15], v[48:51]\n"
        "buffer_load_dword v4, v2, s[40:43], s76 offen sc1\n"
        "v_mfma_f32_16x16x32_bf16 v[52:55], a[224:227], v[12:15], v[52:55]\n"
        "ds_read_b128 v[12:15], v8 offset:8192\n"
        "v_mfma_f32_16x16x32_bf16 v[56:59], a[216:219], v[16:19], v[56:59]\n"
        "v_mfma_f32_16x16x32_bf16 v[60:63], a[220:223], v[16:19], v[60:63]\n"
        "v_mfma_f32_16x16x32_bf16 v[64:67], a[224:227], v[16:19], v[64:67]\n"
        "ds_read_b128 v[16:19], v8 offset:9216\n"
        "s_waitcnt lgkmcnt(2)\n"
        "v_mfma_f32_16x16x32_bf16 v[44:47], a[228:231], v[20:23], v[44:47]\n"
        "v_mfma_f32_16x16x32_bf16 v[48:51], a[232:235], v[20:23], v[48:51]\n"
        "v_mfma_f32_16x16x32_bf16 v[52:55], a[236:239], v[20:23], v[52:55]\n"
        "ds_read_b128 v[20:23], v8 offset:10240\n"
        "v_mfma_f32_16x16x32_bf16 v[56:59], a[228:231], v[24:27], v[56:59]\n"
        "v_mfma_f32_16x16x32_bf16 v[60:63], a[232:235], v[24:27], v[60:63]\n"
        "v_mfma_f32_16x16x32_bf16 v[64:67], a[236:239], v[24:27], v[64:67]\n"
        "ds_read_b128 v[24:27], v8 offset:11264\n"
        "s_waitcnt lgkmcnt(2)\n"
        "v_mfma_f32_16x16x32_bf16 v[44:47], a[240:243], v[12:15], v[44:47]\n"
        "v_mfma_f32_16x16x32_bf16 v[48:51], a[244:247], v[12:15], v[48:51]\n"
        "v_mfma_f32_16x16x32_bf16 v[52:55], a[248:251], v[12:15], v[52:55]\n"
        "ds_read_b128 v[12:15], v8 offset:12288\n"
        "v_mfma_f32_16x16x32_bf16 v[56:59], a[240:243], v[16:19], v[56:59]\n"
        "v_mfma_f32_16x16x32_bf16 v[60:63], a[244:247], v[16:19], v[60:63]\n"
        "v_mfma_f32_16x16x32_bf16 v[64:67], a[248:251], v[16:19], v[64:67]\n"
        "ds_read_b128 v[16:19], v8 offset:13312\n"
        "s_waitcnt lgkmcnt(2)\n"
        "v_mfma_f32_16x16x32_bf16 v[44:47], a[252:255], v[20:23], v[44:47]\n"
        "v_mfma_f32_16x16x32_bf16 v[48:51], v[80:83], v[20:23], v[48:51]\n"
        "v_mfma_f32_16x16x32_bf16 v[52:55], v[84:87], v[20:23], v[52:55]\n"
        "ds_read_b128 v[20:23], v8 offset:14336\n"
        "v_mfma_f32_16x16x32_bf16 v[56:59], a[252:255], v[24:27], v[56:59]\n"
        "v_mfma_f32_16x16x32_bf16 v[60:63], v[80:83], v[24:27], v[60:63]\n"
        "v_mfma_f32_16x16x32_bf16 v[64:67], v[84:87], v[24:27], v[64:67]\n"
        "ds_read_b128 v[24:27], v8 offset:15360\n"
        "s_waitcnt lgkmcnt(2)\n"
        "v_mfma_f32_16x16x32_bf16 v[44:47], v[88:91], v[12:15], v[44:47]\n"
        "v_mfma_f32_16x16x32_bf16 v[48:51], v[92:95], v[12:15], v[48:51]\n"
        "s_waitcnt vmcnt(1)\n"
        "s_barrier\n"
        "v_mfma_f32_16x16x32_bf16 v[52:55], v[96:99], v[12:15], v[52:55]\n"
        "ds_read_b128 v[12:15], v8 offset:16384\n"
        "v_mfma_f32_16x16x32_bf16 v[56:59], v[88:91], v[16:19], v[56:59]\n"
        "v_mfma_f32_16x16x32_bf16 v[60:63], v[92:95], v[16:19], v[60:63]\n"
        "v_mfma_f32_16x16x32_bf16 v[64:67], v[96:99], v[16:19], v[64:67]\n"
        "ds_read_b128 v[16:19], v8 offset:17408\n"
        "s_waitcnt lgkmcnt(2)\n"
        "v_mfma_f32_16x16x32_bf16 v[44:47], v[100:103], v[20:23], v[44:47]\n"
        "s_waitcnt vmcnt(0)\n"
        "v_cmp_gt_i32 vcc, s70, v4\n"
        "v_mfma_f32_16x16x32_bf16 v[48:51], v[104:107], v[20:23], v[48:51]\n"
        "s_and_b64 vcc, vcc, 15\n"
        "v_mfma_f32_16x16x32_bf16 v[52:55], v[108:111], v[20:23], v[52:55]\n"
        "ds_read_b128 v[20:23], v8 offset:18432\n"
        "v_mfma_f32_16x16x32_bf16 v[56:59], v[100:103], v[24:27], v[56:59]\n"
        "v_mfma_f32_16x16x32_bf16 v[60:63], v[104:107], v[24:27], v[60:63]\n"
        "v_mfma_f32_16x16x32_bf16 v[64:67], v[108:111], v[24:27], v[64:67]\n"
        "ds_read_b128 v[24:27], v8 offset:19456\n"
        "s_waitcnt lgkmcnt(2)\n"
        "s_cbranch_vccnz .Lop_c30_%=\n"
        "s_branch .Lop_c31_%=\n"
        ".Lop_c20_%=:\n"
        "v_mfma_f32_16x16x32_bf16 v[44:47], a[192:195], v[12:15], v[44:47]\n"
        "v_mfma_f32_16x16x32_bf16 v[48:51], a[196:199], v[12:15], v[48:51]\n"
        "v_mfma_f32_16x16x32_bf16 v[52:55], a[200:203], v[12:15], v[52:55]\n"
        "ds_read_b128 v[12:15], v8 offset:4096\n"
        "v_mfma_f32_16x16x32_bf16 v[56:59], a[192:195], v[16:19], v[56:59]\n"
        "v_mfma_f32_16x16x32_bf16 v[60:63], a[196:199], v[16:19], v[60:63]\n"
        "v_mfma_f32_16x16x32_bf16 v[64:67], a[200:203], v[16:19], v[64:67]\n"
        "ds_read_b128 v[16:19], v8 offset:5120\n"
        "s_waitcnt lgkmcnt(2)\n"
        "v_mfma_f32_16x16x32_bf16 v[44:47], a[204:207], v[20:23], v[44:47]\n"
        "v_mfma_f32_16x16x32_bf16 v[48:51], a[208:211], v[20:23], v[48:51]\n"
        "v_mfma_f32_16x16x32_bf16 v[52:55], a[212:215], v[20:23], v[52:55]\n"
        "ds_read_b128 v[20:23], v8 offset:6144\n"
        "v_mfma_f32_16x16x32_bf16 v[56:59], a[204:207], v[24:27], v[56:59]\n"
        "v_mfma_f32_16x16x32_bf16 v[60:63], a[208:211], v[24:27], v[60:63]\n"
        "v_mfma_f32_16x16x32_bf16 v[64:67], a[212:215], v[24:27], v[64:67]\n"
        "ds_read_b128 v[24:27], v8 offset:7168\n"
        "s_waitcnt lgkmcnt(2)\n"
        "v_mfma_f32_16x16x32_bf16 v[44:47], a[216:219], v[12:15], v[44:47]\n"
        "buffer_load_dword v4, v2, s[40:43], s76 offen sc1\n"
        "v_mfma_f32_16x16x32_bf16 v[48:51], a[220:223], v[12:15], v[48:51]\n"
        "v_mfma_f32_16x16x32_bf16 v[52:55], a[224:227], v[12:15], v[52:55]\n"
        "ds_read_b128 v[12:15], v8 offset:8192\n"
        "v_mfma_f32_16x16x32_bf16 v[56:59], a[216:219], v[16:19], v[56:59]\n"
        "v_mfma_f32_16x16x32_bf16 v[60:63], a[220:223], v[16:19], v[60:63]\n"
        "v_mfma_f32_16x16x32_bf16 v[64:67], a[224:227], v[16:19], v[64:67]\n"
        "ds_read_b128 v[16:19], v8 offset:9216\n"
        "s_waitcnt lgkmcnt(2)\n"
        "v_mfma_f32_16x16x32_bf16 v[44:47], a[228:231], v[20:23], v[44:47]\n"
        "v_mfma_f32_16x16x32_bf16 v[48:51], a[232:235], v[20:23], v[48:51]\n"
        "v_mfma_f32_16x16x32_bf16 v[52:55], a[236:239], v[20:23], v[52:55]\n"
        "ds_read_b128 v[20:23], v8 offset:10240\n"
        "v_mfma_f32_16x16x32_bf16 v[56:59], a[228:231], v[24:27], v[56:59]\n"
        "v_mfma_f32_16x16x32_bf16 v[60:63], a[232:235], v[24:27], v[60:63]\n"
        "v_mfma_f32_16x16x32_bf16 v[64:67], a[236:239], v[24:27], v[64:67]\n"
        "ds_read_b128 v[24:27], v8 offset:11264\n"
        "s_waitcnt lgkmcnt(2)\n"
        "v_mfma_f32_16x16x32_bf16 v[44:47], a[240:243], v[12:15], v[44:47]\n"
        "v_mfma_f32_16x16x32_bf16 v[48:51], a[244:247], v[12:15], v[48:51]\n"
        "v_mfma_f32_16x16x32_bf16 v[52:55], a[248:251], v[12:15], v[52:55]\n"
        "ds_read_b128 v[12:15], v8 offset:12288\n"
        "v_mfma_f32_16x16x32_bf16 v[56:59], a[240:243], v[16:19], v[56:59]\n"
        "v_mfma_f32_16x16x32_bf16 v[60:63], a[244:247], v[16:19], v[60:63]\n"
        "v_mfma_f32_16x16x32_bf16 v[64:67], a[248:251], v[16:19], v[64:67]\n"
        "ds_read_b128 v[16:19], v8 offset:13312\n"
        "s_waitcnt lgkmcnt(2)\n"
        "v_mfma_f32_16x16x32_bf16 v[44:47], a[252:255], v[20:23], v[44:47]\n"
        "v_mfma_f32_16x16x32_bf16 v[48:51], v[80:83], v[20:23], v[48:51]\n"
        "v_mfma_f32_16x16x32_bf16 v[52:55], v[84:87], v[20:23], v[52:55]\n"
        "ds_read_b128 v[20:23], v8 offset:14336\n"
        "v_mfma_f32_16x16x32_bf16 v[56:59], a[252:255], v[24:27], v[56:59]\n"
        "v_mfma_f32_16x16x32_bf16 v[60:63], v[80:83], v[24:27], v[60:63]\n"
        "v_mfma_f32_16x16x32_bf16 v[64:67], v[84:87], v[24:27], v[64:67]\n"
        "ds_read_b128 v[24:27], v8 offset:15360\n"
        "s_waitcnt lgkmcnt(2)\n"
        "v_mfma_f32_16x16x32_bf16 v[44:47], v[88:91], v[12:15], v[44:47]\n"
        "v_mfma_f32_16x16x32_bf16 v[48:51], v[92:95], v[12:15], v[48:51]\n"
        "v_mfma_f32_16x16x32_bf16 v[52:55], v[96:99], v[12:15], v[52:55]\n"
        "v_mfma_f32_16x16x32_bf16 v[56:59], v[88:91], v[16:19], v[56:59]\n"
        "v_mfma_f32_16x16x32_bf16 v[60:63], v[92:95], v[16:19], v[60:63]\n"
        "v_mfma_f32_16x16x32_bf16 v[64:67], v[96:99], v[16:19], v[64:67]\n"
        "s_waitcnt lgkmcnt(0)\n"
        "v_mfma_f32_16x16x32_bf16 v[44:47], v[100:103], v[20:23], v[44:47]\n"
        "v_mfma_f32_16x16x32_bf16 v[48:51], v[104:107], v[20:23], v[48:51]\n"
        "v_mfma_f32_16x16x32_bf16 v[52:55], v[108:111], v[20:23], v[52:55]\n"
        "v_mfma_f32_16x16x32_bf16 v[56:59], v[100:103], v[24:27], v[56:59]\n"
        "v_mfma_f32_16x16x32_bf16 v[60:63], v[104:107], v[24:27], v[60:63]\n"
        "v_mfma_f32_16x16x32_bf16 v[64:67], v[108:111], v[24:27], v[64:67]\n"
        "s_waitcnt vmcnt(0)\n"
        "v_cmp_gt_i32 vcc, s70, v4\n"
        "s_nop 0\n"
        "s_and_b64 vcc, vcc, 15\n"
        "s_cbranch_vccnz .Lop_late2_%=\n"
        "s_add_u32 m0, s61, 49152\n"
        "s_add_u32 s71, s71, 32768\n"
        "buffer_load_dwordx4 v1, s[40:43], s71 offen offset:0 sc1 lds\n"
        "buffer_load_dwordx4 v1, s[40:43], s71 offen offset:1024 sc1 lds\n"
        "buffer_load_dwordx4 v1, s[40:43], s71 offen offset:2048 sc1 lds\n"
        "buffer_load_dwordx4 v1, s[40:43], s71 offen offset:3072 sc1 lds\n"
        "s_add_u32 s69, s69, 1\n"
        "s_cmp_lt_u32 s69, 64\n"
        "s_cselect_b32 s70, s69, 2147483647\n"
        "s_branch .Lop_go2_%=\n"
        ".Lop_late2_%=:\n"
        "s_mov_b32 s64, 0\n"
        ".Lop_in2_top_%=:\n"
        "buffer_load_dword v4, v2, s[40:43], s76 offen sc1\n"
        "s_waitcnt vmcnt(0)\n"
        "v_cmp_gt_i32 vcc, s70, v4\n"
        "s_nop 0\n"
        "s_and_b64 vcc, vcc, 15\n"
        "s_cbranch_vccz .Lop_in2_ok_%=\n"
        "s_add_u32 s64, s64, 1\n"
        "s_and_b32 s65, s64, 255\n"
        "s_cbranch_scc1 .Lop_in2_go_%=\n"
        "s_mov_b32 s65, 40960\n"
        "s_cmp_lt_u32 s64, 1048576\n"
        "s_cbranch_scc1 .Lop_in2_chk_%=\n"
        "v_mov_b32 v4, 1\n"
        "buffer_store_dword v4, off, s[40:43], s65 sc1\n"
        "s_waitcnt vmcnt(0)\n"
        ".Lop_in2_chk_%=:\n"
        "buffer_load_dword v4, off, s[40:43], s65 sc1\n"
        "s_waitcnt vmcnt(0)\n"
        "v_readfirstlane_b32 s65, v4\n"
        "s_nop 0\n"
        "s_cmp_lg_u32 s65, 1\n"
        "s_cbranch_scc1 .Lop_in2_go_%=\n"
        "s_endpgm\n"
        ".Lop_in2_go_%=:\n"
        "s_sleep 1\n"
        "s_branch .Lop_in2_top_%=\n"
        ".Lop_in2_ok_%=:\n"
        "s_add_u32 m0, s61, 49152\n"
        "s_add_u32 s71, s71, 32768\n"
        "buffer_load_dwordx4 v1, s[40:43], s71 offen offset:0 sc1 lds\n"
        "buffer_load_dwordx4 v1, s[40:43], s71 offen offset:1024 sc1 lds\n"
        "buffer_load_dwordx4 v1, s[40:43], s71 offen offset:2048 sc1 lds\n"
        "buffer_load_dwordx4 v1, s[40:43], s71 offen offset:3072 sc1 lds\n"
        "s_add_u32 s69, s69, 1\n"
        "s_cmp_lt_u32 s69, 64\n"
        "s_cselect_b32 s70, s69, 2147483647\n"
        ".Lop_go2_%=:\n"
        "buffer_load_dword v4, v2, s[40:43], s76 offen sc1\n"
        "s_waitcnt vmcnt(0)\n"
        "s_barrier\n"
        "ds_read_b128 v[12:15], v8 offset:16384\n"
        "ds_read_b128 v[16:19], v8 offset:17408\n"
        "ds_read_b128 v[20:23], v8 offset:18432\n"
        "ds_read_b128 v[24:27], v8 offset:19456\n"
        "v_cmp_gt_i32 vcc, s70, v4\n"
        "s_nop 0\n"
        "s_and_b64 vcc, vcc, 15\n"
        "s_waitcnt lgkmcnt(2)\n"
        "s_cbranch_vccnz .Lop_c30_%=\n"
        "s_branch .Lop_c31_%=\n"
        ".Lop_c31_%=:\n"
        "v_mfma_f32_16x16x32_bf16 v[44:47], v[112:115], v[12:15], v[44:47]\n"
        "s_add_u32 m0, s61, 0\n"
        "v_mfma_f32_16x16x32_bf16 v[48:51], v[116:119], v[12:15], v[48:51]\n"
        "s_bitcmp1_b64 s[52:53], s69\n"
        "s_cselect_b32 s65, 8192, 0\n"
        "v_mfma_f32_16x16x32_bf16 v[52:55], v[120:123], v[12:15], v[52:55]\n"
        "ds_read_b128 v[12:15], v8 offset:20480\n"
        "s_lshl_b32 s71, s69, 21\n"
        "v_mfma_f32_16x16x32_bf16 v[56:59], v[112:115], v[16:19], v[56:59]\n"
        "s_add_u32 s71, s71, s65\n"
        "s_add_u32 s71, s71, s58\n"
        "v_mfma_f32_16x16x32_bf16 v[60:63], v[116:119], v[16:19], v[60:63]\n"
        "buffer_load_dwordx4 v1, s[40:43], s71 offen offset:0 sc1 lds\n"
        "buffer_load_dwordx4 v1, s[40:43], s71 offen offset:1024 sc1 lds\n"
        "v_mfma_f32_16x16x32_bf16 v[64:67], v[120:123], v[16:19], v[64:67]\n"
        "ds_read_b128 v[16:19], v8 offset:21504\n"
        "s_waitcnt lgkmcnt(2)\n"
        "v_mfma_f32_16x16x32_bf16 v[44:47], v[124:127], v[20:23], v[44:47]\n"
        "v_mfma_f32_16x16x32_bf16 v[48:51], v[128:131], v[20:23], v[48:51]\n"
        "buffer_load_dwordx4 v1, s[40:43], s71 offen offset:2048 sc1 lds\n"
        "v_mfma_f32_16x16x32_bf16 v[52:55], v[132:135], v[20:23], v[52:55]\n"
        "ds_read_b128 v[20:23], v8 offset:22528\n"
        "v_mfma_f32_16x16x32_bf16 v[56:59], v[124:127], v[24:27], v[56:59]\n"
        "v_mfma_f32_16x16x32_bf16 v[60:63], v[128:131], v[24:27], v[60:63]\n"
        "buffer_load_dwordx4 v1, s[40:43], s71 offen offset:3072 sc1 lds\n"
        "v_mfma_f32_16x16x32_bf16 v[64:67], v[132:135], v[24:27], v[64:67]\n"
        "ds_read_b128 v[24:27], v8 offset:23552\n"
        "s_waitcnt lgkmcnt(2)\n"
        "v_mfma_f32_16x16x32_bf16 v[44:47], v[136:139], v[12:15], v[44:47]\n"
        "buffer_load_dword v4, v2, s[40:43], s76 offen sc1\n"
        "v_mfma_f32_16x16x32_bf16 v[48:51], v[140:143], v[12:15], v[48:51]\n"
        "v_mfma_f32_16x16x32_bf16 v[52:55], v[144:147], v[12:15], v[52:55]\n"
        "ds_read_b128 v[12:15], v8 offset:24576\n"
        "v_mfma_f32_16x16x32_bf16 v[56:59], v[136:139], v[16:19], v[56:59]\n"
        "v_mfma_f32_16x16x32_bf16 v[60:63], v[140:143], v[16:19], v[60:63]\n"
        "v_mfma_f32_16x16x32_bf16 v[64:67], v[144:147], v[16:19], v[64:67]\n"
        "ds_read_b128 v[16:19], v8 offset:25600\n"
        "s_waitcnt lgkmcnt(2)\n"
        "v_mfma_f32_16x16x32_bf16 v[44:47], v[148:151], v[20:23], v[44:47]\n"
        "v_mfma_f32_16x16x32_bf16 v[48:51], v[152:155], v[20:23], v[48:51]\n"
        "v_mfma_f32_16x16x32_bf16 v[52:55], v[156:159], v[20:23], v[52:55]\n"
        "ds_read_b128 v[20:23], v8 offset:26624\n"
        "v_mfma_f32_16x16x32_bf16 v[56:59], v[148:151], v[24:27], v[56:59]\n"
        "v_mfma_f32_16x16x32_bf16 v[60:63], v[152:155], v[24:27], v[60:63]\n"
        "v_mfma_f32_16x16x32_bf16 v[64:67], v[156:159], v[24:27], v[64:67]\n"
        "ds_read_b128 v[24:27], v8 offset:27648\n"
        "s_waitcnt lgkmcnt(2)\n"
        "v_mfma_f32_16x16x32_bf16 v[44:47], v[160:163], v[12:15], v[44:47]\n"
        "v_mfma_f32_16x16x32_bf16 v[48:51], v[164:167], v[12:15], v[48:51]\n"
        "v_mfma_f32_16x16x32_bf16 v[52:55], v[168:171], v[12:15], v[52:55]\n"
        "ds_read_b128 v[12:15], v8 offset:28672\n"
        "v_mfma_f32_16x16x32_bf16 v[56:59], v[160:163], v[16:19], v[56:59]\n"
        "v_mfma_f32_16x16x32_bf16 v[60:63], v[164:167], v[16:19], v[60:63]\n"
        "v_mfma_f32_16x16x32_bf16 v[64:67], v[168:171], v[16:19], v[64:67]\n"
        "ds_read_b128 v[16:19], v8 offset:29696\n"
        "s_waitcnt lgkmcnt(2)\n"
        "v_mfma_f32_16x16x32_bf16 v[44:47], v[172:175], v[20:23], v[44:47]\n"
        "v_mfma_f32_16x16x32_bf16 v[48:51], v[176:179], v[20:23], v[48:51]\n"
        "v_mfma_f32_16x16x32_bf16 v[52:55], v[180:183], v[20:23], v[52:55]\n"
        "ds_read_b128 v[20:23], v8 offset:30720\n"
        "v_mfma_f32_16x16x32_bf16 v[56:59], v[172:175], v[24:27], v[56:59]\n"
        "v_mfma_f32_16x16x32_bf16 v[60:63], v[176:179], v[24:27], v[60:63]\n"
        "v_mfma_f32_16x16x32_bf16 v[64:67], v[180:183], v[24:27], v[64:67]\n"
        "ds_read_b128 v[24:27], v8 offset:31744\n"
        "s_waitcnt lgkmcnt(2)\n"
        "v_mfma_f32_16x16x32_bf16 v[44:47], v[184:187], v[12:15], v[44:47]\n"
        "v_mfma_f32_16x16x32_bf16 v[48:51], v[188:191], v[12:15], v[48:51]\n"
        "s_waitcnt vmcnt(1)\n"
        "s_barrier\n"
        "v_mfma_f32_16x16x32_bf16 v[52:55], v[192:195], v[12:15], v[52:55]\n"
        "ds_read_b128 v[12:15], v0 offset:0\n"
        "v_mfma_f32_16x16x32_bf16 v[56:59], v[184:187], v[16:19], v[56:59]\n"
        "v_mfma_f32_16x16x32_bf16 v[60:63], v[188:191], v[16:19], v[60:63]\n"
        "v_mfma_f32_16x16x32_bf16 v[64:67], v[192:195], v[16:19], v[64:67]\n"
        "ds_read_b128 v[16:19], v0 offset:1024\n"
        "s_waitcnt lgkmcnt(2)\n"
        "v_mfma_f32_16x16x32_bf16 v[44:47], v[196:199], v[20:23], v[44:47]\n"
        "s_waitcnt vmcnt(0)\n"
        "v_cmp_gt_i32 vcc, s70, v4\n"
        "v_mfma_f32_16x16x32_bf16 v[48:51], v[200:203], v[20:23], v[48:51]\n"
        "s_and_b64 vcc, vcc, 15\n"
        "v_mfma_f32_16x16x32_bf16 v[52:55], v[204:207], v[20:23], v[52:55]\n"
        "ds_read_b128 v[20:23], v0 offset:2048\n"
        "v_cvt_pk_bf16_f32 v68, v44, v45\n"
        "v_cvt_pk_bf16_f32 v69, v46, v47\n"
        "v_mfma_f32_16x16x32_bf16 v[56:59], v[196:199], v[24:27], v[56:59]\n"
        "v_cvt_pk_bf16_f32 v70, v48, v49\n"
        "v_cvt_pk_bf16_f32 v71, v50, v51\n"
        "v_mfma_f32_16x16x32_bf16 v[60:63], v[200:203], v[24:27], v[60:63]\n"
        "v_cvt_pk_bf16_f32 v76, v52, v53\n"
        "v_cvt_pk_bf16_f32 v77, v54, v55\n"
        "v_mfma_f32_16x16x32_bf16 v[64:67], v[204:207], v[24:27], v[64:67]\n"
        "ds_read_b128 v[24:27], v0 offset:3072\n"
        "s_waitcnt lgkmcnt(2)\n"
        "v_cvt_pk_bf16_f32 v72, v56, v57\n"
        "v_cvt_pk_bf16_f32 v73, v58, v59\n"
        "s_cbranch_vccnz .Lop_c00_%=\n"
        "s_branch .Lop_c01_%=\n"
        ".Lop_c30_%=:\n"
        "v_mfma_f32_16x16x32_bf16 v[44:47], v[112:115], v[12:15], v[44:47]\n"
        "v_mfma_f32_16x16x32_bf16 v[48:51], v[116:119], v[12:15], v[48:51]\n"
        "v_mfma_f32_16x16x32_bf16 v[52:55], v[120:123], v[12:15], v[52:55]\n"
        "ds_read_b128 v[12:15], v8 offset:20480\n"
        "v_mfma_f32_16x16x32_bf16 v[56:59], v[112:115], v[16:19], v[56:59]\n"
        "v_mfma_f32_16x16x32_bf16 v[60:63], v[116:119], v[16:19], v[60:63]\n"
        "v_mfma_f32_16x16x32_bf16 v[64:67], v[120:123], v[16:19], v[64:67]\n"
        "ds_read_b128 v[16:19], v8 offset:21504\n"
        "s_waitcnt lgkmcnt(2)\n"
        "v_mfma_f32_16x16x32_bf16 v[44:47], v[124:127], v[20:23], v[44:47]\n"
        "v_mfma_f32_16x16x32_bf16 v[48:51], v[128:131], v[20:23], v[48:51]\n"
        "v_mfma_f32_16x16x32_bf16 v[52:55], v[132:135], v[20:23], v[52:55]\n"
        "ds_read_b128 v[20:23], v8 offset:22528\n"
        "v_mfma_f32_16x16x32_bf16 v[56:59], v[124:127], v[24:27], v[56:59]\n"
        "v_mfma_f32_16x16x32_bf16 v[60:63], v[128:131], v[24:27], v[60:63]\n"
        "v_mfma_f32_16x16x32_bf16 v[64:67], v[132:135], v[24:27], v[64:67]\n"
        "ds_read_b128 v[24:27], v8 offset:23552\n"
        "s_waitcnt lgkmcnt(2)\n"
        "v_mfma_f32_16x16x32_bf16 v[44:47], v[136:139], v[12:15], v[44:47]\n"
        "buffer_load_dword v4, v2, s[40:43], s76 offen sc1\n"
        "v_mfma_f32_16x16x32_bf16 v[48:51], v[140:143], v[12:15], v[48:51]\n"
        "v_mfma_f32_16x16x32_bf16 v[52:55], v[144:147], v[12:15], v[52:55]\n"
        "ds_read_b128 v[12:15], v8 offset:24576\n"
        "v_mfma_f32_16x16x32_bf16 v[56:59], v[136:139], v[16:19], v[56:59]\n"
        "v_mfma_f32_16x16x32_bf16 v[60:63], v[140:143], v[16:19], v[60:63]\n"
        "v_mfma_f32_16x16x32_bf16 v[64:67], v[144:147], v[16:19], v[64:67]\n"
        "ds_read_b128 v[16:19], v8 offset:25600\n"
        "s_waitcnt lgkmcnt(2)\n"
        "v_mfma_f32_16x16x32_bf16 v[44:47], v[148:151], v[20:23], v[44:47]\n"
        "v_mfma_f32_16x16x32_bf16 v[48:51], v[152:155], v[20:23], v[48:51]\n"
        "v_mfma_f32_16x16x32_bf16 v[52:55], v[156:159], v[20:23], v[52:55]\n"
        "ds_read_b128 v[20:23], v8 offset:26624\n"
        "v_mfma_f32_16x16x32_bf16 v[56:59], v[148:151], v[24:27], v[56:59]\n"
        "v_mfma_f32_16x16x32_bf16 v[60:63], v[152:155], v[24:27], v[60:63]\n"
        "v_mfma_f32_16x16x32_bf16 v[64:67], v[156:159], v[24:27], v[64:67]\n"
        "ds_read_b128 v[24:27], v8 offset:27648\n"
        "s_waitcnt lgkmcnt(2)\n"
        "v_mfma_f32_16x16x32_bf16 v[44:47], v[160:163], v[12:15], v[44:47]\n"
        "v_mfma_f32_16x16x32_bf16 v[48:51], v[164:167], v[12:15], v[48:51]\n"
        "v_mfma_f32_16x16x32_bf16 v[52:55], v[168:171], v[12:15], v[52:55]\n"
        "ds_read_b128 v[12:15], v8 offset:28672\n"
        "v_mfma_f32_16x16x32_bf16 v[56:59], v[160:163], v[16:19], v[56:59]\n"
        "v_mfma_f32_16x16x32_bf16 v[60:63], v[164:167], v[16:19], v[60:63]\n"
        "v_mfma_f32_16x16x32_bf16 v[64:67], v[168:171], v[16:19], v[64:67]\n"
        "ds_read_b128 v[16:19], v8 offset:29696\n"
        "s_waitcnt lgkmcnt(2)\n"
        "v_mfma_f32_16x16x32_bf16 v[44:47], v[172:175], v[20:23], v[44:47]\n"
        "v_mfma_f32_16x16x32_bf16 v[48:51], v[176:179], v[20:23], v[48:51]\n"
        "v_mfma_f32_16x16x32_bf16 v[52:55], v[180:183], v[20:23], v[52:55]\n"
        "ds_read_b128 v[20:23], v8 offset:30720\n"
        "v_mfma_f32_16x16x32_bf16 v[56:59], v[172:175], v[24:27], v[56:59]\n"
        "v_mfma_f32_16x16x32_bf16 v[60:63], v[176:179], v[24:27], v[60:63]\n"
        "v_mfma_f32_16x16x32_bf16 v[64:67], v[180:183], v[24:27], v[64:67]\n"
        "ds_read_b128 v[24:27], v8 offset:31744\n"
        "s_waitcnt lgkmcnt(2)\n"
        "v_mfma_f32_16x16x32_bf16 v[44:47], v[184:187], v[12:15], v[44:47]\n"
        "v_mfma_f32_16x16x32_bf16 v[48:51], v[188:191], v[12:15], v[48:51]\n"
        "v_mfma_f32_16x16x32_bf16 v[52:55], v[192:195], v[12:15], v[52:55]\n"
        "v_mfma_f32_16x16x32_bf16 v[56:59], v[184:187], v[16:19], v[56:59]\n"
        "v_mfma_f32_16x16x32_bf16 v[60:63], v[188:191], v[16:19], v[60:63]\n"
        "v_mfma_f32_16x16x32_bf16 v[64:67], v[192:195], v[16:19], v[64:67]\n"
        "s_waitcnt lgkmcnt(0)\n"
        "v_mfma_f32_16x16x32_bf16 v[44:47], v[196:199], v[20:23], v[44:47]\n"
        "v_mfma_f32_16x16x32_bf16 v[48:51], v[200:203], v[20:23], v[48:51]\n"
        "v_mfma_f32_16x16x32_bf16 v[52:55], v[204:207], v[20:23], v[52:55]\n"
        "v_cvt_pk_bf16_f32 v68, v44, v45\n"
        "v_cvt_pk_bf16_f32 v69, v46, v47\n"
        "v_mfma_f32_16x16x32_bf16 v[56:59], v[196:199], v[24:27], v[56:59]\n"
        "v_cvt_pk_bf16_f32 v70, v48, v49\n"
        "v_cvt_pk_bf16_f32 v71, v50, v51\n"
        "v_mfma_f32_16x16x32_bf16 v[60:63], v[200:203], v[24:27], v[60:63]\n"
        "v_cvt_pk_bf16_f32 v76, v52, v53\n"
        "v_cvt_pk_bf16_f32 v77, v54, v55\n"
        "v_mfma_f32_16x16x32_bf16 v[64:67], v[204:207], v[24:27], v[64:67]\n"
        "v_cvt_pk_bf16_f32 v72, v56, v57\n"
        "v_cvt_pk_bf16_f32 v73, v58, v59\n"
        "s_nop 5\n"
        "v_cvt_pk_bf16_f32 v74, v60, v61\n"
        "v_cvt_pk_bf16_f32 v75, v62, v63\n"
        "v_cvt_pk_bf16_f32 v78, v64, v65\n"
        "v_cvt_pk_bf16_f32 v79, v66, v67\n"
        "buffer_store_dwordx4 v[68:71], v6, s[40:43], s72 offen sc1\n"
        "s_add_u32 s66, s72, 147456\n"
        "buffer_store_dwordx4 v[72:75], v6, s[40:43], s66 offen sc1\n"
        "v_permlane16_swap_b32 v76, v78\n"
        "v_permlane16_swap_b32 v77, v79\n"
        "buffer_store_dwordx4 v[76:79], v7, s[40:43], s72 offen sc1\n"
        "s_waitcnt vmcnt(3)\n"
        "v_cmp_gt_i32 vcc, s70, v4\n"
        "s_nop 0\n"
        "s_and_b64 vcc, vcc, 15\n"
        "s_cbranch_vccnz .Lop_late3_%=\n"
        "s_add_u32 m0, s61, 0\n"
        "s_bitcmp1_b64 s[52:53], s69\n"
        "s_cselect_b32 s65, 8192, 0\n"
        "s_lshl_b32 s71, s69, 21\n"
        "s_add_u32 s71, s71, s65\n"
        "s_add_u32 s71, s71, s58\n"
        "buffer_load_dwordx4 v1, s[40:43], s71 offen offset:0 sc1 lds\n"
        "buffer_load_dwordx4 v1, s[40:43], s71 offen offset:1024 sc1 lds\n"
        "buffer_load_dwordx4 v1, s[40:43], s71 offen offset:2048 sc1 lds\n"
        "buffer_load_dwordx4 v1, s[40:43], s71 offen offset:3072 sc1 lds\n"
        "s_waitcnt vmcnt(4)\n"
        "v_mov_b32 v5, s74\n"
        "buffer_store_dword v5, v3, s[40:43], s73 offen sc1\n"
        "s_branch .Lop_go3_%=\n"
        ".Lop_late3_%=:\n"
        "s_waitcnt vmcnt(0)\n"
        "v_mov_b32 v5, s74\n"
        "buffer_store_dword v5, v3, s[40:43], s73 offen sc1\n"
        "s_cmp_eq_u32 s68, 63\n"
        "s_cbranch_scc0 .Lop_wait3b_%=\n"
        "s_endpgm\n"
        ".Lop_wait3_%=:\n"
        "s_mov_b32 s70, 0\n"
        ".Lop_wait3b_%=:\n"
        "s_mov_b32 s64, 0\n"
        ".Lop_in3_top_%=:\n"
        "buffer_load_dword v4, v2, s[40:43], s76 offen sc1\n"
        "s_waitcnt vmcnt(0)\n"
        "v_cmp_gt_i32 vcc, s70, v4\n"
        "s_nop 0\n"
        "s_and_b64 vcc, vcc, 15\n"
        "s_cbranch_vccz .Lop_in3_ok_%=\n"
        "s_add_u32 s64, s64, 1\n"
        "s_and_b32 s65, s64, 255\n"
        "s_cbranch_scc1 .Lop_in3_go_%=\n"
        "s_mov_b32 s65, 40960\n"
        "s_cmp_lt_u32 s64, 1048576\n"
        "s_cbranch_scc1 .Lop_in3_chk_%=\n"
        "v_mov_b32 v4, 1\n"
        "buffer_store_dword v4, off, s[40:43], s65 sc1\n"
        "s_waitcnt vmcnt(0)\n"
        ".Lop_in3_chk_%=:\n"
        "buffer_load_dword v4, off, s[40:43], s65 sc1\n"
        "s_waitcnt vmcnt(0)\n"
        "v_readfirstlane_b32 s65, v4\n"
        "s_nop 0\n"
        "s_cmp_lg_u32 s65, 1\n"
        "s_cbranch_scc1 .Lop_in3_go_%=\n"
        "s_endpgm\n"
        ".Lop_in3_go_%=:\n"
        "s_sleep 1\n"
        "s_branch .Lop_in3_top_%=\n"
        ".Lop_in3_ok_%=:\n"
        "s_add_u32 m0, s61, 0\n"
        "s_bitcmp1_b64 s[52:53], s69\n"
        "s_cselect_b32 s65, 8192, 0\n"
        "s_lshl_b32 s71, s69, 21\n"
        "s_add_u32 s71, s71, s65\n"
        "s_add_u32 s71, s71, s58\n"
        "buffer_load_dwordx4 v1, s[40:43], s71 offen offset:0 sc1 lds\n"
        "buffer_load_dwordx4 v1, s[40:43], s71 offen offset:1024 sc1 lds\n"
        "buffer_load_dwordx4 v1, s[40:43], s71 offen offset:2048 sc1 lds\n"
        "buffer_load_dwordx4 v1, s[40:43], s71 offen offset:3072 sc1 lds\n"
        ".Lop_go3_%=:\n"
        "s_mov_b32 s72, 241172480\n"
        "s_mov_b32 s73, 241172480\n"
        "buffer_load_dword v4, v2, s[40:43], s76 offen sc1\n"
        "s_waitcnt vmcnt(0)\n"
        "s_barrier\n"
        "ds_read_b128 v[12:15], v0 offset:0\n"
        "ds_read_b128 v[16:19], v0 offset:1024\n"
        "ds_read_b128 v[20:23], v0 offset:2048\n"
        "ds_read_b128 v[24:27], v0 offset:3072\n"
        "v_cmp_gt_i32 vcc, s70, v4\n"
        "s_nop 0\n"
        "s_and_b64 vcc, vcc, 15\n"
        "s_waitcnt lgkmcnt(2)\n"
        "s_cbranch_vccnz .Lop_c00_%=\n"
        "s_branch .Lop_c01_%=\n"
            :: "s"(p.ws), "s"(p.Wo), "s"(p.Wo), "s"(p.Wo), "s"(pair(w, delay)), "s"(pair(x4, pr)),
               "s"(pair(PAGE + spread(A_Q) + (8 * kap + (w >> 1)) * 16384 + (w & 1) * 4096, 65 * PAGE + spread(B_P3))), "s"(pair(flag(F_OP + fl), w * 4096)),
               "v"(pair(lane * 16, lane * 16)), "v"(pair(lane < 4 ? flag(F_AT + (8 * kap + 2 * lane + (w >> 1)) * 6 + 2 * nu + (w & 1)) : OOB, fo4)), "v"(pair(tr + 64 + 8 * pp, 0)),
               "v"(pair(s01, s2)), "v"(pair(vo[0], vo[1])), "v"(pair(vo[2], tr + 16 * pp))
            : "v0", "v1", "v2", "v3", "v4", "v5", "v6", "v7", "v8", "v9", "v10", "v11", "v12", "v13", "v14", "v15", "v16", "v17", "v18", "v19", "v20", "v21", "v22", "v23", "v24", "v25", "v26", "v27", "v28", "v29", "v30", "v31", "v32", "v33", "v34", "v35", "v36", "v37", "v38", "v39", "v40", "v41", "v42", "v43", "v44", "v45", "v46", "v47", "v48", "v49", "v50", "v51", "v52", "v53", "v54", "v55", "v56", "v57", "v58", "v59", "v60", "v61", "v62", "v63", "v64", "v65", "v66", "v67", "v68", "v69", "v70", "v71", "v72", "v73", "v74", "v75", "v76", "v77", "v78", "v79", "v255", "a255", "s34", "s35", "s36", "s37", "s38", "s39", "s40", "s41", "s42", "s43", "s44", "s45", "s46", "s47", "s48", "s49", "s50", "s51", "s52", "s53", "s54", "s55", "s56", "s57", "s58", "s59", "s60", "s61", "s62", "s63", "s64", "s65", "s66", "s67", "s68", "s69", "s70", "s71", "s72", "s73", "s74", "s75", "s76", "s77", "s78", "s79", "s80", "s81", "s82", "s83", "s84", "s85", "s86", "s87", "s88", "s89", "s90", "s91", "s92", "s93", "s94", "s95", "s96", "s97", "s98", "s99", "vcc", "scc", "memory");
    } else {
        asm volatile(
        "s_nop 4\n"
        "s_mov_b64 s[40:41], %0\n"
        "s_and_b32 s41, s41, 0xffff\n"
        "s_mov_b32 s42, 236978176\n"
        "s_mov_b32 s43, 0x20000\n"
        "s_mov_b64 s[48:49], %4\n"
        "s_mov_b64 s[56:57], %5\n"
        "s_mov_b64 s[58:59], %6\n"
        "s_mov_b64 s[60:61], %7\n"
        "v_mov_b64 v[0:1], %8\n"
        "v_mov_b64 v[2:3], %9\n"
        "v_mov_b64 v[4:5], %10\n"
        "v_mov_b64 v[6:7], %11\n"
        "v_mov_b64 v[8:9], %12\n"
        "v_mov_b64 v[10:11], %13\n"
        "s_mov_b32 s47, 0x20000\n"
        "s_mul_i32 s79, s48, 40960\n"
        ".Lf2_start_%=:\n"
        "s_sub_u32 s49, s49, 1\n"
        "s_cbranch_scc1 .Lf2_start2_%=\n"
        "s_sleep 16\n"
        "s_branch .Lf2_start_%=\n"
        ".Lf2_start2_%=:\n"
        "s_mov_b64 s[44:45], %1\n"
        "s_and_b32 s45, s45, 0xffff\n"
        "s_mov_b32 s46, 5308416\n"
        "s_add_u32 m0, s79, 0\n"
        "s_mov_b32 s65, 0\n"
        "buffer_load_dwordx4 v8, s[44:47], s65 offen lds\n"
        "buffer_load_dwordx4 v9, s[44:47], s65 offen offset:1024 lds\n"
        "buffer_load_dwordx4 v10, s[44:47], s65 offen offset:2048 lds\n"
        "s_add_u32 m0, s79, 3072\n"
        "s_mov_b32 s65, 110592\n"
        "buffer_load_dwordx4 v8, s[44:47], s65 offen lds\n"
        "buffer_load_dwordx4 v9, s[44:47], s65 offen offset:1024 lds\n"
        "buffer_load_dwordx4 v10, s[44:47], s65 offen offset:2048 lds\n"
        "s_add_u32 m0, s79, 6144\n"
        "s_mov_b32 s65, 221184\n"
        "buffer_load_dwordx4 v8, s[44:47], s65 offen lds\n"
        "buffer_load_dwordx4 v9, s[44:47], s65 offen offset:1024 lds\n"
        "buffer_load_dwordx4 v10, s[44:47], s65 offen offset:2048 lds\n"
        "s_add_u32 m0, s79, 9216\n"
        "s_mov_b32 s65, 331776\n"
        "buffer_load_dwordx4 v8, s[44:47], s65 offen lds\n"
        "buffer_load_dwordx4 v9, s[44:47], s65 offen offset:1024 lds\n"
        "buffer_load_dwordx4 v10, s[44:47], s65 offen offset:2048 lds\n"
        "s_add_u32 m0, s79, 12288\n"
        "s_mov_b32 s65, 442368\n"
        "buffer_load_dwordx4 v8, s[44:47], s65 offen lds\n"
        "buffer_load_dwordx4 v9, s[44:47], s65 offen offset:1024 lds\n"
        "buffer_load_dwordx4 v10, s[44:47], s65 offen offset:2048 lds\n"
        "s_add_u32 m0, s79, 15360\n"
        "s_mov_b32 s65, 552960\n"
        "buffer_load_dwordx4 v8, s[44:47], s65 offen lds\n"
        "buffer_load_dwordx4 v9, s[44:47], s65 offen offset:1024 lds\n"
        "buffer_load_dwordx4 v10, s[44:47], s65 offen offset:2048 lds\n"
        "s_add_u32 m0, s79, 18432\n"
        "s_mov_b32 s65, 663552\n"
        "buffer_load_dwordx4 v8, s[44:47], s65 offen lds\n"
        "buffer_load_dwordx4 v9, s[44:47], s65 offen offset:1024 lds\n"
        "buffer_load_dwordx4 v10, s[44:47], s65 offen offset:2048 lds\n"
        "s_add_u32 m0, s79, 21504\n"
        "s_mov_b32 s65, 774144\n"
        "buffer_load_dwordx4 v8, s[44:47], s65 offen lds\n"
        "buffer_load_dwordx4 v9, s[44:47], s65 offen offset:1024 lds\n"
        "buffer_load_dwordx4 v10, s[44:47], s65 offen offset:2048 lds\n"
        "s_add_u32 m0, s79, 24576\n"
        "s_mov_b32 s65, 884736\n"
        "buffer_load_dwordx4 v8, s[44:47], s65 offen lds\n"
        "buffer_load_dwordx4 v9, s[44:47], s65 offen offset:1024 lds\n"
        "buffer_load_dwordx4 v10, s[44:47], s65 offen offset:2048 lds\n"
        "s_add_u32 m0, s79, 27648\n"
        "s_mov_b32 s65, 995328\n"
        "buffer_load_dwordx4 v8, s[44:47], s65 offen lds\n"
        "buffer_load_dwordx4 v9, s[44:47], s65 offen offset:1024 lds\n"
        "buffer_load_dwordx4 v10, s[44:47], s65 offen offset:2048 lds\n"
        "s_add_u32 m0, s79, 30720\n"
        "s_mov_b32 s65, 1105920\n"
        "buffer_load_dwordx4 v8, s[44:47], s65 offen lds\n"
        "buffer_load_dwordx4 v9, s[44:47], s65 offen offset:1024 lds\n"
        "buffer_load_dwordx4 v10, s[44:47], s65 offen offset:2048 lds\n"
        "s_add_u32 m0, s79, 33792\n"
        "s_mov_b32 s65, 1216512\n"
        "buffer_load_dwordx4 v8, s[44:47], s65 offen lds\n"
        "buffer_load_dwordx4 v9, s[44:47], s65 offen offset:1024 lds\n"
        "buffer_load_dwordx4 v10, s[44:47], s65 offen offset:2048 lds\n"
        "s_waitcnt vmcnt(33)\n"
        "ds_read_b64_tr_b16 a[0:1], v11 offset:0\n"
        "ds_read_b64_tr_b16 a[2:3], v11 offset:1536\n"
        "ds_read_b64_tr_b16 a[4:5], v11 offset:8\n"
        "ds_read_b64_tr_b16 a[6:7], v11 offset:1544\n"
        "ds_read_b64_tr_b16 a[8:9], v4 offset:0\n"
        "ds_read_b64_tr_b16 a[10:11], v4 offset:1536\n"
        "s_waitcnt vmcnt(30)\n"
        "ds_read_b64_tr_b16 a[12:13], v11 offset:3072\n"
        "ds_read_b64_tr_b16 a[14:15], v11 offset:4608\n"
        "ds_read_b64_tr_b16 a[16:17], v11 offset:3080\n"
        "ds_read_b64_tr_b16 a[18:19], v11 offset:4616\n"
        "ds_read_b64_tr_b16 a[20:21], v4 offset:3072\n"
        "ds_read_b64_tr_b16 a[22:23], v4 offset:4608\n"
        "s_waitcnt lgkmcnt(6)\n"
        "s_add_u32 m0, s79, 0\n"
        "s_mov_b32 s65, 18432\n"
        "buffer_load_dwordx4 v8, s[44:47], s65 offen lds\n"
        "buffer_load_dwordx4 v9, s[44:47], s65 offen offset:1024 lds\n"
        "buffer_load_dwordx4 v10, s[44:47], s65 offen offset:2048 lds\n"
        "s_waitcnt vmcnt(30)\n"
        "ds_read_b64_tr_b16 a[24:25], v11 offset:6144\n"
        "ds_read_b64_tr_b16 a[26:27], v11 offset:7680\n"
        "ds_read_b64_tr_b16 a[28:29], v11 offset:6152\n"
        "ds_read_b64_tr_b16 a[30:31], v11 offset:7688\n"
        "ds_read_b64_tr_b16 a[32:33], v4 offset:6144\n"
        "ds_read_b64_tr_b16 a[34:35], v4 offset:7680\n"
        "s_waitcnt lgkmcnt(6)\n"
        "s_add_u32 m0, s79, 3072\n"
        "s_mov_b32 s65, 129024\n"
        "buffer_load_dwordx4 v8, s[44:47], s65 offen lds\n"
        "buffer_load_dwordx4 v9, s[44:47], s65 offen offset:1024 lds\n"
        "buffer_load_dwordx4 v10, s[44:47], s65 offen offset:2048 lds\n"
        "s_waitcnt vmcnt(30)\n"
        "ds_read_b64_tr_b16 a[36:37], v11 offset:9216\n"
        "ds_read_b64_tr_b16 a[38:39], v11 offset:10752\n"
        "ds_read_b64_tr_b16 a[40:41], v11 offset:9224\n"
        "ds_read_b64_tr_b16 a[42:43], v11 offset:10760\n"
        "ds_read_b64_tr_b16 a[44:45], v4 offset:9216\n"
        "ds_read_b64_tr_b16 a[46:47], v4 offset:10752\n"
        "s_waitcnt lgkmcnt(6)\n"
        "s_add_u32 m0, s79, 6144\n"
        "s_mov_b32 s65, 239616\n"
        "buffer_load_dwordx4 v8, s[44:47], s65 offen lds\n"
        "buffer_load_dwordx4 v9, s[44:47], s65 offen offset:1024 lds\n"
        "buffer_load_dwordx4 v10, s[44:47], s65 offen offset:2048 lds\n"
        "s_waitcnt vmcnt(30)\n"
        "ds_read_b64_tr_b16 a[48:49], v11 offset:12288\n"
        "ds_read_b64_tr_b16 a[50:51], v11 offset:13824\n"
        "ds_read_b64_tr_b16 a[52:53], v11 offset:12296\n"
        "ds_read_b64_tr_b16 a[54:55], v11 offset:13832\n"
        "ds_read_b64_tr_b16 a[56:57], v4 offset:12288\n"
        "ds_read_b64_tr_b16 a[58:59], v4 offset:13824\n"
        "s_waitcnt lgkmcnt(6)\n"
        "s_add_u32 m0, s79, 9216\n"
        "s_mov_b32 s65, 350208\n"
        "buffer_load_dwordx4 v8, s[44:47], s65 offen lds\n"
        "buffer_load_dwordx4 v9, s[44:47], s65 offen offset:1024 lds\n"
        "buffer_load_dwordx4 v10, s[44:47], s65 offen offset:2048 lds\n"
        "s_waitcnt vmcnt(30)\n"
        "ds_read_b64_tr_b16 a[60:61], v11 offset:15360\n"
        "ds_read_b64_tr_b16 a[62:63], v11 offset:16896\n"
        "ds_read_b64_tr_b16 a[64:65], v11 offset:15368\n"
        "ds_read_b64_tr_b16 a[66:67], v11 offset:16904\n"
        "ds_read_b64_tr_b16 a[68:69], v4 offset:15360\n"
        "ds_read_b64_tr_b16 a[70:71], v4 offset:16896\n"
        "s_waitcnt lgkmcnt(6)\n"
        "s_add_u32 m0, s79, 12288\n"
        "s_mov_b32 s65, 460800\n"
        "buffer_load_dwordx4 v8, s[44:47], s65 offen lds\n"
        "buffer_load_dwordx4 v9, s[44:47], s65 offen offset:1024 lds\n"
        "buffer_load_dwordx4 v10, s[44:47], s65 offen offset:2048 lds\n"
        "s_waitcnt vmcnt(30)\n"
        "ds_read_b64_tr_b16 a[72:73], v11 offset:18432\n"
        "ds_read_b64_tr_b16 a[74:75], v11 offset:19968\n"
        "ds_read_b64_tr_b16 a[76:77], v11 offset:18440\n"
        "ds_read_b64_tr_b16 a[78:79], v11 offset:19976\n"
        "ds_read_b64_tr_b16 a[80:81], v4 offset:18432\n"
        "ds_read_b64_tr_b16 a[82:83], v4 offset:19968\n"
        "s_waitcnt lgkmcnt(6)\n"
        "s_add_u32 m0, s79, 15360\n"
        "s_mov_b32 s65, 571392\n"
        "buffer_load_dwordx4 v8, s[44:47], s65 offen lds\n"
        "buffer_load_dwordx4 v9, s[44:47], s65 offen offset:1024 lds\n"
        "buffer_load_dwordx4 v10, s[44:47], s65 offen offset:2048 lds\n"
        "s_waitcnt vmcnt(30)\n"
        "ds_read_b64_tr_b16 a[84:85], v11 offset:21504\n"
        "ds_read_b64_tr_b16 a[86:87], v11 offset:23040\n"
        "ds_read_b64_tr_b16 a[88:89], v11 offset:21512\n"
        "ds_read_b64_tr_b16 a[90:91], v11 offset:23048\n"
        "ds_read_b64_tr_b16 a[92:93], v4 offset:21504\n"
        "ds_read_b64_tr_b16 a[94:95], v4 offset:23040\n"
        "s_waitcnt lgkmcnt(6)\n"
        "s_add_u32 m0, s79, 18432\n"
        "s_mov_b32 s65, 681984\n"
        "buffer_load_dwordx4 v8, s[44:47], s65 offen lds\n"
        "buffer_load_dwordx4 v9, s[44:47], s65 offen offset:1024 lds\n"
        "buffer_load_dwordx4 v10, s[44:47], s65 offen offset:2048 lds\n"
        "s_waitcnt vmcnt(30)\n"
        "ds_read_b64_tr_b16 a[96:97], v11 offset:24576\n"
        "ds_read_b64_tr_b16 a[98:99], v11 offset:26112\n"
        "ds_read_b64_tr_b16 a[100:101], v11 offset:24584\n"
        "ds_read_b64_tr_b16 a[102:103], v11 offset:26120\n"
        "ds_read_b64_tr_b16 a[104:105], v4 offset:24576\n"
        "ds_read_b64_tr_b16 a[106:107], v4 offset:26112\n"
        "s_waitcnt lgkmcnt(6)\n"
        "s_add_u32 m0, s79, 21504\n"
        "s_mov_b32 s65, 792576\n"
        "buffer_load_dwordx4 v8, s[44:47], s65 offen lds\n"
        "buffer_load_dwordx4 v9, s[44:47], s65 offen offset:1024 lds\n"
        "buffer_load_dwordx4 v10, s[44:47], s65 offen offset:2048 lds\n"
        "s_waitcnt vmcnt(30)\n"
        "ds_read_b64_tr_b16 a[108:109], v11 offset:27648\n"
        "ds_read_b64_tr_b16 a[110:111], v11 offset:29184\n"
        "ds_read_b64_tr_b16 a[112:113], v11 offset:27656\n"
        "ds_read_b64_tr_b16 a[114:115], v11 offset:29192\n"
        "ds_read_b64_tr_b16 a[116:117], v4 offset:27648\n"
        "ds_read_b64_tr_b16 a[118:119], v4 offset:29184\n"
        "s_waitcnt lgkmcnt(6)\n"
        "s_add_u32 m0, s79, 24576\n"
        "s_mov_b32 s65, 903168\n"
        "buffer_load_dwordx4 v8, s[44:47], s65 offen lds\n"
        "buffer_load_dwordx4 v9, s[44:47], s65 offen offset:1024 lds\n"
        "buffer_load_dwordx4 v10, s[44:47], s65 offen offset:2048 lds\n"
        "s_waitcnt vmcnt(30)\n"
        "ds_read_b64_tr_b16 a[120:121], v11 offset:30720\n"
        "ds_read_b64_tr_b16 a[122:123], v11 offset:32256\n"
        "ds_read_b64_tr_b16 a[124:125], v11 offset:30728\n"
        "ds_read_b64_tr_b16 a[126:127], v11 offset:32264\n"
        "ds_read_b64_tr_b16 a[128:129], v4 offset:30720\n"
        "ds_read_b64_tr_b16 a[130:131], v4 offset:32256\n"
        "s_waitcnt lgkmcnt(6)\n"
        "s_add_u32 m0, s79, 27648\n"
        "s_mov_b32 s65, 1013760\n"
        "buffer_load_dwordx4 v8, s[44:47], s65 offen lds\n"
        "buffer_load_dwordx4 v9, s[44:47], s65 offen offset:1024 lds\n"
        "buffer_load_dwordx4 v10, s[44:47], s65 offen offset:2048 lds\n"
        "s_waitcnt vmcnt(30)\n"
        "ds_read_b64_tr_b16 a[132:133], v11 offset:33792\n"
        "ds_read_b64_tr_b16 a[134:135], v11 offset:35328\n"
        "ds_read_b64_tr_b16 a[136:137], v11 offset:33800\n"
        "ds_read_b64_tr_b16 a[138:139], v11 offset:35336\n"
        "ds_read_b64_tr_b16 a[140:141], v4 offset:33792\n"
        "ds_read_b64_tr_b16 a[142:143], v4 offset:35328\n"
        "s_waitcnt lgkmcnt(6)\n"
        "s_add_u32 m0, s79, 30720\n"
        "s_mov_b32 s65, 1124352\n"
        "buffer_load_dwordx4 v8, s[44:47], s65 offen lds\n"
        "buffer_load_dwordx4 v9, s[44:47], s65 offen offset:1024 lds\n"
        "buffer_load_dwordx4 v10, s[44:47], s65 offen offset:2048 lds\n"
        "s_waitcnt vmcnt(30)\n"
        "ds_read_b64_tr_b16 a[144:145], v11 offset:0\n"
        "ds_read_b64_tr_b16 a[146:147], v11 offset:1536\n"
        "ds_read_b64_tr_b16 a[148:149], v11 offset:8\n"
        "ds_read_b64_tr_b16 a[150:151], v11 offset:1544\n"
        "ds_read_b64_tr_b16 a[152:153], v4 offset:0\n"
        "ds_read_b64_tr_b16 a[154:155], v4 offset:1536\n"
        "s_waitcnt lgkmcnt(6)\n"
        "s_add_u32 m0, s79, 33792\n"
        "s_mov_b32 s65, 1234944\n"
        "buffer_load_dwordx4 v8, s[44:47], s65 offen lds\n"
        "buffer_load_dwordx4 v9, s[44:47], s65 offen offset:1024 lds\n"
        "buffer_load_dwordx4 v10, s[44:47], s65 offen offset:2048 lds\n"
        "s_waitcnt vmcnt(30)\n"
        "ds_read_b64_tr_b16 a[156:157], v11 offset:3072\n"
        "ds_read_b64_tr_b16 a[158:159], v11 offset:4608\n"
        "ds_read_b64_tr_b16 a[160:161], v11 offset:3080\n"
        "ds_read_b64_tr_b16 a[162:163], v11 offset:4616\n"
        "ds_read_b64_tr_b16 a[164:165], v4 offset:3072\n"
        "ds_read_b64_tr_b16 a[166:167], v4 offset:4608\n"
        "s_waitcnt lgkmcnt(6)\n"
        "s_add_u32 m0, s79, 0\n"
        "s_mov_b32 s65, 36864\n"
        "buffer_load_dwordx4 v8, s[44:47], s65 offen lds\n"
        "buffer_load_dwordx4 v9, s[44:47], s65 offen offset:1024 lds\n"
        "buffer_load_dwordx4 v10, s[44:47], s65 offen offset:2048 lds\n"
        "s_waitcnt vmcnt(30)\n"
        "ds_read_b64_tr_b16 a[168:169], v11 offset:6144\n"
        "ds_read_b64_tr_b16 a[170:171], v11 offset:7680\n"
        "ds_read_b64_tr_b16 a[172:173], v11 offset:6152\n"
        "ds_read_b64_tr_b16 a[174:175], v11 offset:7688\n"
        "ds_read_b64_tr_b16 a[176:177], v4 offset:6144\n"
        "ds_read_b64_tr_b16 a[178:179], v4 offset:7680\n"
        "s_waitcnt lgkmcnt(6)\n"
        "s_add_u32 m0, s79, 3072\n"
        "s_mov_b32 s65, 147456\n"
        "buffer_load_dwordx4 v8, s[44:47], s65 offen lds\n"
        "buffer_load_dwordx4 v9, s[44:47], s65 offen offset:1024 lds\n"
        "buffer_load_dwordx4 v10, s[44:47], s65 offen offset:2048 lds\n"
        "s_waitcnt vmcnt(30)\n"
        "ds_read_b64_tr_b16 a[180:181], v11 offset:9216\n"
        "ds_read_b64_tr_b16 a[182:183], v11 offset:10752\n"
        "ds_read_b64_tr_b16 a[184:185], v11 offset:9224\n"
        "ds_read_b64_tr_b16 a[186:187], v11 offset:10760\n"
        "ds_read_b64_tr_b16 a[188:189], v4 offset:9216\n"
        "ds_read_b64_tr_b16 a[190:191], v4 offset:10752\n"
        "s_waitcnt lgkmcnt(6)\n"
        "s_add_u32 m0, s79, 6144\n"
        "s_mov_b32 s65, 258048\n"
        "buffer_load_dwordx4 v8, s[44:47], s65 offen lds\n"
        "buffer_load_dwordx4 v9, s[44:47], s65 offen offset:1024 lds\n"
        "buffer_load_dwordx4 v10, s[44:47], s65 offen offset:2048 lds\n"
        "s_waitcnt vmcnt(30)\n"
        "ds_read_b64_tr_b16 a[192:193], v11 offset:12288\n"
        "ds_read_b64_tr_b16 a[194:195], v11 offset:13824\n"
        "ds_read_b64_tr_b16 a[196:197], v11 offset:12296\n"
        "ds_read_b64_tr_b16 a[198:199], v11 offset:13832\n"
        "ds_read_b64_tr_b16 a[200:201], v4 offset:12288\n"
        "ds_read_b64_tr_b16 a[202:203], v4 offset:13824\n"
        "s_waitcnt lgkmcnt(6)\n"
        "s_add_u32 m0, s79, 9216\n"
        "s_mov_b32 s65, 368640\n"
        "buffer_load_dwordx4 v8, s[44:47], s65 offen lds\n"
        "buffer_load_dwordx4 v9, s[44:47], s65 offen offset:1024 lds\n"
        "buffer_load_dwordx4 v10, s[44:47], s65 offen offset:2048 lds\n"
        "s_waitcnt vmcnt(30)\n"
        "ds_read_b64_tr_b16 a[204:205], v11 offset:15360\n"
        "ds_read_b64_tr_b16 a[206:207], v11 offset:16896\n"
        "ds_read_b64_tr_b16 a[208:209], v11 offset:15368\n"
        "ds_read_b64_tr_b16 a[210:211], v11 offset:16904\n"
        "ds_read_b64_tr_b16 a[212:213], v4 offset:15360\n"
        "ds_read_b64_tr_b16 a[214:215], v4 offset:16896\n"
        "s_waitcnt lgkmcnt(6)\n"
        "s_add_u32 m0, s79, 12288\n"
        "s_mov_b32 s65, 479232\n"
        "buffer_load_dwordx4 v8, s[44:47], s65 offen lds\n"
        "buffer_load_dwordx4 v9, s[44:47], s65 offen offset:1024 lds\n"
        "buffer_load_dwordx4 v10, s[44:47], s65 offen offset:2048 lds\n"
        "s_waitcnt vmcnt(30)\n"
        "ds_read_b64_tr_b16 a[216:217], v11 offset:18432\n"
        "ds_read_b64_tr_b16 a[218:219], v11 offset:19968\n"
        "ds_read_b64_tr_b16 a[220:221], v11 offset:18440\n"
        "ds_read_b64_tr_b16 a[222:223], v11 offset:19976\n"
        "ds_read_b64_tr_b16 a[224:225], v4 offset:18432\n"
        "ds_read_b64_tr_b16 a[226:227], v4 offset:19968\n"
        "s_waitcnt lgkmcnt(6)\n"
        "s_add_u32 m0, s79, 15360\n"
        "s_mov_b32 s65, 589824\n"
        "buffer_load_dwordx4 v8, s[44:47], s65 offen lds\n"
        "buffer_load_dwordx4 v9, s[44:47], s65 offen offset:1024 lds\n"
        "buffer_load_dwordx4 v10, s[44:47], s65 offen offset:2048 lds\n"
        "s_waitcnt vmcnt(30)\n"
        "ds_read_b64_tr_b16 a[228:229], v11 offset:21504\n"
        "ds_read_b64_tr_b16 a[230:231], v11 offset:23040\n"
        "ds_read_b64_tr_b16 a[232:233], v11 offset:21512\n"
        "ds_read_b64_tr_b16 a[234:235], v11 offset:23048\n"
        "ds_read_b64_tr_b16 a[236:237], v4 offset:21504\n"
        "ds_read_b64_tr_b16 a[238:239], v4 offset:23040\n"
        "s_waitcnt lgkmcnt(6)\n"
        "s_add_u32 m0, s79, 18432\n"
        "s_mov_b32 s65, 700416\n"
        "buffer_load_dwordx4 v8, s[44:47], s65 offen lds\n"
        "buffer_load_dwordx4 v9, s[44:47], s65 offen offset:1024 lds\n"
        "buffer_load_dwordx4 v10, s[44:47], s65 offen offset:2048 lds\n"
        "s_waitcnt vmcnt(30)\n"
        "ds_read_b64_tr_b16 a[240:241], v11 offset:24576\n"
        "ds_read_b64_tr_b16 a[242:243], v11 offset:26112\n"
        "ds_read_b64_tr_b16 a[244:245], v11 offset:24584\n"
        "ds_read_b64_tr_b16 a[246:247], v11 offset:26120\n"
        "ds_read_b64_tr_b16 a[248:249], v4 offset:24576\n"
        "ds_read_b64_tr_b16 a[250:251], v4 offset:26112\n"
        "s_waitcnt lgkmcnt(6)\n"
        "s_add_u32 m0, s79, 21504\n"
        "s_mov_b32 s65, 811008\n"
        "buffer_load_dwordx4 v8, s[44:47], s65 offen lds\n"
        "buffer_load_dwordx4 v9, s[44:47], s65 offen offset:1024 lds\n"
        "buffer_load_dwordx4 v10, s[44:47], s65 offen offset:2048 lds\n"
        "s_waitcnt vmcnt(30)\n"
        "ds_read_b64_tr_b16 a[252:253], v11 offset:27648\n"
        "ds_read_b64_tr_b16 a[254:255], v11 offset:29184\n"
        "ds_read_b64_tr_b16 v[80:81], v11 offset:27656\n"
        "ds_read_b64_tr_b16 v[82:83], v11 offset:29192\n"
        "ds_read_b64_tr_b16 v[84:85], v4 offset:27648\n"
        "ds_read_b64_tr_b16 v[86:87], v4 offset:29184\n"
        "s_waitcnt lgkmcnt(6)\n"
        "s_add_u32 m0, s79, 24576\n"
        "s_mov_b32 s65, 921600\n"
        "buffer_load_dwordx4 v8, s[44:47], s65 offen lds\n"
        "buffer_load_dwordx4 v9, s[44:47], s65 offen offset:1024 lds\n"
        "buffer_load_dwordx4 v10, s[44:47], s65 offen offset:2048 lds\n"
        "s_waitcnt vmcnt(30)\n"
        "ds_read_b64_tr_b16 v[88:89], v11 offset:30720\n"
        "ds_read_b64_tr_b16 v[90:91], v11 offset:32256\n"
        "ds_read_b64_tr_b16 v[92:93], v11 offset:30728\n"
        "ds_read_b64_tr_b16 v[94:95], v11 offset:32264\n"
        "ds_read_b64_tr_b16 v[96:97], v4 offset:30720\n"
        "ds_read_b64_tr_b16 v[98:99], v4 offset:32256\n"
        "s_waitcnt lgkmcnt(6)\n"
        "s_add_u32 m0, s79, 27648\n"
        "s_mov_b32 s65, 1032192\n"
        "buffer_load_dwordx4 v8, s[44:47], s65 offen lds\n"
        "buffer_load_dwordx4 v9, s[44:47], s65 offen offset:1024 lds\n"
        "buffer_load_dwordx4 v10, s[44:47], s65 offen offset:2048 lds\n"
        "s_waitcnt vmcnt(30)\n"
        "ds_read_b64_tr_b16 v[100:101], v11 offset:33792\n"
        "ds_read_b64_tr_b16 v[102:103], v11 offset:35328\n"
        "ds_read_b64_tr_b16 v[104:105], v11 offset:33800\n"
        "ds_read_b64_tr_b16 v[106:107], v11 offset:35336\n"
        "ds_read_b64_tr_b16 v[108:109], v4 offset:33792\n"
        "ds_read_b64_tr_b16 v[110:111], v4 offset:35328\n"
        "s_waitcnt lgkmcnt(6)\n"
        "s_add_u32 m0, s79, 30720\n"
        "s_mov_b32 s65, 1142784\n"
        "buffer_load_dwordx4 v8, s[44:47], s65 offen lds\n"
        "buffer_load_dwordx4 v9, s[44:47], s65 offen offset:1024 lds\n"
        "buffer_load_dwordx4 v10, s[44:47], s65 offen offset:2048 lds\n"
        "s_waitcnt vmcnt(30)\n"
        "ds_read_b64_tr_b16 v[112:113], v11 offset:0\n"
        "ds_read_b64_tr_b16 v[114:115], v11 offset:1536\n"
        "ds_read_b64_tr_b16 v[116:117], v11 offset:8\n"
        "ds_read_b64_tr_b16 v[118:119], v11 offset:1544\n"
        "ds_read_b64_tr_b16 v[120:121], v4 offset:0\n"
        "ds_read_b64_tr_b16 v[122:123], v4 offset:1536\n"
        "s_waitcnt lgkmcnt(6)\n"
        "s_add_u32 m0, s79, 33792\n"
        "s_mov_b32 s65, 1253376\n"
        "buffer_load_dwordx4 v8, s[44:47], s65 offen lds\n"
        "buffer_load_dwordx4 v9, s[44:47], s65 offen offset:1024 lds\n"
        "buffer_load_dwordx4 v10, s[44:47], s65 offen offset:2048 lds\n"
        "s_waitcnt vmcnt(30)\n"
        "ds_read_b64_tr_b16 v[124:125], v11 offset:3072\n"
        "ds_read_b64_tr_b16 v[126:127], v11 offset:4608\n"
        "ds_read_b64_tr_b16 v[128:129], v11 offset:3080\n"
        "ds_read_b64_tr_b16 v[130:131], v11 offset:4616\n"
        "ds_read_b64_tr_b16 v[132:133], v4 offset:3072\n"
        "ds_read_b64_tr_b16 v[134:135], v4 offset:4608\n"
        "s_waitcnt vmcnt(27)\n"
        "ds_read_b64_tr_b16 v[136:137], v11 offset:6144\n"
        "ds_read_b64_tr_b16 v[138:139], v11 offset:7680\n"
        "ds_read_b64_tr_b16 v[140:141], v11 offset:6152\n"
        "ds_read_b64_tr_b16 v[142:143], v11 offset:7688\n"
        "ds_read_b64_tr_b16 v[144:145], v4 offset:6144\n"
        "ds_read_b64_tr_b16 v[146:147], v4 offset:7680\n"
        "s_waitcnt vmcnt(24)\n"
        "ds_read_b64_tr_b16 v[148:149], v11 offset:9216\n"
        "ds_read_b64_tr_b16 v[150:151], v11 offset:10752\n"
        "ds_read_b64_tr_b16 v[152:153], v11 offset:9224\n"
        "ds_read_b64_tr_b16 v[154:155], v11 offset:10760\n"
        "ds_read_b64_tr_b16 v[156:157], v4 offset:9216\n"
        "ds_read_b64_tr_b16 v[158:159], v4 offset:10752\n"
        "s_waitcnt vmcnt(21)\n"
        "ds_read_b64_tr_b16 v[160:161], v11 offset:12288\n"
        "ds_read_b64_tr_b16 v[162:163], v11 offset:13824\n"
        "ds_read_b64_tr_b16 v[164:165], v11 offset:12296\n"
        "ds_read_b64_tr_b16 v[166:167], v11 offset:13832\n"
        "ds_read_b64_tr_b16 v[168:169], v4 offset:12288\n"
        "ds_read_b64_tr_b16 v[170:171], v4 offset:13824\n"
        "s_waitcnt vmcnt(18)\n"
        "ds_read_b64_tr_b16 v[172:173], v11 offset:15360\n"
        "ds_read_b64_tr_b16 v[174:175], v11 offset:16896\n"
        "ds_read_b64_tr_b16 v[176:177], v11 offset:15368\n"
        "ds_read_b64_tr_b16 v[178:179], v11 offset:16904\n"
        "ds_read_b64_tr_b16 v[180:181], v4 offset:15360\n"
        "ds_read_b64_tr_b16 v[182:183], v4 offset:16896\n"
        "s_waitcnt vmcnt(15)\n"
        "ds_read_b64_tr_b16 v[184:185], v11 offset:18432\n"
        "ds_read_b64_tr_b16 v[186:187], v11 offset:19968\n"
        "ds_read_b64_tr_b16 v[188:189], v11 offset:18440\n"
        "ds_read_b64_tr_b16 v[190:191], v11 offset:19976\n"
        "ds_read_b64_tr_b16 v[192:193], v4 offset:18432\n"
        "ds_read_b64_tr_b16 v[194:195], v4 offset:19968\n"
        "s_waitcnt vmcnt(12)\n"
        "ds_read_b64_tr_b16 v[196:197], v11 offset:21504\n"
        "ds_read_b64_tr_b16 v[198:199], v11 offset:23040\n"
        "ds_read_b64_tr_b16 v[200:201], v11 offset:21512\n"
        "ds_read_b64_tr_b16 v[202:203], v11 offset:23048\n"
        "ds_read_b64_tr_b16 v[204:205], v4 offset:21504\n"
        "ds_read_b64_tr_b16 v[206:207], v4 offset:23040\n"
        "s_waitcnt vmcnt(9)\n"
        "ds_read_b64_tr_b16 v[208:209], v11 offset:24576\n"
        "ds_read_b64_tr_b16 v[210:211], v11 offset:26112\n"
        "ds_read_b64_tr_b16 v[212:213], v11 offset:24584\n"
        "ds_read_b64_tr_b16 v[214:215], v11 offset:26120\n"
        "ds_read_b64_tr_b16 v[216:217], v4 offset:24576\n"
        "ds_read_b64_tr_b16 v[218:219], v4 offset:26112\n"
        "s_waitcnt vmcnt(6)\n"
        "ds_read_b64_tr_b16 v[220:221], v11 offset:27648\n"
        "ds_read_b64_tr_b16 v[222:223], v11 offset:29184\n"
        "ds_read_b64_tr_b16 v[224:225], v11 offset:27656\n"
        "ds_read_b64_tr_b16 v[226:227], v11 offset:29192\n"
        "ds_read_b64_tr_b16 v[228:229], v4 offset:27648\n"
        "ds_read_b64_tr_b16 v[230:231], v4 offset:29184\n"
        "s_waitcnt vmcnt(3)\n"
        "ds_read_b64_tr_b16 v[232:233], v11 offset:30720\n"
        "ds_read_b64_tr_b16 v[234:235], v11 offset:32256\n"
        "ds_read_b64_tr_b16 v[236:237], v11 offset:30728\n"
        "ds_read_b64_tr_b16 v[238:239], v11 offset:32264\n"
        "ds_read_b64_tr_b16 v[240:241], v4 offset:30720\n"
        "ds_read_b64_tr_b16 v[242:243], v4 offset:32256\n"
        "s_waitcnt vmcnt(0)\n"
        "ds_read_b64_tr_b16 v[244:245], v11 offset:33792\n"
        "ds_read_b64_tr_b16 v[246:247], v11 offset:35328\n"
        "ds_read_b64_tr_b16 v[248:249], v11 offset:33800\n"
        "ds_read_b64_tr_b16 v[250:251], v11 offset:35336\n"
        "ds_read_b64_tr_b16 v[252:253], v4 offset:33792\n"
        "ds_read_b64_tr_b16 v[254:255], v4 offset:35328\n"
        "s_waitcnt lgkmcnt(0)\n"
        "v_lshrrev_b32 v12, 4, v1\n"
        "s_lshl_b32 s66, s57, 7\n"
        "s_mov_b32 s64, 0\n"
        ".Lf2_pair_top_%=:\n"
        "s_mov_b64 exec, 1\n"
        "buffer_load_dword v13, off, s[40:43], s66 sc1\n"
        "s_mov_b64 exec, -1\n"
        "s_waitcnt vmcnt(0)\n"
        "v_cmp_gt_i32 vcc, 0, v13\n"
        "s_nop 0\n"
        "s_and_b64 vcc, vcc, 1\n"
        "s_cbranch_vccz .Lf2_pair_ok_%=\n"
        "s_add_u32 s64, s64, 1\n"
        "s_and_b32 s65, s64, 255\n"
        "s_cbranch_scc1 .Lf2_pair_go_%=\n"
        "s_mov_b32 s65, 40960\n"
        "s_cmp_lt_u32 s64, 1048576\n"
        "s_cbranch_scc1 .Lf2_pair_chk_%=\n"
        "v_mov_b32 v13, 1\n"
        "buffer_store_dword v13, off, s[40:43], s65 sc1\n"
        "s_waitcnt vmcnt(0)\n"
        ".Lf2_pair_chk_%=:\n"
        "buffer_load_dword v13, off, s[40:43], s65 sc1\n"
        "s_waitcnt vmcnt(0)\n"
        "v_readfirstlane_b32 s65, v13\n"
        "s_nop 0\n"
        "s_cmp_lg_u32 s65, 1\n"
        "s_cbranch_scc1 .Lf2_pair_go_%=\n"
        "s_endpgm\n"
        ".Lf2_pair_go_%=:\n"
        "s_sleep 2\n"
        "s_branch .Lf2_pair_top_%=\n"
        ".Lf2_pair_ok_%=:\n"
        "v_readfirstlane_b32 s50, v13\n"
        "s_nop 0\n"
        "s_xor_b32 s50, s50, s56\n"
        "v_lshlrev_b32 v14, 7, v12\n"
        "s_sub_u32 s66, 0, s50\n"
        "s_mov_b32 s52, 24576\n"
        "s_mov_b32 s64, 0\n"
        ".Lf2_pg0_top_%=:\n"
        "buffer_load_dword v13, v14, s[40:43], s52 offen sc1\n"
        "s_waitcnt vmcnt(0)\n"
        "v_cmp_gt_i32 vcc, 0, v13\n"
        "s_cbranch_vccz .Lf2_pg0_ok_%=\n"
        "s_add_u32 s64, s64, 1\n"
        "s_and_b32 s65, s64, 255\n"
        "s_cbranch_scc1 .Lf2_pg0_go_%=\n"
        "s_mov_b32 s65, 40960\n"
        "s_cmp_lt_u32 s64, 1048576\n"
        "s_cbranch_scc1 .Lf2_pg0_chk_%=\n"
        "v_mov_b32 v13, 1\n"
        "buffer_store_dword v13, off, s[40:43], s65 sc1\n"
        "s_waitcnt vmcnt(0)\n"
        ".Lf2_pg0_chk_%=:\n"
        "buffer_load_dword v13, off, s[40:43], s65 sc1\n"
        "s_waitcnt vmcnt(0)\n"
        "v_readfirstlane_b32 s65, v13\n"
        "s_nop 0\n"
        "s_cmp_lg_u32 s65, 1\n"
        "s_cbranch_scc1 .Lf2_pg0_go_%=\n"
        "s_endpgm\n"
        ".Lf2_pg0_go_%=:\n"
        "s_sleep 2\n"
        "s_branch .Lf2_pg0_top_%=\n"
        ".Lf2_pg0_ok_%=:\n"
        "v_cmp_eq_u32 s[52:53], 1, v13\n"
        "s_nop 1\n"
        "s_mov_b32 s54, 32768\n"
        "s_mov_b32 s64, 0\n"
        ".Lf2_pg1_top_%=:\n"
        "buffer_load_dword v13, v14, s[40:43], s54 offen sc1\n"
        "s_waitcnt vmcnt(0)\n"
        "v_cmp_gt_i32 vcc, 0, v13\n"
        "s_cbranch_vccz .Lf2_pg1_ok_%=\n"
        "s_add_u32 s64, s64, 1\n"
        "s_and_b32 s65, s64, 255\n"
        "s_cbranch_scc1 .Lf2_pg1_go_%=\n"
        "s_mov_b32 s65, 40960\n"
        "s_cmp_lt_u32 s64, 1048576\n"
        "s_cbranch_scc1 .Lf2_pg1_chk_%=\n"
        "v_mov_b32 v13, 1\n"
        "buffer_store_dword v13, off, s[40:43], s65 sc1\n"
        "s_waitcnt vmcnt(0)\n"
        ".Lf2_pg1_chk_%=:\n"
        "buffer_load_dword v13, off, s[40:43], s65 sc1\n"
        "s_waitcnt vmcnt(0)\n"
        "v_readfirstlane_b32 s65, v13\n"
        "s_nop 0\n"
        "s_cmp_lg_u32 s65, 1\n"
        "s_cbranch_scc1 .Lf2_pg1_go_%=\n"
        "s_endpgm\n"
        ".Lf2_pg1_go_%=:\n"
        "s_sleep 2\n"
        "s_branch .Lf2_pg1_top_%=\n"
        ".Lf2_pg1_ok_%=:\n"
        "v_cmp_eq_u32 s[54:55], 1, v13\n"
        "s_nop 1\n"
        "s_mov_b32 vcc_lo, 0x55555555\n"
        "s_mov_b32 vcc_hi, 0x55555555\n"
        "s_xor_b64 vcc, vcc, s[52:53]\n"
        "s_bcnt1_i32_b64 s65, vcc\n"
        "s_sub_u32 s64, s65, 23\n"
        "s_cmp_lt_u32 s64, 19\n"
        "s_cbranch_scc1 .Lf2_rawmap_%=\n"
        "s_cmp_lt_u32 s65, 32\n"
        "s_cselect_b32 s65, 0, -1\n"
        "s_xor_b32 s52, s65, 0x55555555\n"
        "s_xor_b32 s53, s65, 0x55555555\n"
        "s_xor_b32 s54, s65, 0x55555555\n"
        "s_xor_b32 s55, s65, 0xaaaaaaaa\n"
        ".Lf2_rawmap_%=:\n"
        "s_xor_b32 s52, s52, s66\n"
        "s_xor_b32 s53, s53, s66\n"
        "s_xor_b32 s54, s54, s66\n"
        "s_xor_b32 s55, s55, s66\n"
        "s_and_b32 s76, s55, 1\n"
        "s_lshl_b32 s76, s76, 13\n"
        "s_add_u32 s60, s60, s76\n"
        "s_barrier\n"
        "s_mov_b32 s68, -1\n"
        "s_mov_b32 s69, 0\n"
        "v_add_u32 v8, 49152, v0\n"
        "s_mov_b32 s72, 241172480\n"
        "s_mov_b32 s73, 241172480\n"
        "s_branch .Lf2_wait2_%=\n"
        ".Lf2_c01_%=:\n"
        "v_mfma_f32_16x16x32_bf16 v[44:47], a[0:3], v[12:15], 0\n"
        "v_cvt_pk_bf16_f32 v74, v60, v61\n"
        "v_cvt_pk_bf16_f32 v75, v62, v63\n"
        "v_mfma_f32_16x16x32_bf16 v[48:51], a[4:7], v[12:15], 0\n"
        "v_cvt_pk_bf16_f32 v78, v64, v65\n"
        "v_cvt_pk_bf16_f32 v79, v66, v67\n"
        "v_mfma_f32_16x16x32_bf16 v[52:55], a[8:11], v[12:15], 0\n"
        "ds_read_b128 v[12:15], v0 offset:4096\n"
        "s_add_u32 m0, s61, 24576\n"
        "v_mfma_f32_16x16x32_bf16 v[56:59], a[0:3], v[16:19], 0\n"
        "s_add_u32 s71, s71, 16384\n"
        "buffer_load_dwordx4 v1, s[40:43], s71 offen offset:0 sc1 lds\n"
        "v_mfma_f32_16x16x32_bf16 v[60:63], a[4:7], v[16:19], 0\n"
        "buffer_load_dwordx4 v1, s[40:43], s71 offen offset:1024 sc1 lds\n"
        "s_add_u32 m0, m0, 8192\n"
        "v_mfma_f32_16x16x32_bf16 v[64:67], a[8:11], v[16:19], 0\n"
        "ds_read_b128 v[16:19], v0 offset:4352\n"
        "s_waitcnt lgkmcnt(2)\n"
        "v_mfma_f32_16x16x32_bf16 v[44:47], a[12:15], v[20:23], v[44:47]\n"
        "s_add_u32 s71, s71, 16384\n"
        "v_mfma_f32_16x16x32_bf16 v[48:51], a[16:19], v[20:23], v[48:51]\n"
        "buffer_load_dwordx4 v1, s[40:43], s71 offen offset:0 sc1 lds\n"
        "v_mfma_f32_16x16x32_bf16 v[52:55], a[20:23], v[20:23], v[52:55]\n"
        "ds_read_b128 v[20:23], v0 offset:6144\n"
        "v_mfma_f32_16x16x32_bf16 v[56:59], a[12:15], v[24:27], v[56:59]\n"
        "buffer_store_dwordx4 v[68:71], v6, s[40:43], s72 offen sc1\n"
        "v_mfma_f32_16x16x32_bf16 v[60:63], a[16:19], v[24:27], v[60:63]\n"
        "buffer_load_dwordx4 v1, s[40:43], s71 offen offset:1024 sc1 lds\n"
        "s_add_u32 m0, m0, 8192\n"
        "v_mfma_f32_16x16x32_bf16 v[64:67], a[20:23], v[24:27], v[64:67]\n"
        "ds_read_b128 v[24:27], v0 offset:6400\n"
        "s_waitcnt lgkmcnt(2)\n"
        "v_mfma_f32_16x16x32_bf16 v[44:47], a[24:27], v[12:15], v[44:47]\n"
        "s_add_u32 s71, s71, 16384\n"
        "s_add_u32 s66, s72, 147456\n"
        "v_mfma_f32_16x16x32_bf16 v[48:51], a[28:31], v[12:15], v[48:51]\n"
        "buffer_load_dwordx4 v1, s[40:43], s71 offen offset:0 sc1 lds\n"
        "v_mfma_f32_16x16x32_bf16 v[52:55], a[32:35], v[12:15], v[52:55]\n"
        "ds_read_b128 v[12:15], v0 offset:8192\n"
        "v_mfma_f32_16x16x32_bf16 v[56:59], a[24:27], v[16:19], v[56:59]\n"
        "buffer_store_dwordx4 v[72:75], v6, s[40:43], s66 offen sc1\n"
        "v_mfma_f32_16x16x32_bf16 v[60:63], a[28:31], v[16:19], v[60:63]\n"
        "buffer_load_dwordx4 v1, s[40:43], s71 offen offset:1024 sc1 lds\n"
        "s_add_u32 s70, s70, 1\n"
        "v_mfma_f32_16x16x32_bf16 v[64:67], a[32:35], v[16:19], v[64:67]\n"
        "ds_read_b128 v[16:19], v0 offset:8448\n"
        "s_waitcnt lgkmcnt(2)\n"
        "v_mfma_f32_16x16x32_bf16 v[44:47], a[36:39], v[20:23], v[44:47]\n"
        "v_permlane16_swap_b32 v76, v78\n"
        "v_mfma_f32_16x16x32_bf16 v[48:51], a[40:43], v[20:23], v[48:51]\n"
        "v_permlane16_swap_b32 v77, v79\n"
        "v_mfma_f32_16x16x32_bf16 v[52:55], a[44:47], v[20:23], v[52:55]\n"
        "ds_read_b128 v[20:23], v0 offset:10240\n"
        "v_mfma_f32_16x16x32_bf16 v[56:59], a[36:39], v[24:27], v[56:59]\n"
        "buffer_store_dwordx4 v[76:79], v7, s[40:43], s72 offen sc1\n"
        "v_mfma_f32_16x16x32_bf16 v[60:63], a[40:43], v[24:27], v[60:63]\n"
        "v_mfma_f32_16x16x32_bf16 v[64:67], a[44:47], v[24:27], v[64:67]\n"
        "ds_read_b128 v[24:27], v0 offset:10496\n"
        "s_waitcnt lgkmcnt(2)\n"
        "v_mfma_f32_16x16x32_bf16 v[44:47], a[48:51], v[12:15], v[44:47]\n"
        "v_mfma_f32_16x16x32_bf16 v[48:51], a[52:55], v[12:15], v[48:51]\n"
        "v_mfma_f32_16x16x32_bf16 v[52:55], a[56:59], v[12:15], v[52:55]\n"
        "ds_read_b128 v[12:15], v0 offset:12288\n"
        "v_mfma_f32_16x16x32_bf16 v[56:59], a[48:51], v[16:19], v[56:59]\n"
        "v_mfma_f32_16x16x32_bf16 v[60:63], a[52:55], v[16:19], v[60:63]\n"
        "v_mfma_f32_16x16x32_bf16 v[64:67], a[56:59], v[16:19], v[64:67]\n"
        "ds_read_b128 v[16:19], v0 offset:12544\n"
        "s_waitcnt lgkmcnt(2)\n"
        "v_mfma_f32_16x16x32_bf16 v[44:47], a[60:63], v[20:23], v[44:47]\n"
        "v_mfma_f32_16x16x32_bf16 v[48:51], a[64:67], v[20:23], v[48:51]\n"
        "v_mfma_f32_16x16x32_bf16 v[52:55], a[68:71], v[20:23], v[52:55]\n"
        "ds_read_b128 v[20:23], v0 offset:14336\n"
        "v_mfma_f32_16x16x32_bf16 v[56:59], a[60:63], v[24:27], v[56:59]\n"
        "v_mfma_f32_16x16x32_bf16 v[60:63], a[64:67], v[24:27], v[60:63]\n"
        "v_mfma_f32_16x16x32_bf16 v[64:67], a[68:71], v[24:27], v[64:67]\n"
        "ds_read_b128 v[24:27], v0 offset:14592\n"
        "s_waitcnt lgkmcnt(2)\n"
        "v_mfma_f32_16x16x32_bf16 v[44:47], a[72:75], v[12:15], v[44:47]\n"
        "buffer_load_dword v4, v2, s[40:43], s76 offen sc1\n"
        "v_mfma_f32_16x16x32_bf16 v[48:51], a[76:79], v[12:15], v[48:51]\n"
        "v_mfma_f32_16x16x32_bf16 v[52:55], a[80:83], v[12:15], v[52:55]\n"
        "ds_read_b128 v[12:15], v0 offset:16384\n"
        "v_mfma_f32_16x16x32_bf16 v[56:59], a[72:75], v[16:19], v[56:59]\n"
        "v_mfma_f32_16x16x32_bf16 v[60:63], a[76:79], v[16:19], v[60:63]\n"
        "v_mfma_f32_16x16x32_bf16 v[64:67], a[80:83], v[16:19], v[64:67]\n"
        "ds_read_b128 v[16:19], v0 offset:16640\n"
        "s_waitcnt lgkmcnt(2)\n"
        "v_mfma_f32_16x16x32_bf16 v[44:47], a[84:87], v[20:23], v[44:47]\n"
        "v_mfma_f32_16x16x32_bf16 v[48:51], a[88:91], v[20:23], v[48:51]\n"
        "v_mfma_f32_16x16x32_bf16 v[52:55], a[92:95], v[20:23], v[52:55]\n"
        "ds_read_b128 v[20:23], v0 offset:18432\n"
        "v_mfma_f32_16x16x32_bf16 v[56:59], a[84:87], v[24:27], v[56:59]\n"
        "v_mfma_f32_16x16x32_bf16 v[60:63], a[88:91], v[24:27], v[60:63]\n"
        "v_mfma_f32_16x16x32_bf16 v[64:67], a[92:95], v[24:27], v[64:67]\n"
        "ds_read_b128 v[24:27], v0 offset:18688\n"
        "s_waitcnt lgkmcnt(2)\n"
        "v_mfma_f32_16x16x32_bf16 v[44:47], a[96:99], v[12:15], v[44:47]\n"
        "v_mfma_f32_16x16x32_bf16 v[48:51], a[100:103], v[12:15], v[48:51]\n"
        "v_mfma_f32_16x16x32_bf16 v[52:55], a[104:107], v[12:15], v[52:55]\n"
        "ds_read_b128 v[12:15], v0 offset:20480\n"
        "v_mfma_f32_16x16x32_bf16 v[56:59], a[96:99], v[16:19], v[56:59]\n"
        "s_waitcnt vmcnt(1)\n"
        "v_mov_b32 v5, s74\n"
        "v_mfma_f32_16x16x32_bf16 v[60:63], a[100:103], v[16:19], v[60:63]\n"
        "buffer_store_dword v5, v3, s[40:43], s73 offen sc1\n"
        "s_add_u32 s68, s68, 1\n"
        "v_mfma_f32_16x16x32_bf16 v[64:67], a[104:107], v[16:19], v[64:67]\n"
        "ds_read_b128 v[16:19], v0 offset:20736\n"
        "s_waitcnt lgkmcnt(2)\n"
        "v_mfma_f32_16x16x32_bf16 v[44:47], a[108:111], v[20:23], v[44:47]\n"
        "s_lshr_b32 s66, s68, 2\n"
        "s_add_u32 s66, s66, 1\n"
        "v_mfma_f32_16x16x32_bf16 v[48:51], a[112:115], v[20:23], v[48:51]\n"
        "s_bitcmp1_b32 s55, s66\n"
        "s_cselect_b32 s66, 8192, 0\n"
        "v_mfma_f32_16x16x32_bf16 v[52:55], a[116:119], v[20:23], v[52:55]\n"
        "ds_read_b128 v[20:23], v0 offset:22528\n"
        "s_lshl_b32 s72, s68, 19\n"
        "v_mfma_f32_16x16x32_bf16 v[56:59], a[108:111], v[24:27], v[56:59]\n"
        "s_add_u32 s72, s72, s66\n"
        "s_add_u32 s72, s72, s59\n"
        "v_mfma_f32_16x16x32_bf16 v[60:63], a[112:115], v[24:27], v[60:63]\n"
        "s_mov_b32 s74, s68\n"
        "s_mov_b32 s73, s60\n"
        "v_mfma_f32_16x16x32_bf16 v[64:67], a[116:119], v[24:27], v[64:67]\n"
        "ds_read_b128 v[24:27], v0 offset:22784\n"
        "s_waitcnt lgkmcnt(2)\n"
        "v_mfma_f32_16x16x32_bf16 v[44:47], a[120:123], v[12:15], v[44:47]\n"
        "v_mfma_f32_16x16x32_bf16 v[48:51], a[124:127], v[12:15], v[48:51]\n"
        "s_barrier\n"
        "v_mfma_f32_16x16x32_bf16 v[52:55], a[128:131], v[12:15], v[52:55]\n"
        "ds_read_b128 v[12:15], v0 offset:24576\n"
        "v_mfma_f32_16x16x32_bf16 v[56:59], a[120:123], v[16:19], v[56:59]\n"
        "v_mfma_f32_16x16x32_bf16 v[60:63], a[124:127], v[16:19], v[60:63]\n"
        "v_mfma_f32_16x16x32_bf16 v[64:67], a[128:131], v[16:19], v[64:67]\n"
        "ds_read_b128 v[16:19], v0 offset:24832\n"
        "s_waitcnt lgkmcnt(2)\n"
        "v_mfma_f32_16x16x32_bf16 v[44:47], a[132:135], v[20:23], v[44:47]\n"
        "s_waitcnt vmcnt(1)\n"
        "v_cmp_gt_i32 vcc, s70, v4\n"
        "v_mfma_f32_16x16x32_bf16 v[48:51], a[136:139], v[20:23], v[48:51]\n"
        "s_and_b64 vcc, vcc, 63\n"
        "v_mfma_f32_16x16x32_bf16 v[52:55], a[140:143], v[20:23], v[52:55]\n"
        "ds_read_b128 v[20:23], v0 offset:26624\n"
        "v_mfma_f32_16x16x32_bf16 v[56:59], a[132:135], v[24:27], v[56:59]\n"
        "v_mfma_f32_16x16x32_bf16 v[60:63], a[136:139], v[24:27], v[60:63]\n"
        "v_mfma_f32_16x16x32_bf16 v[64:67], a[140:143], v[24:27], v[64:67]\n"
        "ds_read_b128 v[24:27], v0 offset:26880\n"
        "s_waitcnt lgkmcnt(2)\n"
        "s_cbranch_vccnz .Lf2_c10_%=\n"
        "s_branch .Lf2_c11_%=\n"
        ".Lf2_c00_%=:\n"
        "v_mfma_f32_16x16x32_bf16 v[44:47], a[0:3], v[12:15], 0\n"
        "v_cvt_pk_bf16_f32 v74, v60, v61\n"
        "v_cvt_pk_bf16_f32 v75, v62, v63\n"
        "v_mfma_f32_16x16x32_bf16 v[48:51], a[4:7], v[12:15], 0\n"
        "v_cvt_pk_bf16_f32 v78, v64, v65\n"
        "v_cvt_pk_bf16_f32 v79, v66, v67\n"
        "v_mfma_f32_16x16x32_bf16 v[52:55], a[8:11], v[12:15], 0\n"
        "ds_read_b128 v[12:15], v0 offset:4096\n"
        "v_mfma_f32_16x16x32_bf16 v[56:59], a[0:3], v[16:19], 0\n"
        "buffer_store_dwordx4 v[68:71], v6, s[40:43], s72 offen sc1\n"
        "v_mfma_f32_16x16x32_bf16 v[60:63], a[4:7], v[16:19], 0\n"
        "s_add_u32 s66, s72, 147456\n"
        "v_mfma_f32_16x16x32_bf16 v[64:67], a[8:11], v[16:19], 0\n"
        "ds_read_b128 v[16:19], v0 offset:4352\n"
        "s_waitcnt lgkmcnt(2)\n"
        "v_mfma_f32_16x16x32_bf16 v[44:47], a[12:15], v[20:23], v[44:47]\n"
        "buffer_store_dwordx4 v[72:75], v6, s[40:43], s66 offen sc1\n"
        "v_mfma_f32_16x16x32_bf16 v[48:51], a[16:19], v[20:23], v[48:51]\n"
        "v_permlane16_swap_b32 v76, v78\n"
        "v_mfma_f32_16x16x32_bf16 v[52:55], a[20:23], v[20:23], v[52:55]\n"
        "ds_read_b128 v[20:23], v0 offset:6144\n"
        "v_mfma_f32_16x16x32_bf16 v[56:59], a[12:15], v[24:27], v[56:59]\n"
        "v_permlane16_swap_b32 v77, v79\n"
        "v_mfma_f32_16x16x32_bf16 v[60:63], a[16:19], v[24:27], v[60:63]\n"
        "buffer_store_dwordx4 v[76:79], v7, s[40:43], s72 offen sc1\n"
        "v_mfma_f32_16x16x32_bf16 v[64:67], a[20:23], v[24:27], v[64:67]\n"
        "ds_read_b128 v[24:27], v0 offset:6400\n"
        "s_waitcnt lgkmcnt(2)\n"
        "v_mfma_f32_16x16x32_bf16 v[44:47], a[24:27], v[12:15], v[44:47]\n"
        "v_mfma_f32_16x16x32_bf16 v[48:51], a[28:31], v[12:15], v[48:51]\n"
        "v_mfma_f32_16x16x32_bf16 v[52:55], a[32:35], v[12:15], v[52:55]\n"
        "ds_read_b128 v[12:15], v0 offset:8192\n"
        "v_mfma_f32_16x16x32_bf16 v[56:59], a[24:27], v[16:19], v[56:59]\n"
        "v_mfma_f32_16x16x32_bf16 v[60:63], a[28:31], v[16:19], v[60:63]\n"
        "v_mfma_f32_16x16x32_bf16 v[64:67], a[32:35], v[16:19], v[64:67]\n"
        "ds_read_b128 v[16:19], v0 offset:8448\n"
        "s_waitcnt lgkmcnt(2)\n"
        "v_mfma_f32_16x16x32_bf16 v[44:47], a[36:39], v[20:23], v[44:47]\n"
        "v_mfma_f32_16x16x32_bf16 v[48:51], a[40:43], v[20:23], v[48:51]\n"
        "v_mfma_f32_16x16x32_bf16 v[52:55], a[44:47], v[20:23], v[52:55]\n"
        "ds_read_b128 v[20:23], v0 offset:10240\n"
        "v_mfma_f32_16x16x32_bf16 v[56:59], a[36:39], v[24:27], v[56:59]\n"
        "v_mfma_f32_16x16x32_bf16 v[60:63], a[40:43], v[24:27], v[60:63]\n"
        "v_mfma_f32_16x16x32_bf16 v[64:67], a[44:47], v[24:27], v[64:67]\n"
        "ds_read_b128 v[24:27], v0 offset:10496\n"
        "s_waitcnt lgkmcnt(2)\n"
        "v_mfma_f32_16x16x32_bf16 v[44:47], a[48:51], v[12:15], v[44:47]\n"
        "v_mfma_f32_16x16x32_bf16 v[48:51], a[52:55], v[12:15], v[48:51]\n"
        "v_mfma_f32_16x16x32_bf16 v[52:55], a[56:59], v[12:15], v[52:55]\n"
        "ds_read_b128 v[12:15], v0 offset:12288\n"
        "v_mfma_f32_16x16x32_bf16 v[56:59], a[48:51], v[16:19], v[56:59]\n"
        "v_mfma_f32_16x16x32_bf16 v[60:63], a[52:55], v[16:19], v[60:63]\n"
        "v_mfma_f32_16x16x32_bf16 v[64:67], a[56:59], v[16:19], v[64:67]\n"
        "ds_read_b128 v[16:19], v0 offset:12544\n"
        "s_waitcnt lgkmcnt(2)\n"
        "v_mfma_f32_16x16x32_bf16 v[44:47], a[60:63], v[20:23], v[44:47]\n"
        "v_mfma_f32_16x16x32_bf16 v[48:51], a[64:67], v[20:23], v[48:51]\n"
        "v_mfma_f32_16x16x32_bf16 v[52:55], a[68:71], v[20:23], v[52:55]\n"
        "ds_read_b128 v[20:23], v0 offset:14336\n"
        "v_mfma_f32_16x16x32_bf16 v[56:59], a[60:63], v[24:27], v[56:59]\n"
        "v_mfma_f32_16x16x32_bf16 v[60:63], a[64:67], v[24:27], v[60:63]\n"
        "v_mfma_f32_16x16x32_bf16 v[64:67], a[68:71], v[24:27], v[64:67]\n"
        "ds_read_b128 v[24:27], v0 offset:14592\n"
        "s_waitcnt lgkmcnt(2)\n"
        "v_mfma_f32_16x16x32_bf16 v[44:47], a[72:75], v[12:15], v[44:47]\n"
        "buffer_load_dword v4, v2, s[40:43], s76 offen sc1\n"
        "v_mfma_f32_16x16x32_bf16 v[48:51], a[76:79], v[12:15], v[48:51]\n"
        "v_mfma_f32_16x16x32_bf16 v[52:55], a[80:83], v[12:15], v[52:55]\n"
        "ds_read_b128 v[12:15], v0 offset:16384\n"
        "v_mfma_f32_16x16x32_bf16 v[56:59], a[72:75], v[16:19], v[56:59]\n"
        "v_mfma_f32_16x16x32_bf16 v[60:63], a[76:79], v[16:19], v[60:63]\n"
        "s_waitcnt vmcnt(1)\n"
        "v_mov_b32 v5, s74\n"
        "v_mfma_f32_16x16x32_bf16 v[64:67], a[80:83], v[16:19], v[64:67]\n"
        "ds_read_b128 v[16:19], v0 offset:16640\n"
        "s_waitcnt lgkmcnt(2)\n"
        "v_mfma_f32_16x16x32_bf16 v[44:47], a[84:87], v[20:23], v[44:47]\n"
        "buffer_store_dword v5, v3, s[40:43], s73 offen sc1\n"
        "s_add_u32 s68, s68, 1\n"
        "v_mfma_f32_16x16x32_bf16 v[48:51], a[88:91], v[20:23], v[48:51]\n"
        "s_lshr_b32 s66, s68, 2\n"
        "s_add_u32 s66, s66, 1\n"
        "v_mfma_f32_16x16x32_bf16 v[52:55], a[92:95], v[20:23], v[52:55]\n"
        "ds_read_b128 v[20:23], v0 offset:18432\n"
        "v_mfma_f32_16x16x32_bf16 v[56:59], a[84:87], v[24:27], v[56:59]\n"
        "s_bitcmp1_b32 s55, s66\n"
        "s_cselect_b32 s66, 8192, 0\n"
        "v_mfma_f32_16x16x32_bf16 v[60:63], a[88:91], v[24:27], v[60:63]\n"
        "s_lshl_b32 s72, s68, 19\n"
        "s_add_u32 s72, s72, s66\n"
        "v_mfma_f32_16x16x32_bf16 v[64:67], a[92:95], v[24:27], v[64:67]\n"
        "ds_read_b128 v[24:27], v0 offset:18688\n"
        "s_waitcnt lgkmcnt(2)\n"
        "v_mfma_f32_16x16x32_bf16 v[44:47], a[96:99], v[12:15], v[44:47]\n"
        "s_add_u32 s72, s72, s59\n"
        "s_mov_b32 s74, s68\n"
        "v_mfma_f32_16x16x32_bf16 v[48:51], a[100:103], v[12:15], v[48:51]\n"
        "s_mov_b32 s73, s60\n"
        "v_mfma_f32_16x16x32_bf16 v[52:55], a[104:107], v[12:15], v[52:55]\n"
        "ds_read_b128 v[12:15], v0 offset:20480\n"
        "v_mfma_f32_16x16x32_bf16 v[56:59], a[96:99], v[16:19], v[56:59]\n"
        "v_mfma_f32_16x16x32_bf16 v[60:63], a[100:103], v[16:19], v[60:63]\n"
        "v_mfma_f32_16x16x32_bf16 v[64:67], a[104:107], v[16:19], v[64:67]\n"
        "ds_read_b128 v[16:19], v0 offset:20736\n"
        "s_waitcnt lgkmcnt(2)\n"
        "v_mfma_f32_16x16x32_bf16 v[44:47], a[108:111], v[20:23], v[44:47]\n"
        "v_mfma_f32_16x16x32_bf16 v[48:51], a[112:115], v[20:23], v[48:51]\n"
        "v_mfma_f32_16x16x32_bf16 v[52:55], a[116:119], v[20:23], v[52:55]\n"
        "ds_read_b128 v[20:23], v0 offset:22528\n"
        "v_mfma_f32_16x16x32_bf16 v[56:59], a[108:111], v[24:27], v[56:59]\n"
        "v_mfma_f32_16x16x32_bf16 v[60:63], a[112:115], v[24:27], v[60:63]\n"
        "v_mfma_f32_16x16x32_bf16 v[64:67], a[116:119], v[24:27], v[64:67]\n"
        "ds_read_b128 v[24:27], v0 offset:22784\n"
        "s_waitcnt lgkmcnt(2)\n"
        "v_mfma_f32_16x16x32_bf16 v[44:47], a[120:123], v[12:15], v[44:47]\n"
        "v_mfma_f32_16x16x32_bf16 v[48:51], a[124:127], v[12:15], v[48:51]\n"
        "v_mfma_f32_16x16x32_bf16 v[52:55], a[128:131], v[12:15], v[52:55]\n"
        "v_mfma_f32_16x16x32_bf16 v[56:59], a[120:123], v[16:19], v[56:59]\n"
        "v_mfma_f32_16x16x32_bf16 v[60:63], a[124:127], v[16:19], v[60:63]\n"
        "v_mfma_f32_16x16x32_bf16 v[64:67], a[128:131], v[16:19], v[64:67]\n"
        "s_waitcnt lgkmcnt(0)\n"
        "v_mfma_f32_16x16x32_bf16 v[44:47], a[132:135], v[20:23], v[44:47]\n"
        "v_mfma_f32_16x16x32_bf16 v[48:51], a[136:139], v[20:23], v[48:51]\n"
        "v_mfma_f32_16x16x32_bf16 v[52:55], a[140:143], v[20:23], v[52:55]\n"
        "v_mfma_f32_16x16x32_bf16 v[56:59], a[132:135], v[24:27], v[56:59]\n"
        "v_mfma_f32_16x16x32_bf16 v[60:63], a[136:139], v[24:27], v[60:63]\n"
        "v_mfma_f32_16x16x32_bf16 v[64:67], a[140:143], v[24:27], v[64:67]\n"
        "s_waitcnt vmcnt(1)\n"
        "v_cmp_gt_i32 vcc, s70, v4\n"
        "s_nop 0\n"
        "s_and_b64 vcc, vcc, 63\n"
        "s_cbranch_vccnz .Lf2_late0_%=\n"
        "s_add_u32 m0, s61, 24576\n"
        "s_add_u32 s71, s71, 16384\n"
        "buffer_load_dwordx4 v1, s[40:43], s71 offen offset:0 sc1 lds\n"
        "buffer_load_dwordx4 v1, s[40:43], s71 offen offset:1024 sc1 lds\n"
        "s_add_u32 m0, m0, 8192\n"
        "s_add_u32 s71, s71, 16384\n"
        "buffer_load_dwordx4 v1, s[40:43], s71 offen offset:0 sc1 lds\n"
        "buffer_load_dwordx4 v1, s[40:43], s71 offen offset:1024 sc1 lds\n"
        "s_add_u32 m0, m0, 8192\n"
        "s_add_u32 s71, s71, 16384\n"
        "buffer_load_dwordx4 v1, s[40:43], s71 offen offset:0 sc1 lds\n"
        "buffer_load_dwordx4 v1, s[40:43], s71 offen offset:1024 sc1 lds\n"
        "s_add_u32 s70, s70, 1\n"
        "s_branch .Lf2_go0_%=\n"
        ".Lf2_late0_%=:\n"
        "s_mov_b32 s64, 0\n"
        ".Lf2_in0_top_%=:\n"
        "buffer_load_dword v4, v2, s[40:43], s76 offen sc1\n"
        "s_waitcnt vmcnt(0)\n"
        "v_cmp_gt_i32 vcc, s70, v4\n"
        "s_nop 0\n"
        "s_and_b64 vcc, vcc, 63\n"
        "s_cbranch_vccz .Lf2_in0_ok_%=\n"
        "s_add_u32 s64, s64, 1\n"
        "s_and_b32 s65, s64, 255\n"
        "s_cbranch_scc1 .Lf2_in0_go_%=\n"
        "s_mov_b32 s65, 40960\n"
        "s_cmp_lt_u32 s64, 1048576\n"
        "s_cbranch_scc1 .Lf2_in0_chk_%=\n"
        "v_mov_b32 v4, 1\n"
        "buffer_store_dword v4, off, s[40:43], s65 sc1\n"
        "s_waitcnt vmcnt(0)\n"
        ".Lf2_in0_chk_%=:\n"
        "buffer_load_dword v4, off, s[40:43], s65 sc1\n"
        "s_waitcnt vmcnt(0)\n"
        "v_readfirstlane_b32 s65, v4\n"
        "s_nop 0\n"
        "s_cmp_lg_u32 s65, 1\n"
        "s_cbranch_scc1 .Lf2_in0_go_%=\n"
        "s_endpgm\n"
        ".Lf2_in0_go_%=:\n"
        "s_sleep 1\n"
        "s_branch .Lf2_in0_top_%=\n"
        ".Lf2_in0_ok_%=:\n"
        "s_add_u32 m0, s61, 24576\n"
        "s_add_u32 s71, s71, 16384\n"
        "buffer_load_dwordx4 v1, s[40:43], s71 offen offset:0 sc1 lds\n"
        "buffer_load_dwordx4 v1, s[40:43], s71 offen offset:1024 sc1 lds\n"
        "s_add_u32 m0, m0, 8192\n"
        "s_add_u32 s71, s71, 16384\n"
        "buffer_load_dwordx4 v1, s[40:43], s71 offen offset:0 sc1 lds\n"
        "buffer_load_dwordx4 v1, s[40:43], s71 offen offset:1024 sc1 lds\n"
        "s_add_u32 m0, m0, 8192\n"
        "s_add_u32 s71, s71, 16384\n"
        "buffer_load_dwordx4 v1, s[40:43], s71 offen offset:0 sc1 lds\n"
        "buffer_load_dwordx4 v1, s[40:43], s71 offen offset:1024 sc1 lds\n"
        "s_add_u32 s70, s70, 1\n"
        ".Lf2_go0_%=:\n"
        "buffer_load_dword v4, v2, s[40:43], s76 offen sc1\n"
        "s_waitcnt vmcnt(0)\n"
        "s_barrier\n"
        "ds_read_b128 v[12:15], v0 offset:24576\n"
        "ds_read_b128 v[16:19], v0 offset:24832\n"
        "ds_read_b128 v[20:23], v0 offset:26624\n"
        "ds_read_b128 v[24:27], v0 offset:26880\n"
        "v_cmp_gt_i32 vcc, s70, v4\n"
        "s_nop 0\n"
        "s_and_b64 vcc, vcc, 63\n"
        "s_waitcnt lgkmcnt(2)\n"
        "s_cbranch_vccnz .Lf2_c10_%=\n"
        "s_branch .Lf2_c11_%=\n"
        ".Lf2_c11_%=:\n"
        "v_mfma_f32_16x16x32_bf16 v[44:47], a[144:147], v[12:15], v[44:47]\n"
        "s_add_u32 m0, s61, 49152\n"
        "s_add_u32 s71, s71, 16384\n"
        "v_mfma_f32_16x16x32_bf16 v[48:51], a[148:151], v[12:15], v[48:51]\n"
        "buffer_load_dwordx4 v1, s[40:43], s71 offen offset:0 sc1 lds\n"
        "v_mfma_f32_16x16x32_bf16 v[52:55], a[152:155], v[12:15], v[52:55]\n"
        "ds_read_b128 v[12:15], v0 offset:28672\n"
        "v_mfma_f32_16x16x32_bf16 v[56:59], a[144:147], v[16:19], v[56:59]\n"
        "v_mfma_f32_16x16x32_bf16 v[60:63], a[148:151], v[16:19], v[60:63]\n"
        "buffer_load_dwordx4 v1, s[40:43], s71 offen offset:1024 sc1 lds\n"
        "s_add_u32 m0, m0, 8192\n"
        "v_mfma_f32_16x16x32_bf16 v[64:67], a[152:155], v[16:19], v[64:67]\n"
        "ds_read_b128 v[16:19], v0 offset:28928\n"
        "s_waitcnt lgkmcnt(2)\n"
        "v_mfma_f32_16x16x32_bf16 v[44:47], a[156:159], v[20:23], v[44:47]\n"
        "s_add_u32 s71, s71, 16384\n"
        "v_mfma_f32_16x16x32_bf16 v[48:51], a[160:163], v[20:23], v[48:51]\n"
        "buffer_load_dwordx4 v1, s[40:43], s71 offen offset:0 sc1 lds\n"
        "v_mfma_f32_16x16x32_bf16 v[52:55], a[164:167], v[20:23], v[52:55]\n"
        "ds_read_b128 v[20:23], v0 offset:30720\n"
        "v_mfma_f32_16x16x32_bf16 v[56:59], a[156:159], v[24:27], v[56:59]\n"
        "v_mfma_f32_16x16x32_bf16 v[60:63], a[160:163], v[24:27], v[60:63]\n"
        "buffer_load_dwordx4 v1, s[40:43], s71 offen offset:1024 sc1 lds\n"
        "s_add_u32 m0, m0, 8192\n"
        "v_mfma_f32_16x16x32_bf16 v[64:67], a[164:167], v[24:27], v[64:67]\n"
        "ds_read_b128 v[24:27], v0 offset:30976\n"
        "s_waitcnt lgkmcnt(2)\n"
        "v_mfma_f32_16x16x32_bf16 v[44:47], a[168:171], v[12:15], v[44:47]\n"
        "s_add_u32 s71, s71, 16384\n"
        "v_mfma_f32_16x16x32_bf16 v[48:51], a[172:175], v[12:15], v[48:51]\n"
        "buffer_load_dwordx4 v1, s[40:43], s71 offen offset:0 sc1 lds\n"
        "v_mfma_f32_16x16x32_bf16 v[52:55], a[176:179], v[12:15], v[52:55]\n"
        "ds_read_b128 v[12:15], v0 offset:32768\n"
        "v_mfma_f32_16x16x32_bf16 v[56:59], a[168:171], v[16:19], v[56:59]\n"
        "v_mfma_f32_16x16x32_bf16 v[60:63], a[172:175], v[16:19], v[60:63]\n"
        "buffer_load_dwordx4 v1, s[40:43], s71 offen offset:1024 sc1 lds\n"
        "s_add_u32 s69, s69, 1\n"
        "v_mfma_f32_16x16x32_bf16 v[64:67], a[176:179], v[16:19], v[64:67]\n"
        "ds_read_b128 v[16:19], v0 offset:33024\n"
        "s_waitcnt lgkmcnt(2)\n"
        "v_mfma_f32_16x16x32_bf16 v[44:47], a[180:183], v[20:23], v[44:47]\n"
        "s_add_u32 s70, s70, 1\n"
        "v_mfma_f32_16x16x32_bf16 v[48:51], a[184:187], v[20:23], v[48:51]\n"
        "v_mfma_f32_16x16x32_bf16 v[52:55], a[188:191], v[20:23], v[52:55]\n"
        "ds_read_b128 v[20:23], v0 offset:34816\n"
        "v_mfma_f32_16x16x32_bf16 v[56:59], a[180:183], v[24:27], v[56:59]\n"
        "v_mfma_f32_16x16x32_bf16 v[60:63], a[184:187], v[24:27], v[60:63]\n"
        "v_mfma_f32_16x16x32_bf16 v[64:67], a[188:191], v[24:27], v[64:67]\n"
        "ds_read_b128 v[24:27], v0 offset:35072\n"
        "s_waitcnt lgkmcnt(2)\n"
        "v_mfma_f32_16x16x32_bf16 v[44:47], a[192:195], v[12:15], v[44:47]\n"
        "v_mfma_f32_16x16x32_bf16 v[48:51], a[196:199], v[12:15], v[48:51]\n"
        "v_mfma_f32_16x16x32_bf16 v[52:55], a[200:203], v[12:15], v[52:55]\n"
        "ds_read_b128 v[12:15], v0 offset:36864\n"
        "v_mfma_f32_16x16x32_bf16 v[56:59], a[192:195], v[16:19], v[56:59]\n"
        "v_mfma_f32_16x16x32_bf16 v[60:63], a[196:199], v[16:19], v[60:63]\n"
        "v_mfma_f32_16x16x32_bf16 v[64:67], a[200:203], v[16:19], v[64:67]\n"
        "ds_read_b128 v[16:19], v0 offset:37120\n"
        "s_waitcnt lgkmcnt(2)\n"
        "v_mfma_f32_16x16x32_bf16 v[44:47], a[204:207], v[20:23], v[44:47]\n"
        "v_mfma_f32_16x16x32_bf16 v[48:51], a[208:211], v[20:23], v[48:51]\n"
        "v_mfma_f32_16x16x32_bf16 v[52:55], a[212:215], v[20:23], v[52:55]\n"
        "ds_read_b128 v[20:23], v0 offset:38912\n"
        "v_mfma_f32_16x16x32_bf16 v[56:59], a[204:207], v[24:27], v[56:59]\n"
        "v_mfma_f32_16x16x32_bf16 v[60:63], a[208:211], v[24:27], v[60:63]\n"
        "v_mfma_f32_16x16x32_bf16 v[64:67], a[212:215], v[24:27], v[64:67]\n"
        "ds_read_b128 v[24:27], v0 offset:39168\n"
        "s_waitcnt lgkmcnt(2)\n"
        "v_mfma_f32_16x16x32_bf16 v[44:47], a[216:219], v[12:15], v[44:47]\n"
        "buffer_load_dword v4, v2, s[40:43], s76 offen sc1\n"
        "v_mfma_f32_16x16x32_bf16 v[48:51], a[220:223], v[12:15], v[48:51]\n"
        "v_mfma_f32_16x16x32_bf16 v[52:55], a[224:227], v[12:15], v[52:55]\n"
        "ds_read_b128 v[12:15], v0 offset:40960\n"
        "v_mfma_f32_16x16x32_bf16 v[56:59], a[216:219], v[16:19], v[56:59]\n"
        "v_mfma_f32_16x16x32_bf16 v[60:63], a[220:223], v[16:19], v[60:63]\n"
        "v_mfma_f32_16x16x32_bf16 v[64:67], a[224:227], v[16:19], v[64:67]\n"
        "ds_read_b128 v[16:19], v0 offset:41216\n"
        "s_waitcnt lgkmcnt(2)\n"
        "v_mfma_f32_16x16x32_bf16 v[44:47], a[228:231], v[20:23], v[44:47]\n"
        "v_mfma_f32_16x16x32_bf16 v[48:51], a[232:235], v[20:23], v[48:51]\n"
        "v_mfma_f32_16x16x32_bf16 v[52:55], a[236:239], v[20:23], v[52:55]\n"
        "ds_read_b128 v[20:23], v0 offset:43008\n"
        "v_mfma_f32_16x16x32_bf16 v[56:59], a[228:231], v[24:27], v[56:59]\n"
        "v_mfma_f32_16x16x32_bf16 v[60:63], a[232:235], v[24:27], v[60:63]\n"
        "v_mfma_f32_16x16x32_bf16 v[64:67], a[236:239], v[24:27], v[64:67]\n"
        "ds_read_b128 v[24:27], v0 offset:43264\n"
        "s_waitcnt lgkmcnt(2)\n"
        "v_mfma_f32_16x16x32_bf16 v[44:47], a[240:243], v[12:15], v[44:47]\n"
        "v_mfma_f32_16x16x32_bf16 v[48:51], a[244:247], v[12:15], v[48:51]\n"
        "v_mfma_f32_16x16x32_bf16 v[52:55], a[248:251], v[12:15], v[52:55]\n"
        "ds_read_b128 v[12:15], v0 offset:45056\n"
        "v_mfma_f32_16x16x32_bf16 v[56:59], a[240:243], v[16:19], v[56:59]\n"
        "v_mfma_f32_16x16x32_bf16 v[60:63], a[244:247], v[16:19], v[60:63]\n"
        "v_mfma_f32_16x16x32_bf16 v[64:67], a[248:251], v[16:19], v[64:67]\n"
        "ds_read_b128 v[16:19], v0 offset:45312\n"
        "s_waitcnt lgkmcnt(2)\n"
        "v_mfma_f32_16x16x32_bf16 v[44:47], a[252:255], v[20:23], v[44:47]\n"
        "v_mfma_f32_16x16x32_bf16 v[48:51], v[80:83], v[20:23], v[48:51]\n"
        "v_mfma_f32_16x16x32_bf16 v[52:55], v[84:87], v[20:23], v[52:55]\n"
        "ds_read_b128 v[20:23], v0 offset:47104\n"
        "v_mfma_f32_16x16x32_bf16 v[56:59], a[252:255], v[24:27], v[56:59]\n"
        "v_mfma_f32_16x16x32_bf16 v[60:63], v[80:83], v[24:27], v[60:63]\n"
        "v_mfma_f32_16x16x32_bf16 v[64:67], v[84:87], v[24:27], v[64:67]\n"
        "ds_read_b128 v[24:27], v0 offset:47360\n"
        "s_waitcnt lgkmcnt(2)\n"
        "v_mfma_f32_16x16x32_bf16 v[44:47], v[88:91], v[12:15], v[44:47]\n"
        "v_mfma_f32_16x16x32_bf16 v[48:51], v[92:95], v[12:15], v[48:51]\n"
        "s_waitcnt vmcnt(1)\n"
        "s_barrier\n"
        "v_mfma_f32_16x16x32_bf16 v[52:55], v[96:99], v[12:15], v[52:55]\n"
        "ds_read_b128 v[12:15], v8 offset:0\n"
        "v_mfma_f32_16x16x32_bf16 v[56:59], v[88:91], v[16:19], v[56:59]\n"
        "v_mfma_f32_16x16x32_bf16 v[60:63], v[92:95], v[16:19], v[60:63]\n"
        "v_mfma_f32_16x16x32_bf16 v[64:67], v[96:99], v[16:19], v[64:67]\n"
        "ds_read_b128 v[16:19], v8 offset:256\n"
        "s_waitcnt lgkmcnt(2)\n"
        "v_mfma_f32_16x16x32_bf16 v[44:47], v[100:103], v[20:23], v[44:47]\n"
        "s_waitcnt vmcnt(0)\n"
        "v_cmp_gt_i32 vcc, s70, v4\n"
        "v_mfma_f32_16x16x32_bf16 v[48:51], v[104:107], v[20:23], v[48:51]\n"
        "s_and_b64 vcc, vcc, 63\n"
        "v_mfma_f32_16x16x32_bf16 v[52:55], v[108:111], v[20:23], v[52:55]\n"
        "ds_read_b128 v[20:23], v8 offset:2048\n"
        "v_mfma_f32_16x16x32_bf16 v[56:59], v[100:103], v[24:27], v[56:59]\n"
        "v_mfma_f32_16x16x32_bf16 v[60:63], v[104:107], v[24:27], v[60:63]\n"
        "v_mfma_f32_16x16x32_bf16 v[64:67], v[108:111], v[24:27], v[64:67]\n"
        "ds_read_b128 v[24:27], v8 offset:2304\n"
        "s_waitcnt lgkmcnt(2)\n"
        "s_cbranch_vccnz .Lf2_c20_%=\n"
        "s_branch .Lf2_c21_%=\n"
        ".Lf2_c10_%=:\n"
        "v_mfma_f32_16x16x32_bf16 v[44:47], a[144:147], v[12:15], v[44:47]\n"
        "v_mfma_f32_16x16x32_bf16 v[48:51], a[148:151], v[12:15], v[48:51]\n"
        "v_mfma_f32_16x16x32_bf16 v[52:55], a[152:155], v[12:15], v[52:55]\n"
        "ds_read_b128 v[12:15], v0 offset:28672\n"
        "v_mfma_f32_16x16x32_bf16 v[56:59], a[144:147], v[16:19], v[56:59]\n"
        "v_mfma_f32_16x16x32_bf16 v[60:63], a[148:151], v[16:19], v[60:63]\n"
        "v_mfma_f32_16x16x32_bf16 v[64:67], a[152:155], v[16:19], v[64:67]\n"
        "ds_read_b128 v[16:19], v0 offset:28928\n"
        "s_waitcnt lgkmcnt(2)\n"
        "v_mfma_f32_16x16x32_bf16 v[44:47], a[156:159], v[20:23], v[44:47]\n"
        "v_mfma_f32_16x16x32_bf16 v[48:51], a[160:163], v[20:23], v[48:51]\n"
        "v_mfma_f32_16x16x32_bf16 v[52:55], a[164:167], v[20:23], v[52:55]\n"
        "ds_read_b128 v[20:23], v0 offset:30720\n"
        "v_mfma_f32_16x16x32_bf16 v[56:59], a[156:159], v[24:27], v[56:59]\n"
        "v_mfma_f32_16x16x32_bf16 v[60:63], a[160:163], v[24:27], v[60:63]\n"
        "v_mfma_f32_16x16x32_bf16 v[64:67], a[164:167], v[24:27], v[64:67]\n"
        "ds_read_b128 v[24:27], v0 offset:30976\n"
        "s_waitcnt lgkmcnt(2)\n"
        "v_mfma_f32_16x16x32_bf16 v[44:47], a[168:171], v[12:15], v[44:47]\n"
        "v_mfma_f32_16x16x32_bf16 v[48:51], a[172:175], v[12:15], v[48:51]\n"
        "v_mfma_f32_16x16x32_bf16 v[52:55], a[176:179], v[12:15], v[52:55]\n"
        "ds_read_b128 v[12:15], v0 offset:32768\n"
        "v_mfma_f32_16x16x32_bf16 v[56:59], a[168:171], v[16:19], v[56:59]\n"
        "v_mfma_f32_16x16x32_bf16 v[60:63], a[172:175], v[16:19], v[60:63]\n"
        "v_mfma_f32_16x16x32_bf16 v[64:67], a[176:179], v[16:19], v[64:67]\n"
        "ds_read_b128 v[16:19], v0 offset:33024\n"
        "s_waitcnt lgkmcnt(2)\n"
        "v_mfma_f32_16x16x32_bf16 v[44:47], a[180:183], v[20:23], v[44:47]\n"
        "v_mfma_f32_16x16x32_bf16 v[48:51], a[184:187], v[20:23], v[48:51]\n"
        "v_mfma_f32_16x16x32_bf16 v[52:55], a[188:191], v[20:23], v[52:55]\n"
        "ds_read_b128 v[20:23], v0 offset:34816\n"
        "v_mfma_f32_16x16x32_bf16 v[56:59], a[180:183], v[24:27], v[56:59]\n"
        "v_mfma_f32_16x16x32_bf16 v[60:63], a[184:187], v[24:27], v[60:63]\n"
        "v_mfma_f32_16x16x32_bf16 v[64:67], a[188:191], v[24:27], v[64:67]\n"
        "ds_read_b128 v[24:27], v0 offset:35072\n"
        "s_waitcnt lgkmcnt(2)\n"
        "v_mfma_f32_16x16x32_bf16 v[44:47], a[192:195], v[12:15], v[44:47]\n"
        "v_mfma_f32_16x16x32_bf16 v[48:51], a[196:199], v[12:15], v[48:51]\n"
        "v_mfma_f32_16x16x32_bf16 v[52:55], a[200:203], v[12:15], v[52:55]\n"
        "ds_read_b128 v[12:15], v0 offset:36864\n"
        "v_mfma_f32_16x16x32_bf16 v[56:59], a[192:195], v[16:19], v[56:59]\n"
        "v_mfma_f32_16x16x32_bf16 v[60:63], a[196:199], v[16:19], v[60:63]\n"
        "v_mfma_f32_16x16x32_bf16 v[64:67], a[200:203], v[16:19], v[64:67]\n"
        "ds_read_b128 v[16:19], v0 offset:37120\n"
        "s_waitcnt lgkmcnt(2)\n"
        "v_mfma_f32_16x16x32_bf16 v[44:47], a[204:207], v[20:23], v[44:47]\n"
        "v_mfma_f32_16x16x32_bf16 v[48:51], a[208:211], v[20:23], v[48:51]\n"
        "v_mfma_f32_16x16x32_bf16 v[52:55], a[212:215], v[20:23], v[52:55]\n"
        "ds_read_b128 v[20:23], v0 offset:38912\n"
        "v_mfma_f32_16x16x32_bf16 v[56:59], a[204:207], v[24:27], v[56:59]\n"
        "v_mfma_f32_16x16x32_bf16 v[60:63], a[208:211], v[24:27], v[60:63]\n"
        "v_mfma_f32_16x16x32_bf16 v[64:67], a[212:215], v[24:27], v[64:67]\n"
        "ds_read_b128 v[24:27], v0 offset:39168\n"
        "s_waitcnt lgkmcnt(2)\n"
        "v_mfma_f32_16x16x32_bf16 v[44:47], a[216:219], v[12:15], v[44:47]\n"
        "buffer_load_dword v4, v2, s[40:43], s76 offen sc1\n"
        "v_mfma_f32_16x16x32_bf16 v[48:51], a[220:223], v[12:15], v[48:51]\n"
        "v_mfma_f32_16x16x32_bf16 v[52:55], a[224:227], v[12:15], v[52:55]\n"
        "ds_read_b128 v[12:15], v0 offset:40960\n"
        "v_mfma_f32_16x16x32_bf16 v[56:59], a[216:219], v[16:19], v[56:59]\n"
        "v_mfma_f32_16x16x32_bf16 v[60:63], a[220:223], v[16:19], v[60:63]\n"
        "v_mfma_f32_16x16x32_bf16 v[64:67], a[224:227], v[16:19], v[64:67]\n"
        "ds_read_b128 v[16:19], v0 offset:41216\n"
        "s_waitcnt lgkmcnt(2)\n"
        "v_mfma_f32_16x16x32_bf16 v[44:47], a[228:231], v[20:23], v[44:47]\n"
        "v_mfma_f32_16x16x32_bf16 v[48:51], a[232:235], v[20:23], v[48:51]\n"
        "v_mfma_f32_16x16x32_bf16 v[52:55], a[236:239], v[20:23], v[52:55]\n"
        "ds_read_b128 v[20:23], v0 offset:43008\n"
        "v_mfma_f32_16x16x32_bf16 v[56:59], a[228:231], v[24:27], v[56:59]\n"
        "v_mfma_f32_16x16x32_bf16 v[60:63], a[232:235], v[24:27], v[60:63]\n"
        "v_mfma_f32_16x16x32_bf16 v[64:67], a[236:239], v[24:27], v[64:67]\n"
        "ds_read_b128 v[24:27], v0 offset:43264\n"
        "s_waitcnt lgkmcnt(2)\n"
        "v_mfma_f32_16x16x32_bf16 v[44:47], a[240:243], v[12:15], v[44:47]\n"
        "v_mfma_f32_16x16x32_bf16 v[48:51], a[244:247], v[12:15], v[48:51]\n"
        "v_mfma_f32_16x16x32_bf16 v[52:55], a[248:251], v[12:15], v[52:55]\n"
        "ds_read_b128 v[12:15], v0 offset:45056\n"
        "v_mfma_f32_16x16x32_bf16 v[56:59], a[240:243], v[16:19], v[56:59]\n"
        "v_mfma_f32_16x16x32_bf16 v[60:63], a[244:247], v[16:19], v[60:63]\n"
        "v_mfma_f32_16x16x32_bf16 v[64:67], a[248:251], v[16:19], v[64:67]\n"
        "ds_read_b128 v[16:19], v0 offset:45312\n"
        "s_waitcnt lgkmcnt(2)\n"
        "v_mfma_f32_16x16x32_bf16 v[44:47], a[252:255], v[20:23], v[44:47]\n"
        "v_mfma_f32_16x16x32_bf16 v[48:51], v[80:83], v[20:23], v[48:51]\n"
        "v_mfma_f32_16x16x32_bf16 v[52:55], v[84:87], v[20:23], v[52:55]\n"
        "ds_read_b128 v[20:23], v0 offset:47104\n"
        "v_mfma_f32_16x16x32_bf16 v[56:59], a[252:255], v[24:27], v[56:59]\n"
        "v_mfma_f32_16x16x32_bf16 v[60:63], v[80:83], v[24:27], v[60:63]\n"
        "v_mfma_f32_16x16x32_bf16 v[64:67], v[84:87], v[24:27], v[64:67]\n"
        "ds_read_b128 v[24:27], v0 offset:47360\n"
        "s_waitcnt lgkmcnt(2)\n"
        "v_mfma_f32_16x16x32_bf16 v[44:47], v[88:91], v[12:15], v[44:47]\n"
        "v_mfma_f32_16x16x32_bf16 v[48:51], v[92:95], v[12:15], v[48:51]\n"
        "v_mfma_f32_16x16x32_bf16 v[52:55], v[96:99], v[12:15], v[52:55]\n"
        "v_mfma_f32_16x16x32_bf16 v[56:59], v[88:91], v[16:19], v[56:59]\n"
        "v_mfma_f32_16x16x32_bf16 v[60:63], v[92:95], v[16:19], v[60:63]\n"
        "v_mfma_f32_16x16x32_bf16 v[64:67], v[96:99], v[16:19], v[64:67]\n"
        "s_waitcnt lgkmcnt(0)\n"
        "v_mfma_f32_16x16x32_bf16 v[44:47], v[100:103], v[20:23], v[44:47]\n"
        "v_mfma_f32_16x16x32_bf16 v[48:51], v[104:107], v[20:23], v[48:51]\n"
        "v_mfma_f32_16x16x32_bf16 v[52:55], v[108:111], v[20:23], v[52:55]\n"
        "v_mfma_f32_16x16x32_bf16 v[56:59], v[100:103], v[24:27], v[56:59]\n"
        "v_mfma_f32_16x16x32_bf16 v[60:63], v[104:107], v[24:27], v[60:63]\n"
        "v_mfma_f32_16x16x32_bf16 v[64:67], v[108:111], v[24:27], v[64:67]\n"
        "s_waitcnt vmcnt(0)\n"
        "v_cmp_gt_i32 vcc, s70, v4\n"
        "s_nop 0\n"
        "s_and_b64 vcc, vcc, 63\n"
        "s_cbranch_vccnz .Lf2_late1_%=\n"
        "s_add_u32 m0, s61, 49152\n"
        "s_add_u32 s71, s71, 16384\n"
        "buffer_load_dwordx4 v1, s[40:43], s71 offen offset:0 sc1 lds\n"
        "buffer_load_dwordx4 v1, s[40:43], s71 offen offset:1024 sc1 lds\n"
        "s_add_u32 m0, m0, 8192\n"
        "s_add_u32 s71, s71, 16384\n"
        "buffer_load_dwordx4 v1, s[40:43], s71 offen offset:0 sc1 lds\n"
        "buffer_load_dwordx4 v1, s[40:43], s71 offen offset:1024 sc1 lds\n"
        "s_add_u32 m0, m0, 8192\n"
        "s_add_u32 s71, s71, 16384\n"
        "buffer_load_dwordx4 v1, s[40:43], s71 offen offset:0 sc1 lds\n"
        "buffer_load_dwordx4 v1, s[40:43], s71 offen offset:1024 sc1 lds\n"
        "s_add_u32 s69, s69, 1\n"
        "s_add_u32 s70, s70, 1\n"
        "s_branch .Lf2_go1_%=\n"
        ".Lf2_late1_%=:\n"
        "s_mov_b32 s64, 0\n"
        ".Lf2_in1_top_%=:\n"
        "buffer_load_dword v4, v2, s[40:43], s76 offen sc1\n"
        "s_waitcnt vmcnt(0)\n"
        "v_cmp_gt_i32 vcc, s70, v4\n"
        "s_nop 0\n"
        "s_and_b64 vcc, vcc, 63\n"
        "s_cbranch_vccz .Lf2_in1_ok_%=\n"
        "s_add_u32 s64, s64, 1\n"
        "s_and_b32 s65, s64, 255\n"
        "s_cbranch_scc1 .Lf2_in1_go_%=\n"
        "s_mov_b32 s65, 40960\n"
        "s_cmp_lt_u32 s64, 1048576\n"
        "s_cbranch_scc1 .Lf2_in1_chk_%=\n"
        "v_mov_b32 v4, 1\n"
        "buffer_store_dword v4, off, s[40:43], s65 sc1\n"
        "s_waitcnt vmcnt(0)\n"
        ".Lf2_in1_chk_%=:\n"
        "buffer_load_dword v4, off, s[40:43], s65 sc1\n"
        "s_waitcnt vmcnt(0)\n"
        "v_readfirstlane_b32 s65, v4\n"
        "s_nop 0\n"
        "s_cmp_lg_u32 s65, 1\n"
        "s_cbranch_scc1 .Lf2_in1_go_%=\n"
        "s_endpgm\n"
        ".Lf2_in1_go_%=:\n"
        "s_sleep 1\n"
        "s_branch .Lf2_in1_top_%=\n"
        ".Lf2_in1_ok_%=:\n"
        "s_add_u32 m0, s61, 49152\n"
        "s_add_u32 s71, s71, 16384\n"
        "buffer_load_dwordx4 v1, s[40:43], s71 offen offset:0 sc1 lds\n"
        "buffer_load_dwordx4 v1, s[40:43], s71 offen offset:1024 sc1 lds\n"
        "s_add_u32 m0, m0, 8192\n"
        "s_add_u32 s71, s71, 16384\n"
        "buffer_load_dwordx4 v1, s[40:43], s71 offen offset:0 sc1 lds\n"
        "buffer_load_dwordx4 v1, s[40:43], s71 offen offset:1024 sc1 lds\n"
        "s_add_u32 m0, m0, 8192\n"
        "s_add_u32 s71, s71, 16384\n"
        "buffer_load_dwordx4 v1, s[40:43], s71 offen offset:0 sc1 lds\n"
        "buffer_load_dwordx4 v1, s[40:43], s71 offen offset:1024 sc1 lds\n"
        "s_add_u32 s69, s69, 1\n"
        "s_add_u32 s70, s70, 1\n"
        ".Lf2_go1_%=:\n"
        "buffer_load_dword v4, v2, s[40:43], s76 offen sc1\n"
        "s_waitcnt vmcnt(0)\n"
        "s_barrier\n"
        "ds_read_b128 v[12:15], v8 offset:0\n"
        "ds_read_b128 v[16:19], v8 offset:256\n"
        "ds_read_b128 v[20:23], v8 offset:2048\n"
        "ds_read_b128 v[24:27], v8 offset:2304\n"
        "v_cmp_gt_i32 vcc, s70, v4\n"
        "s_nop 0\n"
        "s_and_b64 vcc, vcc, 63\n"
        "s_waitcnt lgkmcnt(2)\n"
        "s_cbranch_vccnz .Lf2_c20_%=\n"
        "s_branch .Lf2_c21_%=\n"
        ".Lf2_c21_%=:\n"
        "v_mfma_f32_16x16x32_bf16 v[44:47], v[112:115], v[12:15], v[44:47]\n"
        "s_add_u32 m0, s61, 0\n"
        "s_lshr_b32 s65, s69, 1\n"
        "v_mfma_f32_16x16x32_bf16 v[48:51], v[116:119], v[12:15], v[48:51]\n"
        "s_bitcmp1_b32 s54, s65\n"
        "s_cselect_b32 s65, 8192, 0\n"
        "v_mfma_f32_16x16x32_bf16 v[52:55], v[120:123], v[12:15], v[52:55]\n"
        "ds_read_b128 v[12:15], v8 offset:4096\n"
        "s_lshl_b32 s71, s69, 20\n"
        "v_mfma_f32_16x16x32_bf16 v[56:59], v[112:115], v[16:19], v[56:59]\n"
        "s_add_u32 s71, s71, s65\n"
        "s_add_u32 s71, s71, s58\n"
        "v_mfma_f32_16x16x32_bf16 v[60:63], v[116:119], v[16:19], v[60:63]\n"
        "buffer_load_dwordx4 v1, s[40:43], s71 offen offset:0 sc1 lds\n"
        "buffer_load_dwordx4 v1, s[40:43], s71 offen offset:1024 sc1 lds\n"
        "v_mfma_f32_16x16x32_bf16 v[64:67], v[120:123], v[16:19], v[64:67]\n"
        "ds_read_b128 v[16:19], v8 offset:4352\n"
        "s_waitcnt lgkmcnt(2)\n"
        "v_mfma_f32_16x16x32_bf16 v[44:47], v[124:127], v[20:23], v[44:47]\n"
        "s_add_u32 m0, m0, 8192\n"
        "s_add_u32 s71, s71, 16384\n"
        "v_mfma_f32_16x16x32_bf16 v[48:51], v[128:131], v[20:23], v[48:51]\n"
        "buffer_load_dwordx4 v1, s[40:43], s71 offen offset:0 sc1 lds\n"
        "v_mfma_f32_16x16x32_bf16 v[52:55], v[132:135], v[20:23], v[52:55]\n"
        "ds_read_b128 v[20:23], v8 offset:6144\n"
        "v_mfma_f32_16x16x32_bf16 v[56:59], v[124:127], v[24:27], v[56:59]\n"
        "v_mfma_f32_16x16x32_bf16 v[60:63], v[128:131], v[24:27], v[60:63]\n"
        "buffer_load_dwordx4 v1, s[40:43], s71 offen offset:1024 sc1 lds\n"
        "s_add_u32 m0, m0, 8192\n"
        "v_mfma_f32_16x16x32_bf16 v[64:67], v[132:135], v[24:27], v[64:67]\n"
        "ds_read_b128 v[24:27], v8 offset:6400\n"
        "s_waitcnt lgkmcnt(2)\n"
        "v_mfma_f32_16x16x32_bf16 v[44:47], v[136:139], v[12:15], v[44:47]\n"
        "s_add_u32 s71, s71, 16384\n"
        "v_mfma_f32_16x16x32_bf16 v[48:51], v[140:143], v[12:15], v[48:51]\n"
        "buffer_load_dwordx4 v1, s[40:43], s71 offen offset:0 sc1 lds\n"
        "v_mfma_f32_16x16x32_bf16 v[52:55], v[144:147], v[12:15], v[52:55]\n"
        "ds_read_b128 v[12:15], v8 offset:8192\n"
        "v_mfma_f32_16x16x32_bf16 v[56:59], v[136:139], v[16:19], v[56:59]\n"
        "v_mfma_f32_16x16x32_bf16 v[60:63], v[140:143], v[16:19], v[60:63]\n"
        "buffer_load_dwordx4 v1, s[40:43], s71 offen offset:1024 sc1 lds\n"
        "s_add_u32 s70, s70, 1\n"
        "v_mfma_f32_16x16x32_bf16 v[64:67], v[144:147], v[16:19], v[64:67]\n"
        "ds_read_b128 v[16:19], v8 offset:8448\n"
        "s_waitcnt lgkmcnt(2)\n"
        "v_mfma_f32_16x16x32_bf16 v[44:47], v[148:151], v[20:23], v[44:47]\n"
        "v_mfma_f32_16x16x32_bf16 v[48:51], v[152:155], v[20:23], v[48:51]\n"
        "v_mfma_f32_16x16x32_bf16 v[52:55], v[156:159], v[20:23], v[52:55]\n"
        "ds_read_b128 v[20:23], v8 offset:10240\n"
        "v_mfma_f32_16x16x32_bf16 v[56:59], v[148:151], v[24:27], v[56:59]\n"
        "v_mfma_f32_16x16x32_bf16 v[60:63], v[152:155], v[24:27], v[60:63]\n"
        "v_mfma_f32_16x16x32_bf16 v[64:67], v[156:159], v[24:27], v[64:67]\n"
        "ds_read_b128 v[24:27], v8 offset:10496\n"
        "s_waitcnt lgkmcnt(2)\n"
        "v_mfma_f32_16x16x32_bf16 v[44:47], v[160:163], v[12:15], v[44:47]\n"
        "v_mfma_f32_16x16x32_bf16 v[48:51], v[164:167], v[12:15], v[48:51]\n"
        "v_mfma_f32_16x16x32_bf16 v[52:55], v[168:171], v[12:15], v[52:55]\n"
        "ds_read_b128 v[12:15], v8 offset:12288\n"
        "v_mfma_f32_16x16x32_bf16 v[56:59], v[160:163], v[16:19], v[56:59]\n"
        "v_mfma_f32_16x16x32_bf16 v[60:63], v[164:167], v[16:19], v[60:63]\n"
        "v_mfma_f32_16x16x32_bf16 v[64:67], v[168:171], v[16:19], v[64:67]\n"
        "ds_read_b128 v[16:19], v8 offset:12544\n"
        "s_waitcnt lgkmcnt(2)\n"
        "v_mfma_f32_16x16x32_bf16 v[44:47], v[172:175], v[20:23], v[44:47]\n"
        "v_mfma_f32_16x16x32_bf16 v[48:51], v[176:179], v[20:23], v[48:51]\n"
        "v_mfma_f32_16x16x32_bf16 v[52:55], v[180:183], v[20:23], v[52:55]\n"
        "ds_read_b128 v[20:23], v8 offset:14336\n"
        "v_mfma_f32_16x16x32_bf16 v[56:59], v[172:175], v[24:27], v[56:59]\n"
        "v_mfma_f32_16x16x32_bf16 v[60:63], v[176:179], v[24:27], v[60:63]\n"
        "v_mfma_f32_16x16x32_bf16 v[64:67], v[180:183], v[24:27], v[64:67]\n"
        "ds_read_b128 v[24:27], v8 offset:14592\n"
        "s_waitcnt lgkmcnt(2)\n"
        "v_mfma_f32_16x16x32_bf16 v[44:47], v[184:187], v[12:15], v[44:47]\n"
        "buffer_load_dword v4, v2, s[40:43], s76 offen sc1\n"
        "v_mfma_f32_16x16x32_bf16 v[48:51], v[188:191], v[12:15], v[48:51]\n"
        "v_mfma_f32_16x16x32_bf16 v[52:55], v[192:195], v[12:15], v[52:55]\n"
        "ds_read_b128 v[12:15], v8 offset:16384\n"
        "v_mfma_f32_16x16x32_bf16 v[56:59], v[184:187], v[16:19], v[56:59]\n"
        "v_mfma_f32_16x16x32_bf16 v[60:63], v[188:191], v[16:19], v[60:63]\n"
        "v_mfma_f32_16x16x32_bf16 v[64:67], v[192:195], v[16:19], v[64:67]\n"
        "ds_read_b128 v[16:19], v8 offset:16640\n"
        "s_waitcnt lgkmcnt(2)\n"
        "v_mfma_f32_16x16x32_bf16 v[44:47], v[196:199], v[20:23], v[44:47]\n"
        "v_mfma_f32_16x16x32_bf16 v[48:51], v[200:203], v[20:23], v[48:51]\n"
        "v_mfma_f32_16x16x32_bf16 v[52:55], v[204:207], v[20:23], v[52:55]\n"
        "ds_read_b128 v[20:23], v8 offset:18432\n"
        "v_mfma_f32_16x16x32_bf16 v[56:59], v[196:199], v[24:27], v[56:59]\n"
        "v_mfma_f32_16x16x32_bf16 v[60:63], v[200:203], v[24:27], v[60:63]\n"
        "v_mfma_f32_16x16x32_bf16 v[64:67], v[204:207], v[24:27], v[64:67]\n"
        "ds_read_b128 v[24:27], v8 offset:18688\n"
        "s_waitcnt lgkmcnt(2)\n"
        "v_mfma_f32_16x16x32_bf16 v[44:47], v[208:211], v[12:15], v[44:47]\n"
        "v_mfma_f32_16x16x32_bf16 v[48:51], v[212:215], v[12:15], v[48:51]\n"
        "v_mfma_f32_16x16x32_bf16 v[52:55], v[216:219], v[12:15], v[52:55]\n"
        "ds_read_b128 v[12:15], v8 offset:20480\n"
        "v_mfma_f32_16x16x32_bf16 v[56:59], v[208:211], v[16:19], v[56:59]\n"
        "v_mfma_f32_16x16x32_bf16 v[60:63], v[212:215], v[16:19], v[60:63]\n"
        "v_mfma_f32_16x16x32_bf16 v[64:67], v[216:219], v[16:19], v[64:67]\n"
        "ds_read_b128 v[16:19], v8 offset:20736\n"
        "s_waitcnt lgkmcnt(2)\n"
        "v_mfma_f32_16x16x32_bf16 v[44:47], v[220:223], v[20:23], v[44:47]\n"
        "v_mfma_f32_16x16x32_bf16 v[48:51], v[224:227], v[20:23], v[48:51]\n"
        "v_mfma_f32_16x16x32_bf16 v[52:55], v[228:231], v[20:23], v[52:55]\n"
        "ds_read_b128 v[20:23], v8 offset:22528\n"
        "v_mfma_f32_16x16x32_bf16 v[56:59], v[220:223], v[24:27], v[56:59]\n"
        "v_mfma_f32_16x16x32_bf16 v[60:63], v[224:227], v[24:27], v[60:63]\n"
        "v_mfma_f32_16x16x32_bf16 v[64:67], v[228:231], v[24:27], v[64:67]\n"
        "ds_read_b128 v[24:27], v8 offset:22784\n"
        "s_waitcnt lgkmcnt(2)\n"
        "v_mfma_f32_16x16x32_bf16 v[44:47], v[232:235], v[12:15], v[44:47]\n"
        "v_mfma_f32_16x16x32_bf16 v[48:51], v[236:239], v[12:15], v[48:51]\n"
        "s_waitcnt vmcnt(1)\n"
        "s_barrier\n"
        "v_mfma_f32_16x16x32_bf16 v[52:55], v[240:243], v[12:15], v[52:55]\n"
        "ds_read_b128 v[12:15], v0 offset:0\n"
        "v_mfma_f32_16x16x32_bf16 v[56:59], v[232:235], v[16:19], v[56:59]\n"
        "v_mfma_f32_16x16x32_bf16 v[60:63], v[236:239], v[16:19], v[60:63]\n"
        "v_mfma_f32_16x16x32_bf16 v[64:67], v[240:243], v[16:19], v[64:67]\n"
        "ds_read_b128 v[16:19], v0 offset:256\n"
        "s_waitcnt lgkmcnt(2)\n"
        "v_mfma_f32_16x16x32_bf16 v[44:47], v[244:247], v[20:23], v[44:47]\n"
        "s_waitcnt vmcnt(0)\n"
        "v_cmp_gt_i32 vcc, s70, v4\n"
        "v_mfma_f32_16x16x32_bf16 v[48:51], v[248:251], v[20:23], v[48:51]\n"
        "s_and_b64 vcc, vcc, 63\n"
        "v_mfma_f32_16x16x32_bf16 v[52:55], v[252:255], v[20:23], v[52:55]\n"
        "ds_read_b128 v[20:23], v0 offset:2048\n"
        "v_cvt_pk_bf16_f32 v68, v44, v45\n"
        "v_cvt_pk_bf16_f32 v69, v46, v47\n"
        "v_mfma_f32_16x16x32_bf16 v[56:59], v[244:247], v[24:27], v[56:59]\n"
        "v_cvt_pk_bf16_f32 v70, v48, v49\n"
        "v_cvt_pk_bf16_f32 v71, v50, v51\n"
        "v_mfma_f32_16x16x32_bf16 v[60:63], v[248:251], v[24:27], v[60:63]\n"
        "v_cvt_pk_bf16_f32 v76, v52, v53\n"
        "v_cvt_pk_bf16_f32 v77, v54, v55\n"
        "v_mfma_f32_16x16x32_bf16 v[64:67], v[252:255], v[24:27], v[64:67]\n"
        "ds_read_b128 v[24:27], v0 offset:2304\n"
        "s_waitcnt lgkmcnt(2)\n"
        "v_cvt_pk_bf16_f32 v72, v56, v57\n"
        "v_cvt_pk_bf16_f32 v73, v58, v59\n"
        "s_cbranch_vccnz .Lf2_c00_%=\n"
        "s_branch .Lf2_c01_%=\n"
        ".Lf2_c20_%=:\n"
        "v_mfma_f32_16x16x32_bf16 v[44:47], v[112:115], v[12:15], v[44:47]\n"
        "v_mfma_f32_16x16x32_bf16 v[48:51], v[116:119], v[12:15], v[48:51]\n"
        "v_mfma_f32_16x16x32_bf16 v[52:55], v[120:123], v[12:15], v[52:55]\n"
        "ds_read_b128 v[12:15], v8 offset:4096\n"
        "v_mfma_f32_16x16x32_bf16 v[56:59], v[112:115], v[16:19], v[56:59]\n"
        "v_mfma_f32_16x16x32_bf16 v[60:63], v[116:119], v[16:19], v[60:63]\n"
        "v_mfma_f32_16x16x32_bf16 v[64:67], v[120:123], v[16:19], v[64:67]\n"
        "ds_read_b128 v[16:19], v8 offset:4352\n"
        "s_waitcnt lgkmcnt(2)\n"
        "v_mfma_f32_16x16x32_bf16 v[44:47], v[124:127], v[20:23], v[44:47]\n"
        "v_mfma_f32_16x16x32_bf16 v[48:51], v[128:131], v[20:23], v[48:51]\n"
        "v_mfma_f32_16x16x32_bf16 v[52:55], v[132:135], v[20:23], v[52:55]\n"
        "ds_read_b128 v[20:23], v8 offset:6144\n"
        "v_mfma_f32_16x16x32_bf16 v[56:59], v[124:127], v[24:27], v[56:59]\n"
        "v_mfma_f32_16x16x32_bf16 v[60:63], v[128:131], v[24:27], v[60:63]\n"
        "v_mfma_f32_16x16x32_bf16 v[64:67], v[132:135], v[24:27], v[64:67]\n"
        "ds_read_b128 v[24:27], v8 offset:6400\n"
        "s_waitcnt lgkmcnt(2)\n"
        "v_mfma_f32_16x16x32_bf16 v[44:47], v[136:139], v[12:15], v[44:47]\n"
        "v_mfma_f32_16x16x32_bf16 v[48:51], v[140:143], v[12:15], v[48:51]\n"
        "v_mfma_f32_16x16x32_bf16 v[52:55], v[144:147], v[12:15], v[52:55]\n"
        "ds_read_b128 v[12:15], v8 offset:8192\n"
        "v_mfma_f32_16x16x32_bf16 v[56:59], v[136:139], v[16:19], v[56:59]\n"
        "v_mfma_f32_16x16x32_bf16 v[60:63], v[140:143], v[16:19], v[60:63]\n"
        "v_mfma_f32_16x16x32_bf16 v[64:67], v[144:147], v[16:19], v[64:67]\n"
        "ds_read_b128 v[16:19], v8 offset:8448\n"
        "s_waitcnt lgkmcnt(2)\n"
        "v_mfma_f32_16x16x32_bf16 v[44:47], v[148:151], v[20:23], v[44:47]\n"
        "v_mfma_f32_16x16x32_bf16 v[48:51], v[152:155], v[20:23], v[48:51]\n"
        "v_mfma_f32_16x16x32_bf16 v[52:55], v[156:159], v[20:23], v[52:55]\n"
        "ds_read_b128 v[20:23], v8 offset:10240\n"
        "v_mfma_f32_16x16x32_bf16 v[56:59], v[148:151], v[24:27], v[56:59]\n"
        "v_mfma_f32_16x16x32_bf16 v[60:63], v[152:155], v[24:27], v[60:63]\n"
        "v_mfma_f32_16x16x32_bf16 v[64:67], v[156:159], v[24:27], v[64:67]\n"
        "ds_read_b128 v[24:27], v8 offset:10496\n"
        "s_waitcnt lgkmcnt(2)\n"
        "v_mfma_f32_16x16x32_bf16 v[44:47], v[160:163], v[12:15], v[44:47]\n"
        "v_mfma_f32_16x16x32_bf16 v[48:51], v[164:167], v[12:15], v[48:51]\n"
        "v_mfma_f32_16x16x32_bf16 v[52:55], v[168:171], v[12:15], v[52:55]\n"
        "ds_read_b128 v[12:15], v8 offset:12288\n"
        "v_mfma_f32_16x16x32_bf16 v[56:59], v[160:163], v[16:19], v[56:59]\n"
        "v_mfma_f32_16x16x32_bf16 v[60:63], v[164:167], v[16:19], v[60:63]\n"
        "v_mfma_f32_16x16x32_bf16 v[64:67], v[168:171], v[16:19], v[64:67]\n"
        "ds_read_b128 v[16:19], v8 offset:12544\n"
        "s_waitcnt lgkmcnt(2)\n"
        "v_mfma_f32_16x16x32_bf16 v[44:47], v[172:175], v[20:23], v[44:47]\n"
        "v_mfma_f32_16x16x32_bf16 v[48:51], v[176:179], v[20:23], v[48:51]\n"
        "v_mfma_f32_16x16x32_bf16 v[52:55], v[180:183], v[20:23], v[52:55]\n"
        "ds_read_b128 v[20:23], v8 offset:14336\n"
        "v_mfma_f32_16x16x32_bf16 v[56:59], v[172:175], v[24:27], v[56:59]\n"
        "v_mfma_f32_16x16x32_bf16 v[60:63], v[176:179], v[24:27], v[60:63]\n"
        "v_mfma_f32_16x16x32_bf16 v[64:67], v[180:183], v[24:27], v[64:67]\n"
        "ds_read_b128 v[24:27], v8 offset:14592\n"
        "s_waitcnt lgkmcnt(2)\n"
        "v_mfma_f32_16x16x32_bf16 v[44:47], v[184:187], v[12:15], v[44:47]\n"
        "buffer_load_dword v4, v2, s[40:43], s76 offen sc1\n"
        "v_mfma_f32_16x16x32_bf16 v[48:51], v[188:191], v[12:15], v[48:51]\n"
        "v_mfma_f32_16x16x32_bf16 v[52:55], v[192:195], v[12:15], v[52:55]\n"
        "ds_read_b128 v[12:15], v8 offset:16384\n"
        "v_mfma_f32_16x16x32_bf16 v[56:59], v[184:187], v[16:19], v[56:59]\n"
        "v_mfma_f32_16x16x32_bf16 v[60:63], v[188:191], v[16:19], v[60:63]\n"
        "v_mfma_f32_16x16x32_bf16 v[64:67], v[192:195], v[16:19], v[64:67]\n"
        "ds_read_b128 v[16:19], v8 offset:16640\n"
        "s_waitcnt lgkmcnt(2)\n"
        "v_mfma_f32_16x16x32_bf16 v[44:47], v[196:199], v[20:23], v[44:47]\n"
        "v_mfma_f32_16x16x32_bf16 v[48:51], v[200:203], v[20:23], v[48:51]\n"
        "v_mfma_f32_16x16x32_bf16 v[52:55], v[204:207], v[20:23], v[52:55]\n"
        "ds_read_b128 v[20:23], v8 offset:18432\n"
        "v_mfma_f32_16x16x32_bf16 v[56:59], v[196:199], v[24:27], v[56:59]\n"
        "v_mfma_f32_16x16x32_bf16 v[60:63], v[200:203], v[24:27], v[60:63]\n"
        "v_mfma_f32_16x16x32_bf16 v[64:67], v[204:207], v[24:27], v[64:67]\n"
        "ds_read_b128 v[24:27], v8 offset:18688\n"
        "s_waitcnt lgkmcnt(2)\n"
        "v_mfma_f32_16x16x32_bf16 v[44:47], v[208:211], v[12:15], v[44:47]\n"
        "v_mfma_f32_16x16x32_bf16 v[48:51], v[212:215], v[12:15], v[48:51]\n"
        "v_mfma_f32_16x16x32_bf16 v[52:55], v[216:219], v[12:15], v[52:55]\n"
        "ds_read_b128 v[12:15], v8 offset:20480\n"
        "v_mfma_f32_16x16x32_bf16 v[56:59], v[208:211], v[16:19], v[56:59]\n"
        "v_mfma_f32_16x16x32_bf16 v[60:63], v[212:215], v[16:19], v[60:63]\n"
        "v_mfma_f32_16x16x32_bf16 v[64:67], v[216:219], v[16:19], v[64:67]\n"
        "ds_read_b128 v[16:19], v8 offset:20736\n"
        "s_waitcnt lgkmcnt(2)\n"
        "v_mfma_f32_16x16x32_bf16 v[44:47], v[220:223], v[20:23], v[44:47]\n"
        "v_mfma_f32_16x16x32_bf16 v[48:51], v[224:227], v[20:23], v[48:51]\n"
        "v_mfma_f32_16x16x32_bf16 v[52:55], v[228:231], v[20:23], v[52:55]\n"
        "ds_read_b128 v[20:23], v8 offset:22528\n"
        "v_mfma_f32_16x16x32_bf16 v[56:59], v[220:223], v[24:27], v[56:59]\n"
        "v_mfma_f32_16x16x32_bf16 v[60:63], v[224:227], v[24:27], v[60:63]\n"
        "v_mfma_f32_16x16x32_bf16 v[64:67], v[228:231], v[24:27], v[64:67]\n"
        "ds_read_b128 v[24:27], v8 offset:22784\n"
        "s_waitcnt lgkmcnt(2)\n"
        "v_mfma_f32_16x16x32_bf16 v[44:47], v[232:235], v[12:15], v[44:47]\n"
        "v_mfma_f32_16x16x32_bf16 v[48:51], v[236:239], v[12:15], v[48:51]\n"
        "v_mfma_f32_16x16x32_bf16 v[52:55], v[240:243], v[12:15], v[52:55]\n"
        "v_mfma_f32_16x16x32_bf16 v[56:59], v[232:235], v[16:19], v[56:59]\n"
        "v_mfma_f32_16x16x32_bf16 v[60:63], v[236:239], v[16:19], v[60:63]\n"
        "v_mfma_f32_16x16x32_bf16 v[64:67], v[240:243], v[16:19], v[64:67]\n"
        "s_waitcnt lgkmcnt(0)\n"
        "v_mfma_f32_16x16x32_bf16 v[44:47], v[244:247], v[20:23], v[44:47]\n"
        "v_mfma_f32_16x16x32_bf16 v[48:51], v[248:251], v[20:23], v[48:51]\n"
        "v_mfma_f32_16x16x32_bf16 v[52:55], v[252:255], v[20:23], v[52:55]\n"
        "v_cvt_pk_bf16_f32 v68, v44, v45\n"
        "v_cvt_pk_bf16_f32 v69, v46, v47\n"
        "v_mfma_f32_16x16x32_bf16 v[56:59], v[244:247], v[24:27], v[56:59]\n"
        "v_cvt_pk_bf16_f32 v70, v48, v49\n"
        "v_cvt_pk_bf16_f32 v71, v50, v51\n"
        "v_mfma_f32_16x16x32_bf16 v[60:63], v[248:251], v[24:27], v[60:63]\n"
        "v_cvt_pk_bf16_f32 v76, v52, v53\n"
        "v_cvt_pk_bf16_f32 v77, v54, v55\n"
        "v_mfma_f32_16x16x32_bf16 v[64:67], v[252:255], v[24:27], v[64:67]\n"
        "v_cvt_pk_bf16_f32 v72, v56, v57\n"
        "v_cvt_pk_bf16_f32 v73, v58, v59\n"
        "s_nop 5\n"
        "v_cvt_pk_bf16_f32 v74, v60, v61\n"
        "v_cvt_pk_bf16_f32 v75, v62, v63\n"
        "v_cvt_pk_bf16_f32 v78, v64, v65\n"
        "v_cvt_pk_bf16_f32 v79, v66, v67\n"
        "buffer_store_dwordx4 v[68:71], v6, s[40:43], s72 offen sc1\n"
        "s_add_u32 s66, s72, 147456\n"
        "buffer_store_dwordx4 v[72:75], v6, s[40:43], s66 offen sc1\n"
        "v_permlane16_swap_b32 v76, v78\n"
        "v_permlane16_swap_b32 v77, v79\n"
        "buffer_store_dwordx4 v[76:79], v7, s[40:43], s72 offen sc1\n"
        "s_waitcnt vmcnt(3)\n"
        "v_cmp_gt_i32 vcc, s70, v4\n"
        "s_nop 0\n"
        "s_and_b64 vcc, vcc, 63\n"
        "s_cbranch_vccnz .Lf2_late2_%=\n"
        "s_add_u32 m0, s61, 0\n"
        "s_lshr_b32 s65, s69, 1\n"
        "s_bitcmp1_b32 s54, s65\n"
        "s_cselect_b32 s65, 8192, 0\n"
        "s_lshl_b32 s71, s69, 20\n"
        "s_add_u32 s71, s71, s65\n"
        "s_add_u32 s71, s71, s58\n"
        "buffer_load_dwordx4 v1, s[40:43], s71 offen offset:0 sc1 lds\n"
        "buffer_load_dwordx4 v1, s[40:43], s71 offen offset:1024 sc1 lds\n"
        "s_add_u32 m0, m0, 8192\n"
        "s_add_u32 s71, s71, 16384\n"
        "buffer_load_dwordx4 v1, s[40:43], s71 offen offset:0 sc1 lds\n"
        "buffer_load_dwordx4 v1, s[40:43], s71 offen offset:1024 sc1 lds\n"
        "s_add_u32 m0, m0, 8192\n"
        "s_add_u32 s71, s71, 16384\n"
        "buffer_load_dwordx4 v1, s[40:43], s71 offen offset:0 sc1 lds\n"
        "buffer_load_dwordx4 v1, s[40:43], s71 offen offset:1024 sc1 lds\n"
        "s_add_u32 s70, s70, 1\n"
        "s_waitcnt vmcnt(6)\n"
        "v_mov_b32 v5, s74\n"
        "buffer_store_dword v5, v3, s[40:43], s73 offen sc1\n"
        "s_branch .Lf2_go2_%=\n"
        ".Lf2_late2_%=:\n"
        "s_waitcnt vmcnt(0)\n"
        "v_mov_b32 v5, s74\n"
        "buffer_store_dword v5, v3, s[40:43], s73 offen sc1\n"
        "s_cmp_eq_u32 s68, 63\n"
        "s_cbranch_scc0 .Lf2_wait2b_%=\n"
        "s_endpgm\n"
        ".Lf2_wait2_%=:\n"
        "s_mov_b32 s70, 0\n"
        ".Lf2_wait2b_%=:\n"
        "s_mov_b32 s64, 0\n"
        ".Lf2_in2_top_%=:\n"
        "buffer_load_dword v4, v2, s[40:43], s76 offen sc1\n"
        "s_waitcnt vmcnt(0)\n"
        "v_cmp_gt_i32 vcc, s70, v4\n"
        "s_nop 0\n"
        "s_and_b64 vcc, vcc, 63\n"
        "s_cbranch_vccz .Lf2_in2_ok_%=\n"
        "s_add_u32 s64, s64, 1\n"
        "s_and_b32 s65, s64, 255\n"
        "s_cbranch_scc1 .Lf2_in2_go_%=\n"
        "s_mov_b32 s65, 40960\n"
        "s_cmp_lt_u32 s64, 1048576\n"
        "s_cbranch_scc1 .Lf2_in2_chk_%=\n"
        "v_mov_b32 v4, 1\n"
        "buffer_store_dword v4, off, s[40:43], s65 sc1\n"
        "s_waitcnt vmcnt(0)\n"
        ".Lf2_in2_chk_%=:\n"
        "buffer_load_dword v4, off, s[40:43], s65 sc1\n"
        "s_waitcnt vmcnt(0)\n"
        "v_readfirstlane_b32 s65, v4\n"
        "s_nop 0\n"
        "s_cmp_lg_u32 s65, 1\n"
        "s_cbranch_scc1 .Lf2_in2_go_%=\n"
        "s_endpgm\n"
        ".Lf2_in2_go_%=:\n"
        "s_sleep 1\n"
        "s_branch .Lf2_in2_top_%=\n"
        ".Lf2_in2_ok_%=:\n"
        "s_add_u32 m0, s61, 0\n"
        "s_lshr_b32 s65, s69, 1\n"
        "s_bitcmp1_b32 s54, s65\n"
        "s_cselect_b32 s65, 8192, 0\n"
        "s_lshl_b32 s71, s69, 20\n"
        "s_add_u32 s71, s71, s65\n"
        "s_add_u32 s71, s71, s58\n"
        "buffer_load_dwordx4 v1, s[40:43], s71 offen offset:0 sc1 lds\n"
        "buffer_load_dwordx4 v1, s[40:43], s71 offen offset:1024 sc1 lds\n"
        "s_add_u32 m0, m0, 8192\n"
        "s_add_u32 s71, s71, 16384\n"
        "buffer_load_dwordx4 v1, s[40:43], s71 offen offset:0 sc1 lds\n"
        "buffer_load_dwordx4 v1, s[40:43], s71 offen offset:1024 sc1 lds\n"
        "s_add_u32 m0, m0, 8192\n"
        "s_add_u32 s71, s71, 16384\n"
        "buffer_load_dwordx4 v1, s[40:43], s71 offen offset:0 sc1 lds\n"
        "buffer_load_dwordx4 v1, s[40:43], s71 offen offset:1024 sc1 lds\n"
        "s_add_u32 s70, s70, 1\n"
        ".Lf2_go2_%=:\n"
        "s_mov_b32 s72, 241172480\n"
        "s_mov_b32 s73, 241172480\n"
        "buffer_load_dword v4, v2, s[40:43], s76 offen sc1\n"
        "s_waitcnt vmcnt(0)\n"
        "s_barrier\n"
        "ds_read_b128 v[12:15], v0 offset:0\n"
        "ds_read_b128 v[16:19], v0 offset:256\n"
        "ds_read_b128 v[20:23], v0 offset:2048\n"
        "ds_read_b128 v[24:27], v0 offset:2304\n"
        "v_cmp_gt_i32 vcc, s70, v4\n"
        "s_nop 0\n"
        "s_and_b64 vcc, vcc, 63\n"
        "s_waitcnt lgkmcnt(2)\n"
        "s_cbranch_vccnz .Lf2_c00_%=\n"
        "s_branch .Lf2_c01_%=\n"
            :: "s"(p.ws), "s"(p.w2), "s"(p.w2), "s"(p.w2), "s"(pair(w, delay)), "s"(pair(x4, pr)),
               "s"(pair(65 * PAGE + spread(B_H + kap * 73728) + w * 2048 + XF2L, 97 * PAGE + spread(C_P5) + XF2S)), "s"(pair(flag(F_F2 + fl), w * 2048)),
               "v"(pair((g >> 1) * 1024 + (g & 1) * 512 + i * 16, lane * 16)), "v"(pair(lane < 6 ? flag(F_F1 + 128 * nu + 24 * kap + 8 * (lane >> 1) + 2 * w + (lane & 1)) : OOB, fo4)), "v"(pair(tr + 64 + 8 * pp, 0)),
               "v"(pair(s01, s2)), "v"(pair(vo[0], vo[1])), "v"(pair(vo[2], tr + 16 * pp))
            : "v0", "v1", "v2", "v3", "v4", "v5", "v6", "v7", "v8", "v9", "v10", "v11", "v12", "v13", "v14", "v15", "v16", "v17", "v18", "v19", "v20", "v21", "v22", "v23", "v24", "v25", "v26", "v27", "v28", "v29", "v30", "v31", "v32", "v33", "v34", "v35", "v36", "v37", "v38", "v39", "v40", "v41", "v42", "v43", "v44", "v45", "v46", "v47", "v48", "v49", "v50", "v51", "v52", "v53", "v54", "v55", "v56", "v57", "v58", "v59", "v60", "v61", "v62", "v63", "v64", "v65", "v66", "v67", "v68", "v69", "v70", "v71", "v72", "v73", "v74", "v75", "v76", "v77", "v78", "v79", "v255", "a255", "s34", "s35", "s36", "s37", "s38", "s39", "s40", "s41", "s42", "s43", "s44", "s45", "s46", "s47", "s48", "s49", "s50", "s51", "s52", "s53", "s54", "s55", "s56", "s57", "s58", "s59", "s60", "s61", "s62", "s63", "s64", "s65", "s66", "s67", "s68", "s69", "s70", "s71", "s72", "s73", "s74", "s75", "s76", "s77", "s78", "s79", "s80", "s81", "s82", "s83", "s84", "s85", "s86", "s87", "s88", "s89", "s90", "s91", "s92", "s93", "s94", "s95", "s96", "s97", "s98", "s99", "vcc", "scc", "memory");
    }
    __builtin_unreachable();
}

__device__ __forceinline__ void role_attn(const Params& p, int h0, int x4, int pr) {
    const unsigned lane = threadIdx.x & 63, i = lane & 15, g = lane >> 4;
    const int w = rfl(threadIdx.x >> 6), h = h0 + w;
    asm volatile(
        "s_nop 4\n"
        "s_mov_b64 s[40:41], %0\n"
        "s_and_b32 s41, s41, 0xffff\n"
        "s_mov_b32 s42, 236978176\n"
        "s_mov_b32 s43, 0x20000\n"
        "s_mov_b64 s[56:57], %1\n"
        "s_mov_b64 s[58:59], %2\n"
        "s_mov_b64 s[60:61], %3\n"
        "v_mov_b64 v[0:1], %4\n"
        "v_mov_b64 v[2:3], %5\n"
        "v_mov_b64 v[4:5], %6\n"
        "v_cmp_ge_i32 s[76:77], 0, v5\n"
        "v_cmp_ge_i32 s[78:79], -1, v5\n"
        "v_cmp_ge_i32 s[80:81], -2, v5\n"
        "v_cmp_ge_i32 s[82:83], -3, v5\n"
        "s_mov_b32 s84, 0\n"
        "s_mov_b32 s85, -1\n"
        "s_mov_b32 s75, 0x3e0293ee\n"
        "v_lshrrev_b32 v12, 4, v0\n"
        "s_lshl_b32 s66, s57, 7\n"
        "s_mov_b32 s64, 0\n"
        ".Lat_pair_top_%=:\n"
        "s_mov_b64 exec, 1\n"
        "buffer_load_dword v13, off, s[40:43], s66 sc1\n"
        "s_mov_b64 exec, -1\n"
        "s_waitcnt vmcnt(0)\n"
        "v_cmp_gt_i32 vcc, 0, v13\n"
        "s_nop 0\n"
        "s_and_b64 vcc, vcc, 1\n"
        "s_cbranch_vccz .Lat_pair_ok_%=\n"
        "s_add_u32 s64, s64, 1\n"
        "s_and_b32 s65, s64, 255\n"
        "s_cbranch_scc1 .Lat_pair_go_%=\n"
        "s_mov_b32 s65, 40960\n"
        "s_cmp_lt_u32 s64, 1048576\n"
        "s_cbranch_scc1 .Lat_pair_chk_%=\n"
        "v_mov_b32 v13, 1\n"
        "buffer_store_dword v13, off, s[40:43], s65 sc1\n"
        "s_waitcnt vmcnt(0)\n"
        ".Lat_pair_chk_%=:\n"
        "buffer_load_dword v13, off, s[40:43], s65 sc1\n"
        "s_waitcnt vmcnt(0)\n"
        "v_readfirstlane_b32 s65, v13\n"
        "s_nop 0\n"
        "s_cmp_lg_u32 s65, 1\n"
        "s_cbranch_scc1 .Lat_pair_go_%=\n"
        "s_endpgm\n"
        ".Lat_pair_go_%=:\n"
        "s_sleep 2\n"
        "s_branch .Lat_pair_top_%=\n"
        ".Lat_pair_ok_%=:\n"
        "v_readfirstlane_b32 s50, v13\n"
        "s_nop 0\n"
        "s_xor_b32 s50, s50, s56\n"
        "v_lshlrev_b32 v14, 7, v12\n"
        "s_sub_u32 s66, 0, s50\n"
        "s_mov_b32 s52, 24576\n"
        "s_mov_b32 s64, 0\n"
        ".Lat_pg0_top_%=:\n"
        "buffer_load_dword v13, v14, s[40:43], s52 offen sc1\n"
        "s_waitcnt vmcnt(0)\n"
        "v_cmp_gt_i32 vcc, 0, v13\n"
        "s_cbranch_vccz .Lat_pg0_ok_%=\n"
        "s_add_u32 s64, s64, 1\n"
        "s_and_b32 s65, s64, 255\n"
        "s_cbranch_scc1 .Lat_pg0_go_%=\n"
        "s_mov_b32 s65, 40960\n"
        "s_cmp_lt_u32 s64, 1048576\n"
        "s_cbranch_scc1 .Lat_pg0_chk_%=\n"
        "v_mov_b32 v13, 1\n"
        "buffer_store_dword v13, off, s[40:43], s65 sc1\n"
        "s_waitcnt vmcnt(0)\n"
        ".Lat_pg0_chk_%=:\n"
        "buffer_load_dword v13, off, s[40:43], s65 sc1\n"
        "s_waitcnt vmcnt(0)\n"
        "v_readfirstlane_b32 s65, v13\n"
        "s_nop 0\n"
        "s_cmp_lg_u32 s65, 1\n"
        "s_cbranch_scc1 .Lat_pg0_go_%=\n"
        "s_endpgm\n"
        ".Lat_pg0_go_%=:\n"
        "s_sleep 2\n"
        "s_branch .Lat_pg0_top_%=\n"
        ".Lat_pg0_ok_%=:\n"
        "v_cmp_eq_u32 s[52:53], 1, v13\n"
        "s_nop 1\n"
        "s_mov_b32 s54, 32768\n"
        "s_mov_b32 s64, 0\n"
        ".Lat_pg1_top_%=:\n"
        "buffer_load_dword v13, v14, s[40:43], s54 offen sc1\n"
        "s_waitcnt vmcnt(0)\n"
        "v_cmp_gt_i32 vcc, 0, v13\n"
        "s_cbranch_vccz .Lat_pg1_ok_%=\n"
        "s_add_u32 s64, s64, 1\n"
        "s_and_b32 s65, s64, 255\n"
        "s_cbranch_scc1 .Lat_pg1_go_%=\n"
        "s_mov_b32 s65, 40960\n"
        "s_cmp_lt_u32 s64, 1048576\n"
        "s_cbranch_scc1 .Lat_pg1_chk_%=\n"
        "v_mov_b32 v13, 1\n"
        "buffer_store_dword v13, off, s[40:43], s65 sc1\n"
        "s_waitcnt vmcnt(0)\n"
        ".Lat_pg1_chk_%=:\n"
        "buffer_load_dword v13, off, s[40:43], s65 sc1\n"
        "s_waitcnt vmcnt(0)\n"
        "v_readfirstlane_b32 s65, v13\n"
        "s_nop 0\n"
        "s_cmp_lg_u32 s65, 1\n"
        "s_cbranch_scc1 .Lat_pg1_go_%=\n"
        "s_endpgm\n"
        ".Lat_pg1_go_%=:\n"
        "s_sleep 2\n"
        "s_branch .Lat_pg1_top_%=\n"
        ".Lat_pg1_ok_%=:\n"
        "v_cmp_eq_u32 s[54:55], 1, v13\n"
        "s_nop 1\n"
        "s_mov_b32 vcc_lo, 0x55555555\n"
        "s_mov_b32 vcc_hi, 0x55555555\n"
        "s_xor_b64 vcc, vcc, s[52:53]\n"
        "s_bcnt1_i32_b64 s65, vcc\n"
        "s_sub_u32 s64, s65, 23\n"
        "s_cmp_lt_u32 s64, 19\n"
        "s_cbranch_scc1 .Lat_rawmap_%=\n"
        "s_cmp_lt_u32 s65, 32\n"
        "s_cselect_b32 s65, 0, -1\n"
        "s_xor_b32 s52, s65, 0x55555555\n"
        "s_xor_b32 s53, s65, 0x55555555\n"
        "s_xor_b32 s54, s65, 0x55555555\n"
        "s_xor_b32 s55, s65, 0xaaaaaaaa\n"
        ".Lat_rawmap_%=:\n"
        "s_xor_b32 s52, s52, s66\n"
        "s_xor_b32 s53, s53, s66\n"
        "s_xor_b32 s54, s54, s66\n"
        "s_xor_b32 s55, s55, s66\n"
        "s_and_b32 s71, s55, 1\n"
        "s_lshl_b32 s71, s71, 13\n"
        "s_add_u32 s60, s60, s71\n"
        "v_mov_b64 v[166:167], 0\n"
        "v_mov_b64 v[172:173], 0\n"
        "v_mov_b64 v[176:177], 0\n"
        "v_mov_b64 v[178:179], 0\n"
        "v_mov_b64 v[182:183], 0\n"
        "v_mov_b32 v10, 0xff61b1e6\n"
        "s_mov_b32 s68, 0\n"
        "s_mov_b32 s72, 0\n"
        "s_bitcmp1_b64 s[52:53], s68\n"
        "s_cselect_b32 s65, 8192, 0\n"
        "s_lshl_b32 s70, s68, 21\n"
        "s_add_u32 s70, s70, s65\n"
        ".Lat_top_%=:\n"
        "s_mov_b32 s69, s70\n"
        "s_mul_i32 s67, s68, 3\n"
        "s_mov_b32 s64, 0\n"
        ".Lat_more_%=:\n"
        "buffer_load_dword v5, v1, s[40:43], s71 offen sc1\n"
        "s_mov_b32 s86, s72\n"
        "s_waitcnt vmcnt(0)\n"
        "v_min_i32_dpp v5, v5, v5 quad_perm:[1,0,3,2] row_mask:0xf bank_mask:0xf\n"
        "s_nop 1\n"
        "v_min_i32_dpp v5, v5, v5 quad_perm:[2,3,0,1] row_mask:0xf bank_mask:0xf\n"
        "s_nop 0\n"
        "v_readlane_b32 s73, v5, 0\n"
        "v_readlane_b32 s74, v5, 4\n"
        "s_nop 0\n"
        "s_bitcmp1_b32 s72, 0\n"
        "s_cbranch_scc1 .Lat_hasK_%=\n"
        "s_add_u32 s65, s67, 0\n"
        "s_cmp_ge_i32 s73, s65\n"
        "s_cbranch_scc0 .Lat_hasK_%=\n"
        "s_add_u32 s65, s69, s58\n"
        "s_add_u32 s65, s65, 524288\n"
        "s_add_u32 s66, s65, 4096\n"
        "buffer_load_dwordx4 v[48:51], v0, s[40:43], s65 offen offset:0 sc1\n"
        "buffer_load_dwordx4 v[52:55], v0, s[40:43], s65 offen offset:1024 sc1\n"
        "buffer_load_dwordx4 v[56:59], v0, s[40:43], s65 offen offset:2048 sc1\n"
        "buffer_load_dwordx4 v[60:63], v0, s[40:43], s65 offen offset:3072 sc1\n"
        "buffer_load_dwordx4 v[64:67], v0, s[40:43], s66 offen offset:0 sc1\n"
        "buffer_load_dwordx4 v[68:71], v0, s[40:43], s66 offen offset:1024 sc1\n"
        "buffer_load_dwordx4 v[72:75], v0, s[40:43], s66 offen offset:2048 sc1\n"
        "buffer_load_dwordx4 v[76:79], v0, s[40:43], s66 offen offset:3072 sc1\n"
        "s_or_b32 s72, s72, 1\n"
        ".Lat_hasK_%=:\n"
        "s_bitcmp1_b32 s72, 1\n"
        "s_cbranch_scc1 .Lat_hasV_%=\n"
        "s_add_u32 s65, s67, 1\n"
        "s_cmp_ge_i32 s73, s65\n"
        "s_cbranch_scc0 .Lat_hasV_%=\n"
        "s_add_u32 s65, s69, s58\n"
        "s_add_u32 s65, s65, 1048576\n"
        "s_add_u32 s66, s65, 4096\n"
        "buffer_load_dwordx4 v[80:83], v0, s[40:43], s65 offen offset:0 sc1\n"
        "buffer_load_dwordx4 v[84:87], v0, s[40:43], s65 offen offset:1024 sc1\n"
        "buffer_load_dwordx4 v[88:91], v0, s[40:43], s65 offen offset:2048 sc1\n"
        "buffer_load_dwordx4 v[92:95], v0, s[40:43], s65 offen offset:3072 sc1\n"
        "buffer_load_dwordx4 v[96:99], v0, s[40:43], s66 offen offset:0 sc1\n"
        "buffer_load_dwordx4 v[100:103], v0, s[40:43], s66 offen offset:1024 sc1\n"
        "buffer_load_dwordx4 v[104:107], v0, s[40:43], s66 offen offset:2048 sc1\n"
        "buffer_load_dwordx4 v[108:111], v0, s[40:43], s66 offen offset:3072 sc1\n"
        "s_or_b32 s72, s72, 2\n"
        ".Lat_hasV_%=:\n"
        "s_bitcmp1_b32 s72, 3\n"
        "s_cbranch_scc1 .Lat_hasM_%=\n"
        "s_cmp_ge_i32 s74, s68\n"
        "s_cbranch_scc0 .Lat_hasM_%=\n"
        "s_add_u32 s65, s69, s59\n"
        "buffer_load_dwordx4 v[112:115], v3, s[40:43], s65 offen offset:0 sc1\n"
        "buffer_load_dwordx4 v[116:119], v3, s[40:43], s65 offen offset:512 sc1\n"
        "buffer_load_dwordx4 v[120:123], v3, s[40:43], s65 offen offset:1024 sc1\n"
        "buffer_load_dwordx4 v[124:127], v3, s[40:43], s65 offen offset:1536 sc1\n"
        "buffer_load_dwordx4 v[128:131], v4, s[40:43], s65 offen offset:2048 sc1\n"
        "buffer_load_dwordx4 v[132:135], v4, s[40:43], s65 offen offset:2560 sc1\n"
        "buffer_load_dwordx4 v[136:139], v4, s[40:43], s65 offen offset:3072 sc1\n"
        "buffer_load_dwordx4 v[140:143], v4, s[40:43], s65 offen offset:3584 sc1\n"
        "s_or_b32 s72, s72, 8\n"
        ".Lat_hasM_%=:\n"
        "s_bitcmp1_b32 s72, 2\n"
        "s_cbranch_scc1 .Lat_hasQ_%=\n"
        "s_add_u32 s65, s67, 2\n"
        "s_cmp_ge_i32 s73, s65\n"
        "s_cbranch_scc0 .Lat_hasQ_%=\n"
        "s_add_u32 s65, s69, s58\n"
        "s_add_u32 s65, s65, 0\n"
        "s_add_u32 s66, s65, 4096\n"
        "buffer_load_dwordx4 v[16:19], v0, s[40:43], s65 offen offset:0 sc1\n"
        "buffer_load_dwordx4 v[20:23], v0, s[40:43], s65 offen offset:1024 sc1\n"
        "buffer_load_dwordx4 v[24:27], v0, s[40:43], s65 offen offset:2048 sc1\n"
        "buffer_load_dwordx4 v[28:31], v0, s[40:43], s65 offen offset:3072 sc1\n"
        "buffer_load_dwordx4 v[32:35], v0, s[40:43], s66 offen offset:0 sc1\n"
        "buffer_load_dwordx4 v[36:39], v0, s[40:43], s66 offen offset:1024 sc1\n"
        "buffer_load_dwordx4 v[40:43], v0, s[40:43], s66 offen offset:2048 sc1\n"
        "buffer_load_dwordx4 v[44:47], v0, s[40:43], s66 offen offset:3072 sc1\n"
        "s_or_b32 s72, s72, 4\n"
        ".Lat_hasQ_%=:\n"
        "s_and_b32 s65, s72, 7\n"
        "s_cmp_eq_u32 s65, 7\n"
        "s_cbranch_scc1 .Lat_T_%=\n"
        "s_add_u32 s64, s64, 1\n"
        "s_and_b32 s65, s64, 255\n"
        "s_cbranch_scc1 .Lat_sp_go_%=\n"
        "s_mov_b32 s65, 40960\n"
        "s_cmp_lt_u32 s64, 1048576\n"
        "s_cbranch_scc1 .Lat_sp_chk_%=\n"
        "v_mov_b32 v6, 1\n"
        "buffer_store_dword v6, off, s[40:43], s65 sc1\n"
        "s_waitcnt vmcnt(0)\n"
        ".Lat_sp_chk_%=:\n"
        "buffer_load_dword v6, off, s[40:43], s65 sc1\n"
        "s_waitcnt vmcnt(0)\n"
        "v_readfirstlane_b32 s65, v6\n"
        "s_nop 0\n"
        "s_cmp_lg_u32 s65, 1\n"
        "s_cbranch_scc1 .Lat_sp_go_%=\n"
        "s_endpgm\n"
        ".Lat_sp_go_%=:\n"
        "s_branch .Lat_more_%=\n"
        ".Lat_T_%=:\n"
        "s_add_u32 s66, s68, 1\n"
        "s_bitcmp1_b64 s[52:53], s66\n"
        "s_cselect_b32 s65, 8192, 0\n"
        "s_lshl_b32 s70, s66, 21\n"
        "s_add_u32 s70, s70, s65\n"
        "s_add_u32 s67, s67, 3\n"
        "s_cmp_eq_u32 s72, 15\n"
        "s_cbranch_scc1 .Lat_T2_%=\n"
        "s_waitcnt vmcnt(0)\n"
        "buffer_load_dword v5, v1, s[40:43], s71 offen sc1\n"
        "s_branch .Lat_T3_%=\n"
        ".Lat_T2_%=:\n"
        "s_bitcmp1_b32 s86, 2\n"
        "s_cbranch_scc1 .Lat_T3_%=\n"
        "s_waitcnt vmcnt(0)\n"
        ".Lat_T3_%=:\n"
        "v_mfma_f32_16x16x32_bf16 v[144:147], v[48:51], v[16:19], 0\n"
        "v_mfma_f32_16x16x32_bf16 v[152:155], v[52:55], v[20:23], 0\n"
        "v_mfma_f32_16x16x32_bf16 v[148:151], v[48:51], v[20:23], 0\n"
        "v_mfma_f32_16x16x32_bf16 v[144:147], v[56:59], v[24:27], v[144:147]\n"
        "v_mfma_f32_16x16x32_bf16 v[152:155], v[60:63], v[28:31], v[152:155]\n"
        "v_mfma_f32_16x16x32_bf16 v[148:151], v[56:59], v[28:31], v[148:151]\n"
        "v_mfma_f32_16x16x32_bf16 v[144:147], v[64:67], v[32:35], v[144:147]\n"
        "v_mfma_f32_16x16x32_bf16 v[152:155], v[68:71], v[36:39], v[152:155]\n"
        "v_mfma_f32_16x16x32_bf16 v[148:151], v[64:67], v[36:39], v[148:151]\n"
        "v_mfma_f32_16x16x32_bf16 v[144:147], v[72:75], v[40:43], v[144:147]\n"
        "v_mfma_f32_16x16x32_bf16 v[152:155], v[76:79], v[44:47], v[152:155]\n"
        "v_mfma_f32_16x16x32_bf16 v[148:151], v[72:75], v[44:47], v[148:151]\n"
        "v_cndmask_b32 v144, v10, v144, s[76:77]\n"
        "v_cndmask_b32 v145, v10, v145, s[78:79]\n"
        "v_cndmask_b32 v146, v10, v146, s[80:81]\n"
        "v_cndmask_b32 v147, v10, v147, s[82:83]\n"
        "v_cndmask_b32 v152, v10, v152, s[76:77]\n"
        "v_cndmask_b32 v153, v10, v153, s[78:79]\n"
        "v_cndmask_b32 v154, v10, v154, s[80:81]\n"
        "v_cndmask_b32 v155, v10, v155, s[82:83]\n"
        "v_max3_f32 v8, v144, v145, v146\n"
        "v_max3_f32 v9, v152, v153, v154\n"
        "v_max_f32 v8, v8, v147\n"
        "v_max3_f32 v9, v9, v155, v148\n"
        "v_max3_f32 v9, v9, v149, v150\n"
        "v_max_f32 v9, v9, v151\n"
        "v_mul_f32 v144, s75, v144\n"
        "v_mul_f32 v145, s75, v145\n"
        "v_mul_f32 v146, s75, v146\n"
        "v_mul_f32 v147, s75, v147\n"
        "v_mul_f32 v148, s75, v148\n"
        "v_mul_f32 v149, s75, v149\n"
        "v_mul_f32 v150, s75, v150\n"
        "v_mul_f32 v151, s75, v151\n"
        "v_mul_f32 v152, s75, v152\n"
        "v_mul_f32 v153, s75, v153\n"
        "v_mul_f32 v154, s75, v154\n"
        "v_mul_f32 v155, s75, v155\n"
        "s_cmp_eq_u32 s72, 15\n"
        "s_cbranch_scc1 .Lat_hasm_%=\n"
        "s_mov_b32 s64, 0\n"
        ".Lat_wm_%=:\n"
        "s_waitcnt vmcnt(0)\n"
        "v_min_i32_dpp v5, v5, v5 quad_perm:[1,0,3,2] row_mask:0xf bank_mask:0xf\n"
        "s_nop 1\n"
        "v_min_i32_dpp v5, v5, v5 quad_perm:[2,3,0,1] row_mask:0xf bank_mask:0xf\n"
        "s_nop 0\n"
        "v_readlane_b32 s73, v5, 0\n"
        "v_readlane_b32 s74, v5, 4\n"
        "s_nop 0\n"
        "s_cmp_ge_i32 s74, s68\n"
        "s_cbranch_scc1 .Lat_gm_%=\n"
        "s_add_u32 s64, s64, 1\n"
        "s_and_b32 s65, s64, 255\n"
        "s_cbranch_scc1 .Lat_sq_go_%=\n"
        "s_mov_b32 s65, 40960\n"
        "s_cmp_lt_u32 s64, 1048576\n"
        "s_cbranch_scc1 .Lat_sq_chk_%=\n"
        "v_mov_b32 v6, 1\n"
        "buffer_store_dword v6, off, s[40:43], s65 sc1\n"
        "s_waitcnt vmcnt(0)\n"
        ".Lat_sq_chk_%=:\n"
        "buffer_load_dword v6, off, s[40:43], s65 sc1\n"
        "s_waitcnt vmcnt(0)\n"
        "v_readfirstlane_b32 s65, v6\n"
        "s_nop 0\n"
        "s_cmp_lg_u32 s65, 1\n"
        "s_cbranch_scc1 .Lat_sq_go_%=\n"
        "s_endpgm\n"
        ".Lat_sq_go_%=:\n"
        "buffer_load_dword v5, v1, s[40:43], s71 offen sc1\n"
        "s_branch .Lat_wm_%=\n"
        ".Lat_gm_%=:\n"
        "s_add_u32 s65, s69, s59\n"
        "buffer_load_dwordx4 v[112:115], v3, s[40:43], s65 offen offset:0 sc1\n"
        "buffer_load_dwordx4 v[116:119], v3, s[40:43], s65 offen offset:512 sc1\n"
        "buffer_load_dwordx4 v[120:123], v3, s[40:43], s65 offen offset:1024 sc1\n"
        "buffer_load_dwordx4 v[124:127], v3, s[40:43], s65 offen offset:1536 sc1\n"
        "buffer_load_dwordx4 v[128:131], v4, s[40:43], s65 offen offset:2048 sc1\n"
        "buffer_load_dwordx4 v[132:135], v4, s[40:43], s65 offen offset:2560 sc1\n"
        "buffer_load_dwordx4 v[136:139], v4, s[40:43], s65 offen offset:3072 sc1\n"
        "buffer_load_dwordx4 v[140:143], v4, s[40:43], s65 offen offset:3584 sc1\n"
        ".Lat_hasm_%=:\n"
        "s_mov_b32 s72, 0\n"
        "s_add_u32 s65, s70, s58\n"
        "s_add_u32 s65, s65, 524288\n"
        "s_add_u32 s66, s67, 0\n"
        "s_cmp_ge_i32 s73, s66\n"
        "s_cselect_b32 s65, s65, 241172480\n"
        "s_cselect_b32 s66, 1, 0\n"
        "s_or_b32 s72, s72, s66\n"
        "s_add_u32 s66, s65, 4096\n"
        "buffer_load_dwordx4 v[48:51], v0, s[40:43], s65 offen offset:0 sc1\n"
        "buffer_load_dwordx4 v[52:55], v0, s[40:43], s65 offen offset:1024 sc1\n"
        "buffer_load_dwordx4 v[56:59], v0, s[40:43], s65 offen offset:2048 sc1\n"
        "buffer_load_dwordx4 v[60:63], v0, s[40:43], s65 offen offset:3072 sc1\n"
        "buffer_load_dwordx4 v[64:67], v0, s[40:43], s66 offen offset:0 sc1\n"
        "buffer_load_dwordx4 v[68:71], v0, s[40:43], s66 offen offset:1024 sc1\n"
        "buffer_load_dwordx4 v[72:75], v0, s[40:43], s66 offen offset:2048 sc1\n"
        "buffer_load_dwordx4 v[76:79], v0, s[40:43], s66 offen offset:3072 sc1\n"
        "s_add_u32 s65, s70, s58\n"
        "s_add_u32 s65, s65, 0\n"
        "s_add_u32 s66, s67, 2\n"
        "s_cmp_ge_i32 s73, s66\n"
        "s_cselect_b32 s65, s65, 241172480\n"
        "s_cselect_b32 s66, 4, 0\n"
        "s_or_b32 s72, s72, s66\n"
        "s_add_u32 s66, s65, 4096\n"
        "s_waitcnt vmcnt(15)\n"
        "v_mfma_f32_16x16x32_bf16 v[156:159], v[112:115], v[16:19], 0\n"
        "s_waitcnt vmcnt(14)\n"
        "v_mfma_f32_16x16x32_bf16 v[156:159], v[116:119], v[24:27], v[156:159]\n"
        "s_waitcnt vmcnt(13)\n"
        "v_mfma_f32_16x16x32_bf16 v[156:159], v[120:123], v[32:35], v[156:159]\n"
        "s_waitcnt vmcnt(12)\n"
        "v_mfma_f32_16x16x32_bf16 v[156:159], v[124:127], v[40:43], v[156:159]\n"
        "v_mfma_f32_16x16x32_bf16 v[160:163], v[112:115], v[20:23], 0\n"
        "v_mfma_f32_16x16x32_bf16 v[160:163], v[116:119], v[28:31], v[160:163]\n"
        "v_mfma_f32_16x16x32_bf16 v[160:163], v[120:123], v[36:39], v[160:163]\n"
        "v_mfma_f32_16x16x32_bf16 v[160:163], v[124:127], v[44:47], v[160:163]\n"
        "v_max3_f32 v8, v8, v156, v157\n"
        "buffer_load_dwordx4 v[16:19], v0, s[40:43], s65 offen offset:0 sc1\n"
        "v_max3_f32 v216, v8, v158, v159\n"
        "v_max3_f32 v217, v8, v158, v159\n"
        "v_mul_f32 v156, s75, v156\n"
        "v_mul_f32 v157, s75, v157\n"
        "v_permlane16_swap_b32 v216, v217\n"
        "v_max_f32 v8, v216, v217\n"
        "buffer_load_dwordx4 v[20:23], v0, s[40:43], s65 offen offset:1024 sc1\n"
        "v_max_f32 v216, v216, v217\n"
        "v_mul_f32 v158, s75, v158\n"
        "v_mul_f32 v159, s75, v159\n"
        "v_permlane32_swap_b32 v8, v216\n"
        "v_max_f32 v8, v8, v216\n"
        "v_mul_f32 v8, s75, v8\n"
        "buffer_load_dwordx4 v[24:27], v0, s[40:43], s65 offen offset:2048 sc1\n"
        "v_cndmask_b32 v216, v8, -v10, s[84:85]\n"
        "v_sub_f32 v144, v144, v8\n"
        "v_exp_f32 v144, v144\n"
        "v_sub_f32 v145, v145, v8\n"
        "v_exp_f32 v145, v145\n"
        "v_sub_f32 v146, v146, v8\n"
        "buffer_load_dwordx4 v[28:31], v0, s[40:43], s65 offen offset:3072 sc1\n"
        "v_exp_f32 v146, v146\n"
        "v_sub_f32 v147, v147, v8\n"
        "v_exp_f32 v147, v147\n"
        "v_sub_f32 v156, v156, v216\n"
        "v_exp_f32 v156, v156\n"
        "v_sub_f32 v157, v157, v216\n"
        "buffer_load_dwordx4 v[32:35], v0, s[40:43], s66 offen offset:0 sc1\n"
        "v_exp_f32 v157, v157\n"
        "v_sub_f32 v158, v158, v216\n"
        "v_exp_f32 v158, v158\n"
        "v_sub_f32 v159, v159, v216\n"
        "v_exp_f32 v159, v159\n"
        "v_add_f32 v216, v144, v145\n"
        "buffer_load_dwordx4 v[36:39], v0, s[40:43], s66 offen offset:1024 sc1\n"
        "v_add_f32 v217, v146, v147\n"
        "v_add_f32 v216, v216, v156\n"
        "v_add_f32 v217, v217, v157\n"
        "v_add_f32 v216, v216, v158\n"
        "v_add_f32 v217, v217, v159\n"
        "v_add_f32 v8, v216, v217\n"
        "buffer_load_dwordx4 v[40:43], v0, s[40:43], s66 offen offset:2048 sc1\n"
        "v_add_f32 v216, v216, v217\n"
        "s_nop 1\n"
        "v_permlane16_swap_b32 v8, v216\n"
        "v_add_f32 v217, v8, v216\n"
        "v_add_f32 v216, v8, v216\n"
        "s_nop 1\n"
        "buffer_load_dwordx4 v[44:47], v0, s[40:43], s66 offen offset:3072 sc1\n"
        "v_permlane32_swap_b32 v216, v217\n"
        "v_add_f32 v8, v216, v217\n"
        "v_rcp_f32 v8, v8\n"
        "s_nop 0\n"
        "v_mul_f32 v156, v156, v8\n"
        "v_mul_f32 v157, v157, v8\n"
        "v_mul_f32 v158, v158, v8\n"
        "v_mul_f32 v159, v159, v8\n"
        "v_cvt_pk_bf16_f32 v174, v156, v157\n"
        "v_cvt_pk_bf16_f32 v175, v158, v159\n"
        "v_mul_f32 v144, v144, v8\n"
        "v_mul_f32 v145, v145, v8\n"
        "v_cvt_pk_bf16_f32 v164, v144, v145\n"
        "v_mul_f32 v146, v146, v8\n"
        "v_mul_f32 v147, v147, v8\n"
        "v_cvt_pk_bf16_f32 v165, v146, v147\n"
        "buffer_load_dword v5, v1, s[40:43], s71 offen sc1\n"
        "s_add_u32 s65, s69, s58\n"
        "s_add_u32 s66, s65, 4096\n"
        "s_waitcnt vmcnt(17)\n"
        "v_mfma_f32_16x16x32_bf16 v[184:187], v[128:131], v[174:177], 0\n"
        "v_max3_f32 v9, v9, v160, v161\n"
        "v_max3_f32 v220, v9, v162, v163\n"
        "v_max3_f32 v221, v9, v162, v163\n"
        "v_mul_f32 v160, s75, v160\n"
        "v_mul_f32 v161, s75, v161\n"
        "v_mfma_f32_16x16x32_bf16 v[188:191], v[128:131], v[172:175], 0\n"
        "v_permlane16_swap_b32 v220, v221\n"
        "v_max_f32 v9, v220, v221\n"
        "v_max_f32 v220, v220, v221\n"
        "v_mul_f32 v162, s75, v162\n"
        "v_mul_f32 v163, s75, v163\n"
        "v_mfma_f32_16x16x32_bf16 v[184:187], v[80:83], v[164:167], v[184:187]\n"
        "v_permlane32_swap_b32 v9, v220\n"
        "v_max_f32 v9, v9, v220\n"
        "v_mul_f32 v9, s75, v9\n"
        "v_cndmask_b32 v220, v9, -v10, s[84:85]\n"
        "v_sub_f32 v148, v148, v9\n"
        "v_mfma_f32_16x16x32_bf16 v[188:191], v[84:87], v[164:167], v[188:191]\n"
        "v_exp_f32 v148, v148\n"
        "v_sub_f32 v149, v149, v9\n"
        "v_exp_f32 v149, v149\n"
        "v_sub_f32 v150, v150, v9\n"
        "v_mfma_f32_16x16x32_bf16 v[192:195], v[132:135], v[174:177], 0\n"
        "v_exp_f32 v150, v150\n"
        "v_sub_f32 v151, v151, v9\n"
        "v_exp_f32 v151, v151\n"
        "v_sub_f32 v152, v152, v9\n"
        "v_mfma_f32_16x16x32_bf16 v[196:199], v[132:135], v[172:175], 0\n"
        "v_cvt_pk_bf16_f32 v184, v184, v185\n"
        "v_cvt_pk_bf16_f32 v185, v186, v187\n"
        "v_cvt_pk_bf16_f32 v186, v188, v189\n"
        "v_cvt_pk_bf16_f32 v187, v190, v191\n"
        "buffer_store_dwordx4 v[184:187], v0, s[40:43], s65 offen offset:0 sc1\n"
        "v_mfma_f32_16x16x32_bf16 v[192:195], v[88:91], v[164:167], v[192:195]\n"
        "v_exp_f32 v152, v152\n"
        "v_sub_f32 v153, v153, v9\n"
        "v_exp_f32 v153, v153\n"
        "v_sub_f32 v154, v154, v9\n"
        "v_mfma_f32_16x16x32_bf16 v[196:199], v[92:95], v[164:167], v[196:199]\n"
        "v_exp_f32 v154, v154\n"
        "v_sub_f32 v155, v155, v9\n"
        "v_exp_f32 v155, v155\n"
        "v_sub_f32 v160, v160, v220\n"
        "v_mfma_f32_16x16x32_bf16 v[200:203], v[136:139], v[174:177], 0\n"
        "v_exp_f32 v160, v160\n"
        "v_sub_f32 v161, v161, v220\n"
        "v_exp_f32 v161, v161\n"
        "v_sub_f32 v162, v162, v220\n"
        "v_mfma_f32_16x16x32_bf16 v[204:207], v[136:139], v[172:175], 0\n"
        "v_cvt_pk_bf16_f32 v192, v192, v193\n"
        "v_cvt_pk_bf16_f32 v193, v194, v195\n"
        "v_cvt_pk_bf16_f32 v194, v196, v197\n"
        "v_cvt_pk_bf16_f32 v195, v198, v199\n"
        "buffer_store_dwordx4 v[192:195], v0, s[40:43], s65 offen offset:2048 sc1\n"
        "v_mfma_f32_16x16x32_bf16 v[200:203], v[96:99], v[164:167], v[200:203]\n"
        "v_exp_f32 v162, v162\n"
        "v_sub_f32 v163, v163, v220\n"
        "v_exp_f32 v163, v163\n"
        "v_add_f32 v220, v148, v149\n"
        "v_mfma_f32_16x16x32_bf16 v[204:207], v[100:103], v[164:167], v[204:207]\n"
        "v_add_f32 v221, v150, v151\n"
        "v_add_f32 v220, v220, v152\n"
        "v_add_f32 v221, v221, v153\n"
        "v_add_f32 v220, v220, v154\n"
        "v_add_f32 v221, v221, v155\n"
        "v_add_f32 v220, v220, v160\n"
        "v_mfma_f32_16x16x32_bf16 v[208:211], v[140:143], v[174:177], 0\n"
        "v_add_f32 v221, v221, v161\n"
        "v_add_f32 v220, v220, v162\n"
        "v_add_f32 v221, v221, v163\n"
        "v_add_f32 v9, v220, v221\n"
        "v_add_f32 v220, v220, v221\n"
        "v_mfma_f32_16x16x32_bf16 v[212:215], v[140:143], v[172:175], 0\n"
        "v_cvt_pk_bf16_f32 v200, v200, v201\n"
        "v_cvt_pk_bf16_f32 v201, v202, v203\n"
        "v_cvt_pk_bf16_f32 v202, v204, v205\n"
        "v_cvt_pk_bf16_f32 v203, v206, v207\n"
        "buffer_store_dwordx4 v[200:203], v0, s[40:43], s66 offen offset:0 sc1\n"
        "v_mfma_f32_16x16x32_bf16 v[208:211], v[104:107], v[164:167], v[208:211]\n"
        "s_nop 1\n"
        "v_permlane16_swap_b32 v9, v220\n"
        "v_add_f32 v221, v9, v220\n"
        "v_add_f32 v220, v9, v220\n"
        "v_mfma_f32_16x16x32_bf16 v[212:215], v[108:111], v[164:167], v[212:215]\n"
        "s_nop 1\n"
        "v_permlane32_swap_b32 v220, v221\n"
        "v_add_f32 v9, v220, v221\n"
        "v_rcp_f32 v9, v9\n"
        "s_nop 0\n"
        "v_mul_f32 v160, v160, v9\n"
        "v_mul_f32 v161, v161, v9\n"
        "v_mul_f32 v162, v162, v9\n"
        "v_mul_f32 v163, v163, v9\n"
        "v_cvt_pk_bf16_f32 v180, v160, v161\n"
        "v_cvt_pk_bf16_f32 v181, v162, v163\n"
        "v_mul_f32 v148, v148, v9\n"
        "v_mul_f32 v149, v149, v9\n"
        "v_cvt_pk_bf16_f32 v168, v148, v149\n"
        "v_mul_f32 v150, v150, v9\n"
        "v_mul_f32 v151, v151, v9\n"
        "v_cvt_pk_bf16_f32 v169, v150, v151\n"
        "v_mul_f32 v152, v152, v9\n"
        "v_mul_f32 v153, v153, v9\n"
        "v_cvt_pk_bf16_f32 v170, v152, v153\n"
        "v_mul_f32 v154, v154, v9\n"
        "v_mul_f32 v155, v155, v9\n"
        "v_cvt_pk_bf16_f32 v171, v154, v155\n"
        "v_cvt_pk_bf16_f32 v208, v208, v209\n"
        "v_cvt_pk_bf16_f32 v209, v210, v211\n"
        "v_cvt_pk_bf16_f32 v210, v212, v213\n"
        "v_cvt_pk_bf16_f32 v211, v214, v215\n"
        "buffer_store_dwordx4 v[208:211], v0, s[40:43], s66 offen offset:2048 sc1\n"
        "v_mfma_f32_16x16x32_bf16 v[184:187], v[128:131], v[180:183], 0\n"
        "v_mfma_f32_16x16x32_bf16 v[188:191], v[128:131], v[178:181], 0\n"
        "v_mfma_f32_16x16x32_bf16 v[184:187], v[80:83], v[168:171], v[184:187]\n"
        "v_mfma_f32_16x16x32_bf16 v[188:191], v[84:87], v[168:171], v[188:191]\n"
        "v_mfma_f32_16x16x32_bf16 v[192:195], v[132:135], v[180:183], 0\n"
        "v_mfma_f32_16x16x32_bf16 v[196:199], v[132:135], v[178:181], 0\n"
        "v_cvt_pk_bf16_f32 v184, v184, v185\n"
        "v_cvt_pk_bf16_f32 v185, v186, v187\n"
        "v_cvt_pk_bf16_f32 v186, v188, v189\n"
        "v_mfma_f32_16x16x32_bf16 v[192:195], v[88:91], v[168:171], v[192:195]\n"
        "v_cvt_pk_bf16_f32 v187, v190, v191\n"
        "buffer_store_dwordx4 v[184:187], v0, s[40:43], s65 offen offset:1024 sc1\n"
        "v_mfma_f32_16x16x32_bf16 v[196:199], v[92:95], v[168:171], v[196:199]\n"
        "v_mfma_f32_16x16x32_bf16 v[200:203], v[136:139], v[180:183], 0\n"
        "v_mfma_f32_16x16x32_bf16 v[204:207], v[136:139], v[178:181], 0\n"
        "v_cvt_pk_bf16_f32 v192, v192, v193\n"
        "v_cvt_pk_bf16_f32 v193, v194, v195\n"
        "v_cvt_pk_bf16_f32 v194, v196, v197\n"
        "v_mfma_f32_16x16x32_bf16 v[200:203], v[96:99], v[168:171], v[200:203]\n"
        "v_cvt_pk_bf16_f32 v195, v198, v199\n"
        "buffer_store_dwordx4 v[192:195], v0, s[40:43], s65 offen offset:3072 sc1\n"
        "v_mfma_f32_16x16x32_bf16 v[204:207], v[100:103], v[168:171], v[204:207]\n"
        "v_mfma_f32_16x16x32_bf16 v[208:211], v[140:143], v[180:183], 0\n"
        "v_mfma_f32_16x16x32_bf16 v[212:215], v[140:143], v[178:181], 0\n"
        "v_cvt_pk_bf16_f32 v200, v200, v201\n"
        "v_cvt_pk_bf16_f32 v201, v202, v203\n"
        "v_cvt_pk_bf16_f32 v202, v204, v205\n"
        "v_mfma_f32_16x16x32_bf16 v[208:211], v[104:107], v[168:171], v[208:211]\n"
        "v_cvt_pk_bf16_f32 v203, v206, v207\n"
        "buffer_store_dwordx4 v[200:203], v0, s[40:43], s66 offen offset:1024 sc1\n"
        "v_mfma_f32_16x16x32_bf16 v[212:215], v[108:111], v[168:171], v[212:215]\n"
        "s_waitcnt vmcnt(7)\n"
        "v_min_i32_dpp v5, v5, v5 quad_perm:[1,0,3,2] row_mask:0xf bank_mask:0xf\n"
        "s_nop 1\n"
        "v_min_i32_dpp v5, v5, v5 quad_perm:[2,3,0,1] row_mask:0xf bank_mask:0xf\n"
        "s_nop 0\n"
        "v_readlane_b32 s73, v5, 0\n"
        "v_readlane_b32 s74, v5, 4\n"
        "s_nop 0\n"
        "s_nop 0\n"
        "v_cvt_pk_bf16_f32 v208, v208, v209\n"
        "v_cvt_pk_bf16_f32 v209, v210, v211\n"
        "v_cvt_pk_bf16_f32 v210, v212, v213\n"
        "v_cvt_pk_bf16_f32 v211, v214, v215\n"
        "buffer_store_dwordx4 v[208:211], v0, s[40:43], s66 offen offset:3072 sc1\n"
        "s_add_u32 s65, s70, s58\n"
        "s_add_u32 s65, s65, 1048576\n"
        "s_add_u32 s66, s67, 1\n"
        "s_cmp_ge_i32 s73, s66\n"
        "s_cselect_b32 s65, s65, 241172480\n"
        "s_cselect_b32 s66, 2, 0\n"
        "s_or_b32 s72, s72, s66\n"
        "s_add_u32 s66, s65, 4096\n"
        "buffer_load_dwordx4 v[80:83], v0, s[40:43], s65 offen offset:0 sc1\n"
        "buffer_load_dwordx4 v[84:87], v0, s[40:43], s65 offen offset:1024 sc1\n"
        "buffer_load_dwordx4 v[88:91], v0, s[40:43], s65 offen offset:2048 sc1\n"
        "buffer_load_dwordx4 v[92:95], v0, s[40:43], s65 offen offset:3072 sc1\n"
        "buffer_load_dwordx4 v[96:99], v0, s[40:43], s66 offen offset:0 sc1\n"
        "buffer_load_dwordx4 v[100:103], v0, s[40:43], s66 offen offset:1024 sc1\n"
        "buffer_load_dwordx4 v[104:107], v0, s[40:43], s66 offen offset:2048 sc1\n"
        "buffer_load_dwordx4 v[108:111], v0, s[40:43], s66 offen offset:3072 sc1\n"
        "v_mov_b32 v6, s68\n"
        "s_waitcnt vmcnt(8)\n"
        "buffer_store_dword v6, v2, s[40:43], s60 offen sc1\n"
        "s_add_u32 s68, s68, 1\n"
        "s_cmp_lt_u32 s68, 64\n"
        "s_cbranch_scc1 .Lat_top_%=\n"
        "s_endpgm\n"
        :: "s"(p.ws), "s"(pair(x4, pr)), "s"(pair(PAGE + h * 16384, PAGE + spread(A_KM) + (h >> 1) * 16384 + (h & 1) * 4096)), "s"(pair(0, w)),
           "v"(pair(lane * 16, lane < 8 ? flag(((lane & 4) ? F_CH : F_S1 + 128) + 4 * h + (lane & 3)) : OOB)), "v"(pair(lane < 6 ? flag(F_AT + 6 * h + lane) : OOB, (g * 8 + (i & 7)) * 16)),
           "v"(pair((g & 1) * 256 + i * 16, 4 * g - i))
        : "v0", "v1", "v2", "v3", "v4", "v5", "v6", "v7", "v8", "v9", "v10", "v11", "v12", "v13", "v14", "v15", "v16", "v17", "v18", "v19", "v20", "v21", "v22", "v23", "v24", "v25", "v26", "v27", "v28", "v29", "v30", "v31", "v32", "v33", "v34", "v35", "v36", "v37", "v38", "v39", "v40", "v41", "v42", "v43", "v44", "v45", "v46", "v47", "v48", "v49", "v50", "v51", "v52", "v53", "v54", "v55", "v56", "v57", "v58", "v59", "v60", "v61", "v62", "v63", "v64", "v65", "v66", "v67", "v68", "v69", "v70", "v71", "v72", "v73", "v74", "v75", "v76", "v77", "v78", "v79", "v255", "a255", "s34", "s35", "s36", "s37", "s38", "s39", "s40", "s41", "s42", "s43", "s44", "s45", "s46", "s47", "s48", "s49", "s50", "s51", "s52", "s53", "s54", "s55", "s56", "s57", "s58", "s59", "s60", "s61", "s62", "s63", "s64", "s65", "s66", "s67", "s68", "s69", "s70", "s71", "s72", "s73", "s74", "s75", "s76", "s77", "s78", "s79", "s80", "s81", "s82", "s83", "s84", "s85", "s86", "s87", "s88", "s89", "s90", "s91", "s92", "s93", "s94", "s95", "s96", "s97", "s98", "s99", "vcc", "scc", "memory");
    __builtin_unreachable();
}

template <bool R5>
__device__ __forceinline__ void role_red(const Params& p, int a, int x4, int pr) {
    const unsigned lane = threadIdx.x & 63, i = lane & 15, g = lane >> 4;
    const int w = rfl(threadIdx.x >> 6);
    if (w == 3) return;
    unsigned to[3], po[3], one[4];
#pragma unroll
    for (int n = 0; n < 3; ++n) { const unsigned b = 3 * w + n; to[n] = b < 8 ? a * 16384 + b * 1024 : 65536 + a * 1024; po[n] = part_off(a, 0, 8 * b); }
#pragma unroll
    for (int e = 0; e < 4; ++e) one[e] = ((i & 7) == 2 * e) ? 0x3f80u : (((i & 7) == 2 * e + 1) ? 0x3f800000u : 0u);
    const u64 o0 = pair(one[0], one[1]), o1 = pair(one[2], one[3]);
    const bool lo = i < 8, r0 = g == 2 * (i >> 3), r1 = g == 1 + 2 * (i >> 3);
    const unsigned fin = lane < 16 ? flag((R5 ? F_F2 : F_OP) + a * 64 + w * 16 + lane) : OOB;
    const unsigned fout = (w == 0 && lane < (R5 ? 32 : 24)) ? flag((R5 ? F_XN : F_R3) + 4 * lane + a) : OOB;
#define RED_ARGS :: "s"(p.ws), "s"(p.x), "s"(p.out), "s"(pair(x4, pr)), "s"(pair(to[0], to[1])), "s"(pair(to[2], po[0])), "s"(pair(po[1], po[2])), \
        "v"(pair(lane * 16, fin)), "v"(pair(fout, g * 16384 + i * 16)), "v"(pair(g * (w == 2 ? 1024 : 16384) + i * 16, (8 * a + (lane & 7)) * 1152 + w * 384 + (lane >> 3) * 16)), \
        "v"(pair(w * 64 + (lane & 7) * 8, (lane & 7) * 8)), "v"(lo ? o0 : 0), "v"(lo ? o1 : 0), "v"(lo ? 0 : o0), "v"(lo ? 0 : o1), \
        "v"(r0 ? o0 : 0), "v"(r0 ? o1 : 0), "v"(r1 ? o0 : 0), "v"(r1 ? o1 : 0)
    if constexpr (R5) {
        asm volatile(
        "s_nop 4\n"
        "s_mov_b64 s[40:41], %0\n"
        "s_and_b32 s41, s41, 0xffff\n"
        "s_mov_b32 s42, 236978176\n"
        "s_mov_b32 s43, 0x20000\n"
        "s_mov_b64 s[44:45], %1\n"
        "s_and_b32 s45, s45, 0xffff\n"
        "s_mov_b32 s46, 2359296\n"
        "s_mov_b32 s47, 0x20000\n"
        "s_mov_b64 s[56:57], %3\n"
        "s_mov_b64 s[58:59], %4\n"
        "s_mov_b64 s[60:61], %5\n"
        "s_mov_b64 s[62:63], %6\n"
        "v_mov_b64 v[0:1], %7\n"
        "v_mov_b64 v[2:3], %8\n"
        "v_mov_b64 v[4:5], %9\n"
        "v_mov_b64 v[6:7], %10\n"
        "v_mov_b64 v[8:9], %11\n"
        "v_mov_b64 v[10:11], %12\n"
        "v_mov_b64 v[12:13], %13\n"
        "v_mov_b64 v[14:15], %14\n"
        "v_mov_b64 v[16:17], %15\n"
        "v_mov_b64 v[18:19], %16\n"
        "v_mov_b64 v[20:21], %17\n"
        "v_mov_b64 v[22:23], %18\n"
        "s_mov_b32 s68, 0\n"
        "s_mul_i32 s65, s68, 36864\n"
        "buffer_load_dwordx4 v[80:83], v5, s[44:47], s65 offen offset:0\n"
        "buffer_load_dwordx4 v[84:87], v5, s[44:47], s65 offen offset:128\n"
        "buffer_load_dwordx4 v[88:91], v5, s[44:47], s65 offen offset:256\n"
        "v_lshrrev_b32 v26, 4, v0\n"
        "s_lshl_b32 s66, s57, 7\n"
        "s_mov_b32 s64, 0\n"
        ".Lr5_pair_top_%=:\n"
        "s_mov_b64 exec, 1\n"
        "buffer_load_dword v27, off, s[40:43], s66 sc1\n"
        "s_mov_b64 exec, -1\n"
        "s_waitcnt vmcnt(0)\n"
        "v_cmp_gt_i32 vcc, 0, v27\n"
        "s_nop 0\n"
        "s_and_b64 vcc, vcc, 1\n"
        "s_cbranch_vccz .Lr5_pair_ok_%=\n"
        "s_add_u32 s64, s64, 1\n"
        "s_and_b32 s65, s64, 255\n"
        "s_cbranch_scc1 .Lr5_pair_go_%=\n"
        "s_mov_b32 s65, 40960\n"
        "s_cmp_lt_u32 s64, 1048576\n"
        "s_cbranch_scc1 .Lr5_pair_chk_%=\n"
        "v_mov_b32 v27, 1\n"
        "buffer_store_dword v27, off, s[40:43], s65 sc1\n"
        "s_waitcnt vmcnt(0)\n"
        ".Lr5_pair_chk_%=:\n"
        "buffer_load_dword v27, off, s[40:43], s65 sc1\n"
        "s_waitcnt vmcnt(0)\n"
        "v_readfirstlane_b32 s65, v27\n"
        "s_nop 0\n"
        "s_cmp_lg_u32 s65, 1\n"
        "s_cbranch_scc1 .Lr5_pair_go_%=\n"
        "s_endpgm\n"
        ".Lr5_pair_go_%=:\n"
        "s_sleep 2\n"
        "s_branch .Lr5_pair_top_%=\n"
        ".Lr5_pair_ok_%=:\n"
        "v_readfirstlane_b32 s50, v27\n"
        "s_nop 0\n"
        "s_xor_b32 s50, s50, s56\n"
        "v_lshlrev_b32 v28, 7, v26\n"
        "s_sub_u32 s66, 0, s50\n"
        "s_mov_b32 s52, 24576\n"
        "s_mov_b32 s64, 0\n"
        ".Lr5_pg0_top_%=:\n"
        "buffer_load_dword v27, v28, s[40:43], s52 offen sc1\n"
        "s_waitcnt vmcnt(0)\n"
        "v_cmp_gt_i32 vcc, 0, v27\n"
        "s_cbranch_vccz .Lr5_pg0_ok_%=\n"
        "s_add_u32 s64, s64, 1\n"
        "s_and_b32 s65, s64, 255\n"
        "s_cbranch_scc1 .Lr5_pg0_go_%=\n"
        "s_mov_b32 s65, 40960\n"
        "s_cmp_lt_u32 s64, 1048576\n"
        "s_cbranch_scc1 .Lr5_pg0_chk_%=\n"
        "v_mov_b32 v27, 1\n"
        "buffer_store_dword v27, off, s[40:43], s65 sc1\n"
        "s_waitcnt vmcnt(0)\n"
        ".Lr5_pg0_chk_%=:\n"
        "buffer_load_dword v27, off, s[40:43], s65 sc1\n"
        "s_waitcnt vmcnt(0)\n"
        "v_readfirstlane_b32 s65, v27\n"
        "s_nop 0\n"
        "s_cmp_lg_u32 s65, 1\n"
        "s_cbranch_scc1 .Lr5_pg0_go_%=\n"
        "s_endpgm\n"
        ".Lr5_pg0_go_%=:\n"
        "s_sleep 2\n"
        "s_branch .Lr5_pg0_top_%=\n"
        ".Lr5_pg0_ok_%=:\n"
        "v_cmp_eq_u32 s[52:53], 1, v27\n"
        "s_nop 1\n"
        "s_mov_b32 s54, 32768\n"
        "s_mov_b32 s64, 0\n"
        ".Lr5_pg1_top_%=:\n"
        "buffer_load_dword v27, v28, s[40:43], s54 offen sc1\n"
        "s_waitcnt vmcnt(0)\n"
        "v_cmp_gt_i32 vcc, 0, v27\n"
        "s_cbranch_vccz .Lr5_pg1_ok_%=\n"
        "s_add_u32 s64, s64, 1\n"
        "s_and_b32 s65, s64, 255\n"
        "s_cbranch_scc1 .Lr5_pg1_go_%=\n"
        "s_mov_b32 s65, 40960\n"
        "s_cmp_lt_u32 s64, 1048576\n"
        "s_cbranch_scc1 .Lr5_pg1_chk_%=\n"
        "v_mov_b32 v27, 1\n"
        "buffer_store_dword v27, off, s[40:43], s65 sc1\n"
        "s_waitcnt vmcnt(0)\n"
        ".Lr5_pg1_chk_%=:\n"
        "buffer_load_dword v27, off, s[40:43], s65 sc1\n"
        "s_waitcnt vmcnt(0)\n"
        "v_readfirstlane_b32 s65, v27\n"
        "s_nop 0\n"
        "s_cmp_lg_u32 s65, 1\n"
        "s_cbranch_scc1 .Lr5_pg1_go_%=\n"
        "s_endpgm\n"
        ".Lr5_pg1_go_%=:\n"
        "s_sleep 2\n"
        "s_branch .Lr5_pg1_top_%=\n"
        ".Lr5_pg1_ok_%=:\n"
        "v_cmp_eq_u32 s[54:55], 1, v27\n"
        "s_nop 1\n"
        "s_mov_b32 vcc_lo, 0x55555555\n"
        "s_mov_b32 vcc_hi, 0x55555555\n"
        "s_xor_b64 vcc, vcc, s[52:53]\n"
        "s_bcnt1_i32_b64 s65, vcc\n"
        "s_sub_u32 s64, s65, 23\n"
        "s_cmp_lt_u32 s64, 19\n"
        "s_cbranch_scc1 .Lr5_rawmap_%=\n"
        "s_cmp_lt_u32 s65, 32\n"
        "s_cselect_b32 s65, 0, -1\n"
        "s_xor_b32 s52, s65, 0x55555555\n"
        "s_xor_b32 s53, s65, 0x55555555\n"
        "s_xor_b32 s54, s65, 0x55555555\n"
        "s_xor_b32 s55, s65, 0xaaaaaaaa\n"
        ".Lr5_rawmap_%=:\n"
        "s_xor_b32 s52, s52, s66\n"
        "s_xor_b32 s53, s53, s66\n"
        "s_xor_b32 s54, s54, s66\n"
        "s_xor_b32 s55, s55, s66\n"
        "s_and_b32 s69, s55, 1\n"
        "s_lshl_b32 s69, s69, 13\n"
        "s_xor_b32 s70, s69, 8192\n"
        "s_cmp_eq_u32 s50, 0\n"
        "s_cbranch_scc0 .Lr5_l1_%=\n"
        ".Lr5_l0_%=:\n"
        "s_bitcmp1_b64 s[52:53], s68\n"
        "s_cselect_b32 s65, 8192, 0\n"
        "s_lshl_b32 s71, s68, 21\n"
        "s_add_u32 s71, s71, s65\n"
        "s_add_u32 s71, s71, 2097152\n"
        "s_lshr_b32 s65, s68, 2\n"
        "s_add_u32 s65, s65, 1\n"
        "s_bitcmp1_b32 s55, s65\n"
        "s_cselect_b32 s65, 8192, 0\n"
        "s_lshl_b32 s72, s68, 19\n"
        "s_add_u32 s72, s72, s65\n"
        "s_add_u32 s72, s72, 203423744\n"
        "s_add_u32 s76, s72, s61\n"
        "s_add_u32 s79, s71, s58\n"
        "s_add_u32 s79, s79, 1998848\n"
        "s_add_u32 s77, s72, s62\n"
        "s_add_u32 s80, s71, s59\n"
        "s_add_u32 s80, s80, 1998848\n"
        "s_add_u32 s78, s72, s63\n"
        "s_add_u32 s81, s71, s60\n"
        "s_add_u32 s81, s81, 1998848\n"
        "s_mov_b32 s64, 0\n"
        ".Lr5_l0w_top_%=:\n"
        "buffer_load_dword v24, v1, s[40:43], s69 offen sc1\n"
        "s_waitcnt vmcnt(0)\n"
        "v_cmp_gt_i32 vcc, s68, v24\n"
        "s_nop 0\n"
        "s_and_b64 vcc, vcc, 0xffff\n"
        "s_cbranch_vccz .Lr5_l0w_ok_%=\n"
        "s_add_u32 s64, s64, 1\n"
        "s_and_b32 s65, s64, 255\n"
        "s_cbranch_scc1 .Lr5_l0w_go_%=\n"
        "s_mov_b32 s65, 40960\n"
        "s_cmp_lt_u32 s64, 1048576\n"
        "s_cbranch_scc1 .Lr5_l0w_chk_%=\n"
        "v_mov_b32 v24, 1\n"
        "buffer_store_dword v24, off, s[40:43], s65 sc1\n"
        "s_waitcnt vmcnt(0)\n"
        ".Lr5_l0w_chk_%=:\n"
        "buffer_load_dword v24, off, s[40:43], s65 sc1\n"
        "s_waitcnt vmcnt(0)\n"
        "v_readfirstlane_b32 s65, v24\n"
        "s_nop 0\n"
        "s_cmp_lg_u32 s65, 1\n"
        "s_cbranch_scc1 .Lr5_l0w_go_%=\n"
        "s_endpgm\n"
        ".Lr5_l0w_go_%=:\n"
        "s_branch .Lr5_l0w_top_%=\n"
        ".Lr5_l0w_ok_%=:\n"
        "buffer_load_dwordx4 v[32:35], v3, s[40:43], s76 offen offset:0 sc1\n"
        "buffer_load_dwordx4 v[40:43], v3, s[40:43], s76 offen offset:512 sc1\n"
        "buffer_load_dwordx4 v[36:39], v3, s[40:43], s76 offen offset:256 sc1\n"
        "buffer_load_dwordx4 v[44:47], v3, s[40:43], s76 offen offset:768 sc1\n"
        "buffer_load_dwordx4 v[80:83], v0, s[40:43], s79 offen sc1\n"
        "buffer_load_dwordx4 v[48:51], v3, s[40:43], s77 offen offset:0 sc1\n"
        "buffer_load_dwordx4 v[56:59], v3, s[40:43], s77 offen offset:512 sc1\n"
        "buffer_load_dwordx4 v[52:55], v3, s[40:43], s77 offen offset:256 sc1\n"
        "buffer_load_dwordx4 v[60:63], v3, s[40:43], s77 offen offset:768 sc1\n"
        "buffer_load_dwordx4 v[84:87], v0, s[40:43], s80 offen sc1\n"
        "buffer_load_dwordx4 v[64:67], v4, s[40:43], s78 offen offset:0 sc1\n"
        "buffer_load_dwordx4 v[72:75], v4, s[40:43], s78 offen offset:512 sc1\n"
        "buffer_load_dwordx4 v[68:71], v4, s[40:43], s78 offen offset:256 sc1\n"
        "buffer_load_dwordx4 v[76:79], v4, s[40:43], s78 offen offset:768 sc1\n"
        "buffer_load_dwordx4 v[88:91], v0, s[40:43], s81 offen sc1\n"
        "s_xor_b32 s71, s71, 8192\n"
        "s_add_u32 s72, s71, 1835008\n"
        "s_add_u32 s71, s71, 1916928\n"
        "s_add_u32 s82, s72, s58\n"
        "s_add_u32 s85, s71, s58\n"
        "s_add_u32 s83, s72, s59\n"
        "s_add_u32 s86, s71, s59\n"
        "s_add_u32 s84, s72, s60\n"
        "s_add_u32 s87, s71, s60\n"
        "s_waitcnt vmcnt(14)\n"
        "v_mfma_f32_16x16x32_bf16 v[92:95], v[8:11], v[32:35], 0\n"
        "s_waitcnt vmcnt(13)\n"
        "v_mfma_f32_16x16x32_bf16 v[92:95], v[12:15], v[40:43], v[92:95]\n"
        "s_waitcnt vmcnt(12)\n"
        "v_mfma_f32_16x16x32_bf16 v[96:99], v[8:11], v[36:39], 0\n"
        "s_waitcnt vmcnt(11)\n"
        "v_mfma_f32_16x16x32_bf16 v[96:99], v[12:15], v[44:47], v[96:99]\n"
        "s_waitcnt vmcnt(10)\n"
        "v_mfma_f32_16x16x32_bf16 v[92:95], v[16:19], v[80:83], v[92:95]\n"
        "v_mfma_f32_16x16x32_bf16 v[96:99], v[20:23], v[80:83], v[96:99]\n"
        "s_waitcnt vmcnt(9)\n"
        "v_mfma_f32_16x16x32_bf16 v[100:103], v[8:11], v[48:51], 0\n"
        "s_waitcnt vmcnt(8)\n"
        "v_mfma_f32_16x16x32_bf16 v[100:103], v[12:15], v[56:59], v[100:103]\n"
        "s_waitcnt vmcnt(7)\n"
        "v_mfma_f32_16x16x32_bf16 v[104:107], v[8:11], v[52:55], 0\n"
        "s_waitcnt vmcnt(6)\n"
        "v_mfma_f32_16x16x32_bf16 v[104:107], v[12:15], v[60:63], v[104:107]\n"
        "s_waitcnt vmcnt(5)\n"
        "v_mfma_f32_16x16x32_bf16 v[100:103], v[16:19], v[84:87], v[100:103]\n"
        "v_mfma_f32_16x16x32_bf16 v[104:107], v[20:23], v[84:87], v[104:107]\n"
        "s_waitcnt vmcnt(4)\n"
        "v_mfma_f32_16x16x32_bf16 v[108:111], v[8:11], v[64:67], 0\n"
        "s_waitcnt vmcnt(3)\n"
        "v_mfma_f32_16x16x32_bf16 v[108:111], v[12:15], v[72:75], v[108:111]\n"
        "s_waitcnt vmcnt(2)\n"
        "v_mfma_f32_16x16x32_bf16 v[112:115], v[8:11], v[68:71], 0\n"
        "s_waitcnt vmcnt(1)\n"
        "v_mfma_f32_16x16x32_bf16 v[112:115], v[12:15], v[76:79], v[112:115]\n"
        "s_waitcnt vmcnt(0)\n"
        "v_mfma_f32_16x16x32_bf16 v[108:111], v[16:19], v[88:91], v[108:111]\n"
        "v_mfma_f32_16x16x32_bf16 v[112:115], v[20:23], v[88:91], v[112:115]\n"
        "s_add_u32 s68, s68, 1\n"
        "s_sub_u32 s68, s68, 1\n"
        "v_pk_add_f32 v[164:165], v[92:93], v[94:95]\n"
        "v_pk_add_f32 v[166:167], v[96:97], v[98:99]\n"
        "v_pk_add_f32 v[168:169], v[100:101], v[102:103]\n"
        "v_pk_add_f32 v[170:171], v[104:105], v[106:107]\n"
        "v_pk_add_f32 v[164:165], v[164:165], v[166:167]\n"
        "v_pk_add_f32 v[168:169], v[168:169], v[170:171]\n"
        "v_pk_add_f32 v[164:165], v[164:165], v[168:169]\n"
        "s_nop 0\n"
        "v_pk_add_f32 v[166:167], v[108:109], v[110:111]\n"
        "v_pk_add_f32 v[168:169], v[112:113], v[114:115]\n"
        "v_pk_add_f32 v[164:165], v[164:165], v[166:167]\n"
        "v_pk_add_f32 v[164:165], v[164:165], v[168:169]\n"
        "v_add_f32 v164, v164, v165\n"
        "v_cvt_pk_bf16_f32 v140, v92, v93\n"
        "v_cvt_pk_bf16_f32 v141, v94, v95\n"
        "v_add_f32_dpp v166, v164, v164 row_ror:8 row_mask:0xf bank_mask:0xf\n"
        "v_add_f32_dpp v167, v164, v164 row_ror:8 row_mask:0xf bank_mask:0xf\n"
        "v_cvt_pk_bf16_f32 v142, v96, v97\n"
        "v_cvt_pk_bf16_f32 v143, v98, v99\n"
        "v_permlane16_swap_b32 v166, v167\n"
        "v_add_f32 v164, v166, v167\n"
        "v_add_f32 v166, v166, v167\n"
        "v_cvt_pk_bf16_f32 v144, v100, v101\n"
        "v_cvt_pk_bf16_f32 v145, v102, v103\n"
        "v_permlane32_swap_b32 v164, v166\n"
        "v_add_f32 v164, v164, v166\n"
        "v_mul_f32 v164, 0x3baaaaab, v164\n"
        "v_pk_add_f32 v[116:117], v[92:93], v[164:165] op_sel_hi:[1,0] neg_lo:[0,1] neg_hi:[0,1]\n"
        "v_pk_add_f32 v[118:119], v[94:95], v[164:165] op_sel_hi:[1,0] neg_lo:[0,1] neg_hi:[0,1]\n"
        "v_pk_add_f32 v[120:121], v[96:97], v[164:165] op_sel_hi:[1,0] neg_lo:[0,1] neg_hi:[0,1]\n"
        "v_pk_add_f32 v[122:123], v[98:99], v[164:165] op_sel_hi:[1,0] neg_lo:[0,1] neg_hi:[0,1]\n"
        "v_pk_add_f32 v[124:125], v[100:101], v[164:165] op_sel_hi:[1,0] neg_lo:[0,1] neg_hi:[0,1]\n"
        "v_pk_add_f32 v[126:127], v[102:103], v[164:165] op_sel_hi:[1,0] neg_lo:[0,1] neg_hi:[0,1]\n"
        "v_pk_add_f32 v[128:129], v[104:105], v[164:165] op_sel_hi:[1,0] neg_lo:[0,1] neg_hi:[0,1]\n"
        "v_pk_add_f32 v[130:131], v[106:107], v[164:165] op_sel_hi:[1,0] neg_lo:[0,1] neg_hi:[0,1]\n"
        "v_pk_add_f32 v[132:133], v[108:109], v[164:165] op_sel_hi:[1,0] neg_lo:[0,1] neg_hi:[0,1]\n"
        "v_pk_add_f32 v[134:135], v[110:111], v[164:165] op_sel_hi:[1,0] neg_lo:[0,1] neg_hi:[0,1]\n"
        "v_pk_add_f32 v[136:137], v[112:113], v[164:165] op_sel_hi:[1,0] neg_lo:[0,1] neg_hi:[0,1]\n"
        "v_pk_add_f32 v[138:139], v[114:115], v[164:165] op_sel_hi:[1,0] neg_lo:[0,1] neg_hi:[0,1]\n"
        "v_pk_mul_f32 v[166:167], v[116:117], v[116:117]\n"
        "v_pk_mul_f32 v[168:169], v[118:119], v[118:119]\n"
        "v_pk_fma_f32 v[166:167], v[120:121], v[120:121], v[166:167]\n"
        "v_pk_fma_f32 v[168:169], v[122:123], v[122:123], v[168:169]\n"
        "v_pk_fma_f32 v[166:167], v[124:125], v[124:125], v[166:167]\n"
        "v_pk_fma_f32 v[168:169], v[126:127], v[126:127], v[168:169]\n"
        "v_pk_fma_f32 v[166:167], v[128:129], v[128:129], v[166:167]\n"
        "v_pk_fma_f32 v[168:169], v[130:131], v[130:131], v[168:169]\n"
        "v_pk_fma_f32 v[166:167], v[132:133], v[132:133], v[166:167]\n"
        "v_pk_fma_f32 v[168:169], v[134:135], v[134:135], v[168:169]\n"
        "v_pk_fma_f32 v[166:167], v[136:137], v[136:137], v[166:167]\n"
        "v_pk_fma_f32 v[168:169], v[138:139], v[138:139], v[168:169]\n"
        "v_pk_add_f32 v[166:167], v[166:167], v[168:169]\n"
        "v_add_f32 v165, v166, v167\n"
        "v_cvt_pk_bf16_f32 v146, v104, v105\n"
        "v_cvt_pk_bf16_f32 v147, v106, v107\n"
        "v_add_f32_dpp v166, v165, v165 row_ror:8 row_mask:0xf bank_mask:0xf\n"
        "v_add_f32_dpp v167, v165, v165 row_ror:8 row_mask:0xf bank_mask:0xf\n"
        "v_cvt_pk_bf16_f32 v148, v108, v109\n"
        "v_cvt_pk_bf16_f32 v149, v110, v111\n"
        "v_permlane16_swap_b32 v166, v167\n"
        "v_add_f32 v165, v166, v167\n"
        "v_add_f32 v166, v166, v167\n"
        "v_cvt_pk_bf16_f32 v150, v112, v113\n"
        "v_cvt_pk_bf16_f32 v151, v114, v115\n"
        "v_permlane32_swap_b32 v165, v166\n"
        "v_add_f32 v165, v165, v166\n"
        "ds_write_b64 v6, v[164:165]\n"
        "v_permlane16_swap_b32 v140, v142\n"
        "v_permlane16_swap_b32 v141, v143\n"
        "v_permlane16_swap_b32 v144, v146\n"
        "v_permlane16_swap_b32 v145, v147\n"
        "s_waitcnt lgkmcnt(0)\n"
        "s_barrier\n"
        "ds_read_b64 v[166:167], v7 offset:0\n"
        "ds_read_b64 v[168:169], v7 offset:64\n"
        "ds_read_b64 v[170:171], v7 offset:128\n"
        "v_permlane16_swap_b32 v148, v150\n"
        "v_permlane16_swap_b32 v149, v151\n"
        "buffer_store_dwordx4 v[140:143], v0, s[40:43], s85 offen sc1\n"
        "buffer_store_dwordx4 v[144:147], v0, s[40:43], s86 offen sc1\n"
        "buffer_store_dwordx4 v[148:151], v0, s[40:43], s87 offen sc1\n"
        "s_waitcnt lgkmcnt(0)\n"
        "v_add_f32 v172, v166, v168\n"
        "v_add_f32 v173, v167, v169\n"
        "v_add_f32 v172, v172, v170\n"
        "v_add_f32 v173, v173, v171\n"
        "v_mul_f32 v172, 0x3eaaaaab, v172\n"
        "v_sub_f32 v166, v166, v172\n"
        "v_sub_f32 v168, v168, v172\n"
        "v_sub_f32 v170, v170, v172\n"
        "v_mul_f32 v166, v166, v166\n"
        "v_fmac_f32 v166, v168, v168\n"
        "v_fmac_f32 v166, v170, v170\n"
        "v_fmamk_f32 v173, v166, 0x43400000, v173\n"
        "v_mul_f32 v173, 0x3ae38e39, v173\n"
        "v_cvt_pk_bf16_f32 v172, v172, v173\n"
        "v_and_b32 v173, 0xffff0000, v172\n"
        "v_lshlrev_b32 v172, 16, v172\n"
        "v_add_f32 v173, 0x3727c5ac, v173\n"
        "v_cvt_pk_bf16_f32 v173, v173, v173\n"
        "v_lshlrev_b32 v173, 16, v173\n"
        "v_sqrt_f32 v173, v173\n"
        "v_sub_f32 v172, v164, v172\n"
        "v_cvt_pk_bf16_f32 v173, v173, v173\n"
        "v_lshlrev_b32 v173, 16, v173\n"
        "v_rcp_f32 v173, v173\n"
        "v_mov_b32 v25, s68\n"
        "v_mul_f32 v172, v172, v173\n"
        "v_pk_fma_f32 v[116:117], v[116:117], v[172:173], v[172:173] op_sel:[0,1,0] op_sel_hi:[1,1,0]\n"
        "v_pk_fma_f32 v[118:119], v[118:119], v[172:173], v[172:173] op_sel:[0,1,0] op_sel_hi:[1,1,0]\n"
        "v_pk_fma_f32 v[120:121], v[120:121], v[172:173], v[172:173] op_sel:[0,1,0] op_sel_hi:[1,1,0]\n"
        "v_pk_fma_f32 v[122:123], v[122:123], v[172:173], v[172:173] op_sel:[0,1,0] op_sel_hi:[1,1,0]\n"
        "v_cvt_pk_bf16_f32 v152, v116, v117\n"
        "v_cvt_pk_bf16_f32 v153, v118, v119\n"
        "v_cvt_pk_bf16_f32 v154, v120, v121\n"
        "v_cvt_pk_bf16_f32 v155, v122, v123\n"
        "s_nop 0\n"
        "v_permlane16_swap_b32 v152, v154\n"
        "v_permlane16_swap_b32 v153, v155\n"
        "buffer_store_dwordx4 v[152:155], v0, s[40:43], s82 offen sc1\n"
        "v_pk_fma_f32 v[124:125], v[124:125], v[172:173], v[172:173] op_sel:[0,1,0] op_sel_hi:[1,1,0]\n"
        "v_pk_fma_f32 v[126:127], v[126:127], v[172:173], v[172:173] op_sel:[0,1,0] op_sel_hi:[1,1,0]\n"
        "v_pk_fma_f32 v[128:129], v[128:129], v[172:173], v[172:173] op_sel:[0,1,0] op_sel_hi:[1,1,0]\n"
        "v_pk_fma_f32 v[130:131], v[130:131], v[172:173], v[172:173] op_sel:[0,1,0] op_sel_hi:[1,1,0]\n"
        "v_cvt_pk_bf16_f32 v156, v124, v125\n"
        "v_cvt_pk_bf16_f32 v157, v126, v127\n"
        "v_cvt_pk_bf16_f32 v158, v128, v129\n"
        "v_cvt_pk_bf16_f32 v159, v130, v131\n"
        "s_nop 0\n"
        "v_permlane16_swap_b32 v156, v158\n"
        "v_permlane16_swap_b32 v157, v159\n"
        "buffer_store_dwordx4 v[156:159], v0, s[40:43], s83 offen sc1\n"
        "v_pk_fma_f32 v[132:133], v[132:133], v[172:173], v[172:173] op_sel:[0,1,0] op_sel_hi:[1,1,0]\n"
        "v_pk_fma_f32 v[134:135], v[134:135], v[172:173], v[172:173] op_sel:[0,1,0] op_sel_hi:[1,1,0]\n"
        "v_pk_fma_f32 v[136:137], v[136:137], v[172:173], v[172:173] op_sel:[0,1,0] op_sel_hi:[1,1,0]\n"
        "v_pk_fma_f32 v[138:139], v[138:139], v[172:173], v[172:173] op_sel:[0,1,0] op_sel_hi:[1,1,0]\n"
        "v_cvt_pk_bf16_f32 v160, v132, v133\n"
        "v_cvt_pk_bf16_f32 v161, v134, v135\n"
        "v_cvt_pk_bf16_f32 v162, v136, v137\n"
        "v_cvt_pk_bf16_f32 v163, v138, v139\n"
        "s_nop 0\n"
        "v_permlane16_swap_b32 v160, v162\n"
        "v_permlane16_swap_b32 v161, v163\n"
        "buffer_store_dwordx4 v[160:163], v0, s[40:43], s84 offen sc1\n"
        "s_waitcnt vmcnt(0)\n"
        "s_barrier\n"
        "buffer_store_dword v25, v2, s[40:43], s70 offen sc1\n"
        "s_add_u32 s68, s68, 1\n"
        "s_cmp_lt_u32 s68, 64\n"
        "s_cbranch_scc1 .Lr5_l0_%=\n"
        "s_endpgm\n"
        ".Lr5_l1_%=:\n"
        "s_bitcmp1_b64 s[52:53], s68\n"
        "s_cselect_b32 s65, 8192, 0\n"
        "s_lshl_b32 s71, s68, 21\n"
        "s_add_u32 s71, s71, s65\n"
        "s_add_u32 s71, s71, 2097152\n"
        "s_xor_b32 s71, s71, 8192\n"
        "s_add_u32 s72, s71, 1835008\n"
        "s_add_u32 s71, s71, 1916928\n"
        "s_add_u32 s82, s72, s58\n"
        "s_add_u32 s85, s71, s58\n"
        "s_add_u32 s83, s72, s59\n"
        "s_add_u32 s86, s71, s59\n"
        "s_add_u32 s84, s72, s60\n"
        "s_add_u32 s87, s71, s60\n"
        "s_waitcnt vmcnt(0)\n"
        "v_mfma_f32_16x16x32_bf16 v[92:95], v[16:19], v[80:83], 0\n"
        "v_mfma_f32_16x16x32_bf16 v[96:99], v[20:23], v[80:83], 0\n"
        "v_mfma_f32_16x16x32_bf16 v[100:103], v[16:19], v[84:87], 0\n"
        "v_mfma_f32_16x16x32_bf16 v[104:107], v[20:23], v[84:87], 0\n"
        "v_mfma_f32_16x16x32_bf16 v[108:111], v[16:19], v[88:91], 0\n"
        "v_mfma_f32_16x16x32_bf16 v[112:115], v[20:23], v[88:91], 0\n"
        "s_add_u32 s68, s68, 1\n"
        "s_mul_i32 s65, s68, 36864\n"
        "buffer_load_dwordx4 v[80:83], v5, s[44:47], s65 offen offset:0\n"
        "buffer_load_dwordx4 v[84:87], v5, s[44:47], s65 offen offset:128\n"
        "buffer_load_dwordx4 v[88:91], v5, s[44:47], s65 offen offset:256\n"
        "s_sub_u32 s68, s68, 1\n"
        "v_pk_add_f32 v[164:165], v[92:93], v[94:95]\n"
        "v_pk_add_f32 v[166:167], v[96:97], v[98:99]\n"
        "v_pk_add_f32 v[168:169], v[100:101], v[102:103]\n"
        "v_pk_add_f32 v[170:171], v[104:105], v[106:107]\n"
        "v_pk_add_f32 v[164:165], v[164:165], v[166:167]\n"
        "v_pk_add_f32 v[168:169], v[168:169], v[170:171]\n"
        "v_pk_add_f32 v[164:165], v[164:165], v[168:169]\n"
        "v_pk_add_f32 v[166:167], v[108:109], v[110:111]\n"
        "v_pk_add_f32 v[168:169], v[112:113], v[114:115]\n"
        "v_pk_add_f32 v[164:165], v[164:165], v[166:167]\n"
        "v_pk_add_f32 v[164:165], v[164:165], v[168:169]\n"
        "v_add_f32 v164, v164, v165\n"
        "s_nop 1\n"
        "v_add_f32_dpp v166, v164, v164 row_ror:8 row_mask:0xf bank_mask:0xf\n"
        "v_add_f32_dpp v167, v164, v164 row_ror:8 row_mask:0xf bank_mask:0xf\n"
        "s_nop 1\n"
        "v_permlane16_swap_b32 v166, v167\n"
        "v_add_f32 v164, v166, v167\n"
        "v_add_f32 v166, v166, v167\n"
        "s_nop 1\n"
        "v_permlane32_swap_b32 v164, v166\n"
        "v_add_f32 v164, v164, v166\n"
        "v_mul_f32 v164, 0x3baaaaab, v164\n"
        "v_pk_add_f32 v[116:117], v[92:93], v[164:165] op_sel_hi:[1,0] neg_lo:[0,1] neg_hi:[0,1]\n"
        "v_pk_add_f32 v[118:119], v[94:95], v[164:165] op_sel_hi:[1,0] neg_lo:[0,1] neg_hi:[0,1]\n"
        "v_pk_add_f32 v[120:121], v[96:97], v[164:165] op_sel_hi:[1,0] neg_lo:[0,1] neg_hi:[0,1]\n"
        "v_pk_add_f32 v[122:123], v[98:99], v[164:165] op_sel_hi:[1,0] neg_lo:[0,1] neg_hi:[0,1]\n"
        "v_pk_add_f32 v[124:125], v[100:101], v[164:165] op_sel_hi:[1,0] neg_lo:[0,1] neg_hi:[0,1]\n"
        "v_pk_add_f32 v[126:127], v[102:103], v[164:165] op_sel_hi:[1,0] neg_lo:[0,1] neg_hi:[0,1]\n"
        "v_pk_add_f32 v[128:129], v[104:105], v[164:165] op_sel_hi:[1,0] neg_lo:[0,1] neg_hi:[0,1]\n"
        "v_pk_add_f32 v[130:131], v[106:107], v[164:165] op_sel_hi:[1,0] neg_lo:[0,1] neg_hi:[0,1]\n"
        "v_pk_add_f32 v[132:133], v[108:109], v[164:165] op_sel_hi:[1,0] neg_lo:[0,1] neg_hi:[0,1]\n"
        "v_pk_add_f32 v[134:135], v[110:111], v[164:165] op_sel_hi:[1,0] neg_lo:[0,1] neg_hi:[0,1]\n"
        "v_pk_add_f32 v[136:137], v[112:113], v[164:165] op_sel_hi:[1,0] neg_lo:[0,1] neg_hi:[0,1]\n"
        "v_pk_add_f32 v[138:139], v[114:115], v[164:165] op_sel_hi:[1,0] neg_lo:[0,1] neg_hi:[0,1]\n"
        "v_pk_mul_f32 v[166:167], v[116:117], v[116:117]\n"
        "v_pk_mul_f32 v[168:169], v[118:119], v[118:119]\n"
        "v_pk_fma_f32 v[166:167], v[120:121], v[120:121], v[166:167]\n"
        "v_pk_fma_f32 v[168:169], v[122:123], v[122:123], v[168:169]\n"
        "v_pk_fma_f32 v[166:167], v[124:125], v[124:125], v[166:167]\n"
        "v_pk_fma_f32 v[168:169], v[126:127], v[126:127], v[168:169]\n"
        "v_pk_fma_f32 v[166:167], v[128:129], v[128:129], v[166:167]\n"
        "v_pk_fma_f32 v[168:169], v[130:131], v[130:131], v[168:169]\n"
        "v_pk_fma_f32 v[166:167], v[132:133], v[132:133], v[166:167]\n"
        "v_pk_fma_f32 v[168:169], v[134:135], v[134:135], v[168:169]\n"
        "v_pk_fma_f32 v[166:167], v[136:137], v[136:137], v[166:167]\n"
        "v_pk_fma_f32 v[168:169], v[138:139], v[138:139], v[168:169]\n"
        "v_pk_add_f32 v[166:167], v[166:167], v[168:169]\n"
        "v_add_f32 v165, v166, v167\n"
        "s_nop 1\n"
        "v_add_f32_dpp v166, v165, v165 row_ror:8 row_mask:0xf bank_mask:0xf\n"
        "v_add_f32_dpp v167, v165, v165 row_ror:8 row_mask:0xf bank_mask:0xf\n"
        "s_nop 1\n"
        "v_permlane16_swap_b32 v166, v167\n"
        "v_add_f32 v165, v166, v167\n"
        "v_add_f32 v166, v166, v167\n"
        "s_nop 1\n"
        "v_permlane32_swap_b32 v165, v166\n"
        "v_add_f32 v165, v165, v166\n"
        "ds_write_b64 v6, v[164:165]\n"
        "s_waitcnt lgkmcnt(0)\n"
        "s_barrier\n"
        "ds_read_b64 v[166:167], v7 offset:0\n"
        "ds_read_b64 v[168:169], v7 offset:64\n"
        "ds_read_b64 v[170:171], v7 offset:128\n"
        "s_waitcnt lgkmcnt(0)\n"
        "v_add_f32 v172, v166, v168\n"
        "v_add_f32 v173, v167, v169\n"
        "v_add_f32 v172, v172, v170\n"
        "v_add_f32 v173, v173, v171\n"
        "v_mul_f32 v172, 0x3eaaaaab, v172\n"
        "v_sub_f32 v166, v166, v172\n"
        "v_sub_f32 v168, v168, v172\n"
        "v_sub_f32 v170, v170, v172\n"
        "v_mul_f32 v166, v166, v166\n"
        "v_fmac_f32 v166, v168, v168\n"
        "v_fmac_f32 v166, v170, v170\n"
        "v_fmamk_f32 v173, v166, 0x43400000, v173\n"
        "v_mul_f32 v173, 0x3ae38e39, v173\n"
        "v_cvt_pk_bf16_f32 v172, v172, v173\n"
        "v_and_b32 v173, 0xffff0000, v172\n"
        "v_lshlrev_b32 v172, 16, v172\n"
        "v_add_f32 v173, 0x3727c5ac, v173\n"
        "v_cvt_pk_bf16_f32 v173, v173, v173\n"
        "v_lshlrev_b32 v173, 16, v173\n"
        "v_sqrt_f32 v173, v173\n"
        "v_sub_f32 v172, v164, v172\n"
        "v_cvt_pk_bf16_f32 v173, v173, v173\n"
        "v_lshlrev_b32 v173, 16, v173\n"
        "v_rcp_f32 v173, v173\n"
        "v_mov_b32 v25, s68\n"
        "v_mul_f32 v172, v172, v173\n"
        "v_pk_fma_f32 v[116:117], v[116:117], v[172:173], v[172:173] op_sel:[0,1,0] op_sel_hi:[1,1,0]\n"
        "v_pk_fma_f32 v[118:119], v[118:119], v[172:173], v[172:173] op_sel:[0,1,0] op_sel_hi:[1,1,0]\n"
        "v_pk_fma_f32 v[120:121], v[120:121], v[172:173], v[172:173] op_sel:[0,1,0] op_sel_hi:[1,1,0]\n"
        "v_pk_fma_f32 v[122:123], v[122:123], v[172:173], v[172:173] op_sel:[0,1,0] op_sel_hi:[1,1,0]\n"
        "v_cvt_pk_bf16_f32 v152, v116, v117\n"
        "v_cvt_pk_bf16_f32 v153, v118, v119\n"
        "v_cvt_pk_bf16_f32 v154, v120, v121\n"
        "v_cvt_pk_bf16_f32 v155, v122, v123\n"
        "s_nop 0\n"
        "v_permlane16_swap_b32 v152, v154\n"
        "v_permlane16_swap_b32 v153, v155\n"
        "buffer_store_dwordx4 v[152:155], v0, s[40:43], s82 offen sc1\n"
        "v_pk_fma_f32 v[124:125], v[124:125], v[172:173], v[172:173] op_sel:[0,1,0] op_sel_hi:[1,1,0]\n"
        "v_pk_fma_f32 v[126:127], v[126:127], v[172:173], v[172:173] op_sel:[0,1,0] op_sel_hi:[1,1,0]\n"
        "v_pk_fma_f32 v[128:129], v[128:129], v[172:173], v[172:173] op_sel:[0,1,0] op_sel_hi:[1,1,0]\n"
        "v_pk_fma_f32 v[130:131], v[130:131], v[172:173], v[172:173] op_sel:[0,1,0] op_sel_hi:[1,1,0]\n"
        "v_cvt_pk_bf16_f32 v156, v124, v125\n"
        "v_cvt_pk_bf16_f32 v157, v126, v127\n"
        "v_cvt_pk_bf16_f32 v158, v128, v129\n"
        "v_cvt_pk_bf16_f32 v159, v130, v131\n"
        "s_nop 0\n"
        "v_permlane16_swap_b32 v156, v158\n"
        "v_permlane16_swap_b32 v157, v159\n"
        "buffer_store_dwordx4 v[156:159], v0, s[40:43], s83 offen sc1\n"
        "v_pk_fma_f32 v[132:133], v[132:133], v[172:173], v[172:173] op_sel:[0,1,0] op_sel_hi:[1,1,0]\n"
        "v_pk_fma_f32 v[134:135], v[134:135], v[172:173], v[172:173] op_sel:[0,1,0] op_sel_hi:[1,1,0]\n"
        "v_pk_fma_f32 v[136:137], v[136:137], v[172:173], v[172:173] op_sel:[0,1,0] op_sel_hi:[1,1,0]\n"
        "v_pk_fma_f32 v[138:139], v[138:139], v[172:173], v[172:173] op_sel:[0,1,0] op_sel_hi:[1,1,0]\n"
        "v_cvt_pk_bf16_f32 v160, v132, v133\n"
        "v_cvt_pk_bf16_f32 v161, v134, v135\n"
        "v_cvt_pk_bf16_f32 v162, v136, v137\n"
        "v_cvt_pk_bf16_f32 v163, v138, v139\n"
        "s_nop 0\n"
        "v_permlane16_swap_b32 v160, v162\n"
        "v_permlane16_swap_b32 v161, v163\n"
        "buffer_store_dwordx4 v[160:163], v0, s[40:43], s84 offen sc1\n"
        "s_waitcnt vmcnt(0)\n"
        "s_barrier\n"
        "buffer_store_dword v25, v2, s[40:43], s70 offen sc1\n"
        "s_add_u32 s68, s68, 1\n"
        "s_cmp_lt_u32 s68, 64\n"
        "s_cbranch_scc1 .Lr5_l1_%=\n"
        "s_mov_b32 s68, 0\n"
        "s_mov_b64 s[44:45], %2\n"
        "s_and_b32 s45, s45, 0xffff\n"
        ".Lr5_fin_%=:\n"
        "s_bitcmp1_b64 s[52:53], s68\n"
        "s_cselect_b32 s65, 8192, 0\n"
        "s_lshl_b32 s71, s68, 21\n"
        "s_add_u32 s71, s71, s65\n"
        "s_add_u32 s71, s71, 2097152\n"
        "s_lshr_b32 s65, s68, 2\n"
        "s_add_u32 s65, s65, 1\n"
        "s_bitcmp1_b32 s55, s65\n"
        "s_cselect_b32 s65, 8192, 0\n"
        "s_lshl_b32 s72, s68, 19\n"
        "s_add_u32 s72, s72, s65\n"
        "s_add_u32 s72, s72, 203423744\n"
        "s_add_u32 s76, s72, s61\n"
        "s_add_u32 s79, s71, s58\n"
        "s_add_u32 s79, s79, 1998848\n"
        "s_add_u32 s77, s72, s62\n"
        "s_add_u32 s80, s71, s59\n"
        "s_add_u32 s80, s80, 1998848\n"
        "s_add_u32 s78, s72, s63\n"
        "s_add_u32 s81, s71, s60\n"
        "s_add_u32 s81, s81, 1998848\n"
        "s_mov_b32 s64, 0\n"
        ".Lr5_finw_top_%=:\n"
        "buffer_load_dword v24, v1, s[40:43], s69 offen sc1\n"
        "s_waitcnt vmcnt(0)\n"
        "v_cmp_gt_i32 vcc, s68, v24\n"
        "s_nop 0\n"
        "s_and_b64 vcc, vcc, 0xffff\n"
        "s_cbranch_vccz .Lr5_finw_ok_%=\n"
        "s_add_u32 s64, s64, 1\n"
        "s_and_b32 s65, s64, 255\n"
        "s_cbranch_scc1 .Lr5_finw_go_%=\n"
        "s_mov_b32 s65, 40960\n"
        "s_cmp_lt_u32 s64, 1048576\n"
        "s_cbranch_scc1 .Lr5_finw_chk_%=\n"
        "v_mov_b32 v24, 1\n"
        "buffer_store_dword v24, off, s[40:43], s65 sc1\n"
        "s_waitcnt vmcnt(0)\n"
        ".Lr5_finw_chk_%=:\n"
        "buffer_load_dword v24, off, s[40:43], s65 sc1\n"
        "s_waitcnt vmcnt(0)\n"
        "v_readfirstlane_b32 s65, v24\n"
        "s_nop 0\n"
        "s_cmp_lg_u32 s65, 1\n"
        "s_cbranch_scc1 .Lr5_finw_go_%=\n"
        "s_endpgm\n"
        ".Lr5_finw_go_%=:\n"
        "s_branch .Lr5_finw_top_%=\n"
        ".Lr5_finw_ok_%=:\n"
        "buffer_load_dwordx4 v[32:35], v3, s[40:43], s76 offen offset:0 sc1\n"
        "buffer_load_dwordx4 v[40:43], v3, s[40:43], s76 offen offset:512 sc1\n"
        "buffer_load_dwordx4 v[36:39], v3, s[40:43], s76 offen offset:256 sc1\n"
        "buffer_load_dwordx4 v[44:47], v3, s[40:43], s76 offen offset:768 sc1\n"
        "buffer_load_dwordx4 v[80:83], v0, s[40:43], s79 offen sc1\n"
        "buffer_load_dwordx4 v[48:51], v3, s[40:43], s77 offen offset:0 sc1\n"
        "buffer_load_dwordx4 v[56:59], v3, s[40:43], s77 offen offset:512 sc1\n"
        "buffer_load_dwordx4 v[52:55], v3, s[40:43], s77 offen offset:256 sc1\n"
        "buffer_load_dwordx4 v[60:63], v3, s[40:43], s77 offen offset:768 sc1\n"
        "buffer_load_dwordx4 v[84:87], v0, s[40:43], s80 offen sc1\n"
        "buffer_load_dwordx4 v[64:67], v4, s[40:43], s78 offen offset:0 sc1\n"
        "buffer_load_dwordx4 v[72:75], v4, s[40:43], s78 offen offset:512 sc1\n"
        "buffer_load_dwordx4 v[68:71], v4, s[40:43], s78 offen offset:256 sc1\n"
        "buffer_load_dwordx4 v[76:79], v4, s[40:43], s78 offen offset:768 sc1\n"
        "buffer_load_dwordx4 v[88:91], v0, s[40:43], s81 offen sc1\n"
        "s_waitcnt vmcnt(14)\n"
        "v_mfma_f32_16x16x32_bf16 v[92:95], v[8:11], v[32:35], 0\n"
        "s_waitcnt vmcnt(13)\n"
        "v_mfma_f32_16x16x32_bf16 v[92:95], v[12:15], v[40:43], v[92:95]\n"
        "s_waitcnt vmcnt(12)\n"
        "v_mfma_f32_16x16x32_bf16 v[96:99], v[8:11], v[36:39], 0\n"
        "s_waitcnt vmcnt(11)\n"
        "v_mfma_f32_16x16x32_bf16 v[96:99], v[12:15], v[44:47], v[96:99]\n"
        "s_waitcnt vmcnt(10)\n"
        "v_mfma_f32_16x16x32_bf16 v[92:95], v[16:19], v[80:83], v[92:95]\n"
        "v_mfma_f32_16x16x32_bf16 v[96:99], v[20:23], v[80:83], v[96:99]\n"
        "s_waitcnt vmcnt(9)\n"
        "v_mfma_f32_16x16x32_bf16 v[100:103], v[8:11], v[48:51], 0\n"
        "s_waitcnt vmcnt(8)\n"
        "v_mfma_f32_16x16x32_bf16 v[100:103], v[12:15], v[56:59], v[100:103]\n"
        "s_waitcnt vmcnt(7)\n"
        "v_mfma_f32_16x16x32_bf16 v[104:107], v[8:11], v[52:55], 0\n"
        "s_waitcnt vmcnt(6)\n"
        "v_mfma_f32_16x16x32_bf16 v[104:107], v[12:15], v[60:63], v[104:107]\n"
        "s_waitcnt vmcnt(5)\n"
        "v_mfma_f32_16x16x32_bf16 v[100:103], v[16:19], v[84:87], v[100:103]\n"
        "v_mfma_f32_16x16x32_bf16 v[104:107], v[20:23], v[84:87], v[104:107]\n"
        "s_waitcnt vmcnt(4)\n"
        "v_mfma_f32_16x16x32_bf16 v[108:111], v[8:11], v[64:67], 0\n"
        "s_waitcnt vmcnt(3)\n"
        "v_mfma_f32_16x16x32_bf16 v[108:111], v[12:15], v[72:75], v[108:111]\n"
        "s_waitcnt vmcnt(2)\n"
        "v_mfma_f32_16x16x32_bf16 v[112:115], v[8:11], v[68:71], 0\n"
        "s_waitcnt vmcnt(1)\n"
        "v_mfma_f32_16x16x32_bf16 v[112:115], v[12:15], v[76:79], v[112:115]\n"
        "s_waitcnt vmcnt(0)\n"
        "v_mfma_f32_16x16x32_bf16 v[108:111], v[16:19], v[88:91], v[108:111]\n"
        "v_mfma_f32_16x16x32_bf16 v[112:115], v[20:23], v[88:91], v[112:115]\n"
        "s_mul_i32 s65, s68, 36864\n"
        "v_cvt_pk_bf16_f32 v140, v92, v93\n"
        "v_cvt_pk_bf16_f32 v141, v94, v95\n"
        "v_cvt_pk_bf16_f32 v142, v96, v97\n"
        "v_cvt_pk_bf16_f32 v143, v98, v99\n"
        "s_nop 0\n"
        "v_permlane16_swap_b32 v140, v142\n"
        "v_permlane16_swap_b32 v141, v143\n"
        "buffer_store_dwordx4 v[140:143], v5, s[44:47], s65 offen offset:0\n"
        "v_cvt_pk_bf16_f32 v144, v100, v101\n"
        "v_cvt_pk_bf16_f32 v145, v102, v103\n"
        "v_cvt_pk_bf16_f32 v146, v104, v105\n"
        "v_cvt_pk_bf16_f32 v147, v106, v107\n"
        "s_nop 0\n"
        "v_permlane16_swap_b32 v144, v146\n"
        "v_permlane16_swap_b32 v145, v147\n"
        "buffer_store_dwordx4 v[144:147], v5, s[44:47], s65 offen offset:128\n"
        "s_nop 0\n"
        "v_cvt_pk_bf16_f32 v148, v108, v109\n"
        "v_cvt_pk_bf16_f32 v149, v110, v111\n"
        "v_cvt_pk_bf16_f32 v150, v112, v113\n"
        "v_cvt_pk_bf16_f32 v151, v114, v115\n"
        "s_nop 0\n"
        "v_permlane16_swap_b32 v148, v150\n"
        "v_permlane16_swap_b32 v149, v151\n"
        "buffer_store_dwordx4 v[148:151], v5, s[44:47], s65 offen offset:256\n"
        "s_add_u32 s68, s68, 1\n"
        "s_cmp_lt_u32 s68, 64\n"
        "s_cbranch_scc1 .Lr5_fin_%=\n"
        "s_endpgm\n"
            RED_ARGS : "v0", "v1", "v2", "v3", "v4", "v5", "v6", "v7", "v8", "v9", "v10", "v11", "v12", "v13", "v14", "v15", "v16", "v17", "v18", "v19", "v20", "v21", "v22", "v23", "v24", "v25", "v26", "v27", "v28", "v29", "v30", "v31", "v32", "v33", "v34", "v35", "v36", "v37", "v38", "v39", "v40", "v41", "v42", "v43", "v44", "v45", "v46", "v47", "v48", "v49", "v50", "v51", "v52", "v53", "v54", "v55", "v56", "v57", "v58", "v59", "v60", "v61", "v62", "v63", "v64", "v65", "v66", "v67", "v68", "v69", "v70", "v71", "v72", "v73", "v74", "v75", "v76", "v77", "v78", "v79", "v255", "a255", "s34", "s35", "s36", "s37", "s38", "s39", "s40", "s41", "s42", "s43", "s44", "s45", "s46", "s47", "s48", "s49", "s50", "s51", "s52", "s53", "s54", "s55", "s56", "s57", "s58", "s59", "s60", "s61", "s62", "s63", "s64", "s65", "s66", "s67", "s68", "s69", "s70", "s71", "s72", "s73", "s74", "s75", "s76", "s77", "s78", "s79", "s80", "s81", "s82", "s83", "s84", "s85", "s86", "s87", "s88", "s89", "s90", "s91", "s92", "s93", "s94", "s95", "s96", "s97", "s98", "s99", "vcc", "scc", "memory");
    } else {
        asm volatile(
        "s_nop 4\n"
        "s_mov_b64 s[40:41], %0\n"
        "s_and_b32 s41, s41, 0xffff\n"
        "s_mov_b32 s42, 236978176\n"
        "s_mov_b32 s43, 0x20000\n"
        "s_mov_b64 s[44:45], %1\n"
        "s_and_b32 s45, s45, 0xffff\n"
        "s_mov_b32 s46, 2359296\n"
        "s_mov_b32 s47, 0x20000\n"
        "s_mov_b64 s[56:57], %3\n"
        "s_mov_b64 s[58:59], %4\n"
        "s_mov_b64 s[60:61], %5\n"
        "s_mov_b64 s[62:63], %6\n"
        "v_mov_b64 v[0:1], %7\n"
        "v_mov_b64 v[2:3], %8\n"
        "v_mov_b64 v[4:5], %9\n"
        "v_mov_b64 v[6:7], %10\n"
        "v_mov_b64 v[8:9], %11\n"
        "v_mov_b64 v[10:11], %12\n"
        "v_mov_b64 v[12:13], %13\n"
        "v_mov_b64 v[14:15], %14\n"
        "v_mov_b64 v[16:17], %15\n"
        "v_mov_b64 v[18:19], %16\n"
        "v_mov_b64 v[20:21], %17\n"
        "v_mov_b64 v[22:23], %18\n"
        "s_mov_b32 s68, 0\n"
        "s_mul_i32 s65, s68, 36864\n"
        "buffer_load_dwordx4 v[80:83], v5, s[44:47], s65 offen offset:0\n"
        "buffer_load_dwordx4 v[84:87], v5, s[44:47], s65 offen offset:128\n"
        "buffer_load_dwordx4 v[88:91], v5, s[44:47], s65 offen offset:256\n"
        "v_lshrrev_b32 v26, 4, v0\n"
        "s_lshl_b32 s66, s57, 7\n"
        "s_mov_b32 s64, 0\n"
        ".Lr3_pair_top_%=:\n"
        "s_mov_b64 exec, 1\n"
        "buffer_load_dword v27, off, s[40:43], s66 sc1\n"
        "s_mov_b64 exec, -1\n"
        "s_waitcnt vmcnt(0)\n"
        "v_cmp_gt_i32 vcc, 0, v27\n"
        "s_nop 0\n"
        "s_and_b64 vcc, vcc, 1\n"
        "s_cbranch_vccz .Lr3_pair_ok_%=\n"
        "s_add_u32 s64, s64, 1\n"
        "s_and_b32 s65, s64, 255\n"
        "s_cbranch_scc1 .Lr3_pair_go_%=\n"
        "s_mov_b32 s65, 40960\n"
        "s_cmp_lt_u32 s64, 1048576\n"
        "s_cbranch_scc1 .Lr3_pair_chk_%=\n"
        "v_mov_b32 v27, 1\n"
        "buffer_store_dword v27, off, s[40:43], s65 sc1\n"
        "s_waitcnt vmcnt(0)\n"
        ".Lr3_pair_chk_%=:\n"
        "buffer_load_dword v27, off, s[40:43], s65 sc1\n"
        "s_waitcnt vmcnt(0)\n"
        "v_readfirstlane_b32 s65, v27\n"
        "s_nop 0\n"
        "s_cmp_lg_u32 s65, 1\n"
        "s_cbranch_scc1 .Lr3_pair_go_%=\n"
        "s_endpgm\n"
        ".Lr3_pair_go_%=:\n"
        "s_sleep 2\n"
        "s_branch .Lr3_pair_top_%=\n"
        ".Lr3_pair_ok_%=:\n"
        "v_readfirstlane_b32 s50, v27\n"
        "s_nop 0\n"
        "s_xor_b32 s50, s50, s56\n"
        "v_lshlrev_b32 v28, 7, v26\n"
        "s_sub_u32 s66, 0, s50\n"
        "s_mov_b32 s52, 24576\n"
        "s_mov_b32 s64, 0\n"
        ".Lr3_pg0_top_%=:\n"
        "buffer_load_dword v27, v28, s[40:43], s52 offen sc1\n"
        "s_waitcnt vmcnt(0)\n"
        "v_cmp_gt_i32 vcc, 0, v27\n"
        "s_cbranch_vccz .Lr3_pg0_ok_%=\n"
        "s_add_u32 s64, s64, 1\n"
        "s_and_b32 s65, s64, 255\n"
        "s_cbranch_scc1 .Lr3_pg0_go_%=\n"
        "s_mov_b32 s65, 40960\n"
        "s_cmp_lt_u32 s64, 1048576\n"
        "s_cbranch_scc1 .Lr3_pg0_chk_%=\n"
        "v_mov_b32 v27, 1\n"
        "buffer_store_dword v27, off, s[40:43], s65 sc1\n"
        "s_waitcnt vmcnt(0)\n"
        ".Lr3_pg0_chk_%=:\n"
        "buffer_load_dword v27, off, s[40:43], s65 sc1\n"
        "s_waitcnt vmcnt(0)\n"
        "v_readfirstlane_b32 s65, v27\n"
        "s_nop 0\n"
        "s_cmp_lg_u32 s65, 1\n"
        "s_cbranch_scc1 .Lr3_pg0_go_%=\n"
        "s_endpgm\n"
        ".Lr3_pg0_go_%=:\n"
        "s_sleep 2\n"
        "s_branch .Lr3_pg0_top_%=\n"
        ".Lr3_pg0_ok_%=:\n"
        "v_cmp_eq_u32 s[52:53], 1, v27\n"
        "s_nop 1\n"
        "s_mov_b32 s54, 32768\n"
        "s_mov_b32 s64, 0\n"
        ".Lr3_pg1_top_%=:\n"
        "buffer_load_dword v27, v28, s[40:43], s54 offen sc1\n"
        "s_waitcnt vmcnt(0)\n"
        "v_cmp_gt_i32 vcc, 0, v27\n"
        "s_cbranch_vccz .Lr3_pg1_ok_%=\n"
        "s_add_u32 s64, s64, 1\n"
        "s_and_b32 s65, s64, 255\n"
        "s_cbranch_scc1 .Lr3_pg1_go_%=\n"
        "s_mov_b32 s65, 40960\n"
        "s_cmp_lt_u32 s64, 1048576\n"
        "s_cbranch_scc1 .Lr3_pg1_chk_%=\n"
        "v_mov_b32 v27, 1\n"
        "buffer_store_dword v27, off, s[40:43], s65 sc1\n"
        "s_waitcnt vmcnt(0)\n"
        ".Lr3_pg1_chk_%=:\n"
        "buffer_load_dword v27, off, s[40:43], s65 sc1\n"
        "s_waitcnt vmcnt(0)\n"
        "v_readfirstlane_b32 s65, v27\n"
        "s_nop 0\n"
        "s_cmp_lg_u32 s65, 1\n"
        "s_cbranch_scc1 .Lr3_pg1_go_%=\n"
        "s_endpgm\n"
        ".Lr3_pg1_go_%=:\n"
        "s_sleep 2\n"
        "s_branch .Lr3_pg1_top_%=\n"
        ".Lr3_pg1_ok_%=:\n"
        "v_cmp_eq_u32 s[54:55], 1, v27\n"
        "s_nop 1\n"
        "s_mov_b32 vcc_lo, 0x55555555\n"
        "s_mov_b32 vcc_hi, 0x55555555\n"
        "s_xor_b64 vcc, vcc, s[52:53]\n"
        "s_bcnt1_i32_b64 s65, vcc\n"
        "s_sub_u32 s64, s65, 23\n"
        "s_cmp_lt_u32 s64, 19\n"
        "s_cbranch_scc1 .Lr3_rawmap_%=\n"
        "s_cmp_lt_u32 s65, 32\n"
        "s_cselect_b32 s65, 0, -1\n"
        "s_xor_b32 s52, s65, 0x55555555\n"
        "s_xor_b32 s53, s65, 0x55555555\n"
        "s_xor_b32 s54, s65, 0x55555555\n"
        "s_xor_b32 s55, s65, 0xaaaaaaaa\n"
        ".Lr3_rawmap_%=:\n"
        "s_xor_b32 s52, s52, s66\n"
        "s_xor_b32 s53, s53, s66\n"
        "s_xor_b32 s54, s54, s66\n"
        "s_xor_b32 s55, s55, s66\n"
        "s_and_b32 s69, s55, 1\n"
        "s_lshl_b32 s69, s69, 13\n"
        "s_xor_b32 s70, s69, 0\n"
        "s_cmp_eq_u32 s50, 0\n"
        "s_cbranch_scc0 .Lr3_l1_%=\n"
        ".Lr3_l0_%=:\n"
        "s_bitcmp1_b64 s[52:53], s68\n"
        "s_cselect_b32 s65, 8192, 0\n"
        "s_lshl_b32 s71, s68, 21\n"
        "s_add_u32 s71, s71, s65\n"
        "s_add_u32 s71, s71, 2097152\n"
        "s_lshr_b32 s65, s68, 1\n"
        "s_bitcmp1_b32 s54, s65\n"
        "s_cselect_b32 s65, 8192, 0\n"
        "s_lshl_b32 s72, s68, 20\n"
        "s_add_u32 s72, s72, s65\n"
        "s_add_u32 s72, s72, 136904704\n"
        "s_add_u32 s76, s72, s61\n"
        "s_add_u32 s79, s71, s58\n"
        "s_add_u32 s79, s79, 1916928\n"
        "s_add_u32 s77, s72, s62\n"
        "s_add_u32 s80, s71, s59\n"
        "s_add_u32 s80, s80, 1916928\n"
        "s_add_u32 s78, s72, s63\n"
        "s_add_u32 s81, s71, s60\n"
        "s_add_u32 s81, s81, 1916928\n"
        "s_mov_b32 s64, 0\n"
        ".Lr3_l0w_top_%=:\n"
        "buffer_load_dword v24, v1, s[40:43], s69 offen sc1\n"
        "s_waitcnt vmcnt(0)\n"
        "v_cmp_gt_i32 vcc, s68, v24\n"
        "s_nop 0\n"
        "s_and_b64 vcc, vcc, 0xffff\n"
        "s_cbranch_vccz .Lr3_l0w_ok_%=\n"
        "s_add_u32 s64, s64, 1\n"
        "s_and_b32 s65, s64, 255\n"
        "s_cbranch_scc1 .Lr3_l0w_go_%=\n"
        "s_mov_b32 s65, 40960\n"
        "s_cmp_lt_u32 s64, 1048576\n"
        "s_cbranch_scc1 .Lr3_l0w_chk_%=\n"
        "v_mov_b32 v24, 1\n"
        "buffer_store_dword v24, off, s[40:43], s65 sc1\n"
        "s_waitcnt vmcnt(0)\n"
        ".Lr3_l0w_chk_%=:\n"
        "buffer_load_dword v24, off, s[40:43], s65 sc1\n"
        "s_waitcnt vmcnt(0)\n"
        "v_readfirstlane_b32 s65, v24\n"
        "s_nop 0\n"
        "s_cmp_lg_u32 s65, 1\n"
        "s_cbranch_scc1 .Lr3_l0w_go_%=\n"
        "s_endpgm\n"
        ".Lr3_l0w_go_%=:\n"
        "s_branch .Lr3_l0w_top_%=\n"
        ".Lr3_l0w_ok_%=:\n"
        "buffer_load_dwordx4 v[32:35], v3, s[40:43], s76 offen offset:0 sc1\n"
        "buffer_load_dwordx4 v[40:43], v3, s[40:43], s76 offen offset:512 sc1\n"
        "buffer_load_dwordx4 v[36:39], v3, s[40:43], s76 offen offset:256 sc1\n"
        "buffer_load_dwordx4 v[44:47], v3, s[40:43], s76 offen offset:768 sc1\n"
        "buffer_load_dwordx4 v[48:51], v3, s[40:43], s77 offen offset:0 sc1\n"
        "buffer_load_dwordx4 v[56:59], v3, s[40:43], s77 offen offset:512 sc1\n"
        "buffer_load_dwordx4 v[52:55], v3, s[40:43], s77 offen offset:256 sc1\n"
        "buffer_load_dwordx4 v[60:63], v3, s[40:43], s77 offen offset:768 sc1\n"
        "buffer_load_dwordx4 v[64:67], v4, s[40:43], s78 offen offset:0 sc1\n"
        "buffer_load_dwordx4 v[72:75], v4, s[40:43], s78 offen offset:512 sc1\n"
        "buffer_load_dwordx4 v[68:71], v4, s[40:43], s78 offen offset:256 sc1\n"
        "buffer_load_dwordx4 v[76:79], v4, s[40:43], s78 offen offset:768 sc1\n"
        "s_add_u32 s72, s72, 294912\n"
        "s_add_u32 s71, s71, 1998848\n"
        "s_add_u32 s82, s72, s58\n"
        "s_add_u32 s85, s71, s58\n"
        "s_add_u32 s83, s72, s59\n"
        "s_add_u32 s86, s71, s59\n"
        "s_add_u32 s84, s72, s60\n"
        "s_add_u32 s87, s71, s60\n"
        "s_waitcnt vmcnt(11)\n"
        "v_mfma_f32_16x16x32_bf16 v[92:95], v[8:11], v[32:35], 0\n"
        "s_waitcnt vmcnt(10)\n"
        "v_mfma_f32_16x16x32_bf16 v[92:95], v[12:15], v[40:43], v[92:95]\n"
        "s_waitcnt vmcnt(9)\n"
        "v_mfma_f32_16x16x32_bf16 v[96:99], v[8:11], v[36:39], 0\n"
        "s_waitcnt vmcnt(8)\n"
        "v_mfma_f32_16x16x32_bf16 v[96:99], v[12:15], v[44:47], v[96:99]\n"
        "v_mfma_f32_16x16x32_bf16 v[92:95], v[16:19], v[80:83], v[92:95]\n"
        "v_mfma_f32_16x16x32_bf16 v[96:99], v[20:23], v[80:83], v[96:99]\n"
        "s_waitcnt vmcnt(7)\n"
        "v_mfma_f32_16x16x32_bf16 v[100:103], v[8:11], v[48:51], 0\n"
        "s_waitcnt vmcnt(6)\n"
        "v_mfma_f32_16x16x32_bf16 v[100:103], v[12:15], v[56:59], v[100:103]\n"
        "s_waitcnt vmcnt(5)\n"
        "v_mfma_f32_16x16x32_bf16 v[104:107], v[8:11], v[52:55], 0\n"
        "s_waitcnt vmcnt(4)\n"
        "v_mfma_f32_16x16x32_bf16 v[104:107], v[12:15], v[60:63], v[104:107]\n"
        "v_mfma_f32_16x16x32_bf16 v[100:103], v[16:19], v[84:87], v[100:103]\n"
        "v_mfma_f32_16x16x32_bf16 v[104:107], v[20:23], v[84:87], v[104:107]\n"
        "s_waitcnt vmcnt(3)\n"
        "v_mfma_f32_16x16x32_bf16 v[108:111], v[8:11], v[64:67], 0\n"
        "s_waitcnt vmcnt(2)\n"
        "v_mfma_f32_16x16x32_bf16 v[108:111], v[12:15], v[72:75], v[108:111]\n"
        "s_waitcnt vmcnt(1)\n"
        "v_mfma_f32_16x16x32_bf16 v[112:115], v[8:11], v[68:71], 0\n"
        "s_waitcnt vmcnt(0)\n"
        "v_mfma_f32_16x16x32_bf16 v[112:115], v[12:15], v[76:79], v[112:115]\n"
        "v_mfma_f32_16x16x32_bf16 v[108:111], v[16:19], v[88:91], v[108:111]\n"
        "v_mfma_f32_16x16x32_bf16 v[112:115], v[20:23], v[88:91], v[112:115]\n"
        "s_add_u32 s68, s68, 1\n"
        "s_mul_i32 s65, s68, 36864\n"
        "buffer_load_dwordx4 v[80:83], v5, s[44:47], s65 offen offset:0\n"
        "buffer_load_dwordx4 v[84:87], v5, s[44:47], s65 offen offset:128\n"
        "buffer_load_dwordx4 v[88:91], v5, s[44:47], s65 offen offset:256\n"
        "s_sub_u32 s68, s68, 1\n"
        "v_pk_add_f32 v[164:165], v[92:93], v[94:95]\n"
        "v_pk_add_f32 v[166:167], v[96:97], v[98:99]\n"
        "v_pk_add_f32 v[168:169], v[100:101], v[102:103]\n"
        "v_pk_add_f32 v[170:171], v[104:105], v[106:107]\n"
        "v_pk_add_f32 v[164:165], v[164:165], v[166:167]\n"
        "v_pk_add_f32 v[168:169], v[168:169], v[170:171]\n"
        "v_pk_add_f32 v[164:165], v[164:165], v[168:169]\n"
        "v_pk_add_f32 v[166:167], v[108:109], v[110:111]\n"
        "v_pk_add_f32 v[168:169], v[112:113], v[114:115]\n"
        "v_pk_add_f32 v[164:165], v[164:165], v[166:167]\n"
        "v_pk_add_f32 v[164:165], v[164:165], v[168:169]\n"
        "v_add_f32 v164, v164, v165\n"
        "v_cvt_pk_bf16_f32 v140, v92, v93\n"
        "v_cvt_pk_bf16_f32 v141, v94, v95\n"
        "v_add_f32_dpp v166, v164, v164 row_ror:8 row_mask:0xf bank_mask:0xf\n"
        "v_add_f32_dpp v167, v164, v164 row_ror:8 row_mask:0xf bank_mask:0xf\n"
        "v_cvt_pk_bf16_f32 v142, v96, v97\n"
        "v_cvt_pk_bf16_f32 v143, v98, v99\n"
        "v_permlane16_swap_b32 v166, v167\n"
        "v_add_f32 v164, v166, v167\n"
        "v_add_f32 v166, v166, v167\n"
        "v_cvt_pk_bf16_f32 v144, v100, v101\n"
        "v_cvt_pk_bf16_f32 v145, v102, v103\n"
        "v_permlane32_swap_b32 v164, v166\n"
        "v_add_f32 v164, v164, v166\n"
        "v_mul_f32 v164, 0x3baaaaab, v164\n"
        "v_pk_add_f32 v[116:117], v[92:93], v[164:165] op_sel_hi:[1,0] neg_lo:[0,1] neg_hi:[0,1]\n"
        "v_pk_add_f32 v[118:119], v[94:95], v[164:165] op_sel_hi:[1,0] neg_lo:[0,1] neg_hi:[0,1]\n"
        "v_pk_add_f32 v[120:121], v[96:97], v[164:165] op_sel_hi:[1,0] neg_lo:[0,1] neg_hi:[0,1]\n"
        "v_pk_add_f32 v[122:123], v[98:99], v[164:165] op_sel_hi:[1,0] neg_lo:[0,1] neg_hi:[0,1]\n"
        "v_pk_add_f32 v[124:125], v[100:101], v[164:165] op_sel_hi:[1,0] neg_lo:[0,1] neg_hi:[0,1]\n"
        "v_pk_add_f32 v[126:127], v[102:103], v[164:165] op_sel_hi:[1,0] neg_lo:[0,1] neg_hi:[0,1]\n"
        "v_pk_add_f32 v[128:129], v[104:105], v[164:165] op_sel_hi:[1,0] neg_lo:[0,1] neg_hi:[0,1]\n"
        "v_pk_add_f32 v[130:131], v[106:107], v[164:165] op_sel_hi:[1,0] neg_lo:[0,1] neg_hi:[0,1]\n"
        "v_pk_add_f32 v[132:133], v[108:109], v[164:165] op_sel_hi:[1,0] neg_lo:[0,1] neg_hi:[0,1]\n"
        "v_pk_add_f32 v[134:135], v[110:111], v[164:165] op_sel_hi:[1,0] neg_lo:[0,1] neg_hi:[0,1]\n"
        "v_pk_add_f32 v[136:137], v[112:113], v[164:165] op_sel_hi:[1,0] neg_lo:[0,1] neg_hi:[0,1]\n"
        "v_pk_add_f32 v[138:139], v[114:115], v[164:165] op_sel_hi:[1,0] neg_lo:[0,1] neg_hi:[0,1]\n"
        "v_pk_mul_f32 v[166:167], v[116:117], v[116:117]\n"
        "v_pk_mul_f32 v[168:169], v[118:119], v[118:119]\n"
        "v_pk_fma_f32 v[166:167], v[120:121], v[120:121], v[166:167]\n"
        "v_pk_fma_f32 v[168:169], v[122:123], v[122:123], v[168:169]\n"
        "v_pk_fma_f32 v[166:167], v[124:125], v[124:125], v[166:167]\n"
        "v_pk_fma_f32 v[168:169], v[126:127], v[126:127], v[168:169]\n"
        "v_pk_fma_f32 v[166:167], v[128:129], v[128:129], v[166:167]\n"
        "v_pk_fma_f32 v[168:169], v[130:131], v[130:131], v[168:169]\n"
        "v_pk_fma_f32 v[166:167], v[132:133], v[132:133], v[166:167]\n"
        "v_pk_fma_f32 v[168:169], v[134:135], v[134:135], v[168:169]\n"
        "v_pk_fma_f32 v[166:167], v[136:137], v[136:137], v[166:167]\n"
        "v_pk_fma_f32 v[168:169], v[138:139], v[138:139], v[168:169]\n"
        "v_pk_add_f32 v[166:167], v[166:167], v[168:169]\n"
        "v_add_f32 v165, v166, v167\n"
        "v_cvt_pk_bf16_f32 v146, v104, v105\n"
        "v_cvt_pk_bf16_f32 v147, v106, v107\n"
        "v_add_f32_dpp v166, v165, v165 row_ror:8 row_mask:0xf bank_mask:0xf\n"
        "v_add_f32_dpp v167, v165, v165 row_ror:8 row_mask:0xf bank_mask:0xf\n"
        "v_cvt_pk_bf16_f32 v148, v108, v109\n"
        "v_cvt_pk_bf16_f32 v149, v110, v111\n"
        "v_permlane16_swap_b32 v166, v167\n"
        "v_add_f32 v165, v166, v167\n"
        "v_add_f32 v166, v166, v167\n"
        "v_cvt_pk_bf16_f32 v150, v112, v113\n"
        "v_cvt_pk_bf16_f32 v151, v114, v115\n"
        "v_permlane32_swap_b32 v165, v166\n"
        "v_add_f32 v165, v165, v166\n"
        "ds_write_b64 v6, v[164:165]\n"
        "v_permlane16_swap_b32 v140, v142\n"
        "v_permlane16_swap_b32 v141, v143\n"
        "v_permlane16_swap_b32 v144, v146\n"
        "v_permlane16_swap_b32 v145, v147\n"
        "s_waitcnt lgkmcnt(0)\n"
        "s_barrier\n"
        "ds_read_b64 v[166:167], v7 offset:0\n"
        "ds_read_b64 v[168:169], v7 offset:64\n"
        "ds_read_b64 v[170:171], v7 offset:128\n"
        "v_permlane16_swap_b32 v148, v150\n"
        "v_permlane16_swap_b32 v149, v151\n"
        "buffer_store_dwordx4 v[140:143], v0, s[40:43], s85 offen sc1\n"
        "buffer_store_dwordx4 v[144:147], v0, s[40:43], s86 offen sc1\n"
        "buffer_store_dwordx4 v[148:151], v0, s[40:43], s87 offen sc1\n"
        "s_waitcnt lgkmcnt(0)\n"
        "v_add_f32 v172, v166, v168\n"
        "v_add_f32 v173, v167, v169\n"
        "v_add_f32 v172, v172, v170\n"
        "v_add_f32 v173, v173, v171\n"
        "v_mul_f32 v172, 0x3eaaaaab, v172\n"
        "v_sub_f32 v166, v166, v172\n"
        "v_sub_f32 v168, v168, v172\n"
        "v_sub_f32 v170, v170, v172\n"
        "v_mul_f32 v166, v166, v166\n"
        "v_fmac_f32 v166, v168, v168\n"
        "v_fmac_f32 v166, v170, v170\n"
        "v_fmamk_f32 v173, v166, 0x43400000, v173\n"
        "v_mul_f32 v173, 0x3ae38e39, v173\n"
        "v_cvt_pk_bf16_f32 v172, v172, v173\n"
        "v_and_b32 v173, 0xffff0000, v172\n"
        "v_lshlrev_b32 v172, 16, v172\n"
        "v_add_f32 v173, 0x3727c5ac, v173\n"
        "v_cvt_pk_bf16_f32 v173, v173, v173\n"
        "v_lshlrev_b32 v173, 16, v173\n"
        "v_sqrt_f32 v173, v173\n"
        "v_sub_f32 v172, v164, v172\n"
        "v_cvt_pk_bf16_f32 v173, v173, v173\n"
        "v_lshlrev_b32 v173, 16, v173\n"
        "v_rcp_f32 v173, v173\n"
        "v_mov_b32 v25, s68\n"
        "v_mul_f32 v172, v172, v173\n"
        "v_pk_fma_f32 v[116:117], v[116:117], v[172:173], v[172:173] op_sel:[0,1,0] op_sel_hi:[1,1,0]\n"
        "v_pk_fma_f32 v[118:119], v[118:119], v[172:173], v[172:173] op_sel:[0,1,0] op_sel_hi:[1,1,0]\n"
        "v_pk_fma_f32 v[120:121], v[120:121], v[172:173], v[172:173] op_sel:[0,1,0] op_sel_hi:[1,1,0]\n"
        "v_pk_fma_f32 v[122:123], v[122:123], v[172:173], v[172:173] op_sel:[0,1,0] op_sel_hi:[1,1,0]\n"
        "v_cvt_pk_bf16_f32 v152, v116, v117\n"
        "v_cvt_pk_bf16_f32 v153, v118, v119\n"
        "v_cvt_pk_bf16_f32 v154, v120, v121\n"
        "v_cvt_pk_bf16_f32 v155, v122, v123\n"
        "s_nop 0\n"
        "v_permlane16_swap_b32 v152, v154\n"
        "v_permlane16_swap_b32 v153, v155\n"
        "buffer_store_dwordx4 v[152:155], v0, s[40:43], s82 offen sc1\n"
        "v_pk_fma_f32 v[124:125], v[124:125], v[172:173], v[172:173] op_sel:[0,1,0] op_sel_hi:[1,1,0]\n"
        "v_pk_fma_f32 v[126:127], v[126:127], v[172:173], v[172:173] op_sel:[0,1,0] op_sel_hi:[1,1,0]\n"
        "v_pk_fma_f32 v[128:129], v[128:129], v[172:173], v[172:173] op_sel:[0,1,0] op_sel_hi:[1,1,0]\n"
        "v_pk_fma_f32 v[130:131], v[130:131], v[172:173], v[172:173] op_sel:[0,1,0] op_sel_hi:[1,1,0]\n"
        "v_cvt_pk_bf16_f32 v156, v124, v125\n"
        "v_cvt_pk_bf16_f32 v157, v126, v127\n"
        "v_cvt_pk_bf16_f32 v158, v128, v129\n"
        "v_cvt_pk_bf16_f32 v159, v130, v131\n"
        "s_nop 0\n"
        "v_permlane16_swap_b32 v156, v158\n"
        "v_permlane16_swap_b32 v157, v159\n"
        "buffer_store_dwordx4 v[156:159], v0, s[40:43], s83 offen sc1\n"
        "v_pk_fma_f32 v[132:133], v[132:133], v[172:173], v[172:173] op_sel:[0,1,0] op_sel_hi:[1,1,0]\n"
        "v_pk_fma_f32 v[134:135], v[134:135], v[172:173], v[172:173] op_sel:[0,1,0] op_sel_hi:[1,1,0]\n"
        "v_pk_fma_f32 v[136:137], v[136:137], v[172:173], v[172:173] op_sel:[0,1,0] op_sel_hi:[1,1,0]\n"
        "v_pk_fma_f32 v[138:139], v[138:139], v[172:173], v[172:173] op_sel:[0,1,0] op_sel_hi:[1,1,0]\n"
        "v_cvt_pk_bf16_f32 v160, v132, v133\n"
        "v_cvt_pk_bf16_f32 v161, v134, v135\n"
        "v_cvt_pk_bf16_f32 v162, v136, v137\n"
        "v_cvt_pk_bf16_f32 v163, v138, v139\n"
        "s_nop 0\n"
        "v_permlane16_swap_b32 v160, v162\n"
        "v_permlane16_swap_b32 v161, v163\n"
        "buffer_store_dwordx4 v[160:163], v0, s[40:43], s84 offen sc1\n"
        "s_waitcnt vmcnt(0)\n"
        "s_barrier\n"
        "buffer_store_dword v25, v2, s[40:43], s70 offen sc1\n"
        "s_add_u32 s68, s68, 1\n"
        "s_cmp_lt_u32 s68, 64\n"
        "s_cbranch_scc1 .Lr3_l0_%=\n"
        "s_endpgm\n"
        ".Lr3_l1_%=:\n"
        "s_bitcmp1_b64 s[52:53], s68\n"
        "s_cselect_b32 s65, 8192, 0\n"
        "s_lshl_b32 s71, s68, 21\n"
        "s_add_u32 s71, s71, s65\n"
        "s_add_u32 s71, s71, 2097152\n"
        "s_lshr_b32 s65, s68, 1\n"
        "s_bitcmp1_b32 s54, s65\n"
        "s_cselect_b32 s65, 8192, 0\n"
        "s_lshl_b32 s72, s68, 20\n"
        "s_add_u32 s72, s72, s65\n"
        "s_add_u32 s72, s72, 136904704\n"
        "s_add_u32 s76, s72, s61\n"
        "s_add_u32 s79, s71, s58\n"
        "s_add_u32 s79, s79, 1916928\n"
        "s_add_u32 s77, s72, s62\n"
        "s_add_u32 s80, s71, s59\n"
        "s_add_u32 s80, s80, 1916928\n"
        "s_add_u32 s78, s72, s63\n"
        "s_add_u32 s81, s71, s60\n"
        "s_add_u32 s81, s81, 1916928\n"
        "s_mov_b32 s64, 0\n"
        ".Lr3_l1w_top_%=:\n"
        "buffer_load_dword v24, v1, s[40:43], s69 offen sc1\n"
        "s_waitcnt vmcnt(0)\n"
        "v_cmp_gt_i32 vcc, s68, v24\n"
        "s_nop 0\n"
        "s_and_b64 vcc, vcc, 0xffff\n"
        "s_cbranch_vccz .Lr3_l1w_ok_%=\n"
        "s_add_u32 s64, s64, 1\n"
        "s_and_b32 s65, s64, 255\n"
        "s_cbranch_scc1 .Lr3_l1w_go_%=\n"
        "s_mov_b32 s65, 40960\n"
        "s_cmp_lt_u32 s64, 1048576\n"
        "s_cbranch_scc1 .Lr3_l1w_chk_%=\n"
        "v_mov_b32 v24, 1\n"
        "buffer_store_dword v24, off, s[40:43], s65 sc1\n"
        "s_waitcnt vmcnt(0)\n"
        ".Lr3_l1w_chk_%=:\n"
        "buffer_load_dword v24, off, s[40:43], s65 sc1\n"
        "s_waitcnt vmcnt(0)\n"
        "v_readfirstlane_b32 s65, v24\n"
        "s_nop 0\n"
        "s_cmp_lg_u32 s65, 1\n"
        "s_cbranch_scc1 .Lr3_l1w_go_%=\n"
        "s_endpgm\n"
        ".Lr3_l1w_go_%=:\n"
        "s_branch .Lr3_l1w_top_%=\n"
        ".Lr3_l1w_ok_%=:\n"
        "buffer_load_dwordx4 v[32:35], v3, s[40:43], s76 offen offset:0 sc1\n"
        "buffer_load_dwordx4 v[40:43], v3, s[40:43], s76 offen offset:512 sc1\n"
        "buffer_load_dwordx4 v[36:39], v3, s[40:43], s76 offen offset:256 sc1\n"
        "buffer_load_dwordx4 v[44:47], v3, s[40:43], s76 offen offset:768 sc1\n"
        "buffer_load_dwordx4 v[80:83], v0, s[40:43], s79 offen sc1\n"
        "buffer_load_dwordx4 v[48:51], v3, s[40:43], s77 offen offset:0 sc1\n"
        "buffer_load_dwordx4 v[56:59], v3, s[40:43], s77 offen offset:512 sc1\n"
        "buffer_load_dwordx4 v[52:55], v3, s[40:43], s77 offen offset:256 sc1\n"
        "buffer_load_dwordx4 v[60:63], v3, s[40:43], s77 offen offset:768 sc1\n"
        "buffer_load_dwordx4 v[84:87], v0, s[40:43], s80 offen sc1\n"
        "buffer_load_dwordx4 v[64:67], v4, s[40:43], s78 offen offset:0 sc1\n"
        "buffer_load_dwordx4 v[72:75], v4, s[40:43], s78 offen offset:512 sc1\n"
        "buffer_load_dwordx4 v[68:71], v4, s[40:43], s78 offen offset:256 sc1\n"
        "buffer_load_dwordx4 v[76:79], v4, s[40:43], s78 offen offset:768 sc1\n"
        "buffer_load_dwordx4 v[88:91], v0, s[40:43], s81 offen sc1\n"
        "s_add_u32 s72, s72, 294912\n"
        "s_add_u32 s71, s71, 1998848\n"
        "s_add_u32 s82, s72, s58\n"
        "s_add_u32 s85, s71, s58\n"
        "s_add_u32 s83, s72, s59\n"
        "s_add_u32 s86, s71, s59\n"
        "s_add_u32 s84, s72, s60\n"
        "s_add_u32 s87, s71, s60\n"
        "s_waitcnt vmcnt(14)\n"
        "v_mfma_f32_16x16x32_bf16 v[92:95], v[8:11], v[32:35], 0\n"
        "s_waitcnt vmcnt(13)\n"
        "v_mfma_f32_16x16x32_bf16 v[92:95], v[12:15], v[40:43], v[92:95]\n"
        "s_waitcnt vmcnt(12)\n"
        "v_mfma_f32_16x16x32_bf16 v[96:99], v[8:11], v[36:39], 0\n"
        "s_waitcnt vmcnt(11)\n"
        "v_mfma_f32_16x16x32_bf16 v[96:99], v[12:15], v[44:47], v[96:99]\n"
        "s_waitcnt vmcnt(10)\n"
        "v_mfma_f32_16x16x32_bf16 v[92:95], v[16:19], v[80:83], v[92:95]\n"
        "v_mfma_f32_16x16x32_bf16 v[96:99], v[20:23], v[80:83], v[96:99]\n"
        "s_waitcnt vmcnt(9)\n"
        "v_mfma_f32_16x16x32_bf16 v[100:103], v[8:11], v[48:51], 0\n"
        "s_waitcnt vmcnt(8)\n"
        "v_mfma_f32_16x16x32_bf16 v[100:103], v[12:15], v[56:59], v[100:103]\n"
        "s_waitcnt vmcnt(7)\n"
        "v_mfma_f32_16x16x32_bf16 v[104:107], v[8:11], v[52:55], 0\n"
        "s_waitcnt vmcnt(6)\n"
        "v_mfma_f32_16x16x32_bf16 v[104:107], v[12:15], v[60:63], v[104:107]\n"
        "s_waitcnt vmcnt(5)\n"
        "v_mfma_f32_16x16x32_bf16 v[100:103], v[16:19], v[84:87], v[100:103]\n"
        "v_mfma_f32_16x16x32_bf16 v[104:107], v[20:23], v[84:87], v[104:107]\n"
        "s_waitcnt vmcnt(4)\n"
        "v_mfma_f32_16x16x32_bf16 v[108:111], v[8:11], v[64:67], 0\n"
        "s_waitcnt vmcnt(3)\n"
        "v_mfma_f32_16x16x32_bf16 v[108:111], v[12:15], v[72:75], v[108:111]\n"
        "s_waitcnt vmcnt(2)\n"
        "v_mfma_f32_16x16x32_bf16 v[112:115], v[8:11], v[68:71], 0\n"
        "s_waitcnt vmcnt(1)\n"
        "v_mfma_f32_16x16x32_bf16 v[112:115], v[12:15], v[76:79], v[112:115]\n"
        "s_waitcnt vmcnt(0)\n"
        "v_mfma_f32_16x16x32_bf16 v[108:111], v[16:19], v[88:91], v[108:111]\n"
        "v_mfma_f32_16x16x32_bf16 v[112:115], v[20:23], v[88:91], v[112:115]\n"
        "s_add_u32 s68, s68, 1\n"
        "s_sub_u32 s68, s68, 1\n"
        "v_pk_add_f32 v[164:165], v[92:93], v[94:95]\n"
        "v_pk_add_f32 v[166:167], v[96:97], v[98:99]\n"
        "v_pk_add_f32 v[168:169], v[100:101], v[102:103]\n"
        "v_pk_add_f32 v[170:171], v[104:105], v[106:107]\n"
        "v_pk_add_f32 v[164:165], v[164:165], v[166:167]\n"
        "v_pk_add_f32 v[168:169], v[168:169], v[170:171]\n"
        "v_pk_add_f32 v[164:165], v[164:165], v[168:169]\n"
        "s_nop 0\n"
        "v_pk_add_f32 v[166:167], v[108:109], v[110:111]\n"
        "v_pk_add_f32 v[168:169], v[112:113], v[114:115]\n"
        "v_pk_add_f32 v[164:165], v[164:165], v[166:167]\n"
        "v_pk_add_f32 v[164:165], v[164:165], v[168:169]\n"
        "v_add_f32 v164, v164, v165\n"
        "v_cvt_pk_bf16_f32 v140, v92, v93\n"
        "v_cvt_pk_bf16_f32 v141, v94, v95\n"
        "v_add_f32_dpp v166, v164, v164 row_ror:8 row_mask:0xf bank_mask:0xf\n"
        "v_add_f32_dpp v167, v164, v164 row_ror:8 row_mask:0xf bank_mask:0xf\n"
        "v_cvt_pk_bf16_f32 v142, v96, v97\n"
        "v_cvt_pk_bf16_f32 v143, v98, v99\n"
        "v_permlane16_swap_b32 v166, v167\n"
        "v_add_f32 v164, v166, v167\n"
        "v_add_f32 v166, v166, v167\n"
        "v_cvt_pk_bf16_f32 v144, v100, v101\n"
        "v_cvt_pk_bf16_f32 v145, v102, v103\n"
        "v_permlane32_swap_b32 v164, v166\n"
        "v_add_f32 v164, v164, v166\n"
        "v_mul_f32 v164, 0x3baaaaab, v164\n"
        "v_pk_add_f32 v[116:117], v[92:93], v[164:165] op_sel_hi:[1,0] neg_lo:[0,1] neg_hi:[0,1]\n"
        "v_pk_add_f32 v[118:119], v[94:95], v[164:165] op_sel_hi:[1,0] neg_lo:[0,1] neg_hi:[0,1]\n"
        "v_pk_add_f32 v[120:121], v[96:97], v[164:165] op_sel_hi:[1,0] neg_lo:[0,1] neg_hi:[0,1]\n"
        "v_pk_add_f32 v[122:123], v[98:99], v[164:165] op_sel_hi:[1,0] neg_lo:[0,1] neg_hi:[0,1]\n"
        "v_pk_add_f32 v[124:125], v[100:101], v[164:165] op_sel_hi:[1,0] neg_lo:[0,1] neg_hi:[0,1]\n"
        "v_pk_add_f32 v[126:127], v[102:103], v[164:165] op_sel_hi:[1,0] neg_lo:[0,1] neg_hi:[0,1]\n"
        "v_pk_add_f32 v[128:129], v[104:105], v[164:165] op_sel_hi:[1,0] neg_lo:[0,1] neg_hi:[0,1]\n"
        "v_pk_add_f32 v[130:131], v[106:107], v[164:165] op_sel_hi:[1,0] neg_lo:[0,1] neg_hi:[0,1]\n"
        "v_pk_add_f32 v[132:133], v[108:109], v[164:165] op_sel_hi:[1,0] neg_lo:[0,1] neg_hi:[0,1]\n"
        "v_pk_add_f32 v[134:135], v[110:111], v[164:165] op_sel_hi:[1,0] neg_lo:[0,1] neg_hi:[0,1]\n"
        "v_pk_add_f32 v[136:137], v[112:113], v[164:165] op_sel_hi:[1,0] neg_lo:[0,1] neg_hi:[0,1]\n"
        "v_pk_add_f32 v[138:139], v[114:115], v[164:165] op_sel_hi:[1,0] neg_lo:[0,1] neg_hi:[0,1]\n"
        "v_pk_mul_f32 v[166:167], v[116:117], v[116:117]\n"
        "v_pk_mul_f32 v[168:169], v[118:119], v[118:119]\n"
        "v_pk_fma_f32 v[166:167], v[120:121], v[120:121], v[166:167]\n"
        "v_pk_fma_f32 v[168:169], v[122:123], v[122:123], v[168:169]\n"
        "v_pk_fma_f32 v[166:167], v[124:125], v[124:125], v[166:167]\n"
        "v_pk_fma_f32 v[168:169], v[126:127], v[126:127], v[168:169]\n"
        "v_pk_fma_f32 v[166:167], v[128:129], v[128:129], v[166:167]\n"
        "v_pk_fma_f32 v[168:169], v[130:131], v[130:131], v[168:169]\n"
        "v_pk_fma_f32 v[166:167], v[132:133], v[132:133], v[166:167]\n"
        "v_pk_fma_f32 v[168:169], v[134:135], v[134:135], v[168:169]\n"
        "v_pk_fma_f32 v[166:167], v[136:137], v[136:137], v[166:167]\n"
        "v_pk_fma_f32 v[168:169], v[138:139], v[138:139], v[168:169]\n"
        "v_pk_add_f32 v[166:167], v[166:167], v[168:169]\n"
        "v_add_f32 v165, v166, v167\n"
        "v_cvt_pk_bf16_f32 v146, v104, v105\n"
        "v_cvt_pk_bf16_f32 v147, v106, v107\n"
        "v_add_f32_dpp v166, v165, v165 row_ror:8 row_mask:0xf bank_mask:0xf\n"
        "v_add_f32_dpp v167, v165, v165 row_ror:8 row_mask:0xf bank_mask:0xf\n"
        "v_cvt_pk_bf16_f32 v148, v108, v109\n"
        "v_cvt_pk_bf16_f32 v149, v110, v111\n"
        "v_permlane16_swap_b32 v166, v167\n"
        "v_add_f32 v165, v166, v167\n"
        "v_add_f32 v166, v166, v167\n"
        "v_cvt_pk_bf16_f32 v150, v112, v113\n"
        "v_cvt_pk_bf16_f32 v151, v114, v115\n"
        "v_permlane32_swap_b32 v165, v166\n"
        "v_add_f32 v165, v165, v166\n"
        "ds_write_b64 v6, v[164:165]\n"
        "v_permlane16_swap_b32 v140, v142\n"
        "v_permlane16_swap_b32 v141, v143\n"
        "v_permlane16_swap_b32 v144, v146\n"
        "v_permlane16_swap_b32 v145, v147\n"
        "s_waitcnt lgkmcnt(0)\n"
        "s_barrier\n"
        "ds_read_b64 v[166:167], v7 offset:0\n"
        "ds_read_b64 v[168:169], v7 offset:64\n"
        "ds_read_b64 v[170:171], v7 offset:128\n"
        "v_permlane16_swap_b32 v148, v150\n"
        "v_permlane16_swap_b32 v149, v151\n"
        "buffer_store_dwordx4 v[140:143], v0, s[40:43], s85 offen sc1\n"
        "buffer_store_dwordx4 v[144:147], v0, s[40:43], s86 offen sc1\n"
        "buffer_store_dwordx4 v[148:151], v0, s[40:43], s87 offen sc1\n"
        "s_waitcnt lgkmcnt(0)\n"
        "v_add_f32 v172, v166, v168\n"
        "v_add_f32 v173, v167, v169\n"
        "v_add_f32 v172, v172, v170\n"
        "v_add_f32 v173, v173, v171\n"
        "v_mul_f32 v172, 0x3eaaaaab, v172\n"
        "v_sub_f32 v166, v166, v172\n"
        "v_sub_f32 v168, v168, v172\n"
        "v_sub_f32 v170, v170, v172\n"
        "v_mul_f32 v166, v166, v166\n"
        "v_fmac_f32 v166, v168, v168\n"
        "v_fmac_f32 v166, v170, v170\n"
        "v_fmamk_f32 v173, v166, 0x43400000, v173\n"
        "v_mul_f32 v173, 0x3ae38e39, v173\n"
        "v_cvt_pk_bf16_f32 v172, v172, v173\n"
        "v_and_b32 v173, 0xffff0000, v172\n"
        "v_lshlrev_b32 v172, 16, v172\n"
        "v_add_f32 v173, 0x3727c5ac, v173\n"
        "v_cvt_pk_bf16_f32 v173, v173, v173\n"
        "v_lshlrev_b32 v173, 16, v173\n"
        "v_sqrt_f32 v173, v173\n"
        "v_sub_f32 v172, v164, v172\n"
        "v_cvt_pk_bf16_f32 v173, v173, v173\n"
        "v_lshlrev_b32 v173, 16, v173\n"
        "v_rcp_f32 v173, v173\n"
        "v_mov_b32 v25, s68\n"
        "v_mul_f32 v172, v172, v173\n"
        "v_pk_fma_f32 v[116:117], v[116:117], v[172:173], v[172:173] op_sel:[0,1,0] op_sel_hi:[1,1,0]\n"
        "v_pk_fma_f32 v[118:119], v[118:119], v[172:173], v[172:173] op_sel:[0,1,0] op_sel_hi:[1,1,0]\n"
        "v_pk_fma_f32 v[120:121], v[120:121], v[172:173], v[172:173] op_sel:[0,1,0] op_sel_hi:[1,1,0]\n"
        "v_pk_fma_f32 v[122:123], v[122:123], v[172:173], v[172:173] op_sel:[0,1,0] op_sel_hi:[1,1,0]\n"
        "v_cvt_pk_bf16_f32 v152, v116, v117\n"
        "v_cvt_pk_bf16_f32 v153, v118, v119\n"
        "v_cvt_pk_bf16_f32 v154, v120, v121\n"
        "v_cvt_pk_bf16_f32 v155, v122, v123\n"
        "s_nop 0\n"
        "v_permlane16_swap_b32 v152, v154\n"
        "v_permlane16_swap_b32 v153, v155\n"
        "buffer_store_dwordx4 v[152:155], v0, s[40:43], s82 offen sc1\n"
        "v_pk_fma_f32 v[124:125], v[124:125], v[172:173], v[172:173] op_sel:[0,1,0] op_sel_hi:[1,1,0]\n"
        "v_pk_fma_f32 v[126:127], v[126:127], v[172:173], v[172:173] op_sel:[0,1,0] op_sel_hi:[1,1,0]\n"
        "v_pk_fma_f32 v[128:129], v[128:129], v[172:173], v[172:173] op_sel:[0,1,0] op_sel_hi:[1,1,0]\n"
        "v_pk_fma_f32 v[130:131], v[130:131], v[172:173], v[172:173] op_sel:[0,1,0] op_sel_hi:[1,1,0]\n"
        "v_cvt_pk_bf16_f32 v156, v124, v125\n"
        "v_cvt_pk_bf16_f32 v157, v126, v127\n"
        "v_cvt_pk_bf16_f32 v158, v128, v129\n"
        "v_cvt_pk_bf16_f32 v159, v130, v131\n"
        "s_nop 0\n"
        "v_permlane16_swap_b32 v156, v158\n"
        "v_permlane16_swap_b32 v157, v159\n"
        "buffer_store_dwordx4 v[156:159], v0, s[40:43], s83 offen sc1\n"
        "v_pk_fma_f32 v[132:133], v[132:133], v[172:173], v[172:173] op_sel:[0,1,0] op_sel_hi:[1,1,0]\n"
        "v_pk_fma_f32 v[134:135], v[134:135], v[172:173], v[172:173] op_sel:[0,1,0] op_sel_hi:[1,1,0]\n"
        "v_pk_fma_f32 v[136:137], v[136:137], v[172:173], v[172:173] op_sel:[0,1,0] op_sel_hi:[1,1,0]\n"
        "v_pk_fma_f32 v[138:139], v[138:139], v[172:173], v[172:173] op_sel:[0,1,0] op_sel_hi:[1,1,0]\n"
        "v_cvt_pk_bf16_f32 v160, v132, v133\n"
        "v_cvt_pk_bf16_f32 v161, v134, v135\n"
        "v_cvt_pk_bf16_f32 v162, v136, v137\n"
        "v_cvt_pk_bf16_f32 v163, v138, v139\n"
        "s_nop 0\n"
        "v_permlane16_swap_b32 v160, v162\n"
        "v_permlane16_swap_b32 v161, v163\n"
        "buffer_store_dwordx4 v[160:163], v0, s[40:43], s84 offen sc1\n"
        "s_waitcnt vmcnt(0)\n"
        "s_barrier\n"
        "buffer_store_dword v25, v2, s[40:43], s70 offen sc1\n"
        "s_add_u32 s68, s68, 1\n"
        "s_cmp_lt_u32 s68, 64\n"
        "s_cbranch_scc1 .Lr3_l1_%=\n"
        "s_endpgm\n"
            RED_ARGS : "v0", "v1", "v2", "v3", "v4", "v5", "v6", "v7", "v8", "v9", "v10", "v11", "v12", "v13", "v14", "v15", "v16", "v17", "v18", "v19", "v20", "v21", "v22", "v23", "v24", "v25", "v26", "v27", "v28", "v29", "v30", "v31", "v32", "v33", "v34", "v35", "v36", "v37", "v38", "v39", "v40", "v41", "v42", "v43", "v44", "v45", "v46", "v47", "v48", "v49", "v50", "v51", "v52", "v53", "v54", "v55", "v56", "v57", "v58", "v59", "v60", "v61", "v62", "v63", "v64", "v65", "v66", "v67", "v68", "v69", "v70", "v71", "v72", "v73", "v74", "v75", "v76", "v77", "v78", "v79", "v255", "a255", "s34", "s35", "s36", "s37", "s38", "s39", "s40", "s41", "s42", "s43", "s44", "s45", "s46", "s47", "s48", "s49", "s50", "s51", "s52", "s53", "s54", "s55", "s56", "s57", "s58", "s59", "s60", "s61", "s62", "s63", "s64", "s65", "s66", "s67", "s68", "s69", "s70", "s71", "s72", "s73", "s74", "s75", "s76", "s77", "s78", "s79", "s80", "s81", "s82", "s83", "s84", "s85", "s86", "s87", "s88", "s89", "s90", "s91", "s92", "s93", "s94", "s95", "s96", "s97", "s98", "s99", "vcc", "scc", "memory");
    }
    __builtin_unreachable();
}

__global__ __launch_bounds__(256, 1) void mega_kernel(Params p) {
    extern __shared__ __attribute__((aligned(16))) char smem[];
    const rsrc_t rs = __builtin_amdgcn_make_buffer_rsrc(p.ws, 0, WS_USED, 0x00020000);
    const int x = blockIdx.x & 7, k = blockIdx.x >> 3, grp = x & 3, x4 = x >> 2, pr = k * 4 + grp;
    const int half = my_half();
    if (x4 == 0 && threadIdx.x == 0) st4(rs, (G_PAIR + pr) * 128, half);
    if (half == 1 && k < 28 && !(p.tune2 >> 60)) {
        const unsigned first = (pr * 4 + rfl(threadIdx.x >> 6)) * 1024;
        warm(p.x, 2359296, first); warm(p.Wq, 4718592, first); warm(p.Wk, 4718592, first); warm(p.Wv, 4718592, first); warm(p.Wo, 4718592, first);
        warm(p.w1, 5308416, first); warm(p.w3, 5308416, first); warm(p.w2, 5308416, first);
        DRAIN();
        BAR();
    }
    auto dl = [&](int r) -> int { return (int)(p.tune2 >> (6 * (r + 5 * half))) & 63; };
    if (k < 8) role_chain(p, grp * 8 + k, x4 | dl(0) << 8, pr, smem);
    else if (k < 16) role_s1(p, grp * 8 + k - 8, x4, pr, dl(1));
    else if (k < 22) role_f1(p, grp, k - 16, x4, pr, dl(3));
    else if (k < 25) role_k<true>(p, grp, k - 22, x4, pr, dl(2));
    else if (k < 28) role_k<false>(p, grp, k - 25, x4, pr, dl(4));
    else if (k >= 28) {
        startup(rs, ((k - 28) * 8 + x) * 4 + rfl(threadIdx.x >> 6));
        if (k < 30) role_attn(p, grp * 8 + (k - 28) * 4, x4, pr);
        else if (k == 30) role_red<false>(p, grp, x4, pr);
        else role_red<true>(p, grp, x4, pr);
    }
}

}

extern "C" void kernel_launch(void* const* d_in, const int* in_sizes, int n_in, void* d_out, int out_size,
                              void* d_ws, size_t ws_size, hipStream_t stream) {
    if (ws_size < (size_t)WS_USED + 2 * PAGE) return;
    Params p;
    p.x = (const u16*)d_in[0]; p.mem0 = (const u16*)d_in[1];
    p.Wq = (const u16*)d_in[2]; p.Wk = (const u16*)d_in[3]; p.Wv = (const u16*)d_in[4]; p.Wo = (const u16*)d_in[5];
    p.w1 = (const u16*)d_in[8]; p.w3 = (const u16*)d_in[9]; p.w2 = (const u16*)d_in[10];
    p.out = (u16*)d_out;
    p.ws = (char*)d_ws + ((0 - (size_t)d_ws) & (PAGE - 1));
    p.tune0 = (u64)D1 | (u64)D2 << 32; p.tune1 = (u64)D3 | (u64)D4 << 32;
    p.tune2 = (u64)DL0 | (u64)DL1 << 30;
    (void)hipMemsetAsync(p.ws, 0xFF, FLAG_BYTES, stream);
    (void)hipFuncSetAttribute((const void*)mega_kernel, hipFuncAttributeMaxDynamicSharedMemorySize, LDS_BYTES);
    mega_kernel<<<256, 256, LDS_BYTES, stream>>>(p);
}
```

```cpp
#include <hip/hip_runtime.h>
#include <stdint.h>

typedef unsigned short u16;
typedef unsigned long long u64;
typedef unsigned v4u __attribute__((ext_vector_type(4)));
typedef unsigned v2u __attribute__((ext_vector_type(2)));
typedef short bf16x8 __attribute__((ext_vector_type(8)));
typedef float f32x4 __attribute__((ext_vector_type(4)));
typedef __bf16 bf2_t __attribute__((ext_vector_type(2)));
typedef __amdgpu_buffer_rsrc_t rsrc_t;

#define XF2L 0
#define XF2S 0
#define XF2M 24
#define XFN 1
#define RSLEEP 1
namespace {

constexpr unsigned A_K = 262144u;
constexpr unsigned A_KM = 786432u;
constexpr unsigned A_PROBE = 1044480u;
constexpr unsigned A_Q = 0u;
constexpr unsigned A_ST = 1036288u;
constexpr unsigned A_V = 524288u;
constexpr unsigned A_X1 = 999424u;
constexpr unsigned A_XN = 917504u;
constexpr unsigned A_XR = 958464u;
constexpr unsigned B_H = 0u;
constexpr unsigned B_P3 = 294912u;
constexpr unsigned B_XN1 = 442368u;
constexpr unsigned C_P5 = 0u;
constexpr unsigned DBG = 236978176u;
constexpr unsigned DBG2 = 238026752u;
constexpr unsigned EXB = 294912u;
constexpr unsigned EXL = 245760u;
constexpr unsigned FLAG_BYTES = 491520u;
constexpr unsigned F_AT = 640u;
constexpr unsigned F_CH = 512u;
constexpr unsigned F_F1 = 832u;
constexpr unsigned F_F2 = 1472u;
constexpr unsigned F_LINES = 1728u;
constexpr unsigned F_OP = 1216u;
constexpr unsigned F_R3 = 128u;
constexpr unsigned F_S1 = 256u;
constexpr unsigned F_XN = 0u;
constexpr unsigned G_ABORT = 320u;
constexpr unsigned G_CI = 128u;
constexpr unsigned G_LINES = 384u;
constexpr unsigned G_PAIR = 0u;
constexpr unsigned G_PG = 192u;
constexpr unsigned K1 = 1024u;
constexpr unsigned LF0 = 24576u;
constexpr unsigned MEMB = 40960u;
constexpr unsigned MEML = 835584u;
constexpr unsigned NPAGES = 113u;
constexpr unsigned OOB = 2147483648u;
constexpr unsigned PAGE = 2097152u;
constexpr unsigned SPIN_CAP = 1048576u;
constexpr unsigned WS_USED = 236978176u;
constexpr unsigned CH_WB0 = 0u;
constexpr unsigned CH_WB1 = 40960u;
constexpr unsigned CH_WB2 = 81920u;
constexpr unsigned CH_WB3 = 116736u;
constexpr unsigned CH_IMG = 151552u;
constexpr unsigned CH_SO = 156672u;
constexpr unsigned CH_SELT = 160768u;
constexpr unsigned NPROBE = 6u;
constexpr unsigned NCOPY = 4u;
constexpr unsigned DL0 = 476135424u;
constexpr unsigned DL1 = 170434560u;
constexpr unsigned D1 = 605552680u;
constexpr unsigned D2 = 1077149696u;
constexpr unsigned D3 = 0u;
constexpr unsigned D4 = 793630u;
constexpr int LDS_BYTES = 160 * 1024;
constexpr unsigned spread(unsigned o) { return (o >> 13) * 16384u + (o & 8191u); }

__device__ const unsigned char role_tab[128] = {0, 4, 8, 12, 16, 20, 24, 28, 1, 5, 9, 13, 17, 21, 25, 29, 2, 6, 10, 14, 18, 22, 26, 30, 3, 7, 11, 15, 19, 23, 27, 31, 32, 36, 40, 44, 48, 52, 56, 60, 33, 37, 64, 68, 72, 76, 80, 84, 65, 69, 88, 92, 96, 89, 93, 97, 100, 104, 108, 112, 116, 113, 120, 124, 41, 45, 49, 53, 57, 61, 34, 38, 42, 46, 73, 77, 81, 85, 66, 70, 74, 78, 90, 94, 98, 101, 105, 109, 102, 106, 110, 117, 114, 121, 125, 122, 50, 54, 58, 62, 35, 39, 43, 47, 51, 55, 59, 63, 82, 86, 67, 71, 75, 79, 83, 87, 91, 95, 99, 103, 107, 111, 118, 115, 119, 126, 123, 127};

struct Params {
    const u16* x; const u16* mem0; const u16* Wq; const u16* Wk; const u16* Wv; const u16* Wo;
    const u16* w1; const u16* w3; const u16* w2; u16* out; char* ws;
    u64 tune0, tune1, tune2;
};

__device__ __forceinline__ v4u ld16(rsrc_t rs, unsigned voff, unsigned soff) { return __builtin_amdgcn_raw_buffer_load_b128(rs, voff, soff, 16); }
__device__ __forceinline__ int ld4(rsrc_t rs, unsigned off) { return (int)__builtin_amdgcn_raw_buffer_load_b32(rs, off, 0, 16); }
__device__ __forceinline__ void st16(rsrc_t rs, unsigned voff, unsigned soff, v4u v) { __builtin_amdgcn_raw_buffer_store_b128(v, rs, voff, soff, 16); }
__device__ __forceinline__ void st4(rsrc_t rs, unsigned off, int v) { __builtin_amdgcn_raw_buffer_store_b32((unsigned)v, rs, off, 0, 16); }
#define DRAIN() asm volatile("s_waitcnt vmcnt(0)" ::: "memory")
#define BAR() do { asm volatile("s_waitcnt lgkmcnt(0)" ::: "memory"); __builtin_amdgcn_s_barrier(); asm volatile("" ::: "memory"); } while (0)
__device__ __forceinline__ int rfl(int v) { return __builtin_amdgcn_readfirstlane(v); }
__device__ __forceinline__ u64 pair(unsigned lo, unsigned hi) { return (u64)lo | (u64)hi << 32; }
__device__ __forceinline__ int my_half() {
    unsigned id;
    asm volatile("s_getreg_b32 %0, hwreg(HW_REG_XCC_ID)" : "=s"(id));
    return (id >> 2) & 1;
}

__device__ __forceinline__ void startup(rsrc_t rs, int hw) {
    const int lane = threadIdx.x & 63;
    const v4u ones = {~0u, ~0u, ~0u, ~0u};
    const unsigned o = (unsigned)(hw < 96 ? hw + 1 : (hw == 96 ? 0 : hw)) * PAGE + spread(A_PROBE);
    unsigned best[2] = {~0u, ~0u};
    if (hw < NPAGES) {
#pragma unroll 1
        for (int r = 0; r < 2 * NPROBE; ++r) {
            const unsigned t0 = (unsigned)__builtin_amdgcn_s_memtime();
            st16(rs, lane < 8 ? lane * 16 : OOB, o + (r & 1) * 8192, ones);
            DRAIN();
            const unsigned t = (unsigned)__builtin_amdgcn_s_memtime() - t0;
            if (r & 1) best[1] = min(best[1], t); else best[0] = min(best[0], t);
        }
    }
#pragma unroll
    for (int q = 0; q < 12; ++q) st16(rs, lane * 16, 2 * EXL + (hw * 12 + q) * 1024, ones);
    static_assert(128 * 12 * 1024 >= 2 * (2 * EXB + 4 * MEMB) && 2 * EXL + 128 * 12 * 1024 <= 2 * A_PROBE, "exchange area");
#pragma unroll
    for (int q = 0; q < 4; ++q) st16(rs, lane * 16, (unsigned)(1 + (hw >> 1)) * PAGE + spread(A_ST + q * 1024) + (hw & 1) * 8192, ones);
    DRAIN();
    if (lane == 0) st4(rs, (G_PG + hw) * 128, (best[1] < best[0] ? 1 : 0) ^ my_half());
}

__device__ __forceinline__ void warm(const u16* W, unsigned bytes, unsigned first) {
    const rsrc_t r = __builtin_amdgcn_make_buffer_rsrc(const_cast<u16*>(W), 0, bytes, 0x00020000);
    const unsigned vo = (threadIdx.x & 63) * 16;
#pragma unroll 1
    for (unsigned o = first; o < bytes; o += 448 * 1024)
        asm volatile("s_mov_b32 m0, 0\n\ts_nop 0\n\tbuffer_load_dwordx4 %0, %1, %2 offen lds\n\ts_waitcnt vmcnt(40)" :: "v"(vo), "s"(r), "s"(o) : "memory");
}

__device__ __forceinline__ void role_chain(const Params& p, int h, int x4, int pr, char* smem) {
    const unsigned tid = threadIdx.x, lane = tid & 63, c = lane & 15, g = lane >> 4;
    const int w = rfl(tid >> 6);
    const unsigned wb = w == 0 ? CH_WB0 : (w == 1 ? CH_WB1 : (w == 2 ? CH_WB2 : CH_WB3));
    const unsigned slot = (g * 8 + (c & 7)) * 16;
    const unsigned spr = CH_IMG + (c >> 3) * 512 + slot;
    const unsigned gam = lane >> 4, nh = (lane >> 3) & 1, ksl = (lane >> 2) & 1, tau = ((lane ^ gam) >> 1) & 1, rho = lane & 1;
    auto img = [&](unsigned r) -> unsigned { return (2 * (r & 3) + (g & 1)) * 128 + (((g ^ r) & 2) + (r >> 2)) * 16; };
    if (w >= 2) {
        v4u sel;
#pragma unroll
        for (int e = 0; e < 4; ++e) sel[e] = ((c & 7) == 2 * e) ? 0x3f80u : (((c & 7) == 2 * e + 1) ? 0x3f800000u : 0u);
        const unsigned g3 = ksl * 512 + (2 * (gam & 1) + nh) * 128 + (4 * (gam >> 1) + 2 * tau + rho) * 16;
        *reinterpret_cast<v4u*>(smem + wb + 32768 + lane * 16) = sel;
        *reinterpret_cast<v4u*>(smem + wb + 32768 + 1024 + lane * 16) =
            v4u{lane < 8 * NCOPY ? (lane >> 3) * 1024 + (lane & 7) * 16 : OOB, g3, lane < 8 * NCOPY ? (lane >> 3) * 16384 + ((lane & 7) ^ ((h >> 1) & 2)) * 16 : OOB, 0u};
    } else if (w == 0) {
        const bool onq = g == (c & 3), upq = (c & 4) != 0, onk = g == (c >> 2) && (c & 3) < 2, upk = (c & 1) != 0;
        *reinterpret_cast<v4u*>(smem + spr + CH_SELT - CH_IMG) = v4u{onq ? (upq ? 0x3f800000u : 0x3f80u) : 0u, onq ? (upq ? 0x3f80u : 0x3f800000u) : 0u,
                                                                     onk ? (upk ? 0x3f800000u : 0x3f80u) : 0u, onk ? (upk ? 0x3f80u : 0x3f800000u) : 0u};
    } else {
        const unsigned pub = (lane >> 4) * 16384 + ((lane >> 3) & 1) * 4096 + (lane & 7) * 16;
        const unsigned kmo = (c & 3) < 2 ? (g * 8 + 2 * (c >> 2) + (c & 3)) * 16 : OOB;
        const unsigned vmo = (g & 1) ? OOB : (g >> 1) * 256 + c * 16;
        *reinterpret_cast<v4u*>(smem + spr + CH_SELT - CH_IMG + 1024) = v4u{kmo, vmo, pub, CH_SO + img(c & 7) + (c >> 3) * 8};
    }
    if (w == 0) *reinterpret_cast<unsigned*>(smem + spr + CH_SELT - CH_IMG + 2048) = ~0u;
    BAR();
    const unsigned idx = (lane & 2) ? 0xeeeeu : 0x4444u;
    const unsigned va = CH_IMG + img((c >> 2) + 4 * ((((c & 3) + 1) >> 1) & 1));
    const unsigned pp = lane >> 2, row = (pp & 9) | ((pp & 2) << 1) | ((pp & 4) >> 1);
    const unsigned qo = row * 8192 + (128 * h + 32 * w) * 2 + (lane & 3) * 16;
    const unsigned oo = (128 * h + 16 * ((row >> 2) & 1) + 4 * (row >> 3) + (row & 3)) * 1152 + w * 128 + (lane & 3) * 16;
    const unsigned q = c >> 2;
    const unsigned tr = wb + ((q & 1) + 2 * (g & 1) + 8 * (g >> 1)) * 64 + (lane & 3) * 8;
    const unsigned m0 = (gam + 4 * rho) * 1152 + (4 * ksl + 2 * tau + nh) * 16;
    asm volatile(
        "s_nop 4\n"
        "s_mov_b64 s[40:41], %0\n"
        "s_and_b32 s41, s41, 0xffff\n"
        "s_mov_b32 s42, 236978176\n"
        "s_mov_b32 s43, 0x20000\n"
        "s_mov_b64 s[48:49], %6\n"
        "s_mov_b64 s[56:57], %7\n"
        "s_mov_b64 s[60:61], %8\n"
        "s_mov_b64 s[62:63], %9\n"
        "v_mov_b64 v[0:1], %10\n"
        "v_mov_b64 v[2:3], %11\n"
        "v_mov_b64 v[4:5], %12\n"
        "v_mov_b64 v[6:7], %13\n"
        "v_mov_b64 v[8:9], %14\n"
        "v_mov_b64 v[10:11], %15\n"
        "s_mov_b32 s47, 0x20000\n"
        "v_readfirstlane_b32 s82, v2\n"
        "s_lshr_b32 s65, s56, 8\n"
        "s_and_b32 s56, s56, 1\n"
        ".Lch_start_%=:\n"
        "s_sub_u32 s65, s65, 1\n"
        "s_cbranch_scc1 .Lch_start2_%=\n"
        "s_sleep 16\n"
        "s_branch .Lch_start_%=\n"
        ".Lch_start2_%=:\n"
        "s_mov_b64 s[44:45], %5\n"
        "s_and_b32 s45, s45, 0xffff\n"
        "s_mov_b32 s46, 9216\n"
        "s_lshl_b32 s65, s48, 10\n"
        "s_add_u32 m0, s65, 151552\n"
        "s_lshl_b32 s65, s48, 7\n"
        "s_add_u32 s65, s65, 0\n"
        "buffer_load_dwordx4 v9, s[44:47], s65 offen lds\n"
        "s_lshl_b32 s65, s48, 10\n"
        "s_add_u32 m0, s65, 155648\n"
        "s_lshl_b32 s65, s48, 7\n"
        "s_add_u32 s65, s65, 512\n"
        "buffer_load_dwordx4 v9, s[44:47], s65 offen lds\n"
        "s_mov_b32 m0, 159744\n"
        "s_mov_b32 s65, 1024\n"
        "buffer_load_dwordx4 v9, s[44:47], s65 offen lds\n"
        "s_mov_b64 s[44:45], %1\n"
        "s_and_b32 s45, s45, 0xffff\n"
        "s_mov_b32 s46, 4718592\n"
        "s_add_u32 m0, s82, 0\n"
        "s_mov_b32 s65, 0\n"
        "buffer_load_dwordx4 v6, s[44:47], s65 offen lds\n"
        "s_mov_b32 s65, 130048\n"
        "buffer_load_dwordx4 v6, s[44:47], s65 offen offset:1024 lds\n"
        "s_add_u32 m0, s82, 2048\n"
        "s_mov_b32 s65, 262144\n"
        "buffer_load_dwordx4 v6, s[44:47], s65 offen lds\n"
        "s_mov_b32 s65, 392192\n"
        "buffer_load_dwordx4 v6, s[44:47], s65 offen offset:1024 lds\n"
        "s_add_u32 m0, s82, 4096\n"
        "s_mov_b32 s65, 524288\n"
        "buffer_load_dwordx4 v6, s[44:47], s65 offen lds\n"
        "s_mov_b32 s65, 654336\n"
        "buffer_load_dwordx4 v6, s[44:47], s65 offen offset:1024 lds\n"
        "s_add_u32 m0, s82, 6144\n"
        "s_mov_b32 s65, 786432\n"
        "buffer_load_dwordx4 v6, s[44:47], s65 offen lds\n"
        "s_mov_b32 s65, 916480\n"
        "buffer_load_dwordx4 v6, s[44:47], s65 offen offset:1024 lds\n"
        "s_add_u32 m0, s82, 8192\n"
        "s_mov_b32 s65, 1048576\n"
        "buffer_load_dwordx4 v6, s[44:47], s65 offen lds\n"
        "s_mov_b32 s65, 1178624\n"
        "buffer_load_dwordx4 v6, s[44:47], s65 offen offset:1024 lds\n"
        "s_add_u32 m0, s82, 10240\n"
        "s_mov_b32 s65, 1310720\n"
        "buffer_load_dwordx4 v6, s[44:47], s65 offen lds\n"
        "s_mov_b32 s65, 1440768\n"
        "buffer_load_dwordx4 v6, s[44:47], s65 offen offset:1024 lds\n"
        "s_add_u32 m0, s82, 12288\n"
        "s_mov_b32 s65, 1572864\n"
        "buffer_load_dwordx4 v6, s[44:47], s65 offen lds\n"
        "s_mov_b32 s65, 1702912\n"
        "buffer_load_dwordx4 v6, s[44:47], s65 offen offset:1024 lds\n"
        "s_add_u32 m0, s82, 14336\n"
        "s_mov_b32 s65, 1835008\n"
        "buffer_load_dwordx4 v6, s[44:47], s65 offen lds\n"
        "s_mov_b32 s65, 1965056\n"
        "buffer_load_dwordx4 v6, s[44:47], s65 offen offset:1024 lds\n"
        "s_add_u32 m0, s82, 16384\n"
        "s_mov_b32 s65, 2097152\n"
        "buffer_load_dwordx4 v6, s[44:47], s65 offen lds\n"
        "s_mov_b32 s65, 2227200\n"
        "buffer_load_dwordx4 v6, s[44:47], s65 offen offset:1024 lds\n"
        "s_add_u32 m0, s82, 18432\n"
        "s_mov_b32 s65, 2359296\n"
        "buffer_load_dwordx4 v6, s[44:47], s65 offen lds\n"
        "s_mov_b32 s65, 2489344\n"
        "buffer_load_dwordx4 v6, s[44:47], s65 offen offset:1024 lds\n"
        "s_add_u32 m0, s82, 20480\n"
        "s_mov_b32 s65, 2621440\n"
        "buffer_load_dwordx4 v6, s[44:47], s65 offen lds\n"
        "s_mov_b32 s65, 2751488\n"
        "buffer_load_dwordx4 v6, s[44:47], s65 offen offset:1024 lds\n"
        "s_add_u32 m0, s82, 22528\n"
        "s_mov_b32 s65, 2883584\n"
        "buffer_load_dwordx4 v6, s[44:47], s65 offen lds\n"
        "s_mov_b32 s65, 3013632\n"
        "buffer_load_dwordx4 v6, s[44:47], s65 offen offset:1024 lds\n"
        "s_add_u32 m0, s82, 24576\n"
        "s_mov_b32 s65, 3145728\n"
        "buffer_load_dwordx4 v6, s[44:47], s65 offen lds\n"
        "s_mov_b32 s65, 3275776\n"
        "buffer_load_dwordx4 v6, s[44:47], s65 offen offset:1024 lds\n"
        "s_add_u32 m0, s82, 26624\n"
        "s_mov_b32 s65, 3407872\n"
        "buffer_load_dwordx4 v6, s[44:47], s65 offen lds\n"
        "s_mov_b32 s65, 3537920\n"
        "buffer_load_dwordx4 v6, s[44:47], s65 offen offset:1024 lds\n"
        "s_add_u32 m0, s82, 28672\n"
        "s_mov_b32 s65, 3670016\n"
        "buffer_load_dwordx4 v6, s[44:47], s65 offen lds\n"
        "s_mov_b32 s65, 3800064\n"
        "buffer_load_dwordx4 v6, s[44:47], s65 offen offset:1024 lds\n"
        "s_add_u32 m0, s82, 30720\n"
        "s_mov_b32 s65, 3932160\n"
        "buffer_load_dwordx4 v6, s[44:47], s65 offen lds\n"
        "s_mov_b32 s65, 4062208\n"
        "buffer_load_dwordx4 v6, s[44:47], s65 offen offset:1024 lds\n"
        "s_waitcnt vmcnt(28)\n"
        "ds_read_b64_tr_b16 a[0:1], v10 offset:0\n"
        "ds_read_b64_tr_b16 a[2:3], v10 offset:256\n"
        "ds_read_b64_tr_b16 a[4:5], v10 offset:1024\n"
        "ds_read_b64_tr_b16 a[6:7], v10 offset:1280\n"
        "ds_read_b64_tr_b16 a[8:9], v10 offset:32\n"
        "ds_read_b64_tr_b16 a[10:11], v10 offset:288\n"
        "ds_read_b64_tr_b16 a[12:13], v10 offset:1056\n"
        "ds_read_b64_tr_b16 a[14:15], v10 offset:1312\n"
        "s_waitcnt vmcnt(24)\n"
        "ds_read_b64_tr_b16 a[16:17], v10 offset:4096\n"
        "ds_read_b64_tr_b16 a[18:19], v10 offset:4352\n"
        "ds_read_b64_tr_b16 a[20:21], v10 offset:5120\n"
        "ds_read_b64_tr_b16 a[22:23], v10 offset:5376\n"
        "ds_read_b64_tr_b16 a[24:25], v10 offset:4128\n"
        "ds_read_b64_tr_b16 a[26:27], v10 offset:4384\n"
        "ds_read_b64_tr_b16 a[28:29], v10 offset:5152\n"
        "ds_read_b64_tr_b16 a[30:31], v10 offset:5408\n"
        "s_waitcnt lgkmcnt(8)\n"
        "s_add_u32 m0, s82, 0\n"
        "s_mov_b32 s65, 4194304\n"
        "buffer_load_dwordx4 v6, s[44:47], s65 offen lds\n"
        "s_mov_b32 s65, 4324352\n"
        "buffer_load_dwordx4 v6, s[44:47], s65 offen offset:1024 lds\n"
        "s_add_u32 m0, s82, 2048\n"
        "s_mov_b32 s65, 4456448\n"
        "buffer_load_dwordx4 v6, s[44:47], s65 offen lds\n"
        "s_mov_b32 s65, 4586496\n"
        "buffer_load_dwordx4 v6, s[44:47], s65 offen offset:1024 lds\n"
        "s_waitcnt vmcnt(24)\n"
        "ds_read_b64_tr_b16 a[32:33], v10 offset:8192\n"
        "ds_read_b64_tr_b16 a[34:35], v10 offset:8448\n"
        "ds_read_b64_tr_b16 a[36:37], v10 offset:9216\n"
        "ds_read_b64_tr_b16 a[38:39], v10 offset:9472\n"
        "ds_read_b64_tr_b16 a[40:41], v10 offset:8224\n"
        "ds_read_b64_tr_b16 a[42:43], v10 offset:8480\n"
        "ds_read_b64_tr_b16 a[44:45], v10 offset:9248\n"
        "ds_read_b64_tr_b16 a[46:47], v10 offset:9504\n"
        "s_waitcnt lgkmcnt(8)\n"
        "s_mov_b64 s[44:45], %2\n"
        "s_and_b32 s45, s45, 0xffff\n"
        "s_mov_b32 s46, 4718592\n"
        "s_add_u32 m0, s82, 4096\n"
        "s_mov_b32 s65, 0\n"
        "buffer_load_dwordx4 v6, s[44:47], s65 offen lds\n"
        "s_mov_b32 s65, 130048\n"
        "buffer_load_dwordx4 v6, s[44:47], s65 offen offset:1024 lds\n"
        "s_add_u32 m0, s82, 6144\n"
        "s_mov_b32 s65, 262144\n"
        "buffer_load_dwordx4 v6, s[44:47], s65 offen lds\n"
        "s_mov_b32 s65, 392192\n"
        "buffer_load_dwordx4 v6, s[44:47], s65 offen offset:1024 lds\n"
        "s_waitcnt vmcnt(24)\n"
        "ds_read_b64_tr_b16 a[48:49], v10 offset:12288\n"
        "ds_read_b64_tr_b16 a[50:51], v10 offset:12544\n"
        "ds_read_b64_tr_b16 a[52:53], v10 offset:13312\n"
        "ds_read_b64_tr_b16 a[54:55], v10 offset:13568\n"
        "ds_read_b64_tr_b16 a[56:57], v10 offset:12320\n"
        "ds_read_b64_tr_b16 a[58:59], v10 offset:12576\n"
        "ds_read_b64_tr_b16 a[60:61], v10 offset:13344\n"
        "ds_read_b64_tr_b16 a[62:63], v10 offset:13600\n"
        "s_waitcnt lgkmcnt(8)\n"
        "s_add_u32 m0, s82, 8192\n"
        "s_mov_b32 s65, 524288\n"
        "buffer_load_dwordx4 v6, s[44:47], s65 offen lds\n"
        "s_mov_b32 s65, 654336\n"
        "buffer_load_dwordx4 v6, s[44:47], s65 offen offset:1024 lds\n"
        "s_add_u32 m0, s82, 10240\n"
        "s_mov_b32 s65, 786432\n"
        "buffer_load_dwordx4 v6, s[44:47], s65 offen lds\n"
        "s_mov_b32 s65, 916480\n"
        "buffer_load_dwordx4 v6, s[44:47], s65 offen offset:1024 lds\n"
        "s_waitcnt vmcnt(24)\n"
        "ds_read_b64_tr_b16 a[64:65], v10 offset:16384\n"
        "ds_read_b64_tr_b16 a[66:67], v10 offset:16640\n"
        "ds_read_b64_tr_b16 a[68:69], v10 offset:17408\n"
        "ds_read_b64_tr_b16 a[70:71], v10 offset:17664\n"
        "ds_read_b64_tr_b16 a[72:73], v10 offset:16416\n"
        "ds_read_b64_tr_b16 a[74:75], v10 offset:16672\n"
        "ds_read_b64_tr_b16 a[76:77], v10 offset:17440\n"
        "ds_read_b64_tr_b16 a[78:79], v10 offset:17696\n"
        "s_waitcnt lgkmcnt(8)\n"
        "s_add_u32 m0, s82, 12288\n"
        "s_mov_b32 s65, 1048576\n"
        "buffer_load_dwordx4 v6, s[44:47], s65 offen lds\n"
        "s_mov_b32 s65, 1178624\n"
        "buffer_load_dwordx4 v6, s[44:47], s65 offen offset:1024 lds\n"
        "s_add_u32 m0, s82, 14336\n"
        "s_mov_b32 s65, 1310720\n"
        "buffer_load_dwordx4 v6, s[44:47], s65 offen lds\n"
        "s_mov_b32 s65, 1440768\n"
        "buffer_load_dwordx4 v6, s[44:47], s65 offen offset:1024 lds\n"
        "s_waitcnt vmcnt(24)\n"
        "ds_read_b64_tr_b16 a[80:81], v10 offset:20480\n"
        "ds_read_b64_tr_b16 a[82:83], v10 offset:20736\n"
        "ds_read_b64_tr_b16 a[84:85], v10 offset:21504\n"
        "ds_read_b64_tr_b16 a[86:87], v10 offset:21760\n"
        "ds_read_b64_tr_b16 a[88:89], v10 offset:20512\n"
        "ds_read_b64_tr_b16 a[90:91], v10 offset:20768\n"
        "ds_read_b64_tr_b16 a[92:93], v10 offset:21536\n"
        "ds_read_b64_tr_b16 a[94:95], v10 offset:21792\n"
        "s_waitcnt lgkmcnt(8)\n"
        "s_add_u32 m0, s82, 16384\n"
        "s_mov_b32 s65, 1572864\n"
        "buffer_load_dwordx4 v6, s[44:47], s65 offen lds\n"
        "s_mov_b32 s65, 1702912\n"
        "buffer_load_dwordx4 v6, s[44:47], s65 offen offset:1024 lds\n"
        "s_add_u32 m0, s82, 18432\n"
        "s_mov_b32 s65, 1835008\n"
        "buffer_load_dwordx4 v6, s[44:47], s65 offen lds\n"
        "s_mov_b32 s65, 1965056\n"
        "buffer_load_dwordx4 v6, s[44:47], s65 offen offset:1024 lds\n"
        "s_waitcnt vmcnt(24)\n"
        "ds_read_b64_tr_b16 a[96:97], v10 offset:24576\n"
        "ds_read_b64_tr_b16 a[98:99], v10 offset:24832\n"
        "ds_read_b64_tr_b16 a[100:101], v10 offset:25600\n"
        "ds_read_b64_tr_b16 a[102:103], v10 offset:25856\n"
        "ds_read_b64_tr_b16 a[104:105], v10 offset:24608\n"
        "ds_read_b64_tr_b16 a[106:107], v10 offset:24864\n"
        "ds_read_b64_tr_b16 a[108:109], v10 offset:25632\n"
        "ds_read_b64_tr_b16 a[110:111], v10 offset:25888\n"
        "s_waitcnt lgkmcnt(8)\n"
        "s_add_u32 m0, s82, 20480\n"
        "s_mov_b32 s65, 2097152\n"
        "buffer_load_dwordx4 v6, s[44:47], s65 offen lds\n"
        "s_mov_b32 s65, 2227200\n"
        "buffer_load_dwordx4 v6, s[44:47], s65 offen offset:1024 lds\n"
        "s_add_u32 m0, s82, 22528\n"
        "s_mov_b32 s65, 2359296\n"
        "buffer_load_dwordx4 v6, s[44:47], s65 offen lds\n"
        "s_mov_b32 s65, 2489344\n"
        "buffer_load_dwordx4 v6, s[44:47], s65 offen offset:1024 lds\n"
        "s_waitcnt vmcnt(24)\n"
        "ds_read_b64_tr_b16 a[112:113], v10 offset:28672\n"
        "ds_read_b64_tr_b16 a[114:115], v10 offset:28928\n"
        "ds_read_b64_tr_b16 a[116:117], v10 offset:29696\n"
        "ds_read_b64_tr_b16 a[118:119], v10 offset:29952\n"
        "ds_read_b64_tr_b16 a[120:121], v10 offset:28704\n"
        "ds_read_b64_tr_b16 a[122:123], v10 offset:28960\n"
        "ds_read_b64_tr_b16 a[124:125], v10 offset:29728\n"
        "ds_read_b64_tr_b16 a[126:127], v10 offset:29984\n"
        "s_waitcnt lgkmcnt(8)\n"
        "s_add_u32 m0, s82, 24576\n"
        "s_mov_b32 s65, 2621440\n"
        "buffer_load_dwordx4 v6, s[44:47], s65 offen lds\n"
        "s_mov_b32 s65, 2751488\n"
        "buffer_load_dwordx4 v6, s[44:47], s65 offen offset:1024 lds\n"
        "s_add_u32 m0, s82, 26624\n"
        "s_mov_b32 s65, 2883584\n"
        "buffer_load_dwordx4 v6, s[44:47], s65 offen lds\n"
        "s_mov_b32 s65, 3013632\n"
        "buffer_load_dwordx4 v6, s[44:47], s65 offen offset:1024 lds\n"
        "s_waitcnt vmcnt(24)\n"
        "ds_read_b64_tr_b16 a[128:129], v10 offset:0\n"
        "ds_read_b64_tr_b16 a[130:131], v10 offset:256\n"
        "ds_read_b64_tr_b16 a[132:133], v10 offset:1024\n"
        "ds_read_b64_tr_b16 a[134:135], v10 offset:1280\n"
        "ds_read_b64_tr_b16 a[136:137], v10 offset:32\n"
        "ds_read_b64_tr_b16 a[138:139], v10 offset:288\n"
        "ds_read_b64_tr_b16 a[140:141], v10 offset:1056\n"
        "ds_read_b64_tr_b16 a[142:143], v10 offset:1312\n"
        "s_waitcnt lgkmcnt(8)\n"
        "s_add_u32 m0, s82, 28672\n"
        "s_mov_b32 s65, 3145728\n"
        "buffer_load_dwordx4 v6, s[44:47], s65 offen lds\n"
        "s_mov_b32 s65, 3275776\n"
        "buffer_load_dwordx4 v6, s[44:47], s65 offen offset:1024 lds\n"
        "s_add_u32 m0, s82, 30720\n"
        "s_mov_b32 s65, 3407872\n"
        "buffer_load_dwordx4 v6, s[44:47], s65 offen lds\n"
        "s_mov_b32 s65, 3537920\n"
        "buffer_load_dwordx4 v6, s[44:47], s65 offen offset:1024 lds\n"
        "s_waitcnt vmcnt(24)\n"
        "ds_read_b64_tr_b16 a[144:145], v10 offset:4096\n"
        "ds_read_b64_tr_b16 a[146:147], v10 offset:4352\n"
        "ds_read_b64_tr_b16 a[148:149], v10 offset:5120\n"
        "ds_read_b64_tr_b16 a[150:151], v10 offset:5376\n"
        "ds_read_b64_tr_b16 a[152:153], v10 offset:4128\n"
        "ds_read_b64_tr_b16 a[154:155], v10 offset:4384\n"
        "ds_read_b64_tr_b16 a[156:157], v10 offset:5152\n"
        "ds_read_b64_tr_b16 a[158:159], v10 offset:5408\n"
        "s_waitcnt lgkmcnt(8)\n"
        "s_add_u32 m0, s82, 0\n"
        "s_mov_b32 s65, 3670016\n"
        "buffer_load_dwordx4 v6, s[44:47], s65 offen lds\n"
        "s_mov_b32 s65, 3800064\n"
        "buffer_load_dwordx4 v6, s[44:47], s65 offen offset:1024 lds\n"
        "s_add_u32 m0, s82, 2048\n"
        "s_mov_b32 s65, 3932160\n"
        "buffer_load_dwordx4 v6, s[44:47], s65 offen lds\n"
        "s_mov_b32 s65, 4062208\n"
        "buffer_load_dwordx4 v6, s[44:47], s65 offen offset:1024 lds\n"
        "s_waitcnt vmcnt(24)\n"
        "ds_read_b64_tr_b16 a[160:161], v10 offset:8192\n"
        "ds_read_b64_tr_b16 a[162:163], v10 offset:8448\n"
        "ds_read_b64_tr_b16 a[164:165], v10 offset:9216\n"
        "ds_read_b64_tr_b16 a[166:167], v10 offset:9472\n"
        "ds_read_b64_tr_b16 a[168:169], v10 offset:8224\n"
        "ds_read_b64_tr_b16 a[170:171], v10 offset:8480\n"
        "ds_read_b64_tr_b16 a[172:173], v10 offset:9248\n"
        "ds_read_b64_tr_b16 a[174:175], v10 offset:9504\n"
        "s_waitcnt lgkmcnt(8)\n"
        "s_add_u32 m0, s82, 4096\n"
        "s_mov_b32 s65, 4194304\n"
        "buffer_load_dwordx4 v6, s[44:47], s65 offen lds\n"
        "s_mov_b32 s65, 4324352\n"
        "buffer_load_dwordx4 v6, s[44:47], s65 offen offset:1024 lds\n"
        "s_add_u32 m0, s82, 6144\n"
        "s_mov_b32 s65, 4456448\n"
        "buffer_load_dwordx4 v6, s[44:47], s65 offen lds\n"
        "s_mov_b32 s65, 4586496\n"
        "buffer_load_dwordx4 v6, s[44:47], s65 offen offset:1024 lds\n"
        "s_waitcnt vmcnt(24)\n"
        "ds_read_b64_tr_b16 a[176:177], v10 offset:12288\n"
        "ds_read_b64_tr_b16 a[178:179], v10 offset:12544\n"
        "ds_read_b64_tr_b16 a[180:181], v10 offset:13312\n"
        "ds_read_b64_tr_b16 a[182:183], v10 offset:13568\n"
        "ds_read_b64_tr_b16 a[184:185], v10 offset:12320\n"
        "ds_read_b64_tr_b16 a[186:187], v10 offset:12576\n"
        "ds_read_b64_tr_b16 a[188:189], v10 offset:13344\n"
        "ds_read_b64_tr_b16 a[190:191], v10 offset:13600\n"
        "s_waitcnt lgkmcnt(8)\n"
        "s_mov_b64 s[44:45], %4\n"
        "s_and_b32 s45, s45, 0xffff\n"
        "s_mov_b32 s46, 4718592\n"
        "s_add_u32 m0, s82, 8192\n"
        "s_mov_b32 s65, 0\n"
        "buffer_load_dwordx4 v7, s[44:47], s65 offen lds\n"
        "s_mov_b32 s65, 8192\n"
        "buffer_load_dwordx4 v7, s[44:47], s65 offen offset:1024 lds\n"
        "s_add_u32 m0, s82, 10240\n"
        "s_mov_b32 s65, 36864\n"
        "buffer_load_dwordx4 v7, s[44:47], s65 offen lds\n"
        "s_mov_b32 s65, 45056\n"
        "buffer_load_dwordx4 v7, s[44:47], s65 offen offset:1024 lds\n"
        "s_waitcnt vmcnt(24)\n"
        "ds_read_b64_tr_b16 a[192:193], v10 offset:16384\n"
        "ds_read_b64_tr_b16 a[194:195], v10 offset:16640\n"
        "ds_read_b64_tr_b16 a[196:197], v10 offset:17408\n"
        "ds_read_b64_tr_b16 a[198:199], v10 offset:17664\n"
        "ds_read_b64_tr_b16 a[200:201], v10 offset:16416\n"
        "ds_read_b64_tr_b16 a[202:203], v10 offset:16672\n"
        "ds_read_b64_tr_b16 a[204:205], v10 offset:17440\n"
        "ds_read_b64_tr_b16 a[206:207], v10 offset:17696\n"
        "s_waitcnt lgkmcnt(8)\n"
        "s_add_u32 m0, s82, 12288\n"
        "s_mov_b32 s65, 73728\n"
        "buffer_load_dwordx4 v7, s[44:47], s65 offen lds\n"
        "s_mov_b32 s65, 81920\n"
        "buffer_load_dwordx4 v7, s[44:47], s65 offen offset:1024 lds\n"
        "s_add_u32 m0, s82, 14336\n"
        "s_mov_b32 s65, 110592\n"
        "buffer_load_dwordx4 v7, s[44:47], s65 offen lds\n"
        "s_mov_b32 s65, 118784\n"
        "buffer_load_dwordx4 v7, s[44:47], s65 offen offset:1024 lds\n"
        "s_waitcnt vmcnt(24)\n"
        "ds_read_b64_tr_b16 a[208:209], v10 offset:20480\n"
        "ds_read_b64_tr_b16 a[210:211], v10 offset:20736\n"
        "ds_read_b64_tr_b16 a[212:213], v10 offset:21504\n"
        "ds_read_b64_tr_b16 a[214:215], v10 offset:21760\n"
        "ds_read_b64_tr_b16 a[216:217], v10 offset:20512\n"
        "ds_read_b64_tr_b16 a[218:219], v10 offset:20768\n"
        "ds_read_b64_tr_b16 a[220:221], v10 offset:21536\n"
        "ds_read_b64_tr_b16 a[222:223], v10 offset:21792\n"
        "s_waitcnt lgkmcnt(8)\n"
        "s_add_u32 m0, s82, 16384\n"
        "s_mov_b32 s65, 64\n"
        "buffer_load_dwordx4 v7, s[44:47], s65 offen lds\n"
        "s_mov_b32 s65, 8256\n"
        "buffer_load_dwordx4 v7, s[44:47], s65 offen offset:1024 lds\n"
        "s_add_u32 m0, s82, 18432\n"
        "s_mov_b32 s65, 36928\n"
        "buffer_load_dwordx4 v7, s[44:47], s65 offen lds\n"
        "s_mov_b32 s65, 45120\n"
        "buffer_load_dwordx4 v7, s[44:47], s65 offen offset:1024 lds\n"
        "s_waitcnt vmcnt(24)\n"
        "ds_read_b64_tr_b16 a[224:225], v10 offset:24576\n"
        "ds_read_b64_tr_b16 a[226:227], v10 offset:24832\n"
        "ds_read_b64_tr_b16 a[228:229], v10 offset:25600\n"
        "ds_read_b64_tr_b16 a[230:231], v10 offset:25856\n"
        "ds_read_b64_tr_b16 a[232:233], v10 offset:24608\n"
        "ds_read_b64_tr_b16 a[234:235], v10 offset:24864\n"
        "ds_read_b64_tr_b16 a[236:237], v10 offset:25632\n"
        "ds_read_b64_tr_b16 a[238:239], v10 offset:25888\n"
        "s_waitcnt lgkmcnt(8)\n"
        "s_add_u32 m0, s82, 20480\n"
        "s_mov_b32 s65, 73792\n"
        "buffer_load_dwordx4 v7, s[44:47], s65 offen lds\n"
        "s_mov_b32 s65, 81984\n"
        "buffer_load_dwordx4 v7, s[44:47], s65 offen offset:1024 lds\n"
        "s_add_u32 m0, s82, 22528\n"
        "s_mov_b32 s65, 110656\n"
        "buffer_load_dwordx4 v7, s[44:47], s65 offen lds\n"
        "s_mov_b32 s65, 118848\n"
        "buffer_load_dwordx4 v7, s[44:47], s65 offen offset:1024 lds\n"
        "s_waitcnt vmcnt(24)\n"
        "ds_read_b64_tr_b16 a[240:241], v10 offset:28672\n"
        "ds_read_b64_tr_b16 a[242:243], v10 offset:28928\n"
        "ds_read_b64_tr_b16 a[244:245], v10 offset:29696\n"
        "ds_read_b64_tr_b16 a[246:247], v10 offset:29952\n"
        "ds_read_b64_tr_b16 a[248:249], v10 offset:28704\n"
        "ds_read_b64_tr_b16 a[250:251], v10 offset:28960\n"
        "ds_read_b64_tr_b16 a[252:253], v10 offset:29728\n"
        "ds_read_b64_tr_b16 a[254:255], v10 offset:29984\n"
        "s_waitcnt lgkmcnt(8)\n"
        "s_add_u32 m0, s82, 24576\n"
        "s_mov_b32 s65, 512\n"
        "buffer_load_dwordx4 v7, s[44:47], s65 offen lds\n"
        "s_mov_b32 s65, 8704\n"
        "buffer_load_dwordx4 v7, s[44:47], s65 offen offset:1024 lds\n"
        "s_add_u32 m0, s82, 26624\n"
        "s_mov_b32 s65, 37376\n"
        "buffer_load_dwordx4 v7, s[44:47], s65 offen lds\n"
        "s_mov_b32 s65, 45568\n"
        "buffer_load_dwordx4 v7, s[44:47], s65 offen offset:1024 lds\n"
        "s_waitcnt vmcnt(24)\n"
        "ds_read_b64_tr_b16 v[80:81], v10 offset:0\n"
        "ds_read_b64_tr_b16 v[82:83], v10 offset:256\n"
        "ds_read_b64_tr_b16 v[84:85], v10 offset:1024\n"
        "ds_read_b64_tr_b16 v[86:87], v10 offset:1280\n"
        "ds_read_b64_tr_b16 v[88:89], v10 offset:32\n"
        "ds_read_b64_tr_b16 v[90:91], v10 offset:288\n"
        "ds_read_b64_tr_b16 v[92:93], v10 offset:1056\n"
        "ds_read_b64_tr_b16 v[94:95], v10 offset:1312\n"
        "s_waitcnt lgkmcnt(8)\n"
        "s_add_u32 m0, s82, 28672\n"
        "s_mov_b32 s65, 74240\n"
        "buffer_load_dwordx4 v7, s[44:47], s65 offen lds\n"
        "s_mov_b32 s65, 82432\n"
        "buffer_load_dwordx4 v7, s[44:47], s65 offen offset:1024 lds\n"
        "s_add_u32 m0, s82, 30720\n"
        "s_mov_b32 s65, 111104\n"
        "buffer_load_dwordx4 v7, s[44:47], s65 offen lds\n"
        "s_mov_b32 s65, 119296\n"
        "buffer_load_dwordx4 v7, s[44:47], s65 offen offset:1024 lds\n"
        "s_waitcnt vmcnt(24)\n"
        "ds_read_b64_tr_b16 v[96:97], v10 offset:4096\n"
        "ds_read_b64_tr_b16 v[98:99], v10 offset:4352\n"
        "ds_read_b64_tr_b16 v[100:101], v10 offset:5120\n"
        "ds_read_b64_tr_b16 v[102:103], v10 offset:5376\n"
        "ds_read_b64_tr_b16 v[104:105], v10 offset:4128\n"
        "ds_read_b64_tr_b16 v[106:107], v10 offset:4384\n"
        "ds_read_b64_tr_b16 v[108:109], v10 offset:5152\n"
        "ds_read_b64_tr_b16 v[110:111], v10 offset:5408\n"
        "s_waitcnt lgkmcnt(8)\n"
        "s_add_u32 m0, s82, 0\n"
        "s_mov_b32 s65, 576\n"
        "buffer_load_dwordx4 v7, s[44:47], s65 offen lds\n"
        "s_mov_b32 s65, 8768\n"
        "buffer_load_dwordx4 v7, s[44:47], s65 offen offset:1024 lds\n"
        "s_add_u32 m0, s82, 2048\n"
        "s_mov_b32 s65, 37440\n"
        "buffer_load_dwordx4 v7, s[44:47], s65 offen lds\n"
        "s_mov_b32 s65, 45632\n"
        "buffer_load_dwordx4 v7, s[44:47], s65 offen offset:1024 lds\n"
        "s_waitcnt vmcnt(24)\n"
        "ds_read_b64_tr_b16 v[112:113], v8 offset:8192\n"
        "ds_read_b64_tr_b16 v[114:115], v8 offset:8448\n"
        "ds_read_b64_tr_b16 v[116:117], v8 offset:9216\n"
        "ds_read_b64_tr_b16 v[118:119], v8 offset:9472\n"
        "ds_read_b64_tr_b16 v[120:121], v8 offset:10240\n"
        "ds_read_b64_tr_b16 v[122:123], v8 offset:10496\n"
        "ds_read_b64_tr_b16 v[124:125], v8 offset:11264\n"
        "ds_read_b64_tr_b16 v[126:127], v8 offset:11520\n"
        "s_waitcnt lgkmcnt(8)\n"
        "s_add_u32 m0, s82, 4096\n"
        "s_mov_b32 s65, 74304\n"
        "buffer_load_dwordx4 v7, s[44:47], s65 offen lds\n"
        "s_mov_b32 s65, 82496\n"
        "buffer_load_dwordx4 v7, s[44:47], s65 offen offset:1024 lds\n"
        "s_add_u32 m0, s82, 6144\n"
        "s_mov_b32 s65, 111168\n"
        "buffer_load_dwordx4 v7, s[44:47], s65 offen lds\n"
        "s_mov_b32 s65, 119360\n"
        "buffer_load_dwordx4 v7, s[44:47], s65 offen offset:1024 lds\n"
        "s_cmp_lt_u32 s48, 2\n"
        "s_cbranch_scc0 .Lch_noex_%=\n"
        "s_lshl_b32 s66, s48, 6\n"
        "s_add_u32 m0, s82, 32768\n"
        "s_sub_u32 s65, 1024, s66\n"
        "buffer_load_dwordx4 v7, s[44:47], s65 offen lds\n"
        "s_sub_u32 s65, 9216, s66\n"
        "buffer_load_dwordx4 v7, s[44:47], s65 offen offset:1024 lds\n"
        "s_add_u32 m0, s82, 34816\n"
        "s_sub_u32 s65, 37888, s66\n"
        "buffer_load_dwordx4 v7, s[44:47], s65 offen lds\n"
        "s_sub_u32 s65, 46080, s66\n"
        "buffer_load_dwordx4 v7, s[44:47], s65 offen offset:1024 lds\n"
        "s_add_u32 m0, s82, 36864\n"
        "s_sub_u32 s65, 74752, s66\n"
        "buffer_load_dwordx4 v7, s[44:47], s65 offen lds\n"
        "s_sub_u32 s65, 82944, s66\n"
        "buffer_load_dwordx4 v7, s[44:47], s65 offen offset:1024 lds\n"
        "s_add_u32 m0, s82, 38912\n"
        "s_sub_u32 s65, 111616, s66\n"
        "buffer_load_dwordx4 v7, s[44:47], s65 offen lds\n"
        "s_sub_u32 s65, 119808, s66\n"
        "buffer_load_dwordx4 v7, s[44:47], s65 offen offset:1024 lds\n"
        ".Lch_noex_%=:\n"
        "s_waitcnt vmcnt(24)\n"
        "ds_read_b64_tr_b16 v[128:129], v8 offset:12288\n"
        "ds_read_b64_tr_b16 v[130:131], v8 offset:12544\n"
        "ds_read_b64_tr_b16 v[132:133], v8 offset:13312\n"
        "ds_read_b64_tr_b16 v[134:135], v8 offset:13568\n"
        "ds_read_b64_tr_b16 v[136:137], v8 offset:14336\n"
        "ds_read_b64_tr_b16 v[138:139], v8 offset:14592\n"
        "ds_read_b64_tr_b16 v[140:141], v8 offset:15360\n"
        "ds_read_b64_tr_b16 v[142:143], v8 offset:15616\n"
        "s_waitcnt lgkmcnt(8)\n"
        "s_mov_b64 s[44:45], %3\n"
        "s_and_b32 s45, s45, 0xffff\n"
        "s_mov_b32 s46, 4718592\n"
        "s_add_u32 m0, s82, 8192\n"
        "s_mov_b32 s65, 4194304\n"
        "buffer_load_dwordx4 v6, s[44:47], s65 offen lds\n"
        "s_mov_b32 s65, 4324352\n"
        "buffer_load_dwordx4 v6, s[44:47], s65 offen offset:1024 lds\n"
        "s_add_u32 m0, s82, 10240\n"
        "s_mov_b32 s65, 4456448\n"
        "buffer_load_dwordx4 v6, s[44:47], s65 offen lds\n"
        "s_mov_b32 s65, 4586496\n"
        "buffer_load_dwordx4 v6, s[44:47], s65 offen offset:1024 lds\n"
        "s_waitcnt vmcnt(24)\n"
        "ds_read_b64_tr_b16 v[144:145], v8 offset:16384\n"
        "ds_read_b64_tr_b16 v[146:147], v8 offset:16640\n"
        "ds_read_b64_tr_b16 v[148:149], v8 offset:17408\n"
        "ds_read_b64_tr_b16 v[150:151], v8 offset:17664\n"
        "ds_read_b64_tr_b16 v[152:153], v8 offset:18432\n"
        "ds_read_b64_tr_b16 v[154:155], v8 offset:18688\n"
        "ds_read_b64_tr_b16 v[156:157], v8 offset:19456\n"
        "ds_read_b64_tr_b16 v[158:159], v8 offset:19712\n"
        "s_waitcnt lgkmcnt(8)\n"
        "s_add_u32 m0, s82, 12288\n"
        "s_mov_b32 s65, 1572864\n"
        "buffer_load_dwordx4 v6, s[44:47], s65 offen lds\n"
        "s_mov_b32 s65, 1702912\n"
        "buffer_load_dwordx4 v6, s[44:47], s65 offen offset:1024 lds\n"
        "s_add_u32 m0, s82, 14336\n"
        "s_mov_b32 s65, 1835008\n"
        "buffer_load_dwordx4 v6, s[44:47], s65 offen lds\n"
        "s_mov_b32 s65, 1965056\n"
        "buffer_load_dwordx4 v6, s[44:47], s65 offen offset:1024 lds\n"
        "s_waitcnt vmcnt(24)\n"
        "ds_read_b64_tr_b16 v[160:161], v8 offset:20480\n"
        "ds_read_b64_tr_b16 v[162:163], v8 offset:20736\n"
        "ds_read_b64_tr_b16 v[164:165], v8 offset:21504\n"
        "ds_read_b64_tr_b16 v[166:167], v8 offset:21760\n"
        "ds_read_b64_tr_b16 v[168:169], v8 offset:22528\n"
        "ds_read_b64_tr_b16 v[170:171], v8 offset:22784\n"
        "ds_read_b64_tr_b16 v[172:173], v8 offset:23552\n"
        "ds_read_b64_tr_b16 v[174:175], v8 offset:23808\n"
        "s_waitcnt lgkmcnt(8)\n"
        "s_add_u32 m0, s82, 16384\n"
        "s_mov_b32 s65, 2097152\n"
        "buffer_load_dwordx4 v6, s[44:47], s65 offen lds\n"
        "s_mov_b32 s65, 2227200\n"
        "buffer_load_dwordx4 v6, s[44:47], s65 offen offset:1024 lds\n"
        "s_add_u32 m0, s82, 18432\n"
        "s_mov_b32 s65, 2359296\n"
        "buffer_load_dwordx4 v6, s[44:47], s65 offen lds\n"
        "s_mov_b32 s65, 2489344\n"
        "buffer_load_dwordx4 v6, s[44:47], s65 offen offset:1024 lds\n"
        "s_waitcnt vmcnt(24)\n"
        "ds_read_b64_tr_b16 v[176:177], v8 offset:24576\n"
        "ds_read_b64_tr_b16 v[178:179], v8 offset:24832\n"
        "ds_read_b64_tr_b16 v[180:181], v8 offset:25600\n"
        "ds_read_b64_tr_b16 v[182:183], v8 offset:25856\n"
        "ds_read_b64_tr_b16 v[184:185], v8 offset:26624\n"
        "ds_read_b64_tr_b16 v[186:187], v8 offset:26880\n"
        "ds_read_b64_tr_b16 v[188:189], v8 offset:27648\n"
        "ds_read_b64_tr_b16 v[190:191], v8 offset:27904\n"
        "s_waitcnt lgkmcnt(8)\n"
        "s_add_u32 m0, s82, 20480\n"
        "s_mov_b32 s65, 2621440\n"
        "buffer_load_dwordx4 v6, s[44:47], s65 offen lds\n"
        "s_mov_b32 s65, 2751488\n"
        "buffer_load_dwordx4 v6, s[44:47], s65 offen offset:1024 lds\n"
        "s_add_u32 m0, s82, 22528\n"
        "s_mov_b32 s65, 2883584\n"
        "buffer_load_dwordx4 v6, s[44:47], s65 offen lds\n"
        "s_mov_b32 s65, 3013632\n"
        "buffer_load_dwordx4 v6, s[44:47], s65 offen offset:1024 lds\n"
        "s_waitcnt vmcnt(24)\n"
        "ds_read_b64_tr_b16 v[192:193], v8 offset:28672\n"
        "ds_read_b64_tr_b16 v[194:195], v8 offset:28928\n"
        "ds_read_b64_tr_b16 v[196:197], v8 offset:29696\n"
        "ds_read_b64_tr_b16 v[198:199], v8 offset:29952\n"
        "ds_read_b64_tr_b16 v[200:201], v8 offset:30720\n"
        "ds_read_b64_tr_b16 v[202:203], v8 offset:30976\n"
        "ds_read_b64_tr_b16 v[204:205], v8 offset:31744\n"
        "ds_read_b64_tr_b16 v[206:207], v8 offset:32000\n"
        "s_waitcnt lgkmcnt(8)\n"
        "s_add_u32 m0, s82, 24576\n"
        "s_mov_b32 s65, 3145728\n"
        "buffer_load_dwordx4 v6, s[44:47], s65 offen lds\n"
        "s_mov_b32 s65, 3275776\n"
        "buffer_load_dwordx4 v6, s[44:47], s65 offen offset:1024 lds\n"
        "s_add_u32 m0, s82, 26624\n"
        "s_mov_b32 s65, 3407872\n"
        "buffer_load_dwordx4 v6, s[44:47], s65 offen lds\n"
        "s_mov_b32 s65, 3537920\n"
        "buffer_load_dwordx4 v6, s[44:47], s65 offen offset:1024 lds\n"
        "s_waitcnt vmcnt(24)\n"
        "ds_read_b64_tr_b16 v[208:209], v8 offset:0\n"
        "ds_read_b64_tr_b16 v[210:211], v8 offset:256\n"
        "ds_read_b64_tr_b16 v[212:213], v8 offset:1024\n"
        "ds_read_b64_tr_b16 v[214:215], v8 offset:1280\n"
        "ds_read_b64_tr_b16 v[216:217], v8 offset:2048\n"
        "ds_read_b64_tr_b16 v[218:219], v8 offset:2304\n"
        "ds_read_b64_tr_b16 v[220:221], v8 offset:3072\n"
        "ds_read_b64_tr_b16 v[222:223], v8 offset:3328\n"
        "s_waitcnt lgkmcnt(8)\n"
        "s_add_u32 m0, s82, 28672\n"
        "s_mov_b32 s65, 3670016\n"
        "buffer_load_dwordx4 v6, s[44:47], s65 offen lds\n"
        "s_mov_b32 s65, 3800064\n"
        "buffer_load_dwordx4 v6, s[44:47], s65 offen offset:1024 lds\n"
        "s_add_u32 m0, s82, 30720\n"
        "s_mov_b32 s65, 3932160\n"
        "buffer_load_dwordx4 v6, s[44:47], s65 offen lds\n"
        "s_mov_b32 s65, 4062208\n"
        "buffer_load_dwordx4 v6, s[44:47], s65 offen offset:1024 lds\n"
        "s_waitcnt vmcnt(24)\n"
        "ds_read_b64_tr_b16 v[224:225], v8 offset:4096\n"
        "ds_read_b64_tr_b16 v[226:227], v8 offset:4352\n"
        "ds_read_b64_tr_b16 v[228:229], v8 offset:5120\n"
        "ds_read_b64_tr_b16 v[230:231], v8 offset:5376\n"
        "ds_read_b64_tr_b16 v[232:233], v8 offset:6144\n"
        "ds_read_b64_tr_b16 v[234:235], v8 offset:6400\n"
        "ds_read_b64_tr_b16 v[236:237], v8 offset:7168\n"
        "ds_read_b64_tr_b16 v[238:239], v8 offset:7424\n"
        "s_waitcnt lgkmcnt(8)\n"
        "s_add_u32 m0, s82, 0\n"
        "s_mov_b32 s65, 0\n"
        "buffer_load_dwordx4 v6, s[44:47], s65 offen lds\n"
        "s_mov_b32 s65, 130048\n"
        "buffer_load_dwordx4 v6, s[44:47], s65 offen offset:1024 lds\n"
        "s_add_u32 m0, s82, 2048\n"
        "s_mov_b32 s65, 262144\n"
        "buffer_load_dwordx4 v6, s[44:47], s65 offen lds\n"
        "s_mov_b32 s65, 392192\n"
        "buffer_load_dwordx4 v6, s[44:47], s65 offen offset:1024 lds\n"
        "s_waitcnt vmcnt(24)\n"
        "ds_read_b64_tr_b16 v[240:241], v10 offset:8192\n"
        "ds_read_b64_tr_b16 v[242:243], v10 offset:8448\n"
        "ds_read_b64_tr_b16 v[244:245], v10 offset:9216\n"
        "ds_read_b64_tr_b16 v[246:247], v10 offset:9472\n"
        "ds_read_b64_tr_b16 v[248:249], v10 offset:8224\n"
        "ds_read_b64_tr_b16 v[250:251], v10 offset:8480\n"
        "ds_read_b64_tr_b16 v[252:253], v10 offset:9248\n"
        "ds_read_b64_tr_b16 v[254:255], v10 offset:9504\n"
        "s_waitcnt lgkmcnt(8)\n"
        "s_add_u32 m0, s82, 4096\n"
        "s_mov_b32 s65, 524288\n"
        "buffer_load_dwordx4 v6, s[44:47], s65 offen lds\n"
        "s_mov_b32 s65, 654336\n"
        "buffer_load_dwordx4 v6, s[44:47], s65 offen offset:1024 lds\n"
        "s_add_u32 m0, s82, 6144\n"
        "s_mov_b32 s65, 786432\n"
        "buffer_load_dwordx4 v6, s[44:47], s65 offen lds\n"
        "s_mov_b32 s65, 916480\n"
        "buffer_load_dwordx4 v6, s[44:47], s65 offen offset:1024 lds\n"
        "s_waitcnt vmcnt(24)\n"
        "ds_read_b64_tr_b16 v[40:41], v10 offset:12288\n"
        "ds_read_b64_tr_b16 v[42:43], v10 offset:12544\n"
        "ds_read_b64_tr_b16 v[44:45], v10 offset:13312\n"
        "ds_read_b64_tr_b16 v[46:47], v10 offset:13568\n"
        "ds_read_b64_tr_b16 v[48:49], v10 offset:12320\n"
        "ds_read_b64_tr_b16 v[50:51], v10 offset:12576\n"
        "ds_read_b64_tr_b16 v[52:53], v10 offset:13344\n"
        "ds_read_b64_tr_b16 v[54:55], v10 offset:13600\n"
        "s_waitcnt lgkmcnt(8)\n"
        "s_add_u32 m0, s82, 8192\n"
        "s_mov_b32 s65, 1048576\n"
        "buffer_load_dwordx4 v6, s[44:47], s65 offen lds\n"
        "s_mov_b32 s65, 1178624\n"
        "buffer_load_dwordx4 v6, s[44:47], s65 offen offset:1024 lds\n"
        "s_add_u32 m0, s82, 10240\n"
        "s_mov_b32 s65, 1310720\n"
        "buffer_load_dwordx4 v6, s[44:47], s65 offen lds\n"
        "s_mov_b32 s65, 1440768\n"
        "buffer_load_dwordx4 v6, s[44:47], s65 offen offset:1024 lds\n"
        "s_waitcnt vmcnt(24)\n"
        "ds_read_b64_tr_b16 v[24:25], v10 offset:16384\n"
        "ds_read_b64_tr_b16 v[26:27], v10 offset:16640\n"
        "ds_read_b64_tr_b16 v[28:29], v10 offset:17408\n"
        "ds_read_b64_tr_b16 v[30:31], v10 offset:17664\n"
        "ds_read_b64_tr_b16 v[32:33], v10 offset:16416\n"
        "ds_read_b64_tr_b16 v[34:35], v10 offset:16672\n"
        "ds_read_b64_tr_b16 v[36:37], v10 offset:17440\n"
        "ds_read_b64_tr_b16 v[38:39], v10 offset:17696\n"
        "s_waitcnt lgkmcnt(8)\n"
        "ds_write_b128 v2, v[40:43] offset:12288\n"
        "ds_write_b128 v2, v[44:47] offset:13312\n"
        "ds_write_b128 v2, v[48:51] offset:14336\n"
        "ds_write_b128 v2, v[52:55] offset:15360\n"
        "s_waitcnt vmcnt(20)\n"
        "ds_read_b64_tr_b16 v[40:41], v10 offset:20480\n"
        "ds_read_b64_tr_b16 v[42:43], v10 offset:20736\n"
        "ds_read_b64_tr_b16 v[44:45], v10 offset:21504\n"
        "ds_read_b64_tr_b16 v[46:47], v10 offset:21760\n"
        "ds_read_b64_tr_b16 v[48:49], v10 offset:20512\n"
        "ds_read_b64_tr_b16 v[50:51], v10 offset:20768\n"
        "ds_read_b64_tr_b16 v[52:53], v10 offset:21536\n"
        "ds_read_b64_tr_b16 v[54:55], v10 offset:21792\n"
        "s_waitcnt lgkmcnt(8)\n"
        "ds_write_b128 v2, v[24:27] offset:16384\n"
        "ds_write_b128 v2, v[28:31] offset:17408\n"
        "ds_write_b128 v2, v[32:35] offset:18432\n"
        "ds_write_b128 v2, v[36:39] offset:19456\n"
        "s_waitcnt vmcnt(16)\n"
        "ds_read_b64_tr_b16 v[24:25], v10 offset:24576\n"
        "ds_read_b64_tr_b16 v[26:27], v10 offset:24832\n"
        "ds_read_b64_tr_b16 v[28:29], v10 offset:25600\n"
        "ds_read_b64_tr_b16 v[30:31], v10 offset:25856\n"
        "ds_read_b64_tr_b16 v[32:33], v10 offset:24608\n"
        "ds_read_b64_tr_b16 v[34:35], v10 offset:24864\n"
        "ds_read_b64_tr_b16 v[36:37], v10 offset:25632\n"
        "ds_read_b64_tr_b16 v[38:39], v10 offset:25888\n"
        "s_waitcnt lgkmcnt(8)\n"
        "ds_write_b128 v2, v[40:43] offset:20480\n"
        "ds_write_b128 v2, v[44:47] offset:21504\n"
        "ds_write_b128 v2, v[48:51] offset:22528\n"
        "ds_write_b128 v2, v[52:55] offset:23552\n"
        "s_waitcnt vmcnt(12)\n"
        "ds_read_b64_tr_b16 v[40:41], v10 offset:28672\n"
        "ds_read_b64_tr_b16 v[42:43], v10 offset:28928\n"
        "ds_read_b64_tr_b16 v[44:45], v10 offset:29696\n"
        "ds_read_b64_tr_b16 v[46:47], v10 offset:29952\n"
        "ds_read_b64_tr_b16 v[48:49], v10 offset:28704\n"
        "ds_read_b64_tr_b16 v[50:51], v10 offset:28960\n"
        "ds_read_b64_tr_b16 v[52:53], v10 offset:29728\n"
        "ds_read_b64_tr_b16 v[54:55], v10 offset:29984\n"
        "s_waitcnt lgkmcnt(8)\n"
        "ds_write_b128 v2, v[24:27] offset:24576\n"
        "ds_write_b128 v2, v[28:31] offset:25600\n"
        "ds_write_b128 v2, v[32:35] offset:26624\n"
        "ds_write_b128 v2, v[36:39] offset:27648\n"
        "s_waitcnt vmcnt(8)\n"
        "ds_read_b64_tr_b16 v[24:25], v10 offset:0\n"
        "ds_read_b64_tr_b16 v[26:27], v10 offset:256\n"
        "ds_read_b64_tr_b16 v[28:29], v10 offset:1024\n"
        "ds_read_b64_tr_b16 v[30:31], v10 offset:1280\n"
        "ds_read_b64_tr_b16 v[32:33], v10 offset:32\n"
        "ds_read_b64_tr_b16 v[34:35], v10 offset:288\n"
        "ds_read_b64_tr_b16 v[36:37], v10 offset:1056\n"
        "ds_read_b64_tr_b16 v[38:39], v10 offset:1312\n"
        "s_waitcnt lgkmcnt(8)\n"
        "ds_write_b128 v2, v[40:43] offset:28672\n"
        "ds_write_b128 v2, v[44:47] offset:29696\n"
        "ds_write_b128 v2, v[48:51] offset:30720\n"
        "ds_write_b128 v2, v[52:55] offset:31744\n"
        "s_waitcnt vmcnt(4)\n"
        "ds_read_b64_tr_b16 v[40:41], v10 offset:4096\n"
        "ds_read_b64_tr_b16 v[42:43], v10 offset:4352\n"
        "ds_read_b64_tr_b16 v[44:45], v10 offset:5120\n"
        "ds_read_b64_tr_b16 v[46:47], v10 offset:5376\n"
        "ds_read_b64_tr_b16 v[48:49], v10 offset:4128\n"
        "ds_read_b64_tr_b16 v[50:51], v10 offset:4384\n"
        "ds_read_b64_tr_b16 v[52:53], v10 offset:5152\n"
        "ds_read_b64_tr_b16 v[54:55], v10 offset:5408\n"
        "s_waitcnt lgkmcnt(8)\n"
        "ds_write_b128 v2, v[24:27] offset:0\n"
        "ds_write_b128 v2, v[28:31] offset:1024\n"
        "ds_write_b128 v2, v[32:35] offset:2048\n"
        "ds_write_b128 v2, v[36:39] offset:3072\n"
        "s_waitcnt vmcnt(0)\n"
        "ds_read_b64_tr_b16 v[24:25], v10 offset:8192\n"
        "ds_read_b64_tr_b16 v[26:27], v10 offset:8448\n"
        "ds_read_b64_tr_b16 v[28:29], v10 offset:9216\n"
        "ds_read_b64_tr_b16 v[30:31], v10 offset:9472\n"
        "ds_read_b64_tr_b16 v[32:33], v10 offset:8224\n"
        "ds_read_b64_tr_b16 v[34:35], v10 offset:8480\n"
        "ds_read_b64_tr_b16 v[36:37], v10 offset:9248\n"
        "ds_read_b64_tr_b16 v[38:39], v10 offset:9504\n"
        "s_waitcnt lgkmcnt(8)\n"
        "ds_write_b128 v2, v[40:43] offset:4096\n"
        "ds_write_b128 v2, v[44:47] offset:5120\n"
        "ds_write_b128 v2, v[48:51] offset:6144\n"
        "ds_write_b128 v2, v[52:55] offset:7168\n"
        "s_waitcnt lgkmcnt(0)\n"
        "ds_write_b128 v2, v[24:27] offset:8192\n"
        "ds_write_b128 v2, v[28:31] offset:9216\n"
        "ds_write_b128 v2, v[32:35] offset:10240\n"
        "ds_write_b128 v2, v[36:39] offset:11264\n"
        "s_cmp_lt_u32 s48, 2\n"
        "s_cbranch_scc0 .Lch_noex2_%=\n"
        "ds_read_b64_tr_b16 v[40:41], v8 offset:32768\n"
        "ds_read_b64_tr_b16 v[42:43], v8 offset:33024\n"
        "ds_read_b64_tr_b16 v[44:45], v8 offset:33792\n"
        "ds_read_b64_tr_b16 v[46:47], v8 offset:34048\n"
        "ds_read_b64_tr_b16 v[48:49], v8 offset:34816\n"
        "ds_read_b64_tr_b16 v[50:51], v8 offset:35072\n"
        "ds_read_b64_tr_b16 v[52:53], v8 offset:35840\n"
        "ds_read_b64_tr_b16 v[54:55], v8 offset:36096\n"
        "ds_read_b64_tr_b16 v[56:57], v8 offset:36864\n"
        "ds_read_b64_tr_b16 v[58:59], v8 offset:37120\n"
        "ds_read_b64_tr_b16 v[60:61], v8 offset:37888\n"
        "ds_read_b64_tr_b16 v[62:63], v8 offset:38144\n"
        "ds_read_b64_tr_b16 v[64:65], v8 offset:38912\n"
        "ds_read_b64_tr_b16 v[66:67], v8 offset:39168\n"
        "ds_read_b64_tr_b16 v[68:69], v8 offset:39936\n"
        "ds_read_b64_tr_b16 v[70:71], v8 offset:40192\n"
        "s_waitcnt lgkmcnt(0)\n"
        "ds_write_b128 v2, v[40:43] offset:32768\n"
        "ds_write_b128 v2, v[44:47] offset:33792\n"
        "ds_write_b128 v2, v[48:51] offset:34816\n"
        "ds_write_b128 v2, v[52:55] offset:35840\n"
        "ds_write_b128 v2, v[56:59] offset:36864\n"
        "ds_write_b128 v2, v[60:63] offset:37888\n"
        "ds_write_b128 v2, v[64:67] offset:38912\n"
        "ds_write_b128 v2, v[68:71] offset:39936\n"
        ".Lch_noex2_%=:\n"
        "s_waitcnt lgkmcnt(0)\n"
        "v_lshrrev_b32 v12, 4, v3\n"
        "s_lshl_b32 s66, s57, 7\n"
        "s_mov_b32 s64, 0\n"
        ".Lch_pair_top_%=:\n"
        "s_mov_b64 exec, 1\n"
        "buffer_load_dword v13, off, s[40:43], s66 sc1\n"
        "s_mov_b64 exec, -1\n"
        "s_waitcnt vmcnt(0)\n"
        "v_cmp_gt_i32 vcc, 0, v13\n"
        "s_nop 0\n"
        "s_and_b64 vcc, vcc, 1\n"
        "s_cbranch_vccz .Lch_pair_ok_%=\n"
        "s_add_u32 s64, s64, 1\n"
        "s_and_b32 s65, s64, 255\n"
        "s_cbranch_scc1 .Lch_pair_go_%=\n"
        "s_mov_b32 s65, 40960\n"
        "s_cmp_lt_u32 s64, 1048576\n"
        "s_cbranch_scc1 .Lch_pair_chk_%=\n"
        "v_mov_b32 v13, 1\n"
        "buffer_store_dword v13, off, s[40:43], s65 sc1\n"
        "s_waitcnt vmcnt(0)\n"
        ".Lch_pair_chk_%=:\n"
        "buffer_load_dword v13, off, s[40:43], s65 sc1\n"
        "s_waitcnt vmcnt(0)\n"
        "v_readfirstlane_b32 s65, v13\n"
        "s_nop 0\n"
        "s_cmp_lg_u32 s65, 1\n"
        "s_cbranch_scc1 .Lch_pair_go_%=\n"
        "s_endpgm\n"
        ".Lch_pair_go_%=:\n"
        "s_sleep 2\n"
        "s_branch .Lch_pair_top_%=\n"
        ".Lch_pair_ok_%=:\n"
        "v_readfirstlane_b32 s50, v13\n"
        "s_nop 0\n"
        "s_xor_b32 s50, s50, s56\n"
        "v_lshlrev_b32 v14, 7, v12\n"
        "s_sub_u32 s66, 0, s50\n"
        "s_mov_b32 s52, 24576\n"
        "s_mov_b32 s64, 0\n"
        ".Lch_pg0_top_%=:\n"
        "buffer_load_dword v13, v14, s[40:43], s52 offen sc1\n"
        "s_waitcnt vmcnt(0)\n"
        "v_cmp_gt_i32 vcc, 0, v13\n"
        "s_cbranch_vccz .Lch_pg0_ok_%=\n"
        "s_add_u32 s64, s64, 1\n"
        "s_and_b32 s65, s64, 255\n"
        "s_cbranch_scc1 .Lch_pg0_go_%=\n"
        "s_mov_b32 s65, 40960\n"
        "s_cmp_lt_u32 s64, 1048576\n"
        "s_cbranch_scc1 .Lch_pg0_chk_%=\n"
        "v_mov_b32 v13, 1\n"
        "buffer_store_dword v13, off, s[40:43], s65 sc1\n"
        "s_waitcnt vmcnt(0)\n"
        ".Lch_pg0_chk_%=:\n"
        "buffer_load_dword v13, off, s[40:43], s65 sc1\n"
        "s_waitcnt vmcnt(0)\n"
        "v_readfirstlane_b32 s65, v13\n"
        "s_nop 0\n"
        "s_cmp_lg_u32 s65, 1\n"
        "s_cbranch_scc1 .Lch_pg0_go_%=\n"
        "s_endpgm\n"
        ".Lch_pg0_go_%=:\n"
        "s_sleep 2\n"
        "s_branch .Lch_pg0_top_%=\n"
        ".Lch_pg0_ok_%=:\n"
        "v_cmp_eq_u32 s[52:53], 1, v13\n"
        "s_nop 1\n"
        "s_mov_b32 s54, 32768\n"
        "s_mov_b32 s64, 0\n"
        ".Lch_pg1_top_%=:\n"
        "buffer_load_dword v13, v14, s[40:43], s54 offen sc1\n"
        "s_waitcnt vmcnt(0)\n"
        "v_cmp_gt_i32 vcc, 0, v13\n"
        "s_cbranch_vccz .Lch_pg1_ok_%=\n"
        "s_add_u32 s64, s64, 1\n"
        "s_and_b32 s65, s64, 255\n"
        "s_cbranch_scc1 .Lch_pg1_go_%=\n"
        "s_mov_b32 s65, 40960\n"
        "s_cmp_lt_u32 s64, 1048576\n"
        "s_cbranch_scc1 .Lch_pg1_chk_%=\n"
        "v_mov_b32 v13, 1\n"
        "buffer_store_dword v13, off, s[40:43], s65 sc1\n"
        "s_waitcnt vmcnt(0)\n"
        ".Lch_pg1_chk_%=:\n"
        "buffer_load_dword v13, off, s[40:43], s65 sc1\n"
        "s_waitcnt vmcnt(0)\n"
        "v_readfirstlane_b32 s65, v13\n"
        "s_nop 0\n"
        "s_cmp_lg_u32 s65, 1\n"
        "s_cbranch_scc1 .Lch_pg1_go_%=\n"
        "s_endpgm\n"
        ".Lch_pg1_go_%=:\n"
        "s_sleep 2\n"
        "s_branch .Lch_pg1_top_%=\n"
        ".Lch_pg1_ok_%=:\n"
        "v_cmp_eq_u32 s[54:55], 1, v13\n"
        "s_nop 1\n"
        "s_mov_b32 vcc_lo, 0x55555555\n"
        "s_mov_b32 vcc_hi, 0x55555555\n"
        "s_xor_b64 vcc, vcc, s[52:53]\n"
        "s_bcnt1_i32_b64 s65, vcc\n"
        "s_sub_u32 s64, s65, 23\n"
        "s_cmp_lt_u32 s64, 19\n"
        "s_cbranch_scc1 .Lch_rawmap_%=\n"
        "s_cmp_lt_u32 s65, 32\n"
        "s_cselect_b32 s65, 0, -1\n"
        "s_xor_b32 s52, s65, 0x55555555\n"
        "s_xor_b32 s53, s65, 0x55555555\n"
        "s_xor_b32 s54, s65, 0x55555555\n"
        "s_xor_b32 s55, s65, 0xaaaaaaaa\n"
        ".Lch_rawmap_%=:\n"
        "s_xor_b32 s52, s52, s66\n"
        "s_xor_b32 s53, s53, s66\n"
        "s_xor_b32 s54, s54, s66\n"
        "s_xor_b32 s55, s55, s66\n"
        "s_mov_b32 s58, 0xff00ff00\n"
        "s_mov_b32 s59, 0xff00ff00\n"
        "s_and_b32 s67, s55, 1\n"
        "s_lshl_b32 s67, s67, 13\n"
        "s_lshl_b32 s65, s49, 2\n"
        "s_add_u32 s65, s65, s48\n"
        "s_lshl_b32 s65, s65, 7\n"
        "s_lshr_b32 s66, s65, 13\n"
        "s_lshl_b32 s66, s66, 13\n"
        "s_add_u32 s66, s66, s65\n"
        "s_add_u32 s66, s66, s67\n"
        "s_add_u32 s78, s66, 114688\n"
        "s_add_u32 s79, s66, 180224\n"
        "s_lshl_b32 s83, s49, 14\n"
        "s_lshl_b32 s65, s48, 11\n"
        "s_add_u32 s83, s83, s65\n"
        "s_add_u32 s83, s83, 524288\n"
        "s_lshl_b32 s65, s49, 12\n"
        "s_lshr_b32 s84, s65, 13\n"
        "s_lshl_b32 s84, s84, 13\n"
        "s_add_u32 s84, s84, s65\n"
        "s_lshl_b32 s65, s48, 9\n"
        "s_add_u32 s84, s84, s65\n"
        "s_add_u32 s84, s84, 1572864\n"
        "s_lshr_b32 s65, s49, 2\n"
        "s_add_u32 s65, s65, 64\n"
        "s_cmp_eq_u32 s48, 2\n"
        "s_cselect_b32 s65, s49, s65\n"
        "s_add_u32 s66, s49, 32\n"
        "s_cmp_eq_u32 s48, 3\n"
        "s_cselect_b32 s65, s66, s65\n"
        "s_lshl_b32 s85, s65, 7\n"
        "s_cmp_eq_u32 s48, 0\n"
        "s_cselect_b32 s66, 57344, 0\n"
        "s_add_u32 s85, s85, s66\n"
        "s_add_u32 s85, s85, s67\n"
        "s_add_u32 s85, s85, 1671168\n"
        "s_lshr_b32 s66, s49, 3\n"
        "s_lshl_b32 s54, s66, 14\n"
        "s_lshl_b32 s55, s66, 10\n"
        "s_and_b32 s66, s48, 1\n"
        "s_lshl_b32 s66, s66, 12\n"
        "s_add_u32 s54, s54, s66\n"
        "s_add_u32 s66, s67, 1671168\n"
        "s_add_u32 s54, s54, s66\n"
        "s_add_u32 s55, s55, s66\n"
        "s_add_u32 s55, s55, 65536\n"
        "s_add_u32 s72, s85, 245760\n"
        "s_lshl_b32 s66, s65, 12\n"
        "s_lshr_b32 s87, s66, 13\n"
        "s_lshl_b32 s87, s87, 13\n"
        "s_add_u32 s87, s87, s66\n"
        "s_and_b32 s65, s49, 3\n"
        "s_or_b32 s88, s65, s48\n"
        "s_lshl_b32 s86, s48, 16\n"
        "s_lshl_b32 s65, s49, 7\n"
        "s_add_u32 s86, s86, s65\n"
        "s_add_u32 s67, s67, 491520\n"
        "s_add_u32 s86, s86, s67\n"
        "s_add_u32 s87, s87, s67\n"
        "s_mov_b32 s73, s86\n"
        "s_add_u32 s74, s73, 262144\n"
        "s_mov_b32 s76, s87\n"
        "s_lshl_b32 s65, s48, 15\n"
        "s_sub_u32 s75, s73, s65\n"
        "s_add_u32 s75, s75, 524288\n"
        "s_lshl_b32 s65, s48, 3\n"
        "s_lshr_b32 s60, s60, s65\n"
        "s_and_b32 s60, s60, 255\n"
        "s_sub_u32 s60, s60, 32\n"
        "s_lshr_b32 s34, s61, s65\n"
        "s_and_b32 s34, s34, 255\n"
        "s_lshr_b32 s61, s62, s65\n"
        "s_and_b32 s61, s61, 255\n"
        "s_mov_b32 s35, 90\n"
        "s_lshl_b32 s33, s48, 10\n"
        "s_lshl_b32 s32, s48, 2\n"
        "s_lshr_b32 s65, s48, 1\n"
        "s_lshl_b32 s31, s65, 10\n"
        "s_and_b32 s65, s48, 1\n"
        "s_lshl_b32 s65, s65, 6\n"
        "s_add_u32 s31, s31, s65\n"
        "s_mov_b32 s51, -1\n"
        "s_mov_b32 s80, -2\n"
        "s_lshl_b32 s62, s49, 7\n"
        "s_cmp_eq_u32 s48, 1\n"
        "s_cselect_b32 s62, s62, 2147483648\n"
        "s_bfe_u32 s56, s63, 0x80008\n"
        "s_lshr_b32 s57, s63, 16\n"
        "s_and_b32 s63, s63, 255\n"
        "s_mov_b32 s44, 0x3f803f80\n"
        "s_mov_b32 s45, 0x3f803f80\n"
        "s_branch .Lch_idle2_%=\n"
        ".Lch_Q_%=:\n"
        "ds_read_b32 v7, v5 offset:0\n"
        "ds_read_b128 v[32:35], v1 offset:0\n"
        "ds_read_b128 v[36:39], v1 offset:1024\n"
        "s_waitcnt lgkmcnt(2)\n"
        "v_cmp_eq_u32 vcc, -1, v7\n"
        "s_cbranch_vccnz .Lch_rt0_%=\n"
        ".Lch_ok0_%=:\n"
        "s_waitcnt lgkmcnt(1)\n"
        "v_smfmac_f32_16x16x64_bf16 v[8:11], v[32:35], a[0:7], v0\n"
        "buffer_load_dwordx4 v[64:67], v3, s[40:43], s30 offen offset:0 sc1\n"
        "v_max3_u32 v6, v6, v32, v33\n"
        "v_smfmac_f32_16x16x64_bf16 v[16:19], v[32:35], a[144:151], v0\n"
        "v_smfmac_f32_16x16x64_bf16 v[24:27], v[32:35], v[40:47], v0\n"
        "ds_read_b128 v[40:43], v2 offset:6144\n"
        "v_max3_u32 v6, v6, v34, v35\n"
        "v_smfmac_f32_16x16x64_bf16 v[12:15], v[32:35], a[8:15], v0\n"
        "ds_read_b128 v[44:47], v2 offset:7168\n"
        "buffer_load_dwordx4 v[68:71], v3, s[40:43], s30 offen offset:1024 sc1\n"
        "v_smfmac_f32_16x16x64_bf16 v[20:23], v[32:35], a[152:159], v0\n"
        "s_waitcnt lgkmcnt(2)\n"
        "v_smfmac_f32_16x16x64_bf16 v[28:31], v[32:35], v[48:55], v0\n"
        "ds_read_b128 v[48:51], v2 offset:8192\n"
        "v_smfmac_f32_16x16x64_bf16 v[8:11], v[36:39], a[16:23], v0\n"
        "ds_read_b128 v[32:35], v1 offset:2048\n"
        "v_max3_u32 v6, v6, v36, v37\n"
        "v_smfmac_f32_16x16x64_bf16 v[16:19], v[36:39], a[160:167], v0\n"
        "ds_read_b128 v[52:55], v2 offset:9216\n"
        "v_smfmac_f32_16x16x64_bf16 v[24:27], v[36:39], v[56:63], v0\n"
        "ds_read_b128 v[56:59], v2 offset:10240\n"
        "v_max3_u32 v6, v6, v38, v39\n"
        "v_smfmac_f32_16x16x64_bf16 v[12:15], v[36:39], a[24:31], v0\n"
        "ds_read_b128 v[60:63], v2 offset:11264\n"
        "v_smfmac_f32_16x16x64_bf16 v[20:23], v[36:39], a[168:175], v0\n"
        "s_waitcnt lgkmcnt(3)\n"
        "v_smfmac_f32_16x16x64_bf16 v[28:31], v[36:39], v[40:47], v0\n"
        "ds_read_b128 v[40:43], v2 offset:12288\n"
        "v_smfmac_f32_16x16x64_bf16 v[8:11], v[32:35], a[32:39], v0\n"
        "ds_read_b128 v[36:39], v1 offset:3072\n"
        "v_max3_u32 v6, v6, v32, v33\n"
        "v_smfmac_f32_16x16x64_bf16 v[16:19], v[32:35], a[176:183], v0\n"
        "ds_read_b128 v[44:47], v2 offset:13312\n"
        "s_waitcnt lgkmcnt(5)\n"
        "v_smfmac_f32_16x16x64_bf16 v[24:27], v[32:35], v[48:55], v0\n"
        "ds_read_b128 v[48:51], v2 offset:14336\n"
        "v_max3_u32 v6, v6, v34, v35\n"
        "v_smfmac_f32_16x16x64_bf16 v[12:15], v[32:35], a[40:47], v0\n"
        "ds_read_b128 v[52:55], v2 offset:15360\n"
        "buffer_load_dwordx4 v[72:75], v3, s[40:43], s89 offen offset:0 sc1\n"
        "v_smfmac_f32_16x16x64_bf16 v[20:23], v[32:35], a[184:191], v0\n"
        "s_waitcnt lgkmcnt(3)\n"
        "v_smfmac_f32_16x16x64_bf16 v[28:31], v[32:35], v[56:63], v0\n"
        "ds_read_b128 v[56:59], v2 offset:16384\n"
        "v_smfmac_f32_16x16x64_bf16 v[8:11], v[36:39], a[48:55], v0\n"
        "s_waitcnt vmcnt(4)\n"
        "s_barrier\n"
        "ds_read_b32 v7, v5 offset:4096\n"
        "ds_read_b128 v[32:35], v1 offset:4096\n"
        "v_max3_u32 v6, v6, v36, v37\n"
        "v_smfmac_f32_16x16x64_bf16 v[16:19], v[36:39], a[192:199], v0\n"
        "ds_read_b128 v[60:63], v2 offset:17408\n"
        "s_waitcnt lgkmcnt(6)\n"
        "v_smfmac_f32_16x16x64_bf16 v[24:27], v[36:39], v[40:47], v0\n"
        "ds_read_b128 v[40:43], v2 offset:18432\n"
        "v_max3_u32 v6, v6, v38, v39\n"
        "v_smfmac_f32_16x16x64_bf16 v[12:15], v[36:39], a[56:63], v0\n"
        "ds_read_b128 v[44:47], v2 offset:19456\n"
        "v_smfmac_f32_16x16x64_bf16 v[20:23], v[36:39], a[200:207], v0\n"
        "s_waitcnt lgkmcnt(3)\n"
        "v_cmp_eq_u32 vcc, -1, v7\n"
        "v_smfmac_f32_16x16x64_bf16 v[28:31], v[36:39], v[48:55], v0\n"
        "ds_read_b128 v[48:51], v2 offset:20480\n"
        "s_cbranch_vccnz .Lch_rt1_%=\n"
        ".Lch_ok1_%=:\n"
        "v_smfmac_f32_16x16x64_bf16 v[8:11], v[32:35], a[64:71], v0\n"
        "ds_read_b128 v[36:39], v1 offset:5120\n"
        "v_max3_u32 v6, v6, v32, v33\n"
        "v_smfmac_f32_16x16x64_bf16 v[16:19], v[32:35], a[208:215], v0\n"
        "ds_read_b128 v[52:55], v2 offset:21504\n"
        "s_waitcnt lgkmcnt(5)\n"
        "v_smfmac_f32_16x16x64_bf16 v[24:27], v[32:35], v[56:63], v0\n"
        "ds_read_b128 v[56:59], v2 offset:22528\n"
        "v_max3_u32 v6, v6, v34, v35\n"
        "v_smfmac_f32_16x16x64_bf16 v[12:15], v[32:35], a[72:79], v0\n"
        "ds_read_b128 v[60:63], v2 offset:23552\n"
        "buffer_load_dwordx4 v[76:79], v3, s[40:43], s89 offen offset:1024 sc1\n"
        "v_smfmac_f32_16x16x64_bf16 v[20:23], v[32:35], a[216:223], v0\n"
        "s_waitcnt lgkmcnt(3)\n"
        "v_smfmac_f32_16x16x64_bf16 v[28:31], v[32:35], v[40:47], v0\n"
        "ds_read_b128 v[40:43], v2 offset:24576\n"
        "v_smfmac_f32_16x16x64_bf16 v[8:11], v[36:39], a[80:87], v0\n"
        "ds_read_b128 v[32:35], v1 offset:6144\n"
        "v_max3_u32 v6, v6, v36, v37\n"
        "v_smfmac_f32_16x16x64_bf16 v[16:19], v[36:39], a[224:231], v0\n"
        "ds_read_b128 v[44:47], v2 offset:25600\n"
        "s_waitcnt lgkmcnt(5)\n"
        "v_smfmac_f32_16x16x64_bf16 v[24:27], v[36:39], v[48:55], v0\n"
        "ds_read_b128 v[48:51], v2 offset:26624\n"
        "v_max3_u32 v6, v6, v38, v39\n"
        "v_smfmac_f32_16x16x64_bf16 v[12:15], v[36:39], a[88:95], v0\n"
        "ds_read_b128 v[52:55], v2 offset:27648\n"
        "v_smfmac_f32_16x16x64_bf16 v[20:23], v[36:39], a[232:239], v0\n"
        "s_waitcnt lgkmcnt(3)\n"
        "v_smfmac_f32_16x16x64_bf16 v[28:31], v[36:39], v[56:63], v0\n"
        "ds_read_b128 v[56:59], v2 offset:28672\n"
        "s_waitcnt lgkmcnt(3)\n"
        "v_smfmac_f32_16x16x64_bf16 v[24:27], v[32:35], v[40:47], v0\n"
        "ds_read_b128 v[36:39], v1 offset:7168\n"
        "s_waitcnt lgkmcnt(2)\n"
        "v_smfmac_f32_16x16x64_bf16 v[28:31], v[32:35], v[48:55], v0\n"
        "s_waitcnt vmcnt(4)\n"
        "s_barrier\n"
        "ds_read_b32 v7, v5 offset:8192\n"
        "v_smfmac_f32_16x16x64_bf16 v[8:11], v[32:35], a[96:103], v0\n"
        "ds_read_b128 v[48:51], v1 offset:8192\n"
        "v_max3_u32 v6, v6, v32, v33\n"
        "v_smfmac_f32_16x16x64_bf16 v[16:19], v[32:35], a[240:247], v0\n"
        "ds_read_b128 v[52:55], v4 offset:9216\n"
        "v_max3_u32 v6, v6, v34, v35\n"
        "v_smfmac_f32_16x16x64_bf16 v[12:15], v[32:35], a[104:111], v0\n"
        "ds_read_b32 v40, v4 offset:11264\n"
        "s_memrealtime s[36:37]\n"
        "v_smfmac_f32_16x16x64_bf16 v[20:23], v[32:35], a[248:255], v0\n"
        "s_waitcnt lgkmcnt(5)\n"
        "v_smfmac_f32_16x16x64_bf16 v[8:11], v[36:39], a[112:119], v0\n"
        "v_max3_u32 v6, v6, v36, v37\n"
        "v_smfmac_f32_16x16x64_bf16 v[12:15], v[36:39], a[120:127], v0\n"
        "s_waitcnt lgkmcnt(0)\n"
        "v_cmp_eq_u32 vcc, -1, v7\n"
        "s_add_u32 s37, s36, s35\n"
        "s_and_b64 vcc, vcc, 0xffff\n"
        "s_cbranch_vccnz .Lch_rt2_%=\n"
        ".Lch_ok2_%=:\n"
        "v_smfmac_f32_16x16x64_bf16 v[8:11], v[48:51], a[128:135], v0\n"
        "ds_read_b128 v[44:47], v2 offset:31744\n"
        "v_readfirstlane_b32 s81, v40\n"
        "v_smfmac_f32_16x16x64_bf16 v[12:15], v[48:51], a[136:143], v0\n"
        "s_cmp_eq_u32 s81, s51\n"
        "v_and_b32 v7, -2, s37\n"
        "v_smfmac_f32_16x16x64_bf16 v[16:19], v[36:39], v[80:87], v0\n"
        "s_cselect_b32 s65, 2147483648, s46\n"
        "v_cvt_pk_bf16_f32 v8, v8, v9\n"
        "v_smfmac_f32_16x16x64_bf16 v[16:19], v[48:51], v[96:103], v0\n"
        "buffer_store_dword v7, off, s[40:43], s65 sc1\n"
        "v_cvt_pk_bf16_f32 v9, v10, v11\n"
        "v_smfmac_f32_16x16x64_bf16 v[20:23], v[36:39], v[88:95], v0\n"
        "ds_read_b128 v[60:63], v2 offset:29696\n"
        "v_cvt_pk_bf16_f32 v12, v12, v13\n"
        "v_smfmac_f32_16x16x64_bf16 v[20:23], v[48:51], v[104:111], v0\n"
        "ds_read_b128 v[40:43], v2 offset:30720\n"
        "v_cvt_pk_bf16_f32 v13, v14, v15\n"
        "v_mfma_f32_16x16x16_bf16 v[32:35], v[8:9], v[52:53], 0\n"
        "v_cvt_pk_bf16_f32 v16, v16, v17\n"
        "v_cvt_pk_bf16_f32 v17, v18, v19\n"
        "v_mfma_f32_16x16x16_bf16 v[8:11], v[12:13], v[52:53], 0\n"
        "v_cvt_pk_bf16_f32 v20, v20, v21\n"
        "v_cvt_pk_bf16_f32 v21, v22, v23\n"
        "v_mfma_f32_16x16x16_bf16 v[12:15], v[16:17], v[54:55], 0\n"
        "v_cvt_pk_bf16_f32 v32, v32, v33\n"
        "v_cvt_pk_bf16_f32 v33, v34, v35\n"
        "v_mfma_f32_16x16x16_bf16 v[16:19], v[20:21], v[54:55], 0\n"
        "v_cvt_pk_bf16_f32 v34, v8, v9\n"
        "v_cvt_pk_bf16_f32 v35, v10, v11\n"
        "s_cbranch_scc1 .Lch_slow_%=\n"
        ".Lch_back_%=:\n"
        "s_waitcnt vmcnt(3)\n"
        "v_mfma_f32_16x16x32_bf16 v[8:11], v[64:67], v[32:35], 0\n"
        "v_cvt_pk_bf16_f32 v12, v12, v13\n"
        "v_cvt_pk_bf16_f32 v13, v14, v15\n"
        "v_mfma_f32_16x16x32_bf16 v[20:23], v[68:71], v[32:35], 0\n"
        "v_cvt_pk_bf16_f32 v14, v16, v17\n"
        "v_cvt_pk_bf16_f32 v15, v18, v19\n"
        "v_add_u32 v52, s33, v4\n"
        "v_add_u32 v53, s32, v4\n"
        "v_mfma_f32_16x16x32_bf16 v[16:19], v[12:15], v[32:35], 0\n"
        "v_max3_u32 v6, v6, v38, v39\n"
        "v_max3_u32 v6, v6, v48, v49\n"
        "v_max3_u32 v6, v6, v50, v51\n"
        "v_mov_b32_dpp v8, v20 quad_perm:[0,1,2,3] row_mask:0xf bank_mask:0xc\n"
        "v_mov_b32_dpp v9, v21 quad_perm:[0,1,2,3] row_mask:0xf bank_mask:0xc\n"
        "v_mov_b32_dpp v10, v22 quad_perm:[0,1,2,3] row_mask:0xf bank_mask:0xc\n"
        "v_mov_b32_dpp v11, v23 quad_perm:[0,1,2,3] row_mask:0xf bank_mask:0xc\n"
        "ds_write_b128 v52, v[8:11]\n"
        "v_mov_b32_dpp v16, v17 quad_perm:[0,1,2,3] row_mask:0xf bank_mask:0xc\n"
        "ds_write_b32 v53, v16 offset:4096\n"
        "v_cmp_eq_u32 vcc, -1, v6\n"
        "s_waitcnt lgkmcnt(2)\n"
        "s_cbranch_vccnz .Lch_redo_%=\n"
        "v_smfmac_f32_16x16x64_bf16 v[24:27], v[36:39], v[56:63], v0\n"
        "ds_read_b128 v[68:71], v4 offset:10240\n"
        "v_mov_b64 v[52:53], s[44:45]\n"
        "v_smfmac_f32_16x16x64_bf16 v[28:31], v[36:39], v[40:47], v0\n"
        "v_mov_b64 v[54:55], s[44:45]\n"
        "v_smfmac_f32_16x16x64_bf16 v[24:27], v[48:51], v[240:247], v0\n"
        "v_and_b32 v7, 0xffffffbf, v1\n"
        "v_smfmac_f32_16x16x64_bf16 v[28:31], v[48:51], v[248:255], v0\n"
        "s_waitcnt lgkmcnt(0)\n"
        "s_barrier\n"
        "ds_read_b128 v[32:35], v4 offset:0\n"
        "ds_read_b128 v[36:39], v4 offset:1024\n"
        "ds_read_b128 v[40:43], v4 offset:2048\n"
        "ds_read_b128 v[44:47], v4 offset:3072\n"
        "ds_read_b128 v[48:51], v4 offset:4096\n"
        "buffer_store_dwordx4 v[12:15], v68, s[40:43], s70 offen sc1\n"
        "v_add_u32 v71, s31, v71\n"
        "v_add_f32 v24, v24, v27\n"
        "v_add_f32 v25, v25, v26\n"
        "v_add_f32 v28, v28, v31\n"
        "v_add_f32 v29, v29, v30\n"
        "v_cvt_pk_bf16_f32 v30, v24, v25\n"
        "v_cvt_pk_bf16_f32 v31, v29, v28\n"
        "s_waitcnt lgkmcnt(3)\n"
        "v_pk_add_f32 v[32:33], v[32:33], v[36:37]\n"
        "v_pk_add_f32 v[34:35], v[34:35], v[38:39]\n"
        "s_waitcnt lgkmcnt(1)\n"
        "v_pk_add_f32 v[40:41], v[40:41], v[44:45]\n"
        "v_pk_add_f32 v[42:43], v[42:43], v[46:47]\n"
        "v_pk_add_f32 v[32:33], v[32:33], v[40:41]\n"
        "v_pk_add_f32 v[34:35], v[34:35], v[42:43]\n"
        "s_waitcnt lgkmcnt(0)\n"
        "v_pk_add_f32 v[48:49], v[48:49], v[50:51]\n"
        "v_max3_f32 v36, v32, v33, v34\n"
        "v_add_f32 v48, v48, v49\n"
        "v_max3_f32 v36, v36, v35, v48\n"
        "v_mul_f32 v48, 0x3e0293ee, v48\n"
        "v_mul_f32 v32, 0x3e0293ee, v32\n"
        "v_max_f32_dpp v37, v36, v36 row_ror:8 row_mask:0xf bank_mask:0xf\n"
        "v_max_f32_dpp v38, v36, v36 row_ror:8 row_mask:0xf bank_mask:0xf\n"
        "v_mul_f32 v33, 0x3e0293ee, v33\n"
        "v_mul_f32 v34, 0x3e0293ee, v34\n"
        "v_permlane16_swap_b32 v37, v38\n"
        "v_max_f32 v36, v37, v38\n"
        "v_max_f32 v39, v37, v38\n"
        "v_mul_f32 v35, 0x3e0293ee, v35\n"
        "v_lshlrev_b32 v6, 3, v3\n"
        "v_permlane32_swap_b32 v36, v39\n"
        "v_max_f32 v36, v36, v39\n"
        "v_fmamk_f32 v48, v36, 0xbe0293ee, v48\n"
        "v_exp_f32 v48, v48\n"
        "v_fmamk_f32 v32, v36, 0xbe0293ee, v32\n"
        "v_fmamk_f32 v33, v36, 0xbe0293ee, v33\n"
        "v_mov_b32_dpp v49, v48 row_ror:8 row_mask:0xf bank_mask:0xf\n"
        "v_exp_f32 v32, v32\n"
        "v_cvt_pk_bf16_f32 v49, v48, v49\n"
        "v_exp_f32 v33, v33\n"
        "v_cndmask_b32 v44, v49, 0, s[58:59]\n"
        "v_cndmask_b32 v45, 0, v49, s[58:59]\n"
        "v_fmamk_f32 v34, v36, 0xbe0293ee, v34\n"
        "v_fmamk_f32 v35, v36, 0xbe0293ee, v35\n"
        "v_mfma_f32_16x16x16_bf16 v[56:59], v[52:53], v[44:45], 0\n"
        "v_exp_f32 v34, v34\n"
        "v_exp_f32 v35, v35\n"
        "v_cvt_pk_bf16_f32 v40, v32, v33\n"
        "v_mfma_f32_16x16x16_bf16 v[60:63], v[30:31], v[44:45], 0\n"
        "v_cvt_pk_bf16_f32 v41, v34, v35\n"
        "v_mov_b32_dpp v42, v40 row_ror:8 row_mask:0xf bank_mask:0xf\n"
        "s_waitcnt vmcnt(2)\n"
        "v_mov_b32_dpp v43, v41 row_ror:8 row_mask:0xf bank_mask:0xf\n"
        "v_swap_b32 v76, v78\n"
        "v_swap_b32 v77, v79\n"
        "v_mfma_f32_16x16x32_bf16 v[56:59], v[52:55], v[40:43], v[56:59]\n"
        "v_mov_b64 v[10:11], 0\n"
        "v_mov_b64 v[12:13], 0\n"
        "v_mfma_f32_16x16x32_bf16 v[32:35], v[72:75], v[40:43], v[60:63]\n"
        "s_mov_b32 exec_hi, 0\n"
        "buffer_load_dword v6, v6, s[40:43], s47 offen sc1\n"
        "s_mov_b32 exec_hi, -1\n"
        "v_mfma_f32_16x16x32_bf16 v[48:51], v[76:79], v[40:43], v[60:63]\n"
        "v_rcp_f32 v36, v56\n"
        "v_mov_b64 v[14:15], 0\n"
        "v_mov_b64 v[16:17], 0\n"
        "v_mov_b64 v[18:19], 0\n"
        "v_mul_f32 v32, v32, v36\n"
        "v_mul_f32 v33, v33, v36\n"
        "v_mul_f32 v34, v34, v36\n"
        "v_mul_f32 v35, v35, v36\n"
        "v_mul_f32_dpp v32, v48, v36 quad_perm:[0,1,2,3] row_mask:0xf bank_mask:0xc\n"
        "v_mul_f32_dpp v33, v49, v36 quad_perm:[0,1,2,3] row_mask:0xf bank_mask:0xc\n"
        "v_mul_f32_dpp v34, v50, v36 quad_perm:[0,1,2,3] row_mask:0xf bank_mask:0xc\n"
        "v_mul_f32_dpp v35, v51, v36 quad_perm:[0,1,2,3] row_mask:0xf bank_mask:0xc\n"
        "v_cvt_pk_bf16_f32 v44, v32, v33\n"
        "v_cvt_pk_bf16_f32 v45, v34, v35\n"
        "ds_write_b64 v71, v[44:45]\n"
        "v_cvt_pk_bf16_f32 v24, v24, v25\n"
        "v_cvt_pk_bf16_f32 v26, v28, v29\n"
        "v_mov_b64 v[8:9], 0\n"
        "v_mov_b64 v[20:21], 0\n"
        "v_mov_b64 v[22:23], 0\n"
        "s_waitcnt lgkmcnt(0)\n"
        "s_barrier\n"
        "ds_read_b128 v[28:31], v7 offset:5120\n"
        "ds_read_b128 v[32:35], v7 offset:5184\n"
        "ds_read_b128 v[36:39], v7 offset:6144\n"
        "ds_read_b128 v[40:43], v7 offset:6208\n"
        "v_permlane16_swap_b32 v24, v25\n"
        "v_permlane16_swap_b32 v26, v27\n"
        "buffer_store_dwordx4 v[24:27], v69, s[40:43], s70 offen offset:2048 sc1\n"
        "s_waitcnt lgkmcnt(3)\n"
        "v_smfmac_f32_16x16x64_bf16 v[8:11], v[28:31], v[112:119], v0\n"
        "s_waitcnt lgkmcnt(2)\n"
        "v_smfmac_f32_16x16x64_bf16 v[8:11], v[32:35], v[120:127], v0\n"
        "v_mov_b64 v[24:25], 0\n"
        "s_waitcnt lgkmcnt(1)\n"
        "v_smfmac_f32_16x16x64_bf16 v[8:11], v[36:39], v[128:135], v0\n"
        "v_mov_b64 v[26:27], 0\n"
        "s_waitcnt lgkmcnt(0)\n"
        "v_smfmac_f32_16x16x64_bf16 v[8:11], v[40:43], v[136:143], v0\n"
        "s_barrier\n"
        "v_smfmac_f32_16x16x64_bf16 v[12:15], v[28:31], v[144:151], v0\n"
        "ds_read_b128 v[44:47], v2 offset:32768\n"
        "v_smfmac_f32_16x16x64_bf16 v[12:15], v[32:35], v[152:159], v0\n"
        "ds_read_b128 v[48:51], v2 offset:33792\n"
        "v_cvt_pk_bf16_f32 v8, v8, v9\n"
        "v_smfmac_f32_16x16x64_bf16 v[12:15], v[36:39], v[160:167], v0\n"
        "ds_read_b128 v[52:55], v2 offset:34816\n"
        "v_cvt_pk_bf16_f32 v9, v11, v10\n"
        "v_smfmac_f32_16x16x64_bf16 v[12:15], v[40:43], v[168:175], v0\n"
        "ds_read_b128 v[56:59], v2 offset:35840\n"
        "v_bfi_b32 v8, -2, v8, s51\n"
        "v_smfmac_f32_16x16x64_bf16 v[16:19], v[28:31], v[176:183], v0\n"
        "ds_read_b128 v[60:63], v2 offset:36864\n"
        "v_bfi_b32 v9, -2, v9, s51\n"
        "v_smfmac_f32_16x16x64_bf16 v[16:19], v[32:35], v[184:191], v0\n"
        "v_cvt_pk_bf16_f32 v10, v12, v13\n"
        "v_cvt_pk_bf16_f32 v11, v15, v14\n"
        "v_bfi_b32 v10, -2, v10, s51\n"
        "v_bfi_b32 v11, -2, v11, s51\n"
        "buffer_store_dwordx4 v[8:11], v70, s[40:43], s73 offen sc1\n"
        "v_smfmac_f32_16x16x64_bf16 v[16:19], v[36:39], v[192:199], v0\n"
        "ds_read_b128 v[64:67], v2 offset:37888\n"
        "v_smfmac_f32_16x16x64_bf16 v[16:19], v[40:43], v[200:207], v0\n"
        "ds_read_b128 v[72:75], v2 offset:38912\n"
        "v_smfmac_f32_16x16x64_bf16 v[20:23], v[28:31], v[208:215], v0\n"
        "ds_read_b128 v[76:79], v2 offset:39936\n"
        "v_smfmac_f32_16x16x64_bf16 v[20:23], v[32:35], v[216:223], v0\n"
        "v_cvt_pk_bf16_f32 v16, v16, v17\n"
        "v_smfmac_f32_16x16x64_bf16 v[20:23], v[36:39], v[224:231], v0\n"
        "s_cmp_lt_u32 s48, 2\n"
        "v_cvt_pk_bf16_f32 v17, v19, v18\n"
        "v_smfmac_f32_16x16x64_bf16 v[20:23], v[40:43], v[232:239], v0\n"
        "v_bfi_b32 v16, -2, v16, s51\n"
        "s_cbranch_scc0 .Lch_R23_%=\n"
        "s_waitcnt lgkmcnt(0)\n"
        "v_smfmac_f32_16x16x64_bf16 v[24:27], v[28:31], v[44:51], v0\n"
        "v_bfi_b32 v17, -2, v17, s51\n"
        "v_smfmac_f32_16x16x64_bf16 v[24:27], v[32:35], v[52:59], v0\n"
        "v_cvt_pk_bf16_f32 v18, v20, v21\n"
        "v_cvt_pk_bf16_f32 v19, v23, v22\n"
        "v_bfi_b32 v18, -2, v18, s51\n"
        "v_bfi_b32 v19, -2, v19, s51\n"
        "buffer_store_dwordx4 v[16:19], v70, s[40:43], s74 offen sc1\n"
        "v_smfmac_f32_16x16x64_bf16 v[24:27], v[36:39], v[60:67], v0\n"
        "v_smfmac_f32_16x16x64_bf16 v[24:27], v[40:43], v[72:79], v0\n"
        "v_add_u32 v12, 114688, v3\n"
        "ds_read_b128 v[44:47], v12\n"
        "ds_read_b32 v50, v12 offset:1024\n"
        "s_waitcnt vmcnt(3)\n"
        "s_nop 3\n"
        "v_cvt_pk_bf16_f32 v24, v24, v25\n"
        "v_cvt_pk_bf16_f32 v25, v27, v26\n"
        "v_bfi_b32 v24, -2, v24, s51\n"
        "v_bfi_b32 v25, -2, v25, s51\n"
        "s_nop 0\n"
        "v_permlane32_swap_b32 v24, v26\n"
        "v_permlane32_swap_b32 v25, v27\n"
        "s_mov_b32 exec_hi, 0\n"
        "buffer_store_dwordx4 v[24:27], v70, s[40:43], s75 offen sc1\n"
        "s_memrealtime s[90:91]\n"
        "s_cmp_eq_u32 s88, 0\n"
        "s_cbranch_scc1 .Lch_R_%=\n"
        "s_mov_b32 exec_hi, -1\n"
        "s_waitcnt lgkmcnt(0)\n"
        "s_sub_u32 s65, s90, s36\n"
        "s_min_u32 s65, s65, 120\n"
        "s_lshl_b32 s65, s65, 3\n"
        "s_cmp_eq_u32 s51, 0\n"
        "s_cselect_b32 s38, s65, s38\n"
        "s_sub_u32 s65, s65, s38\n"
        "s_ashr_i32 s65, s65, 2\n"
        "s_add_u32 s38, s38, s65\n"
        "s_lshr_b32 s35, s38, 3\n"
        "s_waitcnt vmcnt(3)\n"
        "v_mov_b32 v12, s51\n"
        "s_mov_b64 exec, 1\n"
        "buffer_store_dword v12, off, s[40:43], s79 sc1\n"
        "s_mov_b64 exec, -1\n"
        "s_mov_b32 s65, s63\n"
        ".Lch_d4_%=:\n"
        "s_sub_u32 s65, s65, 1\n"
        "s_cbranch_scc0 .Lch_d4_%=\n"
        ".Lch_idle2_%=:\n"
        "s_mov_b64 exec, 1\n"
        "buffer_load_dword v7, off, s[40:43], s78 sc1\n"
        "s_mov_b64 exec, -1\n"
        "s_waitcnt vmcnt(0)\n"
        "s_branch .Lch_nog_%=\n"
        ".Lch_R23_%=:\n"
        "s_waitcnt vmcnt(2)\n"
        "v_bfi_b32 v17, -2, v17, s51\n"
        "s_nop 3\n"
        "v_cvt_pk_bf16_f32 v18, v20, v21\n"
        "v_cvt_pk_bf16_f32 v19, v23, v22\n"
        "v_bfi_b32 v18, -2, v18, s51\n"
        "v_bfi_b32 v19, -2, v19, s51\n"
        "buffer_store_dwordx4 v[16:19], v70, s[40:43], s74 offen sc1\n"
        "s_memrealtime s[90:91]\n"
        "s_mov_b32 exec_hi, 0\n"
        ".Lch_R_%=:\n"
        ".Lch_st1_%=:\n"
        "v_cmp_eq_u32 vcc, -1, v6\n"
        "s_cbranch_vccnz .Lch_stwait_%=\n"
        ".Lch_st2_%=:\n"
        "s_waitcnt lgkmcnt(0)\n"
        "s_sub_u32 s65, s90, s60\n"
        "s_memrealtime s[90:91]\n"
        "v_sub_u32 v12, s65, v6\n"
        "v_cmp_lt_u32 vcc, 0xfffffc18, v12\n"
        "s_cbranch_vccnz .Lch_st2_%=\n"
        "s_mov_b32 exec_hi, -1\n"
        ".Lch_rl_%=:\n"
        "buffer_load_dwordx4 v[28:31], v3, s[40:43], s76 offen offset:0 sc1\n"
        "buffer_load_dwordx4 v[32:35], v3, s[40:43], s76 offen offset:1024 sc1\n"
        "buffer_load_dwordx4 v[36:39], v3, s[40:43], s76 offen offset:2048 sc1\n"
        "buffer_load_dwordx4 v[40:43], v3, s[40:43], s76 offen offset:3072 sc1\n"
        "s_mov_b64 exec, 1\n"
        "buffer_load_dword v7, off, s[40:43], s78 sc1\n"
        "s_mov_b64 exec, -1\n"
        "v_subrev_u32 v8, s36, v6\n"
        "v_subrev_u32 v9, s36, v6\n"
        "s_nop 0\n"
        "v_max_i32_dpp v8, v8, v8 row_ror:8 row_mask:0xf bank_mask:0xf\n"
        "v_min_i32_dpp v9, v9, v9 row_ror:8 row_mask:0xf bank_mask:0xf\n"
        "s_nop 0\n"
        "v_max_i32_dpp v8, v8, v8 row_ror:4 row_mask:0xf bank_mask:0xf\n"
        "v_min_i32_dpp v9, v9, v9 row_ror:4 row_mask:0xf bank_mask:0xf\n"
        "s_nop 0\n"
        "v_max_i32_dpp v8, v8, v8 row_ror:2 row_mask:0xf bank_mask:0xf\n"
        "v_min_i32_dpp v9, v9, v9 row_ror:2 row_mask:0xf bank_mask:0xf\n"
        "s_nop 0\n"
        "v_max_i32_dpp v8, v8, v8 row_ror:1 row_mask:0xf bank_mask:0xf\n"
        "v_min_i32_dpp v9, v9, v9 row_ror:1 row_mask:0xf bank_mask:0xf\n"
        "s_waitcnt lgkmcnt(0)\n"
        "v_readlane_b32 s65, v8, 0\n"
        "v_readlane_b32 s66, v8, 16\n"
        "v_readlane_b32 s90, v9, 0\n"
        "v_readlane_b32 s91, v9, 16\n"
        "s_max_i32 s65, s65, s66\n"
        "s_min_i32 s90, s90, s91\n"
        "s_sub_u32 s90, s65, s90\n"
        "s_add_u32 s65, s65, s36\n"
        "s_add_u32 s65, s65, s34\n"
        "s_cmp_gt_u32 s90, s56\n"
        "s_cselect_b32 s90, s57, 0\n"
        "s_add_u32 s82, s65, s90\n"
        "s_and_b32 s71, s51, 3\n"
        "s_mul_i32 s71, s71, 81920\n"
        "s_add_u32 s39, s71, s85\n"
        "v_mov_b64 v[64:65], -1\n"
        "v_mov_b64 v[66:67], -1\n"
        "v_mov_b32 v9, s51\n"
        "s_waitcnt vmcnt(4)\n"
        "v_mfma_f32_16x16x32_bf16 v[12:15], v[28:31], v[44:47], 0\n"
        "v_bitop3_b32 v10, v28, v29, s51 bitop3:0x7e\n"
        "v_bitop3_b32 v11, v30, v31, s51 bitop3:0x7e\n"
        "v_or_b32 v8, v10, v11\n"
        "s_waitcnt vmcnt(3)\n"
        "v_mfma_f32_16x16x32_bf16 v[12:15], v[32:35], v[44:47], v[12:15]\n"
        "v_bitop3_b32 v10, v32, v33, s51 bitop3:0x7e\n"
        "v_bitop3_b32 v11, v34, v35, s51 bitop3:0x7e\n"
        "v_or3_b32 v8, v8, v10, v11\n"
        "s_waitcnt vmcnt(2)\n"
        "v_mfma_f32_16x16x32_bf16 v[12:15], v[36:39], v[44:47], v[12:15]\n"
        "v_bitop3_b32 v10, v36, v37, s51 bitop3:0x7e\n"
        "v_bitop3_b32 v11, v38, v39, s51 bitop3:0x7e\n"
        "v_or3_b32 v8, v8, v10, v11\n"
        "s_waitcnt vmcnt(1)\n"
        "v_mfma_f32_16x16x32_bf16 v[12:15], v[40:43], v[44:47], v[12:15]\n"
        "v_bitop3_b32 v10, v40, v41, s51 bitop3:0x7e\n"
        "v_bitop3_b32 v11, v42, v43, s51 bitop3:0x7e\n"
        "v_or3_b32 v8, v8, v10, v11\n"
        "v_and_b32 v8, 1, v8\n"
        "v_cmp_ne_u32 vcc, 0, v8\n"
        "s_cbranch_vccnz .Lch_rretry_%=\n"
        "s_nop 1\n"
        "v_permlane32_swap_b32 v12, v14\n"
        "v_permlane32_swap_b32 v13, v15\n"
        "v_add_f32 v12, v12, v14\n"
        "v_add_f32 v13, v13, v15\n"
        "v_cvt_pk_bf16_f32 v16, v12, v13\n"
        "v_cvt_pk_bf16_f32 v18, v12, v13\n"
        "s_nop 1\n"
        "v_permlane16_swap_b32 v16, v18\n"
        "s_nop 1\n"
        "v_permlane32_swap_b32 v16, v17\n"
        "v_permlane32_swap_b32 v18, v19\n"
        "buffer_store_dwordx4 v[16:19], v50, s[40:43], s39 offen sc1\n"
        "s_mov_b64 exec, 1\n"
        "buffer_store_dword v9, off, s[40:43], s79 sc1\n"
        "s_mov_b64 exec, -1\n"
        "buffer_store_dwordx4 v[64:67], v50, s[40:43], s72 offen sc1\n"
        "s_mov_b32 s72, s39\n"
        "s_waitcnt vmcnt(3)\n"
        "v_mov_b32 v32, v49\n"
        "s_cmp_eq_u32 s48, 2\n"
        "s_cbranch_scc0 .Lch_nog_%=\n"
        "s_and_b32 s66, s33, 1024\n"
        "s_lshl_b32 s66, s66, 2\n"
        "s_add_u32 m0, s66, 151552\n"
        "s_add_u32 s66, s54, s71\n"
        ".Lch_gwa_%=:\n"
        "s_memrealtime s[90:91]\n"
        "s_waitcnt lgkmcnt(0)\n"
        "s_sub_u32 s65, s90, s82\n"
        "s_cmp_gt_u32 s65, -1000\n"
        "s_cbranch_scc1 .Lch_gwa_%=\n"
        "s_mov_b32 s65, s61\n"
        ".Lch_d2a_%=:\n"
        "s_sub_u32 s65, s65, 1\n"
        "s_cbranch_scc0 .Lch_d2a_%=\n"
        "buffer_load_dwordx4 v3, s[40:43], s66 offen offset:0 sc1 lds\n"
        "buffer_load_dwordx4 v3, s[40:43], s66 offen offset:1024 sc1 lds\n"
        "buffer_load_dwordx4 v3, s[40:43], s66 offen offset:2048 sc1 lds\n"
        "buffer_load_dwordx4 v3, s[40:43], s66 offen offset:3072 sc1 lds\n"
        ".Lch_nog_%=:\n"
        "s_add_u32 s51, s51, 1\n"
        "s_add_u32 s80, s80, 3\n"
        "s_bitcmp1_b64 s[52:53], s51\n"
        "s_cselect_b32 s65, 8192, 0\n"
        "s_lshl_b32 s68, s51, 21\n"
        "s_add_u32 s68, s68, s65\n"
        "s_add_u32 s68, s68, 2097152\n"
        "s_add_u32 s70, s68, s84\n"
        "s_add_u32 s69, s68, s83\n"
        "s_add_u32 s47, s68, 2068480\n"
        "s_add_u32 s46, s47, s62\n"
        "ds_read_b128 v[40:43], v2 offset:0\n"
        "ds_read_b128 v[44:47], v2 offset:1024\n"
        "ds_read_b128 v[48:51], v2 offset:2048\n"
        "ds_read_b128 v[52:55], v2 offset:3072\n"
        "ds_read_b128 v[56:59], v2 offset:4096\n"
        "ds_read_b128 v[60:63], v2 offset:5120\n"
        "v_mov_b64 v[8:9], 0\n"
        "v_mov_b64 v[10:11], 0\n"
        "v_mov_b64 v[12:13], 0\n"
        "v_mov_b64 v[14:15], 0\n"
        "v_mov_b64 v[16:17], 0\n"
        "v_mov_b64 v[18:19], 0\n"
        "v_mov_b64 v[20:21], 0\n"
        "v_mov_b64 v[22:23], 0\n"
        "v_mov_b64 v[24:25], 0\n"
        "v_mov_b64 v[26:27], 0\n"
        "v_mov_b64 v[28:29], 0\n"
        "v_mov_b64 v[30:31], 0\n"
        "v_mov_b32 v6, 0\n"
        "v_readfirstlane_b32 s65, v7\n"
        "s_cmp_eq_u32 s51, 63\n"
        "s_cselect_b32 s65, -2, s65\n"
        "s_cmp_ge_i32 s65, s80\n"
        "s_cselect_b32 s30, s69, 2147483648\n"
        "s_cbranch_scc1 .Lch_fastb_%=\n"
        "v_mov_b32 v7, s51\n"
        "ds_write_b32 v4, v7 offset:11264\n"
        ".Lch_fastb_%=:\n"
        "s_add_u32 s89, s30, 524288\n"
        "s_cmp_lt_u32 s48, 2\n"
        "s_cbranch_scc1 .Lch_A_%=\n"
        "s_cmp_eq_u32 s48, 2\n"
        "s_cbranch_scc1 .Lch_A2_%=\n"
        "s_cmp_eq_u32 s51, 0\n"
        "s_cbranch_scc1 .Lch_A_%=\n"
        "s_and_b32 s66, s33, 1024\n"
        "s_lshl_b32 s66, s66, 2\n"
        "s_add_u32 m0, s66, 151552\n"
        "s_add_u32 s66, s54, s71\n"
        ".Lch_gwb_%=:\n"
        "s_memrealtime s[90:91]\n"
        "s_waitcnt lgkmcnt(0)\n"
        "s_sub_u32 s65, s90, s82\n"
        "s_cmp_gt_u32 s65, -1000\n"
        "s_cbranch_scc1 .Lch_gwb_%=\n"
        "s_mov_b32 s65, s61\n"
        ".Lch_d2b_%=:\n"
        "s_sub_u32 s65, s65, 1\n"
        "s_cbranch_scc0 .Lch_d2b_%=\n"
        "buffer_load_dwordx4 v3, s[40:43], s66 offen offset:0 sc1 lds\n"
        "buffer_load_dwordx4 v3, s[40:43], s66 offen offset:1024 sc1 lds\n"
        "buffer_load_dwordx4 v3, s[40:43], s66 offen offset:2048 sc1 lds\n"
        "buffer_load_dwordx4 v3, s[40:43], s66 offen offset:3072 sc1 lds\n"
        "s_mov_b32 m0, 159744\n"
        "s_add_u32 s66, s71, s55\n"
        "buffer_load_dwordx4 v32, s[40:43], s66 offen sc1 lds\n"
        "s_branch .Lch_A_%=\n"
        ".Lch_A2_%=:\n"
        "s_waitcnt vmcnt(0)\n"
        ".Lch_A_%=:\n"
        "s_waitcnt lgkmcnt(0)\n"
        "s_barrier\n"
        "s_branch .Lch_Q_%=\n"
        ".Lch_rretry_%=:\n"
        "s_waitcnt vmcnt(0)\n"
        "s_add_u32 s64, s64, 1\n"
        "s_and_b32 s65, s64, 255\n"
        "s_cbranch_scc1 .Lch_rr_go_%=\n"
        "s_mov_b32 s65, 40960\n"
        "s_cmp_lt_u32 s64, 1048576\n"
        "s_cbranch_scc1 .Lch_rr_chk_%=\n"
        "v_mov_b32 v8, 1\n"
        "buffer_store_dword v8, off, s[40:43], s65 sc1\n"
        "s_waitcnt vmcnt(0)\n"
        ".Lch_rr_chk_%=:\n"
        "buffer_load_dword v8, off, s[40:43], s65 sc1\n"
        "s_waitcnt vmcnt(0)\n"
        "v_readfirstlane_b32 s65, v8\n"
        "s_nop 0\n"
        "s_cmp_lg_u32 s65, 1\n"
        "s_cbranch_scc1 .Lch_rr_go_%=\n"
        "s_endpgm\n"
        ".Lch_rr_go_%=:\n"
        "s_branch .Lch_rl_%=\n"
        ".Lch_stwait_%=:\n"
        "s_add_u32 s64, s64, 1\n"
        "s_and_b32 s65, s64, 255\n"
        "s_cbranch_scc1 .Lch_sw_go_%=\n"
        "s_mov_b32 s65, 40960\n"
        "s_cmp_lt_u32 s64, 1048576\n"
        "s_cbranch_scc1 .Lch_sw_chk_%=\n"
        "v_mov_b32 v12, 1\n"
        "buffer_store_dword v12, off, s[40:43], s65 sc1\n"
        "s_waitcnt vmcnt(0)\n"
        ".Lch_sw_chk_%=:\n"
        "buffer_load_dword v12, off, s[40:43], s65 sc1\n"
        "s_waitcnt vmcnt(0)\n"
        "v_readfirstlane_b32 s65, v12\n"
        "s_nop 0\n"
        "s_cmp_lg_u32 s65, 1\n"
        "s_cbranch_scc1 .Lch_sw_go_%=\n"
        "s_endpgm\n"
        ".Lch_sw_go_%=:\n"
        "s_sleep 1\n"
        "v_lshlrev_b32 v6, 3, v3\n"
        "buffer_load_dword v6, v6, s[40:43], s47 offen sc1\n"
        "s_waitcnt vmcnt(0)\n"
        "s_branch .Lch_st1_%=\n"
        ".Lch_rt0_%=:\n"
        "s_waitcnt lgkmcnt(0)\n"
        "s_barrier\n"
        "s_add_u32 s64, s64, 1\n"
        "s_and_b32 s65, s64, 255\n"
        "s_cbranch_scc1 .Lch_rt0_go_%=\n"
        "s_mov_b32 s65, 40960\n"
        "s_cmp_lt_u32 s64, 1048576\n"
        "s_cbranch_scc1 .Lch_rt0_chk_%=\n"
        "v_mov_b32 v7, 1\n"
        "buffer_store_dword v7, off, s[40:43], s65 sc1\n"
        "s_waitcnt vmcnt(0)\n"
        ".Lch_rt0_chk_%=:\n"
        "buffer_load_dword v7, off, s[40:43], s65 sc1\n"
        "s_waitcnt vmcnt(0)\n"
        "v_readfirstlane_b32 s65, v7\n"
        "s_nop 0\n"
        "s_cmp_lg_u32 s65, 1\n"
        "s_cbranch_scc1 .Lch_rt0_go_%=\n"
        "s_endpgm\n"
        ".Lch_rt0_go_%=:\n"
        "s_cmp_eq_u32 s48, 2\n"
        "s_cbranch_scc0 .Lch_rw0_%=\n"
        "s_and_b32 s66, s33, 1024\n"
        "s_lshl_b32 s66, s66, 2\n"
        "s_add_u32 m0, s66, 151552\n"
        "s_add_u32 s66, s54, s71\n"
        "buffer_load_dwordx4 v3, s[40:43], s66 offen offset:0 sc1 lds\n"
        "buffer_load_dwordx4 v3, s[40:43], s66 offen offset:1024 sc1 lds\n"
        "buffer_load_dwordx4 v3, s[40:43], s66 offen offset:2048 sc1 lds\n"
        "buffer_load_dwordx4 v3, s[40:43], s66 offen offset:3072 sc1 lds\n"
        "s_waitcnt vmcnt(0)\n"
        ".Lch_rw0_%=:\n"
        "s_barrier\n"
        "ds_read_b32 v7, v5 offset:0\n"
        "ds_read_b128 v[32:35], v1 offset:0\n"
        "ds_read_b128 v[36:39], v1 offset:1024\n"
        "s_waitcnt lgkmcnt(0)\n"
        "v_cmp_eq_u32 vcc, -1, v7\n"
        "s_cbranch_vccnz .Lch_rt0_%=\n"
        "s_branch .Lch_ok0_%=\n"
        ".Lch_rt1_%=:\n"
        "s_waitcnt lgkmcnt(0)\n"
        "s_barrier\n"
        "s_add_u32 s64, s64, 1\n"
        "s_and_b32 s65, s64, 255\n"
        "s_cbranch_scc1 .Lch_rt1_go_%=\n"
        "s_mov_b32 s65, 40960\n"
        "s_cmp_lt_u32 s64, 1048576\n"
        "s_cbranch_scc1 .Lch_rt1_chk_%=\n"
        "v_mov_b32 v7, 1\n"
        "buffer_store_dword v7, off, s[40:43], s65 sc1\n"
        "s_waitcnt vmcnt(0)\n"
        ".Lch_rt1_chk_%=:\n"
        "buffer_load_dword v7, off, s[40:43], s65 sc1\n"
        "s_waitcnt vmcnt(0)\n"
        "v_readfirstlane_b32 s65, v7\n"
        "s_nop 0\n"
        "s_cmp_lg_u32 s65, 1\n"
        "s_cbranch_scc1 .Lch_rt1_go_%=\n"
        "s_endpgm\n"
        ".Lch_rt1_go_%=:\n"
        "s_cmp_eq_u32 s48, 3\n"
        "s_cbranch_scc0 .Lch_rw1_%=\n"
        "s_and_b32 s66, s33, 1024\n"
        "s_lshl_b32 s66, s66, 2\n"
        "s_add_u32 m0, s66, 151552\n"
        "s_add_u32 s66, s54, s71\n"
        "buffer_load_dwordx4 v3, s[40:43], s66 offen offset:0 sc1 lds\n"
        "buffer_load_dwordx4 v3, s[40:43], s66 offen offset:1024 sc1 lds\n"
        "buffer_load_dwordx4 v3, s[40:43], s66 offen offset:2048 sc1 lds\n"
        "buffer_load_dwordx4 v3, s[40:43], s66 offen offset:3072 sc1 lds\n"
        "s_waitcnt vmcnt(0)\n"
        ".Lch_rw1_%=:\n"
        "s_barrier\n"
        "ds_read_b32 v7, v5 offset:4096\n"
        "ds_read_b128 v[32:35], v1 offset:4096\n"
        "s_waitcnt lgkmcnt(0)\n"
        "v_cmp_eq_u32 vcc, -1, v7\n"
        "s_cbranch_vccnz .Lch_rt1_%=\n"
        "s_branch .Lch_ok1_%=\n"
        ".Lch_rt2_%=:\n"
        "s_waitcnt lgkmcnt(0)\n"
        "s_barrier\n"
        "s_add_u32 s64, s64, 1\n"
        "s_and_b32 s65, s64, 255\n"
        "s_cbranch_scc1 .Lch_rt2_go_%=\n"
        "s_mov_b32 s65, 40960\n"
        "s_cmp_lt_u32 s64, 1048576\n"
        "s_cbranch_scc1 .Lch_rt2_chk_%=\n"
        "v_mov_b32 v7, 1\n"
        "buffer_store_dword v7, off, s[40:43], s65 sc1\n"
        "s_waitcnt vmcnt(0)\n"
        ".Lch_rt2_chk_%=:\n"
        "buffer_load_dword v7, off, s[40:43], s65 sc1\n"
        "s_waitcnt vmcnt(0)\n"
        "v_readfirstlane_b32 s65, v7\n"
        "s_nop 0\n"
        "s_cmp_lg_u32 s65, 1\n"
        "s_cbranch_scc1 .Lch_rt2_go_%=\n"
        "s_endpgm\n"
        ".Lch_rt2_go_%=:\n"
        "s_cmp_eq_u32 s48, 3\n"
        "s_cbranch_scc0 .Lch_rw2_%=\n"
        "ds_read_b32 v7, v2 offset:33796\n"
        "s_waitcnt lgkmcnt(0)\n"
        "s_mov_b32 m0, 159744\n"
        "s_add_u32 s66, s71, s55\n"
        "buffer_load_dwordx4 v7, s[40:43], s66 offen sc1 lds\n"
        "s_waitcnt vmcnt(0)\n"
        ".Lch_rw2_%=:\n"
        "s_barrier\n"
        "ds_read_b32 v7, v5 offset:8192\n"
        "ds_read_b128 v[48:51], v1 offset:8192\n"
        "s_memrealtime s[36:37]\n"
        "s_waitcnt lgkmcnt(0)\n"
        "v_cmp_eq_u32 vcc, -1, v7\n"
        "s_add_u32 s37, s36, s35\n"
        "s_and_b64 vcc, vcc, 0xffff\n"
        "s_cbranch_vccnz .Lch_rt2_%=\n"
        "s_branch .Lch_ok2_%=\n"
        ".Lch_redo_%=:\n"
        "s_waitcnt vmcnt(0) lgkmcnt(0)\n"
        "s_barrier\n"
        "s_add_u32 s64, s64, 1\n"
        "s_and_b32 s65, s64, 255\n"
        "s_cbranch_scc1 .Lch_rd_go_%=\n"
        "s_mov_b32 s65, 40960\n"
        "s_cmp_lt_u32 s64, 1048576\n"
        "s_cbranch_scc1 .Lch_rd_chk_%=\n"
        "v_mov_b32 v7, 1\n"
        "buffer_store_dword v7, off, s[40:43], s65 sc1\n"
        "s_waitcnt vmcnt(0)\n"
        ".Lch_rd_chk_%=:\n"
        "buffer_load_dword v7, off, s[40:43], s65 sc1\n"
        "s_waitcnt vmcnt(0)\n"
        "v_readfirstlane_b32 s65, v7\n"
        "s_nop 0\n"
        "s_cmp_lg_u32 s65, 1\n"
        "s_cbranch_scc1 .Lch_rd_go_%=\n"
        "s_endpgm\n"
        ".Lch_rd_go_%=:\n"
        "s_cmp_lt_u32 s48, 2\n"
        "s_cbranch_scc1 .Lch_rd2_%=\n"
        "s_and_b32 s66, s33, 1024\n"
        "s_lshl_b32 s66, s66, 2\n"
        "s_add_u32 m0, s66, 151552\n"
        "s_add_u32 s66, s54, s71\n"
        "buffer_load_dwordx4 v3, s[40:43], s66 offen offset:0 sc1 lds\n"
        "buffer_load_dwordx4 v3, s[40:43], s66 offen offset:1024 sc1 lds\n"
        "buffer_load_dwordx4 v3, s[40:43], s66 offen offset:2048 sc1 lds\n"
        "buffer_load_dwordx4 v3, s[40:43], s66 offen offset:3072 sc1 lds\n"
        "ds_read_b32 v7, v2 offset:33796\n"
        "s_waitcnt lgkmcnt(0)\n"
        "s_mov_b32 m0, 159744\n"
        "s_add_u32 s66, s71, s55\n"
        "buffer_load_dwordx4 v7, s[40:43], s66 offen sc1 lds\n"
        ".Lch_rd2_%=:\n"
        "s_nop 7\n"
        "ds_read_b128 v[40:43], v2 offset:0\n"
        "ds_read_b128 v[44:47], v2 offset:1024\n"
        "ds_read_b128 v[48:51], v2 offset:2048\n"
        "ds_read_b128 v[52:55], v2 offset:3072\n"
        "ds_read_b128 v[56:59], v2 offset:4096\n"
        "ds_read_b128 v[60:63], v2 offset:5120\n"
        "v_mov_b64 v[8:9], 0\n"
        "v_mov_b64 v[10:11], 0\n"
        "v_mov_b64 v[12:13], 0\n"
        "v_mov_b64 v[14:15], 0\n"
        "v_mov_b64 v[16:17], 0\n"
        "v_mov_b64 v[18:19], 0\n"
        "v_mov_b64 v[20:21], 0\n"
        "v_mov_b64 v[22:23], 0\n"
        "v_mov_b64 v[24:25], 0\n"
        "v_mov_b64 v[26:27], 0\n"
        "v_mov_b64 v[28:29], 0\n"
        "v_mov_b64 v[30:31], 0\n"
        "v_mov_b32 v6, 0\n"
        "s_waitcnt vmcnt(0)\n"
        "s_branch .Lch_A_%=\n"
        ".Lch_slow_%=:\n"
        "v_max3_u32 v6, v6, v38, v39\n"
        "v_max3_u32 v6, v6, v48, v49\n"
        "v_max3_u32 v6, v6, v50, v51\n"
        "v_cmp_eq_u32 vcc, -1, v6\n"
        "s_cbranch_vccnz .Lch_redo_%=\n"
        "s_waitcnt vmcnt(0) lgkmcnt(0)\n"
        "ds_read_b128 v[68:71], v4 offset:10240\n"
        "v_mov_b64 v[72:73], v[24:25]\n"
        "v_mov_b64 v[74:75], v[26:27]\n"
        "v_mov_b64 v[76:77], v[28:29]\n"
        "v_mov_b64 v[78:79], v[30:31]\n"
        "v_cvt_pk_bf16_f32 v64, v12, v13\n"
        "v_cvt_pk_bf16_f32 v65, v14, v15\n"
        "v_cvt_pk_bf16_f32 v66, v16, v17\n"
        "v_cvt_pk_bf16_f32 v67, v18, v19\n"
        "v_smfmac_f32_16x16x64_bf16 v[72:75], v[36:39], v[56:63], v0\n"
        "v_smfmac_f32_16x16x64_bf16 v[76:79], v[36:39], v[40:47], v0\n"
        "v_smfmac_f32_16x16x64_bf16 v[72:75], v[48:51], v[240:247], v0\n"
        "v_smfmac_f32_16x16x64_bf16 v[76:79], v[48:51], v[248:255], v0\n"
        "s_waitcnt lgkmcnt(0)\n"
        "buffer_store_dwordx4 v[64:67], v68, s[40:43], s70 offen sc1\n"
        "s_nop 5\n"
        "v_add_f32 v72, v72, v75\n"
        "v_add_f32 v73, v73, v74\n"
        "v_add_f32 v76, v76, v79\n"
        "v_add_f32 v77, v77, v78\n"
        "v_cvt_pk_bf16_f32 v72, v72, v73\n"
        "v_cvt_pk_bf16_f32 v74, v76, v77\n"
        "s_nop 1\n"
        "v_permlane16_swap_b32 v72, v73\n"
        "v_permlane16_swap_b32 v74, v75\n"
        "buffer_store_dwordx4 v[72:75], v69, s[40:43], s70 offen offset:2048 sc1\n"
        "s_waitcnt vmcnt(0)\n"
        "v_mov_b32 v7, s51\n"
        "s_mov_b64 exec, 1\n"
        "buffer_store_dword v7, off, s[40:43], s79 sc1\n"
        "s_mov_b64 exec, -1\n"
        "s_cmp_eq_u32 s51, 63\n"
        "s_cbranch_scc0 .Lch_slow2_%=\n"
        "s_endpgm\n"
        ".Lch_slow2_%=:\n"
        "s_mov_b32 s70, 241172480\n"
        "s_mov_b32 s64, 0\n"
        ".Lch_kvw_top_%=:\n"
        "s_mov_b64 exec, 1\n"
        "buffer_load_dword v7, off, s[40:43], s78 sc1\n"
        "s_mov_b64 exec, -1\n"
        "s_waitcnt vmcnt(0)\n"
        "v_cmp_gt_i32 vcc, s80, v7\n"
        "s_nop 0\n"
        "s_and_b64 vcc, vcc, 1\n"
        "s_cbranch_vccz .Lch_kvw_ok_%=\n"
        "s_add_u32 s64, s64, 1\n"
        "s_and_b32 s65, s64, 255\n"
        "s_cbranch_scc1 .Lch_kvw_go_%=\n"
        "s_mov_b32 s65, 40960\n"
        "s_cmp_lt_u32 s64, 1048576\n"
        "s_cbranch_scc1 .Lch_kvw_chk_%=\n"
        "v_mov_b32 v7, 1\n"
        "buffer_store_dword v7, off, s[40:43], s65 sc1\n"
        "s_waitcnt vmcnt(0)\n"
        ".Lch_kvw_chk_%=:\n"
        "buffer_load_dword v7, off, s[40:43], s65 sc1\n"
        "s_waitcnt vmcnt(0)\n"
        "v_readfirstlane_b32 s65, v7\n"
        "s_nop 0\n"
        "s_cmp_lg_u32 s65, 1\n"
        "s_cbranch_scc1 .Lch_kvw_go_%=\n"
        "s_endpgm\n"
        ".Lch_kvw_go_%=:\n"
        "s_sleep 1\n"
        "s_branch .Lch_kvw_top_%=\n"
        ".Lch_kvw_ok_%=:\n"
        "s_add_u32 s89, s69, 524288\n"
        "buffer_load_dwordx4 v[68:71], v3, s[40:43], s69 offen offset:1024 sc1\n"
        "buffer_load_dwordx4 v[64:67], v3, s[40:43], s69 offen sc1\n"
        "buffer_load_dwordx4 v[72:75], v3, s[40:43], s89 offen sc1\n"
        "buffer_load_dwordx4 v[76:79], v3, s[40:43], s89 offen offset:1024 sc1\n"
        "s_barrier\n"
        "s_memrealtime s[36:37]\n"
        "s_waitcnt lgkmcnt(0)\n"
        "s_add_u32 s36, s36, 12\n"
        "s_add_u32 s37, s36, s35\n"
        "v_and_b32 v7, -2, s37\n"
        "s_mov_b64 exec, 1\n"
        "buffer_store_dword v7, off, s[40:43], s46 sc1\n"
        "s_mov_b64 exec, -1\n"
        "s_mov_b32 s30, s69\n"
        "s_branch .Lch_back_%=\n"
        :: "s"(p.ws), "s"(p.Wq), "s"(p.Wk), "s"(p.Wv), "s"(p.Wo), "s"(p.mem0), "s"(pair(w, h)), "s"(pair(x4, pr)), "s"(p.tune0), "s"(p.tune1),
           "v"(pair(idx, va + (lane & 2) * 32)), "v"(pair(wb + lane * 16, lane * 16)), "v"(pair(spr, CH_IMG + lane * 64 + c * 4)),
           "v"(pair(qo, oo)), "v"(pair(tr + (q >> 1) * 32, m0)), "v"(pair(tr + (q >> 1) * 2048, 0))
        : "v0", "v1", "v2", "v3", "v4", "v5", "v6", "v7", "v8", "v9", "v10", "v11", "v12", "v13", "v14", "v15", "v16", "v17", "v18", "v19", "v20", "v21", "v22", "v23", "v24", "v25", "v26", "v27", "v28", "v29", "v30", "v31", "v32", "v33", "v34", "v35", "v36", "v37", "v38", "v39", "v40", "v41", "v42", "v43", "v44", "v45", "v46", "v47", "v48", "v49", "v50", "v51", "v52", "v53", "v54", "v55", "v56", "v57", "v58", "v59", "v60", "v61", "v62", "v63", "v64", "v65", "v66", "v67", "v68", "v69", "v70", "v71", "v72", "v73", "v74", "v75", "v76", "v77", "v78", "v79", "v255", "a255", "s30", "s31", "s32", "s33", "s34", "s35", "s36", "s37", "s38", "s39", "s40", "s41", "s42", "s43", "s44", "s45", "s46", "s47", "s48", "s49", "s50", "s51", "s52", "s53", "s54", "s55", "s56", "s57", "s58", "s59", "s60", "s61", "s62", "s63", "s64", "s65", "s66", "s67", "s68", "s69", "s70", "s71", "s72", "s73", "s74", "s75", "s76", "s77", "s78", "s79", "s80", "s81", "s82", "s83", "s84", "s85", "s86", "s87", "s88", "s89", "s90", "s91", "s92", "s93", "s94", "s95", "s96", "s97", "s98", "s99", "vcc", "scc", "memory");
    __builtin_unreachable();
}

constexpr unsigned flag(unsigned i) { return spread(LF0 + i * 128); }
#define GEMM_HEAD const unsigned lane = threadIdx.x & 63, i = lane & 15, g = lane >> 4, q = i >> 2, pp = lane & 3; const int w = rfl(threadIdx.x >> 6)
__device__ __forceinline__ void role_s1(const Params& p, int h, int x4, int pr, int delay) {
    GEMM_HEAD;
    const unsigned vo = (8 * g + q) * 8192 + (128 * h + 32 * w) * 2 + (pp ^ (2 * (g & 1))) * 16;
    const unsigned tr = w * 40960 + (4 * g + q) * 64 + pp * 8;
    asm volatile(
        "s_nop 4\n"
        "s_mov_b64 s[40:41], %0\n"
        "s_and_b32 s41, s41, 0xffff\n"
        "s_mov_b32 s42, 236978176\n"
        "s_mov_b32 s43, 0x20000\n"
        "s_mov_b64 s[48:49], %4\n"
        "s_mov_b64 s[56:57], %5\n"
        "s_mov_b64 s[58:59], %6\n"
        "s_mov_b64 s[60:61], %7\n"
        "v_mov_b64 v[0:1], %8\n"
        "v_mov_b64 v[2:3], %9\n"
        "v_mov_b64 v[4:5], %10\n"
        "v_mov_b64 v[6:7], %11\n"
        "v_mov_b64 v[8:9], %12\n"
        "s_mov_b32 s47, 0x20000\n"
        "s_mul_i32 s79, s48, 40960\n"
        ".Ls1_start_%=:\n"
        "s_sub_u32 s49, s49, 1\n"
        "s_cbranch_scc1 .Ls1_start2_%=\n"
        "s_sleep 16\n"
        "s_branch .Ls1_start_%=\n"
        ".Ls1_start2_%=:\n"
        "s_mov_b64 s[44:45], %3\n"
        "s_and_b32 s45, s45, 0xffff\n"
        "s_mov_b32 s46, 4718592\n"
        "s_add_u32 m0, s79, 0\n"
        "s_mov_b32 s65, 0\n"
        "buffer_load_dwordx4 v6, s[44:47], s65 offen lds\n"
        "s_mov_b32 s65, 31744\n"
        "buffer_load_dwordx4 v6, s[44:47], s65 offen offset:1024 lds\n"
        "s_add_u32 m0, s79, 2048\n"
        "s_mov_b32 s65, 262144\n"
        "buffer_load_dwordx4 v6, s[44:47], s65 offen lds\n"
        "s_mov_b32 s65, 293888\n"
        "buffer_load_dwordx4 v6, s[44:47], s65 offen offset:1024 lds\n"
        "s_add_u32 m0, s79, 4096\n"
        "s_mov_b32 s65, 524288\n"
        "buffer_load_dwordx4 v6, s[44:47], s65 offen lds\n"
        "s_mov_b32 s65, 556032\n"
        "buffer_load_dwordx4 v6, s[44:47], s65 offen offset:1024 lds\n"
        "s_add_u32 m0, s79, 6144\n"
        "s_mov_b32 s65, 786432\n"
        "buffer_load_dwordx4 v6, s[44:47], s65 offen lds\n"
        "s_mov_b32 s65, 818176\n"
        "buffer_load_dwordx4 v6, s[44:47], s65 offen offset:1024 lds\n"
        "s_add_u32 m0, s79, 8192\n"
        "s_mov_b32 s65, 1048576\n"
        "buffer_load_dwordx4 v6, s[44:47], s65 offen lds\n"
        "s_mov_b32 s65, 1080320\n"
        "buffer_load_dwordx4 v6, s[44:47], s65 offen offset:1024 lds\n"
        "s_add_u32 m0, s79, 10240\n"
        "s_mov_b32 s65, 1310720\n"
        "buffer_load_dwordx4 v6, s[44:47], s65 offen lds\n"
        "s_mov_b32 s65, 1342464\n"
        "buffer_load_dwordx4 v6, s[44:47], s65 offen offset:1024 lds\n"
        "s_add_u32 m0, s79, 12288\n"
        "s_mov_b32 s65, 1572864\n"
        "buffer_load_dwordx4 v6, s[44:47], s65 offen lds\n"
        "s_mov_b32 s65, 1604608\n"
        "buffer_load_dwordx4 v6, s[44:47], s65 offen offset:1024 lds\n"
        "s_add_u32 m0, s79, 14336\n"
        "s_mov_b32 s65, 1835008\n"
        "buffer_load_dwordx4 v6, s[44:47], s65 offen lds\n"
        "s_mov_b32 s65, 1866752\n"
        "buffer_load_dwordx4 v6, s[44:47], s65 offen offset:1024 lds\n"
        "s_add_u32 m0, s79, 16384\n"
        "s_mov_b32 s65, 2097152\n"
        "buffer_load_dwordx4 v6, s[44:47], s65 offen lds\n"
        "s_mov_b32 s65, 2128896\n"
        "buffer_load_dwordx4 v6, s[44:47], s65 offen offset:1024 lds\n"
        "s_add_u32 m0, s79, 18432\n"
        "s_mov_b32 s65, 2359296\n"
        "buffer_load_dwordx4 v6, s[44:47], s65 offen lds\n"
        "s_mov_b32 s65, 2391040\n"
        "buffer_load_dwordx4 v6, s[44:47], s65 offen offset:1024 lds\n"
        "s_add_u32 m0, s79, 20480\n"
        "s_mov_b32 s65, 2621440\n"
        "buffer_load_dwordx4 v6, s[44:47], s65 offen lds\n"
        "s_mov_b32 s65, 2653184\n"
        "buffer_load_dwordx4 v6, s[44:47], s65 offen offset:1024 lds\n"
        "s_add_u32 m0, s79, 22528\n"
        "s_mov_b32 s65, 2883584\n"
        "buffer_load_dwordx4 v6, s[44:47], s65 offen lds\n"
        "s_mov_b32 s65, 2915328\n"
        "buffer_load_dwordx4 v6, s[44:47], s65 offen offset:1024 lds\n"
        "s_add_u32 m0, s79, 24576\n"
        "s_mov_b32 s65, 3145728\n"
        "buffer_load_dwordx4 v6, s[44:47], s65 offen lds\n"
        "s_mov_b32 s65, 3177472\n"
        "buffer_load_dwordx4 v6, s[44:47], s65 offen offset:1024 lds\n"
        "s_add_u32 m0, s79, 26624\n"
        "s_mov_b32 s65, 3407872\n"
        "buffer_load_dwordx4 v6, s[44:47], s65 offen lds\n"
        "s_mov_b32 s65, 3439616\n"
        "buffer_load_dwordx4 v6, s[44:47], s65 offen offset:1024 lds\n"
        "s_add_u32 m0, s79, 28672\n"
        "s_mov_b32 s65, 3670016\n"
        "buffer_load_dwordx4 v6, s[44:47], s65 offen lds\n"
        "s_mov_b32 s65, 3701760\n"
        "buffer_load_dwordx4 v6, s[44:47], s65 offen offset:1024 lds\n"
        "s_add_u32 m0, s79, 30720\n"
        "s_mov_b32 s65, 3932160\n"
        "buffer_load_dwordx4 v6, s[44:47], s65 offen lds\n"
        "s_mov_b32 s65, 3963904\n"
        "buffer_load_dwordx4 v6, s[44:47], s65 offen offset:1024 lds\n"
        "s_waitcnt vmcnt(30)\n"
        "ds_read_b64_tr_b16 v[112:113], v8 offset:0\n"
        "ds_read_b64_tr_b16 v[114:115], v8 offset:1024\n"
        "ds_read_b64_tr_b16 v[116:117], v9 offset:0\n"
        "ds_read_b64_tr_b16 v[118:119], v9 offset:1024\n"
        "s_waitcnt vmcnt(28)\n"
        "ds_read_b64_tr_b16 v[120:121], v8 offset:2048\n"
        "ds_read_b64_tr_b16 v[122:123], v8 offset:3072\n"
        "ds_read_b64_tr_b16 v[124:125], v9 offset:2048\n"
        "ds_read_b64_tr_b16 v[126:127], v9 offset:3072\n"
        "s_waitcnt lgkmcnt(4)\n"
        "s_add_u32 m0, s79, 0\n"
        "s_mov_b32 s65, 4194304\n"
        "buffer_load_dwordx4 v6, s[44:47], s65 offen lds\n"
        "s_mov_b32 s65, 4226048\n"
        "buffer_load_dwordx4 v6, s[44:47], s65 offen offset:1024 lds\n"
        "s_waitcnt vmcnt(28)\n"
        "ds_read_b64_tr_b16 v[128:129], v8 offset:4096\n"
        "ds_read_b64_tr_b16 v[130:131], v8 offset:5120\n"
        "ds_read_b64_tr_b16 v[132:133], v9 offset:4096\n"
        "ds_read_b64_tr_b16 v[134:135], v9 offset:5120\n"
        "s_waitcnt lgkmcnt(4)\n"
        "s_add_u32 m0, s79, 2048\n"
        "s_mov_b32 s65, 4456448\n"
        "buffer_load_dwordx4 v6, s[44:47], s65 offen lds\n"
        "s_mov_b32 s65, 4488192\n"
        "buffer_load_dwordx4 v6, s[44:47], s65 offen offset:1024 lds\n"
        "s_waitcnt vmcnt(28)\n"
        "ds_read_b64_tr_b16 v[136:137], v8 offset:6144\n"
        "ds_read_b64_tr_b16 v[138:139], v8 offset:7168\n"
        "ds_read_b64_tr_b16 v[140:141], v9 offset:6144\n"
        "ds_read_b64_tr_b16 v[142:143], v9 offset:7168\n"
        "s_waitcnt lgkmcnt(4)\n"
        "s_mov_b64 s[44:45], %1\n"
        "s_and_b32 s45, s45, 0xffff\n"
        "s_mov_b32 s46, 4718592\n"
        "s_add_u32 m0, s79, 4096\n"
        "s_mov_b32 s65, 0\n"
        "buffer_load_dwordx4 v6, s[44:47], s65 offen lds\n"
        "s_mov_b32 s65, 31744\n"
        "buffer_load_dwordx4 v6, s[44:47], s65 offen offset:1024 lds\n"
        "s_waitcnt vmcnt(28)\n"
        "ds_read_b64_tr_b16 v[144:145], v8 offset:8192\n"
        "ds_read_b64_tr_b16 v[146:147], v8 offset:9216\n"
        "ds_read_b64_tr_b16 v[148:149], v9 offset:8192\n"
        "ds_read_b64_tr_b16 v[150:151], v9 offset:9216\n"
        "s_waitcnt lgkmcnt(4)\n"
        "s_add_u32 m0, s79, 6144\n"
        "s_mov_b32 s65, 262144\n"
        "buffer_load_dwordx4 v6, s[44:47], s65 offen lds\n"
        "s_mov_b32 s65, 293888\n"
        "buffer_load_dwordx4 v6, s[44:47], s65 offen offset:1024 lds\n"
        "s_waitcnt vmcnt(28)\n"
        "ds_read_b64_tr_b16 v[152:153], v8 offset:10240\n"
        "ds_read_b64_tr_b16 v[154:155], v8 offset:11264\n"
        "ds_read_b64_tr_b16 v[156:157], v9 offset:10240\n"
        "ds_read_b64_tr_b16 v[158:159], v9 offset:11264\n"
        "s_waitcnt lgkmcnt(4)\n"
        "s_add_u32 m0, s79, 8192\n"
        "s_mov_b32 s65, 524288\n"
        "buffer_load_dwordx4 v6, s[44:47], s65 offen lds\n"
        "s_mov_b32 s65, 556032\n"
        "buffer_load_dwordx4 v6, s[44:47], s65 offen offset:1024 lds\n"
        "s_waitcnt vmcnt(28)\n"
        "ds_read_b64_tr_b16 v[160:161], v8 offset:12288\n"
        "ds_read_b64_tr_b16 v[162:163], v8 offset:13312\n"
        "ds_read_b64_tr_b16 v[164:165], v9 offset:12288\n"
        "ds_read_b64_tr_b16 v[166:167], v9 offset:13312\n"
        "s_waitcnt lgkmcnt(4)\n"
        "s_add_u32 m0, s79, 10240\n"
        "s_mov_b32 s65, 786432\n"
        "buffer_load_dwordx4 v6, s[44:47], s65 offen lds\n"
        "s_mov_b32 s65, 818176\n"
        "buffer_load_dwordx4 v6, s[44:47], s65 offen offset:1024 lds\n"
        "s_waitcnt vmcnt(28)\n"
        "ds_read_b64_tr_b16 v[168:169], v8 offset:14336\n"
        "ds_read_b64_tr_b16 v[170:171], v8 offset:15360\n"
        "ds_read_b64_tr_b16 v[172:173], v9 offset:14336\n"
        "ds_read_b64_tr_b16 v[174:175], v9 offset:15360\n"
        "s_waitcnt lgkmcnt(4)\n"
        "s_add_u32 m0, s79, 12288\n"
        "s_mov_b32 s65, 1048576\n"
        "buffer_load_dwordx4 v6, s[44:47], s65 offen lds\n"
        "s_mov_b32 s65, 1080320\n"
        "buffer_load_dwordx4 v6, s[44:47], s65 offen offset:1024 lds\n"
        "s_waitcnt vmcnt(28)\n"
        "ds_read_b64_tr_b16 v[176:177], v8 offset:16384\n"
        "ds_read_b64_tr_b16 v[178:179], v8 offset:17408\n"
        "ds_read_b64_tr_b16 v[180:181], v9 offset:16384\n"
        "ds_read_b64_tr_b16 v[182:183], v9 offset:17408\n"
        "s_waitcnt lgkmcnt(4)\n"
        "s_add_u32 m0, s79, 14336\n"
        "s_mov_b32 s65, 1310720\n"
        "buffer_load_dwordx4 v6, s[44:47], s65 offen lds\n"
        "s_mov_b32 s65, 1342464\n"
        "buffer_load_dwordx4 v6, s[44:47], s65 offen offset:1024 lds\n"
        "s_waitcnt vmcnt(28)\n"
        "ds_read_b64_tr_b16 v[184:185], v8 offset:18432\n"
        "ds_read_b64_tr_b16 v[186:187], v8 offset:19456\n"
        "ds_read_b64_tr_b16 v[188:189], v9 offset:18432\n"
        "ds_read_b64_tr_b16 v[190:191], v9 offset:19456\n"
        "s_waitcnt lgkmcnt(4)\n"
        "s_add_u32 m0, s79, 16384\n"
        "s_mov_b32 s65, 1572864\n"
        "buffer_load_dwordx4 v6, s[44:47], s65 offen lds\n"
        "s_mov_b32 s65, 1604608\n"
        "buffer_load_dwordx4 v6, s[44:47], s65 offen offset:1024 lds\n"
        "s_waitcnt vmcnt(28)\n"
        "ds_read_b64_tr_b16 v[192:193], v8 offset:20480\n"
        "ds_read_b64_tr_b16 v[194:195], v8 offset:21504\n"
        "ds_read_b64_tr_b16 v[196:197], v9 offset:20480\n"
        "ds_read_b64_tr_b16 v[198:199], v9 offset:21504\n"
        "s_waitcnt lgkmcnt(4)\n"
        "s_add_u32 m0, s79, 18432\n"
        "s_mov_b32 s65, 1835008\n"
        "buffer_load_dwordx4 v6, s[44:47], s65 offen lds\n"
        "s_mov_b32 s65, 1866752\n"
        "buffer_load_dwordx4 v6, s[44:47], s65 offen offset:1024 lds\n"
        "s_waitcnt vmcnt(28)\n"
        "ds_read_b64_tr_b16 v[200:201], v8 offset:22528\n"
        "ds_read_b64_tr_b16 v[202:203], v8 offset:23552\n"
        "ds_read_b64_tr_b16 v[204:205], v9 offset:22528\n"
        "ds_read_b64_tr_b16 v[206:207], v9 offset:23552\n"
        "s_waitcnt lgkmcnt(4)\n"
        "s_add_u32 m0, s79, 20480\n"
        "s_mov_b32 s65, 2097152\n"
        "buffer_load_dwordx4 v6, s[44:47], s65 offen lds\n"
        "s_mov_b32 s65, 2128896\n"
        "buffer_load_dwordx4 v6, s[44:47], s65 offen offset:1024 lds\n"
        "s_waitcnt vmcnt(28)\n"
        "ds_read_b64_tr_b16 v[208:209], v8 offset:24576\n"
        "ds_read_b64_tr_b16 v[210:211], v8 offset:25600\n"
        "ds_read_b64_tr_b16 v[212:213], v9 offset:24576\n"
        "ds_read_b64_tr_b16 v[214:215], v9 offset:25600\n"
        "s_waitcnt lgkmcnt(4)\n"
        "s_add_u32 m0, s79, 22528\n"
        "s_mov_b32 s65, 2359296\n"
        "buffer_load_dwordx4 v6, s[44:47], s65 offen lds\n"
        "s_mov_b32 s65, 2391040\n"
        "buffer_load_dwordx4 v6, s[44:47], s65 offen offset:1024 lds\n"
        "s_waitcnt vmcnt(28)\n"
        "ds_read_b64_tr_b16 v[216:217], v8 offset:26624\n"
        "ds_read_b64_tr_b16 v[218:219], v8 offset:27648\n"
        "ds_read_b64_tr_b16 v[220:221], v9 offset:26624\n"
        "ds_read_b64_tr_b16 v[222:223], v9 offset:27648\n"
        "s_waitcnt lgkmcnt(4)\n"
        "s_add_u32 m0, s79, 24576\n"
        "s_mov_b32 s65, 2621440\n"
        "buffer_load_dwordx4 v6, s[44:47], s65 offen lds\n"
        "s_mov_b32 s65, 2653184\n"
        "buffer_load_dwordx4 v6, s[44:47], s65 offen offset:1024 lds\n"
        "s_waitcnt vmcnt(28)\n"
        "ds_read_b64_tr_b16 v[224:225], v8 offset:28672\n"
        "ds_read_b64_tr_b16 v[226:227], v8 offset:29696\n"
        "ds_read_b64_tr_b16 v[228:229], v9 offset:28672\n"
        "ds_read_b64_tr_b16 v[230:231], v9 offset:29696\n"
        "s_waitcnt lgkmcnt(4)\n"
        "s_add_u32 m0, s79, 26624\n"
        "s_mov_b32 s65, 2883584\n"
        "buffer_load_dwordx4 v6, s[44:47], s65 offen lds\n"
        "s_mov_b32 s65, 2915328\n"
        "buffer_load_dwordx4 v6, s[44:47], s65 offen offset:1024 lds\n"
        "s_waitcnt vmcnt(28)\n"
        "ds_read_b64_tr_b16 v[232:233], v8 offset:30720\n"
        "ds_read_b64_tr_b16 v[234:235], v8 offset:31744\n"
        "ds_read_b64_tr_b16 v[236:237], v9 offset:30720\n"
        "ds_read_b64_tr_b16 v[238:239], v9 offset:31744\n"
        "s_waitcnt lgkmcnt(4)\n"
        "s_add_u32 m0, s79, 28672\n"
        "s_mov_b32 s65, 3145728\n"
        "buffer_load_dwordx4 v6, s[44:47], s65 offen lds\n"
        "s_mov_b32 s65, 3177472\n"
        "buffer_load_dwordx4 v6, s[44:47], s65 offen offset:1024 lds\n"
        "s_waitcnt vmcnt(28)\n"
        "ds_read_b64_tr_b16 v[240:241], v8 offset:0\n"
        "ds_read_b64_tr_b16 v[242:243], v8 offset:1024\n"
        "ds_read_b64_tr_b16 v[244:245], v9 offset:0\n"
        "ds_read_b64_tr_b16 v[246:247], v9 offset:1024\n"
        "s_waitcnt lgkmcnt(4)\n"
        "s_add_u32 m0, s79, 30720\n"
        "s_mov_b32 s65, 3407872\n"
        "buffer_load_dwordx4 v6, s[44:47], s65 offen lds\n"
        "s_mov_b32 s65, 3439616\n"
        "buffer_load_dwordx4 v6, s[44:47], s65 offen offset:1024 lds\n"
        "s_waitcnt vmcnt(28)\n"
        "ds_read_b64_tr_b16 v[248:249], v8 offset:2048\n"
        "ds_read_b64_tr_b16 v[250:251], v8 offset:3072\n"
        "ds_read_b64_tr_b16 v[252:253], v9 offset:2048\n"
        "ds_read_b64_tr_b16 v[254:255], v9 offset:3072\n"
        "s_waitcnt lgkmcnt(4)\n"
        "s_add_u32 m0, s79, 0\n"
        "s_mov_b32 s65, 3670016\n"
        "buffer_load_dwordx4 v6, s[44:47], s65 offen lds\n"
        "s_mov_b32 s65, 3701760\n"
        "buffer_load_dwordx4 v6, s[44:47], s65 offen offset:1024 lds\n"
        "s_waitcnt vmcnt(28)\n"
        "ds_read_b64_tr_b16 a[0:1], v8 offset:4096\n"
        "ds_read_b64_tr_b16 a[2:3], v8 offset:5120\n"
        "ds_read_b64_tr_b16 a[4:5], v9 offset:4096\n"
        "ds_read_b64_tr_b16 a[6:7], v9 offset:5120\n"
        "s_waitcnt lgkmcnt(4)\n"
        "s_add_u32 m0, s79, 2048\n"
        "s_mov_b32 s65, 3932160\n"
        "buffer_load_dwordx4 v6, s[44:47], s65 offen lds\n"
        "s_mov_b32 s65, 3963904\n"
        "buffer_load_dwordx4 v6, s[44:47], s65 offen offset:1024 lds\n"
        "s_waitcnt vmcnt(28)\n"
        "ds_read_b64_tr_b16 a[8:9], v8 offset:6144\n"
        "ds_read_b64_tr_b16 a[10:11], v8 offset:7168\n"
        "ds_read_b64_tr_b16 a[12:13], v9 offset:6144\n"
        "ds_read_b64_tr_b16 a[14:15], v9 offset:7168\n"
        "s_waitcnt lgkmcnt(4)\n"
        "s_add_u32 m0, s79, 4096\n"
        "s_mov_b32 s65, 4194304\n"
        "buffer_load_dwordx4 v6, s[44:47], s65 offen lds\n"
        "s_mov_b32 s65, 4226048\n"
        "buffer_load_dwordx4 v6, s[44:47], s65 offen offset:1024 lds\n"
        "s_waitcnt vmcnt(28)\n"
        "ds_read_b64_tr_b16 a[16:17], v8 offset:8192\n"
        "ds_read_b64_tr_b16 a[18:19], v8 offset:9216\n"
        "ds_read_b64_tr_b16 a[20:21], v9 offset:8192\n"
        "ds_read_b64_tr_b16 a[22:23], v9 offset:9216\n"
        "s_waitcnt lgkmcnt(4)\n"
        "s_add_u32 m0, s79, 6144\n"
        "s_mov_b32 s65, 4456448\n"
        "buffer_load_dwordx4 v6, s[44:47], s65 offen lds\n"
        "s_mov_b32 s65, 4488192\n"
        "buffer_load_dwordx4 v6, s[44:47], s65 offen offset:1024 lds\n"
        "s_waitcnt vmcnt(28)\n"
        "ds_read_b64_tr_b16 a[24:25], v8 offset:10240\n"
        "ds_read_b64_tr_b16 a[26:27], v8 offset:11264\n"
        "ds_read_b64_tr_b16 a[28:29], v9 offset:10240\n"
        "ds_read_b64_tr_b16 a[30:31], v9 offset:11264\n"
        "s_waitcnt lgkmcnt(4)\n"
        "s_mov_b64 s[44:45], %2\n"
        "s_and_b32 s45, s45, 0xffff\n"
        "s_mov_b32 s46, 4718592\n"
        "s_add_u32 m0, s79, 8192\n"
        "s_mov_b32 s65, 0\n"
        "buffer_load_dwordx4 v6, s[44:47], s65 offen lds\n"
        "s_mov_b32 s65, 31744\n"
        "buffer_load_dwordx4 v6, s[44:47], s65 offen offset:1024 lds\n"
        "s_waitcnt vmcnt(28)\n"
        "ds_read_b64_tr_b16 a[32:33], v8 offset:12288\n"
        "ds_read_b64_tr_b16 a[34:35], v8 offset:13312\n"
        "ds_read_b64_tr_b16 a[36:37], v9 offset:12288\n"
        "ds_read_b64_tr_b16 a[38:39], v9 offset:13312\n"
        "s_waitcnt lgkmcnt(4)\n"
        "s_add_u32 m0, s79, 10240\n"
        "s_mov_b32 s65, 262144\n"
        "buffer_load_dwordx4 v6, s[44:47], s65 offen lds\n"
        "s_mov_b32 s65, 293888\n"
        "buffer_load_dwordx4 v6, s[44:47], s65 offen offset:1024 lds\n"
        "s_waitcnt vmcnt(28)\n"
        "ds_read_b64_tr_b16 a[40:41], v8 offset:14336\n"
        "ds_read_b64_tr_b16 a[42:43], v8 offset:15360\n"
        "ds_read_b64_tr_b16 a[44:45], v9 offset:14336\n"
        "ds_read_b64_tr_b16 a[46:47], v9 offset:15360\n"
        "s_waitcnt lgkmcnt(4)\n"
        "s_add_u32 m0, s79, 12288\n"
        "s_mov_b32 s65, 524288\n"
        "buffer_load_dwordx4 v6, s[44:47], s65 offen lds\n"
        "s_mov_b32 s65, 556032\n"
        "buffer_load_dwordx4 v6, s[44:47], s65 offen offset:1024 lds\n"
        "s_waitcnt vmcnt(28)\n"
        "ds_read_b64_tr_b16 a[48:49], v8 offset:16384\n"
        "ds_read_b64_tr_b16 a[50:51], v8 offset:17408\n"
        "ds_read_b64_tr_b16 a[52:53], v9 offset:16384\n"
        "ds_read_b64_tr_b16 a[54:55], v9 offset:17408\n"
        "s_waitcnt lgkmcnt(4)\n"
        "s_add_u32 m0, s79, 14336\n"
        "s_mov_b32 s65, 786432\n"
        "buffer_load_dwordx4 v6, s[44:47], s65 offen lds\n"
        "s_mov_b32 s65, 818176\n"
        "buffer_load_dwordx4 v6, s[44:47], s65 offen offset:1024 lds\n"
        "s_waitcnt vmcnt(28)\n"
        "ds_read_b64_tr_b16 a[56:57], v8 offset:18432\n"
        "ds_read_b64_tr_b16 a[58:59], v8 offset:19456\n"
        "ds_read_b64_tr_b16 a[60:61], v9 offset:18432\n"
        "ds_read_b64_tr_b16 a[62:63], v9 offset:19456\n"
        "s_waitcnt lgkmcnt(4)\n"
        "s_add_u32 m0, s79, 16384\n"
        "s_mov_b32 s65, 1048576\n"
        "buffer_load_dwordx4 v6, s[44:47], s65 offen lds\n"
        "s_mov_b32 s65, 1080320\n"
        "buffer_load_dwordx4 v6, s[44:47], s65 offen offset:1024 lds\n"
        "s_waitcnt vmcnt(28)\n"
        "ds_read_b64_tr_b16 a[64:65], v8 offset:20480\n"
        "ds_read_b64_tr_b16 a[66:67], v8 offset:21504\n"
        "ds_read_b64_tr_b16 a[68:69], v9 offset:20480\n"
        "ds_read_b64_tr_b16 a[70:71], v9 offset:21504\n"
        "s_waitcnt lgkmcnt(4)\n"
        "s_add_u32 m0, s79, 18432\n"
        "s_mov_b32 s65, 1310720\n"
        "buffer_load_dwordx4 v6, s[44:47], s65 offen lds\n"
        "s_mov_b32 s65, 1342464\n"
        "buffer_load_dwordx4 v6, s[44:47], s65 offen offset:1024 lds\n"
        "s_waitcnt vmcnt(28)\n"
        "ds_read_b64_tr_b16 a[72:73], v8 offset:22528\n"
        "ds_read_b64_tr_b16 a[74:75], v8 offset:23552\n"
        "ds_read_b64_tr_b16 a[76:77], v9 offset:22528\n"
        "ds_read_b64_tr_b16 a[78:79], v9 offset:23552\n"
        "s_waitcnt lgkmcnt(4)\n"
        "s_add_u32 m0, s79, 20480\n"
        "s_mov_b32 s65, 1572864\n"
        "buffer_load_dwordx4 v6, s[44:47], s65 offen lds\n"
        "s_mov_b32 s65, 1604608\n"
        "buffer_load_dwordx4 v6, s[44:47], s65 offen offset:1024 lds\n"
        "s_waitcnt vmcnt(28)\n"
        "ds_read_b64_tr_b16 a[80:81], v8 offset:24576\n"
        "ds_read_b64_tr_b16 a[82:83], v8 offset:25600\n"
        "ds_read_b64_tr_b16 a[84:85], v9 offset:24576\n"
        "ds_read_b64_tr_b16 a[86:87], v9 offset:25600\n"
        "s_waitcnt lgkmcnt(4)\n"
        "s_add_u32 m0, s79, 22528\n"
        "s_mov_b32 s65, 1835008\n"
        "buffer_load_dwordx4 v6, s[44:47], s65 offen lds\n"
        "s_mov_b32 s65, 1866752\n"
        "buffer_load_dwordx4 v6, s[44:47], s65 offen offset:1024 lds\n"
        "s_waitcnt vmcnt(28)\n"
        "ds_read_b64_tr_b16 a[88:89], v8 offset:26624\n"
        "ds_read_b64_tr_b16 a[90:91], v8 offset:27648\n"
        "ds_read_b64_tr_b16 a[92:93], v9 offset:26624\n"
        "ds_read_b64_tr_b16 a[94:95], v9 offset:27648\n"
        "s_waitcnt lgkmcnt(4)\n"
        "s_add_u32 m0, s79, 24576\n"
        "s_mov_b32 s65, 2097152\n"
        "buffer_load_dwordx4 v6, s[44:47], s65 offen lds\n"
        "s_mov_b32 s65, 2128896\n"
        "buffer_load_dwordx4 v6, s[44:47], s65 offen offset:1024 lds\n"
        "s_waitcnt vmcnt(28)\n"
        "ds_read_b64_tr_b16 a[96:97], v8 offset:28672\n"
        "ds_read_b64_tr_b16 a[98:99], v8 offset:29696\n"
        "ds_read_b64_tr_b16 a[100:101], v9 offset:28672\n"
        "ds_read_b64_tr_b16 a[102:103], v9 offset:29696\n"
        "s_waitcnt lgkmcnt(4)\n"
        "s_add_u32 m0, s79, 26624\n"
        "s_mov_b32 s65, 2359296\n"
        "buffer_load_dwordx4 v6, s[44:47], s65 offen lds\n"
        "s_mov_b32 s65, 2391040\n"
        "buffer_load_dwordx4 v6, s[44:47], s65 offen offset:1024 lds\n"
        "s_waitcnt vmcnt(28)\n"
        "ds_read_b64_tr_b16 a[104:105], v8 offset:30720\n"
        "ds_read_b64_tr_b16 a[106:107], v8 offset:31744\n"
        "ds_read_b64_tr_b16 a[108:109], v9 offset:30720\n"
        "ds_read_b64_tr_b16 a[110:111], v9 offset:31744\n"
        "s_waitcnt lgkmcnt(4)\n"
        "s_add_u32 m0, s79, 28672\n"
        "s_mov_b32 s65, 2621440\n"
        "buffer_load_dwordx4 v6, s[44:47], s65 offen lds\n"
        "s_mov_b32 s65, 2653184\n"
        "buffer_load_dwordx4 v6, s[44:47], s65 offen offset:1024 lds\n"
        "s_waitcnt vmcnt(28)\n"
        "ds_read_b64_tr_b16 a[112:113], v8 offset:0\n"
        "ds_read_b64_tr_b16 a[114:115], v8 offset:1024\n"
        "ds_read_b64_tr_b16 a[116:117], v9 offset:0\n"
        "ds_read_b64_tr_b16 a[118:119], v9 offset:1024\n"
        "s_waitcnt lgkmcnt(4)\n"
        "s_add_u32 m0, s79, 30720\n"
        "s_mov_b32 s65, 2883584\n"
        "buffer_load_dwordx4 v6, s[44:47], s65 offen lds\n"
        "s_mov_b32 s65, 2915328\n"
        "buffer_load_dwordx4 v6, s[44:47], s65 offen offset:1024 lds\n"
        "s_waitcnt vmcnt(28)\n"
        "ds_read_b64_tr_b16 a[120:121], v8 offset:2048\n"
        "ds_read_b64_tr_b16 a[122:123], v8 offset:3072\n"
        "ds_read_b64_tr_b16 a[124:125], v9 offset:2048\n"
        "ds_read_b64_tr_b16 a[126:127], v9 offset:3072\n"
        "s_waitcnt lgkmcnt(4)\n"
        "s_add_u32 m0, s79, 0\n"
        "s_mov_b32 s65, 3145728\n"
        "buffer_load_dwordx4 v6, s[44:47], s65 offen lds\n"
        "s_mov_b32 s65, 3177472\n"
        "buffer_load_dwordx4 v6, s[44:47], s65 offen offset:1024 lds\n"
        "s_waitcnt vmcnt(28)\n"
        "ds_read_b64_tr_b16 a[128:129], v8 offset:4096\n"
        "ds_read_b64_tr_b16 a[130:131], v8 offset:5120\n"
        "ds_read_b64_tr_b16 a[132:133], v9 offset:4096\n"
        "ds_read_b64_tr_b16 a[134:135], v9 offset:5120\n"
        "s_waitcnt lgkmcnt(4)\n"
        "s_add_u32 m0, s79, 2048\n"
        "s_mov_b32 s65, 3407872\n"
        "buffer_load_dwordx4 v6, s[44:47], s65 offen lds\n"
        "s_mov_b32 s65, 3439616\n"
        "buffer_load_dwordx4 v6, s[44:47], s65 offen offset:1024 lds\n"
        "s_waitcnt vmcnt(28)\n"
        "ds_read_b64_tr_b16 a[136:137], v8 offset:6144\n"
        "ds_read_b64_tr_b16 a[138:139], v8 offset:7168\n"
        "ds_read_b64_tr_b16 a[140:141], v9 offset:6144\n"
        "ds_read_b64_tr_b16 a[142:143], v9 offset:7168\n"
        "s_waitcnt lgkmcnt(4)\n"
        "s_add_u32 m0, s79, 4096\n"
        "s_mov_b32 s65, 3670016\n"
        "buffer_load_dwordx4 v6, s[44:47], s65 offen lds\n"
        "s_mov_b32 s65, 3701760\n"
        "buffer_load_dwordx4 v6, s[44:47], s65 offen offset:1024 lds\n"
        "s_waitcnt vmcnt(28)\n"
        "ds_read_b64_tr_b16 a[144:145], v8 offset:8192\n"
        "ds_read_b64_tr_b16 a[146:147], v8 offset:9216\n"
        "ds_read_b64_tr_b16 a[148:149], v9 offset:8192\n"
        "ds_read_b64_tr_b16 a[150:151], v9 offset:9216\n"
        "s_waitcnt lgkmcnt(4)\n"
        "s_add_u32 m0, s79, 6144\n"
        "s_mov_b32 s65, 3932160\n"
        "buffer_load_dwordx4 v6, s[44:47], s65 offen lds\n"
        "s_mov_b32 s65, 3963904\n"
        "buffer_load_dwordx4 v6, s[44:47], s65 offen offset:1024 lds\n"
        "s_waitcnt vmcnt(28)\n"
        "ds_read_b64_tr_b16 a[152:153], v8 offset:10240\n"
        "ds_read_b64_tr_b16 a[154:155], v8 offset:11264\n"
        "ds_read_b64_tr_b16 a[156:157], v9 offset:10240\n"
        "ds_read_b64_tr_b16 a[158:159], v9 offset:11264\n"
        "s_waitcnt lgkmcnt(4)\n"
        "s_add_u32 m0, s79, 8192\n"
        "s_mov_b32 s65, 4194304\n"
        "buffer_load_dwordx4 v6, s[44:47], s65 offen lds\n"
        "s_mov_b32 s65, 4226048\n"
        "buffer_load_dwordx4 v6, s[44:47], s65 offen offset:1024 lds\n"
        "s_waitcnt vmcnt(28)\n"
        "ds_read_b64_tr_b16 a[160:161], v8 offset:12288\n"
        "ds_read_b64_tr_b16 a[162:163], v8 offset:13312\n"
        "ds_read_b64_tr_b16 a[164:165], v9 offset:12288\n"
        "ds_read_b64_tr_b16 a[166:167], v9 offset:13312\n"
        "s_waitcnt lgkmcnt(4)\n"
        "s_add_u32 m0, s79, 10240\n"
        "s_mov_b32 s65, 4456448\n"
        "buffer_load_dwordx4 v6, s[44:47], s65 offen lds\n"
        "s_mov_b32 s65, 4488192\n"
        "buffer_load_dwordx4 v6, s[44:47], s65 offen offset:1024 lds\n"
        "s_waitcnt vmcnt(28)\n"
        "ds_read_b64_tr_b16 a[168:169], v8 offset:14336\n"
        "ds_read_b64_tr_b16 a[170:171], v8 offset:15360\n"
        "ds_read_b64_tr_b16 a[172:173], v9 offset:14336\n"
        "ds_read_b64_tr_b16 a[174:175], v9 offset:15360\n"
        "s_waitcnt vmcnt(26)\n"
        "ds_read_b64_tr_b16 a[176:177], v8 offset:16384\n"
        "ds_read_b64_tr_b16 a[178:179], v8 offset:17408\n"
        "ds_read_b64_tr_b16 a[180:181], v9 offset:16384\n"
        "ds_read_b64_tr_b16 a[182:183], v9 offset:17408\n"
        "s_waitcnt vmcnt(24)\n"
        "ds_read_b64_tr_b16 a[184:185], v8 offset:18432\n"
        "ds_read_b64_tr_b16 a[186:187], v8 offset:19456\n"
        "ds_read_b64_tr_b16 a[188:189], v9 offset:18432\n"
        "ds_read_b64_tr_b16 a[190:191], v9 offset:19456\n"
        "s_waitcnt vmcnt(22)\n"
        "ds_read_b64_tr_b16 a[192:193], v8 offset:20480\n"
        "ds_read_b64_tr_b16 a[194:195], v8 offset:21504\n"
        "ds_read_b64_tr_b16 a[196:197], v9 offset:20480\n"
        "ds_read_b64_tr_b16 a[198:199], v9 offset:21504\n"
        "s_waitcnt vmcnt(20)\n"
        "ds_read_b64_tr_b16 a[200:201], v8 offset:22528\n"
        "ds_read_b64_tr_b16 a[202:203], v8 offset:23552\n"
        "ds_read_b64_tr_b16 a[204:205], v9 offset:22528\n"
        "ds_read_b64_tr_b16 a[206:207], v9 offset:23552\n"
        "s_waitcnt vmcnt(18)\n"
        "ds_read_b64_tr_b16 a[208:209], v8 offset:24576\n"
        "ds_read_b64_tr_b16 a[210:211], v8 offset:25600\n"
        "ds_read_b64_tr_b16 a[212:213], v9 offset:24576\n"
        "ds_read_b64_tr_b16 a[214:215], v9 offset:25600\n"
        "s_waitcnt vmcnt(16)\n"
        "ds_read_b64_tr_b16 a[216:217], v8 offset:26624\n"
        "ds_read_b64_tr_b16 a[218:219], v8 offset:27648\n"
        "ds_read_b64_tr_b16 a[220:221], v9 offset:26624\n"
        "ds_read_b64_tr_b16 a[222:223], v9 offset:27648\n"
        "s_waitcnt vmcnt(14)\n"
        "ds_read_b64_tr_b16 a[224:225], v8 offset:28672\n"
        "ds_read_b64_tr_b16 a[226:227], v8 offset:29696\n"
        "ds_read_b64_tr_b16 a[228:229], v9 offset:28672\n"
        "ds_read_b64_tr_b16 a[230:231], v9 offset:29696\n"
        "s_waitcnt vmcnt(12)\n"
        "ds_read_b64_tr_b16 a[232:233], v8 offset:30720\n"
        "ds_read_b64_tr_b16 a[234:235], v8 offset:31744\n"
        "ds_read_b64_tr_b16 a[236:237], v9 offset:30720\n"
        "ds_read_b64_tr_b16 a[238:239], v9 offset:31744\n"
        "s_waitcnt vmcnt(10)\n"
        "ds_read_b64_tr_b16 a[240:241], v8 offset:0\n"
        "ds_read_b64_tr_b16 a[242:243], v8 offset:1024\n"
        "ds_read_b64_tr_b16 a[244:245], v9 offset:0\n"
        "ds_read_b64_tr_b16 a[246:247], v9 offset:1024\n"
        "s_waitcnt vmcnt(8)\n"
        "ds_read_b64_tr_b16 a[248:249], v8 offset:2048\n"
        "ds_read_b64_tr_b16 a[250:251], v8 offset:3072\n"
        "ds_read_b64_tr_b16 a[252:253], v9 offset:2048\n"
        "ds_read_b64_tr_b16 a[254:255], v9 offset:3072\n"
        "s_waitcnt vmcnt(6)\n"
        "ds_read_b64_tr_b16 v[80:81], v8 offset:4096\n"
        "ds_read_b64_tr_b16 v[82:83], v8 offset:5120\n"
        "ds_read_b64_tr_b16 v[84:85], v9 offset:4096\n"
        "ds_read_b64_tr_b16 v[86:87], v9 offset:5120\n"
        "s_waitcnt vmcnt(4)\n"
        "ds_read_b64_tr_b16 v[88:89], v8 offset:6144\n"
        "ds_read_b64_tr_b16 v[90:91], v8 offset:7168\n"
        "ds_read_b64_tr_b16 v[92:93], v9 offset:6144\n"
        "ds_read_b64_tr_b16 v[94:95], v9 offset:7168\n"
        "s_waitcnt vmcnt(2)\n"
        "ds_read_b64_tr_b16 v[96:97], v8 offset:8192\n"
        "ds_read_b64_tr_b16 v[98:99], v8 offset:9216\n"
        "ds_read_b64_tr_b16 v[100:101], v9 offset:8192\n"
        "ds_read_b64_tr_b16 v[102:103], v9 offset:9216\n"
        "s_waitcnt vmcnt(0)\n"
        "ds_read_b64_tr_b16 v[104:105], v8 offset:10240\n"
        "ds_read_b64_tr_b16 v[106:107], v8 offset:11264\n"
        "ds_read_b64_tr_b16 v[108:109], v9 offset:10240\n"
        "ds_read_b64_tr_b16 v[110:111], v9 offset:11264\n"
        "s_waitcnt lgkmcnt(0)\n"
        "v_lshrrev_b32 v12, 4, v1\n"
        "s_lshl_b32 s66, s57, 7\n"
        "s_mov_b32 s64, 0\n"
        ".Ls1_pair_top_%=:\n"
        "s_mov_b64 exec, 1\n"
        "buffer_load_dword v13, off, s[40:43], s66 sc1\n"
        "s_mov_b64 exec, -1\n"
        "s_waitcnt vmcnt(0)\n"
        "v_cmp_gt_i32 vcc, 0, v13\n"
        "s_nop 0\n"
        "s_and_b64 vcc, vcc, 1\n"
        "s_cbranch_vccz .Ls1_pair_ok_%=\n"
        "s_add_u32 s64, s64, 1\n"
        "s_and_b32 s65, s64, 255\n"
        "s_cbranch_scc1 .Ls1_pair_go_%=\n"
        "s_mov_b32 s65, 40960\n"
        "s_cmp_lt_u32 s64, 1048576\n"
        "s_cbranch_scc1 .Ls1_pair_chk_%=\n"
        "v_mov_b32 v13, 1\n"
        "buffer_store_dword v13, off, s[40:43], s65 sc1\n"
        "s_waitcnt vmcnt(0)\n"
        ".Ls1_pair_chk_%=:\n"
        "buffer_load_dword v13, off, s[40:43], s65 sc1\n"
        "s_waitcnt vmcnt(0)\n"
        "v_readfirstlane_b32 s65, v13\n"
        "s_nop 0\n"
        "s_cmp_lg_u32 s65, 1\n"
        "s_cbranch_scc1 .Ls1_pair_go_%=\n"
        "s_endpgm\n"
        ".Ls1_pair_go_%=:\n"
        "s_sleep 2\n"
        "s_branch .Ls1_pair_top_%=\n"
        ".Ls1_pair_ok_%=:\n"
        "v_readfirstlane_b32 s50, v13\n"
        "s_nop 0\n"
        "s_xor_b32 s50, s50, s56\n"
        "v_lshlrev_b32 v14, 7, v12\n"
        "s_sub_u32 s66, 0, s50\n"
        "s_mov_b32 s52, 24576\n"
        "s_mov_b32 s64, 0\n"
        ".Ls1_pg0_top_%=:\n"
        "buffer_load_dword v13, v14, s[40:43], s52 offen sc1\n"
        "s_waitcnt vmcnt(0)\n"
        "v_cmp_gt_i32 vcc, 0, v13\n"
        "s_cbranch_vccz .Ls1_pg0_ok_%=\n"
        "s_add_u32 s64, s64, 1\n"
        "s_and_b32 s65, s64, 255\n"
        "s_cbranch_scc1 .Ls1_pg0_go_%=\n"
        "s_mov_b32 s65, 40960\n"
        "s_cmp_lt_u32 s64, 1048576\n"
        "s_cbranch_scc1 .Ls1_pg0_chk_%=\n"
        "v_mov_b32 v13, 1\n"
        "buffer_store_dword v13, off, s[40:43], s65 sc1\n"
        "s_waitcnt vmcnt(0)\n"
        ".Ls1_pg0_chk_%=:\n"
        "buffer_load_dword v13, off, s[40:43], s65 sc1\n"
        "s_waitcnt vmcnt(0)\n"
        "v_readfirstlane_b32 s65, v13\n"
        "s_nop 0\n"
        "s_cmp_lg_u32 s65, 1\n"
        "s_cbranch_scc1 .Ls1_pg0_go_%=\n"
        "s_endpgm\n"
        ".Ls1_pg0_go_%=:\n"
        "s_sleep 2\n"
        "s_branch .Ls1_pg0_top_%=\n"
        ".Ls1_pg0_ok_%=:\n"
        "v_cmp_eq_u32 s[52:53], 1, v13\n"
        "s_nop 1\n"
        "s_mov_b32 s54, 32768\n"
        "s_mov_b32 s64, 0\n"
        ".Ls1_pg1_top_%=:\n"
        "buffer_load_dword v13, v14, s[40:43], s54 offen sc1\n"
        "s_waitcnt vmcnt(0)\n"
        "v_cmp_gt_i32 vcc, 0, v13\n"
        "s_cbranch_vccz .Ls1_pg1_ok_%=\n"
        "s_add_u32 s64, s64, 1\n"
        "s_and_b32 s65, s64, 255\n"
        "s_cbranch_scc1 .Ls1_pg1_go_%=\n"
        "s_mov_b32 s65, 40960\n"
        "s_cmp_lt_u32 s64, 1048576\n"
        "s_cbranch_scc1 .Ls1_pg1_chk_%=\n"
        "v_mov_b32 v13, 1\n"
        "buffer_store_dword v13, off, s[40:43], s65 sc1\n"
        "s_waitcnt vmcnt(0)\n"
        ".Ls1_pg1_chk_%=:\n"
        "buffer_load_dword v13, off, s[40:43], s65 sc1\n"
        "s_waitcnt vmcnt(0)\n"
        "v_readfirstlane_b32 s65, v13\n"
        "s_nop 0\n"
        "s_cmp_lg_u32 s65, 1\n"
        "s_cbranch_scc1 .Ls1_pg1_go_%=\n"
        "s_endpgm\n"
        ".Ls1_pg1_go_%=:\n"
        "s_sleep 2\n"
        "s_branch .Ls1_pg1_top_%=\n"
        ".Ls1_pg1_ok_%=:\n"
        "v_cmp_eq_u32 s[54:55], 1, v13\n"
        "s_nop 1\n"
        "s_mov_b32 vcc_lo, 0x55555555\n"
        "s_mov_b32 vcc_hi, 0x55555555\n"
        "s_xor_b64 vcc, vcc, s[52:53]\n"
        "s_bcnt1_i32_b64 s65, vcc\n"
        "s_sub_u32 s64, s65, 23\n"
        "s_cmp_lt_u32 s64, 19\n"
        "s_cbranch_scc1 .Ls1_rawmap_%=\n"
        "s_cmp_lt_u32 s65, 32\n"
        "s_cselect_b32 s65, 0, -1\n"
        "s_xor_b32 s52, s65, 0x55555555\n"
        "s_xor_b32 s53, s65, 0x55555555\n"
        "s_xor_b32 s54, s65, 0x55555555\n"
        "s_xor_b32 s55, s65, 0xaaaaaaaa\n"
        ".Ls1_rawmap_%=:\n"
        "s_xor_b32 s52, s52, s66\n"
        "s_xor_b32 s53, s53, s66\n"
        "s_xor_b32 s54, s54, s66\n"
        "s_xor_b32 s55, s55, s66\n"
        "s_and_b32 s76, s55, 1\n"
        "s_lshl_b32 s76, s76, 13\n"
        "s_add_u32 s60, s60, s76\n"
        "s_barrier\n"
        "s_mov_b32 s68, -1\n"
        "s_mov_b32 s69, 0\n"
        "s_mov_b32 s77, 0\n"
        "s_mov_b32 s78, 0\n"
        "v_mov_b32 v6, v0\n"
        "s_mov_b32 s80, 0xbfb8aa3b\n"
        "s_branch .Ls1_wait_%=\n"
        ".Ls1_p00_%=:\n"
        "v_mfma_f32_16x16x32_bf16 v[32:35], a[0:3], v[8:11], 0\n"
        "v_mfma_f32_16x16x32_bf16 v[36:39], a[4:7], v[8:11], 0\n"
        "ds_read_b128 v[8:11], v0 offset:1536\n"
        "v_mfma_f32_16x16x32_bf16 v[40:43], a[0:3], v[12:15], 0\n"
        "v_cvt_pk_bf16_f32 v64, v64, v65\n"
        "v_cvt_pk_bf16_f32 v65, v66, v67\n"
        "v_mfma_f32_16x16x32_bf16 v[44:47], a[4:7], v[12:15], 0\n"
        "ds_read_b128 v[12:15], v0 offset:19968\n"
        "s_waitcnt lgkmcnt(4)\n"
        "v_mfma_f32_16x16x32_bf16 v[32:35], a[8:11], v[16:19], v[32:35]\n"
        "v_cvt_pk_bf16_f32 v66, v68, v69\n"
        "v_cvt_pk_bf16_f32 v67, v70, v71\n"
        "v_mfma_f32_16x16x32_bf16 v[36:39], a[12:15], v[16:19], v[36:39]\n"
        "ds_read_b128 v[16:19], v0 offset:2048\n"
        "v_mfma_f32_16x16x32_bf16 v[40:43], a[8:11], v[20:23], v[40:43]\n"
        "buffer_store_dwordx4 v[64:67], v1, s[40:43], s72 offen offset:0 sc1\n"
        "v_mfma_f32_16x16x32_bf16 v[44:47], a[12:15], v[20:23], v[44:47]\n"
        "ds_read_b128 v[20:23], v0 offset:20480\n"
        "s_waitcnt lgkmcnt(4)\n"
        "v_mfma_f32_16x16x32_bf16 v[32:35], a[16:19], v[24:27], v[32:35]\n"
        "v_cvt_pk_bf16_f32 v72, v72, v73\n"
        "v_cvt_pk_bf16_f32 v73, v74, v75\n"
        "v_mfma_f32_16x16x32_bf16 v[36:39], a[20:23], v[24:27], v[36:39]\n"
        "ds_read_b128 v[24:27], v0 offset:2560\n"
        "v_cvt_pk_bf16_f32 v74, v76, v77\n"
        "v_mfma_f32_16x16x32_bf16 v[40:43], a[16:19], v[28:31], v[40:43]\n"
        "v_cvt_pk_bf16_f32 v75, v78, v79\n"
        "v_mfma_f32_16x16x32_bf16 v[44:47], a[20:23], v[28:31], v[44:47]\n"
        "ds_read_b128 v[28:31], v0 offset:20992\n"
        "s_waitcnt lgkmcnt(4)\n"
        "v_mfma_f32_16x16x32_bf16 v[32:35], a[24:27], v[8:11], v[32:35]\n"
        "v_mfma_f32_16x16x32_bf16 v[36:39], a[28:31], v[8:11], v[36:39]\n"
        "ds_read_b128 v[8:11], v0 offset:3072\n"
        "v_mfma_f32_16x16x32_bf16 v[40:43], a[24:27], v[12:15], v[40:43]\n"
        "buffer_store_dwordx4 v[72:75], v1, s[40:43], s72 offen offset:1024 sc1\n"
        "v_mfma_f32_16x16x32_bf16 v[44:47], a[28:31], v[12:15], v[44:47]\n"
        "ds_read_b128 v[12:15], v0 offset:21504\n"
        "s_waitcnt lgkmcnt(4)\n"
        "v_mfma_f32_16x16x32_bf16 v[32:35], a[32:35], v[16:19], v[32:35]\n"
        "v_mfma_f32_16x16x32_bf16 v[36:39], a[36:39], v[16:19], v[36:39]\n"
        "ds_read_b128 v[16:19], v0 offset:3584\n"
        "v_mfma_f32_16x16x32_bf16 v[40:43], a[32:35], v[20:23], v[40:43]\n"
        "v_mfma_f32_16x16x32_bf16 v[44:47], a[36:39], v[20:23], v[44:47]\n"
        "ds_read_b128 v[20:23], v0 offset:22016\n"
        "s_waitcnt lgkmcnt(4)\n"
        "v_mfma_f32_16x16x32_bf16 v[32:35], a[40:43], v[24:27], v[32:35]\n"
        "s_waitcnt vmcnt(2)\n"
        "s_barrier\n"
        "v_mfma_f32_16x16x32_bf16 v[36:39], a[44:47], v[24:27], v[36:39]\n"
        "ds_read_b128 v[24:27], v0 offset:4096\n"
        "v_mfma_f32_16x16x32_bf16 v[40:43], a[40:43], v[28:31], v[40:43]\n"
        "v_mfma_f32_16x16x32_bf16 v[44:47], a[44:47], v[28:31], v[44:47]\n"
        "ds_read_b128 v[28:31], v0 offset:22528\n"
        "s_waitcnt lgkmcnt(4)\n"
        "v_mfma_f32_16x16x32_bf16 v[32:35], a[48:51], v[8:11], v[32:35]\n"
        "v_mfma_f32_16x16x32_bf16 v[36:39], a[52:55], v[8:11], v[36:39]\n"
        "ds_read_b128 v[8:11], v0 offset:4608\n"
        "v_mfma_f32_16x16x32_bf16 v[40:43], a[48:51], v[12:15], v[40:43]\n"
        "v_mfma_f32_16x16x32_bf16 v[44:47], a[52:55], v[12:15], v[44:47]\n"
        "ds_read_b128 v[12:15], v0 offset:23040\n"
        "s_waitcnt lgkmcnt(4)\n"
        "v_mfma_f32_16x16x32_bf16 v[32:35], a[56:59], v[16:19], v[32:35]\n"
        "v_mfma_f32_16x16x32_bf16 v[36:39], a[60:63], v[16:19], v[36:39]\n"
        "ds_read_b128 v[16:19], v0 offset:5120\n"
        "v_mfma_f32_16x16x32_bf16 v[40:43], a[56:59], v[20:23], v[40:43]\n"
        "v_mfma_f32_16x16x32_bf16 v[44:47], a[60:63], v[20:23], v[44:47]\n"
        "ds_read_b128 v[20:23], v0 offset:23552\n"
        "s_waitcnt lgkmcnt(4)\n"
        "v_mfma_f32_16x16x32_bf16 v[32:35], a[64:67], v[24:27], v[32:35]\n"
        "buffer_load_dword v4, v2, s[40:43], s76 offen sc1\n"
        "v_mfma_f32_16x16x32_bf16 v[36:39], a[68:71], v[24:27], v[36:39]\n"
        "ds_read_b128 v[24:27], v0 offset:5632\n"
        "v_mfma_f32_16x16x32_bf16 v[40:43], a[64:67], v[28:31], v[40:43]\n"
        "v_mfma_f32_16x16x32_bf16 v[44:47], a[68:71], v[28:31], v[44:47]\n"
        "ds_read_b128 v[28:31], v0 offset:24064\n"
        "s_waitcnt lgkmcnt(4)\n"
        "v_mfma_f32_16x16x32_bf16 v[32:35], a[72:75], v[8:11], v[32:35]\n"
        "v_mfma_f32_16x16x32_bf16 v[36:39], a[76:79], v[8:11], v[36:39]\n"
        "ds_read_b128 v[8:11], v0 offset:6144\n"
        "v_mfma_f32_16x16x32_bf16 v[40:43], a[72:75], v[12:15], v[40:43]\n"
        "v_mfma_f32_16x16x32_bf16 v[44:47], a[76:79], v[12:15], v[44:47]\n"
        "ds_read_b128 v[12:15], v0 offset:24576\n"
        "s_waitcnt lgkmcnt(4)\n"
        "v_mfma_f32_16x16x32_bf16 v[32:35], a[80:83], v[16:19], v[32:35]\n"
        "v_mfma_f32_16x16x32_bf16 v[36:39], a[84:87], v[16:19], v[36:39]\n"
        "ds_read_b128 v[16:19], v0 offset:6656\n"
        "v_mfma_f32_16x16x32_bf16 v[40:43], a[80:83], v[20:23], v[40:43]\n"
        "v_mfma_f32_16x16x32_bf16 v[44:47], a[84:87], v[20:23], v[44:47]\n"
        "ds_read_b128 v[20:23], v0 offset:25088\n"
        "s_waitcnt lgkmcnt(4)\n"
        "v_mfma_f32_16x16x32_bf16 v[32:35], a[88:91], v[24:27], v[32:35]\n"
        "s_waitcnt vmcnt(1)\n"
        "v_mov_b32 v5, s74\n"
        "v_mfma_f32_16x16x32_bf16 v[36:39], a[92:95], v[24:27], v[36:39]\n"
        "ds_read_b128 v[24:27], v0 offset:7168\n"
        "buffer_store_dword v5, v3, s[40:43], s73 offen sc1\n"
        "v_mfma_f32_16x16x32_bf16 v[40:43], a[88:91], v[28:31], v[40:43]\n"
        "s_add_u32 s68, s68, 1\n"
        "v_mfma_f32_16x16x32_bf16 v[44:47], a[92:95], v[28:31], v[44:47]\n"
        "ds_read_b128 v[28:31], v0 offset:25600\n"
        "s_waitcnt lgkmcnt(4)\n"
        "v_mfma_f32_16x16x32_bf16 v[32:35], a[96:99], v[8:11], v[32:35]\n"
        "s_bitcmp1_b64 s[52:53], s68\n"
        "s_cselect_b32 s66, 8192, 0\n"
        "v_mfma_f32_16x16x32_bf16 v[36:39], a[100:103], v[8:11], v[36:39]\n"
        "ds_read_b128 v[8:11], v0 offset:7680\n"
        "s_lshl_b32 s75, s68, 21\n"
        "v_mfma_f32_16x16x32_bf16 v[40:43], a[96:99], v[12:15], v[40:43]\n"
        "s_add_u32 s75, s75, s66\n"
        "s_add_u32 s75, s75, s59\n"
        "v_mfma_f32_16x16x32_bf16 v[44:47], a[100:103], v[12:15], v[44:47]\n"
        "ds_read_b128 v[12:15], v0 offset:26112\n"
        "s_waitcnt lgkmcnt(4)\n"
        "v_mfma_f32_16x16x32_bf16 v[32:35], a[104:107], v[16:19], v[32:35]\n"
        "s_mul_i32 s74, s68, 3\n"
        "s_mov_b32 s73, s60\n"
        "v_mfma_f32_16x16x32_bf16 v[36:39], a[108:111], v[16:19], v[36:39]\n"
        "ds_read_b128 v[16:19], v0 offset:8192\n"
        "s_add_u32 s72, s75, 524288\n"
        "v_mfma_f32_16x16x32_bf16 v[40:43], a[104:107], v[20:23], v[40:43]\n"
        "v_mfma_f32_16x16x32_bf16 v[44:47], a[108:111], v[20:23], v[44:47]\n"
        "ds_read_b128 v[20:23], v0 offset:26624\n"
        "s_waitcnt lgkmcnt(4)\n"
        "v_mfma_f32_16x16x32_bf16 v[32:35], a[112:115], v[24:27], v[32:35]\n"
        "v_mfma_f32_16x16x32_bf16 v[36:39], a[116:119], v[24:27], v[36:39]\n"
        "ds_read_b128 v[24:27], v0 offset:8704\n"
        "v_mfma_f32_16x16x32_bf16 v[40:43], a[112:115], v[28:31], v[40:43]\n"
        "v_mfma_f32_16x16x32_bf16 v[44:47], a[116:119], v[28:31], v[44:47]\n"
        "ds_read_b128 v[28:31], v0 offset:27136\n"
        "s_waitcnt lgkmcnt(4)\n"
        "v_mfma_f32_16x16x32_bf16 v[32:35], a[120:123], v[8:11], v[32:35]\n"
        "v_mfma_f32_16x16x32_bf16 v[36:39], a[124:127], v[8:11], v[36:39]\n"
        "ds_read_b128 v[8:11], v0 offset:0\n"
        "v_mfma_f32_16x16x32_bf16 v[40:43], a[120:123], v[12:15], v[40:43]\n"
        "v_mfma_f32_16x16x32_bf16 v[44:47], a[124:127], v[12:15], v[44:47]\n"
        "ds_read_b128 v[12:15], v0 offset:18432\n"
        "s_waitcnt lgkmcnt(4)\n"
        "v_mfma_f32_16x16x32_bf16 v[32:35], a[128:131], v[16:19], v[32:35]\n"
        "v_mfma_f32_16x16x32_bf16 v[36:39], a[132:135], v[16:19], v[36:39]\n"
        "ds_read_b128 v[16:19], v0 offset:512\n"
        "v_mfma_f32_16x16x32_bf16 v[40:43], a[128:131], v[20:23], v[40:43]\n"
        "v_mfma_f32_16x16x32_bf16 v[44:47], a[132:135], v[20:23], v[44:47]\n"
        "ds_read_b128 v[20:23], v0 offset:18944\n"
        "s_waitcnt lgkmcnt(4)\n"
        "v_mfma_f32_16x16x32_bf16 v[32:35], a[136:139], v[24:27], v[32:35]\n"
        "s_waitcnt vmcnt(1)\n"
        "v_cmp_gt_i32 vcc, s70, v4\n"
        "v_mfma_f32_16x16x32_bf16 v[36:39], a[140:143], v[24:27], v[36:39]\n"
        "ds_read_b128 v[24:27], v0 offset:1024\n"
        "v_mfma_f32_16x16x32_bf16 v[40:43], a[136:139], v[28:31], v[40:43]\n"
        "s_and_b64 vcc, vcc, 1\n"
        "v_mfma_f32_16x16x32_bf16 v[44:47], a[140:143], v[28:31], v[44:47]\n"
        "ds_read_b128 v[28:31], v0 offset:19456\n"
        "s_waitcnt lgkmcnt(4)\n"
        "s_cbranch_vccz .Ls1_p11_%=\n"
        ".Ls1_p10_%=:\n"
        "v_mfma_f32_16x16x32_bf16 v[48:51], v[8:11], a[144:147], 0\n"
        "v_mfma_f32_16x16x32_bf16 v[52:55], v[8:11], a[148:151], 0\n"
        "ds_read_b128 v[8:11], v0 offset:1536\n"
        "v_mfma_f32_16x16x32_bf16 v[56:59], v[12:15], a[144:147], 0\n"
        "v_cvt_pk_bf16_f32 v32, v32, v33\n"
        "v_cvt_pk_bf16_f32 v33, v34, v35\n"
        "v_mfma_f32_16x16x32_bf16 v[60:63], v[12:15], a[148:151], 0\n"
        "ds_read_b128 v[12:15], v0 offset:19968\n"
        "s_waitcnt lgkmcnt(4)\n"
        "v_mfma_f32_16x16x32_bf16 v[48:51], v[16:19], a[152:155], v[48:51]\n"
        "v_cvt_pk_bf16_f32 v34, v36, v37\n"
        "v_cvt_pk_bf16_f32 v35, v38, v39\n"
        "v_mfma_f32_16x16x32_bf16 v[52:55], v[16:19], a[156:159], v[52:55]\n"
        "ds_read_b128 v[16:19], v0 offset:2048\n"
        "v_mfma_f32_16x16x32_bf16 v[56:59], v[20:23], a[152:155], v[56:59]\n"
        "buffer_store_dwordx4 v[32:35], v1, s[40:43], s72 offen offset:0 sc1\n"
        "v_mfma_f32_16x16x32_bf16 v[60:63], v[20:23], a[156:159], v[60:63]\n"
        "ds_read_b128 v[20:23], v0 offset:20480\n"
        "s_waitcnt lgkmcnt(4)\n"
        "v_mfma_f32_16x16x32_bf16 v[48:51], v[24:27], a[160:163], v[48:51]\n"
        "v_cvt_pk_bf16_f32 v40, v40, v41\n"
        "v_cvt_pk_bf16_f32 v41, v42, v43\n"
        "v_mfma_f32_16x16x32_bf16 v[52:55], v[24:27], a[164:167], v[52:55]\n"
        "ds_read_b128 v[24:27], v0 offset:2560\n"
        "v_cvt_pk_bf16_f32 v42, v44, v45\n"
        "v_mfma_f32_16x16x32_bf16 v[56:59], v[28:31], a[160:163], v[56:59]\n"
        "v_cvt_pk_bf16_f32 v43, v46, v47\n"
        "v_mfma_f32_16x16x32_bf16 v[60:63], v[28:31], a[164:167], v[60:63]\n"
        "ds_read_b128 v[28:31], v0 offset:20992\n"
        "s_waitcnt lgkmcnt(4)\n"
        "v_mfma_f32_16x16x32_bf16 v[48:51], v[8:11], a[168:171], v[48:51]\n"
        "v_mfma_f32_16x16x32_bf16 v[52:55], v[8:11], a[172:175], v[52:55]\n"
        "ds_read_b128 v[8:11], v0 offset:3072\n"
        "v_mfma_f32_16x16x32_bf16 v[56:59], v[12:15], a[168:171], v[56:59]\n"
        "buffer_store_dwordx4 v[40:43], v1, s[40:43], s72 offen offset:1024 sc1\n"
        "v_mfma_f32_16x16x32_bf16 v[60:63], v[12:15], a[172:175], v[60:63]\n"
        "ds_read_b128 v[12:15], v0 offset:21504\n"
        "s_waitcnt lgkmcnt(4)\n"
        "v_mfma_f32_16x16x32_bf16 v[48:51], v[16:19], a[176:179], v[48:51]\n"
        "v_mfma_f32_16x16x32_bf16 v[52:55], v[16:19], a[180:183], v[52:55]\n"
        "ds_read_b128 v[16:19], v0 offset:3584\n"
        "v_mfma_f32_16x16x32_bf16 v[56:59], v[20:23], a[176:179], v[56:59]\n"
        "v_mfma_f32_16x16x32_bf16 v[60:63], v[20:23], a[180:183], v[60:63]\n"
        "ds_read_b128 v[20:23], v0 offset:22016\n"
        "s_waitcnt lgkmcnt(4)\n"
        "v_mfma_f32_16x16x32_bf16 v[48:51], v[24:27], a[184:187], v[48:51]\n"
        "v_mfma_f32_16x16x32_bf16 v[52:55], v[24:27], a[188:191], v[52:55]\n"
        "ds_read_b128 v[24:27], v0 offset:4096\n"
        "v_mfma_f32_16x16x32_bf16 v[56:59], v[28:31], a[184:187], v[56:59]\n"
        "v_mfma_f32_16x16x32_bf16 v[60:63], v[28:31], a[188:191], v[60:63]\n"
        "ds_read_b128 v[28:31], v0 offset:22528\n"
        "s_waitcnt lgkmcnt(4)\n"
        "v_mfma_f32_16x16x32_bf16 v[48:51], v[8:11], a[192:195], v[48:51]\n"
        "v_mfma_f32_16x16x32_bf16 v[52:55], v[8:11], a[196:199], v[52:55]\n"
        "ds_read_b128 v[8:11], v0 offset:4608\n"
        "v_mfma_f32_16x16x32_bf16 v[56:59], v[12:15], a[192:195], v[56:59]\n"
        "v_mfma_f32_16x16x32_bf16 v[60:63], v[12:15], a[196:199], v[60:63]\n"
        "ds_read_b128 v[12:15], v0 offset:23040\n"
        "s_waitcnt lgkmcnt(4)\n"
        "v_mfma_f32_16x16x32_bf16 v[48:51], v[16:19], a[200:203], v[48:51]\n"
        "v_mfma_f32_16x16x32_bf16 v[52:55], v[16:19], a[204:207], v[52:55]\n"
        "ds_read_b128 v[16:19], v0 offset:5120\n"
        "v_mfma_f32_16x16x32_bf16 v[56:59], v[20:23], a[200:203], v[56:59]\n"
        "v_mfma_f32_16x16x32_bf16 v[60:63], v[20:23], a[204:207], v[60:63]\n"
        "ds_read_b128 v[20:23], v0 offset:23552\n"
        "s_waitcnt lgkmcnt(4)\n"
        "v_mfma_f32_16x16x32_bf16 v[48:51], v[24:27], a[208:211], v[48:51]\n"
        "buffer_load_dword v4, v2, s[40:43], s76 offen sc1\n"
        "v_mfma_f32_16x16x32_bf16 v[52:55], v[24:27], a[212:215], v[52:55]\n"
        "ds_read_b128 v[24:27], v0 offset:5632\n"
        "v_mfma_f32_16x16x32_bf16 v[56:59], v[28:31], a[208:211], v[56:59]\n"
        "v_mfma_f32_16x16x32_bf16 v[60:63], v[28:31], a[212:215], v[60:63]\n"
        "ds_read_b128 v[28:31], v0 offset:24064\n"
        "s_waitcnt lgkmcnt(4)\n"
        "v_mfma_f32_16x16x32_bf16 v[48:51], v[8:11], a[216:219], v[48:51]\n"
        "v_mfma_f32_16x16x32_bf16 v[52:55], v[8:11], a[220:223], v[52:55]\n"
        "ds_read_b128 v[8:11], v0 offset:6144\n"
        "v_mfma_f32_16x16x32_bf16 v[56:59], v[12:15], a[216:219], v[56:59]\n"
        "v_mfma_f32_16x16x32_bf16 v[60:63], v[12:15], a[220:223], v[60:63]\n"
        "ds_read_b128 v[12:15], v0 offset:24576\n"
        "s_waitcnt lgkmcnt(4)\n"
        "v_mfma_f32_16x16x32_bf16 v[48:51], v[16:19], a[224:227], v[48:51]\n"
        "v_mfma_f32_16x16x32_bf16 v[52:55], v[16:19], a[228:231], v[52:55]\n"
        "ds_read_b128 v[16:19], v0 offset:6656\n"
        "v_mfma_f32_16x16x32_bf16 v[56:59], v[20:23], a[224:227], v[56:59]\n"
        "v_mfma_f32_16x16x32_bf16 v[60:63], v[20:23], a[228:231], v[60:63]\n"
        "ds_read_b128 v[20:23], v0 offset:25088\n"
        "s_waitcnt lgkmcnt(4)\n"
        "v_mfma_f32_16x16x32_bf16 v[48:51], v[24:27], a[232:235], v[48:51]\n"
        "s_waitcnt vmcnt(1)\n"
        "v_mov_b32 v5, s74\n"
        "v_mfma_f32_16x16x32_bf16 v[52:55], v[24:27], a[236:239], v[52:55]\n"
        "ds_read_b128 v[24:27], v0 offset:7168\n"
        "buffer_store_dword v5, v3, s[40:43], s73 offen sc1\n"
        "v_mfma_f32_16x16x32_bf16 v[56:59], v[28:31], a[232:235], v[56:59]\n"
        "s_add_u32 s74, s74, 1\n"
        "s_add_u32 s72, s75, 1048576\n"
        "v_mfma_f32_16x16x32_bf16 v[60:63], v[28:31], a[236:239], v[60:63]\n"
        "ds_read_b128 v[28:31], v0 offset:25600\n"
        "s_waitcnt lgkmcnt(4)\n"
        "v_mfma_f32_16x16x32_bf16 v[48:51], v[8:11], a[240:243], v[48:51]\n"
        "v_mfma_f32_16x16x32_bf16 v[52:55], v[8:11], a[244:247], v[52:55]\n"
        "ds_read_b128 v[8:11], v0 offset:7680\n"
        "v_mfma_f32_16x16x32_bf16 v[56:59], v[12:15], a[240:243], v[56:59]\n"
        "v_mfma_f32_16x16x32_bf16 v[60:63], v[12:15], a[244:247], v[60:63]\n"
        "ds_read_b128 v[12:15], v0 offset:26112\n"
        "s_waitcnt lgkmcnt(4)\n"
        "v_mfma_f32_16x16x32_bf16 v[48:51], v[16:19], a[248:251], v[48:51]\n"
        "v_mfma_f32_16x16x32_bf16 v[52:55], v[16:19], a[252:255], v[52:55]\n"
        "ds_read_b128 v[16:19], v0 offset:8192\n"
        "v_mfma_f32_16x16x32_bf16 v[56:59], v[20:23], a[248:251], v[56:59]\n"
        "v_mfma_f32_16x16x32_bf16 v[60:63], v[20:23], a[252:255], v[60:63]\n"
        "ds_read_b128 v[20:23], v0 offset:26624\n"
        "s_waitcnt lgkmcnt(4)\n"
        "v_mfma_f32_16x16x32_bf16 v[48:51], v[24:27], v[80:83], v[48:51]\n"
        "v_mfma_f32_16x16x32_bf16 v[52:55], v[24:27], v[84:87], v[52:55]\n"
        "ds_read_b128 v[24:27], v0 offset:8704\n"
        "v_mfma_f32_16x16x32_bf16 v[56:59], v[28:31], v[80:83], v[56:59]\n"
        "v_mfma_f32_16x16x32_bf16 v[60:63], v[28:31], v[84:87], v[60:63]\n"
        "ds_read_b128 v[28:31], v0 offset:27136\n"
        "s_waitcnt lgkmcnt(4)\n"
        "v_mfma_f32_16x16x32_bf16 v[48:51], v[8:11], v[88:91], v[48:51]\n"
        "v_mfma_f32_16x16x32_bf16 v[52:55], v[8:11], v[92:95], v[52:55]\n"
        "ds_read_b128 v[8:11], v0 offset:0\n"
        "v_mfma_f32_16x16x32_bf16 v[56:59], v[12:15], v[88:91], v[56:59]\n"
        "v_mfma_f32_16x16x32_bf16 v[60:63], v[12:15], v[92:95], v[60:63]\n"
        "ds_read_b128 v[12:15], v0 offset:18432\n"
        "s_waitcnt lgkmcnt(4)\n"
        "v_mfma_f32_16x16x32_bf16 v[48:51], v[16:19], v[96:99], v[48:51]\n"
        "v_mfma_f32_16x16x32_bf16 v[52:55], v[16:19], v[100:103], v[52:55]\n"
        "ds_read_b128 v[16:19], v0 offset:512\n"
        "v_mfma_f32_16x16x32_bf16 v[56:59], v[20:23], v[96:99], v[56:59]\n"
        "v_mfma_f32_16x16x32_bf16 v[60:63], v[20:23], v[100:103], v[60:63]\n"
        "ds_read_b128 v[20:23], v0 offset:18944\n"
        "s_waitcnt lgkmcnt(4)\n"
        "v_mfma_f32_16x16x32_bf16 v[48:51], v[24:27], v[104:107], v[48:51]\n"
        "s_waitcnt vmcnt(1)\n"
        "v_cmp_gt_i32 vcc, s70, v4\n"
        "v_mfma_f32_16x16x32_bf16 v[52:55], v[24:27], v[108:111], v[52:55]\n"
        "ds_read_b128 v[24:27], v0 offset:1024\n"
        "v_mfma_f32_16x16x32_bf16 v[56:59], v[28:31], v[104:107], v[56:59]\n"
        "s_and_b64 vcc, vcc, 1\n"
        "v_mfma_f32_16x16x32_bf16 v[60:63], v[28:31], v[108:111], v[60:63]\n"
        "ds_read_b128 v[28:31], v0 offset:19456\n"
        "s_waitcnt lgkmcnt(4)\n"
        "s_cbranch_vccz .Ls1_p21_%=\n"
        ".Ls1_p20_%=:\n"
        "v_mfma_f32_16x16x32_bf16 v[64:67], v[112:115], v[8:11], 0\n"
        "v_mfma_f32_16x16x32_bf16 v[68:71], v[116:119], v[8:11], 0\n"
        "ds_read_b128 v[8:11], v0 offset:1536\n"
        "v_mfma_f32_16x16x32_bf16 v[72:75], v[112:115], v[12:15], 0\n"
        "v_cvt_pk_bf16_f32 v48, v48, v49\n"
        "v_cvt_pk_bf16_f32 v49, v50, v51\n"
        "v_mfma_f32_16x16x32_bf16 v[76:79], v[116:119], v[12:15], 0\n"
        "ds_read_b128 v[12:15], v0 offset:19968\n"
        "s_waitcnt lgkmcnt(4)\n"
        "v_mfma_f32_16x16x32_bf16 v[64:67], v[120:123], v[16:19], v[64:67]\n"
        "v_cvt_pk_bf16_f32 v50, v56, v57\n"
        "v_cvt_pk_bf16_f32 v51, v58, v59\n"
        "v_mfma_f32_16x16x32_bf16 v[68:71], v[124:127], v[16:19], v[68:71]\n"
        "ds_read_b128 v[16:19], v0 offset:2048\n"
        "v_mfma_f32_16x16x32_bf16 v[72:75], v[120:123], v[20:23], v[72:75]\n"
        "buffer_store_dwordx4 v[48:51], v1, s[40:43], s72 offen offset:0 sc1\n"
        "v_mfma_f32_16x16x32_bf16 v[76:79], v[124:127], v[20:23], v[76:79]\n"
        "ds_read_b128 v[20:23], v0 offset:20480\n"
        "s_waitcnt lgkmcnt(4)\n"
        "v_mfma_f32_16x16x32_bf16 v[64:67], v[128:131], v[24:27], v[64:67]\n"
        "v_cvt_pk_bf16_f32 v52, v52, v53\n"
        "v_cvt_pk_bf16_f32 v53, v54, v55\n"
        "v_mfma_f32_16x16x32_bf16 v[68:71], v[132:135], v[24:27], v[68:71]\n"
        "ds_read_b128 v[24:27], v0 offset:2560\n"
        "v_cvt_pk_bf16_f32 v54, v60, v61\n"
        "v_mfma_f32_16x16x32_bf16 v[72:75], v[128:131], v[28:31], v[72:75]\n"
        "v_cvt_pk_bf16_f32 v55, v62, v63\n"
        "v_mfma_f32_16x16x32_bf16 v[76:79], v[132:135], v[28:31], v[76:79]\n"
        "ds_read_b128 v[28:31], v0 offset:20992\n"
        "s_waitcnt lgkmcnt(4)\n"
        "v_mfma_f32_16x16x32_bf16 v[64:67], v[136:139], v[8:11], v[64:67]\n"
        "v_mfma_f32_16x16x32_bf16 v[68:71], v[140:143], v[8:11], v[68:71]\n"
        "ds_read_b128 v[8:11], v0 offset:3072\n"
        "v_mfma_f32_16x16x32_bf16 v[72:75], v[136:139], v[12:15], v[72:75]\n"
        "buffer_store_dwordx4 v[52:55], v1, s[40:43], s72 offen offset:1024 sc1\n"
        "v_mfma_f32_16x16x32_bf16 v[76:79], v[140:143], v[12:15], v[76:79]\n"
        "ds_read_b128 v[12:15], v0 offset:21504\n"
        "s_waitcnt lgkmcnt(4)\n"
        "v_mfma_f32_16x16x32_bf16 v[64:67], v[144:147], v[16:19], v[64:67]\n"
        "v_mfma_f32_16x16x32_bf16 v[68:71], v[148:151], v[16:19], v[68:71]\n"
        "ds_read_b128 v[16:19], v0 offset:3584\n"
        "v_mfma_f32_16x16x32_bf16 v[72:75], v[144:147], v[20:23], v[72:75]\n"
        "v_mfma_f32_16x16x32_bf16 v[76:79], v[148:151], v[20:23], v[76:79]\n"
        "ds_read_b128 v[20:23], v0 offset:22016\n"
        "s_waitcnt lgkmcnt(4)\n"
        "v_mfma_f32_16x16x32_bf16 v[64:67], v[152:155], v[24:27], v[64:67]\n"
        "v_mfma_f32_16x16x32_bf16 v[68:71], v[156:159], v[24:27], v[68:71]\n"
        "ds_read_b128 v[24:27], v0 offset:4096\n"
        "v_mfma_f32_16x16x32_bf16 v[72:75], v[152:155], v[28:31], v[72:75]\n"
        "v_mfma_f32_16x16x32_bf16 v[76:79], v[156:159], v[28:31], v[76:79]\n"
        "ds_read_b128 v[28:31], v0 offset:22528\n"
        "s_waitcnt lgkmcnt(4)\n"
        "v_mfma_f32_16x16x32_bf16 v[64:67], v[160:163], v[8:11], v[64:67]\n"
        "v_mfma_f32_16x16x32_bf16 v[68:71], v[164:167], v[8:11], v[68:71]\n"
        "ds_read_b128 v[8:11], v0 offset:4608\n"
        "v_mfma_f32_16x16x32_bf16 v[72:75], v[160:163], v[12:15], v[72:75]\n"
        "v_mfma_f32_16x16x32_bf16 v[76:79], v[164:167], v[12:15], v[76:79]\n"
        "ds_read_b128 v[12:15], v0 offset:23040\n"
        "s_waitcnt lgkmcnt(4)\n"
        "v_mfma_f32_16x16x32_bf16 v[64:67], v[168:171], v[16:19], v[64:67]\n"
        "v_mfma_f32_16x16x32_bf16 v[68:71], v[172:175], v[16:19], v[68:71]\n"
        "ds_read_b128 v[16:19], v0 offset:5120\n"
        "v_mfma_f32_16x16x32_bf16 v[72:75], v[168:171], v[20:23], v[72:75]\n"
        "v_mfma_f32_16x16x32_bf16 v[76:79], v[172:175], v[20:23], v[76:79]\n"
        "ds_read_b128 v[20:23], v0 offset:23552\n"
        "s_waitcnt lgkmcnt(4)\n"
        "v_mfma_f32_16x16x32_bf16 v[64:67], v[176:179], v[24:27], v[64:67]\n"
        "buffer_load_dword v4, v2, s[40:43], s76 offen sc1\n"
        "v_mfma_f32_16x16x32_bf16 v[68:71], v[180:183], v[24:27], v[68:71]\n"
        "ds_read_b128 v[24:27], v0 offset:5632\n"
        "v_mfma_f32_16x16x32_bf16 v[72:75], v[176:179], v[28:31], v[72:75]\n"
        "v_mfma_f32_16x16x32_bf16 v[76:79], v[180:183], v[28:31], v[76:79]\n"
        "ds_read_b128 v[28:31], v0 offset:24064\n"
        "s_waitcnt lgkmcnt(4)\n"
        "v_mfma_f32_16x16x32_bf16 v[64:67], v[184:187], v[8:11], v[64:67]\n"
        "v_mfma_f32_16x16x32_bf16 v[68:71], v[188:191], v[8:11], v[68:71]\n"
        "ds_read_b128 v[8:11], v0 offset:6144\n"
        "v_mfma_f32_16x16x32_bf16 v[72:75], v[184:187], v[12:15], v[72:75]\n"
        "v_mfma_f32_16x16x32_bf16 v[76:79], v[188:191], v[12:15], v[76:79]\n"
        "ds_read_b128 v[12:15], v0 offset:24576\n"
        "s_waitcnt lgkmcnt(4)\n"
        "v_mfma_f32_16x16x32_bf16 v[64:67], v[192:195], v[16:19], v[64:67]\n"
        "s_add_u32 s78, s78, 36864\n"
        "v_mfma_f32_16x16x32_bf16 v[68:71], v[196:199], v[16:19], v[68:71]\n"
        "ds_read_b128 v[16:19], v0 offset:6656\n"
        "v_mfma_f32_16x16x32_bf16 v[72:75], v[192:195], v[20:23], v[72:75]\n"
        "s_cmp_eq_u32 s78, 110592\n"
        "s_cselect_b32 s78, 0, s78\n"
        "v_mfma_f32_16x16x32_bf16 v[76:79], v[196:199], v[20:23], v[76:79]\n"
        "ds_read_b128 v[20:23], v0 offset:25088\n"
        "s_waitcnt lgkmcnt(4)\n"
        "v_mfma_f32_16x16x32_bf16 v[64:67], v[200:203], v[24:27], v[64:67]\n"
        "s_waitcnt vmcnt(1)\n"
        "v_mov_b32 v5, s74\n"
        "v_mfma_f32_16x16x32_bf16 v[68:71], v[204:207], v[24:27], v[68:71]\n"
        "ds_read_b128 v[24:27], v0 offset:7168\n"
        "buffer_store_dword v5, v3, s[40:43], s73 offen sc1\n"
        "v_mfma_f32_16x16x32_bf16 v[72:75], v[200:203], v[28:31], v[72:75]\n"
        "s_add_u32 s74, s74, 1\n"
        "s_add_u32 s72, s75, 0\n"
        "v_mfma_f32_16x16x32_bf16 v[76:79], v[204:207], v[28:31], v[76:79]\n"
        "ds_read_b128 v[28:31], v0 offset:25600\n"
        "s_waitcnt lgkmcnt(4)\n"
        "v_mfma_f32_16x16x32_bf16 v[64:67], v[208:211], v[8:11], v[64:67]\n"
        "v_mfma_f32_16x16x32_bf16 v[68:71], v[212:215], v[8:11], v[68:71]\n"
        "ds_read_b128 v[8:11], v0 offset:7680\n"
        "v_mfma_f32_16x16x32_bf16 v[72:75], v[208:211], v[12:15], v[72:75]\n"
        "v_mfma_f32_16x16x32_bf16 v[76:79], v[212:215], v[12:15], v[76:79]\n"
        "ds_read_b128 v[12:15], v0 offset:26112\n"
        "s_waitcnt lgkmcnt(4)\n"
        "v_mfma_f32_16x16x32_bf16 v[64:67], v[216:219], v[16:19], v[64:67]\n"
        "v_mfma_f32_16x16x32_bf16 v[68:71], v[220:223], v[16:19], v[68:71]\n"
        "ds_read_b128 v[16:19], v0 offset:8192\n"
        "v_mfma_f32_16x16x32_bf16 v[72:75], v[216:219], v[20:23], v[72:75]\n"
        "v_mfma_f32_16x16x32_bf16 v[76:79], v[220:223], v[20:23], v[76:79]\n"
        "ds_read_b128 v[20:23], v0 offset:26624\n"
        "s_waitcnt lgkmcnt(4)\n"
        "v_mfma_f32_16x16x32_bf16 v[64:67], v[224:227], v[24:27], v[64:67]\n"
        "v_mfma_f32_16x16x32_bf16 v[68:71], v[228:231], v[24:27], v[68:71]\n"
        "ds_read_b128 v[24:27], v0 offset:8704\n"
        "v_mfma_f32_16x16x32_bf16 v[72:75], v[224:227], v[28:31], v[72:75]\n"
        "v_mfma_f32_16x16x32_bf16 v[76:79], v[228:231], v[28:31], v[76:79]\n"
        "ds_read_b128 v[28:31], v0 offset:27136\n"
        "s_waitcnt lgkmcnt(4)\n"
        "v_mfma_f32_16x16x32_bf16 v[64:67], v[232:235], v[8:11], v[64:67]\n"
        "s_sub_u32 s65, s69, s68\n"
        "s_cmp_eq_u32 s65, 2\n"
        "s_cbranch_scc0 .Ls1_tail_%=\n"
        "s_barrier\n"
        "v_add_u32 v0, s78, v6\n"
        "v_mfma_f32_16x16x32_bf16 v[68:71], v[236:239], v[8:11], v[68:71]\n"
        "ds_read_b128 v[8:11], v0 offset:0\n"
        "v_mfma_f32_16x16x32_bf16 v[72:75], v[232:235], v[12:15], v[72:75]\n"
        "s_cmp_lt_u32 s69, 64\n"
        "s_cselect_b32 s70, s69, 2147483647\n"
        "v_mfma_f32_16x16x32_bf16 v[76:79], v[236:239], v[12:15], v[76:79]\n"
        "ds_read_b128 v[12:15], v0 offset:18432\n"
        "s_waitcnt lgkmcnt(4)\n"
        "v_mfma_f32_16x16x32_bf16 v[64:67], v[240:243], v[16:19], v[64:67]\n"
        "v_mfma_f32_16x16x32_bf16 v[68:71], v[244:247], v[16:19], v[68:71]\n"
        "ds_read_b128 v[16:19], v0 offset:512\n"
        "v_mfma_f32_16x16x32_bf16 v[72:75], v[240:243], v[20:23], v[72:75]\n"
        "v_mfma_f32_16x16x32_bf16 v[76:79], v[244:247], v[20:23], v[76:79]\n"
        "ds_read_b128 v[20:23], v0 offset:18944\n"
        "s_waitcnt lgkmcnt(4)\n"
        "v_mfma_f32_16x16x32_bf16 v[64:67], v[248:251], v[24:27], v[64:67]\n"
        "s_waitcnt vmcnt(1)\n"
        "v_cmp_gt_i32 vcc, s70, v4\n"
        "v_mfma_f32_16x16x32_bf16 v[68:71], v[252:255], v[24:27], v[68:71]\n"
        "ds_read_b128 v[24:27], v0 offset:1024\n"
        "v_mfma_f32_16x16x32_bf16 v[72:75], v[248:251], v[28:31], v[72:75]\n"
        "s_and_b64 vcc, vcc, 1\n"
        "v_mfma_f32_16x16x32_bf16 v[76:79], v[252:255], v[28:31], v[76:79]\n"
        "ds_read_b128 v[28:31], v0 offset:19456\n"
        "s_waitcnt lgkmcnt(4)\n"
        "s_cbranch_vccz .Ls1_p01_%=\n"
        "s_branch .Ls1_p00_%=\n"
        ".Ls1_p01_%=:\n"
        "v_mfma_f32_16x16x32_bf16 v[32:35], a[0:3], v[8:11], 0\n"
        "s_add_u32 m0, s77, s61\n"
        "v_mfma_f32_16x16x32_bf16 v[36:39], a[4:7], v[8:11], 0\n"
        "ds_read_b128 v[8:11], v0 offset:1536\n"
        "v_mfma_f32_16x16x32_bf16 v[40:43], a[0:3], v[12:15], 0\n"
        "v_cvt_pk_bf16_f32 v64, v64, v65\n"
        "v_cvt_pk_bf16_f32 v65, v66, v67\n"
        "v_mfma_f32_16x16x32_bf16 v[44:47], a[4:7], v[12:15], 0\n"
        "ds_read_b128 v[12:15], v0 offset:19968\n"
        "s_waitcnt lgkmcnt(4)\n"
        "v_mfma_f32_16x16x32_bf16 v[32:35], a[8:11], v[16:19], v[32:35]\n"
        "v_cvt_pk_bf16_f32 v66, v68, v69\n"
        "v_cvt_pk_bf16_f32 v67, v70, v71\n"
        "v_mfma_f32_16x16x32_bf16 v[36:39], a[12:15], v[16:19], v[36:39]\n"
        "ds_read_b128 v[16:19], v0 offset:2048\n"
        "v_mfma_f32_16x16x32_bf16 v[40:43], a[8:11], v[20:23], v[40:43]\n"
        "buffer_store_dwordx4 v[64:67], v1, s[40:43], s72 offen offset:0 sc1\n"
        "v_mfma_f32_16x16x32_bf16 v[44:47], a[12:15], v[20:23], v[44:47]\n"
        "ds_read_b128 v[20:23], v0 offset:20480\n"
        "s_waitcnt lgkmcnt(4)\n"
        "v_mfma_f32_16x16x32_bf16 v[32:35], a[16:19], v[24:27], v[32:35]\n"
        "v_cvt_pk_bf16_f32 v72, v72, v73\n"
        "v_cvt_pk_bf16_f32 v73, v74, v75\n"
        "v_mfma_f32_16x16x32_bf16 v[36:39], a[20:23], v[24:27], v[36:39]\n"
        "ds_read_b128 v[24:27], v0 offset:2560\n"
        "v_cvt_pk_bf16_f32 v74, v76, v77\n"
        "v_mfma_f32_16x16x32_bf16 v[40:43], a[16:19], v[28:31], v[40:43]\n"
        "v_cvt_pk_bf16_f32 v75, v78, v79\n"
        "v_mfma_f32_16x16x32_bf16 v[44:47], a[20:23], v[28:31], v[44:47]\n"
        "ds_read_b128 v[28:31], v0 offset:20992\n"
        "s_waitcnt lgkmcnt(4)\n"
        "v_mfma_f32_16x16x32_bf16 v[32:35], a[24:27], v[8:11], v[32:35]\n"
        "s_bitcmp1_b64 s[52:53], s69\n"
        "s_cselect_b32 s65, 8192, 0\n"
        "v_mfma_f32_16x16x32_bf16 v[36:39], a[28:31], v[8:11], v[36:39]\n"
        "ds_read_b128 v[8:11], v0 offset:3072\n"
        "s_lshl_b32 s71, s69, 21\n"
        "v_mfma_f32_16x16x32_bf16 v[40:43], a[24:27], v[12:15], v[40:43]\n"
        "buffer_store_dwordx4 v[72:75], v1, s[40:43], s72 offen offset:1024 sc1\n"
        "v_mfma_f32_16x16x32_bf16 v[44:47], a[28:31], v[12:15], v[44:47]\n"
        "ds_read_b128 v[12:15], v0 offset:21504\n"
        "s_waitcnt lgkmcnt(4)\n"
        "v_mfma_f32_16x16x32_bf16 v[32:35], a[32:35], v[16:19], v[32:35]\n"
        "s_add_u32 s71, s71, s65\n"
        "s_add_u32 s71, s71, s58\n"
        "v_mfma_f32_16x16x32_bf16 v[36:39], a[36:39], v[16:19], v[36:39]\n"
        "ds_read_b128 v[16:19], v0 offset:3584\n"
        "buffer_load_dwordx4 v1, s[40:43], s71 offen offset:0 sc1 lds\n"
        "v_mfma_f32_16x16x32_bf16 v[40:43], a[32:35], v[20:23], v[40:43]\n"
        "s_add_u32 s69, s69, 1\n"
        "s_add_u32 s77, s77, 36864\n"
        "v_mfma_f32_16x16x32_bf16 v[44:47], a[36:39], v[20:23], v[44:47]\n"
        "ds_read_b128 v[20:23], v0 offset:22016\n"
        "s_waitcnt lgkmcnt(4)\n"
        "v_mfma_f32_16x16x32_bf16 v[32:35], a[40:43], v[24:27], v[32:35]\n"
        "s_waitcnt vmcnt(3)\n"
        "s_barrier\n"
        "v_mfma_f32_16x16x32_bf16 v[36:39], a[44:47], v[24:27], v[36:39]\n"
        "ds_read_b128 v[24:27], v0 offset:4096\n"
        "v_mfma_f32_16x16x32_bf16 v[40:43], a[40:43], v[28:31], v[40:43]\n"
        "buffer_load_dwordx4 v1, s[40:43], s71 offen offset:1024 sc1 lds\n"
        "v_mfma_f32_16x16x32_bf16 v[44:47], a[44:47], v[28:31], v[44:47]\n"
        "ds_read_b128 v[28:31], v0 offset:22528\n"
        "s_waitcnt lgkmcnt(4)\n"
        "v_mfma_f32_16x16x32_bf16 v[32:35], a[48:51], v[8:11], v[32:35]\n"
        "s_cmp_eq_u32 s77, 110592\n"
        "s_cselect_b32 s77, 0, s77\n"
        "v_mfma_f32_16x16x32_bf16 v[36:39], a[52:55], v[8:11], v[36:39]\n"
        "ds_read_b128 v[8:11], v0 offset:4608\n"
        "s_mov_b32 s70, 2147483647\n"
        "v_mfma_f32_16x16x32_bf16 v[40:43], a[48:51], v[12:15], v[40:43]\n"
        "v_mfma_f32_16x16x32_bf16 v[44:47], a[52:55], v[12:15], v[44:47]\n"
        "ds_read_b128 v[12:15], v0 offset:23040\n"
        "s_waitcnt lgkmcnt(4)\n"
        "v_mfma_f32_16x16x32_bf16 v[32:35], a[56:59], v[16:19], v[32:35]\n"
        "buffer_load_dwordx4 v1, s[40:43], s71 offen offset:2048 sc1 lds\n"
        "v_mfma_f32_16x16x32_bf16 v[36:39], a[60:63], v[16:19], v[36:39]\n"
        "ds_read_b128 v[16:19], v0 offset:5120\n"
        "v_mfma_f32_16x16x32_bf16 v[40:43], a[56:59], v[20:23], v[40:43]\n"
        "v_mfma_f32_16x16x32_bf16 v[44:47], a[60:63], v[20:23], v[44:47]\n"
        "ds_read_b128 v[20:23], v0 offset:23552\n"
        "s_waitcnt lgkmcnt(4)\n"
        "v_mfma_f32_16x16x32_bf16 v[32:35], a[64:67], v[24:27], v[32:35]\n"
        "v_mfma_f32_16x16x32_bf16 v[36:39], a[68:71], v[24:27], v[36:39]\n"
        "ds_read_b128 v[24:27], v0 offset:5632\n"
        "v_mfma_f32_16x16x32_bf16 v[40:43], a[64:67], v[28:31], v[40:43]\n"
        "buffer_load_dwordx4 v1, s[40:43], s71 offen offset:3072 sc1 lds\n"
        "s_add_u32 m0, m0, 4096\n"
        "v_mfma_f32_16x16x32_bf16 v[44:47], a[68:71], v[28:31], v[44:47]\n"
        "ds_read_b128 v[28:31], v0 offset:24064\n"
        "s_waitcnt lgkmcnt(4)\n"
        "v_mfma_f32_16x16x32_bf16 v[32:35], a[72:75], v[8:11], v[32:35]\n"
        "s_add_u32 s71, s71, 4096\n"
        "s_mul_i32 s65, s48, -15360\n"
        "v_mfma_f32_16x16x32_bf16 v[36:39], a[76:79], v[8:11], v[36:39]\n"
        "ds_read_b128 v[8:11], v0 offset:6144\n"
        "v_mfma_f32_16x16x32_bf16 v[40:43], a[72:75], v[12:15], v[40:43]\n"
        "v_mfma_f32_16x16x32_bf16 v[44:47], a[76:79], v[12:15], v[44:47]\n"
        "ds_read_b128 v[12:15], v0 offset:24576\n"
        "s_waitcnt lgkmcnt(4)\n"
        "v_mfma_f32_16x16x32_bf16 v[32:35], a[80:83], v[16:19], v[32:35]\n"
        "buffer_load_dwordx4 v1, s[40:43], s71 offen offset:0 sc1 lds\n"
        "v_mfma_f32_16x16x32_bf16 v[36:39], a[84:87], v[16:19], v[36:39]\n"
        "ds_read_b128 v[16:19], v0 offset:6656\n"
        "v_mfma_f32_16x16x32_bf16 v[40:43], a[80:83], v[20:23], v[40:43]\n"
        "v_mfma_f32_16x16x32_bf16 v[44:47], a[84:87], v[20:23], v[44:47]\n"
        "ds_read_b128 v[20:23], v0 offset:25088\n"
        "s_waitcnt lgkmcnt(4)\n"
        "v_mfma_f32_16x16x32_bf16 v[32:35], a[88:91], v[24:27], v[32:35]\n"
        "s_waitcnt vmcnt(5)\n"
        "v_mov_b32 v5, s74\n"
        "v_mfma_f32_16x16x32_bf16 v[36:39], a[92:95], v[24:27], v[36:39]\n"
        "ds_read_b128 v[24:27], v0 offset:7168\n"
        "buffer_store_dword v5, v3, s[40:43], s73 offen sc1\n"
        "v_mfma_f32_16x16x32_bf16 v[40:43], a[88:91], v[28:31], v[40:43]\n"
        "buffer_load_dwordx4 v1, s[40:43], s71 offen offset:1024 sc1 lds\n"
        "s_add_u32 s68, s68, 1\n"
        "v_mfma_f32_16x16x32_bf16 v[44:47], a[92:95], v[28:31], v[44:47]\n"
        "ds_read_b128 v[28:31], v0 offset:25600\n"
        "s_waitcnt lgkmcnt(4)\n"
        "v_mfma_f32_16x16x32_bf16 v[32:35], a[96:99], v[8:11], v[32:35]\n"
        "s_bitcmp1_b64 s[52:53], s68\n"
        "s_cselect_b32 s66, 8192, 0\n"
        "v_mfma_f32_16x16x32_bf16 v[36:39], a[100:103], v[8:11], v[36:39]\n"
        "ds_read_b128 v[8:11], v0 offset:7680\n"
        "s_lshl_b32 s75, s68, 21\n"
        "v_mfma_f32_16x16x32_bf16 v[40:43], a[96:99], v[12:15], v[40:43]\n"
        "s_add_u32 s75, s75, s66\n"
        "s_add_u32 s75, s75, s59\n"
        "v_mfma_f32_16x16x32_bf16 v[44:47], a[100:103], v[12:15], v[44:47]\n"
        "ds_read_b128 v[12:15], v0 offset:26112\n"
        "s_waitcnt lgkmcnt(4)\n"
        "v_mfma_f32_16x16x32_bf16 v[32:35], a[104:107], v[16:19], v[32:35]\n"
        "buffer_load_dwordx4 v1, s[40:43], s71 offen offset:2048 sc1 lds\n"
        "s_mul_i32 s74, s68, 3\n"
        "v_mfma_f32_16x16x32_bf16 v[36:39], a[108:111], v[16:19], v[36:39]\n"
        "ds_read_b128 v[16:19], v0 offset:8192\n"
        "s_mov_b32 s73, s60\n"
        "v_mfma_f32_16x16x32_bf16 v[40:43], a[104:107], v[20:23], v[40:43]\n"
        "s_add_u32 s72, s75, 524288\n"
        "v_mfma_f32_16x16x32_bf16 v[44:47], a[108:111], v[20:23], v[44:47]\n"
        "ds_read_b128 v[20:23], v0 offset:26624\n"
        "s_waitcnt lgkmcnt(4)\n"
        "v_mfma_f32_16x16x32_bf16 v[32:35], a[112:115], v[24:27], v[32:35]\n"
        "v_mfma_f32_16x16x32_bf16 v[36:39], a[116:119], v[24:27], v[36:39]\n"
        "ds_read_b128 v[24:27], v0 offset:8704\n"
        "v_mfma_f32_16x16x32_bf16 v[40:43], a[112:115], v[28:31], v[40:43]\n"
        "buffer_load_dwordx4 v1, s[40:43], s71 offen offset:3072 sc1 lds\n"
        "s_add_u32 m0, m0, 4096\n"
        "v_mfma_f32_16x16x32_bf16 v[44:47], a[116:119], v[28:31], v[44:47]\n"
        "ds_read_b128 v[28:31], v0 offset:27136\n"
        "s_waitcnt lgkmcnt(4)\n"
        "v_mfma_f32_16x16x32_bf16 v[32:35], a[120:123], v[8:11], v[32:35]\n"
        "s_add_u32 s71, s71, s65\n"
        "s_add_u32 s71, s71, 61440\n"
        "v_mfma_f32_16x16x32_bf16 v[36:39], a[124:127], v[8:11], v[36:39]\n"
        "ds_read_b128 v[8:11], v0 offset:0\n"
        "v_mfma_f32_16x16x32_bf16 v[40:43], a[120:123], v[12:15], v[40:43]\n"
        "v_mfma_f32_16x16x32_bf16 v[44:47], a[124:127], v[12:15], v[44:47]\n"
        "ds_read_b128 v[12:15], v0 offset:18432\n"
        "s_waitcnt lgkmcnt(4)\n"
        "v_mfma_f32_16x16x32_bf16 v[32:35], a[128:131], v[16:19], v[32:35]\n"
        "buffer_load_dwordx4 v1, s[40:43], s71 offen offset:0 sc1 lds\n"
        "v_mfma_f32_16x16x32_bf16 v[36:39], a[132:135], v[16:19], v[36:39]\n"
        "ds_read_b128 v[16:19], v0 offset:512\n"
        "v_mfma_f32_16x16x32_bf16 v[40:43], a[128:131], v[20:23], v[40:43]\n"
        "v_mfma_f32_16x16x32_bf16 v[44:47], a[132:135], v[20:23], v[44:47]\n"
        "ds_read_b128 v[20:23], v0 offset:18944\n"
        "s_waitcnt lgkmcnt(4)\n"
        "v_mfma_f32_16x16x32_bf16 v[32:35], a[136:139], v[24:27], v[32:35]\n"
        "v_mfma_f32_16x16x32_bf16 v[36:39], a[140:143], v[24:27], v[36:39]\n"
        "ds_read_b128 v[24:27], v0 offset:1024\n"
        "v_mfma_f32_16x16x32_bf16 v[40:43], a[136:139], v[28:31], v[40:43]\n"
        "v_mfma_f32_16x16x32_bf16 v[44:47], a[140:143], v[28:31], v[44:47]\n"
        "ds_read_b128 v[28:31], v0 offset:19456\n"
        "s_waitcnt lgkmcnt(4)\n"
        "s_branch .Ls1_p10_%=\n"
        ".Ls1_p11_%=:\n"
        "v_mfma_f32_16x16x32_bf16 v[48:51], v[8:11], a[144:147], 0\n"
        "s_add_u32 m0, s77, s61\n"
        "v_mfma_f32_16x16x32_bf16 v[52:55], v[8:11], a[148:151], 0\n"
        "ds_read_b128 v[8:11], v0 offset:1536\n"
        "v_mfma_f32_16x16x32_bf16 v[56:59], v[12:15], a[144:147], 0\n"
        "v_cvt_pk_bf16_f32 v32, v32, v33\n"
        "v_cvt_pk_bf16_f32 v33, v34, v35\n"
        "v_mfma_f32_16x16x32_bf16 v[60:63], v[12:15], a[148:151], 0\n"
        "ds_read_b128 v[12:15], v0 offset:19968\n"
        "s_waitcnt lgkmcnt(4)\n"
        "v_mfma_f32_16x16x32_bf16 v[48:51], v[16:19], a[152:155], v[48:51]\n"
        "v_cvt_pk_bf16_f32 v34, v36, v37\n"
        "v_cvt_pk_bf16_f32 v35, v38, v39\n"
        "v_mfma_f32_16x16x32_bf16 v[52:55], v[16:19], a[156:159], v[52:55]\n"
        "ds_read_b128 v[16:19], v0 offset:2048\n"
        "v_mfma_f32_16x16x32_bf16 v[56:59], v[20:23], a[152:155], v[56:59]\n"
        "buffer_store_dwordx4 v[32:35], v1, s[40:43], s72 offen offset:0 sc1\n"
        "v_mfma_f32_16x16x32_bf16 v[60:63], v[20:23], a[156:159], v[60:63]\n"
        "ds_read_b128 v[20:23], v0 offset:20480\n"
        "s_waitcnt lgkmcnt(4)\n"
        "v_mfma_f32_16x16x32_bf16 v[48:51], v[24:27], a[160:163], v[48:51]\n"
        "v_cvt_pk_bf16_f32 v40, v40, v41\n"
        "v_cvt_pk_bf16_f32 v41, v42, v43\n"
        "v_mfma_f32_16x16x32_bf16 v[52:55], v[24:27], a[164:167], v[52:55]\n"
        "ds_read_b128 v[24:27], v0 offset:2560\n"
        "v_cvt_pk_bf16_f32 v42, v44, v45\n"
        "v_mfma_f32_16x16x32_bf16 v[56:59], v[28:31], a[160:163], v[56:59]\n"
        "v_cvt_pk_bf16_f32 v43, v46, v47\n"
        "v_mfma_f32_16x16x32_bf16 v[60:63], v[28:31], a[164:167], v[60:63]\n"
        "ds_read_b128 v[28:31], v0 offset:20992\n"
        "s_waitcnt lgkmcnt(4)\n"
        "v_mfma_f32_16x16x32_bf16 v[48:51], v[8:11], a[168:171], v[48:51]\n"
        "s_bitcmp1_b64 s[52:53], s69\n"
        "s_cselect_b32 s65, 8192, 0\n"
        "v_mfma_f32_16x16x32_bf16 v[52:55], v[8:11], a[172:175], v[52:55]\n"
        "ds_read_b128 v[8:11], v0 offset:3072\n"
        "s_lshl_b32 s71, s69, 21\n"
        "v_mfma_f32_16x16x32_bf16 v[56:59], v[12:15], a[168:171], v[56:59]\n"
        "buffer_store_dwordx4 v[40:43], v1, s[40:43], s72 offen offset:1024 sc1\n"
        "v_mfma_f32_16x16x32_bf16 v[60:63], v[12:15], a[172:175], v[60:63]\n"
        "ds_read_b128 v[12:15], v0 offset:21504\n"
        "s_waitcnt lgkmcnt(4)\n"
        "v_mfma_f32_16x16x32_bf16 v[48:51], v[16:19], a[176:179], v[48:51]\n"
        "s_add_u32 s71, s71, s65\n"
        "s_add_u32 s71, s71, s58\n"
        "v_mfma_f32_16x16x32_bf16 v[52:55], v[16:19], a[180:183], v[52:55]\n"
        "ds_read_b128 v[16:19], v0 offset:3584\n"
        "buffer_load_dwordx4 v1, s[40:43], s71 offen offset:0 sc1 lds\n"
        "v_mfma_f32_16x16x32_bf16 v[56:59], v[20:23], a[176:179], v[56:59]\n"
        "s_add_u32 s69, s69, 1\n"
        "s_add_u32 s77, s77, 36864\n"
        "v_mfma_f32_16x16x32_bf16 v[60:63], v[20:23], a[180:183], v[60:63]\n"
        "ds_read_b128 v[20:23], v0 offset:22016\n"
        "s_waitcnt lgkmcnt(4)\n"
        "v_mfma_f32_16x16x32_bf16 v[48:51], v[24:27], a[184:187], v[48:51]\n"
        "s_cmp_eq_u32 s77, 110592\n"
        "s_cselect_b32 s77, 0, s77\n"
        "v_mfma_f32_16x16x32_bf16 v[52:55], v[24:27], a[188:191], v[52:55]\n"
        "ds_read_b128 v[24:27], v0 offset:4096\n"
        "s_mov_b32 s70, 2147483647\n"
        "v_mfma_f32_16x16x32_bf16 v[56:59], v[28:31], a[184:187], v[56:59]\n"
        "buffer_load_dwordx4 v1, s[40:43], s71 offen offset:1024 sc1 lds\n"
        "v_mfma_f32_16x16x32_bf16 v[60:63], v[28:31], a[188:191], v[60:63]\n"
        "ds_read_b128 v[28:31], v0 offset:22528\n"
        "s_waitcnt lgkmcnt(4)\n"
        "v_mfma_f32_16x16x32_bf16 v[48:51], v[8:11], a[192:195], v[48:51]\n"
        "v_mfma_f32_16x16x32_bf16 v[52:55], v[8:11], a[196:199], v[52:55]\n"
        "ds_read_b128 v[8:11], v0 offset:4608\n"
        "v_mfma_f32_16x16x32_bf16 v[56:59], v[12:15], a[192:195], v[56:59]\n"
        "v_mfma_f32_16x16x32_bf16 v[60:63], v[12:15], a[196:199], v[60:63]\n"
        "ds_read_b128 v[12:15], v0 offset:23040\n"
        "s_waitcnt lgkmcnt(4)\n"
        "v_mfma_f32_16x16x32_bf16 v[48:51], v[16:19], a[200:203], v[48:51]\n"
        "buffer_load_dwordx4 v1, s[40:43], s71 offen offset:2048 sc1 lds\n"
        "v_mfma_f32_16x16x32_bf16 v[52:55], v[16:19], a[204:207], v[52:55]\n"
        "ds_read_b128 v[16:19], v0 offset:5120\n"
        "v_mfma_f32_16x16x32_bf16 v[56:59], v[20:23], a[200:203], v[56:59]\n"
        "v_mfma_f32_16x16x32_bf16 v[60:63], v[20:23], a[204:207], v[60:63]\n"
        "ds_read_b128 v[20:23], v0 offset:23552\n"
        "s_waitcnt lgkmcnt(4)\n"
        "v_mfma_f32_16x16x32_bf16 v[48:51], v[24:27], a[208:211], v[48:51]\n"
        "v_mfma_f32_16x16x32_bf16 v[52:55], v[24:27], a[212:215], v[52:55]\n"
        "ds_read_b128 v[24:27], v0 offset:5632\n"
        "v_mfma_f32_16x16x32_bf16 v[56:59], v[28:31], a[208:211], v[56:59]\n"
        "buffer_load_dwordx4 v1, s[40:43], s71 offen offset:3072 sc1 lds\n"
        "s_add_u32 m0, m0, 4096\n"
        "v_mfma_f32_16x16x32_bf16 v[60:63], v[28:31], a[212:215], v[60:63]\n"
        "ds_read_b128 v[28:31], v0 offset:24064\n"
        "s_waitcnt lgkmcnt(4)\n"
        "v_mfma_f32_16x16x32_bf16 v[48:51], v[8:11], a[216:219], v[48:51]\n"
        "s_add_u32 s71, s71, 4096\n"
        "s_mul_i32 s65, s48, -15360\n"
        "v_mfma_f32_16x16x32_bf16 v[52:55], v[8:11], a[220:223], v[52:55]\n"
        "ds_read_b128 v[8:11], v0 offset:6144\n"
        "v_mfma_f32_16x16x32_bf16 v[56:59], v[12:15], a[216:219], v[56:59]\n"
        "v_mfma_f32_16x16x32_bf16 v[60:63], v[12:15], a[220:223], v[60:63]\n"
        "ds_read_b128 v[12:15], v0 offset:24576\n"
        "s_waitcnt lgkmcnt(4)\n"
        "v_mfma_f32_16x16x32_bf16 v[48:51], v[16:19], a[224:227], v[48:51]\n"
        "buffer_load_dwordx4 v1, s[40:43], s71 offen offset:0 sc1 lds\n"
        "v_mfma_f32_16x16x32_bf16 v[52:55], v[16:19], a[228:231], v[52:55]\n"
        "ds_read_b128 v[16:19], v0 offset:6656\n"
        "v_mfma_f32_16x16x32_bf16 v[56:59], v[20:23], a[224:227], v[56:59]\n"
        "v_mfma_f32_16x16x32_bf16 v[60:63], v[20:23], a[228:231], v[60:63]\n"
        "ds_read_b128 v[20:23], v0 offset:25088\n"
        "s_waitcnt lgkmcnt(4)\n"
        "v_mfma_f32_16x16x32_bf16 v[48:51], v[24:27], a[232:235], v[48:51]\n"
        "s_waitcnt vmcnt(5)\n"
        "v_mov_b32 v5, s74\n"
        "v_mfma_f32_16x16x32_bf16 v[52:55], v[24:27], a[236:239], v[52:55]\n"
        "ds_read_b128 v[24:27], v0 offset:7168\n"
        "buffer_store_dword v5, v3, s[40:43], s73 offen sc1\n"
        "v_mfma_f32_16x16x32_bf16 v[56:59], v[28:31], a[232:235], v[56:59]\n"
        "buffer_load_dwordx4 v1, s[40:43], s71 offen offset:1024 sc1 lds\n"
        "s_add_u32 s74, s74, 1\n"
        "v_mfma_f32_16x16x32_bf16 v[60:63], v[28:31], a[236:239], v[60:63]\n"
        "ds_read_b128 v[28:31], v0 offset:25600\n"
        "s_waitcnt lgkmcnt(4)\n"
        "v_mfma_f32_16x16x32_bf16 v[48:51], v[8:11], a[240:243], v[48:51]\n"
        "s_add_u32 s72, s75, 1048576\n"
        "v_mfma_f32_16x16x32_bf16 v[52:55], v[8:11], a[244:247], v[52:55]\n"
        "ds_read_b128 v[8:11], v0 offset:7680\n"
        "v_mfma_f32_16x16x32_bf16 v[56:59], v[12:15], a[240:243], v[56:59]\n"
        "v_mfma_f32_16x16x32_bf16 v[60:63], v[12:15], a[244:247], v[60:63]\n"
        "ds_read_b128 v[12:15], v0 offset:26112\n"
        "s_waitcnt lgkmcnt(4)\n"
        "v_mfma_f32_16x16x32_bf16 v[48:51], v[16:19], a[248:251], v[48:51]\n"
        "buffer_load_dwordx4 v1, s[40:43], s71 offen offset:2048 sc1 lds\n"
        "v_mfma_f32_16x16x32_bf16 v[52:55], v[16:19], a[252:255], v[52:55]\n"
        "ds_read_b128 v[16:19], v0 offset:8192\n"
        "v_mfma_f32_16x16x32_bf16 v[56:59], v[20:23], a[248:251], v[56:59]\n"
        "v_mfma_f32_16x16x32_bf16 v[60:63], v[20:23], a[252:255], v[60:63]\n"
        "ds_read_b128 v[20:23], v0 offset:26624\n"
        "s_waitcnt lgkmcnt(4)\n"
        "v_mfma_f32_16x16x32_bf16 v[48:51], v[24:27], v[80:83], v[48:51]\n"
        "v_mfma_f32_16x16x32_bf16 v[52:55], v[24:27], v[84:87], v[52:55]\n"
        "ds_read_b128 v[24:27], v0 offset:8704\n"
        "v_mfma_f32_16x16x32_bf16 v[56:59], v[28:31], v[80:83], v[56:59]\n"
        "buffer_load_dwordx4 v1, s[40:43], s71 offen offset:3072 sc1 lds\n"
        "s_add_u32 m0, m0, 4096\n"
        "v_mfma_f32_16x16x32_bf16 v[60:63], v[28:31], v[84:87], v[60:63]\n"
        "ds_read_b128 v[28:31], v0 offset:27136\n"
        "s_waitcnt lgkmcnt(4)\n"
        "v_mfma_f32_16x16x32_bf16 v[48:51], v[8:11], v[88:91], v[48:51]\n"
        "s_add_u32 s71, s71, s65\n"
        "s_add_u32 s71, s71, 61440\n"
        "v_mfma_f32_16x16x32_bf16 v[52:55], v[8:11], v[92:95], v[52:55]\n"
        "ds_read_b128 v[8:11], v0 offset:0\n"
        "v_mfma_f32_16x16x32_bf16 v[56:59], v[12:15], v[88:91], v[56:59]\n"
        "v_mfma_f32_16x16x32_bf16 v[60:63], v[12:15], v[92:95], v[60:63]\n"
        "ds_read_b128 v[12:15], v0 offset:18432\n"
        "s_waitcnt lgkmcnt(4)\n"
        "v_mfma_f32_16x16x32_bf16 v[48:51], v[16:19], v[96:99], v[48:51]\n"
        "buffer_load_dwordx4 v1, s[40:43], s71 offen offset:0 sc1 lds\n"
        "v_mfma_f32_16x16x32_bf16 v[52:55], v[16:19], v[100:103], v[52:55]\n"
        "ds_read_b128 v[16:19], v0 offset:512\n"
        "v_mfma_f32_16x16x32_bf16 v[56:59], v[20:23], v[96:99], v[56:59]\n"
        "v_mfma_f32_16x16x32_bf16 v[60:63], v[20:23], v[100:103], v[60:63]\n"
        "ds_read_b128 v[20:23], v0 offset:18944\n"
        "s_waitcnt lgkmcnt(4)\n"
        "v_mfma_f32_16x16x32_bf16 v[48:51], v[24:27], v[104:107], v[48:51]\n"
        "v_mfma_f32_16x16x32_bf16 v[52:55], v[24:27], v[108:111], v[52:55]\n"
        "ds_read_b128 v[24:27], v0 offset:1024\n"
        "v_mfma_f32_16x16x32_bf16 v[56:59], v[28:31], v[104:107], v[56:59]\n"
        "v_mfma_f32_16x16x32_bf16 v[60:63], v[28:31], v[108:111], v[60:63]\n"
        "ds_read_b128 v[28:31], v0 offset:19456\n"
        "s_waitcnt lgkmcnt(4)\n"
        "s_branch .Ls1_p20_%=\n"
        ".Ls1_p21_%=:\n"
        "v_mfma_f32_16x16x32_bf16 v[64:67], v[112:115], v[8:11], 0\n"
        "s_add_u32 m0, s77, s61\n"
        "v_mfma_f32_16x16x32_bf16 v[68:71], v[116:119], v[8:11], 0\n"
        "ds_read_b128 v[8:11], v0 offset:1536\n"
        "v_mfma_f32_16x16x32_bf16 v[72:75], v[112:115], v[12:15], 0\n"
        "v_cvt_pk_bf16_f32 v48, v48, v49\n"
        "v_cvt_pk_bf16_f32 v49, v50, v51\n"
        "v_mfma_f32_16x16x32_bf16 v[76:79], v[116:119], v[12:15], 0\n"
        "ds_read_b128 v[12:15], v0 offset:19968\n"
        "s_waitcnt lgkmcnt(4)\n"
        "v_mfma_f32_16x16x32_bf16 v[64:67], v[120:123], v[16:19], v[64:67]\n"
        "v_cvt_pk_bf16_f32 v50, v56, v57\n"
        "v_cvt_pk_bf16_f32 v51, v58, v59\n"
        "v_mfma_f32_16x16x32_bf16 v[68:71], v[124:127], v[16:19], v[68:71]\n"
        "ds_read_b128 v[16:19], v0 offset:2048\n"
        "v_mfma_f32_16x16x32_bf16 v[72:75], v[120:123], v[20:23], v[72:75]\n"
        "buffer_store_dwordx4 v[48:51], v1, s[40:43], s72 offen offset:0 sc1\n"
        "v_mfma_f32_16x16x32_bf16 v[76:79], v[124:127], v[20:23], v[76:79]\n"
        "ds_read_b128 v[20:23], v0 offset:20480\n"
        "s_waitcnt lgkmcnt(4)\n"
        "v_mfma_f32_16x16x32_bf16 v[64:67], v[128:131], v[24:27], v[64:67]\n"
        "v_cvt_pk_bf16_f32 v52, v52, v53\n"
        "v_cvt_pk_bf16_f32 v53, v54, v55\n"
        "v_mfma_f32_16x16x32_bf16 v[68:71], v[132:135], v[24:27], v[68:71]\n"
        "ds_read_b128 v[24:27], v0 offset:2560\n"
        "v_cvt_pk_bf16_f32 v54, v60, v61\n"
        "v_mfma_f32_16x16x32_bf16 v[72:75], v[128:131], v[28:31], v[72:75]\n"
        "v_cvt_pk_bf16_f32 v55, v62, v63\n"
        "v_mfma_f32_16x16x32_bf16 v[76:79], v[132:135], v[28:31], v[76:79]\n"
        "ds_read_b128 v[28:31], v0 offset:20992\n"
        "s_waitcnt lgkmcnt(4)\n"
        "v_mfma_f32_16x16x32_bf16 v[64:67], v[136:139], v[8:11], v[64:67]\n"
        "s_bitcmp1_b64 s[52:53], s69\n"
        "s_cselect_b32 s65, 8192, 0\n"
        "v_mfma_f32_16x16x32_bf16 v[68:71], v[140:143], v[8:11], v[68:71]\n"
        "ds_read_b128 v[8:11], v0 offset:3072\n"
        "s_lshl_b32 s71, s69, 21\n"
        "v_mfma_f32_16x16x32_bf16 v[72:75], v[136:139], v[12:15], v[72:75]\n"
        "buffer_store_dwordx4 v[52:55], v1, s[40:43], s72 offen offset:1024 sc1\n"
        "v_mfma_f32_16x16x32_bf16 v[76:79], v[140:143], v[12:15], v[76:79]\n"
        "ds_read_b128 v[12:15], v0 offset:21504\n"
        "s_waitcnt lgkmcnt(4)\n"
        "v_mfma_f32_16x16x32_bf16 v[64:67], v[144:147], v[16:19], v[64:67]\n"
        "s_add_u32 s71, s71, s65\n"
        "s_add_u32 s71, s71, s58\n"
        "v_mfma_f32_16x16x32_bf16 v[68:71], v[148:151], v[16:19], v[68:71]\n"
        "ds_read_b128 v[16:19], v0 offset:3584\n"
        "buffer_load_dwordx4 v1, s[40:43], s71 offen offset:0 sc1 lds\n"
        "v_mfma_f32_16x16x32_bf16 v[72:75], v[144:147], v[20:23], v[72:75]\n"
        "s_add_u32 s69, s69, 1\n"
        "s_add_u32 s77, s77, 36864\n"
        "v_mfma_f32_16x16x32_bf16 v[76:79], v[148:151], v[20:23], v[76:79]\n"
        "ds_read_b128 v[20:23], v0 offset:22016\n"
        "s_waitcnt lgkmcnt(4)\n"
        "v_mfma_f32_16x16x32_bf16 v[64:67], v[152:155], v[24:27], v[64:67]\n"
        "buffer_load_dwordx4 v1, s[40:43], s71 offen offset:1024 sc1 lds\n"
        "v_mfma_f32_16x16x32_bf16 v[68:71], v[156:159], v[24:27], v[68:71]\n"
        "ds_read_b128 v[24:27], v0 offset:4096\n"
        "v_mfma_f32_16x16x32_bf16 v[72:75], v[152:155], v[28:31], v[72:75]\n"
        "s_cmp_eq_u32 s77, 110592\n"
        "s_cselect_b32 s77, 0, s77\n"
        "v_mfma_f32_16x16x32_bf16 v[76:79], v[156:159], v[28:31], v[76:79]\n"
        "ds_read_b128 v[28:31], v0 offset:22528\n"
        "s_waitcnt lgkmcnt(4)\n"
        "v_mfma_f32_16x16x32_bf16 v[64:67], v[160:163], v[8:11], v[64:67]\n"
        "buffer_load_dwordx4 v1, s[40:43], s71 offen offset:2048 sc1 lds\n"
        "s_mov_b32 s70, 2147483647\n"
        "v_mfma_f32_16x16x32_bf16 v[68:71], v[164:167], v[8:11], v[68:71]\n"
        "ds_read_b128 v[8:11], v0 offset:4608\n"
        "v_mfma_f32_16x16x32_bf16 v[72:75], v[160:163], v[12:15], v[72:75]\n"
        "v_mfma_f32_16x16x32_bf16 v[76:79], v[164:167], v[12:15], v[76:79]\n"
        "ds_read_b128 v[12:15], v0 offset:23040\n"
        "s_waitcnt lgkmcnt(4)\n"
        "v_mfma_f32_16x16x32_bf16 v[64:67], v[168:171], v[16:19], v[64:67]\n"
        "buffer_load_dwordx4 v1, s[40:43], s71 offen offset:3072 sc1 lds\n"
        "s_add_u32 m0, m0, 4096\n"
        "v_mfma_f32_16x16x32_bf16 v[68:71], v[172:175], v[16:19], v[68:71]\n"
        "ds_read_b128 v[16:19], v0 offset:5120\n"
        "s_add_u32 s71, s71, 4096\n"
        "v_mfma_f32_16x16x32_bf16 v[72:75], v[168:171], v[20:23], v[72:75]\n"
        "s_mul_i32 s65, s48, -15360\n"
        "v_mfma_f32_16x16x32_bf16 v[76:79], v[172:175], v[20:23], v[76:79]\n"
        "ds_read_b128 v[20:23], v0 offset:23552\n"
        "s_waitcnt lgkmcnt(4)\n"
        "v_mfma_f32_16x16x32_bf16 v[64:67], v[176:179], v[24:27], v[64:67]\n"
        "buffer_load_dword v4, v2, s[40:43], s76 offen sc1\n"
        "v_mfma_f32_16x16x32_bf16 v[68:71], v[180:183], v[24:27], v[68:71]\n"
        "ds_read_b128 v[24:27], v0 offset:5632\n"
        "v_mfma_f32_16x16x32_bf16 v[72:75], v[176:179], v[28:31], v[72:75]\n"
        "buffer_load_dwordx4 v1, s[40:43], s71 offen offset:0 sc1 lds\n"
        "v_mfma_f32_16x16x32_bf16 v[76:79], v[180:183], v[28:31], v[76:79]\n"
        "ds_read_b128 v[28:31], v0 offset:24064\n"
        "s_waitcnt lgkmcnt(4)\n"
        "v_mfma_f32_16x16x32_bf16 v[64:67], v[184:187], v[8:11], v[64:67]\n"
        "v_mfma_f32_16x16x32_bf16 v[68:71], v[188:191], v[8:11], v[68:71]\n"
        "ds_read_b128 v[8:11], v0 offset:6144\n"
        "v_mfma_f32_16x16x32_bf16 v[72:75], v[184:187], v[12:15], v[72:75]\n"
        "v_mfma_f32_16x16x32_bf16 v[76:79], v[188:191], v[12:15], v[76:79]\n"
        "ds_read_b128 v[12:15], v0 offset:24576\n"
        "s_waitcnt lgkmcnt(4)\n"
        "v_mfma_f32_16x16x32_bf16 v[64:67], v[192:195], v[16:19], v[64:67]\n"
        "buffer_load_dwordx4 v1, s[40:43], s71 offen offset:1024 sc1 lds\n"
        "s_add_u32 s78, s78, 36864\n"
        "v_mfma_f32_16x16x32_bf16 v[68:71], v[196:199], v[16:19], v[68:71]\n"
        "ds_read_b128 v[16:19], v0 offset:6656\n"
        "v_mfma_f32_16x16x32_bf16 v[72:75], v[192:195], v[20:23], v[72:75]\n"
        "s_cmp_eq_u32 s78, 110592\n"
        "s_cselect_b32 s78, 0, s78\n"
        "v_mfma_f32_16x16x32_bf16 v[76:79], v[196:199], v[20:23], v[76:79]\n"
        "ds_read_b128 v[20:23], v0 offset:25088\n"
        "s_waitcnt lgkmcnt(4)\n"
        "v_mfma_f32_16x16x32_bf16 v[64:67], v[200:203], v[24:27], v[64:67]\n"
        "s_waitcnt vmcnt(7)\n"
        "v_mov_b32 v5, s74\n"
        "v_mfma_f32_16x16x32_bf16 v[68:71], v[204:207], v[24:27], v[68:71]\n"
        "ds_read_b128 v[24:27], v0 offset:7168\n"
        "buffer_store_dword v5, v3, s[40:43], s73 offen sc1\n"
        "v_mfma_f32_16x16x32_bf16 v[72:75], v[200:203], v[28:31], v[72:75]\n"
        "buffer_load_dwordx4 v1, s[40:43], s71 offen offset:2048 sc1 lds\n"
        "s_add_u32 s74, s74, 1\n"
        "v_mfma_f32_16x16x32_bf16 v[76:79], v[204:207], v[28:31], v[76:79]\n"
        "ds_read_b128 v[28:31], v0 offset:25600\n"
        "s_waitcnt lgkmcnt(4)\n"
        "v_mfma_f32_16x16x32_bf16 v[64:67], v[208:211], v[8:11], v[64:67]\n"
        "s_add_u32 s72, s75, 0\n"
        "v_mfma_f32_16x16x32_bf16 v[68:71], v[212:215], v[8:11], v[68:71]\n"
        "ds_read_b128 v[8:11], v0 offset:7680\n"
        "v_mfma_f32_16x16x32_bf16 v[72:75], v[208:211], v[12:15], v[72:75]\n"
        "buffer_load_dwordx4 v1, s[40:43], s71 offen offset:3072 sc1 lds\n"
        "s_add_u32 m0, m0, 4096\n"
        "v_mfma_f32_16x16x32_bf16 v[76:79], v[212:215], v[12:15], v[76:79]\n"
        "ds_read_b128 v[12:15], v0 offset:26112\n"
        "s_waitcnt lgkmcnt(4)\n"
        "v_mfma_f32_16x16x32_bf16 v[64:67], v[216:219], v[16:19], v[64:67]\n"
        "s_add_u32 s71, s71, s65\n"
        "s_add_u32 s71, s71, 61440\n"
        "v_mfma_f32_16x16x32_bf16 v[68:71], v[220:223], v[16:19], v[68:71]\n"
        "ds_read_b128 v[16:19], v0 offset:8192\n"
        "v_mfma_f32_16x16x32_bf16 v[72:75], v[216:219], v[20:23], v[72:75]\n"
        "v_mfma_f32_16x16x32_bf16 v[76:79], v[220:223], v[20:23], v[76:79]\n"
        "ds_read_b128 v[20:23], v0 offset:26624\n"
        "s_waitcnt lgkmcnt(4)\n"
        "v_mfma_f32_16x16x32_bf16 v[64:67], v[224:227], v[24:27], v[64:67]\n"
        "buffer_load_dwordx4 v1, s[40:43], s71 offen offset:0 sc1 lds\n"
        "v_mfma_f32_16x16x32_bf16 v[68:71], v[228:231], v[24:27], v[68:71]\n"
        "ds_read_b128 v[24:27], v0 offset:8704\n"
        "v_mfma_f32_16x16x32_bf16 v[72:75], v[224:227], v[28:31], v[72:75]\n"
        "v_mfma_f32_16x16x32_bf16 v[76:79], v[228:231], v[28:31], v[76:79]\n"
        "ds_read_b128 v[28:31], v0 offset:27136\n"
        "s_waitcnt lgkmcnt(4)\n"
        "v_mfma_f32_16x16x32_bf16 v[64:67], v[232:235], v[8:11], v[64:67]\n"
        "s_waitcnt vmcnt(7)\n"
        "s_barrier\n"
        "v_add_u32 v0, s78, v6\n"
        "v_mfma_f32_16x16x32_bf16 v[68:71], v[236:239], v[8:11], v[68:71]\n"
        "ds_read_b128 v[8:11], v0 offset:0\n"
        "v_mfma_f32_16x16x32_bf16 v[72:75], v[232:235], v[12:15], v[72:75]\n"
        "s_cmp_lt_u32 s69, 64\n"
        "s_cselect_b32 s70, s69, 2147483647\n"
        "v_mfma_f32_16x16x32_bf16 v[76:79], v[236:239], v[12:15], v[76:79]\n"
        "ds_read_b128 v[12:15], v0 offset:18432\n"
        "s_waitcnt lgkmcnt(4)\n"
        "v_mfma_f32_16x16x32_bf16 v[64:67], v[240:243], v[16:19], v[64:67]\n"
        "v_mfma_f32_16x16x32_bf16 v[68:71], v[244:247], v[16:19], v[68:71]\n"
        "ds_read_b128 v[16:19], v0 offset:512\n"
        "v_mfma_f32_16x16x32_bf16 v[72:75], v[240:243], v[20:23], v[72:75]\n"
        "v_mfma_f32_16x16x32_bf16 v[76:79], v[244:247], v[20:23], v[76:79]\n"
        "ds_read_b128 v[20:23], v0 offset:18944\n"
        "s_waitcnt lgkmcnt(4)\n"
        "v_mfma_f32_16x16x32_bf16 v[64:67], v[248:251], v[24:27], v[64:67]\n"
        "s_waitcnt vmcnt(6)\n"
        "v_cmp_gt_i32 vcc, s70, v4\n"
        "v_mfma_f32_16x16x32_bf16 v[68:71], v[252:255], v[24:27], v[68:71]\n"
        "ds_read_b128 v[24:27], v0 offset:1024\n"
        "v_mfma_f32_16x16x32_bf16 v[72:75], v[248:251], v[28:31], v[72:75]\n"
        "s_and_b64 vcc, vcc, 1\n"
        "v_mfma_f32_16x16x32_bf16 v[76:79], v[252:255], v[28:31], v[76:79]\n"
        "ds_read_b128 v[28:31], v0 offset:19456\n"
        "s_waitcnt lgkmcnt(4)\n"
        "s_cbranch_vccz .Ls1_p01_%=\n"
        "s_branch .Ls1_p00_%=\n"
        ".Ls1_tail_%=:\n"
        "v_mfma_f32_16x16x32_bf16 v[68:71], v[236:239], v[8:11], v[68:71]\n"
        "v_mfma_f32_16x16x32_bf16 v[72:75], v[232:235], v[12:15], v[72:75]\n"
        "v_mfma_f32_16x16x32_bf16 v[76:79], v[236:239], v[12:15], v[76:79]\n"
        "s_waitcnt lgkmcnt(2)\n"
        "v_mfma_f32_16x16x32_bf16 v[64:67], v[240:243], v[16:19], v[64:67]\n"
        "v_mfma_f32_16x16x32_bf16 v[68:71], v[244:247], v[16:19], v[68:71]\n"
        "v_mfma_f32_16x16x32_bf16 v[72:75], v[240:243], v[20:23], v[72:75]\n"
        "v_mfma_f32_16x16x32_bf16 v[76:79], v[244:247], v[20:23], v[76:79]\n"
        "s_waitcnt lgkmcnt(0)\n"
        "v_mfma_f32_16x16x32_bf16 v[64:67], v[248:251], v[24:27], v[64:67]\n"
        "v_mfma_f32_16x16x32_bf16 v[68:71], v[252:255], v[24:27], v[68:71]\n"
        "v_mfma_f32_16x16x32_bf16 v[72:75], v[248:251], v[28:31], v[72:75]\n"
        "v_mfma_f32_16x16x32_bf16 v[76:79], v[252:255], v[28:31], v[76:79]\n"
        "s_waitcnt vmcnt(1)\n"
        "buffer_load_dword v4, v2, s[40:43], s76 offen sc1\n"
        "s_nop 5\n"
        "v_cvt_pk_bf16_f32 v64, v64, v65\n"
        "v_cvt_pk_bf16_f32 v65, v66, v67\n"
        "v_cvt_pk_bf16_f32 v66, v68, v69\n"
        "v_cvt_pk_bf16_f32 v67, v70, v71\n"
        "buffer_store_dwordx4 v[64:67], v1, s[40:43], s72 offen offset:0 sc1\n"
        "v_cvt_pk_bf16_f32 v72, v72, v73\n"
        "v_cvt_pk_bf16_f32 v73, v74, v75\n"
        "v_cvt_pk_bf16_f32 v74, v76, v77\n"
        "v_cvt_pk_bf16_f32 v75, v78, v79\n"
        "buffer_store_dwordx4 v[72:75], v1, s[40:43], s72 offen offset:1024 sc1\n"
        "s_waitcnt vmcnt(2)\n"
        "v_cmp_gt_i32 vcc, s70, v4\n"
        "s_nop 0\n"
        "s_and_b64 vcc, vcc, 1\n"
        "s_cbranch_vccnz .Ls1_late_%=\n"
        "s_add_u32 m0, s77, s61\n"
        "s_bitcmp1_b64 s[52:53], s69\n"
        "s_cselect_b32 s65, 8192, 0\n"
        "s_lshl_b32 s71, s69, 21\n"
        "s_add_u32 s71, s71, s65\n"
        "s_add_u32 s71, s71, s58\n"
        "buffer_load_dwordx4 v1, s[40:43], s71 offen offset:0 sc1 lds\n"
        "buffer_load_dwordx4 v1, s[40:43], s71 offen offset:1024 sc1 lds\n"
        "buffer_load_dwordx4 v1, s[40:43], s71 offen offset:2048 sc1 lds\n"
        "buffer_load_dwordx4 v1, s[40:43], s71 offen offset:3072 sc1 lds\n"
        "s_add_u32 m0, m0, 4096\n"
        "s_add_u32 s71, s71, 4096\n"
        "s_mul_i32 s65, s48, -15360\n"
        "buffer_load_dwordx4 v1, s[40:43], s71 offen offset:0 sc1 lds\n"
        "buffer_load_dwordx4 v1, s[40:43], s71 offen offset:1024 sc1 lds\n"
        "buffer_load_dwordx4 v1, s[40:43], s71 offen offset:2048 sc1 lds\n"
        "buffer_load_dwordx4 v1, s[40:43], s71 offen offset:3072 sc1 lds\n"
        "s_add_u32 m0, m0, 4096\n"
        "s_add_u32 s71, s71, s65\n"
        "s_add_u32 s71, s71, 61440\n"
        "buffer_load_dwordx4 v1, s[40:43], s71 offen offset:0 sc1 lds\n"
        "s_add_u32 s69, s69, 1\n"
        "s_add_u32 s77, s77, 36864\n"
        "s_cmp_eq_u32 s77, 110592\n"
        "s_cselect_b32 s77, 0, s77\n"
        "s_mov_b32 s70, 2147483647\n"
        "s_waitcnt vmcnt(9)\n"
        "v_mov_b32 v5, s74\n"
        "buffer_store_dword v5, v3, s[40:43], s73 offen sc1\n"
        "s_waitcnt vmcnt(6)\n"
        "s_branch .Ls1_go_%=\n"
        ".Ls1_late_%=:\n"
        "s_waitcnt vmcnt(0)\n"
        "v_mov_b32 v5, s74\n"
        "buffer_store_dword v5, v3, s[40:43], s73 offen sc1\n"
        "s_cmp_eq_u32 s68, 63\n"
        "s_cbranch_scc0 .Ls1_wait_%=\n"
        "s_endpgm\n"
        ".Ls1_wait_%=:\n"
        "s_mov_b32 s64, 0\n"
        ".Ls1_in_top_%=:\n"
        "buffer_load_dword v4, v2, s[40:43], s76 offen sc1\n"
        "s_waitcnt vmcnt(0)\n"
        "v_cmp_gt_i32 vcc, s69, v4\n"
        "s_nop 0\n"
        "s_and_b64 vcc, vcc, 1\n"
        "s_cbranch_vccz .Ls1_in_ok_%=\n"
        "s_add_u32 s64, s64, 1\n"
        "s_and_b32 s65, s64, 255\n"
        "s_cbranch_scc1 .Ls1_in_go_%=\n"
        "s_mov_b32 s65, 40960\n"
        "s_cmp_lt_u32 s64, 1048576\n"
        "s_cbranch_scc1 .Ls1_in_chk_%=\n"
        "v_mov_b32 v4, 1\n"
        "buffer_store_dword v4, off, s[40:43], s65 sc1\n"
        "s_waitcnt vmcnt(0)\n"
        ".Ls1_in_chk_%=:\n"
        "buffer_load_dword v4, off, s[40:43], s65 sc1\n"
        "s_waitcnt vmcnt(0)\n"
        "v_readfirstlane_b32 s65, v4\n"
        "s_nop 0\n"
        "s_cmp_lg_u32 s65, 1\n"
        "s_cbranch_scc1 .Ls1_in_go_%=\n"
        "s_endpgm\n"
        ".Ls1_in_go_%=:\n"
        "s_sleep 1\n"
        "s_branch .Ls1_in_top_%=\n"
        ".Ls1_in_ok_%=:\n"
        "s_add_u32 m0, s77, s61\n"
        "s_bitcmp1_b64 s[52:53], s69\n"
        "s_cselect_b32 s65, 8192, 0\n"
        "s_lshl_b32 s71, s69, 21\n"
        "s_add_u32 s71, s71, s65\n"
        "s_add_u32 s71, s71, s58\n"
        "buffer_load_dwordx4 v1, s[40:43], s71 offen offset:0 sc1 lds\n"
        "buffer_load_dwordx4 v1, s[40:43], s71 offen offset:1024 sc1 lds\n"
        "buffer_load_dwordx4 v1, s[40:43], s71 offen offset:2048 sc1 lds\n"
        "buffer_load_dwordx4 v1, s[40:43], s71 offen offset:3072 sc1 lds\n"
        "s_add_u32 m0, m0, 4096\n"
        "s_add_u32 s71, s71, 4096\n"
        "s_mul_i32 s65, s48, -15360\n"
        "buffer_load_dwordx4 v1, s[40:43], s71 offen offset:0 sc1 lds\n"
        "buffer_load_dwordx4 v1, s[40:43], s71 offen offset:1024 sc1 lds\n"
        "buffer_load_dwordx4 v1, s[40:43], s71 offen offset:2048 sc1 lds\n"
        "buffer_load_dwordx4 v1, s[40:43], s71 offen offset:3072 sc1 lds\n"
        "s_add_u32 m0, m0, 4096\n"
        "s_add_u32 s71, s71, s65\n"
        "s_add_u32 s71, s71, 61440\n"
        "buffer_load_dwordx4 v1, s[40:43], s71 offen offset:0 sc1 lds\n"
        "s_add_u32 s69, s69, 1\n"
        "s_add_u32 s77, s77, 36864\n"
        "s_cmp_eq_u32 s77, 110592\n"
        "s_cselect_b32 s77, 0, s77\n"
        "s_mov_b32 s70, 2147483647\n"
        "s_waitcnt vmcnt(5)\n"
        ".Ls1_go_%=:\n"
        "s_mov_b32 s72, 241172480\n"
        "s_mov_b32 s73, 241172480\n"
        "s_cmp_lt_u32 s69, 64\n"
        "s_cselect_b32 s70, s69, 2147483647\n"
        "v_add_u32 v0, s78, v6\n"
        "s_barrier\n"
        "ds_read_b128 v[8:11], v0 offset:0\n"
        "ds_read_b128 v[12:15], v0 offset:18432\n"
        "ds_read_b128 v[16:19], v0 offset:512\n"
        "ds_read_b128 v[20:23], v0 offset:18944\n"
        "ds_read_b128 v[24:27], v0 offset:1024\n"
        "ds_read_b128 v[28:31], v0 offset:19456\n"
        "v_cmp_gt_i32 vcc, s70, v4\n"
        "s_nop 0\n"
        "s_and_b64 vcc, vcc, 1\n"
        "s_waitcnt lgkmcnt(4)\n"
        "s_cbranch_vccz .Ls1_p01_%=\n"
        "s_branch .Ls1_p00_%=\n"
        :: "s"(p.ws), "s"(p.Wk), "s"(p.Wv), "s"(p.Wq), "s"(pair(w, delay)), "s"(pair(x4, pr)),
           "s"(pair(PAGE + spread(A_XN) + w * 16384, PAGE + h * 16384 + w * 2048)), "s"(pair(flag(F_S1 + 4 * h + w), w * 9216)),
           "v"(pair((i >> 3) * 9216 + g * 128 + (i & 7) * 16, lane * 16)), "v"(pair(lane == 0 ? flag(F_XN + 4 * h + w) : OOB, lane < 2 ? lane * 32768 : OOB)), "v"(pair(0, 0)),
           "v"(pair(vo, 0)), "v"(pair(tr + (g & 1) * 32, tr + (1 - (g & 1)) * 32))
        : "v0", "v1", "v2", "v3", "v4", "v5", "v6", "v7", "v8", "v9", "v10", "v11", "v12", "v13", "v14", "v15", "v16", "v17", "v18", "v19", "v20", "v21", "v22", "v23", "v24", "v25", "v26", "v27", "v28", "v29", "v30", "v31", "v32", "v33", "v34", "v35", "v36", "v37", "v38", "v39", "v40", "v41", "v42", "v43", "v44", "v45", "v46", "v47", "v48", "v49", "v50", "v51", "v52", "v53", "v54", "v55", "v56", "v57", "v58", "v59", "v60", "v61", "v62", "v63", "v64", "v65", "v66", "v67", "v68", "v69", "v70", "v71", "v72", "v73", "v74", "v75", "v76", "v77", "v78", "v79", "v255", "a255", "s30", "s31", "s32", "s33", "s34", "s35", "s36", "s37", "s38", "s39", "s40", "s41", "s42", "s43", "s44", "s45", "s46", "s47", "s48", "s49", "s50", "s51", "s52", "s53", "s54", "s55", "s56", "s57", "s58", "s59", "s60", "s61", "s62", "s63", "s64", "s65", "s66", "s67", "s68", "s69", "s70", "s71", "s72", "s73", "s74", "s75", "s76", "s77", "s78", "s79", "s80", "s81", "s82", "s83", "s84", "s85", "s86", "s87", "s88", "s89", "s90", "s91", "s92", "s93", "s94", "s95", "s96", "s97", "s98", "s99", "vcc", "scc", "memory");
    __builtin_unreachable();
}
__device__ __forceinline__ void role_f1(const Params& p, int kap, int jj, int x4, int pr, int delay) {
    GEMM_HEAD;
    const unsigned u = jj * 4 + w;
    unsigned vo[3];
#pragma unroll
    for (int n = 0; n < 3; ++n) vo[n] = ((64 * n + lane) / 6) * 9216 + (1152 * kap + 48 * u) * 2 + ((64 * n + lane) % 6) * 16 - 1024 * n;
    const unsigned tr = w * 40960 + (pp >> 1) * 3072 + (8 * (g >> 1) + q) * 96 + 16 * (pp & 1) + 8 * (g & 1);
    asm volatile(
        "s_nop 4\n"
        "s_mov_b64 s[40:41], %0\n"
        "s_and_b32 s41, s41, 0xffff\n"
        "s_mov_b32 s42, 236978176\n"
        "s_mov_b32 s43, 0x20000\n"
        "s_mov_b64 s[48:49], %4\n"
        "s_mov_b64 s[56:57], %5\n"
        "s_mov_b64 s[58:59], %6\n"
        "s_mov_b64 s[60:61], %7\n"
        "v_mov_b64 v[0:1], %8\n"
        "v_mov_b64 v[2:3], %9\n"
        "v_mov_b64 v[4:5], %10\n"
        "v_mov_b64 v[6:7], %11\n"
        "v_mov_b64 v[8:9], %12\n"
        "s_mov_b32 s47, 0x20000\n"
        "s_mul_i32 s79, s48, 40960\n"
        ".Lf1_start_%=:\n"
        "s_sub_u32 s49, s49, 1\n"
        "s_cbranch_scc1 .Lf1_start2_%=\n"
        "s_sleep 16\n"
        "s_branch .Lf1_start_%=\n"
        ".Lf1_start2_%=:\n"
        "s_mov_b64 s[44:45], %1\n"
        "s_and_b32 s45, s45, 0xffff\n"
        "s_mov_b32 s46, 5308416\n"
        "s_add_u32 m0, s79, 0\n"
        "s_mov_b32 s65, 0\n"
        "buffer_load_dwordx4 v6, s[44:47], s65 offen lds\n"
        "buffer_load_dwordx4 v7, s[44:47], s65 offen offset:1024 lds\n"
        "buffer_load_dwordx4 v8, s[44:47], s65 offen offset:2048 lds\n"
        "s_mov_b64 s[44:45], %2\n"
        "s_and_b32 s45, s45, 0xffff\n"
        "s_mov_b32 s46, 5308416\n"
        "s_add_u32 m0, s79, 3072\n"
        "s_mov_b32 s65, 0\n"
        "buffer_load_dwordx4 v6, s[44:47], s65 offen lds\n"
        "buffer_load_dwordx4 v7, s[44:47], s65 offen offset:1024 lds\n"
        "buffer_load_dwordx4 v8, s[44:47], s65 offen offset:2048 lds\n"
        "s_mov_b64 s[44:45], %1\n"
        "s_and_b32 s45, s45, 0xffff\n"
        "s_mov_b32 s46, 5308416\n"
        "s_add_u32 m0, s79, 6144\n"
        "s_mov_b32 s65, 294912\n"
        "buffer_load_dwordx4 v6, s[44:47], s65 offen lds\n"
        "buffer_load_dwordx4 v7, s[44:47], s65 offen offset:1024 lds\n"
        "buffer_load_dwordx4 v8, s[44:47], s65 offen offset:2048 lds\n"
        "s_mov_b64 s[44:45], %2\n"
        "s_and_b32 s45, s45, 0xffff\n"
        "s_mov_b32 s46, 5308416\n"
        "s_add_u32 m0, s79, 9216\n"
        "s_mov_b32 s65, 294912\n"
        "buffer_load_dwordx4 v6, s[44:47], s65 offen lds\n"
        "buffer_load_dwordx4 v7, s[44:47], s65 offen offset:1024 lds\n"
        "buffer_load_dwordx4 v8, s[44:47], s65 offen offset:2048 lds\n"
        "s_mov_b64 s[44:45], %1\n"
        "s_and_b32 s45, s45, 0xffff\n"
        "s_mov_b32 s46, 5308416\n"
        "s_add_u32 m0, s79, 12288\n"
        "s_mov_b32 s65, 589824\n"
        "buffer_load_dwordx4 v6, s[44:47], s65 offen lds\n"
        "buffer_load_dwordx4 v7, s[44:47], s65 offen offset:1024 lds\n"
        "buffer_load_dwordx4 v8, s[44:47], s65 offen offset:2048 lds\n"
        "s_mov_b64 s[44:45], %2\n"
        "s_and_b32 s45, s45, 0xffff\n"
        "s_mov_b32 s46, 5308416\n"
        "s_add_u32 m0, s79, 15360\n"
        "s_mov_b32 s65, 589824\n"
        "buffer_load_dwordx4 v6, s[44:47], s65 offen lds\n"
        "buffer_load_dwordx4 v7, s[44:47], s65 offen offset:1024 lds\n"
        "buffer_load_dwordx4 v8, s[44:47], s65 offen offset:2048 lds\n"
        "s_mov_b64 s[44:45], %1\n"
        "s_and_b32 s45, s45, 0xffff\n"
        "s_mov_b32 s46, 5308416\n"
        "s_add_u32 m0, s79, 18432\n"
        "s_mov_b32 s65, 884736\n"
        "buffer_load_dwordx4 v6, s[44:47], s65 offen lds\n"
        "buffer_load_dwordx4 v7, s[44:47], s65 offen offset:1024 lds\n"
        "buffer_load_dwordx4 v8, s[44:47], s65 offen offset:2048 lds\n"
        "s_mov_b64 s[44:45], %2\n"
        "s_and_b32 s45, s45, 0xffff\n"
        "s_mov_b32 s46, 5308416\n"
        "s_add_u32 m0, s79, 21504\n"
        "s_mov_b32 s65, 884736\n"
        "buffer_load_dwordx4 v6, s[44:47], s65 offen lds\n"
        "buffer_load_dwordx4 v7, s[44:47], s65 offen offset:1024 lds\n"
        "buffer_load_dwordx4 v8, s[44:47], s65 offen offset:2048 lds\n"
        "s_mov_b64 s[44:45], %1\n"
        "s_and_b32 s45, s45, 0xffff\n"
        "s_mov_b32 s46, 5308416\n"
        "s_add_u32 m0, s79, 24576\n"
        "s_mov_b32 s65, 1179648\n"
        "buffer_load_dwordx4 v6, s[44:47], s65 offen lds\n"
        "buffer_load_dwordx4 v7, s[44:47], s65 offen offset:1024 lds\n"
        "buffer_load_dwordx4 v8, s[44:47], s65 offen offset:2048 lds\n"
        "s_mov_b64 s[44:45], %2\n"
        "s_and_b32 s45, s45, 0xffff\n"
        "s_mov_b32 s46, 5308416\n"
        "s_add_u32 m0, s79, 27648\n"
        "s_mov_b32 s65, 1179648\n"
        "buffer_load_dwordx4 v6, s[44:47], s65 offen lds\n"
        "buffer_load_dwordx4 v7, s[44:47], s65 offen offset:1024 lds\n"
        "buffer_load_dwordx4 v8, s[44:47], s65 offen offset:2048 lds\n"
        "s_mov_b64 s[44:45], %1\n"
        "s_and_b32 s45, s45, 0xffff\n"
        "s_mov_b32 s46, 5308416\n"
        "s_add_u32 m0, s79, 30720\n"
        "s_mov_b32 s65, 1474560\n"
        "buffer_load_dwordx4 v6, s[44:47], s65 offen lds\n"
        "buffer_load_dwordx4 v7, s[44:47], s65 offen offset:1024 lds\n"
        "buffer_load_dwordx4 v8, s[44:47], s65 offen offset:2048 lds\n"
        "s_mov_b64 s[44:45], %2\n"
        "s_and_b32 s45, s45, 0xffff\n"
        "s_mov_b32 s46, 5308416\n"
        "s_add_u32 m0, s79, 33792\n"
        "s_mov_b32 s65, 1474560\n"
        "buffer_load_dwordx4 v6, s[44:47], s65 offen lds\n"
        "buffer_load_dwordx4 v7, s[44:47], s65 offen offset:1024 lds\n"
        "buffer_load_dwordx4 v8, s[44:47], s65 offen offset:2048 lds\n"
        "s_waitcnt vmcnt(30)\n"
        "ds_read_b64_tr_b16 a[0:1], v9 offset:0\n"
        "ds_read_b64_tr_b16 a[2:3], v9 offset:384\n"
        "ds_read_b64_tr_b16 a[4:5], v9 offset:1536\n"
        "ds_read_b64_tr_b16 a[6:7], v9 offset:1920\n"
        "ds_read_b64_tr_b16 a[144:145], v9 offset:32\n"
        "ds_read_b64_tr_b16 a[146:147], v9 offset:416\n"
        "ds_read_b64_tr_b16 a[148:149], v9 offset:1568\n"
        "ds_read_b64_tr_b16 a[150:151], v9 offset:1952\n"
        "ds_read_b64_tr_b16 v[112:113], v9 offset:64\n"
        "ds_read_b64_tr_b16 v[114:115], v9 offset:448\n"
        "ds_read_b64_tr_b16 v[116:117], v9 offset:1600\n"
        "ds_read_b64_tr_b16 v[118:119], v9 offset:1984\n"
        "s_waitcnt vmcnt(24)\n"
        "ds_read_b64_tr_b16 a[8:9], v9 offset:6144\n"
        "ds_read_b64_tr_b16 a[10:11], v9 offset:6528\n"
        "ds_read_b64_tr_b16 a[12:13], v9 offset:7680\n"
        "ds_read_b64_tr_b16 a[14:15], v9 offset:8064\n"
        "ds_read_b64_tr_b16 a[152:153], v9 offset:6176\n"
        "ds_read_b64_tr_b16 a[154:155], v9 offset:6560\n"
        "ds_read_b64_tr_b16 a[156:157], v9 offset:7712\n"
        "ds_read_b64_tr_b16 a[158:159], v9 offset:8096\n"
        "ds_read_b64_tr_b16 v[120:121], v9 offset:6208\n"
        "ds_read_b64_tr_b16 v[122:123], v9 offset:6592\n"
        "ds_read_b64_tr_b16 v[124:125], v9 offset:7744\n"
        "ds_read_b64_tr_b16 v[126:127], v9 offset:8128\n"
        "s_waitcnt lgkmcnt(12)\n"
        "s_mov_b64 s[44:45], %1\n"
        "s_and_b32 s45, s45, 0xffff\n"
        "s_mov_b32 s46, 5308416\n"
        "s_add_u32 m0, s79, 0\n"
        "s_mov_b32 s65, 1769472\n"
        "buffer_load_dwordx4 v6, s[44:47], s65 offen lds\n"
        "buffer_load_dwordx4 v7, s[44:47], s65 offen offset:1024 lds\n"
        "buffer_load_dwordx4 v8, s[44:47], s65 offen offset:2048 lds\n"
        "s_mov_b64 s[44:45], %2\n"
        "s_and_b32 s45, s45, 0xffff\n"
        "s_mov_b32 s46, 5308416\n"
        "s_add_u32 m0, s79, 3072\n"
        "s_mov_b32 s65, 1769472\n"
        "buffer_load_dwordx4 v6, s[44:47], s65 offen lds\n"
        "buffer_load_dwordx4 v7, s[44:47], s65 offen offset:1024 lds\n"
        "buffer_load_dwordx4 v8, s[44:47], s65 offen offset:2048 lds\n"
        "s_waitcnt vmcnt(24)\n"
        "ds_read_b64_tr_b16 a[16:17], v9 offset:12288\n"
        "ds_read_b64_tr_b16 a[18:19], v9 offset:12672\n"
        "ds_read_b64_tr_b16 a[20:21], v9 offset:13824\n"
        "ds_read_b64_tr_b16 a[22:23], v9 offset:14208\n"
        "ds_read_b64_tr_b16 a[160:161], v9 offset:12320\n"
        "ds_read_b64_tr_b16 a[162:163], v9 offset:12704\n"
        "ds_read_b64_tr_b16 a[164:165], v9 offset:13856\n"
        "ds_read_b64_tr_b16 a[166:167], v9 offset:14240\n"
        "ds_read_b64_tr_b16 v[128:129], v9 offset:12352\n"
        "ds_read_b64_tr_b16 v[130:131], v9 offset:12736\n"
        "ds_read_b64_tr_b16 v[132:133], v9 offset:13888\n"
        "ds_read_b64_tr_b16 v[134:135], v9 offset:14272\n"
        "s_waitcnt lgkmcnt(12)\n"
        "s_mov_b64 s[44:45], %1\n"
        "s_and_b32 s45, s45, 0xffff\n"
        "s_mov_b32 s46, 5308416\n"
        "s_add_u32 m0, s79, 6144\n"
        "s_mov_b32 s65, 2064384\n"
        "buffer_load_dwordx4 v6, s[44:47], s65 offen lds\n"
        "buffer_load_dwordx4 v7, s[44:47], s65 offen offset:1024 lds\n"
        "buffer_load_dwordx4 v8, s[44:47], s65 offen offset:2048 lds\n"
        "s_mov_b64 s[44:45], %2\n"
        "s_and_b32 s45, s45, 0xffff\n"
        "s_mov_b32 s46, 5308416\n"
        "s_add_u32 m0, s79, 9216\n"
        "s_mov_b32 s65, 2064384\n"
        "buffer_load_dwordx4 v6, s[44:47], s65 offen lds\n"
        "buffer_load_dwordx4 v7, s[44:47], s65 offen offset:1024 lds\n"
        "buffer_load_dwordx4 v8, s[44:47], s65 offen offset:2048 lds\n"
        "s_waitcnt vmcnt(24)\n"
        "ds_read_b64_tr_b16 a[24:25], v9 offset:18432\n"
        "ds_read_b64_tr_b16 a[26:27], v9 offset:18816\n"
        "ds_read_b64_tr_b16 a[28:29], v9 offset:19968\n"
        "ds_read_b64_tr_b16 a[30:31], v9 offset:20352\n"
        "ds_read_b64_tr_b16 a[168:169], v9 offset:18464\n"
        "ds_read_b64_tr_b16 a[170:171], v9 offset:18848\n"
        "ds_read_b64_tr_b16 a[172:173], v9 offset:20000\n"
        "ds_read_b64_tr_b16 a[174:175], v9 offset:20384\n"
        "ds_read_b64_tr_b16 v[136:137], v9 offset:18496\n"
        "ds_read_b64_tr_b16 v[138:139], v9 offset:18880\n"
        "ds_read_b64_tr_b16 v[140:141], v9 offset:20032\n"
        "ds_read_b64_tr_b16 v[142:143], v9 offset:20416\n"
        "s_waitcnt lgkmcnt(12)\n"
        "s_mov_b64 s[44:45], %1\n"
        "s_and_b32 s45, s45, 0xffff\n"
        "s_mov_b32 s46, 5308416\n"
        "s_add_u32 m0, s79, 12288\n"
        "s_mov_b32 s65, 2359296\n"
        "buffer_load_dwordx4 v6, s[44:47], s65 offen lds\n"
        "buffer_load_dwordx4 v7, s[44:47], s65 offen offset:1024 lds\n"
        "buffer_load_dwordx4 v8, s[44:47], s65 offen offset:2048 lds\n"
        "s_mov_b64 s[44:45], %2\n"
        "s_and_b32 s45, s45, 0xffff\n"
        "s_mov_b32 s46, 5308416\n"
        "s_add_u32 m0, s79, 15360\n"
        "s_mov_b32 s65, 2359296\n"
        "buffer_load_dwordx4 v6, s[44:47], s65 offen lds\n"
        "buffer_load_dwordx4 v7, s[44:47], s65 offen offset:1024 lds\n"
        "buffer_load_dwordx4 v8, s[44:47], s65 offen offset:2048 lds\n"
        "s_waitcnt vmcnt(24)\n"
        "ds_read_b64_tr_b16 a[32:33], v9 offset:24576\n"
        "ds_read_b64_tr_b16 a[34:35], v9 offset:24960\n"
        "ds_read_b64_tr_b16 a[36:37], v9 offset:26112\n"
        "ds_read_b64_tr_b16 a[38:39], v9 offset:26496\n"
        "ds_read_b64_tr_b16 a[176:177], v9 offset:24608\n"
        "ds_read_b64_tr_b16 a[178:179], v9 offset:24992\n"
        "ds_read_b64_tr_b16 a[180:181], v9 offset:26144\n"
        "ds_read_b64_tr_b16 a[182:183], v9 offset:26528\n"
        "ds_read_b64_tr_b16 v[144:145], v9 offset:24640\n"
        "ds_read_b64_tr_b16 v[146:147], v9 offset:25024\n"
        "ds_read_b64_tr_b16 v[148:149], v9 offset:26176\n"
        "ds_read_b64_tr_b16 v[150:151], v9 offset:26560\n"
        "s_waitcnt lgkmcnt(12)\n"
        "s_mov_b64 s[44:45], %1\n"
        "s_and_b32 s45, s45, 0xffff\n"
        "s_mov_b32 s46, 5308416\n"
        "s_add_u32 m0, s79, 18432\n"
        "s_mov_b32 s65, 2654208\n"
        "buffer_load_dwordx4 v6, s[44:47], s65 offen lds\n"
        "buffer_load_dwordx4 v7, s[44:47], s65 offen offset:1024 lds\n"
        "buffer_load_dwordx4 v8, s[44:47], s65 offen offset:2048 lds\n"
        "s_mov_b64 s[44:45], %2\n"
        "s_and_b32 s45, s45, 0xffff\n"
        "s_mov_b32 s46, 5308416\n"
        "s_add_u32 m0, s79, 21504\n"
        "s_mov_b32 s65, 2654208\n"
        "buffer_load_dwordx4 v6, s[44:47], s65 offen lds\n"
        "buffer_load_dwordx4 v7, s[44:47], s65 offen offset:1024 lds\n"
        "buffer_load_dwordx4 v8, s[44:47], s65 offen offset:2048 lds\n"
        "s_waitcnt vmcnt(24)\n"
        "ds_read_b64_tr_b16 a[40:41], v9 offset:30720\n"
        "ds_read_b64_tr_b16 a[42:43], v9 offset:31104\n"
        "ds_read_b64_tr_b16 a[44:45], v9 offset:32256\n"
        "ds_read_b64_tr_b16 a[46:47], v9 offset:32640\n"
        "ds_read_b64_tr_b16 a[184:185], v9 offset:30752\n"
        "ds_read_b64_tr_b16 a[186:187], v9 offset:31136\n"
        "ds_read_b64_tr_b16 a[188:189], v9 offset:32288\n"
        "ds_read_b64_tr_b16 a[190:191], v9 offset:32672\n"
        "ds_read_b64_tr_b16 v[152:153], v9 offset:30784\n"
        "ds_read_b64_tr_b16 v[154:155], v9 offset:31168\n"
        "ds_read_b64_tr_b16 v[156:157], v9 offset:32320\n"
        "ds_read_b64_tr_b16 v[158:159], v9 offset:32704\n"
        "s_waitcnt lgkmcnt(12)\n"
        "s_mov_b64 s[44:45], %1\n"
        "s_and_b32 s45, s45, 0xffff\n"
        "s_mov_b32 s46, 5308416\n"
        "s_add_u32 m0, s79, 24576\n"
        "s_mov_b32 s65, 2949120\n"
        "buffer_load_dwordx4 v6, s[44:47], s65 offen lds\n"
        "buffer_load_dwordx4 v7, s[44:47], s65 offen offset:1024 lds\n"
        "buffer_load_dwordx4 v8, s[44:47], s65 offen offset:2048 lds\n"
        "s_mov_b64 s[44:45], %2\n"
        "s_and_b32 s45, s45, 0xffff\n"
        "s_mov_b32 s46, 5308416\n"
        "s_add_u32 m0, s79, 27648\n"
        "s_mov_b32 s65, 2949120\n"
        "buffer_load_dwordx4 v6, s[44:47], s65 offen lds\n"
        "buffer_load_dwordx4 v7, s[44:47], s65 offen offset:1024 lds\n"
        "buffer_load_dwordx4 v8, s[44:47], s65 offen offset:2048 lds\n"
        "s_waitcnt vmcnt(24)\n"
        "ds_read_b64_tr_b16 a[48:49], v9 offset:0\n"
        "ds_read_b64_tr_b16 a[50:51], v9 offset:384\n"
        "ds_read_b64_tr_b16 a[52:53], v9 offset:1536\n"
        "ds_read_b64_tr_b16 a[54:55], v9 offset:1920\n"
        "ds_read_b64_tr_b16 a[192:193], v9 offset:32\n"
        "ds_read_b64_tr_b16 a[194:195], v9 offset:416\n"
        "ds_read_b64_tr_b16 a[196:197], v9 offset:1568\n"
        "ds_read_b64_tr_b16 a[198:199], v9 offset:1952\n"
        "ds_read_b64_tr_b16 v[160:161], v9 offset:64\n"
        "ds_read_b64_tr_b16 v[162:163], v9 offset:448\n"
        "ds_read_b64_tr_b16 v[164:165], v9 offset:1600\n"
        "ds_read_b64_tr_b16 v[166:167], v9 offset:1984\n"
        "s_waitcnt lgkmcnt(12)\n"
        "s_mov_b64 s[44:45], %1\n"
        "s_and_b32 s45, s45, 0xffff\n"
        "s_mov_b32 s46, 5308416\n"
        "s_add_u32 m0, s79, 30720\n"
        "s_mov_b32 s65, 3244032\n"
        "buffer_load_dwordx4 v6, s[44:47], s65 offen lds\n"
        "buffer_load_dwordx4 v7, s[44:47], s65 offen offset:1024 lds\n"
        "buffer_load_dwordx4 v8, s[44:47], s65 offen offset:2048 lds\n"
        "s_mov_b64 s[44:45], %2\n"
        "s_and_b32 s45, s45, 0xffff\n"
        "s_mov_b32 s46, 5308416\n"
        "s_add_u32 m0, s79, 33792\n"
        "s_mov_b32 s65, 3244032\n"
        "buffer_load_dwordx4 v6, s[44:47], s65 offen lds\n"
        "buffer_load_dwordx4 v7, s[44:47], s65 offen offset:1024 lds\n"
        "buffer_load_dwordx4 v8, s[44:47], s65 offen offset:2048 lds\n"
        "s_waitcnt vmcnt(24)\n"
        "ds_read_b64_tr_b16 a[56:57], v9 offset:6144\n"
        "ds_read_b64_tr_b16 a[58:59], v9 offset:6528\n"
        "ds_read_b64_tr_b16 a[60:61], v9 offset:7680\n"
        "ds_read_b64_tr_b16 a[62:63], v9 offset:8064\n"
        "ds_read_b64_tr_b16 a[200:201], v9 offset:6176\n"
        "ds_read_b64_tr_b16 a[202:203], v9 offset:6560\n"
        "ds_read_b64_tr_b16 a[204:205], v9 offset:7712\n"
        "ds_read_b64_tr_b16 a[206:207], v9 offset:8096\n"
        "ds_read_b64_tr_b16 v[168:169], v9 offset:6208\n"
        "ds_read_b64_tr_b16 v[170:171], v9 offset:6592\n"
        "ds_read_b64_tr_b16 v[172:173], v9 offset:7744\n"
        "ds_read_b64_tr_b16 v[174:175], v9 offset:8128\n"
        "s_waitcnt lgkmcnt(12)\n"
        "s_mov_b64 s[44:45], %1\n"
        "s_and_b32 s45, s45, 0xffff\n"
        "s_mov_b32 s46, 5308416\n"
        "s_add_u32 m0, s79, 0\n"
        "s_mov_b32 s65, 3538944\n"
        "buffer_load_dwordx4 v6, s[44:47], s65 offen lds\n"
        "buffer_load_dwordx4 v7, s[44:47], s65 offen offset:1024 lds\n"
        "buffer_load_dwordx4 v8, s[44:47], s65 offen offset:2048 lds\n"
        "s_mov_b64 s[44:45], %2\n"
        "s_and_b32 s45, s45, 0xffff\n"
        "s_mov_b32 s46, 5308416\n"
        "s_add_u32 m0, s79, 3072\n"
        "s_mov_b32 s65, 3538944\n"
        "buffer_load_dwordx4 v6, s[44:47], s65 offen lds\n"
        "buffer_load_dwordx4 v7, s[44:47], s65 offen offset:1024 lds\n"
        "buffer_load_dwordx4 v8, s[44:47], s65 offen offset:2048 lds\n"
        "s_waitcnt vmcnt(24)\n"
        "ds_read_b64_tr_b16 a[64:65], v9 offset:12288\n"
        "ds_read_b64_tr_b16 a[66:67], v9 offset:12672\n"
        "ds_read_b64_tr_b16 a[68:69], v9 offset:13824\n"
        "ds_read_b64_tr_b16 a[70:71], v9 offset:14208\n"
        "ds_read_b64_tr_b16 a[208:209], v9 offset:12320\n"
        "ds_read_b64_tr_b16 a[210:211], v9 offset:12704\n"
        "ds_read_b64_tr_b16 a[212:213], v9 offset:13856\n"
        "ds_read_b64_tr_b16 a[214:215], v9 offset:14240\n"
        "ds_read_b64_tr_b16 v[176:177], v9 offset:12352\n"
        "ds_read_b64_tr_b16 v[178:179], v9 offset:12736\n"
        "ds_read_b64_tr_b16 v[180:181], v9 offset:13888\n"
        "ds_read_b64_tr_b16 v[182:183], v9 offset:14272\n"
        "s_waitcnt lgkmcnt(12)\n"
        "s_mov_b64 s[44:45], %1\n"
        "s_and_b32 s45, s45, 0xffff\n"
        "s_mov_b32 s46, 5308416\n"
        "s_add_u32 m0, s79, 6144\n"
        "s_mov_b32 s65, 3833856\n"
        "buffer_load_dwordx4 v6, s[44:47], s65 offen lds\n"
        "buffer_load_dwordx4 v7, s[44:47], s65 offen offset:1024 lds\n"
        "buffer_load_dwordx4 v8, s[44:47], s65 offen offset:2048 lds\n"
        "s_mov_b64 s[44:45], %2\n"
        "s_and_b32 s45, s45, 0xffff\n"
        "s_mov_b32 s46, 5308416\n"
        "s_add_u32 m0, s79, 9216\n"
        "s_mov_b32 s65, 3833856\n"
        "buffer_load_dwordx4 v6, s[44:47], s65 offen lds\n"
        "buffer_load_dwordx4 v7, s[44:47], s65 offen offset:1024 lds\n"
        "buffer_load_dwordx4 v8, s[44:47], s65 offen offset:2048 lds\n"
        "s_waitcnt vmcnt(24)\n"
        "ds_read_b64_tr_b16 a[72:73], v9 offset:18432\n"
        "ds_read_b64_tr_b16 a[74:75], v9 offset:18816\n"
        "ds_read_b64_tr_b16 a[76:77], v9 offset:19968\n"
        "ds_read_b64_tr_b16 a[78:79], v9 offset:20352\n"
        "ds_read_b64_tr_b16 a[216:217], v9 offset:18464\n"
        "ds_read_b64_tr_b16 a[218:219], v9 offset:18848\n"
        "ds_read_b64_tr_b16 a[220:221], v9 offset:20000\n"
        "ds_read_b64_tr_b16 a[222:223], v9 offset:20384\n"
        "ds_read_b64_tr_b16 v[184:185], v9 offset:18496\n"
        "ds_read_b64_tr_b16 v[186:187], v9 offset:18880\n"
        "ds_read_b64_tr_b16 v[188:189], v9 offset:20032\n"
        "ds_read_b64_tr_b16 v[190:191], v9 offset:20416\n"
        "s_waitcnt lgkmcnt(12)\n"
        "s_mov_b64 s[44:45], %1\n"
        "s_and_b32 s45, s45, 0xffff\n"
        "s_mov_b32 s46, 5308416\n"
        "s_add_u32 m0, s79, 12288\n"
        "s_mov_b32 s65, 4128768\n"
        "buffer_load_dwordx4 v6, s[44:47], s65 offen lds\n"
        "buffer_load_dwordx4 v7, s[44:47], s65 offen offset:1024 lds\n"
        "buffer_load_dwordx4 v8, s[44:47], s65 offen offset:2048 lds\n"
        "s_mov_b64 s[44:45], %2\n"
        "s_and_b32 s45, s45, 0xffff\n"
        "s_mov_b32 s46, 5308416\n"
        "s_add_u32 m0, s79, 15360\n"
        "s_mov_b32 s65, 4128768\n"
        "buffer_load_dwordx4 v6, s[44:47], s65 offen lds\n"
        "buffer_load_dwordx4 v7, s[44:47], s65 offen offset:1024 lds\n"
        "buffer_load_dwordx4 v8, s[44:47], s65 offen offset:2048 lds\n"
        "s_waitcnt vmcnt(24)\n"
        "ds_read_b64_tr_b16 a[80:81], v9 offset:24576\n"
        "ds_read_b64_tr_b16 a[82:83], v9 offset:24960\n"
        "ds_read_b64_tr_b16 a[84:85], v9 offset:26112\n"
        "ds_read_b64_tr_b16 a[86:87], v9 offset:26496\n"
        "ds_read_b64_tr_b16 a[224:225], v9 offset:24608\n"
        "ds_read_b64_tr_b16 a[226:227], v9 offset:24992\n"
        "ds_read_b64_tr_b16 a[228:229], v9 offset:26144\n"
        "ds_read_b64_tr_b16 a[230:231], v9 offset:26528\n"
        "ds_read_b64_tr_b16 v[192:193], v9 offset:24640\n"
        "ds_read_b64_tr_b16 v[194:195], v9 offset:25024\n"
        "ds_read_b64_tr_b16 v[196:197], v9 offset:26176\n"
        "ds_read_b64_tr_b16 v[198:199], v9 offset:26560\n"
        "s_waitcnt lgkmcnt(12)\n"
        "s_mov_b64 s[44:45], %1\n"
        "s_and_b32 s45, s45, 0xffff\n"
        "s_mov_b32 s46, 5308416\n"
        "s_add_u32 m0, s79, 18432\n"
        "s_mov_b32 s65, 4423680\n"
        "buffer_load_dwordx4 v6, s[44:47], s65 offen lds\n"
        "buffer_load_dwordx4 v7, s[44:47], s65 offen offset:1024 lds\n"
        "buffer_load_dwordx4 v8, s[44:47], s65 offen offset:2048 lds\n"
        "s_mov_b64 s[44:45], %2\n"
        "s_and_b32 s45, s45, 0xffff\n"
        "s_mov_b32 s46, 5308416\n"
        "s_add_u32 m0, s79, 21504\n"
        "s_mov_b32 s65, 4423680\n"
        "buffer_load_dwordx4 v6, s[44:47], s65 offen lds\n"
        "buffer_load_dwordx4 v7, s[44:47], s65 offen offset:1024 lds\n"
        "buffer_load_dwordx4 v8, s[44:47], s65 offen offset:2048 lds\n"
        "s_waitcnt vmcnt(24)\n"
        "ds_read_b64_tr_b16 a[88:89], v9 offset:30720\n"
        "ds_read_b64_tr_b16 a[90:91], v9 offset:31104\n"
        "ds_read_b64_tr_b16 a[92:93], v9 offset:32256\n"
        "ds_read_b64_tr_b16 a[94:95], v9 offset:32640\n"
        "ds_read_b64_tr_b16 a[232:233], v9 offset:30752\n"
        "ds_read_b64_tr_b16 a[234:235], v9 offset:31136\n"
        "ds_read_b64_tr_b16 a[236:237], v9 offset:32288\n"
        "ds_read_b64_tr_b16 a[238:239], v9 offset:32672\n"
        "ds_read_b64_tr_b16 v[200:201], v9 offset:30784\n"
        "ds_read_b64_tr_b16 v[202:203], v9 offset:31168\n"
        "ds_read_b64_tr_b16 v[204:205], v9 offset:32320\n"
        "ds_read_b64_tr_b16 v[206:207], v9 offset:32704\n"
        "s_waitcnt lgkmcnt(12)\n"
        "s_mov_b64 s[44:45], %1\n"
        "s_and_b32 s45, s45, 0xffff\n"
        "s_mov_b32 s46, 5308416\n"
        "s_add_u32 m0, s79, 24576\n"
        "s_mov_b32 s65, 4718592\n"
        "buffer_load_dwordx4 v6, s[44:47], s65 offen lds\n"
        "buffer_load_dwordx4 v7, s[44:47], s65 offen offset:1024 lds\n"
        "buffer_load_dwordx4 v8, s[44:47], s65 offen offset:2048 lds\n"
        "s_mov_b64 s[44:45], %2\n"
        "s_and_b32 s45, s45, 0xffff\n"
        "s_mov_b32 s46, 5308416\n"
        "s_add_u32 m0, s79, 27648\n"
        "s_mov_b32 s65, 4718592\n"
        "buffer_load_dwordx4 v6, s[44:47], s65 offen lds\n"
        "buffer_load_dwordx4 v7, s[44:47], s65 offen offset:1024 lds\n"
        "buffer_load_dwordx4 v8, s[44:47], s65 offen offset:2048 lds\n"
        "s_waitcnt vmcnt(24)\n"
        "ds_read_b64_tr_b16 a[96:97], v9 offset:0\n"
        "ds_read_b64_tr_b16 a[98:99], v9 offset:384\n"
        "ds_read_b64_tr_b16 a[100:101], v9 offset:1536\n"
        "ds_read_b64_tr_b16 a[102:103], v9 offset:1920\n"
        "ds_read_b64_tr_b16 a[240:241], v9 offset:32\n"
        "ds_read_b64_tr_b16 a[242:243], v9 offset:416\n"
        "ds_read_b64_tr_b16 a[244:245], v9 offset:1568\n"
        "ds_read_b64_tr_b16 a[246:247], v9 offset:1952\n"
        "ds_read_b64_tr_b16 v[208:209], v9 offset:64\n"
        "ds_read_b64_tr_b16 v[210:211], v9 offset:448\n"
        "ds_read_b64_tr_b16 v[212:213], v9 offset:1600\n"
        "ds_read_b64_tr_b16 v[214:215], v9 offset:1984\n"
        "s_waitcnt lgkmcnt(12)\n"
        "s_mov_b64 s[44:45], %1\n"
        "s_and_b32 s45, s45, 0xffff\n"
        "s_mov_b32 s46, 5308416\n"
        "s_add_u32 m0, s79, 30720\n"
        "s_mov_b32 s65, 5013504\n"
        "buffer_load_dwordx4 v6, s[44:47], s65 offen lds\n"
        "buffer_load_dwordx4 v7, s[44:47], s65 offen offset:1024 lds\n"
        "buffer_load_dwordx4 v8, s[44:47], s65 offen offset:2048 lds\n"
        "s_mov_b64 s[44:45], %2\n"
        "s_and_b32 s45, s45, 0xffff\n"
        "s_mov_b32 s46, 5308416\n"
        "s_add_u32 m0, s79, 33792\n"
        "s_mov_b32 s65, 5013504\n"
        "buffer_load_dwordx4 v6, s[44:47], s65 offen lds\n"
        "buffer_load_dwordx4 v7, s[44:47], s65 offen offset:1024 lds\n"
        "buffer_load_dwordx4 v8, s[44:47], s65 offen offset:2048 lds\n"
        "s_waitcnt vmcnt(24)\n"
        "ds_read_b64_tr_b16 a[104:105], v9 offset:6144\n"
        "ds_read_b64_tr_b16 a[106:107], v9 offset:6528\n"
        "ds_read_b64_tr_b16 a[108:109], v9 offset:7680\n"
        "ds_read_b64_tr_b16 a[110:111], v9 offset:8064\n"
        "ds_read_b64_tr_b16 a[248:249], v9 offset:6176\n"
        "ds_read_b64_tr_b16 a[250:251], v9 offset:6560\n"
        "ds_read_b64_tr_b16 a[252:253], v9 offset:7712\n"
        "ds_read_b64_tr_b16 a[254:255], v9 offset:8096\n"
        "ds_read_b64_tr_b16 v[216:217], v9 offset:6208\n"
        "ds_read_b64_tr_b16 v[218:219], v9 offset:6592\n"
        "ds_read_b64_tr_b16 v[220:221], v9 offset:7744\n"
        "ds_read_b64_tr_b16 v[222:223], v9 offset:8128\n"
        "s_waitcnt vmcnt(18)\n"
        "ds_read_b64_tr_b16 a[112:113], v9 offset:12288\n"
        "ds_read_b64_tr_b16 a[114:115], v9 offset:12672\n"
        "ds_read_b64_tr_b16 a[116:117], v9 offset:13824\n"
        "ds_read_b64_tr_b16 a[118:119], v9 offset:14208\n"
        "ds_read_b64_tr_b16 v[80:81], v9 offset:12320\n"
        "ds_read_b64_tr_b16 v[82:83], v9 offset:12704\n"
        "ds_read_b64_tr_b16 v[84:85], v9 offset:13856\n"
        "ds_read_b64_tr_b16 v[86:87], v9 offset:14240\n"
        "ds_read_b64_tr_b16 v[224:225], v9 offset:12352\n"
        "ds_read_b64_tr_b16 v[226:227], v9 offset:12736\n"
        "ds_read_b64_tr_b16 v[228:229], v9 offset:13888\n"
        "ds_read_b64_tr_b16 v[230:231], v9 offset:14272\n"
        "s_waitcnt vmcnt(12)\n"
        "ds_read_b64_tr_b16 a[120:121], v9 offset:18432\n"
        "ds_read_b64_tr_b16 a[122:123], v9 offset:18816\n"
        "ds_read_b64_tr_b16 a[124:125], v9 offset:19968\n"
        "ds_read_b64_tr_b16 a[126:127], v9 offset:20352\n"
        "ds_read_b64_tr_b16 v[88:89], v9 offset:18464\n"
        "ds_read_b64_tr_b16 v[90:91], v9 offset:18848\n"
        "ds_read_b64_tr_b16 v[92:93], v9 offset:20000\n"
        "ds_read_b64_tr_b16 v[94:95], v9 offset:20384\n"
        "ds_read_b64_tr_b16 v[232:233], v9 offset:18496\n"
        "ds_read_b64_tr_b16 v[234:235], v9 offset:18880\n"
        "ds_read_b64_tr_b16 v[236:237], v9 offset:20032\n"
        "ds_read_b64_tr_b16 v[238:239], v9 offset:20416\n"
        "s_waitcnt vmcnt(6)\n"
        "ds_read_b64_tr_b16 a[128:129], v9 offset:24576\n"
        "ds_read_b64_tr_b16 a[130:131], v9 offset:24960\n"
        "ds_read_b64_tr_b16 a[132:133], v9 offset:26112\n"
        "ds_read_b64_tr_b16 a[134:135], v9 offset:26496\n"
        "ds_read_b64_tr_b16 v[96:97], v9 offset:24608\n"
        "ds_read_b64_tr_b16 v[98:99], v9 offset:24992\n"
        "ds_read_b64_tr_b16 v[100:101], v9 offset:26144\n"
        "ds_read_b64_tr_b16 v[102:103], v9 offset:26528\n"
        "ds_read_b64_tr_b16 v[240:241], v9 offset:24640\n"
        "ds_read_b64_tr_b16 v[242:243], v9 offset:25024\n"
        "ds_read_b64_tr_b16 v[244:245], v9 offset:26176\n"
        "ds_read_b64_tr_b16 v[246:247], v9 offset:26560\n"
        "s_waitcnt vmcnt(0)\n"
        "ds_read_b64_tr_b16 a[136:137], v9 offset:30720\n"
        "ds_read_b64_tr_b16 a[138:139], v9 offset:31104\n"
        "ds_read_b64_tr_b16 a[140:141], v9 offset:32256\n"
        "ds_read_b64_tr_b16 a[142:143], v9 offset:32640\n"
        "ds_read_b64_tr_b16 v[104:105], v9 offset:30752\n"
        "ds_read_b64_tr_b16 v[106:107], v9 offset:31136\n"
        "ds_read_b64_tr_b16 v[108:109], v9 offset:32288\n"
        "ds_read_b64_tr_b16 v[110:111], v9 offset:32672\n"
        "ds_read_b64_tr_b16 v[248:249], v9 offset:30784\n"
        "ds_read_b64_tr_b16 v[250:251], v9 offset:31168\n"
        "ds_read_b64_tr_b16 v[252:253], v9 offset:32320\n"
        "ds_read_b64_tr_b16 v[254:255], v9 offset:32704\n"
        "s_waitcnt lgkmcnt(0)\n"
        "v_lshrrev_b32 v12, 4, v1\n"
        "s_lshl_b32 s66, s57, 7\n"
        "s_mov_b32 s64, 0\n"
        ".Lf1_pair_top_%=:\n"
        "s_mov_b64 exec, 1\n"
        "buffer_load_dword v13, off, s[40:43], s66 sc1\n"
        "s_mov_b64 exec, -1\n"
        "s_waitcnt vmcnt(0)\n"
        "v_cmp_gt_i32 vcc, 0, v13\n"
        "s_nop 0\n"
        "s_and_b64 vcc, vcc, 1\n"
        "s_cbranch_vccz .Lf1_pair_ok_%=\n"
        "s_add_u32 s64, s64, 1\n"
        "s_and_b32 s65, s64, 255\n"
        "s_cbranch_scc1 .Lf1_pair_go_%=\n"
        "s_mov_b32 s65, 40960\n"
        "s_cmp_lt_u32 s64, 1048576\n"
        "s_cbranch_scc1 .Lf1_pair_chk_%=\n"
        "v_mov_b32 v13, 1\n"
        "buffer_store_dword v13, off, s[40:43], s65 sc1\n"
        "s_waitcnt vmcnt(0)\n"
        ".Lf1_pair_chk_%=:\n"
        "buffer_load_dword v13, off, s[40:43], s65 sc1\n"
        "s_waitcnt vmcnt(0)\n"
        "v_readfirstlane_b32 s65, v13\n"
        "s_nop 0\n"
        "s_cmp_lg_u32 s65, 1\n"
        "s_cbranch_scc1 .Lf1_pair_go_%=\n"
        "s_endpgm\n"
        ".Lf1_pair_go_%=:\n"
        "s_sleep 2\n"
        "s_branch .Lf1_pair_top_%=\n"
        ".Lf1_pair_ok_%=:\n"
        "v_readfirstlane_b32 s50, v13\n"
        "s_nop 0\n"
        "s_xor_b32 s50, s50, s56\n"
        "v_lshlrev_b32 v14, 7, v12\n"
        "s_sub_u32 s66, 0, s50\n"
        "s_mov_b32 s52, 24576\n"
        "s_mov_b32 s64, 0\n"
        ".Lf1_pg0_top_%=:\n"
        "buffer_load_dword v13, v14, s[40:43], s52 offen sc1\n"
        "s_waitcnt vmcnt(0)\n"
        "v_cmp_gt_i32 vcc, 0, v13\n"
        "s_cbranch_vccz .Lf1_pg0_ok_%=\n"
        "s_add_u32 s64, s64, 1\n"
        "s_and_b32 s65, s64, 255\n"
        "s_cbranch_scc1 .Lf1_pg0_go_%=\n"
        "s_mov_b32 s65, 40960\n"
        "s_cmp_lt_u32 s64, 1048576\n"
        "s_cbranch_scc1 .Lf1_pg0_chk_%=\n"
        "v_mov_b32 v13, 1\n"
        "buffer_store_dword v13, off, s[40:43], s65 sc1\n"
        "s_waitcnt vmcnt(0)\n"
        ".Lf1_pg0_chk_%=:\n"
        "buffer_load_dword v13, off, s[40:43], s65 sc1\n"
        "s_waitcnt vmcnt(0)\n"
        "v_readfirstlane_b32 s65, v13\n"
        "s_nop 0\n"
        "s_cmp_lg_u32 s65, 1\n"
        "s_cbranch_scc1 .Lf1_pg0_go_%=\n"
        "s_endpgm\n"
        ".Lf1_pg0_go_%=:\n"
        "s_sleep 2\n"
        "s_branch .Lf1_pg0_top_%=\n"
        ".Lf1_pg0_ok_%=:\n"
        "v_cmp_eq_u32 s[52:53], 1, v13\n"
        "s_nop 1\n"
        "s_mov_b32 s54, 32768\n"
        "s_mov_b32 s64, 0\n"
        ".Lf1_pg1_top_%=:\n"
        "buffer_load_dword v13, v14, s[40:43], s54 offen sc1\n"
        "s_waitcnt vmcnt(0)\n"
        "v_cmp_gt_i32 vcc, 0, v13\n"
        "s_cbranch_vccz .Lf1_pg1_ok_%=\n"
        "s_add_u32 s64, s64, 1\n"
        "s_and_b32 s65, s64, 255\n"
        "s_cbranch_scc1 .Lf1_pg1_go_%=\n"
        "s_mov_b32 s65, 40960\n"
        "s_cmp_lt_u32 s64, 1048576\n"
        "s_cbranch_scc1 .Lf1_pg1_chk_%=\n"
        "v_mov_b32 v13, 1\n"
        "buffer_store_dword v13, off, s[40:43], s65 sc1\n"
        "s_waitcnt vmcnt(0)\n"
        ".Lf1_pg1_chk_%=:\n"
        "buffer_load_dword v13, off, s[40:43], s65 sc1\n"
        "s_waitcnt vmcnt(0)\n"
        "v_readfirstlane_b32 s65, v13\n"
        "s_nop 0\n"
        "s_cmp_lg_u32 s65, 1\n"
        "s_cbranch_scc1 .Lf1_pg1_go_%=\n"
        "s_endpgm\n"
        ".Lf1_pg1_go_%=:\n"
        "s_sleep 2\n"
        "s_branch .Lf1_pg1_top_%=\n"
        ".Lf1_pg1_ok_%=:\n"
        "v_cmp_eq_u32 s[54:55], 1, v13\n"
        "s_nop 1\n"
        "s_mov_b32 vcc_lo, 0x55555555\n"
        "s_mov_b32 vcc_hi, 0x55555555\n"
        "s_xor_b64 vcc, vcc, s[52:53]\n"
        "s_bcnt1_i32_b64 s65, vcc\n"
        "s_sub_u32 s64, s65, 23\n"
        "s_cmp_lt_u32 s64, 19\n"
        "s_cbranch_scc1 .Lf1_rawmap_%=\n"
        "s_cmp_lt_u32 s65, 32\n"
        "s_cselect_b32 s65, 0, -1\n"
        "s_xor_b32 s52, s65, 0x55555555\n"
        "s_xor_b32 s53, s65, 0x55555555\n"
        "s_xor_b32 s54, s65, 0x55555555\n"
        "s_xor_b32 s55, s65, 0xaaaaaaaa\n"
        ".Lf1_rawmap_%=:\n"
        "s_xor_b32 s52, s52, s66\n"
        "s_xor_b32 s53, s53, s66\n"
        "s_xor_b32 s54, s54, s66\n"
        "s_xor_b32 s55, s55, s66\n"
        "s_and_b32 s76, s55, 1\n"
        "s_lshl_b32 s76, s76, 13\n"
        "s_add_u32 s60, s60, s76\n"
        "s_barrier\n"
        "s_mov_b32 s68, -1\n"
        "s_mov_b32 s69, 0\n"
        "s_mov_b32 s77, 0\n"
        "s_mov_b32 s78, 0\n"
        "v_mov_b32 v6, v0\n"
        "s_mov_b32 s80, 0xbfb8aa3b\n"
        "s_branch .Lf1_wait_%=\n"
        ".Lf1_p00_%=:\n"
        "v_mfma_f32_32x32x16_bf16 v[32:47], a[0:3], v[8:11], 0\n"
        "ds_read_b128 v[8:11], v0 offset:1536\n"
        "v_mfma_f32_32x32x16_bf16 v[32:47], a[4:7], v[12:15], v[32:47]\n"
        "ds_read_b128 v[12:15], v0 offset:1792\n"
        "s_waitcnt lgkmcnt(4)\n"
        "v_mul_f32 v48, s80, v64\n"
        "v_mul_f32 v49, s80, v65\n"
        "v_mul_f32 v50, s80, v66\n"
        "v_mul_f32 v51, s80, v67\n"
        "v_mfma_f32_32x32x16_bf16 v[32:47], a[8:11], v[16:19], v[32:47]\n"
        "ds_read_b128 v[16:19], v0 offset:2048\n"
        "v_mul_f32 v52, s80, v72\n"
        "v_mul_f32 v53, s80, v73\n"
        "v_mul_f32 v54, s80, v74\n"
        "v_mul_f32 v55, s80, v75\n"
        "v_mfma_f32_32x32x16_bf16 v[32:47], a[12:15], v[20:23], v[32:47]\n"
        "ds_read_b128 v[20:23], v0 offset:2304\n"
        "s_waitcnt lgkmcnt(4)\n"
        "v_exp_f32 v48, v48\n"
        "v_mul_f32 v64, v64, v68\n"
        "v_mfma_f32_32x32x16_bf16 v[32:47], a[16:19], v[24:27], v[32:47]\n"
        "ds_read_b128 v[24:27], v0 offset:2560\n"
        "v_exp_f32 v49, v49\n"
        "v_mul_f32 v65, v65, v69\n"
        "v_exp_f32 v50, v50\n"
        "v_mfma_f32_32x32x16_bf16 v[32:47], a[20:23], v[28:31], v[32:47]\n"
        "ds_read_b128 v[28:31], v0 offset:2816\n"
        "s_waitcnt lgkmcnt(4)\n"
        "v_mul_f32 v66, v66, v70\n"
        "v_exp_f32 v51, v51\n"
        "v_mul_f32 v67, v67, v71\n"
        "v_mfma_f32_32x32x16_bf16 v[32:47], a[24:27], v[8:11], v[32:47]\n"
        "ds_read_b128 v[8:11], v0 offset:3072\n"
        "v_exp_f32 v52, v52\n"
        "v_mul_f32 v72, v72, v76\n"
        "v_exp_f32 v53, v53\n"
        "v_mfma_f32_32x32x16_bf16 v[32:47], a[28:31], v[12:15], v[32:47]\n"
        "ds_read_b128 v[12:15], v0 offset:3328\n"
        "s_waitcnt lgkmcnt(4)\n"
        "v_mul_f32 v73, v73, v77\n"
        "v_exp_f32 v54, v54\n"
        "v_mul_f32 v74, v74, v78\n"
        "v_mfma_f32_32x32x16_bf16 v[32:47], a[32:35], v[16:19], v[32:47]\n"
        "ds_read_b128 v[16:19], v0 offset:3584\n"
        "v_exp_f32 v55, v55\n"
        "v_mul_f32 v75, v75, v79\n"
        "v_add_f32 v48, 1.0, v48\n"
        "v_add_f32 v49, 1.0, v49\n"
        "v_mfma_f32_32x32x16_bf16 v[32:47], a[36:39], v[20:23], v[32:47]\n"
        "s_waitcnt vmcnt(0)\n"
        "s_barrier\n"
        "ds_read_b128 v[20:23], v0 offset:3840\n"
        "s_waitcnt lgkmcnt(4)\n"
        "v_rcp_f32 v48, v48\n"
        "v_mfma_f32_32x32x16_bf16 v[32:47], a[40:43], v[24:27], v[32:47]\n"
        "ds_read_b128 v[24:27], v0 offset:4096\n"
        "v_add_f32 v50, 1.0, v50\n"
        "v_rcp_f32 v49, v49\n"
        "v_add_f32 v51, 1.0, v51\n"
        "v_mfma_f32_32x32x16_bf16 v[32:47], a[44:47], v[28:31], v[32:47]\n"
        "ds_read_b128 v[28:31], v0 offset:4352\n"
        "s_waitcnt lgkmcnt(4)\n"
        "v_rcp_f32 v50, v50\n"
        "v_add_f32 v52, 1.0, v52\n"
        "v_mfma_f32_32x32x16_bf16 v[32:47], a[48:51], v[8:11], v[32:47]\n"
        "ds_read_b128 v[8:11], v0 offset:4608\n"
        "v_rcp_f32 v51, v51\n"
        "v_add_f32 v53, 1.0, v53\n"
        "v_rcp_f32 v52, v52\n"
        "v_mfma_f32_32x32x16_bf16 v[32:47], a[52:55], v[12:15], v[32:47]\n"
        "ds_read_b128 v[12:15], v0 offset:4864\n"
        "s_waitcnt lgkmcnt(4)\n"
        "v_add_f32 v54, 1.0, v54\n"
        "v_rcp_f32 v53, v53\n"
        "v_add_f32 v55, 1.0, v55\n"
        "v_mfma_f32_32x32x16_bf16 v[32:47], a[56:59], v[16:19], v[32:47]\n"
        "ds_read_b128 v[16:19], v0 offset:5120\n"
        "v_rcp_f32 v54, v54\n"
        "v_rcp_f32 v55, v55\n"
        "v_mul_f32 v64, v64, v48\n"
        "v_mfma_f32_32x32x16_bf16 v[32:47], a[60:63], v[20:23], v[32:47]\n"
        "ds_read_b128 v[20:23], v0 offset:5376\n"
        "s_waitcnt lgkmcnt(4)\n"
        "v_mul_f32 v65, v65, v49\n"
        "v_mul_f32 v66, v66, v50\n"
        "v_mul_f32 v67, v67, v51\n"
        "v_mul_f32 v72, v72, v52\n"
        "v_mfma_f32_32x32x16_bf16 v[32:47], a[64:67], v[24:27], v[32:47]\n"
        "ds_read_b128 v[24:27], v0 offset:5632\n"
        "v_mul_f32 v73, v73, v53\n"
        "v_mul_f32 v74, v74, v54\n"
        "v_mul_f32 v75, v75, v55\n"
        "v_cvt_pk_bf16_f32 v56, v64, v65\n"
        "v_cvt_pk_bf16_f32 v57, v66, v67\n"
        "v_mfma_f32_32x32x16_bf16 v[32:47], a[68:71], v[28:31], v[32:47]\n"
        "ds_read_b128 v[28:31], v0 offset:5888\n"
        "s_waitcnt lgkmcnt(4)\n"
        "v_cvt_pk_bf16_f32 v58, v72, v73\n"
        "v_cvt_pk_bf16_f32 v59, v74, v75\n"
        "buffer_load_dword v4, v2, s[40:43], s76 offen sc1\n"
        "v_mfma_f32_32x32x16_bf16 v[32:47], a[72:75], v[8:11], v[32:47]\n"
        "ds_read_b128 v[8:11], v0 offset:6144\n"
        "buffer_store_dwordx4 v[56:59], v1, s[40:43], s72 offen sc1\n"
        "v_mfma_f32_32x32x16_bf16 v[32:47], a[76:79], v[12:15], v[32:47]\n"
        "ds_read_b128 v[12:15], v0 offset:6400\n"
        "s_waitcnt lgkmcnt(4)\n"
        "v_mfma_f32_32x32x16_bf16 v[32:47], a[80:83], v[16:19], v[32:47]\n"
        "ds_read_b128 v[16:19], v0 offset:6656\n"
        "v_mfma_f32_32x32x16_bf16 v[32:47], a[84:87], v[20:23], v[32:47]\n"
        "ds_read_b128 v[20:23], v0 offset:6912\n"
        "s_waitcnt lgkmcnt(4)\n"
        "v_mfma_f32_32x32x16_bf16 v[32:47], a[88:91], v[24:27], v[32:47]\n"
        "ds_read_b128 v[24:27], v0 offset:7168\n"
        "v_mfma_f32_32x32x16_bf16 v[32:47], a[92:95], v[28:31], v[32:47]\n"
        "ds_read_b128 v[28:31], v0 offset:7424\n"
        "s_waitcnt lgkmcnt(4)\n"
        "v_mfma_f32_32x32x16_bf16 v[32:47], a[96:99], v[8:11], v[32:47]\n"
        "ds_read_b128 v[8:11], v0 offset:7680\n"
        "v_mfma_f32_32x32x16_bf16 v[32:47], a[100:103], v[12:15], v[32:47]\n"
        "ds_read_b128 v[12:15], v0 offset:7936\n"
        "s_waitcnt lgkmcnt(4)\n"
        "v_mfma_f32_32x32x16_bf16 v[32:47], a[104:107], v[16:19], v[32:47]\n"
        "ds_read_b128 v[16:19], v0 offset:8192\n"
        "v_mfma_f32_32x32x16_bf16 v[32:47], a[108:111], v[20:23], v[32:47]\n"
        "ds_read_b128 v[20:23], v0 offset:8448\n"
        "s_waitcnt lgkmcnt(4)\n"
        "v_mfma_f32_32x32x16_bf16 v[32:47], a[112:115], v[24:27], v[32:47]\n"
        "ds_read_b128 v[24:27], v0 offset:8704\n"
        "v_mfma_f32_32x32x16_bf16 v[32:47], a[116:119], v[28:31], v[32:47]\n"
        "ds_read_b128 v[28:31], v0 offset:8960\n"
        "s_waitcnt lgkmcnt(4)\n"
        "v_mfma_f32_32x32x16_bf16 v[32:47], a[120:123], v[8:11], v[32:47]\n"
        "ds_read_b128 v[8:11], v0 offset:0\n"
        "v_mfma_f32_32x32x16_bf16 v[32:47], a[124:127], v[12:15], v[32:47]\n"
        "ds_read_b128 v[12:15], v0 offset:256\n"
        "s_waitcnt lgkmcnt(4)\n"
        "v_mfma_f32_32x32x16_bf16 v[32:47], a[128:131], v[16:19], v[32:47]\n"
        "ds_read_b128 v[16:19], v0 offset:512\n"
        "s_waitcnt vmcnt(0)\n"
        "v_mov_b32 v5, s74\n"
        "buffer_store_dword v5, v3, s[40:43], s73 offen sc1\n"
        "s_add_u32 s68, s68, 1\n"
        "s_lshr_b32 s66, s68, 1\n"
        "v_mfma_f32_32x32x16_bf16 v[32:47], a[132:135], v[20:23], v[32:47]\n"
        "ds_read_b128 v[20:23], v0 offset:768\n"
        "s_waitcnt lgkmcnt(4)\n"
        "s_bitcmp1_b32 s54, s66\n"
        "s_cselect_b32 s66, 8192, 0\n"
        "s_lshl_b32 s75, s68, 20\n"
        "s_add_u32 s75, s75, s66\n"
        "v_mfma_f32_32x32x16_bf16 v[32:47], a[136:139], v[24:27], v[32:47]\n"
        "ds_read_b128 v[24:27], v0 offset:1024\n"
        "s_add_u32 s75, s75, s59\n"
        "s_mul_i32 s74, s68, 3\n"
        "s_mov_b32 s73, s60\n"
        "s_add_u32 s72, s75, 0\n"
        "v_cmp_gt_i32 vcc, s70, v4\n"
        "v_mfma_f32_32x32x16_bf16 v[32:47], a[140:143], v[28:31], v[32:47]\n"
        "ds_read_b128 v[28:31], v0 offset:1280\n"
        "s_waitcnt lgkmcnt(4)\n"
        "s_and_b64 vcc, vcc, 1\n"
        "s_cbranch_vccz .Lf1_p11_%=\n"
        ".Lf1_p10_%=:\n"
        "v_mfma_f32_32x32x16_bf16 v[48:63], a[144:147], v[8:11], 0\n"
        "ds_read_b128 v[8:11], v0 offset:1536\n"
        "v_mfma_f32_32x32x16_bf16 v[48:63], a[148:151], v[12:15], v[48:63]\n"
        "ds_read_b128 v[12:15], v0 offset:1792\n"
        "s_waitcnt lgkmcnt(4)\n"
        "v_mul_f32 v64, s80, v32\n"
        "v_mul_f32 v65, s80, v33\n"
        "v_mul_f32 v66, s80, v34\n"
        "v_mul_f32 v67, s80, v35\n"
        "v_mfma_f32_32x32x16_bf16 v[48:63], a[152:155], v[16:19], v[48:63]\n"
        "ds_read_b128 v[16:19], v0 offset:2048\n"
        "v_mul_f32 v68, s80, v40\n"
        "v_mul_f32 v69, s80, v41\n"
        "v_mul_f32 v70, s80, v42\n"
        "v_mul_f32 v71, s80, v43\n"
        "v_mfma_f32_32x32x16_bf16 v[48:63], a[156:159], v[20:23], v[48:63]\n"
        "ds_read_b128 v[20:23], v0 offset:2304\n"
        "s_waitcnt lgkmcnt(4)\n"
        "v_exp_f32 v64, v64\n"
        "v_mul_f32 v32, v32, v36\n"
        "v_mfma_f32_32x32x16_bf16 v[48:63], a[160:163], v[24:27], v[48:63]\n"
        "ds_read_b128 v[24:27], v0 offset:2560\n"
        "v_exp_f32 v65, v65\n"
        "v_mul_f32 v33, v33, v37\n"
        "v_exp_f32 v66, v66\n"
        "v_mfma_f32_32x32x16_bf16 v[48:63], a[164:167], v[28:31], v[48:63]\n"
        "ds_read_b128 v[28:31], v0 offset:2816\n"
        "s_waitcnt lgkmcnt(4)\n"
        "v_mul_f32 v34, v34, v38\n"
        "v_exp_f32 v67, v67\n"
        "v_mul_f32 v35, v35, v39\n"
        "v_mfma_f32_32x32x16_bf16 v[48:63], a[168:171], v[8:11], v[48:63]\n"
        "ds_read_b128 v[8:11], v0 offset:3072\n"
        "v_exp_f32 v68, v68\n"
        "v_mul_f32 v40, v40, v44\n"
        "v_exp_f32 v69, v69\n"
        "v_mfma_f32_32x32x16_bf16 v[48:63], a[172:175], v[12:15], v[48:63]\n"
        "ds_read_b128 v[12:15], v0 offset:3328\n"
        "s_waitcnt lgkmcnt(4)\n"
        "v_mul_f32 v41, v41, v45\n"
        "v_exp_f32 v70, v70\n"
        "v_mul_f32 v42, v42, v46\n"
        "v_mfma_f32_32x32x16_bf16 v[48:63], a[176:179], v[16:19], v[48:63]\n"
        "ds_read_b128 v[16:19], v0 offset:3584\n"
        "v_exp_f32 v71, v71\n"
        "v_mul_f32 v43, v43, v47\n"
        "v_add_f32 v64, 1.0, v64\n"
        "v_add_f32 v65, 1.0, v65\n"
        "v_mfma_f32_32x32x16_bf16 v[48:63], a[180:183], v[20:23], v[48:63]\n"
        "ds_read_b128 v[20:23], v0 offset:3840\n"
        "s_waitcnt lgkmcnt(4)\n"
        "v_rcp_f32 v64, v64\n"
        "v_add_f32 v66, 1.0, v66\n"
        "v_mfma_f32_32x32x16_bf16 v[48:63], a[184:187], v[24:27], v[48:63]\n"
        "ds_read_b128 v[24:27], v0 offset:4096\n"
        "v_rcp_f32 v65, v65\n"
        "v_add_f32 v67, 1.0, v67\n"
        "v_rcp_f32 v66, v66\n"
        "v_mfma_f32_32x32x16_bf16 v[48:63], a[188:191], v[28:31], v[48:63]\n"
        "ds_read_b128 v[28:31], v0 offset:4352\n"
        "s_waitcnt lgkmcnt(4)\n"
        "v_add_f32 v68, 1.0, v68\n"
        "v_rcp_f32 v67, v67\n"
        "v_add_f32 v69, 1.0, v69\n"
        "v_mfma_f32_32x32x16_bf16 v[48:63], a[192:195], v[8:11], v[48:63]\n"
        "ds_read_b128 v[8:11], v0 offset:4608\n"
        "v_rcp_f32 v68, v68\n"
        "v_add_f32 v70, 1.0, v70\n"
        "v_rcp_f32 v69, v69\n"
        "v_mfma_f32_32x32x16_bf16 v[48:63], a[196:199], v[12:15], v[48:63]\n"
        "ds_read_b128 v[12:15], v0 offset:4864\n"
        "s_waitcnt lgkmcnt(4)\n"
        "v_add_f32 v71, 1.0, v71\n"
        "v_rcp_f32 v70, v70\n"
        "v_mfma_f32_32x32x16_bf16 v[48:63], a[200:203], v[16:19], v[48:63]\n"
        "ds_read_b128 v[16:19], v0 offset:5120\n"
        "v_rcp_f32 v71, v71\n"
        "v_mul_f32 v32, v32, v64\n"
        "v_mul_f32 v33, v33, v65\n"
        "v_mul_f32 v34, v34, v66\n"
        "v_mfma_f32_32x32x16_bf16 v[48:63], a[204:207], v[20:23], v[48:63]\n"
        "ds_read_b128 v[20:23], v0 offset:5376\n"
        "s_waitcnt lgkmcnt(4)\n"
        "v_mul_f32 v35, v35, v67\n"
        "v_mul_f32 v40, v40, v68\n"
        "v_mul_f32 v41, v41, v69\n"
        "v_mul_f32 v42, v42, v70\n"
        "v_mfma_f32_32x32x16_bf16 v[48:63], a[208:211], v[24:27], v[48:63]\n"
        "ds_read_b128 v[24:27], v0 offset:5632\n"
        "v_mul_f32 v43, v43, v71\n"
        "v_cvt_pk_bf16_f32 v72, v32, v33\n"
        "v_cvt_pk_bf16_f32 v73, v34, v35\n"
        "v_cvt_pk_bf16_f32 v74, v40, v41\n"
        "v_cvt_pk_bf16_f32 v75, v42, v43\n"
        "v_mfma_f32_32x32x16_bf16 v[48:63], a[212:215], v[28:31], v[48:63]\n"
        "ds_read_b128 v[28:31], v0 offset:5888\n"
        "s_waitcnt lgkmcnt(4)\n"
        "buffer_store_dwordx4 v[72:75], v1, s[40:43], s72 offen sc1\n"
        "v_mfma_f32_32x32x16_bf16 v[48:63], a[216:219], v[8:11], v[48:63]\n"
        "ds_read_b128 v[8:11], v0 offset:6144\n"
        "buffer_load_dword v4, v2, s[40:43], s76 offen sc1\n"
        "v_mfma_f32_32x32x16_bf16 v[48:63], a[220:223], v[12:15], v[48:63]\n"
        "ds_read_b128 v[12:15], v0 offset:6400\n"
        "s_waitcnt lgkmcnt(4)\n"
        "v_mfma_f32_32x32x16_bf16 v[48:63], a[224:227], v[16:19], v[48:63]\n"
        "ds_read_b128 v[16:19], v0 offset:6656\n"
        "v_mfma_f32_32x32x16_bf16 v[48:63], a[228:231], v[20:23], v[48:63]\n"
        "ds_read_b128 v[20:23], v0 offset:6912\n"
        "s_waitcnt lgkmcnt(4)\n"
        "v_mfma_f32_32x32x16_bf16 v[48:63], a[232:235], v[24:27], v[48:63]\n"
        "ds_read_b128 v[24:27], v0 offset:7168\n"
        "v_mfma_f32_32x32x16_bf16 v[48:63], a[236:239], v[28:31], v[48:63]\n"
        "ds_read_b128 v[28:31], v0 offset:7424\n"
        "s_waitcnt lgkmcnt(4)\n"
        "v_mfma_f32_32x32x16_bf16 v[48:63], a[240:243], v[8:11], v[48:63]\n"
        "ds_read_b128 v[8:11], v0 offset:7680\n"
        "v_mfma_f32_32x32x16_bf16 v[48:63], a[244:247], v[12:15], v[48:63]\n"
        "ds_read_b128 v[12:15], v0 offset:7936\n"
        "s_waitcnt lgkmcnt(4)\n"
        "v_mfma_f32_32x32x16_bf16 v[48:63], a[248:251], v[16:19], v[48:63]\n"
        "ds_read_b128 v[16:19], v0 offset:8192\n"
        "v_mfma_f32_32x32x16_bf16 v[48:63], a[252:255], v[20:23], v[48:63]\n"
        "ds_read_b128 v[20:23], v0 offset:8448\n"
        "s_waitcnt lgkmcnt(4)\n"
        "v_mfma_f32_32x32x16_bf16 v[48:63], v[80:83], v[24:27], v[48:63]\n"
        "ds_read_b128 v[24:27], v0 offset:8704\n"
        "v_mfma_f32_32x32x16_bf16 v[48:63], v[84:87], v[28:31], v[48:63]\n"
        "ds_read_b128 v[28:31], v0 offset:8960\n"
        "s_waitcnt lgkmcnt(4)\n"
        "v_mfma_f32_32x32x16_bf16 v[48:63], v[88:91], v[8:11], v[48:63]\n"
        "ds_read_b128 v[8:11], v0 offset:0\n"
        "v_mfma_f32_32x32x16_bf16 v[48:63], v[92:95], v[12:15], v[48:63]\n"
        "ds_read_b128 v[12:15], v0 offset:256\n"
        "s_waitcnt lgkmcnt(4)\n"
        "v_mfma_f32_32x32x16_bf16 v[48:63], v[96:99], v[16:19], v[48:63]\n"
        "ds_read_b128 v[16:19], v0 offset:512\n"
        "s_waitcnt vmcnt(1)\n"
        "v_mov_b32 v5, s74\n"
        "buffer_store_dword v5, v3, s[40:43], s73 offen sc1\n"
        "s_add_u32 s74, s74, 1\n"
        "s_add_u32 s72, s75, 49152\n"
        "v_mfma_f32_32x32x16_bf16 v[48:63], v[100:103], v[20:23], v[48:63]\n"
        "ds_read_b128 v[20:23], v0 offset:768\n"
        "s_waitcnt lgkmcnt(4)\n"
        "v_mfma_f32_32x32x16_bf16 v[48:63], v[104:107], v[24:27], v[48:63]\n"
        "ds_read_b128 v[24:27], v0 offset:1024\n"
        "s_waitcnt vmcnt(1)\n"
        "v_cmp_gt_i32 vcc, s70, v4\n"
        "v_mfma_f32_32x32x16_bf16 v[48:63], v[108:111], v[28:31], v[48:63]\n"
        "ds_read_b128 v[28:31], v0 offset:1280\n"
        "s_waitcnt lgkmcnt(4)\n"
        "s_and_b64 vcc, vcc, 1\n"
        "s_cbranch_vccz .Lf1_p21_%=\n"
        ".Lf1_p20_%=:\n"
        "v_mfma_f32_32x32x16_bf16 v[64:79], v[112:115], v[8:11], 0\n"
        "ds_read_b128 v[8:11], v0 offset:1536\n"
        "v_mfma_f32_32x32x16_bf16 v[64:79], v[116:119], v[12:15], v[64:79]\n"
        "ds_read_b128 v[12:15], v0 offset:1792\n"
        "s_waitcnt lgkmcnt(4)\n"
        "v_mul_f32 v32, s80, v48\n"
        "v_mul_f32 v33, s80, v49\n"
        "v_mul_f32 v34, s80, v50\n"
        "v_mul_f32 v35, s80, v51\n"
        "v_mfma_f32_32x32x16_bf16 v[64:79], v[120:123], v[16:19], v[64:79]\n"
        "ds_read_b128 v[16:19], v0 offset:2048\n"
        "v_mul_f32 v36, s80, v56\n"
        "v_mul_f32 v37, s80, v57\n"
        "v_mul_f32 v38, s80, v58\n"
        "v_mul_f32 v39, s80, v59\n"
        "v_mfma_f32_32x32x16_bf16 v[64:79], v[124:127], v[20:23], v[64:79]\n"
        "ds_read_b128 v[20:23], v0 offset:2304\n"
        "s_waitcnt lgkmcnt(4)\n"
        "v_exp_f32 v32, v32\n"
        "v_mul_f32 v48, v48, v52\n"
        "v_mfma_f32_32x32x16_bf16 v[64:79], v[128:131], v[24:27], v[64:79]\n"
        "ds_read_b128 v[24:27], v0 offset:2560\n"
        "v_exp_f32 v33, v33\n"
        "v_mul_f32 v49, v49, v53\n"
        "v_exp_f32 v34, v34\n"
        "v_mfma_f32_32x32x16_bf16 v[64:79], v[132:135], v[28:31], v[64:79]\n"
        "ds_read_b128 v[28:31], v0 offset:2816\n"
        "s_waitcnt lgkmcnt(4)\n"
        "v_mul_f32 v50, v50, v54\n"
        "v_exp_f32 v35, v35\n"
        "v_mul_f32 v51, v51, v55\n"
        "v_mfma_f32_32x32x16_bf16 v[64:79], v[136:139], v[8:11], v[64:79]\n"
        "ds_read_b128 v[8:11], v0 offset:3072\n"
        "v_exp_f32 v36, v36\n"
        "v_mul_f32 v56, v56, v60\n"
        "v_exp_f32 v37, v37\n"
        "v_mfma_f32_32x32x16_bf16 v[64:79], v[140:143], v[12:15], v[64:79]\n"
        "ds_read_b128 v[12:15], v0 offset:3328\n"
        "s_waitcnt lgkmcnt(4)\n"
        "v_mul_f32 v57, v57, v61\n"
        "v_exp_f32 v38, v38\n"
        "v_mul_f32 v58, v58, v62\n"
        "v_mfma_f32_32x32x16_bf16 v[64:79], v[144:147], v[16:19], v[64:79]\n"
        "ds_read_b128 v[16:19], v0 offset:3584\n"
        "v_exp_f32 v39, v39\n"
        "v_mul_f32 v59, v59, v63\n"
        "v_add_f32 v32, 1.0, v32\n"
        "v_add_f32 v33, 1.0, v33\n"
        "v_mfma_f32_32x32x16_bf16 v[64:79], v[148:151], v[20:23], v[64:79]\n"
        "ds_read_b128 v[20:23], v0 offset:3840\n"
        "s_waitcnt lgkmcnt(4)\n"
        "v_rcp_f32 v32, v32\n"
        "v_add_f32 v34, 1.0, v34\n"
        "v_mfma_f32_32x32x16_bf16 v[64:79], v[152:155], v[24:27], v[64:79]\n"
        "ds_read_b128 v[24:27], v0 offset:4096\n"
        "v_rcp_f32 v33, v33\n"
        "v_add_f32 v35, 1.0, v35\n"
        "v_rcp_f32 v34, v34\n"
        "v_mfma_f32_32x32x16_bf16 v[64:79], v[156:159], v[28:31], v[64:79]\n"
        "ds_read_b128 v[28:31], v0 offset:4352\n"
        "s_waitcnt lgkmcnt(4)\n"
        "v_add_f32 v36, 1.0, v36\n"
        "v_rcp_f32 v35, v35\n"
        "v_add_f32 v37, 1.0, v37\n"
        "v_mfma_f32_32x32x16_bf16 v[64:79], v[160:163], v[8:11], v[64:79]\n"
        "ds_read_b128 v[8:11], v0 offset:4608\n"
        "v_rcp_f32 v36, v36\n"
        "v_add_f32 v38, 1.0, v38\n"
        "v_rcp_f32 v37, v37\n"
        "v_mfma_f32_32x32x16_bf16 v[64:79], v[164:167], v[12:15], v[64:79]\n"
        "ds_read_b128 v[12:15], v0 offset:4864\n"
        "s_waitcnt lgkmcnt(4)\n"
        "v_add_f32 v39, 1.0, v39\n"
        "v_rcp_f32 v38, v38\n"
        "v_mfma_f32_32x32x16_bf16 v[64:79], v[168:171], v[16:19], v[64:79]\n"
        "ds_read_b128 v[16:19], v0 offset:5120\n"
        "v_rcp_f32 v39, v39\n"
        "v_mul_f32 v48, v48, v32\n"
        "v_mul_f32 v49, v49, v33\n"
        "v_mul_f32 v50, v50, v34\n"
        "v_mfma_f32_32x32x16_bf16 v[64:79], v[172:175], v[20:23], v[64:79]\n"
        "ds_read_b128 v[20:23], v0 offset:5376\n"
        "s_waitcnt lgkmcnt(4)\n"
        "v_mul_f32 v51, v51, v35\n"
        "v_mul_f32 v56, v56, v36\n"
        "v_mul_f32 v57, v57, v37\n"
        "v_mul_f32 v58, v58, v38\n"
        "v_mfma_f32_32x32x16_bf16 v[64:79], v[176:179], v[24:27], v[64:79]\n"
        "ds_read_b128 v[24:27], v0 offset:5632\n"
        "v_mul_f32 v59, v59, v39\n"
        "v_cvt_pk_bf16_f32 v40, v48, v49\n"
        "v_cvt_pk_bf16_f32 v41, v50, v51\n"
        "v_cvt_pk_bf16_f32 v42, v56, v57\n"
        "v_cvt_pk_bf16_f32 v43, v58, v59\n"
        "v_mfma_f32_32x32x16_bf16 v[64:79], v[180:183], v[28:31], v[64:79]\n"
        "ds_read_b128 v[28:31], v0 offset:5888\n"
        "s_waitcnt lgkmcnt(4)\n"
        "buffer_store_dwordx4 v[40:43], v1, s[40:43], s72 offen sc1\n"
        "v_mfma_f32_32x32x16_bf16 v[64:79], v[184:187], v[8:11], v[64:79]\n"
        "ds_read_b128 v[8:11], v0 offset:6144\n"
        "buffer_load_dword v4, v2, s[40:43], s76 offen sc1\n"
        "v_mfma_f32_32x32x16_bf16 v[64:79], v[188:191], v[12:15], v[64:79]\n"
        "ds_read_b128 v[12:15], v0 offset:6400\n"
        "s_waitcnt lgkmcnt(4)\n"
        "v_mfma_f32_32x32x16_bf16 v[64:79], v[192:195], v[16:19], v[64:79]\n"
        "ds_read_b128 v[16:19], v0 offset:6656\n"
        "s_add_u32 s78, s78, 37376\n"
        "s_cmp_eq_u32 s78, 112128\n"
        "s_cselect_b32 s78, 0, s78\n"
        "v_mfma_f32_32x32x16_bf16 v[64:79], v[196:199], v[20:23], v[64:79]\n"
        "ds_read_b128 v[20:23], v0 offset:6912\n"
        "s_waitcnt lgkmcnt(4)\n"
        "v_mfma_f32_32x32x16_bf16 v[64:79], v[200:203], v[24:27], v[64:79]\n"
        "ds_read_b128 v[24:27], v0 offset:7168\n"
        "v_mfma_f32_32x32x16_bf16 v[64:79], v[204:207], v[28:31], v[64:79]\n"
        "ds_read_b128 v[28:31], v0 offset:7424\n"
        "s_waitcnt lgkmcnt(4)\n"
        "v_mfma_f32_32x32x16_bf16 v[64:79], v[208:211], v[8:11], v[64:79]\n"
        "ds_read_b128 v[8:11], v0 offset:7680\n"
        "v_mfma_f32_32x32x16_bf16 v[64:79], v[212:215], v[12:15], v[64:79]\n"
        "ds_read_b128 v[12:15], v0 offset:7936\n"
        "s_waitcnt lgkmcnt(4)\n"
        "v_mfma_f32_32x32x16_bf16 v[64:79], v[216:219], v[16:19], v[64:79]\n"
        "ds_read_b128 v[16:19], v0 offset:8192\n"
        "v_mfma_f32_32x32x16_bf16 v[64:79], v[220:223], v[20:23], v[64:79]\n"
        "ds_read_b128 v[20:23], v0 offset:8448\n"
        "s_waitcnt lgkmcnt(4)\n"
        "v_mfma_f32_32x32x16_bf16 v[64:79], v[224:227], v[24:27], v[64:79]\n"
        "ds_read_b128 v[24:27], v0 offset:8704\n"
        "v_mfma_f32_32x32x16_bf16 v[64:79], v[228:231], v[28:31], v[64:79]\n"
        "ds_read_b128 v[28:31], v0 offset:8960\n"
        "s_waitcnt lgkmcnt(4)\n"
        "s_sub_u32 s65, s69, s68\n"
        "s_cmp_eq_u32 s65, 2\n"
        "s_cbranch_scc0 .Lf1_tail_%=\n"
        "s_waitcnt vmcnt(2)\n"
        "s_barrier\n"
        "v_add_u32 v0, s78, v6\n"
        "v_mfma_f32_32x32x16_bf16 v[64:79], v[232:235], v[8:11], v[64:79]\n"
        "ds_read_b128 v[8:11], v0 offset:0\n"
        "s_cmp_lt_u32 s69, 64\n"
        "s_cselect_b32 s70, s69, 2147483647\n"
        "v_mfma_f32_32x32x16_bf16 v[64:79], v[236:239], v[12:15], v[64:79]\n"
        "ds_read_b128 v[12:15], v0 offset:256\n"
        "s_waitcnt lgkmcnt(4)\n"
        "v_mfma_f32_32x32x16_bf16 v[64:79], v[240:243], v[16:19], v[64:79]\n"
        "ds_read_b128 v[16:19], v0 offset:512\n"
        "s_waitcnt vmcnt(1)\n"
        "v_mov_b32 v5, s74\n"
        "buffer_store_dword v5, v3, s[40:43], s73 offen sc1\n"
        "s_add_u32 s74, s74, 1\n"
        "s_add_u32 s72, s75, 98304\n"
        "v_mfma_f32_32x32x16_bf16 v[64:79], v[244:247], v[20:23], v[64:79]\n"
        "ds_read_b128 v[20:23], v0 offset:768\n"
        "s_waitcnt lgkmcnt(4)\n"
        "v_mfma_f32_32x32x16_bf16 v[64:79], v[248:251], v[24:27], v[64:79]\n"
        "ds_read_b128 v[24:27], v0 offset:1024\n"
        "s_waitcnt vmcnt(1)\n"
        "v_cmp_gt_i32 vcc, s70, v4\n"
        "v_mfma_f32_32x32x16_bf16 v[64:79], v[252:255], v[28:31], v[64:79]\n"
        "ds_read_b128 v[28:31], v0 offset:1280\n"
        "s_waitcnt lgkmcnt(4)\n"
        "s_and_b64 vcc, vcc, 1\n"
        "s_cbranch_vccz .Lf1_p01_%=\n"
        "s_branch .Lf1_p00_%=\n"
        ".Lf1_p01_%=:\n"
        "v_mfma_f32_32x32x16_bf16 v[32:47], a[0:3], v[8:11], 0\n"
        "ds_read_b128 v[8:11], v0 offset:1536\n"
        "s_add_u32 m0, s77, s61\n"
        "s_lshr_b32 s65, s69, 1\n"
        "s_bitcmp1_b32 s54, s65\n"
        "s_cselect_b32 s65, 8192, 0\n"
        "s_lshl_b32 s71, s69, 20\n"
        "v_mfma_f32_32x32x16_bf16 v[32:47], a[4:7], v[12:15], v[32:47]\n"
        "ds_read_b128 v[12:15], v0 offset:1792\n"
        "s_waitcnt lgkmcnt(4)\n"
        "s_add_u32 s71, s71, s65\n"
        "s_add_u32 s71, s71, s58\n"
        "v_mul_f32 v48, s80, v64\n"
        "v_mul_f32 v49, s80, v65\n"
        "v_mfma_f32_32x32x16_bf16 v[32:47], a[8:11], v[16:19], v[32:47]\n"
        "ds_read_b128 v[16:19], v0 offset:2048\n"
        "buffer_load_dwordx4 v1, s[40:43], s71 offen offset:0 sc1 lds\n"
        "v_mul_f32 v50, s80, v66\n"
        "v_mul_f32 v51, s80, v67\n"
        "v_mul_f32 v52, s80, v72\n"
        "v_mul_f32 v53, s80, v73\n"
        "v_mfma_f32_32x32x16_bf16 v[32:47], a[12:15], v[20:23], v[32:47]\n"
        "ds_read_b128 v[20:23], v0 offset:2304\n"
        "s_waitcnt lgkmcnt(4)\n"
        "v_mul_f32 v54, s80, v74\n"
        "v_mul_f32 v55, s80, v75\n"
        "v_exp_f32 v48, v48\n"
        "v_mfma_f32_32x32x16_bf16 v[32:47], a[16:19], v[24:27], v[32:47]\n"
        "ds_read_b128 v[24:27], v0 offset:2560\n"
        "buffer_load_dwordx4 v1, s[40:43], s71 offen offset:1024 sc1 lds\n"
        "v_mul_f32 v64, v64, v68\n"
        "v_exp_f32 v49, v49\n"
        "v_mul_f32 v65, v65, v69\n"
        "v_mfma_f32_32x32x16_bf16 v[32:47], a[20:23], v[28:31], v[32:47]\n"
        "ds_read_b128 v[28:31], v0 offset:2816\n"
        "s_waitcnt lgkmcnt(4)\n"
        "v_exp_f32 v50, v50\n"
        "v_mul_f32 v66, v66, v70\n"
        "v_mfma_f32_32x32x16_bf16 v[32:47], a[24:27], v[8:11], v[32:47]\n"
        "ds_read_b128 v[8:11], v0 offset:3072\n"
        "buffer_load_dwordx4 v1, s[40:43], s71 offen offset:2048 sc1 lds\n"
        "v_exp_f32 v51, v51\n"
        "v_mul_f32 v67, v67, v71\n"
        "v_mfma_f32_32x32x16_bf16 v[32:47], a[28:31], v[12:15], v[32:47]\n"
        "ds_read_b128 v[12:15], v0 offset:3328\n"
        "s_waitcnt lgkmcnt(4)\n"
        "v_exp_f32 v52, v52\n"
        "v_mul_f32 v72, v72, v76\n"
        "v_mfma_f32_32x32x16_bf16 v[32:47], a[32:35], v[16:19], v[32:47]\n"
        "ds_read_b128 v[16:19], v0 offset:3584\n"
        "buffer_load_dwordx4 v1, s[40:43], s71 offen offset:3072 sc1 lds\n"
        "s_add_u32 m0, m0, 4096\n"
        "s_add_u32 s71, s71, 4096\n"
        "s_mul_i32 s65, s48, -15360\n"
        "v_mfma_f32_32x32x16_bf16 v[32:47], a[36:39], v[20:23], v[32:47]\n"
        "s_waitcnt vmcnt(4)\n"
        "s_barrier\n"
        "ds_read_b128 v[20:23], v0 offset:3840\n"
        "s_waitcnt lgkmcnt(4)\n"
        "v_exp_f32 v53, v53\n"
        "v_mfma_f32_32x32x16_bf16 v[32:47], a[40:43], v[24:27], v[32:47]\n"
        "ds_read_b128 v[24:27], v0 offset:4096\n"
        "buffer_load_dwordx4 v1, s[40:43], s71 offen offset:0 sc1 lds\n"
        "v_mul_f32 v73, v73, v77\n"
        "v_exp_f32 v54, v54\n"
        "v_mul_f32 v74, v74, v78\n"
        "v_mfma_f32_32x32x16_bf16 v[32:47], a[44:47], v[28:31], v[32:47]\n"
        "ds_read_b128 v[28:31], v0 offset:4352\n"
        "s_waitcnt lgkmcnt(4)\n"
        "v_exp_f32 v55, v55\n"
        "v_mul_f32 v75, v75, v79\n"
        "v_add_f32 v48, 1.0, v48\n"
        "v_mfma_f32_32x32x16_bf16 v[32:47], a[48:51], v[8:11], v[32:47]\n"
        "ds_read_b128 v[8:11], v0 offset:4608\n"
        "buffer_load_dwordx4 v1, s[40:43], s71 offen offset:1024 sc1 lds\n"
        "v_add_f32 v49, 1.0, v49\n"
        "v_rcp_f32 v48, v48\n"
        "v_add_f32 v50, 1.0, v50\n"
        "v_mfma_f32_32x32x16_bf16 v[32:47], a[52:55], v[12:15], v[32:47]\n"
        "ds_read_b128 v[12:15], v0 offset:4864\n"
        "s_waitcnt lgkmcnt(4)\n"
        "v_rcp_f32 v49, v49\n"
        "v_add_f32 v51, 1.0, v51\n"
        "v_mfma_f32_32x32x16_bf16 v[32:47], a[56:59], v[16:19], v[32:47]\n"
        "ds_read_b128 v[16:19], v0 offset:5120\n"
        "buffer_load_dwordx4 v1, s[40:43], s71 offen offset:2048 sc1 lds\n"
        "v_rcp_f32 v50, v50\n"
        "v_add_f32 v52, 1.0, v52\n"
        "v_mfma_f32_32x32x16_bf16 v[32:47], a[60:63], v[20:23], v[32:47]\n"
        "ds_read_b128 v[20:23], v0 offset:5376\n"
        "s_waitcnt lgkmcnt(4)\n"
        "v_rcp_f32 v51, v51\n"
        "v_add_f32 v53, 1.0, v53\n"
        "v_mfma_f32_32x32x16_bf16 v[32:47], a[64:67], v[24:27], v[32:47]\n"
        "ds_read_b128 v[24:27], v0 offset:5632\n"
        "buffer_load_dwordx4 v1, s[40:43], s71 offen offset:3072 sc1 lds\n"
        "s_add_u32 m0, m0, 4096\n"
        "s_add_u32 s71, s71, s65\n"
        "s_add_u32 s71, s71, 61440\n"
        "v_mfma_f32_32x32x16_bf16 v[32:47], a[68:71], v[28:31], v[32:47]\n"
        "ds_read_b128 v[28:31], v0 offset:5888\n"
        "s_waitcnt lgkmcnt(4)\n"
        "v_rcp_f32 v52, v52\n"
        "v_add_f32 v54, 1.0, v54\n"
        "v_mfma_f32_32x32x16_bf16 v[32:47], a[72:75], v[8:11], v[32:47]\n"
        "ds_read_b128 v[8:11], v0 offset:6144\n"
        "buffer_load_dwordx4 v1, s[40:43], s71 offen offset:0 sc1 lds\n"
        "s_add_u32 s69, s69, 1\n"
        "s_add_u32 s77, s77, 37376\n"
        "s_cmp_eq_u32 s77, 112128\n"
        "s_cselect_b32 s77, 0, s77\n"
        "v_mfma_f32_32x32x16_bf16 v[32:47], a[76:79], v[12:15], v[32:47]\n"
        "ds_read_b128 v[12:15], v0 offset:6400\n"
        "s_waitcnt lgkmcnt(4)\n"
        "s_mov_b32 s70, 2147483647\n"
        "v_rcp_f32 v53, v53\n"
        "v_add_f32 v55, 1.0, v55\n"
        "v_mfma_f32_32x32x16_bf16 v[32:47], a[80:83], v[16:19], v[32:47]\n"
        "ds_read_b128 v[16:19], v0 offset:6656\n"
        "v_rcp_f32 v54, v54\n"
        "v_rcp_f32 v55, v55\n"
        "v_mul_f32 v64, v64, v48\n"
        "v_mfma_f32_32x32x16_bf16 v[32:47], a[84:87], v[20:23], v[32:47]\n"
        "ds_read_b128 v[20:23], v0 offset:6912\n"
        "s_waitcnt lgkmcnt(4)\n"
        "v_mul_f32 v65, v65, v49\n"
        "v_mul_f32 v66, v66, v50\n"
        "v_mul_f32 v67, v67, v51\n"
        "v_mul_f32 v72, v72, v52\n"
        "v_mfma_f32_32x32x16_bf16 v[32:47], a[88:91], v[24:27], v[32:47]\n"
        "ds_read_b128 v[24:27], v0 offset:7168\n"
        "v_mul_f32 v73, v73, v53\n"
        "v_mul_f32 v74, v74, v54\n"
        "v_mul_f32 v75, v75, v55\n"
        "v_cvt_pk_bf16_f32 v56, v64, v65\n"
        "v_cvt_pk_bf16_f32 v57, v66, v67\n"
        "v_mfma_f32_32x32x16_bf16 v[32:47], a[92:95], v[28:31], v[32:47]\n"
        "ds_read_b128 v[28:31], v0 offset:7424\n"
        "s_waitcnt lgkmcnt(4)\n"
        "v_cvt_pk_bf16_f32 v58, v72, v73\n"
        "v_cvt_pk_bf16_f32 v59, v74, v75\n"
        "v_mfma_f32_32x32x16_bf16 v[32:47], a[96:99], v[8:11], v[32:47]\n"
        "ds_read_b128 v[8:11], v0 offset:7680\n"
        "buffer_store_dwordx4 v[56:59], v1, s[40:43], s72 offen sc1\n"
        "v_mfma_f32_32x32x16_bf16 v[32:47], a[100:103], v[12:15], v[32:47]\n"
        "ds_read_b128 v[12:15], v0 offset:7936\n"
        "s_waitcnt lgkmcnt(4)\n"
        "v_mfma_f32_32x32x16_bf16 v[32:47], a[104:107], v[16:19], v[32:47]\n"
        "ds_read_b128 v[16:19], v0 offset:8192\n"
        "v_mfma_f32_32x32x16_bf16 v[32:47], a[108:111], v[20:23], v[32:47]\n"
        "ds_read_b128 v[20:23], v0 offset:8448\n"
        "s_waitcnt lgkmcnt(4)\n"
        "v_mfma_f32_32x32x16_bf16 v[32:47], a[112:115], v[24:27], v[32:47]\n"
        "ds_read_b128 v[24:27], v0 offset:8704\n"
        "v_mfma_f32_32x32x16_bf16 v[32:47], a[116:119], v[28:31], v[32:47]\n"
        "ds_read_b128 v[28:31], v0 offset:8960\n"
        "s_waitcnt lgkmcnt(4)\n"
        "v_mfma_f32_32x32x16_bf16 v[32:47], a[120:123], v[8:11], v[32:47]\n"
        "ds_read_b128 v[8:11], v0 offset:0\n"
        "v_mfma_f32_32x32x16_bf16 v[32:47], a[124:127], v[12:15], v[32:47]\n"
        "ds_read_b128 v[12:15], v0 offset:256\n"
        "s_waitcnt lgkmcnt(4)\n"
        "v_mfma_f32_32x32x16_bf16 v[32:47], a[128:131], v[16:19], v[32:47]\n"
        "ds_read_b128 v[16:19], v0 offset:512\n"
        "s_waitcnt vmcnt(0)\n"
        "v_mov_b32 v5, s74\n"
        "buffer_store_dword v5, v3, s[40:43], s73 offen sc1\n"
        "s_add_u32 s68, s68, 1\n"
        "s_lshr_b32 s66, s68, 1\n"
        "v_mfma_f32_32x32x16_bf16 v[32:47], a[132:135], v[20:23], v[32:47]\n"
        "ds_read_b128 v[20:23], v0 offset:768\n"
        "s_waitcnt lgkmcnt(4)\n"
        "s_bitcmp1_b32 s54, s66\n"
        "s_cselect_b32 s66, 8192, 0\n"
        "s_lshl_b32 s75, s68, 20\n"
        "s_add_u32 s75, s75, s66\n"
        "v_mfma_f32_32x32x16_bf16 v[32:47], a[136:139], v[24:27], v[32:47]\n"
        "ds_read_b128 v[24:27], v0 offset:1024\n"
        "s_add_u32 s75, s75, s59\n"
        "s_mul_i32 s74, s68, 3\n"
        "s_mov_b32 s73, s60\n"
        "s_add_u32 s72, s75, 0\n"
        "v_mfma_f32_32x32x16_bf16 v[32:47], a[140:143], v[28:31], v[32:47]\n"
        "ds_read_b128 v[28:31], v0 offset:1280\n"
        "s_waitcnt lgkmcnt(4)\n"
        "s_branch .Lf1_p10_%=\n"
        ".Lf1_p11_%=:\n"
        "v_mfma_f32_32x32x16_bf16 v[48:63], a[144:147], v[8:11], 0\n"
        "ds_read_b128 v[8:11], v0 offset:1536\n"
        "s_add_u32 m0, s77, s61\n"
        "s_lshr_b32 s65, s69, 1\n"
        "s_bitcmp1_b32 s54, s65\n"
        "s_cselect_b32 s65, 8192, 0\n"
        "s_lshl_b32 s71, s69, 20\n"
        "v_mfma_f32_32x32x16_bf16 v[48:63], a[148:151], v[12:15], v[48:63]\n"
        "ds_read_b128 v[12:15], v0 offset:1792\n"
        "s_waitcnt lgkmcnt(4)\n"
        "s_add_u32 s71, s71, s65\n"
        "s_add_u32 s71, s71, s58\n"
        "v_mul_f32 v64, s80, v32\n"
        "v_mul_f32 v65, s80, v33\n"
        "v_mfma_f32_32x32x16_bf16 v[48:63], a[152:155], v[16:19], v[48:63]\n"
        "ds_read_b128 v[16:19], v0 offset:2048\n"
        "buffer_load_dwordx4 v1, s[40:43], s71 offen offset:0 sc1 lds\n"
        "v_mul_f32 v66, s80, v34\n"
        "v_mul_f32 v67, s80, v35\n"
        "v_mul_f32 v68, s80, v40\n"
        "v_mul_f32 v69, s80, v41\n"
        "v_mfma_f32_32x32x16_bf16 v[48:63], a[156:159], v[20:23], v[48:63]\n"
        "ds_read_b128 v[20:23], v0 offset:2304\n"
        "s_waitcnt lgkmcnt(4)\n"
        "v_mul_f32 v70, s80, v42\n"
        "v_mul_f32 v71, s80, v43\n"
        "v_exp_f32 v64, v64\n"
        "v_mfma_f32_32x32x16_bf16 v[48:63], a[160:163], v[24:27], v[48:63]\n"
        "ds_read_b128 v[24:27], v0 offset:2560\n"
        "buffer_load_dwordx4 v1, s[40:43], s71 offen offset:1024 sc1 lds\n"
        "v_mul_f32 v32, v32, v36\n"
        "v_exp_f32 v65, v65\n"
        "v_mul_f32 v33, v33, v37\n"
        "v_mfma_f32_32x32x16_bf16 v[48:63], a[164:167], v[28:31], v[48:63]\n"
        "ds_read_b128 v[28:31], v0 offset:2816\n"
        "s_waitcnt lgkmcnt(4)\n"
        "v_exp_f32 v66, v66\n"
        "v_mul_f32 v34, v34, v38\n"
        "v_mfma_f32_32x32x16_bf16 v[48:63], a[168:171], v[8:11], v[48:63]\n"
        "ds_read_b128 v[8:11], v0 offset:3072\n"
        "buffer_load_dwordx4 v1, s[40:43], s71 offen offset:2048 sc1 lds\n"
        "v_exp_f32 v67, v67\n"
        "v_mul_f32 v35, v35, v39\n"
        "v_mfma_f32_32x32x16_bf16 v[48:63], a[172:175], v[12:15], v[48:63]\n"
        "ds_read_b128 v[12:15], v0 offset:3328\n"
        "s_waitcnt lgkmcnt(4)\n"
        "v_exp_f32 v68, v68\n"
        "v_mul_f32 v40, v40, v44\n"
        "v_mfma_f32_32x32x16_bf16 v[48:63], a[176:179], v[16:19], v[48:63]\n"
        "ds_read_b128 v[16:19], v0 offset:3584\n"
        "buffer_load_dwordx4 v1, s[40:43], s71 offen offset:3072 sc1 lds\n"
        "s_add_u32 m0, m0, 4096\n"
        "s_add_u32 s71, s71, 4096\n"
        "s_mul_i32 s65, s48, -15360\n"
        "v_mfma_f32_32x32x16_bf16 v[48:63], a[180:183], v[20:23], v[48:63]\n"
        "ds_read_b128 v[20:23], v0 offset:3840\n"
        "s_waitcnt lgkmcnt(4)\n"
        "v_exp_f32 v69, v69\n"
        "v_mul_f32 v41, v41, v45\n"
        "v_mfma_f32_32x32x16_bf16 v[48:63], a[184:187], v[24:27], v[48:63]\n"
        "ds_read_b128 v[24:27], v0 offset:4096\n"
        "buffer_load_dwordx4 v1, s[40:43], s71 offen offset:0 sc1 lds\n"
        "v_exp_f32 v70, v70\n"
        "v_mul_f32 v42, v42, v46\n"
        "v_mfma_f32_32x32x16_bf16 v[48:63], a[188:191], v[28:31], v[48:63]\n"
        "ds_read_b128 v[28:31], v0 offset:4352\n"
        "s_waitcnt lgkmcnt(4)\n"
        "v_exp_f32 v71, v71\n"
        "v_mul_f32 v43, v43, v47\n"
        "v_add_f32 v64, 1.0, v64\n"
        "v_mfma_f32_32x32x16_bf16 v[48:63], a[192:195], v[8:11], v[48:63]\n"
        "ds_read_b128 v[8:11], v0 offset:4608\n"
        "buffer_load_dwordx4 v1, s[40:43], s71 offen offset:1024 sc1 lds\n"
        "v_add_f32 v65, 1.0, v65\n"
        "v_rcp_f32 v64, v64\n"
        "v_add_f32 v66, 1.0, v66\n"
        "v_mfma_f32_32x32x16_bf16 v[48:63], a[196:199], v[12:15], v[48:63]\n"
        "ds_read_b128 v[12:15], v0 offset:4864\n"
        "s_waitcnt lgkmcnt(4)\n"
        "v_rcp_f32 v65, v65\n"
        "v_add_f32 v67, 1.0, v67\n"
        "v_mfma_f32_32x32x16_bf16 v[48:63], a[200:203], v[16:19], v[48:63]\n"
        "ds_read_b128 v[16:19], v0 offset:5120\n"
        "buffer_load_dwordx4 v1, s[40:43], s71 offen offset:2048 sc1 lds\n"
        "v_rcp_f32 v66, v66\n"
        "v_add_f32 v68, 1.0, v68\n"
        "v_mfma_f32_32x32x16_bf16 v[48:63], a[204:207], v[20:23], v[48:63]\n"
        "ds_read_b128 v[20:23], v0 offset:5376\n"
        "s_waitcnt lgkmcnt(4)\n"
        "v_rcp_f32 v67, v67\n"
        "v_add_f32 v69, 1.0, v69\n"
        "v_mfma_f32_32x32x16_bf16 v[48:63], a[208:211], v[24:27], v[48:63]\n"
        "ds_read_b128 v[24:27], v0 offset:5632\n"
        "buffer_load_dwordx4 v1, s[40:43], s71 offen offset:3072 sc1 lds\n"
        "s_add_u32 m0, m0, 4096\n"
        "s_add_u32 s71, s71, s65\n"
        "s_add_u32 s71, s71, 61440\n"
        "v_mfma_f32_32x32x16_bf16 v[48:63], a[212:215], v[28:31], v[48:63]\n"
        "ds_read_b128 v[28:31], v0 offset:5888\n"
        "s_waitcnt lgkmcnt(4)\n"
        "v_rcp_f32 v68, v68\n"
        "v_add_f32 v70, 1.0, v70\n"
        "v_mfma_f32_32x32x16_bf16 v[48:63], a[216:219], v[8:11], v[48:63]\n"
        "ds_read_b128 v[8:11], v0 offset:6144\n"
        "buffer_load_dwordx4 v1, s[40:43], s71 offen offset:0 sc1 lds\n"
        "s_add_u32 s69, s69, 1\n"
        "s_add_u32 s77, s77, 37376\n"
        "s_cmp_eq_u32 s77, 112128\n"
        "s_cselect_b32 s77, 0, s77\n"
        "v_mfma_f32_32x32x16_bf16 v[48:63], a[220:223], v[12:15], v[48:63]\n"
        "ds_read_b128 v[12:15], v0 offset:6400\n"
        "s_waitcnt lgkmcnt(4)\n"
        "s_mov_b32 s70, 2147483647\n"
        "v_rcp_f32 v69, v69\n"
        "v_add_f32 v71, 1.0, v71\n"
        "v_mfma_f32_32x32x16_bf16 v[48:63], a[224:227], v[16:19], v[48:63]\n"
        "ds_read_b128 v[16:19], v0 offset:6656\n"
        "v_rcp_f32 v70, v70\n"
        "v_rcp_f32 v71, v71\n"
        "v_mul_f32 v32, v32, v64\n"
        "v_mfma_f32_32x32x16_bf16 v[48:63], a[228:231], v[20:23], v[48:63]\n"
        "ds_read_b128 v[20:23], v0 offset:6912\n"
        "s_waitcnt lgkmcnt(4)\n"
        "v_mul_f32 v33, v33, v65\n"
        "v_mul_f32 v34, v34, v66\n"
        "v_mul_f32 v35, v35, v67\n"
        "v_mul_f32 v40, v40, v68\n"
        "v_mfma_f32_32x32x16_bf16 v[48:63], a[232:235], v[24:27], v[48:63]\n"
        "ds_read_b128 v[24:27], v0 offset:7168\n"
        "v_mul_f32 v41, v41, v69\n"
        "v_mul_f32 v42, v42, v70\n"
        "v_mul_f32 v43, v43, v71\n"
        "v_cvt_pk_bf16_f32 v72, v32, v33\n"
        "v_cvt_pk_bf16_f32 v73, v34, v35\n"
        "v_mfma_f32_32x32x16_bf16 v[48:63], a[236:239], v[28:31], v[48:63]\n"
        "ds_read_b128 v[28:31], v0 offset:7424\n"
        "s_waitcnt lgkmcnt(4)\n"
        "v_cvt_pk_bf16_f32 v74, v40, v41\n"
        "v_cvt_pk_bf16_f32 v75, v42, v43\n"
        "v_mfma_f32_32x32x16_bf16 v[48:63], a[240:243], v[8:11], v[48:63]\n"
        "ds_read_b128 v[8:11], v0 offset:7680\n"
        "buffer_store_dwordx4 v[72:75], v1, s[40:43], s72 offen sc1\n"
        "v_mfma_f32_32x32x16_bf16 v[48:63], a[244:247], v[12:15], v[48:63]\n"
        "ds_read_b128 v[12:15], v0 offset:7936\n"
        "s_waitcnt lgkmcnt(4)\n"
        "v_mfma_f32_32x32x16_bf16 v[48:63], a[248:251], v[16:19], v[48:63]\n"
        "ds_read_b128 v[16:19], v0 offset:8192\n"
        "v_mfma_f32_32x32x16_bf16 v[48:63], a[252:255], v[20:23], v[48:63]\n"
        "ds_read_b128 v[20:23], v0 offset:8448\n"
        "s_waitcnt lgkmcnt(4)\n"
        "v_mfma_f32_32x32x16_bf16 v[48:63], v[80:83], v[24:27], v[48:63]\n"
        "ds_read_b128 v[24:27], v0 offset:8704\n"
        "v_mfma_f32_32x32x16_bf16 v[48:63], v[84:87], v[28:31], v[48:63]\n"
        "ds_read_b128 v[28:31], v0 offset:8960\n"
        "s_waitcnt lgkmcnt(4)\n"
        "v_mfma_f32_32x32x16_bf16 v[48:63], v[88:91], v[8:11], v[48:63]\n"
        "ds_read_b128 v[8:11], v0 offset:0\n"
        "v_mfma_f32_32x32x16_bf16 v[48:63], v[92:95], v[12:15], v[48:63]\n"
        "ds_read_b128 v[12:15], v0 offset:256\n"
        "s_waitcnt lgkmcnt(4)\n"
        "v_mfma_f32_32x32x16_bf16 v[48:63], v[96:99], v[16:19], v[48:63]\n"
        "ds_read_b128 v[16:19], v0 offset:512\n"
        "s_waitcnt vmcnt(0)\n"
        "v_mov_b32 v5, s74\n"
        "buffer_store_dword v5, v3, s[40:43], s73 offen sc1\n"
        "s_add_u32 s74, s74, 1\n"
        "s_add_u32 s72, s75, 49152\n"
        "v_mfma_f32_32x32x16_bf16 v[48:63], v[100:103], v[20:23], v[48:63]\n"
        "ds_read_b128 v[20:23], v0 offset:768\n"
        "s_waitcnt lgkmcnt(4)\n"
        "v_mfma_f32_32x32x16_bf16 v[48:63], v[104:107], v[24:27], v[48:63]\n"
        "ds_read_b128 v[24:27], v0 offset:1024\n"
        "v_mfma_f32_32x32x16_bf16 v[48:63], v[108:111], v[28:31], v[48:63]\n"
        "ds_read_b128 v[28:31], v0 offset:1280\n"
        "s_waitcnt lgkmcnt(4)\n"
        "s_branch .Lf1_p20_%=\n"
        ".Lf1_p21_%=:\n"
        "v_mfma_f32_32x32x16_bf16 v[64:79], v[112:115], v[8:11], 0\n"
        "ds_read_b128 v[8:11], v0 offset:1536\n"
        "s_add_u32 m0, s77, s61\n"
        "s_lshr_b32 s65, s69, 1\n"
        "s_bitcmp1_b32 s54, s65\n"
        "s_cselect_b32 s65, 8192, 0\n"
        "s_lshl_b32 s71, s69, 20\n"
        "v_mfma_f32_32x32x16_bf16 v[64:79], v[116:119], v[12:15], v[64:79]\n"
        "ds_read_b128 v[12:15], v0 offset:1792\n"
        "s_waitcnt lgkmcnt(4)\n"
        "s_add_u32 s71, s71, s65\n"
        "s_add_u32 s71, s71, s58\n"
        "v_mul_f32 v32, s80, v48\n"
        "v_mul_f32 v33, s80, v49\n"
        "v_mfma_f32_32x32x16_bf16 v[64:79], v[120:123], v[16:19], v[64:79]\n"
        "ds_read_b128 v[16:19], v0 offset:2048\n"
        "buffer_load_dwordx4 v1, s[40:43], s71 offen offset:0 sc1 lds\n"
        "v_mul_f32 v34, s80, v50\n"
        "v_mul_f32 v35, s80, v51\n"
        "v_mul_f32 v36, s80, v56\n"
        "v_mul_f32 v37, s80, v57\n"
        "v_mfma_f32_32x32x16_bf16 v[64:79], v[124:127], v[20:23], v[64:79]\n"
        "ds_read_b128 v[20:23], v0 offset:2304\n"
        "s_waitcnt lgkmcnt(4)\n"
        "v_mul_f32 v38, s80, v58\n"
        "v_mul_f32 v39, s80, v59\n"
        "v_exp_f32 v32, v32\n"
        "v_mfma_f32_32x32x16_bf16 v[64:79], v[128:131], v[24:27], v[64:79]\n"
        "ds_read_b128 v[24:27], v0 offset:2560\n"
        "buffer_load_dwordx4 v1, s[40:43], s71 offen offset:1024 sc1 lds\n"
        "v_mul_f32 v48, v48, v52\n"
        "v_exp_f32 v33, v33\n"
        "v_mul_f32 v49, v49, v53\n"
        "v_mfma_f32_32x32x16_bf16 v[64:79], v[132:135], v[28:31], v[64:79]\n"
        "ds_read_b128 v[28:31], v0 offset:2816\n"
        "s_waitcnt lgkmcnt(4)\n"
        "v_exp_f32 v34, v34\n"
        "v_mul_f32 v50, v50, v54\n"
        "v_mfma_f32_32x32x16_bf16 v[64:79], v[136:139], v[8:11], v[64:79]\n"
        "ds_read_b128 v[8:11], v0 offset:3072\n"
        "buffer_load_dwordx4 v1, s[40:43], s71 offen offset:2048 sc1 lds\n"
        "v_exp_f32 v35, v35\n"
        "v_mul_f32 v51, v51, v55\n"
        "v_mfma_f32_32x32x16_bf16 v[64:79], v[140:143], v[12:15], v[64:79]\n"
        "ds_read_b128 v[12:15], v0 offset:3328\n"
        "s_waitcnt lgkmcnt(4)\n"
        "v_exp_f32 v36, v36\n"
        "v_mul_f32 v56, v56, v60\n"
        "v_mfma_f32_32x32x16_bf16 v[64:79], v[144:147], v[16:19], v[64:79]\n"
        "ds_read_b128 v[16:19], v0 offset:3584\n"
        "buffer_load_dwordx4 v1, s[40:43], s71 offen offset:3072 sc1 lds\n"
        "s_add_u32 m0, m0, 4096\n"
        "s_add_u32 s71, s71, 4096\n"
        "s_mul_i32 s65, s48, -15360\n"
        "v_mfma_f32_32x32x16_bf16 v[64:79], v[148:151], v[20:23], v[64:79]\n"
        "ds_read_b128 v[20:23], v0 offset:3840\n"
        "s_waitcnt lgkmcnt(4)\n"
        "v_exp_f32 v37, v37\n"
        "v_mul_f32 v57, v57, v61\n"
        "v_mfma_f32_32x32x16_bf16 v[64:79], v[152:155], v[24:27], v[64:79]\n"
        "ds_read_b128 v[24:27], v0 offset:4096\n"
        "buffer_load_dwordx4 v1, s[40:43], s71 offen offset:0 sc1 lds\n"
        "v_exp_f32 v38, v38\n"
        "v_mul_f32 v58, v58, v62\n"
        "v_mfma_f32_32x32x16_bf16 v[64:79], v[156:159], v[28:31], v[64:79]\n"
        "ds_read_b128 v[28:31], v0 offset:4352\n"
        "s_waitcnt lgkmcnt(4)\n"
        "v_exp_f32 v39, v39\n"
        "v_mul_f32 v59, v59, v63\n"
        "v_add_f32 v32, 1.0, v32\n"
        "v_mfma_f32_32x32x16_bf16 v[64:79], v[160:163], v[8:11], v[64:79]\n"
        "ds_read_b128 v[8:11], v0 offset:4608\n"
        "buffer_load_dwordx4 v1, s[40:43], s71 offen offset:1024 sc1 lds\n"
        "v_add_f32 v33, 1.0, v33\n"
        "v_rcp_f32 v32, v32\n"
        "v_add_f32 v34, 1.0, v34\n"
        "v_mfma_f32_32x32x16_bf16 v[64:79], v[164:167], v[12:15], v[64:79]\n"
        "ds_read_b128 v[12:15], v0 offset:4864\n"
        "s_waitcnt lgkmcnt(4)\n"
        "v_rcp_f32 v33, v33\n"
        "v_add_f32 v35, 1.0, v35\n"
        "v_mfma_f32_32x32x16_bf16 v[64:79], v[168:171], v[16:19], v[64:79]\n"
        "ds_read_b128 v[16:19], v0 offset:5120\n"
        "buffer_load_dwordx4 v1, s[40:43], s71 offen offset:2048 sc1 lds\n"
        "v_rcp_f32 v34, v34\n"
        "v_add_f32 v36, 1.0, v36\n"
        "v_mfma_f32_32x32x16_bf16 v[64:79], v[172:175], v[20:23], v[64:79]\n"
        "ds_read_b128 v[20:23], v0 offset:5376\n"
        "s_waitcnt lgkmcnt(4)\n"
        "v_rcp_f32 v35, v35\n"
        "v_add_f32 v37, 1.0, v37\n"
        "v_mfma_f32_32x32x16_bf16 v[64:79], v[176:179], v[24:27], v[64:79]\n"
        "ds_read_b128 v[24:27], v0 offset:5632\n"
        "buffer_load_dwordx4 v1, s[40:43], s71 offen offset:3072 sc1 lds\n"
        "s_add_u32 m0, m0, 4096\n"
        "s_add_u32 s71, s71, s65\n"
        "s_add_u32 s71, s71, 61440\n"
        "buffer_load_dword v4, v2, s[40:43], s76 offen sc1\n"
        "v_mfma_f32_32x32x16_bf16 v[64:79], v[180:183], v[28:31], v[64:79]\n"
        "ds_read_b128 v[28:31], v0 offset:5888\n"
        "s_waitcnt lgkmcnt(4)\n"
        "v_rcp_f32 v36, v36\n"
        "v_add_f32 v38, 1.0, v38\n"
        "v_mfma_f32_32x32x16_bf16 v[64:79], v[184:187], v[8:11], v[64:79]\n"
        "ds_read_b128 v[8:11], v0 offset:6144\n"
        "buffer_load_dwordx4 v1, s[40:43], s71 offen offset:0 sc1 lds\n"
        "s_add_u32 s69, s69, 1\n"
        "s_add_u32 s77, s77, 37376\n"
        "s_cmp_eq_u32 s77, 112128\n"
        "s_cselect_b32 s77, 0, s77\n"
        "v_mfma_f32_32x32x16_bf16 v[64:79], v[188:191], v[12:15], v[64:79]\n"
        "ds_read_b128 v[12:15], v0 offset:6400\n"
        "s_waitcnt lgkmcnt(4)\n"
        "s_mov_b32 s70, 2147483647\n"
        "v_rcp_f32 v37, v37\n"
        "v_add_f32 v39, 1.0, v39\n"
        "v_mfma_f32_32x32x16_bf16 v[64:79], v[192:195], v[16:19], v[64:79]\n"
        "ds_read_b128 v[16:19], v0 offset:6656\n"
        "v_rcp_f32 v38, v38\n"
        "v_rcp_f32 v39, v39\n"
        "v_mul_f32 v48, v48, v32\n"
        "v_mfma_f32_32x32x16_bf16 v[64:79], v[196:199], v[20:23], v[64:79]\n"
        "ds_read_b128 v[20:23], v0 offset:6912\n"
        "s_waitcnt lgkmcnt(4)\n"
        "v_mul_f32 v49, v49, v33\n"
        "v_mul_f32 v50, v50, v34\n"
        "v_mul_f32 v51, v51, v35\n"
        "v_mul_f32 v56, v56, v36\n"
        "v_mfma_f32_32x32x16_bf16 v[64:79], v[200:203], v[24:27], v[64:79]\n"
        "ds_read_b128 v[24:27], v0 offset:7168\n"
        "v_mul_f32 v57, v57, v37\n"
        "v_mul_f32 v58, v58, v38\n"
        "v_mul_f32 v59, v59, v39\n"
        "v_cvt_pk_bf16_f32 v40, v48, v49\n"
        "v_cvt_pk_bf16_f32 v41, v50, v51\n"
        "v_mfma_f32_32x32x16_bf16 v[64:79], v[204:207], v[28:31], v[64:79]\n"
        "ds_read_b128 v[28:31], v0 offset:7424\n"
        "s_waitcnt lgkmcnt(4)\n"
        "v_cvt_pk_bf16_f32 v42, v56, v57\n"
        "v_cvt_pk_bf16_f32 v43, v58, v59\n"
        "s_add_u32 s78, s78, 37376\n"
        "v_mfma_f32_32x32x16_bf16 v[64:79], v[208:211], v[8:11], v[64:79]\n"
        "ds_read_b128 v[8:11], v0 offset:7680\n"
        "buffer_store_dwordx4 v[40:43], v1, s[40:43], s72 offen sc1\n"
        "v_mfma_f32_32x32x16_bf16 v[64:79], v[212:215], v[12:15], v[64:79]\n"
        "ds_read_b128 v[12:15], v0 offset:7936\n"
        "s_waitcnt lgkmcnt(4)\n"
        "s_cmp_eq_u32 s78, 112128\n"
        "s_cselect_b32 s78, 0, s78\n"
        "v_mfma_f32_32x32x16_bf16 v[64:79], v[216:219], v[16:19], v[64:79]\n"
        "ds_read_b128 v[16:19], v0 offset:8192\n"
        "v_mfma_f32_32x32x16_bf16 v[64:79], v[220:223], v[20:23], v[64:79]\n"
        "ds_read_b128 v[20:23], v0 offset:8448\n"
        "s_waitcnt lgkmcnt(4)\n"
        "v_mfma_f32_32x32x16_bf16 v[64:79], v[224:227], v[24:27], v[64:79]\n"
        "ds_read_b128 v[24:27], v0 offset:8704\n"
        "v_mfma_f32_32x32x16_bf16 v[64:79], v[228:231], v[28:31], v[64:79]\n"
        "ds_read_b128 v[28:31], v0 offset:8960\n"
        "s_waitcnt lgkmcnt(4)\n"
        "s_waitcnt vmcnt(7)\n"
        "s_barrier\n"
        "v_add_u32 v0, s78, v6\n"
        "v_mfma_f32_32x32x16_bf16 v[64:79], v[232:235], v[8:11], v[64:79]\n"
        "ds_read_b128 v[8:11], v0 offset:0\n"
        "s_cmp_lt_u32 s69, 64\n"
        "s_cselect_b32 s70, s69, 2147483647\n"
        "v_mfma_f32_32x32x16_bf16 v[64:79], v[236:239], v[12:15], v[64:79]\n"
        "ds_read_b128 v[12:15], v0 offset:256\n"
        "s_waitcnt lgkmcnt(4)\n"
        "v_mfma_f32_32x32x16_bf16 v[64:79], v[240:243], v[16:19], v[64:79]\n"
        "ds_read_b128 v[16:19], v0 offset:512\n"
        "s_waitcnt vmcnt(0)\n"
        "v_mov_b32 v5, s74\n"
        "buffer_store_dword v5, v3, s[40:43], s73 offen sc1\n"
        "s_add_u32 s74, s74, 1\n"
        "s_add_u32 s72, s75, 98304\n"
        "v_mfma_f32_32x32x16_bf16 v[64:79], v[244:247], v[20:23], v[64:79]\n"
        "ds_read_b128 v[20:23], v0 offset:768\n"
        "s_waitcnt lgkmcnt(4)\n"
        "v_mfma_f32_32x32x16_bf16 v[64:79], v[248:251], v[24:27], v[64:79]\n"
        "ds_read_b128 v[24:27], v0 offset:1024\n"
        "v_cmp_gt_i32 vcc, s70, v4\n"
        "v_mfma_f32_32x32x16_bf16 v[64:79], v[252:255], v[28:31], v[64:79]\n"
        "ds_read_b128 v[28:31], v0 offset:1280\n"
        "s_waitcnt lgkmcnt(4)\n"
        "s_and_b64 vcc, vcc, 1\n"
        "s_cbranch_vccz .Lf1_p01_%=\n"
        "s_branch .Lf1_p00_%=\n"
        ".Lf1_tail_%=:\n"
        "v_mfma_f32_32x32x16_bf16 v[64:79], v[232:235], v[8:11], v[64:79]\n"
        "v_mfma_f32_32x32x16_bf16 v[64:79], v[236:239], v[12:15], v[64:79]\n"
        "s_waitcnt lgkmcnt(2)\n"
        "v_mfma_f32_32x32x16_bf16 v[64:79], v[240:243], v[16:19], v[64:79]\n"
        "s_waitcnt vmcnt(1)\n"
        "v_mov_b32 v5, s74\n"
        "buffer_store_dword v5, v3, s[40:43], s73 offen sc1\n"
        "s_add_u32 s74, s74, 1\n"
        "s_add_u32 s72, s75, 98304\n"
        "v_mfma_f32_32x32x16_bf16 v[64:79], v[244:247], v[20:23], v[64:79]\n"
        "s_waitcnt lgkmcnt(0)\n"
        "v_mfma_f32_32x32x16_bf16 v[64:79], v[248:251], v[24:27], v[64:79]\n"
        "v_mfma_f32_32x32x16_bf16 v[64:79], v[252:255], v[28:31], v[64:79]\n"
        "s_waitcnt vmcnt(1)\n"
        "buffer_load_dword v4, v2, s[40:43], s76 offen sc1\n"
        "s_nop 10\n"
        "v_mul_f32 v48, s80, v64\n"
        "v_mul_f32 v49, s80, v65\n"
        "v_mul_f32 v50, s80, v66\n"
        "v_mul_f32 v51, s80, v67\n"
        "v_mul_f32 v52, s80, v72\n"
        "v_mul_f32 v53, s80, v73\n"
        "v_mul_f32 v54, s80, v74\n"
        "v_mul_f32 v55, s80, v75\n"
        "v_exp_f32 v48, v48\n"
        "v_mul_f32 v64, v64, v68\n"
        "v_exp_f32 v49, v49\n"
        "v_mul_f32 v65, v65, v69\n"
        "v_exp_f32 v50, v50\n"
        "v_mul_f32 v66, v66, v70\n"
        "v_exp_f32 v51, v51\n"
        "v_mul_f32 v67, v67, v71\n"
        "v_exp_f32 v52, v52\n"
        "v_mul_f32 v72, v72, v76\n"
        "v_exp_f32 v53, v53\n"
        "v_mul_f32 v73, v73, v77\n"
        "v_exp_f32 v54, v54\n"
        "v_mul_f32 v74, v74, v78\n"
        "v_exp_f32 v55, v55\n"
        "v_mul_f32 v75, v75, v79\n"
        "v_add_f32 v48, 1.0, v48\n"
        "v_add_f32 v49, 1.0, v49\n"
        "v_rcp_f32 v48, v48\n"
        "v_add_f32 v50, 1.0, v50\n"
        "v_rcp_f32 v49, v49\n"
        "v_add_f32 v51, 1.0, v51\n"
        "v_rcp_f32 v50, v50\n"
        "v_add_f32 v52, 1.0, v52\n"
        "v_rcp_f32 v51, v51\n"
        "v_add_f32 v53, 1.0, v53\n"
        "v_rcp_f32 v52, v52\n"
        "v_add_f32 v54, 1.0, v54\n"
        "v_rcp_f32 v53, v53\n"
        "v_add_f32 v55, 1.0, v55\n"
        "v_rcp_f32 v54, v54\n"
        "v_rcp_f32 v55, v55\n"
        "v_mul_f32 v64, v64, v48\n"
        "v_mul_f32 v65, v65, v49\n"
        "v_mul_f32 v66, v66, v50\n"
        "v_mul_f32 v67, v67, v51\n"
        "v_mul_f32 v72, v72, v52\n"
        "v_mul_f32 v73, v73, v53\n"
        "v_mul_f32 v74, v74, v54\n"
        "v_mul_f32 v75, v75, v55\n"
        "v_cvt_pk_bf16_f32 v56, v64, v65\n"
        "v_cvt_pk_bf16_f32 v57, v66, v67\n"
        "v_cvt_pk_bf16_f32 v58, v72, v73\n"
        "v_cvt_pk_bf16_f32 v59, v74, v75\n"
        "buffer_store_dwordx4 v[56:59], v1, s[40:43], s72 offen sc1\n"
        "s_waitcnt vmcnt(1)\n"
        "v_cmp_gt_i32 vcc, s70, v4\n"
        "s_nop 0\n"
        "s_and_b64 vcc, vcc, 1\n"
        "s_cbranch_vccnz .Lf1_late_%=\n"
        "s_add_u32 m0, s77, s61\n"
        "s_lshr_b32 s65, s69, 1\n"
        "s_bitcmp1_b32 s54, s65\n"
        "s_cselect_b32 s65, 8192, 0\n"
        "s_lshl_b32 s71, s69, 20\n"
        "s_add_u32 s71, s71, s65\n"
        "s_add_u32 s71, s71, s58\n"
        "buffer_load_dwordx4 v1, s[40:43], s71 offen offset:0 sc1 lds\n"
        "buffer_load_dwordx4 v1, s[40:43], s71 offen offset:1024 sc1 lds\n"
        "buffer_load_dwordx4 v1, s[40:43], s71 offen offset:2048 sc1 lds\n"
        "buffer_load_dwordx4 v1, s[40:43], s71 offen offset:3072 sc1 lds\n"
        "s_add_u32 m0, m0, 4096\n"
        "s_add_u32 s71, s71, 4096\n"
        "s_mul_i32 s65, s48, -15360\n"
        "buffer_load_dwordx4 v1, s[40:43], s71 offen offset:0 sc1 lds\n"
        "buffer_load_dwordx4 v1, s[40:43], s71 offen offset:1024 sc1 lds\n"
        "buffer_load_dwordx4 v1, s[40:43], s71 offen offset:2048 sc1 lds\n"
        "buffer_load_dwordx4 v1, s[40:43], s71 offen offset:3072 sc1 lds\n"
        "s_add_u32 m0, m0, 4096\n"
        "s_add_u32 s71, s71, s65\n"
        "s_add_u32 s71, s71, 61440\n"
        "buffer_load_dwordx4 v1, s[40:43], s71 offen offset:0 sc1 lds\n"
        "s_add_u32 s69, s69, 1\n"
        "s_add_u32 s77, s77, 37376\n"
        "s_cmp_eq_u32 s77, 112128\n"
        "s_cselect_b32 s77, 0, s77\n"
        "s_mov_b32 s70, 2147483647\n"
        "s_waitcnt vmcnt(9)\n"
        "v_mov_b32 v5, s74\n"
        "buffer_store_dword v5, v3, s[40:43], s73 offen sc1\n"
        "s_waitcnt vmcnt(6)\n"
        "s_branch .Lf1_go_%=\n"
        ".Lf1_late_%=:\n"
        "s_waitcnt vmcnt(0)\n"
        "v_mov_b32 v5, s74\n"
        "buffer_store_dword v5, v3, s[40:43], s73 offen sc1\n"
        "s_cmp_eq_u32 s68, 63\n"
        "s_cbranch_scc0 .Lf1_wait_%=\n"
        "s_endpgm\n"
        ".Lf1_wait_%=:\n"
        "s_mov_b32 s64, 0\n"
        ".Lf1_in_top_%=:\n"
        "buffer_load_dword v4, v2, s[40:43], s76 offen sc1\n"
        "s_waitcnt vmcnt(0)\n"
        "v_cmp_gt_i32 vcc, s69, v4\n"
        "s_nop 0\n"
        "s_and_b64 vcc, vcc, 1\n"
        "s_cbranch_vccz .Lf1_in_ok_%=\n"
        "s_add_u32 s64, s64, 1\n"
        "s_and_b32 s65, s64, 255\n"
        "s_cbranch_scc1 .Lf1_in_go_%=\n"
        "s_mov_b32 s65, 40960\n"
        "s_cmp_lt_u32 s64, 1048576\n"
        "s_cbranch_scc1 .Lf1_in_chk_%=\n"
        "v_mov_b32 v4, 1\n"
        "buffer_store_dword v4, off, s[40:43], s65 sc1\n"
        "s_waitcnt vmcnt(0)\n"
        ".Lf1_in_chk_%=:\n"
        "buffer_load_dword v4, off, s[40:43], s65 sc1\n"
        "s_waitcnt vmcnt(0)\n"
        "v_readfirstlane_b32 s65, v4\n"
        "s_nop 0\n"
        "s_cmp_lg_u32 s65, 1\n"
        "s_cbranch_scc1 .Lf1_in_go_%=\n"
        "s_endpgm\n"
        ".Lf1_in_go_%=:\n"
        "s_sleep 1\n"
        "s_branch .Lf1_in_top_%=\n"
        ".Lf1_in_ok_%=:\n"
        "s_add_u32 m0, s77, s61\n"
        "s_lshr_b32 s65, s69, 1\n"
        "s_bitcmp1_b32 s54, s65\n"
        "s_cselect_b32 s65, 8192, 0\n"
        "s_lshl_b32 s71, s69, 20\n"
        "s_add_u32 s71, s71, s65\n"
        "s_add_u32 s71, s71, s58\n"
        "buffer_load_dwordx4 v1, s[40:43], s71 offen offset:0 sc1 lds\n"
        "buffer_load_dwordx4 v1, s[40:43], s71 offen offset:1024 sc1 lds\n"
        "buffer_load_dwordx4 v1, s[40:43], s71 offen offset:2048 sc1 lds\n"
        "buffer_load_dwordx4 v1, s[40:43], s71 offen offset:3072 sc1 lds\n"
        "s_add_u32 m0, m0, 4096\n"
        "s_add_u32 s71, s71, 4096\n"
        "s_mul_i32 s65, s48, -15360\n"
        "buffer_load_dwordx4 v1, s[40:43], s71 offen offset:0 sc1 lds\n"
        "buffer_load_dwordx4 v1, s[40:43], s71 offen offset:1024 sc1 lds\n"
        "buffer_load_dwordx4 v1, s[40:43], s71 offen offset:2048 sc1 lds\n"
        "buffer_load_dwordx4 v1, s[40:43], s71 offen offset:3072 sc1 lds\n"
        "s_add_u32 m0, m0, 4096\n"
        "s_add_u32 s71, s71, s65\n"
        "s_add_u32 s71, s71, 61440\n"
        "buffer_load_dwordx4 v1, s[40:43], s71 offen offset:0 sc1 lds\n"
        "s_add_u32 s69, s69, 1\n"
        "s_add_u32 s77, s77, 37376\n"
        "s_cmp_eq_u32 s77, 112128\n"
        "s_cselect_b32 s77, 0, s77\n"
        "s_mov_b32 s70, 2147483647\n"
        "s_waitcnt vmcnt(5)\n"
        ".Lf1_go_%=:\n"
        "s_mov_b32 s72, 241172480\n"
        "s_mov_b32 s73, 241172480\n"
        "s_cmp_lt_u32 s69, 64\n"
        "s_cselect_b32 s70, s69, 2147483647\n"
        "v_add_u32 v0, s78, v6\n"
        "s_barrier\n"
        "ds_read_b128 v[8:11], v0 offset:0\n"
        "ds_read_b128 v[12:15], v0 offset:256\n"
        "ds_read_b128 v[16:19], v0 offset:512\n"
        "ds_read_b128 v[20:23], v0 offset:768\n"
        "ds_read_b128 v[24:27], v0 offset:1024\n"
        "ds_read_b128 v[28:31], v0 offset:1280\n"
        "v_cmp_gt_i32 vcc, s70, v4\n"
        "s_nop 0\n"
        "s_and_b64 vcc, vcc, 1\n"
        "s_waitcnt lgkmcnt(4)\n"
        "s_cbranch_vccz .Lf1_p01_%=\n"
        "s_branch .Lf1_p00_%=\n"
        :: "s"(p.ws), "s"(p.w1), "s"(p.w3), "s"(p.w3), "s"(pair(w, delay)), "s"(pair(x4, pr)),
           "s"(pair(65 * PAGE + spread(B_XN1) + w * 16384, 65 * PAGE + spread(kap * 73728 + u * 1024))), "s"(pair(flag(F_F1 + 24 * kap + u), w * 9344)),
           "v"(pair(((lane & 31) >> 3) * 9344 + (lane >> 5) * 128 + (lane & 7) * 16, lane * 16)), "v"(pair(lane == 0 ? flag(F_R3 + 24 * kap + u) : OOB, lane < 3 ? lane * 32768 : OOB)), "v"(pair(0, 0)),
           "v"(pair(vo[0], vo[1])), "v"(pair(vo[2], tr))
        : "v0", "v1", "v2", "v3", "v4", "v5", "v6", "v7", "v8", "v9", "v10", "v11", "v12", "v13", "v14", "v15", "v16", "v17", "v18", "v19", "v20", "v21", "v22", "v23", "v24", "v25", "v26", "v27", "v28", "v29", "v30", "v31", "v32", "v33", "v34", "v35", "v36", "v37", "v38", "v39", "v40", "v41", "v42", "v43", "v44", "v45", "v46", "v47", "v48", "v49", "v50", "v51", "v52", "v53", "v54", "v55", "v56", "v57", "v58", "v59", "v60", "v61", "v62", "v63", "v64", "v65", "v66", "v67", "v68", "v69", "v70", "v71", "v72", "v73", "v74", "v75", "v76", "v77", "v78", "v79", "v255", "a255", "s30", "s31", "s32", "s33", "s34", "s35", "s36", "s37", "s38", "s39", "s40", "s41", "s42", "s43", "s44", "s45", "s46", "s47", "s48", "s49", "s50", "s51", "s52", "s53", "s54", "s55", "s56", "s57", "s58", "s59", "s60", "s61", "s62", "s63", "s64", "s65", "s66", "s67", "s68", "s69", "s70", "s71", "s72", "s73", "s74", "s75", "s76", "s77", "s78", "s79", "s80", "s81", "s82", "s83", "s84", "s85", "s86", "s87", "s88", "s89", "s90", "s91", "s92", "s93", "s94", "s95", "s96", "s97", "s98", "s99", "vcc", "scc", "memory");
    __builtin_unreachable();
}
__device__ __forceinline__ unsigned part_off(unsigned a, unsigned kap, unsigned chunk) {
    const unsigned b = (a & 1) * 4 + kap;
    return (a >> 1) * (9 * 16384) + (chunk < 64 ? b * 16384 + chunk * 128 : 8 * 16384 + b * 1024 + (chunk - 64) * 128);
}
template <bool OP>
__device__ __forceinline__ void role_k(const Params& p, int kap, int nu, int x4, int pr, int delay) {
    GEMM_HEAD;
    unsigned vo[3];
#pragma unroll
    for (int n = 0; n < 3; ++n) {
        const unsigned ps = (64 * n + lane) / 6, gg = (ps >> 2) & 3;
        const unsigned row = OP ? 1024 * kap + ps : 1152 * kap + 48 * (gg >> 1) + 8 * (gg & 1) + 4 * (ps >> 4) + (ps & 3);
        vo[n] = row * 1152 + (192 * nu + 48 * w) * 2 + ((64 * n + lane) % 6) * 16 - 1024 * n;
    }
    const unsigned tr = w * 40960 + (4 * g + q) * 96;
    const unsigned c0 = 24 * nu + 6 * w;
    const unsigned s01 = part_off(i >> 3, kap, c0 + g) + (i & 7) * 16, s2 = part_off(2 * (g & 1) + (i >> 3), kap, c0 + 4 + (g >> 1)) + (i & 7) * 16;
    const unsigned fl = nu * 16 + kap * 4 + w, fo4 = lane < 4 ? lane * 16384 : OOB;
    if constexpr (OP) {
        asm volatile(
        "s_nop 4\n"
        "s_mov_b64 s[40:41], %0\n"
        "s_and_b32 s41, s41, 0xffff\n"
        "s_mov_b32 s42, 236978176\n"
        "s_mov_b32 s43, 0x20000\n"
        "s_mov_b64 s[48:49], %4\n"
        "s_mov_b64 s[56:57], %5\n"
        "s_mov_b64 s[58:59], %6\n"
        "s_mov_b64 s[60:61], %7\n"
        "v_mov_b64 v[0:1], %8\n"
        "v_mov_b64 v[2:3], %9\n"
        "v_mov_b64 v[4:5], %10\n"
        "v_mov_b64 v[6:7], %11\n"
        "v_mov_b64 v[8:9], %12\n"
        "v_mov_b64 v[10:11], %13\n"
        "s_mov_b32 s47, 0x20000\n"
        "s_mul_i32 s79, s48, 40960\n"
        ".Lop_start_%=:\n"
        "s_sub_u32 s49, s49, 1\n"
        "s_cbranch_scc1 .Lop_start2_%=\n"
        "s_sleep 16\n"
        "s_branch .Lop_start_%=\n"
        ".Lop_start2_%=:\n"
        "s_mov_b64 s[44:45], %1\n"
        "s_and_b32 s45, s45, 0xffff\n"
        "s_mov_b32 s46, 4718592\n"
        "s_add_u32 m0, s79, 0\n"
        "s_mov_b32 s65, 0\n"
        "buffer_load_dwordx4 v8, s[44:47], s65 offen lds\n"
        "buffer_load_dwordx4 v9, s[44:47], s65 offen offset:1024 lds\n"
        "buffer_load_dwordx4 v10, s[44:47], s65 offen offset:2048 lds\n"
        "s_add_u32 m0, s79, 3072\n"
        "s_mov_b32 s65, 36864\n"
        "buffer_load_dwordx4 v8, s[44:47], s65 offen lds\n"
        "buffer_load_dwordx4 v9, s[44:47], s65 offen offset:1024 lds\n"
        "buffer_load_dwordx4 v10, s[44:47], s65 offen offset:2048 lds\n"
        "s_add_u32 m0, s79, 6144\n"
        "s_mov_b32 s65, 73728\n"
        "buffer_load_dwordx4 v8, s[44:47], s65 offen lds\n"
        "buffer_load_dwordx4 v9, s[44:47], s65 offen offset:1024 lds\n"
        "buffer_load_dwordx4 v10, s[44:47], s65 offen offset:2048 lds\n"
        "s_add_u32 m0, s79, 9216\n"
        "s_mov_b32 s65, 110592\n"
        "buffer_load_dwordx4 v8, s[44:47], s65 offen lds\n"
        "buffer_load_dwordx4 v9, s[44:47], s65 offen offset:1024 lds\n"
        "buffer_load_dwordx4 v10, s[44:47], s65 offen offset:2048 lds\n"
        "s_add_u32 m0, s79, 12288\n"
        "s_mov_b32 s65, 147456\n"
        "buffer_load_dwordx4 v8, s[44:47], s65 offen lds\n"
        "buffer_load_dwordx4 v9, s[44:47], s65 offen offset:1024 lds\n"
        "buffer_load_dwordx4 v10, s[44:47], s65 offen offset:2048 lds\n"
        "s_add_u32 m0, s79, 15360\n"
        "s_mov_b32 s65, 184320\n"
        "buffer_load_dwordx4 v8, s[44:47], s65 offen lds\n"
        "buffer_load_dwordx4 v9, s[44:47], s65 offen offset:1024 lds\n"
        "buffer_load_dwordx4 v10, s[44:47], s65 offen offset:2048 lds\n"
        "s_add_u32 m0, s79, 18432\n"
        "s_mov_b32 s65, 221184\n"
        "buffer_load_dwordx4 v8, s[44:47], s65 offen lds\n"
        "buffer_load_dwordx4 v9, s[44:47], s65 offen offset:1024 lds\n"
        "buffer_load_dwordx4 v10, s[44:47], s65 offen offset:2048 lds\n"
        "s_add_u32 m0, s79, 21504\n"
        "s_mov_b32 s65, 258048\n"
        "buffer_load_dwordx4 v8, s[44:47], s65 offen lds\n"
        "buffer_load_dwordx4 v9, s[44:47], s65 offen offset:1024 lds\n"
        "buffer_load_dwordx4 v10, s[44:47], s65 offen offset:2048 lds\n"
        "s_add_u32 m0, s79, 24576\n"
        "s_mov_b32 s65, 294912\n"
        "buffer_load_dwordx4 v8, s[44:47], s65 offen lds\n"
        "buffer_load_dwordx4 v9, s[44:47], s65 offen offset:1024 lds\n"
        "buffer_load_dwordx4 v10, s[44:47], s65 offen offset:2048 lds\n"
        "s_add_u32 m0, s79, 27648\n"
        "s_mov_b32 s65, 331776\n"
        "buffer_load_dwordx4 v8, s[44:47], s65 offen lds\n"
        "buffer_load_dwordx4 v9, s[44:47], s65 offen offset:1024 lds\n"
        "buffer_load_dwordx4 v10, s[44:47], s65 offen offset:2048 lds\n"
        "s_add_u32 m0, s79, 30720\n"
        "s_mov_b32 s65, 368640\n"
        "buffer_load_dwordx4 v8, s[44:47], s65 offen lds\n"
        "buffer_load_dwordx4 v9, s[44:47], s65 offen offset:1024 lds\n"
        "buffer_load_dwordx4 v10, s[44:47], s65 offen offset:2048 lds\n"
        "s_add_u32 m0, s79, 33792\n"
        "s_mov_b32 s65, 405504\n"
        "buffer_load_dwordx4 v8, s[44:47], s65 offen lds\n"
        "buffer_load_dwordx4 v9, s[44:47], s65 offen offset:1024 lds\n"
        "buffer_load_dwordx4 v10, s[44:47], s65 offen offset:2048 lds\n"
        "s_waitcnt vmcnt(33)\n"
        "ds_read_b64_tr_b16 a[0:1], v11 offset:0\n"
        "ds_read_b64_tr_b16 a[2:3], v11 offset:1536\n"
        "ds_read_b64_tr_b16 a[4:5], v11 offset:8\n"
        "ds_read_b64_tr_b16 a[6:7], v11 offset:1544\n"
        "ds_read_b64_tr_b16 a[8:9], v4 offset:0\n"
        "ds_read_b64_tr_b16 a[10:11], v4 offset:1536\n"
        "s_waitcnt vmcnt(30)\n"
        "ds_read_b64_tr_b16 a[12:13], v11 offset:3072\n"
        "ds_read_b64_tr_b16 a[14:15], v11 offset:4608\n"
        "ds_read_b64_tr_b16 a[16:17], v11 offset:3080\n"
        "ds_read_b64_tr_b16 a[18:19], v11 offset:4616\n"
        "ds_read_b64_tr_b16 a[20:21], v4 offset:3072\n"
        "ds_read_b64_tr_b16 a[22:23], v4 offset:4608\n"
        "s_waitcnt lgkmcnt(6)\n"
        "s_add_u32 m0, s79, 0\n"
        "s_mov_b32 s65, 442368\n"
        "buffer_load_dwordx4 v8, s[44:47], s65 offen lds\n"
        "buffer_load_dwordx4 v9, s[44:47], s65 offen offset:1024 lds\n"
        "buffer_load_dwordx4 v10, s[44:47], s65 offen offset:2048 lds\n"
        "s_waitcnt vmcnt(30)\n"
        "ds_read_b64_tr_b16 a[24:25], v11 offset:6144\n"
        "ds_read_b64_tr_b16 a[26:27], v11 offset:7680\n"
        "ds_read_b64_tr_b16 a[28:29], v11 offset:6152\n"
        "ds_read_b64_tr_b16 a[30:31], v11 offset:7688\n"
        "ds_read_b64_tr_b16 a[32:33], v4 offset:6144\n"
        "ds_read_b64_tr_b16 a[34:35], v4 offset:7680\n"
        "s_waitcnt lgkmcnt(6)\n"
        "s_add_u32 m0, s79, 3072\n"
        "s_mov_b32 s65, 479232\n"
        "buffer_load_dwordx4 v8, s[44:47], s65 offen lds\n"
        "buffer_load_dwordx4 v9, s[44:47], s65 offen offset:1024 lds\n"
        "buffer_load_dwordx4 v10, s[44:47], s65 offen offset:2048 lds\n"
        "s_waitcnt vmcnt(30)\n"
        "ds_read_b64_tr_b16 a[36:37], v11 offset:9216\n"
        "ds_read_b64_tr_b16 a[38:39], v11 offset:10752\n"
        "ds_read_b64_tr_b16 a[40:41], v11 offset:9224\n"
        "ds_read_b64_tr_b16 a[42:43], v11 offset:10760\n"
        "ds_read_b64_tr_b16 a[44:45], v4 offset:9216\n"
        "ds_read_b64_tr_b16 a[46:47], v4 offset:10752\n"
        "s_waitcnt lgkmcnt(6)\n"
        "s_add_u32 m0, s79, 6144\n"
        "s_mov_b32 s65, 516096\n"
        "buffer_load_dwordx4 v8, s[44:47], s65 offen lds\n"
        "buffer_load_dwordx4 v9, s[44:47], s65 offen offset:1024 lds\n"
        "buffer_load_dwordx4 v10, s[44:47], s65 offen offset:2048 lds\n"
        "s_waitcnt vmcnt(30)\n"
        "ds_read_b64_tr_b16 a[48:49], v11 offset:12288\n"
        "ds_read_b64_tr_b16 a[50:51], v11 offset:13824\n"
        "ds_read_b64_tr_b16 a[52:53], v11 offset:12296\n"
        "ds_read_b64_tr_b16 a[54:55], v11 offset:13832\n"
        "ds_read_b64_tr_b16 a[56:57], v4 offset:12288\n"
        "ds_read_b64_tr_b16 a[58:59], v4 offset:13824\n"
        "s_waitcnt lgkmcnt(6)\n"
        "s_add_u32 m0, s79, 9216\n"
        "s_mov_b32 s65, 552960\n"
        "buffer_load_dwordx4 v8, s[44:47], s65 offen lds\n"
        "buffer_load_dwordx4 v9, s[44:47], s65 offen offset:1024 lds\n"
        "buffer_load_dwordx4 v10, s[44:47], s65 offen offset:2048 lds\n"
        "s_waitcnt vmcnt(30)\n"
        "ds_read_b64_tr_b16 a[60:61], v11 offset:15360\n"
        "ds_read_b64_tr_b16 a[62:63], v11 offset:16896\n"
        "ds_read_b64_tr_b16 a[64:65], v11 offset:15368\n"
        "ds_read_b64_tr_b16 a[66:67], v11 offset:16904\n"
        "ds_read_b64_tr_b16 a[68:69], v4 offset:15360\n"
        "ds_read_b64_tr_b16 a[70:71], v4 offset:16896\n"
        "s_waitcnt lgkmcnt(6)\n"
        "s_add_u32 m0, s79, 12288\n"
        "s_mov_b32 s65, 589824\n"
        "buffer_load_dwordx4 v8, s[44:47], s65 offen lds\n"
        "buffer_load_dwordx4 v9, s[44:47], s65 offen offset:1024 lds\n"
        "buffer_load_dwordx4 v10, s[44:47], s65 offen offset:2048 lds\n"
        "s_waitcnt vmcnt(30)\n"
        "ds_read_b64_tr_b16 a[72:73], v11 offset:18432\n"
        "ds_read_b64_tr_b16 a[74:75], v11 offset:19968\n"
        "ds_read_b64_tr_b16 a[76:77], v11 offset:18440\n"
        "ds_read_b64_tr_b16 a[78:79], v11 offset:19976\n"
        "ds_read_b64_tr_b16 a[80:81], v4 offset:18432\n"
        "ds_read_b64_tr_b16 a[82:83], v4 offset:19968\n"
        "s_waitcnt lgkmcnt(6)\n"
        "s_add_u32 m0, s79, 15360\n"
        "s_mov_b32 s65, 626688\n"
        "buffer_load_dwordx4 v8, s[44:47], s65 offen lds\n"
        "buffer_load_dwordx4 v9, s[44:47], s65 offen offset:1024 lds\n"
        "buffer_load_dwordx4 v10, s[44:47], s65 offen offset:2048 lds\n"
        "s_waitcnt vmcnt(30)\n"
        "ds_read_b64_tr_b16 a[84:85], v11 offset:21504\n"
        "ds_read_b64_tr_b16 a[86:87], v11 offset:23040\n"
        "ds_read_b64_tr_b16 a[88:89], v11 offset:21512\n"
        "ds_read_b64_tr_b16 a[90:91], v11 offset:23048\n"
        "ds_read_b64_tr_b16 a[92:93], v4 offset:21504\n"
        "ds_read_b64_tr_b16 a[94:95], v4 offset:23040\n"
        "s_waitcnt lgkmcnt(6)\n"
        "s_add_u32 m0, s79, 18432\n"
        "s_mov_b32 s65, 663552\n"
        "buffer_load_dwordx4 v8, s[44:47], s65 offen lds\n"
        "buffer_load_dwordx4 v9, s[44:47], s65 offen offset:1024 lds\n"
        "buffer_load_dwordx4 v10, s[44:47], s65 offen offset:2048 lds\n"
        "s_waitcnt vmcnt(30)\n"
        "ds_read_b64_tr_b16 a[96:97], v11 offset:24576\n"
        "ds_read_b64_tr_b16 a[98:99], v11 offset:26112\n"
        "ds_read_b64_tr_b16 a[100:101], v11 offset:24584\n"
        "ds_read_b64_tr_b16 a[102:103], v11 offset:26120\n"
        "ds_read_b64_tr_b16 a[104:105], v4 offset:24576\n"
        "ds_read_b64_tr_b16 a[106:107], v4 offset:26112\n"
        "s_waitcnt lgkmcnt(6)\n"
        "s_add_u32 m0, s79, 21504\n"
        "s_mov_b32 s65, 700416\n"
        "buffer_load_dwordx4 v8, s[44:47], s65 offen lds\n"
        "buffer_load_dwordx4 v9, s[44:47], s65 offen offset:1024 lds\n"
        "buffer_load_dwordx4 v10, s[44:47], s65 offen offset:2048 lds\n"
        "s_waitcnt vmcnt(30)\n"
        "ds_read_b64_tr_b16 a[108:109], v11 offset:27648\n"
        "ds_read_b64_tr_b16 a[110:111], v11 offset:29184\n"
        "ds_read_b64_tr_b16 a[112:113], v11 offset:27656\n"
        "ds_read_b64_tr_b16 a[114:115], v11 offset:29192\n"
        "ds_read_b64_tr_b16 a[116:117], v4 offset:27648\n"
        "ds_read_b64_tr_b16 a[118:119], v4 offset:29184\n"
        "s_waitcnt lgkmcnt(6)\n"
        "s_add_u32 m0, s79, 24576\n"
        "s_mov_b32 s65, 737280\n"
        "buffer_load_dwordx4 v8, s[44:47], s65 offen lds\n"
        "buffer_load_dwordx4 v9, s[44:47], s65 offen offset:1024 lds\n"
        "buffer_load_dwordx4 v10, s[44:47], s65 offen offset:2048 lds\n"
        "s_waitcnt vmcnt(30)\n"
        "ds_read_b64_tr_b16 a[120:121], v11 offset:30720\n"
        "ds_read_b64_tr_b16 a[122:123], v11 offset:32256\n"
        "ds_read_b64_tr_b16 a[124:125], v11 offset:30728\n"
        "ds_read_b64_tr_b16 a[126:127], v11 offset:32264\n"
        "ds_read_b64_tr_b16 a[128:129], v4 offset:30720\n"
        "ds_read_b64_tr_b16 a[130:131], v4 offset:32256\n"
        "s_waitcnt lgkmcnt(6)\n"
        "s_add_u32 m0, s79, 27648\n"
        "s_mov_b32 s65, 774144\n"
        "buffer_load_dwordx4 v8, s[44:47], s65 offen lds\n"
        "buffer_load_dwordx4 v9, s[44:47], s65 offen offset:1024 lds\n"
        "buffer_load_dwordx4 v10, s[44:47], s65 offen offset:2048 lds\n"
        "s_waitcnt vmcnt(30)\n"
        "ds_read_b64_tr_b16 a[132:133], v11 offset:33792\n"
        "ds_read_b64_tr_b16 a[134:135], v11 offset:35328\n"
        "ds_read_b64_tr_b16 a[136:137], v11 offset:33800\n"
        "ds_read_b64_tr_b16 a[138:139], v11 offset:35336\n"
        "ds_read_b64_tr_b16 a[140:141], v4 offset:33792\n"
        "ds_read_b64_tr_b16 a[142:143], v4 offset:35328\n"
        "s_waitcnt lgkmcnt(6)\n"
        "s_add_u32 m0, s79, 30720\n"
        "s_mov_b32 s65, 811008\n"
        "buffer_load_dwordx4 v8, s[44:47], s65 offen lds\n"
        "buffer_load_dwordx4 v9, s[44:47], s65 offen offset:1024 lds\n"
        "buffer_load_dwordx4 v10, s[44:47], s65 offen offset:2048 lds\n"
        "s_waitcnt vmcnt(30)\n"
        "ds_read_b64_tr_b16 a[144:145], v11 offset:0\n"
        "ds_read_b64_tr_b16 a[146:147], v11 offset:1536\n"
        "ds_read_b64_tr_b16 a[148:149], v11 offset:8\n"
        "ds_read_b64_tr_b16 a[150:151], v11 offset:1544\n"
        "ds_read_b64_tr_b16 a[152:153], v4 offset:0\n"
        "ds_read_b64_tr_b16 a[154:155], v4 offset:1536\n"
        "s_waitcnt lgkmcnt(6)\n"
        "s_add_u32 m0, s79, 33792\n"
        "s_mov_b32 s65, 847872\n"
        "buffer_load_dwordx4 v8, s[44:47], s65 offen lds\n"
        "buffer_load_dwordx4 v9, s[44:47], s65 offen offset:1024 lds\n"
        "buffer_load_dwordx4 v10, s[44:47], s65 offen offset:2048 lds\n"
        "s_waitcnt vmcnt(30)\n"
        "ds_read_b64_tr_b16 a[156:157], v11 offset:3072\n"
        "ds_read_b64_tr_b16 a[158:159], v11 offset:4608\n"
        "ds_read_b64_tr_b16 a[160:161], v11 offset:3080\n"
        "ds_read_b64_tr_b16 a[162:163], v11 offset:4616\n"
        "ds_read_b64_tr_b16 a[164:165], v4 offset:3072\n"
        "ds_read_b64_tr_b16 a[166:167], v4 offset:4608\n"
        "s_waitcnt lgkmcnt(6)\n"
        "s_add_u32 m0, s79, 0\n"
        "s_mov_b32 s65, 884736\n"
        "buffer_load_dwordx4 v8, s[44:47], s65 offen lds\n"
        "buffer_load_dwordx4 v9, s[44:47], s65 offen offset:1024 lds\n"
        "buffer_load_dwordx4 v10, s[44:47], s65 offen offset:2048 lds\n"
        "s_waitcnt vmcnt(30)\n"
        "ds_read_b64_tr_b16 a[168:169], v11 offset:6144\n"
        "ds_read_b64_tr_b16 a[170:171], v11 offset:7680\n"
        "ds_read_b64_tr_b16 a[172:173], v11 offset:6152\n"
        "ds_read_b64_tr_b16 a[174:175], v11 offset:7688\n"
        "ds_read_b64_tr_b16 a[176:177], v4 offset:6144\n"
        "ds_read_b64_tr_b16 a[178:179], v4 offset:7680\n"
        "s_waitcnt lgkmcnt(6)\n"
        "s_add_u32 m0, s79, 3072\n"
        "s_mov_b32 s65, 921600\n"
        "buffer_load_dwordx4 v8, s[44:47], s65 offen lds\n"
        "buffer_load_dwordx4 v9, s[44:47], s65 offen offset:1024 lds\n"
        "buffer_load_dwordx4 v10, s[44:47], s65 offen offset:2048 lds\n"
        "s_waitcnt vmcnt(30)\n"
        "ds_read_b64_tr_b16 a[180:181], v11 offset:9216\n"
        "ds_read_b64_tr_b16 a[182:183], v11 offset:10752\n"
        "ds_read_b64_tr_b16 a[184:185], v11 offset:9224\n"
        "ds_read_b64_tr_b16 a[186:187], v11 offset:10760\n"
        "ds_read_b64_tr_b16 a[188:189], v4 offset:9216\n"
        "ds_read_b64_tr_b16 a[190:191], v4 offset:10752\n"
        "s_waitcnt lgkmcnt(6)\n"
        "s_add_u32 m0, s79, 6144\n"
        "s_mov_b32 s65, 958464\n"
        "buffer_load_dwordx4 v8, s[44:47], s65 offen lds\n"
        "buffer_load_dwordx4 v9, s[44:47], s65 offen offset:1024 lds\n"
        "buffer_load_dwordx4 v10, s[44:47], s65 offen offset:2048 lds\n"
        "s_waitcnt vmcnt(30)\n"
        "ds_read_b64_tr_b16 a[192:193], v11 offset:12288\n"
        "ds_read_b64_tr_b16 a[194:195], v11 offset:13824\n"
        "ds_read_b64_tr_b16 a[196:197], v11 offset:12296\n"
        "ds_read_b64_tr_b16 a[198:199], v11 offset:13832\n"
        "ds_read_b64_tr_b16 a[200:201], v4 offset:12288\n"
        "ds_read_b64_tr_b16 a[202:203], v4 offset:13824\n"
        "s_waitcnt lgkmcnt(6)\n"
        "s_add_u32 m0, s79, 9216\n"
        "s_mov_b32 s65, 995328\n"
        "buffer_load_dwordx4 v8, s[44:47], s65 offen lds\n"
        "buffer_load_dwordx4 v9, s[44:47], s65 offen offset:1024 lds\n"
        "buffer_load_dwordx4 v10, s[44:47], s65 offen offset:2048 lds\n"
        "s_waitcnt vmcnt(30)\n"
        "ds_read_b64_tr_b16 a[204:205], v11 offset:15360\n"
        "ds_read_b64_tr_b16 a[206:207], v11 offset:16896\n"
        "ds_read_b64_tr_b16 a[208:209], v11 offset:15368\n"
        "ds_read_b64_tr_b16 a[210:211], v11 offset:16904\n"
        "ds_read_b64_tr_b16 a[212:213], v4 offset:15360\n"
        "ds_read_b64_tr_b16 a[214:215], v4 offset:16896\n"
        "s_waitcnt lgkmcnt(6)\n"
        "s_add_u32 m0, s79, 12288\n"
        "s_mov_b32 s65, 1032192\n"
        "buffer_load_dwordx4 v8, s[44:47], s65 offen lds\n"
        "buffer_load_dwordx4 v9, s[44:47], s65 offen offset:1024 lds\n"
        "buffer_load_dwordx4 v10, s[44:47], s65 offen offset:2048 lds\n"
        "s_waitcnt vmcnt(30)\n"
        "ds_read_b64_tr_b16 a[216:217], v11 offset:18432\n"
        "ds_read_b64_tr_b16 a[218:219], v11 offset:19968\n"
        "ds_read_b64_tr_b16 a[220:221], v11 offset:18440\n"
        "ds_read_b64_tr_b16 a[222:223], v11 offset:19976\n"
        "ds_read_b64_tr_b16 a[224:225], v4 offset:18432\n"
        "ds_read_b64_tr_b16 a[226:227], v4 offset:19968\n"
        "s_waitcnt lgkmcnt(6)\n"
        "s_add_u32 m0, s79, 15360\n"
        "s_mov_b32 s65, 1069056\n"
        "buffer_load_dwordx4 v8, s[44:47], s65 offen lds\n"
        "buffer_load_dwordx4 v9, s[44:47], s65 offen offset:1024 lds\n"
        "buffer_load_dwordx4 v10, s[44:47], s65 offen offset:2048 lds\n"
        "s_waitcnt vmcnt(30)\n"
        "ds_read_b64_tr_b16 a[228:229], v11 offset:21504\n"
        "ds_read_b64_tr_b16 a[230:231], v11 offset:23040\n"
        "ds_read_b64_tr_b16 a[232:233], v11 offset:21512\n"
        "ds_read_b64_tr_b16 a[234:235], v11 offset:23048\n"
        "ds_read_b64_tr_b16 a[236:237], v4 offset:21504\n"
        "ds_read_b64_tr_b16 a[238:239], v4 offset:23040\n"
        "s_waitcnt lgkmcnt(6)\n"
        "s_add_u32 m0, s79, 18432\n"
        "s_mov_b32 s65, 1105920\n"
        "buffer_load_dwordx4 v8, s[44:47], s65 offen lds\n"
        "buffer_load_dwordx4 v9, s[44:47], s65 offen offset:1024 lds\n"
        "buffer_load_dwordx4 v10, s[44:47], s65 offen offset:2048 lds\n"
        "s_waitcnt vmcnt(30)\n"
        "ds_read_b64_tr_b16 a[240:241], v11 offset:24576\n"
        "ds_read_b64_tr_b16 a[242:243], v11 offset:26112\n"
        "ds_read_b64_tr_b16 a[244:245], v11 offset:24584\n"
        "ds_read_b64_tr_b16 a[246:247], v11 offset:26120\n"
        "ds_read_b64_tr_b16 a[248:249], v4 offset:24576\n"
        "ds_read_b64_tr_b16 a[250:251], v4 offset:26112\n"
        "s_waitcnt lgkmcnt(6)\n"
        "s_add_u32 m0, s79, 21504\n"
        "s_mov_b32 s65, 1142784\n"
        "buffer_load_dwordx4 v8, s[44:47], s65 offen lds\n"
        "buffer_load_dwordx4 v9, s[44:47], s65 offen offset:1024 lds\n"
        "buffer_load_dwordx4 v10, s[44:47], s65 offen offset:2048 lds\n"
        "s_waitcnt vmcnt(30)\n"
        "ds_read_b64_tr_b16 a[252:253], v11 offset:27648\n"
        "ds_read_b64_tr_b16 a[254:255], v11 offset:29184\n"
        "ds_read_b64_tr_b16 v[80:81], v11 offset:27656\n"
        "ds_read_b64_tr_b16 v[82:83], v11 offset:29192\n"
        "ds_read_b64_tr_b16 v[84:85], v4 offset:27648\n"
        "ds_read_b64_tr_b16 v[86:87], v4 offset:29184\n"
        "s_waitcnt vmcnt(27)\n"
        "ds_read_b64_tr_b16 v[88:89], v11 offset:30720\n"
        "ds_read_b64_tr_b16 v[90:91], v11 offset:32256\n"
        "ds_read_b64_tr_b16 v[92:93], v11 offset:30728\n"
        "ds_read_b64_tr_b16 v[94:95], v11 offset:32264\n"
        "ds_read_b64_tr_b16 v[96:97], v4 offset:30720\n"
        "ds_read_b64_tr_b16 v[98:99], v4 offset:32256\n"
        "s_waitcnt vmcnt(24)\n"
        "ds_read_b64_tr_b16 v[100:101], v11 offset:33792\n"
        "ds_read_b64_tr_b16 v[102:103], v11 offset:35328\n"
        "ds_read_b64_tr_b16 v[104:105], v11 offset:33800\n"
        "ds_read_b64_tr_b16 v[106:107], v11 offset:35336\n"
        "ds_read_b64_tr_b16 v[108:109], v4 offset:33792\n"
        "ds_read_b64_tr_b16 v[110:111], v4 offset:35328\n"
        "s_waitcnt vmcnt(21)\n"
        "ds_read_b64_tr_b16 v[112:113], v11 offset:0\n"
        "ds_read_b64_tr_b16 v[114:115], v11 offset:1536\n"
        "ds_read_b64_tr_b16 v[116:117], v11 offset:8\n"
        "ds_read_b64_tr_b16 v[118:119], v11 offset:1544\n"
        "ds_read_b64_tr_b16 v[120:121], v4 offset:0\n"
        "ds_read_b64_tr_b16 v[122:123], v4 offset:1536\n"
        "s_waitcnt vmcnt(18)\n"
        "ds_read_b64_tr_b16 v[124:125], v11 offset:3072\n"
        "ds_read_b64_tr_b16 v[126:127], v11 offset:4608\n"
        "ds_read_b64_tr_b16 v[128:129], v11 offset:3080\n"
        "ds_read_b64_tr_b16 v[130:131], v11 offset:4616\n"
        "ds_read_b64_tr_b16 v[132:133], v4 offset:3072\n"
        "ds_read_b64_tr_b16 v[134:135], v4 offset:4608\n"
        "s_waitcnt vmcnt(15)\n"
        "ds_read_b64_tr_b16 v[136:137], v11 offset:6144\n"
        "ds_read_b64_tr_b16 v[138:139], v11 offset:7680\n"
        "ds_read_b64_tr_b16 v[140:141], v11 offset:6152\n"
        "ds_read_b64_tr_b16 v[142:143], v11 offset:7688\n"
        "ds_read_b64_tr_b16 v[144:145], v4 offset:6144\n"
        "ds_read_b64_tr_b16 v[146:147], v4 offset:7680\n"
        "s_waitcnt vmcnt(12)\n"
        "ds_read_b64_tr_b16 v[148:149], v11 offset:9216\n"
        "ds_read_b64_tr_b16 v[150:151], v11 offset:10752\n"
        "ds_read_b64_tr_b16 v[152:153], v11 offset:9224\n"
        "ds_read_b64_tr_b16 v[154:155], v11 offset:10760\n"
        "ds_read_b64_tr_b16 v[156:157], v4 offset:9216\n"
        "ds_read_b64_tr_b16 v[158:159], v4 offset:10752\n"
        "s_waitcnt vmcnt(9)\n"
        "ds_read_b64_tr_b16 v[160:161], v11 offset:12288\n"
        "ds_read_b64_tr_b16 v[162:163], v11 offset:13824\n"
        "ds_read_b64_tr_b16 v[164:165], v11 offset:12296\n"
        "ds_read_b64_tr_b16 v[166:167], v11 offset:13832\n"
        "ds_read_b64_tr_b16 v[168:169], v4 offset:12288\n"
        "ds_read_b64_tr_b16 v[170:171], v4 offset:13824\n"
        "s_waitcnt vmcnt(6)\n"
        "ds_read_b64_tr_b16 v[172:173], v11 offset:15360\n"
        "ds_read_b64_tr_b16 v[174:175], v11 offset:16896\n"
        "ds_read_b64_tr_b16 v[176:177], v11 offset:15368\n"
        "ds_read_b64_tr_b16 v[178:179], v11 offset:16904\n"
        "ds_read_b64_tr_b16 v[180:181], v4 offset:15360\n"
        "ds_read_b64_tr_b16 v[182:183], v4 offset:16896\n"
        "s_waitcnt vmcnt(3)\n"
        "ds_read_b64_tr_b16 v[184:185], v11 offset:18432\n"
        "ds_read_b64_tr_b16 v[186:187], v11 offset:19968\n"
        "ds_read_b64_tr_b16 v[188:189], v11 offset:18440\n"
        "ds_read_b64_tr_b16 v[190:191], v11 offset:19976\n"
        "ds_read_b64_tr_b16 v[192:193], v4 offset:18432\n"
        "ds_read_b64_tr_b16 v[194:195], v4 offset:19968\n"
        "s_waitcnt vmcnt(0)\n"
        "ds_read_b64_tr_b16 v[196:197], v11 offset:21504\n"
        "ds_read_b64_tr_b16 v[198:199], v11 offset:23040\n"
        "ds_read_b64_tr_b16 v[200:201], v11 offset:21512\n"
        "ds_read_b64_tr_b16 v[202:203], v11 offset:23048\n"
        "ds_read_b64_tr_b16 v[204:205], v4 offset:21504\n"
        "ds_read_b64_tr_b16 v[206:207], v4 offset:23040\n"
        "s_waitcnt lgkmcnt(0)\n"
        "v_lshrrev_b32 v12, 4, v1\n"
        "s_lshl_b32 s66, s57, 7\n"
        "s_mov_b32 s64, 0\n"
        ".Lop_pair_top_%=:\n"
        "s_mov_b64 exec, 1\n"
        "buffer_load_dword v13, off, s[40:43], s66 sc1\n"
        "s_mov_b64 exec, -1\n"
        "s_waitcnt vmcnt(0)\n"
        "v_cmp_gt_i32 vcc, 0, v13\n"
        "s_nop 0\n"
        "s_and_b64 vcc, vcc, 1\n"
        "s_cbranch_vccz .Lop_pair_ok_%=\n"
        "s_add_u32 s64, s64, 1\n"
        "s_and_b32 s65, s64, 255\n"
        "s_cbranch_scc1 .Lop_pair_go_%=\n"
        "s_mov_b32 s65, 40960\n"
        "s_cmp_lt_u32 s64, 1048576\n"
        "s_cbranch_scc1 .Lop_pair_chk_%=\n"
        "v_mov_b32 v13, 1\n"
        "buffer_store_dword v13, off, s[40:43], s65 sc1\n"
        "s_waitcnt vmcnt(0)\n"
        ".Lop_pair_chk_%=:\n"
        "buffer_load_dword v13, off, s[40:43], s65 sc1\n"
        "s_waitcnt vmcnt(0)\n"
        "v_readfirstlane_b32 s65, v13\n"
        "s_nop 0\n"
        "s_cmp_lg_u32 s65, 1\n"
        "s_cbranch_scc1 .Lop_pair_go_%=\n"
        "s_endpgm\n"
        ".Lop_pair_go_%=:\n"
        "s_sleep 2\n"
        "s_branch .Lop_pair_top_%=\n"
        ".Lop_pair_ok_%=:\n"
        "v_readfirstlane_b32 s50, v13\n"
        "s_nop 0\n"
        "s_xor_b32 s50, s50, s56\n"
        "v_lshlrev_b32 v14, 7, v12\n"
        "s_sub_u32 s66, 0, s50\n"
        "s_mov_b32 s52, 24576\n"
        "s_mov_b32 s64, 0\n"
        ".Lop_pg0_top_%=:\n"
        "buffer_load_dword v13, v14, s[40:43], s52 offen sc1\n"
        "s_waitcnt vmcnt(0)\n"
        "v_cmp_gt_i32 vcc, 0, v13\n"
        "s_cbranch_vccz .Lop_pg0_ok_%=\n"
        "s_add_u32 s64, s64, 1\n"
        "s_and_b32 s65, s64, 255\n"
        "s_cbranch_scc1 .Lop_pg0_go_%=\n"
        "s_mov_b32 s65, 40960\n"
        "s_cmp_lt_u32 s64, 1048576\n"
        "s_cbranch_scc1 .Lop_pg0_chk_%=\n"
        "v_mov_b32 v13, 1\n"
        "buffer_store_dword v13, off, s[40:43], s65 sc1\n"
        "s_waitcnt vmcnt(0)\n"
        ".Lop_pg0_chk_%=:\n"
        "buffer_load_dword v13, off, s[40:43], s65 sc1\n"
        "s_waitcnt vmcnt(0)\n"
        "v_readfirstlane_b32 s65, v13\n"
        "s_nop 0\n"
        "s_cmp_lg_u32 s65, 1\n"
        "s_cbranch_scc1 .Lop_pg0_go_%=\n"
        "s_endpgm\n"
        ".Lop_pg0_go_%=:\n"
        "s_sleep 2\n"
        "s_branch .Lop_pg0_top_%=\n"
        ".Lop_pg0_ok_%=:\n"
        "v_cmp_eq_u32 s[52:53], 1, v13\n"
        "s_nop 1\n"
        "s_mov_b32 s54, 32768\n"
        "s_mov_b32 s64, 0\n"
        ".Lop_pg1_top_%=:\n"
        "buffer_load_dword v13, v14, s[40:43], s54 offen sc1\n"
        "s_waitcnt vmcnt(0)\n"
        "v_cmp_gt_i32 vcc, 0, v13\n"
        "s_cbranch_vccz .Lop_pg1_ok_%=\n"
        "s_add_u32 s64, s64, 1\n"
        "s_and_b32 s65, s64, 255\n"
        "s_cbranch_scc1 .Lop_pg1_go_%=\n"
        "s_mov_b32 s65, 40960\n"
        "s_cmp_lt_u32 s64, 1048576\n"
        "s_cbranch_scc1 .Lop_pg1_chk_%=\n"
        "v_mov_b32 v13, 1\n"
        "buffer_store_dword v13, off, s[40:43], s65 sc1\n"
        "s_waitcnt vmcnt(0)\n"
        ".Lop_pg1_chk_%=:\n"
        "buffer_load_dword v13, off, s[40:43], s65 sc1\n"
        "s_waitcnt vmcnt(0)\n"
        "v_readfirstlane_b32 s65, v13\n"
        "s_nop 0\n"
        "s_cmp_lg_u32 s65, 1\n"
        "s_cbranch_scc1 .Lop_pg1_go_%=\n"
        "s_endpgm\n"
        ".Lop_pg1_go_%=:\n"
        "s_sleep 2\n"
        "s_branch .Lop_pg1_top_%=\n"
        ".Lop_pg1_ok_%=:\n"
        "v_cmp_eq_u32 s[54:55], 1, v13\n"
        "s_nop 1\n"
        "s_mov_b32 vcc_lo, 0x55555555\n"
        "s_mov_b32 vcc_hi, 0x55555555\n"
        "s_xor_b64 vcc, vcc, s[52:53]\n"
        "s_bcnt1_i32_b64 s65, vcc\n"
        "s_sub_u32 s64, s65, 23\n"
        "s_cmp_lt_u32 s64, 19\n"
        "s_cbranch_scc1 .Lop_rawmap_%=\n"
        "s_cmp_lt_u32 s65, 32\n"
        "s_cselect_b32 s65, 0, -1\n"
        "s_xor_b32 s52, s65, 0x55555555\n"
        "s_xor_b32 s53, s65, 0x55555555\n"
        "s_xor_b32 s54, s65, 0x55555555\n"
        "s_xor_b32 s55, s65, 0xaaaaaaaa\n"
        ".Lop_rawmap_%=:\n"
        "s_xor_b32 s52, s52, s66\n"
        "s_xor_b32 s53, s53, s66\n"
        "s_xor_b32 s54, s54, s66\n"
        "s_xor_b32 s55, s55, s66\n"
        "s_and_b32 s76, s55, 1\n"
        "s_lshl_b32 s76, s76, 13\n"
        "s_add_u32 s60, s60, s76\n"
        "s_barrier\n"
        "s_mov_b32 s68, -1\n"
        "s_mov_b32 s69, 0\n"
        "v_add_u32 v8, 32768, v0\n"
        "s_mov_b32 s72, 241172480\n"
        "s_mov_b32 s73, 241172480\n"
        "s_branch .Lop_wait3_%=\n"
        ".Lop_c01_%=:\n"
        "v_mfma_f32_16x16x32_bf16 v[44:47], a[0:3], v[12:15], 0\n"
        "v_cvt_pk_bf16_f32 v74, v60, v61\n"
        "v_cvt_pk_bf16_f32 v75, v62, v63\n"
        "v_mfma_f32_16x16x32_bf16 v[48:51], a[4:7], v[12:15], 0\n"
        "v_cvt_pk_bf16_f32 v78, v64, v65\n"
        "v_cvt_pk_bf16_f32 v79, v66, v67\n"
        "v_mfma_f32_16x16x32_bf16 v[52:55], a[8:11], v[12:15], 0\n"
        "ds_read_b128 v[12:15], v0 offset:4096\n"
        "s_add_u32 m0, s61, 16384\n"
        "v_mfma_f32_16x16x32_bf16 v[56:59], a[0:3], v[16:19], 0\n"
        "s_add_u32 s71, s71, 32768\n"
        "buffer_load_dwordx4 v1, s[40:43], s71 offen offset:0 sc1 lds\n"
        "v_mfma_f32_16x16x32_bf16 v[60:63], a[4:7], v[16:19], 0\n"
        "buffer_load_dwordx4 v1, s[40:43], s71 offen offset:1024 sc1 lds\n"
        "v_mfma_f32_16x16x32_bf16 v[64:67], a[8:11], v[16:19], 0\n"
        "ds_read_b128 v[16:19], v0 offset:5120\n"
        "s_waitcnt lgkmcnt(2)\n"
        "v_mfma_f32_16x16x32_bf16 v[44:47], a[12:15], v[20:23], v[44:47]\n"
        "buffer_store_dwordx4 v[68:71], v6, s[40:43], s72 offen sc1\n"
        "v_mfma_f32_16x16x32_bf16 v[48:51], a[16:19], v[20:23], v[48:51]\n"
        "buffer_load_dwordx4 v1, s[40:43], s71 offen offset:2048 sc1 lds\n"
        "s_add_u32 s66, s72, 147456\n"
        "v_mfma_f32_16x16x32_bf16 v[52:55], a[20:23], v[20:23], v[52:55]\n"
        "ds_read_b128 v[20:23], v0 offset:6144\n"
        "v_mfma_f32_16x16x32_bf16 v[56:59], a[12:15], v[24:27], v[56:59]\n"
        "buffer_store_dwordx4 v[72:75], v6, s[40:43], s66 offen sc1\n"
        "v_mfma_f32_16x16x32_bf16 v[60:63], a[16:19], v[24:27], v[60:63]\n"
        "buffer_load_dwordx4 v1, s[40:43], s71 offen offset:3072 sc1 lds\n"
        "v_mfma_f32_16x16x32_bf16 v[64:67], a[20:23], v[24:27], v[64:67]\n"
        "ds_read_b128 v[24:27], v0 offset:7168\n"
        "s_waitcnt lgkmcnt(2)\n"
        "v_mfma_f32_16x16x32_bf16 v[44:47], a[24:27], v[12:15], v[44:47]\n"
        "v_permlane16_swap_b32 v76, v78\n"
        "v_mfma_f32_16x16x32_bf16 v[48:51], a[28:31], v[12:15], v[48:51]\n"
        "v_permlane16_swap_b32 v77, v79\n"
        "v_mfma_f32_16x16x32_bf16 v[52:55], a[32:35], v[12:15], v[52:55]\n"
        "ds_read_b128 v[12:15], v0 offset:8192\n"
        "buffer_load_dword v4, v2, s[40:43], s76 offen sc1\n"
        "v_mfma_f32_16x16x32_bf16 v[56:59], a[24:27], v[16:19], v[56:59]\n"
        "buffer_store_dwordx4 v[76:79], v7, s[40:43], s72 offen sc1\n"
        "v_mfma_f32_16x16x32_bf16 v[60:63], a[28:31], v[16:19], v[60:63]\n"
        "v_mfma_f32_16x16x32_bf16 v[64:67], a[32:35], v[16:19], v[64:67]\n"
        "ds_read_b128 v[16:19], v0 offset:9216\n"
        "s_waitcnt lgkmcnt(2)\n"
        "v_mfma_f32_16x16x32_bf16 v[44:47], a[36:39], v[20:23], v[44:47]\n"
        "v_mfma_f32_16x16x32_bf16 v[48:51], a[40:43], v[20:23], v[48:51]\n"
        "v_mfma_f32_16x16x32_bf16 v[52:55], a[44:47], v[20:23], v[52:55]\n"
        "ds_read_b128 v[20:23], v0 offset:10240\n"
        "v_mfma_f32_16x16x32_bf16 v[56:59], a[36:39], v[24:27], v[56:59]\n"
        "v_mfma_f32_16x16x32_bf16 v[60:63], a[40:43], v[24:27], v[60:63]\n"
        "v_mfma_f32_16x16x32_bf16 v[64:67], a[44:47], v[24:27], v[64:67]\n"
        "ds_read_b128 v[24:27], v0 offset:11264\n"
        "s_waitcnt lgkmcnt(2)\n"
        "v_mfma_f32_16x16x32_bf16 v[44:47], a[48:51], v[12:15], v[44:47]\n"
        "v_mfma_f32_16x16x32_bf16 v[48:51], a[52:55], v[12:15], v[48:51]\n"
        "v_mfma_f32_16x16x32_bf16 v[52:55], a[56:59], v[12:15], v[52:55]\n"
        "ds_read_b128 v[12:15], v0 offset:12288\n"
        "v_mfma_f32_16x16x32_bf16 v[56:59], a[48:51], v[16:19], v[56:59]\n"
        "v_mfma_f32_16x16x32_bf16 v[60:63], a[52:55], v[16:19], v[60:63]\n"
        "v_mfma_f32_16x16x32_bf16 v[64:67], a[56:59], v[16:19], v[64:67]\n"
        "ds_read_b128 v[16:19], v0 offset:13312\n"
        "s_waitcnt lgkmcnt(2)\n"
        "v_mfma_f32_16x16x32_bf16 v[44:47], a[60:63], v[20:23], v[44:47]\n"
        "v_mfma_f32_16x16x32_bf16 v[48:51], a[64:67], v[20:23], v[48:51]\n"
        "v_mfma_f32_16x16x32_bf16 v[52:55], a[68:71], v[20:23], v[52:55]\n"
        "ds_read_b128 v[20:23], v0 offset:14336\n"
        "v_mfma_f32_16x16x32_bf16 v[56:59], a[60:63], v[24:27], v[56:59]\n"
        "v_mfma_f32_16x16x32_bf16 v[60:63], a[64:67], v[24:27], v[60:63]\n"
        "v_mfma_f32_16x16x32_bf16 v[64:67], a[68:71], v[24:27], v[64:67]\n"
        "ds_read_b128 v[24:27], v0 offset:15360\n"
        "s_waitcnt lgkmcnt(2)\n"
        "v_mfma_f32_16x16x32_bf16 v[44:47], a[72:75], v[12:15], v[44:47]\n"
        "v_mfma_f32_16x16x32_bf16 v[48:51], a[76:79], v[12:15], v[48:51]\n"
        "s_waitcnt vmcnt(2)\n"
        "s_barrier\n"
        "v_mfma_f32_16x16x32_bf16 v[52:55], a[80:83], v[12:15], v[52:55]\n"
        "ds_read_b128 v[12:15], v0 offset:16384\n"
        "v_mfma_f32_16x16x32_bf16 v[56:59], a[72:75], v[16:19], v[56:59]\n"
        "v_mfma_f32_16x16x32_bf16 v[60:63], a[76:79], v[16:19], v[60:63]\n"
        "s_waitcnt vmcnt(0)\n"
        "v_mov_b32 v5, s74\n"
        "v_mfma_f32_16x16x32_bf16 v[64:67], a[80:83], v[16:19], v[64:67]\n"
        "ds_read_b128 v[16:19], v0 offset:17408\n"
        "s_waitcnt lgkmcnt(2)\n"
        "v_mfma_f32_16x16x32_bf16 v[44:47], a[84:87], v[20:23], v[44:47]\n"
        "buffer_store_dword v5, v3, s[40:43], s73 offen sc1\n"
        "s_add_u32 s68, s68, 1\n"
        "v_cmp_gt_i32 vcc, s70, v4\n"
        "v_mfma_f32_16x16x32_bf16 v[48:51], a[88:91], v[20:23], v[48:51]\n"
        "s_lshr_b32 s66, s68, 1\n"
        "s_and_b64 vcc, vcc, 15\n"
        "v_mfma_f32_16x16x32_bf16 v[52:55], a[92:95], v[20:23], v[52:55]\n"
        "ds_read_b128 v[20:23], v0 offset:18432\n"
        "v_mfma_f32_16x16x32_bf16 v[56:59], a[84:87], v[24:27], v[56:59]\n"
        "s_bitcmp1_b32 s54, s66\n"
        "s_cselect_b32 s66, 8192, 0\n"
        "v_mfma_f32_16x16x32_bf16 v[60:63], a[88:91], v[24:27], v[60:63]\n"
        "s_lshl_b32 s72, s68, 20\n"
        "s_add_u32 s72, s72, s66\n"
        "v_mfma_f32_16x16x32_bf16 v[64:67], a[92:95], v[24:27], v[64:67]\n"
        "ds_read_b128 v[24:27], v0 offset:19456\n"
        "s_waitcnt lgkmcnt(2)\n"
        "s_add_u32 s72, s72, s59\n"
        "s_mov_b32 s74, s68\n"
        "s_mov_b32 s73, s60\n"
        "s_cbranch_vccnz .Lop_c10_%=\n"
        "s_branch .Lop_c11_%=\n"
        ".Lop_c00_%=:\n"
        "v_mfma_f32_16x16x32_bf16 v[44:47], a[0:3], v[12:15], 0\n"
        "v_cvt_pk_bf16_f32 v74, v60, v61\n"
        "v_cvt_pk_bf16_f32 v75, v62, v63\n"
        "v_mfma_f32_16x16x32_bf16 v[48:51], a[4:7], v[12:15], 0\n"
        "v_cvt_pk_bf16_f32 v78, v64, v65\n"
        "v_cvt_pk_bf16_f32 v79, v66, v67\n"
        "v_mfma_f32_16x16x32_bf16 v[52:55], a[8:11], v[12:15], 0\n"
        "ds_read_b128 v[12:15], v0 offset:4096\n"
        "v_mfma_f32_16x16x32_bf16 v[56:59], a[0:3], v[16:19], 0\n"
        "buffer_store_dwordx4 v[68:71], v6, s[40:43], s72 offen sc1\n"
        "v_mfma_f32_16x16x32_bf16 v[60:63], a[4:7], v[16:19], 0\n"
        "s_add_u32 s66, s72, 147456\n"
        "v_mfma_f32_16x16x32_bf16 v[64:67], a[8:11], v[16:19], 0\n"
        "ds_read_b128 v[16:19], v0 offset:5120\n"
        "s_waitcnt lgkmcnt(2)\n"
        "v_mfma_f32_16x16x32_bf16 v[44:47], a[12:15], v[20:23], v[44:47]\n"
        "buffer_store_dwordx4 v[72:75], v6, s[40:43], s66 offen sc1\n"
        "v_mfma_f32_16x16x32_bf16 v[48:51], a[16:19], v[20:23], v[48:51]\n"
        "v_permlane16_swap_b32 v76, v78\n"
        "v_mfma_f32_16x16x32_bf16 v[52:55], a[20:23], v[20:23], v[52:55]\n"
        "ds_read_b128 v[20:23], v0 offset:6144\n"
        "v_mfma_f32_16x16x32_bf16 v[56:59], a[12:15], v[24:27], v[56:59]\n"
        "v_permlane16_swap_b32 v77, v79\n"
        "v_mfma_f32_16x16x32_bf16 v[60:63], a[16:19], v[24:27], v[60:63]\n"
        "buffer_store_dwordx4 v[76:79], v7, s[40:43], s72 offen sc1\n"
        "v_mfma_f32_16x16x32_bf16 v[64:67], a[20:23], v[24:27], v[64:67]\n"
        "ds_read_b128 v[24:27], v0 offset:7168\n"
        "s_waitcnt lgkmcnt(2)\n"
        "v_mfma_f32_16x16x32_bf16 v[44:47], a[24:27], v[12:15], v[44:47]\n"
        "buffer_load_dword v4, v2, s[40:43], s76 offen sc1\n"
        "v_mfma_f32_16x16x32_bf16 v[48:51], a[28:31], v[12:15], v[48:51]\n"
        "v_mfma_f32_16x16x32_bf16 v[52:55], a[32:35], v[12:15], v[52:55]\n"
        "ds_read_b128 v[12:15], v0 offset:8192\n"
        "v_mfma_f32_16x16x32_bf16 v[56:59], a[24:27], v[16:19], v[56:59]\n"
        "v_mfma_f32_16x16x32_bf16 v[60:63], a[28:31], v[16:19], v[60:63]\n"
        "v_mfma_f32_16x16x32_bf16 v[64:67], a[32:35], v[16:19], v[64:67]\n"
        "ds_read_b128 v[16:19], v0 offset:9216\n"
        "s_waitcnt lgkmcnt(2)\n"
        "v_mfma_f32_16x16x32_bf16 v[44:47], a[36:39], v[20:23], v[44:47]\n"
        "v_mfma_f32_16x16x32_bf16 v[48:51], a[40:43], v[20:23], v[48:51]\n"
        "v_mfma_f32_16x16x32_bf16 v[52:55], a[44:47], v[20:23], v[52:55]\n"
        "ds_read_b128 v[20:23], v0 offset:10240\n"
        "v_mfma_f32_16x16x32_bf16 v[56:59], a[36:39], v[24:27], v[56:59]\n"
        "v_mfma_f32_16x16x32_bf16 v[60:63], a[40:43], v[24:27], v[60:63]\n"
        "v_mfma_f32_16x16x32_bf16 v[64:67], a[44:47], v[24:27], v[64:67]\n"
        "ds_read_b128 v[24:27], v0 offset:11264\n"
        "s_waitcnt lgkmcnt(2)\n"
        "v_mfma_f32_16x16x32_bf16 v[44:47], a[48:51], v[12:15], v[44:47]\n"
        "v_mfma_f32_16x16x32_bf16 v[48:51], a[52:55], v[12:15], v[48:51]\n"
        "v_mfma_f32_16x16x32_bf16 v[52:55], a[56:59], v[12:15], v[52:55]\n"
        "ds_read_b128 v[12:15], v0 offset:12288\n"
        "v_mfma_f32_16x16x32_bf16 v[56:59], a[48:51], v[16:19], v[56:59]\n"
        "v_mfma_f32_16x16x32_bf16 v[60:63], a[52:55], v[16:19], v[60:63]\n"
        "v_mfma_f32_16x16x32_bf16 v[64:67], a[56:59], v[16:19], v[64:67]\n"
        "ds_read_b128 v[16:19], v0 offset:13312\n"
        "s_waitcnt lgkmcnt(2)\n"
        "v_mfma_f32_16x16x32_bf16 v[44:47], a[60:63], v[20:23], v[44:47]\n"
        "v_mfma_f32_16x16x32_bf16 v[48:51], a[64:67], v[20:23], v[48:51]\n"
        "v_mfma_f32_16x16x32_bf16 v[52:55], a[68:71], v[20:23], v[52:55]\n"
        "ds_read_b128 v[20:23], v0 offset:14336\n"
        "v_mfma_f32_16x16x32_bf16 v[56:59], a[60:63], v[24:27], v[56:59]\n"
        "v_mfma_f32_16x16x32_bf16 v[60:63], a[64:67], v[24:27], v[60:63]\n"
        "v_mfma_f32_16x16x32_bf16 v[64:67], a[68:71], v[24:27], v[64:67]\n"
        "ds_read_b128 v[24:27], v0 offset:15360\n"
        "s_waitcnt lgkmcnt(2)\n"
        "v_mfma_f32_16x16x32_bf16 v[44:47], a[72:75], v[12:15], v[44:47]\n"
        "v_mfma_f32_16x16x32_bf16 v[48:51], a[76:79], v[12:15], v[48:51]\n"
        "v_mfma_f32_16x16x32_bf16 v[52:55], a[80:83], v[12:15], v[52:55]\n"
        "v_mfma_f32_16x16x32_bf16 v[56:59], a[72:75], v[16:19], v[56:59]\n"
        "v_mfma_f32_16x16x32_bf16 v[60:63], a[76:79], v[16:19], v[60:63]\n"
        "s_waitcnt vmcnt(1)\n"
        "v_mov_b32 v5, s74\n"
        "v_mfma_f32_16x16x32_bf16 v[64:67], a[80:83], v[16:19], v[64:67]\n"
        "s_waitcnt lgkmcnt(0)\n"
        "buffer_store_dword v5, v3, s[40:43], s73 offen sc1\n"
        "v_mfma_f32_16x16x32_bf16 v[44:47], a[84:87], v[20:23], v[44:47]\n"
        "s_add_u32 s68, s68, 1\n"
        "s_lshr_b32 s66, s68, 1\n"
        "v_mfma_f32_16x16x32_bf16 v[48:51], a[88:91], v[20:23], v[48:51]\n"
        "s_bitcmp1_b32 s54, s66\n"
        "s_cselect_b32 s66, 8192, 0\n"
        "v_mfma_f32_16x16x32_bf16 v[52:55], a[92:95], v[20:23], v[52:55]\n"
        "s_lshl_b32 s72, s68, 20\n"
        "s_add_u32 s72, s72, s66\n"
        "v_mfma_f32_16x16x32_bf16 v[56:59], a[84:87], v[24:27], v[56:59]\n"
        "s_add_u32 s72, s72, s59\n"
        "s_mov_b32 s74, s68\n"
        "v_mfma_f32_16x16x32_bf16 v[60:63], a[88:91], v[24:27], v[60:63]\n"
        "s_mov_b32 s73, s60\n"
        "v_mfma_f32_16x16x32_bf16 v[64:67], a[92:95], v[24:27], v[64:67]\n"
        "s_waitcnt vmcnt(1)\n"
        "v_cmp_gt_i32 vcc, s70, v4\n"
        "s_nop 0\n"
        "s_and_b64 vcc, vcc, 15\n"
        "s_cbranch_vccnz .Lop_late0_%=\n"
        "s_add_u32 m0, s61, 16384\n"
        "s_add_u32 s71, s71, 32768\n"
        "buffer_load_dwordx4 v1, s[40:43], s71 offen offset:0 sc1 lds\n"
        "buffer_load_dwordx4 v1, s[40:43], s71 offen offset:1024 sc1 lds\n"
        "buffer_load_dwordx4 v1, s[40:43], s71 offen offset:2048 sc1 lds\n"
        "buffer_load_dwordx4 v1, s[40:43], s71 offen offset:3072 sc1 lds\n"
        "s_branch .Lop_go0_%=\n"
        ".Lop_late0_%=:\n"
        "s_mov_b32 s64, 0\n"
        ".Lop_in0_top_%=:\n"
        "buffer_load_dword v4, v2, s[40:43], s76 offen sc1\n"
        "s_waitcnt vmcnt(0)\n"
        "v_cmp_gt_i32 vcc, s70, v4\n"
        "s_nop 0\n"
        "s_and_b64 vcc, vcc, 15\n"
        "s_cbranch_vccz .Lop_in0_ok_%=\n"
        "s_add_u32 s64, s64, 1\n"
        "s_and_b32 s65, s64, 255\n"
        "s_cbranch_scc1 .Lop_in0_go_%=\n"
        "s_mov_b32 s65, 40960\n"
        "s_cmp_lt_u32 s64, 1048576\n"
        "s_cbranch_scc1 .Lop_in0_chk_%=\n"
        "v_mov_b32 v4, 1\n"
        "buffer_store_dword v4, off, s[40:43], s65 sc1\n"
        "s_waitcnt vmcnt(0)\n"
        ".Lop_in0_chk_%=:\n"
        "buffer_load_dword v4, off, s[40:43], s65 sc1\n"
        "s_waitcnt vmcnt(0)\n"
        "v_readfirstlane_b32 s65, v4\n"
        "s_nop 0\n"
        "s_cmp_lg_u32 s65, 1\n"
        "s_cbranch_scc1 .Lop_in0_go_%=\n"
        "s_endpgm\n"
        ".Lop_in0_go_%=:\n"
        "s_sleep 1\n"
        "s_branch .Lop_in0_top_%=\n"
        ".Lop_in0_ok_%=:\n"
        "s_add_u32 m0, s61, 16384\n"
        "s_add_u32 s71, s71, 32768\n"
        "buffer_load_dwordx4 v1, s[40:43], s71 offen offset:0 sc1 lds\n"
        "buffer_load_dwordx4 v1, s[40:43], s71 offen offset:1024 sc1 lds\n"
        "buffer_load_dwordx4 v1, s[40:43], s71 offen offset:2048 sc1 lds\n"
        "buffer_load_dwordx4 v1, s[40:43], s71 offen offset:3072 sc1 lds\n"
        ".Lop_go0_%=:\n"
        "buffer_load_dword v4, v2, s[40:43], s76 offen sc1\n"
        "s_waitcnt vmcnt(0)\n"
        "s_barrier\n"
        "ds_read_b128 v[12:15], v0 offset:16384\n"
        "ds_read_b128 v[16:19], v0 offset:17408\n"
        "ds_read_b128 v[20:23], v0 offset:18432\n"
        "ds_read_b128 v[24:27], v0 offset:19456\n"
        "v_cmp_gt_i32 vcc, s70, v4\n"
        "s_nop 0\n"
        "s_and_b64 vcc, vcc, 15\n"
        "s_waitcnt lgkmcnt(2)\n"
        "s_cbranch_vccnz .Lop_c10_%=\n"
        "s_branch .Lop_c11_%=\n"
        ".Lop_c11_%=:\n"
        "v_mfma_f32_16x16x32_bf16 v[44:47], a[96:99], v[12:15], v[44:47]\n"
        "s_add_u32 m0, s61, 32768\n"
        "s_add_u32 s71, s71, 32768\n"
        "v_mfma_f32_16x16x32_bf16 v[48:51], a[100:103], v[12:15], v[48:51]\n"
        "buffer_load_dwordx4 v1, s[40:43], s71 offen offset:0 sc1 lds\n"
        "v_mfma_f32_16x16x32_bf16 v[52:55], a[104:107], v[12:15], v[52:55]\n"
        "ds_read_b128 v[12:15], v0 offset:20480\n"
        "v_mfma_f32_16x16x32_bf16 v[56:59], a[96:99], v[16:19], v[56:59]\n"
        "v_mfma_f32_16x16x32_bf16 v[60:63], a[100:103], v[16:19], v[60:63]\n"
        "buffer_load_dwordx4 v1, s[40:43], s71 offen offset:1024 sc1 lds\n"
        "v_mfma_f32_16x16x32_bf16 v[64:67], a[104:107], v[16:19], v[64:67]\n"
        "ds_read_b128 v[16:19], v0 offset:21504\n"
        "s_waitcnt lgkmcnt(2)\n"
        "v_mfma_f32_16x16x32_bf16 v[44:47], a[108:111], v[20:23], v[44:47]\n"
        "v_mfma_f32_16x16x32_bf16 v[48:51], a[112:115], v[20:23], v[48:51]\n"
        "buffer_load_dwordx4 v1, s[40:43], s71 offen offset:2048 sc1 lds\n"
        "v_mfma_f32_16x16x32_bf16 v[52:55], a[116:119], v[20:23], v[52:55]\n"
        "ds_read_b128 v[20:23], v0 offset:22528\n"
        "v_mfma_f32_16x16x32_bf16 v[56:59], a[108:111], v[24:27], v[56:59]\n"
        "v_mfma_f32_16x16x32_bf16 v[60:63], a[112:115], v[24:27], v[60:63]\n"
        "buffer_load_dwordx4 v1, s[40:43], s71 offen offset:3072 sc1 lds\n"
        "v_mfma_f32_16x16x32_bf16 v[64:67], a[116:119], v[24:27], v[64:67]\n"
        "ds_read_b128 v[24:27], v0 offset:23552\n"
        "s_waitcnt lgkmcnt(2)\n"
        "v_mfma_f32_16x16x32_bf16 v[44:47], a[120:123], v[12:15], v[44:47]\n"
        "buffer_load_dword v4, v2, s[40:43], s76 offen sc1\n"
        "v_mfma_f32_16x16x32_bf16 v[48:51], a[124:127], v[12:15], v[48:51]\n"
        "v_mfma_f32_16x16x32_bf16 v[52:55], a[128:131], v[12:15], v[52:55]\n"
        "ds_read_b128 v[12:15], v0 offset:24576\n"
        "v_mfma_f32_16x16x32_bf16 v[56:59], a[120:123], v[16:19], v[56:59]\n"
        "v_mfma_f32_16x16x32_bf16 v[60:63], a[124:127], v[16:19], v[60:63]\n"
        "v_mfma_f32_16x16x32_bf16 v[64:67], a[128:131], v[16:19], v[64:67]\n"
        "ds_read_b128 v[16:19], v0 offset:25600\n"
        "s_waitcnt lgkmcnt(2)\n"
        "v_mfma_f32_16x16x32_bf16 v[44:47], a[132:135], v[20:23], v[44:47]\n"
        "v_mfma_f32_16x16x32_bf16 v[48:51], a[136:139], v[20:23], v[48:51]\n"
        "v_mfma_f32_16x16x32_bf16 v[52:55], a[140:143], v[20:23], v[52:55]\n"
        "ds_read_b128 v[20:23], v0 offset:26624\n"
        "v_mfma_f32_16x16x32_bf16 v[56:59], a[132:135], v[24:27], v[56:59]\n"
        "v_mfma_f32_16x16x32_bf16 v[60:63], a[136:139], v[24:27], v[60:63]\n"
        "v_mfma_f32_16x16x32_bf16 v[64:67], a[140:143], v[24:27], v[64:67]\n"
        "ds_read_b128 v[24:27], v0 offset:27648\n"
        "s_waitcnt lgkmcnt(2)\n"
        "v_mfma_f32_16x16x32_bf16 v[44:47], a[144:147], v[12:15], v[44:47]\n"
        "v_mfma_f32_16x16x32_bf16 v[48:51], a[148:151], v[12:15], v[48:51]\n"
        "v_mfma_f32_16x16x32_bf16 v[52:55], a[152:155], v[12:15], v[52:55]\n"
        "ds_read_b128 v[12:15], v0 offset:28672\n"
        "v_mfma_f32_16x16x32_bf16 v[56:59], a[144:147], v[16:19], v[56:59]\n"
        "v_mfma_f32_16x16x32_bf16 v[60:63], a[148:151], v[16:19], v[60:63]\n"
        "v_mfma_f32_16x16x32_bf16 v[64:67], a[152:155], v[16:19], v[64:67]\n"
        "ds_read_b128 v[16:19], v0 offset:29696\n"
        "s_waitcnt lgkmcnt(2)\n"
        "v_mfma_f32_16x16x32_bf16 v[44:47], a[156:159], v[20:23], v[44:47]\n"
        "v_mfma_f32_16x16x32_bf16 v[48:51], a[160:163], v[20:23], v[48:51]\n"
        "v_mfma_f32_16x16x32_bf16 v[52:55], a[164:167], v[20:23], v[52:55]\n"
        "ds_read_b128 v[20:23], v0 offset:30720\n"
        "v_mfma_f32_16x16x32_bf16 v[56:59], a[156:159], v[24:27], v[56:59]\n"
        "v_mfma_f32_16x16x32_bf16 v[60:63], a[160:163], v[24:27], v[60:63]\n"
        "v_mfma_f32_16x16x32_bf16 v[64:67], a[164:167], v[24:27], v[64:67]\n"
        "ds_read_b128 v[24:27], v0 offset:31744\n"
        "s_waitcnt lgkmcnt(2)\n"
        "v_mfma_f32_16x16x32_bf16 v[44:47], a[168:171], v[12:15], v[44:47]\n"
        "v_mfma_f32_16x16x32_bf16 v[48:51], a[172:175], v[12:15], v[48:51]\n"
        "s_waitcnt vmcnt(1)\n"
        "s_barrier\n"
        "v_mfma_f32_16x16x32_bf16 v[52:55], a[176:179], v[12:15], v[52:55]\n"
        "ds_read_b128 v[12:15], v8 offset:0\n"
        "v_mfma_f32_16x16x32_bf16 v[56:59], a[168:171], v[16:19], v[56:59]\n"
        "v_mfma_f32_16x16x32_bf16 v[60:63], a[172:175], v[16:19], v[60:63]\n"
        "v_mfma_f32_16x16x32_bf16 v[64:67], a[176:179], v[16:19], v[64:67]\n"
        "ds_read_b128 v[16:19], v8 offset:1024\n"
        "s_waitcnt lgkmcnt(2)\n"
        "v_mfma_f32_16x16x32_bf16 v[44:47], a[180:183], v[20:23], v[44:47]\n"
        "s_waitcnt vmcnt(0)\n"
        "v_cmp_gt_i32 vcc, s70, v4\n"
        "v_mfma_f32_16x16x32_bf16 v[48:51], a[184:187], v[20:23], v[48:51]\n"
        "s_and_b64 vcc, vcc, 15\n"
        "v_mfma_f32_16x16x32_bf16 v[52:55], a[188:191], v[20:23], v[52:55]\n"
        "ds_read_b128 v[20:23], v8 offset:2048\n"
        "v_mfma_f32_16x16x32_bf16 v[56:59], a[180:183], v[24:27], v[56:59]\n"
        "v_mfma_f32_16x16x32_bf16 v[60:63], a[184:187], v[24:27], v[60:63]\n"
        "v_mfma_f32_16x16x32_bf16 v[64:67], a[188:191], v[24:27], v[64:67]\n"
        "ds_read_b128 v[24:27], v8 offset:3072\n"
        "s_waitcnt lgkmcnt(2)\n"
        "s_cbranch_vccnz .Lop_c20_%=\n"
        "s_branch .Lop_c21_%=\n"
        ".Lop_c10_%=:\n"
        "v_mfma_f32_16x16x32_bf16 v[44:47], a[96:99], v[12:15], v[44:47]\n"
        "v_mfma_f32_16x16x32_bf16 v[48:51], a[100:103], v[12:15], v[48:51]\n"
        "v_mfma_f32_16x16x32_bf16 v[52:55], a[104:107], v[12:15], v[52:55]\n"
        "ds_read_b128 v[12:15], v0 offset:20480\n"
        "v_mfma_f32_16x16x32_bf16 v[56:59], a[96:99], v[16:19], v[56:59]\n"
        "v_mfma_f32_16x16x32_bf16 v[60:63], a[100:103], v[16:19], v[60:63]\n"
        "v_mfma_f32_16x16x32_bf16 v[64:67], a[104:107], v[16:19], v[64:67]\n"
        "ds_read_b128 v[16:19], v0 offset:21504\n"
        "s_waitcnt lgkmcnt(2)\n"
        "v_mfma_f32_16x16x32_bf16 v[44:47], a[108:111], v[20:23], v[44:47]\n"
        "v_mfma_f32_16x16x32_bf16 v[48:51], a[112:115], v[20:23], v[48:51]\n"
        "v_mfma_f32_16x16x32_bf16 v[52:55], a[116:119], v[20:23], v[52:55]\n"
        "ds_read_b128 v[20:23], v0 offset:22528\n"
        "v_mfma_f32_16x16x32_bf16 v[56:59], a[108:111], v[24:27], v[56:59]\n"
        "v_mfma_f32_16x16x32_bf16 v[60:63], a[112:115], v[24:27], v[60:63]\n"
        "v_mfma_f32_16x16x32_bf16 v[64:67], a[116:119], v[24:27], v[64:67]\n"
        "ds_read_b128 v[24:27], v0 offset:23552\n"
        "s_waitcnt lgkmcnt(2)\n"
        "v_mfma_f32_16x16x32_bf16 v[44:47], a[120:123], v[12:15], v[44:47]\n"
        "buffer_load_dword v4, v2, s[40:43], s76 offen sc1\n"
        "v_mfma_f32_16x16x32_bf16 v[48:51], a[124:127], v[12:15], v[48:51]\n"
        "v_mfma_f32_16x16x32_bf16 v[52:55], a[128:131], v[12:15], v[52:55]\n"
        "ds_read_b128 v[12:15], v0 offset:24576\n"
        "v_mfma_f32_16x16x32_bf16 v[56:59], a[120:123], v[16:19], v[56:59]\n"
        "v_mfma_f32_16x16x32_bf16 v[60:63], a[124:127], v[16:19], v[60:63]\n"
        "v_mfma_f32_16x16x32_bf16 v[64:67], a[128:131], v[16:19], v[64:67]\n"
        "ds_read_b128 v[16:19], v0 offset:25600\n"
        "s_waitcnt lgkmcnt(2)\n"
        "v_mfma_f32_16x16x32_bf16 v[44:47], a[132:135], v[20:23], v[44:47]\n"
        "v_mfma_f32_16x16x32_bf16 v[48:51], a[136:139], v[20:23], v[48:51]\n"
        "v_mfma_f32_16x16x32_bf16 v[52:55], a[140:143], v[20:23], v[52:55]\n"
        "ds_read_b128 v[20:23], v0 offset:26624\n"
        "v_mfma_f32_16x16x32_bf16 v[56:59], a[132:135], v[24:27], v[56:59]\n"
        "v_mfma_f32_16x16x32_bf16 v[60:63], a[136:139], v[24:27], v[60:63]\n"
        "v_mfma_f32_16x16x32_bf16 v[64:67], a[140:143], v[24:27], v[64:67]\n"
        "ds_read_b128 v[24:27], v0 offset:27648\n"
        "s_waitcnt lgkmcnt(2)\n"
        "v_mfma_f32_16x16x32_bf16 v[44:47], a[144:147], v[12:15], v[44:47]\n"
        "v_mfma_f32_16x16x32_bf16 v[48:51], a[148:151], v[12:15], v[48:51]\n"
        "v_mfma_f32_16x16x32_bf16 v[52:55], a[152:155], v[12:15], v[52:55]\n"
        "ds_read_b128 v[12:15], v0 offset:28672\n"
        "v_mfma_f32_16x16x32_bf16 v[56:59], a[144:147], v[16:19], v[56:59]\n"
        "v_mfma_f32_16x16x32_bf16 v[60:63], a[148:151], v[16:19], v[60:63]\n"
        "v_mfma_f32_16x16x32_bf16 v[64:67], a[152:155], v[16:19], v[64:67]\n"
        "ds_read_b128 v[16:19], v0 offset:29696\n"
        "s_waitcnt lgkmcnt(2)\n"
        "v_mfma_f32_16x16x32_bf16 v[44:47], a[156:159], v[20:23], v[44:47]\n"
        "v_mfma_f32_16x16x32_bf16 v[48:51], a[160:163], v[20:23], v[48:51]\n"
        "v_mfma_f32_16x16x32_bf16 v[52:55], a[164:167], v[20:23], v[52:55]\n"
        "ds_read_b128 v[20:23], v0 offset:30720\n"
        "v_mfma_f32_16x16x32_bf16 v[56:59], a[156:159], v[24:27], v[56:59]\n"
        "v_mfma_f32_16x16x32_bf16 v[60:63], a[160:163], v[24:27], v[60:63]\n"
        "v_mfma_f32_16x16x32_bf16 v[64:67], a[164:167], v[24:27], v[64:67]\n"
        "ds_read_b128 v[24:27], v0 offset:31744\n"
        "s_waitcnt lgkmcnt(2)\n"
        "v_mfma_f32_16x16x32_bf16 v[44:47], a[168:171], v[12:15], v[44:47]\n"
        "v_mfma_f32_16x16x32_bf16 v[48:51], a[172:175], v[12:15], v[48:51]\n"
        "v_mfma_f32_16x16x32_bf16 v[52:55], a[176:179], v[12:15], v[52:55]\n"
        "v_mfma_f32_16x16x32_bf16 v[56:59], a[168:171], v[16:19], v[56:59]\n"
        "v_mfma_f32_16x16x32_bf16 v[60:63], a[172:175], v[16:19], v[60:63]\n"
        "v_mfma_f32_16x16x32_bf16 v[64:67], a[176:179], v[16:19], v[64:67]\n"
        "s_waitcnt lgkmcnt(0)\n"
        "v_mfma_f32_16x16x32_bf16 v[44:47], a[180:183], v[20:23], v[44:47]\n"
        "v_mfma_f32_16x16x32_bf16 v[48:51], a[184:187], v[20:23], v[48:51]\n"
        "v_mfma_f32_16x16x32_bf16 v[52:55], a[188:191], v[20:23], v[52:55]\n"
        "v_mfma_f32_16x16x32_bf16 v[56:59], a[180:183], v[24:27], v[56:59]\n"
        "v_mfma_f32_16x16x32_bf16 v[60:63], a[184:187], v[24:27], v[60:63]\n"
        "v_mfma_f32_16x16x32_bf16 v[64:67], a[188:191], v[24:27], v[64:67]\n"
        "s_waitcnt vmcnt(0)\n"
        "v_cmp_gt_i32 vcc, s70, v4\n"
        "s_nop 0\n"
        "s_and_b64 vcc, vcc, 15\n"
        "s_cbranch_vccnz .Lop_late1_%=\n"
        "s_add_u32 m0, s61, 32768\n"
        "s_add_u32 s71, s71, 32768\n"
        "buffer_load_dwordx4 v1, s[40:43], s71 offen offset:0 sc1 lds\n"
        "buffer_load_dwordx4 v1, s[40:43], s71 offen offset:1024 sc1 lds\n"
        "buffer_load_dwordx4 v1, s[40:43], s71 offen offset:2048 sc1 lds\n"
        "buffer_load_dwordx4 v1, s[40:43], s71 offen offset:3072 sc1 lds\n"
        "s_branch .Lop_go1_%=\n"
        ".Lop_late1_%=:\n"
        "s_mov_b32 s64, 0\n"
        ".Lop_in1_top_%=:\n"
        "buffer_load_dword v4, v2, s[40:43], s76 offen sc1\n"
        "s_waitcnt vmcnt(0)\n"
        "v_cmp_gt_i32 vcc, s70, v4\n"
        "s_nop 0\n"
        "s_and_b64 vcc, vcc, 15\n"
        "s_cbranch_vccz .Lop_in1_ok_%=\n"
        "s_add_u32 s64, s64, 1\n"
        "s_and_b32 s65, s64, 255\n"
        "s_cbranch_scc1 .Lop_in1_go_%=\n"
        "s_mov_b32 s65, 40960\n"
        "s_cmp_lt_u32 s64, 1048576\n"
        "s_cbranch_scc1 .Lop_in1_chk_%=\n"
        "v_mov_b32 v4, 1\n"
        "buffer_store_dword v4, off, s[40:43], s65 sc1\n"
        "s_waitcnt vmcnt(0)\n"
        ".Lop_in1_chk_%=:\n"
        "buffer_load_dword v4, off, s[40:43], s65 sc1\n"
        "s_waitcnt vmcnt(0)\n"
        "v_readfirstlane_b32 s65, v4\n"
        "s_nop 0\n"
        "s_cmp_lg_u32 s65, 1\n"
        "s_cbranch_scc1 .Lop_in1_go_%=\n"
        "s_endpgm\n"
        ".Lop_in1_go_%=:\n"
        "s_sleep 1\n"
        "s_branch .Lop_in1_top_%=\n"
        ".Lop_in1_ok_%=:\n"
        "s_add_u32 m0, s61, 32768\n"
        "s_add_u32 s71, s71, 32768\n"
        "buffer_load_dwordx4 v1, s[40:43], s71 offen offset:0 sc1 lds\n"
        "buffer_load_dwordx4 v1, s[40:43], s71 offen offset:1024 sc1 lds\n"
        "buffer_load_dwordx4 v1, s[40:43], s71 offen offset:2048 sc1 lds\n"
        "buffer_load_dwordx4 v1, s[40:43], s71 offen offset:3072 sc1 lds\n"
        ".Lop_go1_%=:\n"
        "buffer_load_dword v4, v2, s[40:43], s76 offen sc1\n"
        "s_waitcnt vmcnt(0)\n"
        "s_barrier\n"
        "ds_read_b128 v[12:15], v8 offset:0\n"
        "ds_read_b128 v[16:19], v8 offset:1024\n"
        "ds_read_b128 v[20:23], v8 offset:2048\n"
        "ds_read_b128 v[24:27], v8 offset:3072\n"
        "v_cmp_gt_i32 vcc, s70, v4\n"
        "s_nop 0\n"
        "s_and_b64 vcc, vcc, 15\n"
        "s_waitcnt lgkmcnt(2)\n"
        "s_cbranch_vccnz .Lop_c20_%=\n"
        "s_branch .Lop_c21_%=\n"
        ".Lop_c21_%=:\n"
        "v_mfma_f32_16x16x32_bf16 v[44:47], a[192:195], v[12:15], v[44:47]\n"
        "s_add_u32 m0, s61, 49152\n"
        "s_add_u32 s71, s71, 32768\n"
        "v_mfma_f32_16x16x32_bf16 v[48:51], a[196:199], v[12:15], v[48:51]\n"
        "buffer_load_dwordx4 v1, s[40:43], s71 offen offset:0 sc1 lds\n"
        "v_mfma_f32_16x16x32_bf16 v[52:55], a[200:203], v[12:15], v[52:55]\n"
        "ds_read_b128 v[12:15], v8 offset:4096\n"
        "v_mfma_f32_16x16x32_bf16 v[56:59], a[192:195], v[16:19], v[56:59]\n"
        "v_mfma_f32_16x16x32_bf16 v[60:63], a[196:199], v[16:19], v[60:63]\n"
        "buffer_load_dwordx4 v1, s[40:43], s71 offen offset:1024 sc1 lds\n"
        "v_mfma_f32_16x16x32_bf16 v[64:67], a[200:203], v[16:19], v[64:67]\n"
        "ds_read_b128 v[16:19], v8 offset:5120\n"
        "s_waitcnt lgkmcnt(2)\n"
        "v_mfma_f32_16x16x32_bf16 v[44:47], a[204:207], v[20:23], v[44:47]\n"
        "v_mfma_f32_16x16x32_bf16 v[48:51], a[208:211], v[20:23], v[48:51]\n"
        "buffer_load_dwordx4 v1, s[40:43], s71 offen offset:2048 sc1 lds\n"
        "v_mfma_f32_16x16x32_bf16 v[52:55], a[212:215], v[20:23], v[52:55]\n"
        "ds_read_b128 v[20:23], v8 offset:6144\n"
        "v_mfma_f32_16x16x32_bf16 v[56:59], a[204:207], v[24:27], v[56:59]\n"
        "v_mfma_f32_16x16x32_bf16 v[60:63], a[208:211], v[24:27], v[60:63]\n"
        "buffer_load_dwordx4 v1, s[40:43], s71 offen offset:3072 sc1 lds\n"
        "s_add_u32 s69, s69, 1\n"
        "v_mfma_f32_16x16x32_bf16 v[64:67], a[212:215], v[24:27], v[64:67]\n"
        "ds_read_b128 v[24:27], v8 offset:7168\n"
        "s_waitcnt lgkmcnt(2)\n"
        "v_mfma_f32_16x16x32_bf16 v[44:47], a[216:219], v[12:15], v[44:47]\n"
        "s_cmp_lt_u32 s69, 64\n"
        "s_cselect_b32 s70, s69, 2147483647\n"
        "v_mfma_f32_16x16x32_bf16 v[48:51], a[220:223], v[12:15], v[48:51]\n"
        "buffer_load_dword v4, v2, s[40:43], s76 offen sc1\n"
        "v_mfma_f32_16x16x32_bf16 v[52:55], a[224:227], v[12:15], v[52:55]\n"
        "ds_read_b128 v[12:15], v8 offset:8192\n"
        "v_mfma_f32_16x16x32_bf16 v[56:59], a[216:219], v[16:19], v[56:59]\n"
        "v_mfma_f32_16x16x32_bf16 v[60:63], a[220:223], v[16:19], v[60:63]\n"
        "v_mfma_f32_16x16x32_bf16 v[64:67], a[224:227], v[16:19], v[64:67]\n"
        "ds_read_b128 v[16:19], v8 offset:9216\n"
        "s_waitcnt lgkmcnt(2)\n"
        "v_mfma_f32_16x16x32_bf16 v[44:47], a[228:231], v[20:23], v[44:47]\n"
        "v_mfma_f32_16x16x32_bf16 v[48:51], a[232:235], v[20:23], v[48:51]\n"
        "v_mfma_f32_16x16x32_bf16 v[52:55], a[236:239], v[20:23], v[52:55]\n"
        "ds_read_b128 v[20:23], v8 offset:10240\n"
        "v_mfma_f32_16x16x32_bf16 v[56:59], a[228:231], v[24:27], v[56:59]\n"
        "v_mfma_f32_16x16x32_bf16 v[60:63], a[232:235], v[24:27], v[60:63]\n"
        "v_mfma_f32_16x16x32_bf16 v[64:67], a[236:239], v[24:27], v[64:67]\n"
        "ds_read_b128 v[24:27], v8 offset:11264\n"
        "s_waitcnt lgkmcnt(2)\n"
        "v_mfma_f32_16x16x32_bf16 v[44:47], a[240:243], v[12:15], v[44:47]\n"
        "v_mfma_f32_16x16x32_bf16 v[48:51], a[244:247], v[12:15], v[48:51]\n"
        "v_mfma_f32_16x16x32_bf16 v[52:55], a[248:251], v[12:15], v[52:55]\n"
        "ds_read_b128 v[12:15], v8 offset:12288\n"
        "v_mfma_f32_16x16x32_bf16 v[56:59], a[240:243], v[16:19], v[56:59]\n"
        "v_mfma_f32_16x16x32_bf16 v[60:63], a[244:247], v[16:19], v[60:63]\n"
        "v_mfma_f32_16x16x32_bf16 v[64:67], a[248:251], v[16:19], v[64:67]\n"
        "ds_read_b128 v[16:19], v8 offset:13312\n"
        "s_waitcnt lgkmcnt(2)\n"
        "v_mfma_f32_16x16x32_bf16 v[44:47], a[252:255], v[20:23], v[44:47]\n"
        "v_mfma_f32_16x16x32_bf16 v[48:51], v[80:83], v[20:23], v[48:51]\n"
        "v_mfma_f32_16x16x32_bf16 v[52:55], v[84:87], v[20:23], v[52:55]\n"
        "ds_read_b128 v[20:23], v8 offset:14336\n"
        "v_mfma_f32_16x16x32_bf16 v[56:59], a[252:255], v[24:27], v[56:59]\n"
        "v_mfma_f32_16x16x32_bf16 v[60:63], v[80:83], v[24:27], v[60:63]\n"
        "v_mfma_f32_16x16x32_bf16 v[64:67], v[84:87], v[24:27], v[64:67]\n"
        "ds_read_b128 v[24:27], v8 offset:15360\n"
        "s_waitcnt lgkmcnt(2)\n"
        "v_mfma_f32_16x16x32_bf16 v[44:47], v[88:91], v[12:15], v[44:47]\n"
        "v_mfma_f32_16x16x32_bf16 v[48:51], v[92:95], v[12:15], v[48:51]\n"
        "s_waitcnt vmcnt(1)\n"
        "s_barrier\n"
        "v_mfma_f32_16x16x32_bf16 v[52:55], v[96:99], v[12:15], v[52:55]\n"
        "ds_read_b128 v[12:15], v8 offset:16384\n"
        "v_mfma_f32_16x16x32_bf16 v[56:59], v[88:91], v[16:19], v[56:59]\n"
        "v_mfma_f32_16x16x32_bf16 v[60:63], v[92:95], v[16:19], v[60:63]\n"
        "v_mfma_f32_16x16x32_bf16 v[64:67], v[96:99], v[16:19], v[64:67]\n"
        "ds_read_b128 v[16:19], v8 offset:17408\n"
        "s_waitcnt lgkmcnt(2)\n"
        "v_mfma_f32_16x16x32_bf16 v[44:47], v[100:103], v[20:23], v[44:47]\n"
        "s_waitcnt vmcnt(0)\n"
        "v_cmp_gt_i32 vcc, s70, v4\n"
        "v_mfma_f32_16x16x32_bf16 v[48:51], v[104:107], v[20:23], v[48:51]\n"
        "s_and_b64 vcc, vcc, 15\n"
        "v_mfma_f32_16x16x32_bf16 v[52:55], v[108:111], v[20:23], v[52:55]\n"
        "ds_read_b128 v[20:23], v8 offset:18432\n"
        "v_mfma_f32_16x16x32_bf16 v[56:59], v[100:103], v[24:27], v[56:59]\n"
        "v_mfma_f32_16x16x32_bf16 v[60:63], v[104:107], v[24:27], v[60:63]\n"
        "v_mfma_f32_16x16x32_bf16 v[64:67], v[108:111], v[24:27], v[64:67]\n"
        "ds_read_b128 v[24:27], v8 offset:19456\n"
        "s_waitcnt lgkmcnt(2)\n"
        "s_cbranch_vccnz .Lop_c30_%=\n"
        "s_branch .Lop_c31_%=\n"
        ".Lop_c20_%=:\n"
        "v_mfma_f32_16x16x32_bf16 v[44:47], a[192:195], v[12:15], v[44:47]\n"
        "v_mfma_f32_16x16x32_bf16 v[48:51], a[196:199], v[12:15], v[48:51]\n"
        "v_mfma_f32_16x16x32_bf16 v[52:55], a[200:203], v[12:15], v[52:55]\n"
        "ds_read_b128 v[12:15], v8 offset:4096\n"
        "v_mfma_f32_16x16x32_bf16 v[56:59], a[192:195], v[16:19], v[56:59]\n"
        "v_mfma_f32_16x16x32_bf16 v[60:63], a[196:199], v[16:19], v[60:63]\n"
        "v_mfma_f32_16x16x32_bf16 v[64:67], a[200:203], v[16:19], v[64:67]\n"
        "ds_read_b128 v[16:19], v8 offset:5120\n"
        "s_waitcnt lgkmcnt(2)\n"
        "v_mfma_f32_16x16x32_bf16 v[44:47], a[204:207], v[20:23], v[44:47]\n"
        "v_mfma_f32_16x16x32_bf16 v[48:51], a[208:211], v[20:23], v[48:51]\n"
        "v_mfma_f32_16x16x32_bf16 v[52:55], a[212:215], v[20:23], v[52:55]\n"
        "ds_read_b128 v[20:23], v8 offset:6144\n"
        "v_mfma_f32_16x16x32_bf16 v[56:59], a[204:207], v[24:27], v[56:59]\n"
        "v_mfma_f32_16x16x32_bf16 v[60:63], a[208:211], v[24:27], v[60:63]\n"
        "v_mfma_f32_16x16x32_bf16 v[64:67], a[212:215], v[24:27], v[64:67]\n"
        "ds_read_b128 v[24:27], v8 offset:7168\n"
        "s_waitcnt lgkmcnt(2)\n"
        "v_mfma_f32_16x16x32_bf16 v[44:47], a[216:219], v[12:15], v[44:47]\n"
        "buffer_load_dword v4, v2, s[40:43], s76 offen sc1\n"
        "v_mfma_f32_16x16x32_bf16 v[48:51], a[220:223], v[12:15], v[48:51]\n"
        "v_mfma_f32_16x16x32_bf16 v[52:55], a[224:227], v[12:15], v[52:55]\n"
        "ds_read_b128 v[12:15], v8 offset:8192\n"
        "v_mfma_f32_16x16x32_bf16 v[56:59], a[216:219], v[16:19], v[56:59]\n"
        "v_mfma_f32_16x16x32_bf16 v[60:63], a[220:223], v[16:19], v[60:63]\n"
        "v_mfma_f32_16x16x32_bf16 v[64:67], a[224:227], v[16:19], v[64:67]\n"
        "ds_read_b128 v[16:19], v8 offset:9216\n"
        "s_waitcnt lgkmcnt(2)\n"
        "v_mfma_f32_16x16x32_bf16 v[44:47], a[228:231], v[20:23], v[44:47]\n"
        "v_mfma_f32_16x16x32_bf16 v[48:51], a[232:235], v[20:23], v[48:51]\n"
        "v_mfma_f32_16x16x32_bf16 v[52:55], a[236:239], v[20:23], v[52:55]\n"
        "ds_read_b128 v[20:23], v8 offset:10240\n"
        "v_mfma_f32_16x16x32_bf16 v[56:59], a[228:231], v[24:27], v[56:59]\n"
        "v_mfma_f32_16x16x32_bf16 v[60:63], a[232:235], v[24:27], v[60:63]\n"
        "v_mfma_f32_16x16x32_bf16 v[64:67], a[236:239], v[24:27], v[64:67]\n"
        "ds_read_b128 v[24:27], v8 offset:11264\n"
        "s_waitcnt lgkmcnt(2)\n"
        "v_mfma_f32_16x16x32_bf16 v[44:47], a[240:243], v[12:15], v[44:47]\n"
        "v_mfma_f32_16x16x32_bf16 v[48:51], a[244:247], v[12:15], v[48:51]\n"
        "v_mfma_f32_16x16x32_bf16 v[52:55], a[248:251], v[12:15], v[52:55]\n"
        "ds_read_b128 v[12:15], v8 offset:12288\n"
        "v_mfma_f32_16x16x32_bf16 v[56:59], a[240:243], v[16:19], v[56:59]\n"
        "v_mfma_f32_16x16x32_bf16 v[60:63], a[244:247], v[16:19], v[60:63]\n"
        "v_mfma_f32_16x16x32_bf16 v[64:67], a[248:251], v[16:19], v[64:67]\n"
        "ds_read_b128 v[16:19], v8 offset:13312\n"
        "s_waitcnt lgkmcnt(2)\n"
        "v_mfma_f32_16x16x32_bf16 v[44:47], a[252:255], v[20:23], v[44:47]\n"
        "v_mfma_f32_16x16x32_bf16 v[48:51], v[80:83], v[20:23], v[48:51]\n"
        "v_mfma_f32_16x16x32_bf16 v[52:55], v[84:87], v[20:23], v[52:55]\n"
        "ds_read_b128 v[20:23], v8 offset:14336\n"
        "v_mfma_f32_16x16x32_bf16 v[56:59], a[252:255], v[24:27], v[56:59]\n"
        "v_mfma_f32_16x16x32_bf16 v[60:63], v[80:83], v[24:27], v[60:63]\n"
        "v_mfma_f32_16x16x32_bf16 v[64:67], v[84:87], v[24:27], v[64:67]\n"
        "ds_read_b128 v[24:27], v8 offset:15360\n"
        "s_waitcnt lgkmcnt(2)\n"
        "v_mfma_f32_16x16x32_bf16 v[44:47], v[88:91], v[12:15], v[44:47]\n"
        "v_mfma_f32_16x16x32_bf16 v[48:51], v[92:95], v[12:15], v[48:51]\n"
        "v_mfma_f32_16x16x32_bf16 v[52:55], v[96:99], v[12:15], v[52:55]\n"
        "v_mfma_f32_16x16x32_bf16 v[56:59], v[88:91], v[16:19], v[56:59]\n"
        "v_mfma_f32_16x16x32_bf16 v[60:63], v[92:95], v[16:19], v[60:63]\n"
        "v_mfma_f32_16x16x32_bf16 v[64:67], v[96:99], v[16:19], v[64:67]\n"
        "s_waitcnt lgkmcnt(0)\n"
        "v_mfma_f32_16x16x32_bf16 v[44:47], v[100:103], v[20:23], v[44:47]\n"
        "v_mfma_f32_16x16x32_bf16 v[48:51], v[104:107], v[20:23], v[48:51]\n"
        "v_mfma_f32_16x16x32_bf16 v[52:55], v[108:111], v[20:23], v[52:55]\n"
        "v_mfma_f32_16x16x32_bf16 v[56:59], v[100:103], v[24:27], v[56:59]\n"
        "v_mfma_f32_16x16x32_bf16 v[60:63], v[104:107], v[24:27], v[60:63]\n"
        "v_mfma_f32_16x16x32_bf16 v[64:67], v[108:111], v[24:27], v[64:67]\n"
        "s_waitcnt vmcnt(0)\n"
        "v_cmp_gt_i32 vcc, s70, v4\n"
        "s_nop 0\n"
        "s_and_b64 vcc, vcc, 15\n"
        "s_cbranch_vccnz .Lop_late2_%=\n"
        "s_add_u32 m0, s61, 49152\n"
        "s_add_u32 s71, s71, 32768\n"
        "buffer_load_dwordx4 v1, s[40:43], s71 offen offset:0 sc1 lds\n"
        "buffer_load_dwordx4 v1, s[40:43], s71 offen offset:1024 sc1 lds\n"
        "buffer_load_dwordx4 v1, s[40:43], s71 offen offset:2048 sc1 lds\n"
        "buffer_load_dwordx4 v1, s[40:43], s71 offen offset:3072 sc1 lds\n"
        "s_add_u32 s69, s69, 1\n"
        "s_cmp_lt_u32 s69, 64\n"
        "s_cselect_b32 s70, s69, 2147483647\n"
        "s_branch .Lop_go2_%=\n"
        ".Lop_late2_%=:\n"
        "s_mov_b32 s64, 0\n"
        ".Lop_in2_top_%=:\n"
        "buffer_load_dword v4, v2, s[40:43], s76 offen sc1\n"
        "s_waitcnt vmcnt(0)\n"
        "v_cmp_gt_i32 vcc, s70, v4\n"
        "s_nop 0\n"
        "s_and_b64 vcc, vcc, 15\n"
        "s_cbranch_vccz .Lop_in2_ok_%=\n"
        "s_add_u32 s64, s64, 1\n"
        "s_and_b32 s65, s64, 255\n"
        "s_cbranch_scc1 .Lop_in2_go_%=\n"
        "s_mov_b32 s65, 40960\n"
        "s_cmp_lt_u32 s64, 1048576\n"
        "s_cbranch_scc1 .Lop_in2_chk_%=\n"
        "v_mov_b32 v4, 1\n"
        "buffer_store_dword v4, off, s[40:43], s65 sc1\n"
        "s_waitcnt vmcnt(0)\n"
        ".Lop_in2_chk_%=:\n"
        "buffer_load_dword v4, off, s[40:43], s65 sc1\n"
        "s_waitcnt vmcnt(0)\n"
        "v_readfirstlane_b32 s65, v4\n"
        "s_nop 0\n"
        "s_cmp_lg_u32 s65, 1\n"
        "s_cbranch_scc1 .Lop_in2_go_%=\n"
        "s_endpgm\n"
        ".Lop_in2_go_%=:\n"
        "s_sleep 1\n"
        "s_branch .Lop_in2_top_%=\n"
        ".Lop_in2_ok_%=:\n"
        "s_add_u32 m0, s61, 49152\n"
        "s_add_u32 s71, s71, 32768\n"
        "buffer_load_dwordx4 v1, s[40:43], s71 offen offset:0 sc1 lds\n"
        "buffer_load_dwordx4 v1, s[40:43], s71 offen offset:1024 sc1 lds\n"
        "buffer_load_dwordx4 v1, s[40:43], s71 offen offset:2048 sc1 lds\n"
        "buffer_load_dwordx4 v1, s[40:43], s71 offen offset:3072 sc1 lds\n"
        "s_add_u32 s69, s69, 1\n"
        "s_cmp_lt_u32 s69, 64\n"
        "s_cselect_b32 s70, s69, 2147483647\n"
        ".Lop_go2_%=:\n"
        "buffer_load_dword v4, v2, s[40:43], s76 offen sc1\n"
        "s_waitcnt vmcnt(0)\n"
        "s_barrier\n"
        "ds_read_b128 v[12:15], v8 offset:16384\n"
        "ds_read_b128 v[16:19], v8 offset:17408\n"
        "ds_read_b128 v[20:23], v8 offset:18432\n"
        "ds_read_b128 v[24:27], v8 offset:19456\n"
        "v_cmp_gt_i32 vcc, s70, v4\n"
        "s_nop 0\n"
        "s_and_b64 vcc, vcc, 15\n"
        "s_waitcnt lgkmcnt(2)\n"
        "s_cbranch_vccnz .Lop_c30_%=\n"
        "s_branch .Lop_c31_%=\n"
        ".Lop_c31_%=:\n"
        "v_mfma_f32_16x16x32_bf16 v[44:47], v[112:115], v[12:15], v[44:47]\n"
        "s_add_u32 m0, s61, 0\n"
        "v_mfma_f32_16x16x32_bf16 v[48:51], v[116:119], v[12:15], v[48:51]\n"
        "s_bitcmp1_b64 s[52:53], s69\n"
        "s_cselect_b32 s65, 8192, 0\n"
        "v_mfma_f32_16x16x32_bf16 v[52:55], v[120:123], v[12:15], v[52:55]\n"
        "ds_read_b128 v[12:15], v8 offset:20480\n"
        "s_lshl_b32 s71, s69, 21\n"
        "v_mfma_f32_16x16x32_bf16 v[56:59], v[112:115], v[16:19], v[56:59]\n"
        "s_add_u32 s71, s71, s65\n"
        "s_add_u32 s71, s71, s58\n"
        "v_mfma_f32_16x16x32_bf16 v[60:63], v[116:119], v[16:19], v[60:63]\n"
        "buffer_load_dwordx4 v1, s[40:43], s71 offen offset:0 sc1 lds\n"
        "buffer_load_dwordx4 v1, s[40:43], s71 offen offset:1024 sc1 lds\n"
        "v_mfma_f32_16x16x32_bf16 v[64:67], v[120:123], v[16:19], v[64:67]\n"
        "ds_read_b128 v[16:19], v8 offset:21504\n"
        "s_waitcnt lgkmcnt(2)\n"
        "v_mfma_f32_16x16x32_bf16 v[44:47], v[124:127], v[20:23], v[44:47]\n"
        "v_mfma_f32_16x16x32_bf16 v[48:51], v[128:131], v[20:23], v[48:51]\n"
        "buffer_load_dwordx4 v1, s[40:43], s71 offen offset:2048 sc1 lds\n"
        "v_mfma_f32_16x16x32_bf16 v[52:55], v[132:135], v[20:23], v[52:55]\n"
        "ds_read_b128 v[20:23], v8 offset:22528\n"
        "v_mfma_f32_16x16x32_bf16 v[56:59], v[124:127], v[24:27], v[56:59]\n"
        "v_mfma_f32_16x16x32_bf16 v[60:63], v[128:131], v[24:27], v[60:63]\n"
        "buffer_load_dwordx4 v1, s[40:43], s71 offen offset:3072 sc1 lds\n"
        "v_mfma_f32_16x16x32_bf16 v[64:67], v[132:135], v[24:27], v[64:67]\n"
        "ds_read_b128 v[24:27], v8 offset:23552\n"
        "s_waitcnt lgkmcnt(2)\n"
        "v_mfma_f32_16x16x32_bf16 v[44:47], v[136:139], v[12:15], v[44:47]\n"
        "buffer_load_dword v4, v2, s[40:43], s76 offen sc1\n"
        "v_mfma_f32_16x16x32_bf16 v[48:51], v[140:143], v[12:15], v[48:51]\n"
        "v_mfma_f32_16x16x32_bf16 v[52:55], v[144:147], v[12:15], v[52:55]\n"
        "ds_read_b128 v[12:15], v8 offset:24576\n"
        "v_mfma_f32_16x16x32_bf16 v[56:59], v[136:139], v[16:19], v[56:59]\n"
        "v_mfma_f32_16x16x32_bf16 v[60:63], v[140:143], v[16:19], v[60:63]\n"
        "v_mfma_f32_16x16x32_bf16 v[64:67], v[144:147], v[16:19], v[64:67]\n"
        "ds_read_b128 v[16:19], v8 offset:25600\n"
        "s_waitcnt lgkmcnt(2)\n"
        "v_mfma_f32_16x16x32_bf16 v[44:47], v[148:151], v[20:23], v[44:47]\n"
        "v_mfma_f32_16x16x32_bf16 v[48:51], v[152:155], v[20:23], v[48:51]\n"
        "v_mfma_f32_16x16x32_bf16 v[52:55], v[156:159], v[20:23], v[52:55]\n"
        "ds_read_b128 v[20:23], v8 offset:26624\n"
        "v_mfma_f32_16x16x32_bf16 v[56:59], v[148:151], v[24:27], v[56:59]\n"
        "v_mfma_f32_16x16x32_bf16 v[60:63], v[152:155], v[24:27], v[60:63]\n"
        "v_mfma_f32_16x16x32_bf16 v[64:67], v[156:159], v[24:27], v[64:67]\n"
        "ds_read_b128 v[24:27], v8 offset:27648\n"
        "s_waitcnt lgkmcnt(2)\n"
        "v_mfma_f32_16x16x32_bf16 v[44:47], v[160:163], v[12:15], v[44:47]\n"
        "v_mfma_f32_16x16x32_bf16 v[48:51], v[164:167], v[12:15], v[48:51]\n"
        "v_mfma_f32_16x16x32_bf16 v[52:55], v[168:171], v[12:15], v[52:55]\n"
        "ds_read_b128 v[12:15], v8 offset:28672\n"
        "v_mfma_f32_16x16x32_bf16 v[56:59], v[160:163], v[16:19], v[56:59]\n"
        "v_mfma_f32_16x16x32_bf16 v[60:63], v[164:167], v[16:19], v[60:63]\n"
        "v_mfma_f32_16x16x32_bf16 v[64:67], v[168:171], v[16:19], v[64:67]\n"
        "ds_read_b128 v[16:19], v8 offset:29696\n"
        "s_waitcnt lgkmcnt(2)\n"
        "v_mfma_f32_16x16x32_bf16 v[44:47], v[172:175], v[20:23], v[44:47]\n"
        "v_mfma_f32_16x16x32_bf16 v[48:51], v[176:179], v[20:23], v[48:51]\n"
        "v_mfma_f32_16x16x32_bf16 v[52:55], v[180:183], v[20:23], v[52:55]\n"
        "ds_read_b128 v[20:23], v8 offset:30720\n"
        "v_mfma_f32_16x16x32_bf16 v[56:59], v[172:175], v[24:27], v[56:59]\n"
        "v_mfma_f32_16x16x32_bf16 v[60:63], v[176:179], v[24:27], v[60:63]\n"
        "v_mfma_f32_16x16x32_bf16 v[64:67], v[180:183], v[24:27], v[64:67]\n"
        "ds_read_b128 v[24:27], v8 offset:31744\n"
        "s_waitcnt lgkmcnt(2)\n"
        "v_mfma_f32_16x16x32_bf16 v[44:47], v[184:187], v[12:15], v[44:47]\n"
        "v_mfma_f32_16x16x32_bf16 v[48:51], v[188:191], v[12:15], v[48:51]\n"
        "s_waitcnt vmcnt(1)\n"
        "s_barrier\n"
        "v_mfma_f32_16x16x32_bf16 v[52:55], v[192:195], v[12:15], v[52:55]\n"
        "ds_read_b128 v[12:15], v0 offset:0\n"
        "v_mfma_f32_16x16x32_bf16 v[56:59], v[184:187], v[16:19], v[56:59]\n"
        "v_mfma_f32_16x16x32_bf16 v[60:63], v[188:191], v[16:19], v[60:63]\n"
        "v_mfma_f32_16x16x32_bf16 v[64:67], v[192:195], v[16:19], v[64:67]\n"
        "ds_read_b128 v[16:19], v0 offset:1024\n"
        "s_waitcnt lgkmcnt(2)\n"
        "v_mfma_f32_16x16x32_bf16 v[44:47], v[196:199], v[20:23], v[44:47]\n"
        "s_waitcnt vmcnt(0)\n"
        "v_cmp_gt_i32 vcc, s70, v4\n"
        "v_mfma_f32_16x16x32_bf16 v[48:51], v[200:203], v[20:23], v[48:51]\n"
        "s_and_b64 vcc, vcc, 15\n"
        "v_mfma_f32_16x16x32_bf16 v[52:55], v[204:207], v[20:23], v[52:55]\n"
        "ds_read_b128 v[20:23], v0 offset:2048\n"
        "v_cvt_pk_bf16_f32 v68, v44, v45\n"
        "v_cvt_pk_bf16_f32 v69, v46, v47\n"
        "v_mfma_f32_16x16x32_bf16 v[56:59], v[196:199], v[24:27], v[56:59]\n"
        "v_cvt_pk_bf16_f32 v70, v48, v49\n"
        "v_cvt_pk_bf16_f32 v71, v50, v51\n"
        "v_mfma_f32_16x16x32_bf16 v[60:63], v[200:203], v[24:27], v[60:63]\n"
        "v_cvt_pk_bf16_f32 v76, v52, v53\n"
        "v_cvt_pk_bf16_f32 v77, v54, v55\n"
        "v_mfma_f32_16x16x32_bf16 v[64:67], v[204:207], v[24:27], v[64:67]\n"
        "ds_read_b128 v[24:27], v0 offset:3072\n"
        "s_waitcnt lgkmcnt(2)\n"
        "v_cvt_pk_bf16_f32 v72, v56, v57\n"
        "v_cvt_pk_bf16_f32 v73, v58, v59\n"
        "s_cbranch_vccnz .Lop_c00_%=\n"
        "s_branch .Lop_c01_%=\n"
        ".Lop_c30_%=:\n"
        "v_mfma_f32_16x16x32_bf16 v[44:47], v[112:115], v[12:15], v[44:47]\n"
        "v_mfma_f32_16x16x32_bf16 v[48:51], v[116:119], v[12:15], v[48:51]\n"
        "v_mfma_f32_16x16x32_bf16 v[52:55], v[120:123], v[12:15], v[52:55]\n"
        "ds_read_b128 v[12:15], v8 offset:20480\n"
        "v_mfma_f32_16x16x32_bf16 v[56:59], v[112:115], v[16:19], v[56:59]\n"
        "v_mfma_f32_16x16x32_bf16 v[60:63], v[116:119], v[16:19], v[60:63]\n"
        "v_mfma_f32_16x16x32_bf16 v[64:67], v[120:123], v[16:19], v[64:67]\n"
        "ds_read_b128 v[16:19], v8 offset:21504\n"
        "s_waitcnt lgkmcnt(2)\n"
        "v_mfma_f32_16x16x32_bf16 v[44:47], v[124:127], v[20:23], v[44:47]\n"
        "v_mfma_f32_16x16x32_bf16 v[48:51], v[128:131], v[20:23], v[48:51]\n"
        "v_mfma_f32_16x16x32_bf16 v[52:55], v[132:135], v[20:23], v[52:55]\n"
        "ds_read_b128 v[20:23], v8 offset:22528\n"
        "v_mfma_f32_16x16x32_bf16 v[56:59], v[124:127], v[24:27], v[56:59]\n"
        "v_mfma_f32_16x16x32_bf16 v[60:63], v[128:131], v[24:27], v[60:63]\n"
        "v_mfma_f32_16x16x32_bf16 v[64:67], v[132:135], v[24:27], v[64:67]\n"
        "ds_read_b128 v[24:27], v8 offset:23552\n"
        "s_waitcnt lgkmcnt(2)\n"
        "v_mfma_f32_16x16x32_bf16 v[44:47], v[136:139], v[12:15], v[44:47]\n"
        "buffer_load_dword v4, v2, s[40:43], s76 offen sc1\n"
        "v_mfma_f32_16x16x32_bf16 v[48:51], v[140:143], v[12:15], v[48:51]\n"
        "v_mfma_f32_16x16x32_bf16 v[52:55], v[144:147], v[12:15], v[52:55]\n"
        "ds_read_b128 v[12:15], v8 offset:24576\n"
        "v_mfma_f32_16x16x32_bf16 v[56:59], v[136:139], v[16:19], v[56:59]\n"
        "v_mfma_f32_16x16x32_bf16 v[60:63], v[140:143], v[16:19], v[60:63]\n"
        "v_mfma_f32_16x16x32_bf16 v[64:67], v[144:147], v[16:19], v[64:67]\n"
        "ds_read_b128 v[16:19], v8 offset:25600\n"
        "s_waitcnt lgkmcnt(2)\n"
        "v_mfma_f32_16x16x32_bf16 v[44:47], v[148:151], v[20:23], v[44:47]\n"
        "v_mfma_f32_16x16x32_bf16 v[48:51], v[152:155], v[20:23], v[48:51]\n"
        "v_mfma_f32_16x16x32_bf16 v[52:55], v[156:159], v[20:23], v[52:55]\n"
        "ds_read_b128 v[20:23], v8 offset:26624\n"
        "v_mfma_f32_16x16x32_bf16 v[56:59], v[148:151], v[24:27], v[56:59]\n"
        "v_mfma_f32_16x16x32_bf16 v[60:63], v[152:155], v[24:27], v[60:63]\n"
        "v_mfma_f32_16x16x32_bf16 v[64:67], v[156:159], v[24:27], v[64:67]\n"
        "ds_read_b128 v[24:27], v8 offset:27648\n"
        "s_waitcnt lgkmcnt(2)\n"
        "v_mfma_f32_16x16x32_bf16 v[44:47], v[160:163], v[12:15], v[44:47]\n"
        "v_mfma_f32_16x16x32_bf16 v[48:51], v[164:167], v[12:15], v[48:51]\n"
        "v_mfma_f32_16x16x32_bf16 v[52:55], v[168:171], v[12:15], v[52:55]\n"
        "ds_read_b128 v[12:15], v8 offset:28672\n"
        "v_mfma_f32_16x16x32_bf16 v[56:59], v[160:163], v[16:19], v[56:59]\n"
        "v_mfma_f32_16x16x32_bf16 v[60:63], v[164:167], v[16:19], v[60:63]\n"
        "v_mfma_f32_16x16x32_bf16 v[64:67], v[168:171], v[16:19], v[64:67]\n"
        "ds_read_b128 v[16:19], v8 offset:29696\n"
        "s_waitcnt lgkmcnt(2)\n"
        "v_mfma_f32_16x16x32_bf16 v[44:47], v[172:175], v[20:23], v[44:47]\n"
        "v_mfma_f32_16x16x32_bf16 v[48:51], v[176:179], v[20:23], v[48:51]\n"
        "v_mfma_f32_16x16x32_bf16 v[52:55], v[180:183], v[20:23], v[52:55]\n"
        "ds_read_b128 v[20:23], v8 offset:30720\n"
        "v_mfma_f32_16x16x32_bf16 v[56:59], v[172:175], v[24:27], v[56:59]\n"
        "v_mfma_f32_16x16x32_bf16 v[60:63], v[176:179], v[24:27], v[60:63]\n"
        "v_mfma_f32_16x16x32_bf16 v[64:67], v[180:183], v[24:27], v[64:67]\n"
        "ds_read_b128 v[24:27], v8 offset:31744\n"
        "s_waitcnt lgkmcnt(2)\n"
        "v_mfma_f32_16x16x32_bf16 v[44:47], v[184:187], v[12:15], v[44:47]\n"
        "v_mfma_f32_16x16x32_bf16 v[48:51], v[188:191], v[12:15], v[48:51]\n"
        "v_mfma_f32_16x16x32_bf16 v[52:55], v[192:195], v[12:15], v[52:55]\n"
        "v_mfma_f32_16x16x32_bf16 v[56:59], v[184:187], v[16:19], v[56:59]\n"
        "v_mfma_f32_16x16x32_bf16 v[60:63], v[188:191], v[16:19], v[60:63]\n"
        "v_mfma_f32_16x16x32_bf16 v[64:67], v[192:195], v[16:19], v[64:67]\n"
        "s_waitcnt lgkmcnt(0)\n"
        "v_mfma_f32_16x16x32_bf16 v[44:47], v[196:199], v[20:23], v[44:47]\n"
        "v_mfma_f32_16x16x32_bf16 v[48:51], v[200:203], v[20:23], v[48:51]\n"
        "v_mfma_f32_16x16x32_bf16 v[52:55], v[204:207], v[20:23], v[52:55]\n"
        "v_cvt_pk_bf16_f32 v68, v44, v45\n"
        "v_cvt_pk_bf16_f32 v69, v46, v47\n"
        "v_mfma_f32_16x16x32_bf16 v[56:59], v[196:199], v[24:27], v[56:59]\n"
        "v_cvt_pk_bf16_f32 v70, v48, v49\n"
        "v_cvt_pk_bf16_f32 v71, v50, v51\n"
        "v_mfma_f32_16x16x32_bf16 v[60:63], v[200:203], v[24:27], v[60:63]\n"
        "v_cvt_pk_bf16_f32 v76, v52, v53\n"
        "v_cvt_pk_bf16_f32 v77, v54, v55\n"
        "v_mfma_f32_16x16x32_bf16 v[64:67], v[204:207], v[24:27], v[64:67]\n"
        "v_cvt_pk_bf16_f32 v72, v56, v57\n"
        "v_cvt_pk_bf16_f32 v73, v58, v59\n"
        "s_nop 5\n"
        "v_cvt_pk_bf16_f32 v74, v60, v61\n"
        "v_cvt_pk_bf16_f32 v75, v62, v63\n"
        "v_cvt_pk_bf16_f32 v78, v64, v65\n"
        "v_cvt_pk_bf16_f32 v79, v66, v67\n"
        "buffer_store_dwordx4 v[68:71], v6, s[40:43], s72 offen sc1\n"
        "s_add_u32 s66, s72, 147456\n"
        "buffer_store_dwordx4 v[72:75], v6, s[40:43], s66 offen sc1\n"
        "v_permlane16_swap_b32 v76, v78\n"
        "v_permlane16_swap_b32 v77, v79\n"
        "buffer_store_dwordx4 v[76:79], v7, s[40:43], s72 offen sc1\n"
        "s_waitcnt vmcnt(3)\n"
        "v_cmp_gt_i32 vcc, s70, v4\n"
        "s_nop 0\n"
        "s_and_b64 vcc, vcc, 15\n"
        "s_cbranch_vccnz .Lop_late3_%=\n"
        "s_add_u32 m0, s61, 0\n"
        "s_bitcmp1_b64 s[52:53], s69\n"
        "s_cselect_b32 s65, 8192, 0\n"
        "s_lshl_b32 s71, s69, 21\n"
        "s_add_u32 s71, s71, s65\n"
        "s_add_u32 s71, s71, s58\n"
        "buffer_load_dwordx4 v1, s[40:43], s71 offen offset:0 sc1 lds\n"
        "buffer_load_dwordx4 v1, s[40:43], s71 offen offset:1024 sc1 lds\n"
        "buffer_load_dwordx4 v1, s[40:43], s71 offen offset:2048 sc1 lds\n"
        "buffer_load_dwordx4 v1, s[40:43], s71 offen offset:3072 sc1 lds\n"
        "s_waitcnt vmcnt(4)\n"
        "v_mov_b32 v5, s74\n"
        "buffer_store_dword v5, v3, s[40:43], s73 offen sc1\n"
        "s_branch .Lop_go3_%=\n"
        ".Lop_late3_%=:\n"
        "s_waitcnt vmcnt(0)\n"
        "v_mov_b32 v5, s74\n"
        "buffer_store_dword v5, v3, s[40:43], s73 offen sc1\n"
        "s_cmp_eq_u32 s68, 63\n"
        "s_cbranch_scc0 .Lop_wait3b_%=\n"
        "s_endpgm\n"
        ".Lop_wait3_%=:\n"
        "s_mov_b32 s70, 0\n"
        ".Lop_wait3b_%=:\n"
        "s_mov_b32 s64, 0\n"
        ".Lop_in3_top_%=:\n"
        "buffer_load_dword v4, v2, s[40:43], s76 offen sc1\n"
        "s_waitcnt vmcnt(0)\n"
        "v_cmp_gt_i32 vcc, s70, v4\n"
        "s_nop 0\n"
        "s_and_b64 vcc, vcc, 15\n"
        "s_cbranch_vccz .Lop_in3_ok_%=\n"
        "s_add_u32 s64, s64, 1\n"
        "s_and_b32 s65, s64, 255\n"
        "s_cbranch_scc1 .Lop_in3_go_%=\n"
        "s_mov_b32 s65, 40960\n"
        "s_cmp_lt_u32 s64, 1048576\n"
        "s_cbranch_scc1 .Lop_in3_chk_%=\n"
        "v_mov_b32 v4, 1\n"
        "buffer_store_dword v4, off, s[40:43], s65 sc1\n"
        "s_waitcnt vmcnt(0)\n"
        ".Lop_in3_chk_%=:\n"
        "buffer_load_dword v4, off, s[40:43], s65 sc1\n"
        "s_waitcnt vmcnt(0)\n"
        "v_readfirstlane_b32 s65, v4\n"
        "s_nop 0\n"
        "s_cmp_lg_u32 s65, 1\n"
        "s_cbranch_scc1 .Lop_in3_go_%=\n"
        "s_endpgm\n"
        ".Lop_in3_go_%=:\n"
        "s_sleep 1\n"
        "s_branch .Lop_in3_top_%=\n"
        ".Lop_in3_ok_%=:\n"
        "s_add_u32 m0, s61, 0\n"
        "s_bitcmp1_b64 s[52:53], s69\n"
        "s_cselect_b32 s65, 8192, 0\n"
        "s_lshl_b32 s71, s69, 21\n"
        "s_add_u32 s71, s71, s65\n"
        "s_add_u32 s71, s71, s58\n"
        "buffer_load_dwordx4 v1, s[40:43], s71 offen offset:0 sc1 lds\n"
        "buffer_load_dwordx4 v1, s[40:43], s71 offen offset:1024 sc1 lds\n"
        "buffer_load_dwordx4 v1, s[40:43], s71 offen offset:2048 sc1 lds\n"
        "buffer_load_dwordx4 v1, s[40:43], s71 offen offset:3072 sc1 lds\n"
        ".Lop_go3_%=:\n"
        "s_mov_b32 s72, 241172480\n"
        "s_mov_b32 s73, 241172480\n"
        "buffer_load_dword v4, v2, s[40:43], s76 offen sc1\n"
        "s_waitcnt vmcnt(0)\n"
        "s_barrier\n"
        "ds_read_b128 v[12:15], v0 offset:0\n"
        "ds_read_b128 v[16:19], v0 offset:1024\n"
        "ds_read_b128 v[20:23], v0 offset:2048\n"
        "ds_read_b128 v[24:27], v0 offset:3072\n"
        "v_cmp_gt_i32 vcc, s70, v4\n"
        "s_nop 0\n"
        "s_and_b64 vcc, vcc, 15\n"
        "s_waitcnt lgkmcnt(2)\n"
        "s_cbranch_vccnz .Lop_c00_%=\n"
        "s_branch .Lop_c01_%=\n"
            :: "s"(p.ws), "s"(p.Wo), "s"(p.Wo), "s"(p.Wo), "s"(pair(w, delay)), "s"(pair(x4, pr)),
               "s"(pair(PAGE + spread(A_Q) + (8 * kap + (w >> 1)) * 16384 + (w & 1) * 4096, 65 * PAGE + spread(B_P3))), "s"(pair(flag(F_OP + fl), w * 4096)),
               "v"(pair(lane * 16, lane * 16)), "v"(pair(lane < 4 ? flag(F_AT + (8 * kap + 2 * lane + (w >> 1)) * 6 + 2 * nu + (w & 1)) : OOB, fo4)), "v"(pair(tr + 64 + 8 * pp, 0)),
               "v"(pair(s01, s2)), "v"(pair(vo[0], vo[1])), "v"(pair(vo[2], tr + 16 * pp))
            : "v0", "v1", "v2", "v3", "v4", "v5", "v6", "v7", "v8", "v9", "v10", "v11", "v12", "v13", "v14", "v15", "v16", "v17", "v18", "v19", "v20", "v21", "v22", "v23", "v24", "v25", "v26", "v27", "v28", "v29", "v30", "v31", "v32", "v33", "v34", "v35", "v36", "v37", "v38", "v39", "v40", "v41", "v42", "v43", "v44", "v45", "v46", "v47", "v48", "v49", "v50", "v51", "v52", "v53", "v54", "v55", "v56", "v57", "v58", "v59", "v60", "v61", "v62", "v63", "v64", "v65", "v66", "v67", "v68", "v69", "v70", "v71", "v72", "v73", "v74", "v75", "v76", "v77", "v78", "v79", "v255", "a255", "s30", "s31", "s32", "s33", "s34", "s35", "s36", "s37", "s38", "s39", "s40", "s41", "s42", "s43", "s44", "s45", "s46", "s47", "s48", "s49", "s50", "s51", "s52", "s53", "s54", "s55", "s56", "s57", "s58", "s59", "s60", "s61", "s62", "s63", "s64", "s65", "s66", "s67", "s68", "s69", "s70", "s71", "s72", "s73", "s74", "s75", "s76", "s77", "s78", "s79", "s80", "s81", "s82", "s83", "s84", "s85", "s86", "s87", "s88", "s89", "s90", "s91", "s92", "s93", "s94", "s95", "s96", "s97", "s98", "s99", "vcc", "scc", "memory");
    } else {
        asm volatile(
        "s_nop 4\n"
        "s_mov_b64 s[40:41], %0\n"
        "s_and_b32 s41, s41, 0xffff\n"
        "s_mov_b32 s42, 236978176\n"
        "s_mov_b32 s43, 0x20000\n"
        "s_mov_b64 s[48:49], %4\n"
        "s_mov_b64 s[56:57], %5\n"
        "s_mov_b64 s[58:59], %6\n"
        "s_mov_b64 s[60:61], %7\n"
        "v_mov_b64 v[0:1], %8\n"
        "v_mov_b64 v[2:3], %9\n"
        "v_mov_b64 v[4:5], %10\n"
        "v_mov_b64 v[6:7], %11\n"
        "v_mov_b64 v[8:9], %12\n"
        "v_mov_b64 v[10:11], %13\n"
        "s_mov_b32 s47, 0x20000\n"
        "s_mul_i32 s79, s48, 40960\n"
        ".Lf2_start_%=:\n"
        "s_sub_u32 s49, s49, 1\n"
        "s_cbranch_scc1 .Lf2_start2_%=\n"
        "s_sleep 16\n"
        "s_branch .Lf2_start_%=\n"
        ".Lf2_start2_%=:\n"
        "s_mov_b64 s[44:45], %1\n"
        "s_and_b32 s45, s45, 0xffff\n"
        "s_mov_b32 s46, 5308416\n"
        "s_add_u32 m0, s79, 0\n"
        "s_mov_b32 s65, 0\n"
        "buffer_load_dwordx4 v8, s[44:47], s65 offen lds\n"
        "buffer_load_dwordx4 v9, s[44:47], s65 offen offset:1024 lds\n"
        "buffer_load_dwordx4 v10, s[44:47], s65 offen offset:2048 lds\n"
        "s_add_u32 m0, s79, 3072\n"
        "s_mov_b32 s65, 110592\n"
        "buffer_load_dwordx4 v8, s[44:47], s65 offen lds\n"
        "buffer_load_dwordx4 v9, s[44:47], s65 offen offset:1024 lds\n"
        "buffer_load_dwordx4 v10, s[44:47], s65 offen offset:2048 lds\n"
        "s_add_u32 m0, s79, 6144\n"
        "s_mov_b32 s65, 221184\n"
        "buffer_load_dwordx4 v8, s[44:47], s65 offen lds\n"
        "buffer_load_dwordx4 v9, s[44:47], s65 offen offset:1024 lds\n"
        "buffer_load_dwordx4 v10, s[44:47], s65 offen offset:2048 lds\n"
        "s_add_u32 m0, s79, 9216\n"
        "s_mov_b32 s65, 331776\n"
        "buffer_load_dwordx4 v8, s[44:47], s65 offen lds\n"
        "buffer_load_dwordx4 v9, s[44:47], s65 offen offset:1024 lds\n"
        "buffer_load_dwordx4 v10, s[44:47], s65 offen offset:2048 lds\n"
        "s_add_u32 m0, s79, 12288\n"
        "s_mov_b32 s65, 442368\n"
        "buffer_load_dwordx4 v8, s[44:47], s65 offen lds\n"
        "buffer_load_dwordx4 v9, s[44:47], s65 offen offset:1024 lds\n"
        "buffer_load_dwordx4 v10, s[44:47], s65 offen offset:2048 lds\n"
        "s_add_u32 m0, s79, 15360\n"
        "s_mov_b32 s65, 552960\n"
        "buffer_load_dwordx4 v8, s[44:47], s65 offen lds\n"
        "buffer_load_dwordx4 v9, s[44:47], s65 offen offset:1024 lds\n"
        "buffer_load_dwordx4 v10, s[44:47], s65 offen offset:2048 lds\n"
        "s_add_u32 m0, s79, 18432\n"
        "s_mov_b32 s65, 663552\n"
        "buffer_load_dwordx4 v8, s[44:47], s65 offen lds\n"
        "buffer_load_dwordx4 v9, s[44:47], s65 offen offset:1024 lds\n"
        "buffer_load_dwordx4 v10, s[44:47], s65 offen offset:2048 lds\n"
        "s_add_u32 m0, s79, 21504\n"
        "s_mov_b32 s65, 774144\n"
        "buffer_load_dwordx4 v8, s[44:47], s65 offen lds\n"
        "buffer_load_dwordx4 v9, s[44:47], s65 offen offset:1024 lds\n"
        "buffer_load_dwordx4 v10, s[44:47], s65 offen offset:2048 lds\n"
        "s_add_u32 m0, s79, 24576\n"
        "s_mov_b32 s65, 884736\n"
        "buffer_load_dwordx4 v8, s[44:47], s65 offen lds\n"
        "buffer_load_dwordx4 v9, s[44:47], s65 offen offset:1024 lds\n"
        "buffer_load_dwordx4 v10, s[44:47], s65 offen offset:2048 lds\n"
        "s_add_u32 m0, s79, 27648\n"
        "s_mov_b32 s65, 995328\n"
        "buffer_load_dwordx4 v8, s[44:47], s65 offen lds\n"
        "buffer_load_dwordx4 v9, s[44:47], s65 offen offset:1024 lds\n"
        "buffer_load_dwordx4 v10, s[44:47], s65 offen offset:2048 lds\n"
        "s_add_u32 m0, s79, 30720\n"
        "s_mov_b32 s65, 1105920\n"
        "buffer_load_dwordx4 v8, s[44:47], s65 offen lds\n"
        "buffer_load_dwordx4 v9, s[44:47], s65 offen offset:1024 lds\n"
        "buffer_load_dwordx4 v10, s[44:47], s65 offen offset:2048 lds\n"
        "s_add_u32 m0, s79, 33792\n"
        "s_mov_b32 s65, 1216512\n"
        "buffer_load_dwordx4 v8, s[44:47], s65 offen lds\n"
        "buffer_load_dwordx4 v9, s[44:47], s65 offen offset:1024 lds\n"
        "buffer_load_dwordx4 v10, s[44:47], s65 offen offset:2048 lds\n"
        "s_waitcnt vmcnt(33)\n"
        "ds_read_b64_tr_b16 a[0:1], v11 offset:0\n"
        "ds_read_b64_tr_b16 a[2:3], v11 offset:1536\n"
        "ds_read_b64_tr_b16 a[4:5], v11 offset:8\n"
        "ds_read_b64_tr_b16 a[6:7], v11 offset:1544\n"
        "ds_read_b64_tr_b16 a[8:9], v4 offset:0\n"
        "ds_read_b64_tr_b16 a[10:11], v4 offset:1536\n"
        "s_waitcnt vmcnt(30)\n"
        "ds_read_b64_tr_b16 a[12:13], v11 offset:3072\n"
        "ds_read_b64_tr_b16 a[14:15], v11 offset:4608\n"
        "ds_read_b64_tr_b16 a[16:17], v11 offset:3080\n"
        "ds_read_b64_tr_b16 a[18:19], v11 offset:4616\n"
        "ds_read_b64_tr_b16 a[20:21], v4 offset:3072\n"
        "ds_read_b64_tr_b16 a[22:23], v4 offset:4608\n"
        "s_waitcnt lgkmcnt(6)\n"
        "s_add_u32 m0, s79, 0\n"
        "s_mov_b32 s65, 18432\n"
        "buffer_load_dwordx4 v8, s[44:47], s65 offen lds\n"
        "buffer_load_dwordx4 v9, s[44:47], s65 offen offset:1024 lds\n"
        "buffer_load_dwordx4 v10, s[44:47], s65 offen offset:2048 lds\n"
        "s_waitcnt vmcnt(30)\n"
        "ds_read_b64_tr_b16 a[24:25], v11 offset:6144\n"
        "ds_read_b64_tr_b16 a[26:27], v11 offset:7680\n"
        "ds_read_b64_tr_b16 a[28:29], v11 offset:6152\n"
        "ds_read_b64_tr_b16 a[30:31], v11 offset:7688\n"
        "ds_read_b64_tr_b16 a[32:33], v4 offset:6144\n"
        "ds_read_b64_tr_b16 a[34:35], v4 offset:7680\n"
        "s_waitcnt lgkmcnt(6)\n"
        "s_add_u32 m0, s79, 3072\n"
        "s_mov_b32 s65, 129024\n"
        "buffer_load_dwordx4 v8, s[44:47], s65 offen lds\n"
        "buffer_load_dwordx4 v9, s[44:47], s65 offen offset:1024 lds\n"
        "buffer_load_dwordx4 v10, s[44:47], s65 offen offset:2048 lds\n"
        "s_waitcnt vmcnt(30)\n"
        "ds_read_b64_tr_b16 a[36:37], v11 offset:9216\n"
        "ds_read_b64_tr_b16 a[38:39], v11 offset:10752\n"
        "ds_read_b64_tr_b16 a[40:41], v11 offset:9224\n"
        "ds_read_b64_tr_b16 a[42:43], v11 offset:10760\n"
        "ds_read_b64_tr_b16 a[44:45], v4 offset:9216\n"
        "ds_read_b64_tr_b16 a[46:47], v4 offset:10752\n"
        "s_waitcnt lgkmcnt(6)\n"
        "s_add_u32 m0, s79, 6144\n"
        "s_mov_b32 s65, 239616\n"
        "buffer_load_dwordx4 v8, s[44:47], s65 offen lds\n"
        "buffer_load_dwordx4 v9, s[44:47], s65 offen offset:1024 lds\n"
        "buffer_load_dwordx4 v10, s[44:47], s65 offen offset:2048 lds\n"
        "s_waitcnt vmcnt(30)\n"
        "ds_read_b64_tr_b16 a[48:49], v11 offset:12288\n"
        "ds_read_b64_tr_b16 a[50:51], v11 offset:13824\n"
        "ds_read_b64_tr_b16 a[52:53], v11 offset:12296\n"
        "ds_read_b64_tr_b16 a[54:55], v11 offset:13832\n"
        "ds_read_b64_tr_b16 a[56:57], v4 offset:12288\n"
        "ds_read_b64_tr_b16 a[58:59], v4 offset:13824\n"
        "s_waitcnt lgkmcnt(6)\n"
        "s_add_u32 m0, s79, 9216\n"
        "s_mov_b32 s65, 350208\n"
        "buffer_load_dwordx4 v8, s[44:47], s65 offen lds\n"
        "buffer_load_dwordx4 v9, s[44:47], s65 offen offset:1024 lds\n"
        "buffer_load_dwordx4 v10, s[44:47], s65 offen offset:2048 lds\n"
        "s_waitcnt vmcnt(30)\n"
        "ds_read_b64_tr_b16 a[60:61], v11 offset:15360\n"
        "ds_read_b64_tr_b16 a[62:63], v11 offset:16896\n"
        "ds_read_b64_tr_b16 a[64:65], v11 offset:15368\n"
        "ds_read_b64_tr_b16 a[66:67], v11 offset:16904\n"
        "ds_read_b64_tr_b16 a[68:69], v4 offset:15360\n"
        "ds_read_b64_tr_b16 a[70:71], v4 offset:16896\n"
        "s_waitcnt lgkmcnt(6)\n"
        "s_add_u32 m0, s79, 12288\n"
        "s_mov_b32 s65, 460800\n"
        "buffer_load_dwordx4 v8, s[44:47], s65 offen lds\n"
        "buffer_load_dwordx4 v9, s[44:47], s65 offen offset:1024 lds\n"
        "buffer_load_dwordx4 v10, s[44:47], s65 offen offset:2048 lds\n"
        "s_waitcnt vmcnt(30)\n"
        "ds_read_b64_tr_b16 a[72:73], v11 offset:18432\n"
        "ds_read_b64_tr_b16 a[74:75], v11 offset:19968\n"
        "ds_read_b64_tr_b16 a[76:77], v11 offset:18440\n"
        "ds_read_b64_tr_b16 a[78:79], v11 offset:19976\n"
        "ds_read_b64_tr_b16 a[80:81], v4 offset:18432\n"
        "ds_read_b64_tr_b16 a[82:83], v4 offset:19968\n"
        "s_waitcnt lgkmcnt(6)\n"
        "s_add_u32 m0, s79, 15360\n"
        "s_mov_b32 s65, 571392\n"
        "buffer_load_dwordx4 v8, s[44:47], s65 offen lds\n"
        "buffer_load_dwordx4 v9, s[44:47], s65 offen offset:1024 lds\n"
        "buffer_load_dwordx4 v10, s[44:47], s65 offen offset:2048 lds\n"
        "s_waitcnt vmcnt(30)\n"
        "ds_read_b64_tr_b16 a[84:85], v11 offset:21504\n"
        "ds_read_b64_tr_b16 a[86:87], v11 offset:23040\n"
        "ds_read_b64_tr_b16 a[88:89], v11 offset:21512\n"
        "ds_read_b64_tr_b16 a[90:91], v11 offset:23048\n"
        "ds_read_b64_tr_b16 a[92:93], v4 offset:21504\n"
        "ds_read_b64_tr_b16 a[94:95], v4 offset:23040\n"
        "s_waitcnt lgkmcnt(6)\n"
        "s_add_u32 m0, s79, 18432\n"
        "s_mov_b32 s65, 681984\n"
        "buffer_load_dwordx4 v8, s[44:47], s65 offen lds\n"
        "buffer_load_dwordx4 v9, s[44:47], s65 offen offset:1024 lds\n"
        "buffer_load_dwordx4 v10, s[44:47], s65 offen offset:2048 lds\n"
        "s_waitcnt vmcnt(30)\n"
        "ds_read_b64_tr_b16 a[96:97], v11 offset:24576\n"
        "ds_read_b64_tr_b16 a[98:99], v11 offset:26112\n"
        "ds_read_b64_tr_b16 a[100:101], v11 offset:24584\n"
        "ds_read_b64_tr_b16 a[102:103], v11 offset:26120\n"
        "ds_read_b64_tr_b16 a[104:105], v4 offset:24576\n"
        "ds_read_b64_tr_b16 a[106:107], v4 offset:26112\n"
        "s_waitcnt lgkmcnt(6)\n"
        "s_add_u32 m0, s79, 21504\n"
        "s_mov_b32 s65, 792576\n"
        "buffer_load_dwordx4 v8, s[44:47], s65 offen lds\n"
        "buffer_load_dwordx4 v9, s[44:47], s65 offen offset:1024 lds\n"
        "buffer_load_dwordx4 v10, s[44:47], s65 offen offset:2048 lds\n"
        "s_waitcnt vmcnt(30)\n"
        "ds_read_b64_tr_b16 a[108:109], v11 offset:27648\n"
        "ds_read_b64_tr_b16 a[110:111], v11 offset:29184\n"
        "ds_read_b64_tr_b16 a[112:113], v11 offset:27656\n"
        "ds_read_b64_tr_b16 a[114:115], v11 offset:29192\n"
        "ds_read_b64_tr_b16 a[116:117], v4 offset:27648\n"
        "ds_read_b64_tr_b16 a[118:119], v4 offset:29184\n"
        "s_waitcnt lgkmcnt(6)\n"
        "s_add_u32 m0, s79, 24576\n"
        "s_mov_b32 s65, 903168\n"
        "buffer_load_dwordx4 v8, s[44:47], s65 offen lds\n"
        "buffer_load_dwordx4 v9, s[44:47], s65 offen offset:1024 lds\n"
        "buffer_load_dwordx4 v10, s[44:47], s65 offen offset:2048 lds\n"
        "s_waitcnt vmcnt(30)\n"
        "ds_read_b64_tr_b16 a[120:121], v11 offset:30720\n"
        "ds_read_b64_tr_b16 a[122:123], v11 offset:32256\n"
        "ds_read_b64_tr_b16 a[124:125], v11 offset:30728\n"
        "ds_read_b64_tr_b16 a[126:127], v11 offset:32264\n"
        "ds_read_b64_tr_b16 a[128:129], v4 offset:30720\n"
        "ds_read_b64_tr_b16 a[130:131], v4 offset:32256\n"
        "s_waitcnt lgkmcnt(6)\n"
        "s_add_u32 m0, s79, 27648\n"
        "s_mov_b32 s65, 1013760\n"
        "buffer_load_dwordx4 v8, s[44:47], s65 offen lds\n"
        "buffer_load_dwordx4 v9, s[44:47], s65 offen offset:1024 lds\n"
        "buffer_load_dwordx4 v10, s[44:47], s65 offen offset:2048 lds\n"
        "s_waitcnt vmcnt(30)\n"
        "ds_read_b64_tr_b16 a[132:133], v11 offset:33792\n"
        "ds_read_b64_tr_b16 a[134:135], v11 offset:35328\n"
        "ds_read_b64_tr_b16 a[136:137], v11 offset:33800\n"
        "ds_read_b64_tr_b16 a[138:139], v11 offset:35336\n"
        "ds_read_b64_tr_b16 a[140:141], v4 offset:33792\n"
        "ds_read_b64_tr_b16 a[142:143], v4 offset:35328\n"
        "s_waitcnt lgkmcnt(6)\n"
        "s_add_u32 m0, s79, 30720\n"
        "s_mov_b32 s65, 1124352\n"
        "buffer_load_dwordx4 v8, s[44:47], s65 offen lds\n"
        "buffer_load_dwordx4 v9, s[44:47], s65 offen offset:1024 lds\n"
        "buffer_load_dwordx4 v10, s[44:47], s65 offen offset:2048 lds\n"
        "s_waitcnt vmcnt(30)\n"
        "ds_read_b64_tr_b16 a[144:145], v11 offset:0\n"
        "ds_read_b64_tr_b16 a[146:147], v11 offset:1536\n"
        "ds_read_b64_tr_b16 a[148:149], v11 offset:8\n"
        "ds_read_b64_tr_b16 a[150:151], v11 offset:1544\n"
        "ds_read_b64_tr_b16 a[152:153], v4 offset:0\n"
        "ds_read_b64_tr_b16 a[154:155], v4 offset:1536\n"
        "s_waitcnt lgkmcnt(6)\n"
        "s_add_u32 m0, s79, 33792\n"
        "s_mov_b32 s65, 1234944\n"
        "buffer_load_dwordx4 v8, s[44:47], s65 offen lds\n"
        "buffer_load_dwordx4 v9, s[44:47], s65 offen offset:1024 lds\n"
        "buffer_load_dwordx4 v10, s[44:47], s65 offen offset:2048 lds\n"
        "s_waitcnt vmcnt(30)\n"
        "ds_read_b64_tr_b16 a[156:157], v11 offset:3072\n"
        "ds_read_b64_tr_b16 a[158:159], v11 offset:4608\n"
        "ds_read_b64_tr_b16 a[160:161], v11 offset:3080\n"
        "ds_read_b64_tr_b16 a[162:163], v11 offset:4616\n"
        "ds_read_b64_tr_b16 a[164:165], v4 offset:3072\n"
        "ds_read_b64_tr_b16 a[166:167], v4 offset:4608\n"
        "s_waitcnt lgkmcnt(6)\n"
        "s_add_u32 m0, s79, 0\n"
        "s_mov_b32 s65, 36864\n"
        "buffer_load_dwordx4 v8, s[44:47], s65 offen lds\n"
        "buffer_load_dwordx4 v9, s[44:47], s65 offen offset:1024 lds\n"
        "buffer_load_dwordx4 v10, s[44:47], s65 offen offset:2048 lds\n"
        "s_waitcnt vmcnt(30)\n"
        "ds_read_b64_tr_b16 a[168:169], v11 offset:6144\n"
        "ds_read_b64_tr_b16 a[170:171], v11 offset:7680\n"
        "ds_read_b64_tr_b16 a[172:173], v11 offset:6152\n"
        "ds_read_b64_tr_b16 a[174:175], v11 offset:7688\n"
        "ds_read_b64_tr_b16 a[176:177], v4 offset:6144\n"
        "ds_read_b64_tr_b16 a[178:179], v4 offset:7680\n"
        "s_waitcnt lgkmcnt(6)\n"
        "s_add_u32 m0, s79, 3072\n"
        "s_mov_b32 s65, 147456\n"
        "buffer_load_dwordx4 v8, s[44:47], s65 offen lds\n"
        "buffer_load_dwordx4 v9, s[44:47], s65 offen offset:1024 lds\n"
        "buffer_load_dwordx4 v10, s[44:47], s65 offen offset:2048 lds\n"
        "s_waitcnt vmcnt(30)\n"
        "ds_read_b64_tr_b16 a[180:181], v11 offset:9216\n"
        "ds_read_b64_tr_b16 a[182:183], v11 offset:10752\n"
        "ds_read_b64_tr_b16 a[184:185], v11 offset:9224\n"
        "ds_read_b64_tr_b16 a[186:187], v11 offset:10760\n"
        "ds_read_b64_tr_b16 a[188:189], v4 offset:9216\n"
        "ds_read_b64_tr_b16 a[190:191], v4 offset:10752\n"
        "s_waitcnt lgkmcnt(6)\n"
        "s_add_u32 m0, s79, 6144\n"
        "s_mov_b32 s65, 258048\n"
        "buffer_load_dwordx4 v8, s[44:47], s65 offen lds\n"
        "buffer_load_dwordx4 v9, s[44:47], s65 offen offset:1024 lds\n"
        "buffer_load_dwordx4 v10, s[44:47], s65 offen offset:2048 lds\n"
        "s_waitcnt vmcnt(30)\n"
        "ds_read_b64_tr_b16 a[192:193], v11 offset:12288\n"
        "ds_read_b64_tr_b16 a[194:195], v11 offset:13824\n"
        "ds_read_b64_tr_b16 a[196:197], v11 offset:12296\n"
        "ds_read_b64_tr_b16 a[198:199], v11 offset:13832\n"
        "ds_read_b64_tr_b16 a[200:201], v4 offset:12288\n"
        "ds_read_b64_tr_b16 a[202:203], v4 offset:13824\n"
        "s_waitcnt lgkmcnt(6)\n"
        "s_add_u32 m0, s79, 9216\n"
        "s_mov_b32 s65, 368640\n"
        "buffer_load_dwordx4 v8, s[44:47], s65 offen lds\n"
        "buffer_load_dwordx4 v9, s[44:47], s65 offen offset:1024 lds\n"
        "buffer_load_dwordx4 v10, s[44:47], s65 offen offset:2048 lds\n"
        "s_waitcnt vmcnt(30)\n"
        "ds_read_b64_tr_b16 a[204:205], v11 offset:15360\n"
        "ds_read_b64_tr_b16 a[206:207], v11 offset:16896\n"
        "ds_read_b64_tr_b16 a[208:209], v11 offset:15368\n"
        "ds_read_b64_tr_b16 a[210:211], v11 offset:16904\n"
        "ds_read_b64_tr_b16 a[212:213], v4 offset:15360\n"
        "ds_read_b64_tr_b16 a[214:215], v4 offset:16896\n"
        "s_waitcnt lgkmcnt(6)\n"
        "s_add_u32 m0, s79, 12288\n"
        "s_mov_b32 s65, 479232\n"
        "buffer_load_dwordx4 v8, s[44:47], s65 offen lds\n"
        "buffer_load_dwordx4 v9, s[44:47], s65 offen offset:1024 lds\n"
        "buffer_load_dwordx4 v10, s[44:47], s65 offen offset:2048 lds\n"
        "s_waitcnt vmcnt(30)\n"
        "ds_read_b64_tr_b16 a[216:217], v11 offset:18432\n"
        "ds_read_b64_tr_b16 a[218:219], v11 offset:19968\n"
        "ds_read_b64_tr_b16 a[220:221], v11 offset:18440\n"
        "ds_read_b64_tr_b16 a[222:223], v11 offset:19976\n"
        "ds_read_b64_tr_b16 a[224:225], v4 offset:18432\n"
        "ds_read_b64_tr_b16 a[226:227], v4 offset:19968\n"
        "s_waitcnt lgkmcnt(6)\n"
        "s_add_u32 m0, s79, 15360\n"
        "s_mov_b32 s65, 589824\n"
        "buffer_load_dwordx4 v8, s[44:47], s65 offen lds\n"
        "buffer_load_dwordx4 v9, s[44:47], s65 offen offset:1024 lds\n"
        "buffer_load_dwordx4 v10, s[44:47], s65 offen offset:2048 lds\n"
        "s_waitcnt vmcnt(30)\n"
        "ds_read_b64_tr_b16 a[228:229], v11 offset:21504\n"
        "ds_read_b64_tr_b16 a[230:231], v11 offset:23040\n"
        "ds_read_b64_tr_b16 a[232:233], v11 offset:21512\n"
        "ds_read_b64_tr_b16 a[234:235], v11 offset:23048\n"
        "ds_read_b64_tr_b16 a[236:237], v4 offset:21504\n"
        "ds_read_b64_tr_b16 a[238:239], v4 offset:23040\n"
        "s_waitcnt lgkmcnt(6)\n"
        "s_add_u32 m0, s79, 18432\n"
        "s_mov_b32 s65, 700416\n"
        "buffer_load_dwordx4 v8, s[44:47], s65 offen lds\n"
        "buffer_load_dwordx4 v9, s[44:47], s65 offen offset:1024 lds\n"
        "buffer_load_dwordx4 v10, s[44:47], s65 offen offset:2048 lds\n"
        "s_waitcnt vmcnt(30)\n"
        "ds_read_b64_tr_b16 a[240:241], v11 offset:24576\n"
        "ds_read_b64_tr_b16 a[242:243], v11 offset:26112\n"
        "ds_read_b64_tr_b16 a[244:245], v11 offset:24584\n"
        "ds_read_b64_tr_b16 a[246:247], v11 offset:26120\n"
        "ds_read_b64_tr_b16 a[248:249], v4 offset:24576\n"
        "ds_read_b64_tr_b16 a[250:251], v4 offset:26112\n"
        "s_waitcnt lgkmcnt(6)\n"
        "s_add_u32 m0, s79, 21504\n"
        "s_mov_b32 s65, 811008\n"
        "buffer_load_dwordx4 v8, s[44:47], s65 offen lds\n"
        "buffer_load_dwordx4 v9, s[44:47], s65 offen offset:1024 lds\n"
        "buffer_load_dwordx4 v10, s[44:47], s65 offen offset:2048 lds\n"
        "s_waitcnt vmcnt(30)\n"
        "ds_read_b64_tr_b16 a[252:253], v11 offset:27648\n"
        "ds_read_b64_tr_b16 a[254:255], v11 offset:29184\n"
        "ds_read_b64_tr_b16 v[80:81], v11 offset:27656\n"
        "ds_read_b64_tr_b16 v[82:83], v11 offset:29192\n"
        "ds_read_b64_tr_b16 v[84:85], v4 offset:27648\n"
        "ds_read_b64_tr_b16 v[86:87], v4 offset:29184\n"
        "s_waitcnt lgkmcnt(6)\n"
        "s_add_u32 m0, s79, 24576\n"
        "s_mov_b32 s65, 921600\n"
        "buffer_load_dwordx4 v8, s[44:47], s65 offen lds\n"
        "buffer_load_dwordx4 v9, s[44:47], s65 offen offset:1024 lds\n"
        "buffer_load_dwordx4 v10, s[44:47], s65 offen offset:2048 lds\n"
        "s_waitcnt vmcnt(30)\n"
        "ds_read_b64_tr_b16 v[88:89], v11 offset:30720\n"
        "ds_read_b64_tr_b16 v[90:91], v11 offset:32256\n"
        "ds_read_b64_tr_b16 v[92:93], v11 offset:30728\n"
        "ds_read_b64_tr_b16 v[94:95], v11 offset:32264\n"
        "ds_read_b64_tr_b16 v[96:97], v4 offset:30720\n"
        "ds_read_b64_tr_b16 v[98:99], v4 offset:32256\n"
        "s_waitcnt lgkmcnt(6)\n"
        "s_add_u32 m0, s79, 27648\n"
        "s_mov_b32 s65, 1032192\n"
        "buffer_load_dwordx4 v8, s[44:47], s65 offen lds\n"
        "buffer_load_dwordx4 v9, s[44:47], s65 offen offset:1024 lds\n"
        "buffer_load_dwordx4 v10, s[44:47], s65 offen offset:2048 lds\n"
        "s_waitcnt vmcnt(30)\n"
        "ds_read_b64_tr_b16 v[100:101], v11 offset:33792\n"
        "ds_read_b64_tr_b16 v[102:103], v11 offset:35328\n"
        "ds_read_b64_tr_b16 v[104:105], v11 offset:33800\n"
        "ds_read_b64_tr_b16 v[106:107], v11 offset:35336\n"
        "ds_read_b64_tr_b16 v[108:109], v4 offset:33792\n"
        "ds_read_b64_tr_b16 v[110:111], v4 offset:35328\n"
        "s_waitcnt lgkmcnt(6)\n"
        "s_add_u32 m0, s79, 30720\n"
        "s_mov_b32 s65, 1142784\n"
        "buffer_load_dwordx4 v8, s[44:47], s65 offen lds\n"
        "buffer_load_dwordx4 v9, s[44:47], s65 offen offset:1024 lds\n"
        "buffer_load_dwordx4 v10, s[44:47], s65 offen offset:2048 lds\n"
        "s_waitcnt vmcnt(30)\n"
        "ds_read_b64_tr_b16 v[112:113], v11 offset:0\n"
        "ds_read_b64_tr_b16 v[114:115], v11 offset:1536\n"
        "ds_read_b64_tr_b16 v[116:117], v11 offset:8\n"
        "ds_read_b64_tr_b16 v[118:119], v11 offset:1544\n"
        "ds_read_b64_tr_b16 v[120:121], v4 offset:0\n"
        "ds_read_b64_tr_b16 v[122:123], v4 offset:1536\n"
        "s_waitcnt lgkmcnt(6)\n"
        "s_add_u32 m0, s79, 33792\n"
        "s_mov_b32 s65, 1253376\n"
        "buffer_load_dwordx4 v8, s[44:47], s65 offen lds\n"
        "buffer_load_dwordx4 v9, s[44:47], s65 offen offset:1024 lds\n"
        "buffer_load_dwordx4 v10, s[44:47], s65 offen offset:2048 lds\n"
        "s_waitcnt vmcnt(30)\n"
        "ds_read_b64_tr_b16 v[124:125], v11 offset:3072\n"
        "ds_read_b64_tr_b16 v[126:127], v11 offset:4608\n"
        "ds_read_b64_tr_b16 v[128:129], v11 offset:3080\n"
        "ds_read_b64_tr_b16 v[130:131], v11 offset:4616\n"
        "ds_read_b64_tr_b16 v[132:133], v4 offset:3072\n"
        "ds_read_b64_tr_b16 v[134:135], v4 offset:4608\n"
        "s_waitcnt vmcnt(27)\n"
        "ds_read_b64_tr_b16 v[136:137], v11 offset:6144\n"
        "ds_read_b64_tr_b16 v[138:139], v11 offset:7680\n"
        "ds_read_b64_tr_b16 v[140:141], v11 offset:6152\n"
        "ds_read_b64_tr_b16 v[142:143], v11 offset:7688\n"
        "ds_read_b64_tr_b16 v[144:145], v4 offset:6144\n"
        "ds_read_b64_tr_b16 v[146:147], v4 offset:7680\n"
        "s_waitcnt vmcnt(24)\n"
        "ds_read_b64_tr_b16 v[148:149], v11 offset:9216\n"
        "ds_read_b64_tr_b16 v[150:151], v11 offset:10752\n"
        "ds_read_b64_tr_b16 v[152:153], v11 offset:9224\n"
        "ds_read_b64_tr_b16 v[154:155], v11 offset:10760\n"
        "ds_read_b64_tr_b16 v[156:157], v4 offset:9216\n"
        "ds_read_b64_tr_b16 v[158:159], v4 offset:10752\n"
        "s_waitcnt vmcnt(21)\n"
        "ds_read_b64_tr_b16 v[160:161], v11 offset:12288\n"
        "ds_read_b64_tr_b16 v[162:163], v11 offset:13824\n"
        "ds_read_b64_tr_b16 v[164:165], v11 offset:12296\n"
        "ds_read_b64_tr_b16 v[166:167], v11 offset:13832\n"
        "ds_read_b64_tr_b16 v[168:169], v4 offset:12288\n"
        "ds_read_b64_tr_b16 v[170:171], v4 offset:13824\n"
        "s_waitcnt vmcnt(18)\n"
        "ds_read_b64_tr_b16 v[172:173], v11 offset:15360\n"
        "ds_read_b64_tr_b16 v[174:175], v11 offset:16896\n"
        "ds_read_b64_tr_b16 v[176:177], v11 offset:15368\n"
        "ds_read_b64_tr_b16 v[178:179], v11 offset:16904\n"
        "ds_read_b64_tr_b16 v[180:181], v4 offset:15360\n"
        "ds_read_b64_tr_b16 v[182:183], v4 offset:16896\n"
        "s_waitcnt vmcnt(15)\n"
        "ds_read_b64_tr_b16 v[184:185], v11 offset:18432\n"
        "ds_read_b64_tr_b16 v[186:187], v11 offset:19968\n"
        "ds_read_b64_tr_b16 v[188:189], v11 offset:18440\n"
        "ds_read_b64_tr_b16 v[190:191], v11 offset:19976\n"
        "ds_read_b64_tr_b16 v[192:193], v4 offset:18432\n"
        "ds_read_b64_tr_b16 v[194:195], v4 offset:19968\n"
        "s_waitcnt vmcnt(12)\n"
        "ds_read_b64_tr_b16 v[196:197], v11 offset:21504\n"
        "ds_read_b64_tr_b16 v[198:199], v11 offset:23040\n"
        "ds_read_b64_tr_b16 v[200:201], v11 offset:21512\n"
        "ds_read_b64_tr_b16 v[202:203], v11 offset:23048\n"
        "ds_read_b64_tr_b16 v[204:205], v4 offset:21504\n"
        "ds_read_b64_tr_b16 v[206:207], v4 offset:23040\n"
        "s_waitcnt vmcnt(9)\n"
        "ds_read_b64_tr_b16 v[208:209], v11 offset:24576\n"
        "ds_read_b64_tr_b16 v[210:211], v11 offset:26112\n"
        "ds_read_b64_tr_b16 v[212:213], v11 offset:24584\n"
        "ds_read_b64_tr_b16 v[214:215], v11 offset:26120\n"
        "ds_read_b64_tr_b16 v[216:217], v4 offset:24576\n"
        "ds_read_b64_tr_b16 v[218:219], v4 offset:26112\n"
        "s_waitcnt vmcnt(6)\n"
        "ds_read_b64_tr_b16 v[220:221], v11 offset:27648\n"
        "ds_read_b64_tr_b16 v[222:223], v11 offset:29184\n"
        "ds_read_b64_tr_b16 v[224:225], v11 offset:27656\n"
        "ds_read_b64_tr_b16 v[226:227], v11 offset:29192\n"
        "ds_read_b64_tr_b16 v[228:229], v4 offset:27648\n"
        "ds_read_b64_tr_b16 v[230:231], v4 offset:29184\n"
        "s_waitcnt vmcnt(3)\n"
        "ds_read_b64_tr_b16 v[232:233], v11 offset:30720\n"
        "ds_read_b64_tr_b16 v[234:235], v11 offset:32256\n"
        "ds_read_b64_tr_b16 v[236:237], v11 offset:30728\n"
        "ds_read_b64_tr_b16 v[238:239], v11 offset:32264\n"
        "ds_read_b64_tr_b16 v[240:241], v4 offset:30720\n"
        "ds_read_b64_tr_b16 v[242:243], v4 offset:32256\n"
        "s_waitcnt vmcnt(0)\n"
        "ds_read_b64_tr_b16 v[244:245], v11 offset:33792\n"
        "ds_read_b64_tr_b16 v[246:247], v11 offset:35328\n"
        "ds_read_b64_tr_b16 v[248:249], v11 offset:33800\n"
        "ds_read_b64_tr_b16 v[250:251], v11 offset:35336\n"
        "ds_read_b64_tr_b16 v[252:253], v4 offset:33792\n"
        "ds_read_b64_tr_b16 v[254:255], v4 offset:35328\n"
        "s_waitcnt lgkmcnt(0)\n"
        "v_lshrrev_b32 v12, 4, v1\n"
        "s_lshl_b32 s66, s57, 7\n"
        "s_mov_b32 s64, 0\n"
        ".Lf2_pair_top_%=:\n"
        "s_mov_b64 exec, 1\n"
        "buffer_load_dword v13, off, s[40:43], s66 sc1\n"
        "s_mov_b64 exec, -1\n"
        "s_waitcnt vmcnt(0)\n"
        "v_cmp_gt_i32 vcc, 0, v13\n"
        "s_nop 0\n"
        "s_and_b64 vcc, vcc, 1\n"
        "s_cbranch_vccz .Lf2_pair_ok_%=\n"
        "s_add_u32 s64, s64, 1\n"
        "s_and_b32 s65, s64, 255\n"
        "s_cbranch_scc1 .Lf2_pair_go_%=\n"
        "s_mov_b32 s65, 40960\n"
        "s_cmp_lt_u32 s64, 1048576\n"
        "s_cbranch_scc1 .Lf2_pair_chk_%=\n"
        "v_mov_b32 v13, 1\n"
        "buffer_store_dword v13, off, s[40:43], s65 sc1\n"
        "s_waitcnt vmcnt(0)\n"
        ".Lf2_pair_chk_%=:\n"
        "buffer_load_dword v13, off, s[40:43], s65 sc1\n"
        "s_waitcnt vmcnt(0)\n"
        "v_readfirstlane_b32 s65, v13\n"
        "s_nop 0\n"
        "s_cmp_lg_u32 s65, 1\n"
        "s_cbranch_scc1 .Lf2_pair_go_%=\n"
        "s_endpgm\n"
        ".Lf2_pair_go_%=:\n"
        "s_sleep 2\n"
        "s_branch .Lf2_pair_top_%=\n"
        ".Lf2_pair_ok_%=:\n"
        "v_readfirstlane_b32 s50, v13\n"
        "s_nop 0\n"
        "s_xor_b32 s50, s50, s56\n"
        "v_lshlrev_b32 v14, 7, v12\n"
        "s_sub_u32 s66, 0, s50\n"
        "s_mov_b32 s52, 24576\n"
        "s_mov_b32 s64, 0\n"
        ".Lf2_pg0_top_%=:\n"
        "buffer_load_dword v13, v14, s[40:43], s52 offen sc1\n"
        "s_waitcnt vmcnt(0)\n"
        "v_cmp_gt_i32 vcc, 0, v13\n"
        "s_cbranch_vccz .Lf2_pg0_ok_%=\n"
        "s_add_u32 s64, s64, 1\n"
        "s_and_b32 s65, s64, 255\n"
        "s_cbranch_scc1 .Lf2_pg0_go_%=\n"
        "s_mov_b32 s65, 40960\n"
        "s_cmp_lt_u32 s64, 1048576\n"
        "s_cbranch_scc1 .Lf2_pg0_chk_%=\n"
        "v_mov_b32 v13, 1\n"
        "buffer_store_dword v13, off, s[40:43], s65 sc1\n"
        "s_waitcnt vmcnt(0)\n"
        ".Lf2_pg0_chk_%=:\n"
        "buffer_load_dword v13, off, s[40:43], s65 sc1\n"
        "s_waitcnt vmcnt(0)\n"
        "v_readfirstlane_b32 s65, v13\n"
        "s_nop 0\n"
        "s_cmp_lg_u32 s65, 1\n"
        "s_cbranch_scc1 .Lf2_pg0_go_%=\n"
        "s_endpgm\n"
        ".Lf2_pg0_go_%=:\n"
        "s_sleep 2\n"
        "s_branch .Lf2_pg0_top_%=\n"
        ".Lf2_pg0_ok_%=:\n"
        "v_cmp_eq_u32 s[52:53], 1, v13\n"
        "s_nop 1\n"
        "s_mov_b32 s54, 32768\n"
        "s_mov_b32 s64, 0\n"
        ".Lf2_pg1_top_%=:\n"
        "buffer_load_dword v13, v14, s[40:43], s54 offen sc1\n"
        "s_waitcnt vmcnt(0)\n"
        "v_cmp_gt_i32 vcc, 0, v13\n"
        "s_cbranch_vccz .Lf2_pg1_ok_%=\n"
        "s_add_u32 s64, s64, 1\n"
        "s_and_b32 s65, s64, 255\n"
        "s_cbranch_scc1 .Lf2_pg1_go_%=\n"
        "s_mov_b32 s65, 40960\n"
        "s_cmp_lt_u32 s64, 1048576\n"
        "s_cbranch_scc1 .Lf2_pg1_chk_%=\n"
        "v_mov_b32 v13, 1\n"
        "buffer_store_dword v13, off, s[40:43], s65 sc1\n"
        "s_waitcnt vmcnt(0)\n"
        ".Lf2_pg1_chk_%=:\n"
        "buffer_load_dword v13, off, s[40:43], s65 sc1\n"
        "s_waitcnt vmcnt(0)\n"
        "v_readfirstlane_b32 s65, v13\n"
        "s_nop 0\n"
        "s_cmp_lg_u32 s65, 1\n"
        "s_cbranch_scc1 .Lf2_pg1_go_%=\n"
        "s_endpgm\n"
        ".Lf2_pg1_go_%=:\n"
        "s_sleep 2\n"
        "s_branch .Lf2_pg1_top_%=\n"
        ".Lf2_pg1_ok_%=:\n"
        "v_cmp_eq_u32 s[54:55], 1, v13\n"
        "s_nop 1\n"
        "s_mov_b32 vcc_lo, 0x55555555\n"
        "s_mov_b32 vcc_hi, 0x55555555\n"
        "s_xor_b64 vcc, vcc, s[52:53]\n"
        "s_bcnt1_i32_b64 s65, vcc\n"
        "s_sub_u32 s64, s65, 23\n"
        "s_cmp_lt_u32 s64, 19\n"
        "s_cbranch_scc1 .Lf2_rawmap_%=\n"
        "s_cmp_lt_u32 s65, 32\n"
        "s_cselect_b32 s65, 0, -1\n"
        "s_xor_b32 s52, s65, 0x55555555\n"
        "s_xor_b32 s53, s65, 0x55555555\n"
        "s_xor_b32 s54, s65, 0x55555555\n"
        "s_xor_b32 s55, s65, 0xaaaaaaaa\n"
        ".Lf2_rawmap_%=:\n"
        "s_xor_b32 s52, s52, s66\n"
        "s_xor_b32 s53, s53, s66\n"
        "s_xor_b32 s54, s54, s66\n"
        "s_xor_b32 s55, s55, s66\n"
        "s_and_b32 s76, s55, 1\n"
        "s_lshl_b32 s76, s76, 13\n"
        "s_add_u32 s60, s60, s76\n"
        "s_barrier\n"
        "s_mov_b32 s68, -1\n"
        "s_mov_b32 s69, 0\n"
        "v_add_u32 v8, 49152, v0\n"
        "s_mov_b32 s72, 241172480\n"
        "s_mov_b32 s73, 241172480\n"
        "s_branch .Lf2_wait2_%=\n"
        ".Lf2_c01_%=:\n"
        "v_mfma_f32_16x16x32_bf16 v[44:47], a[0:3], v[12:15], 0\n"
        "v_cvt_pk_bf16_f32 v74, v60, v61\n"
        "v_cvt_pk_bf16_f32 v75, v62, v63\n"
        "v_mfma_f32_16x16x32_bf16 v[48:51], a[4:7], v[12:15], 0\n"
        "v_cvt_pk_bf16_f32 v78, v64, v65\n"
        "v_cvt_pk_bf16_f32 v79, v66, v67\n"
        "v_mfma_f32_16x16x32_bf16 v[52:55], a[8:11], v[12:15], 0\n"
        "ds_read_b128 v[12:15], v0 offset:4096\n"
        "s_add_u32 m0, s61, 24576\n"
        "v_mfma_f32_16x16x32_bf16 v[56:59], a[0:3], v[16:19], 0\n"
        "s_add_u32 s71, s71, 16384\n"
        "buffer_load_dwordx4 v1, s[40:43], s71 offen offset:0 sc1 lds\n"
        "v_mfma_f32_16x16x32_bf16 v[60:63], a[4:7], v[16:19], 0\n"
        "buffer_load_dwordx4 v1, s[40:43], s71 offen offset:1024 sc1 lds\n"
        "s_add_u32 m0, m0, 8192\n"
        "v_mfma_f32_16x16x32_bf16 v[64:67], a[8:11], v[16:19], 0\n"
        "ds_read_b128 v[16:19], v0 offset:4352\n"
        "s_waitcnt lgkmcnt(2)\n"
        "v_mfma_f32_16x16x32_bf16 v[44:47], a[12:15], v[20:23], v[44:47]\n"
        "s_add_u32 s71, s71, 16384\n"
        "v_mfma_f32_16x16x32_bf16 v[48:51], a[16:19], v[20:23], v[48:51]\n"
        "buffer_load_dwordx4 v1, s[40:43], s71 offen offset:0 sc1 lds\n"
        "v_mfma_f32_16x16x32_bf16 v[52:55], a[20:23], v[20:23], v[52:55]\n"
        "ds_read_b128 v[20:23], v0 offset:6144\n"
        "v_mfma_f32_16x16x32_bf16 v[56:59], a[12:15], v[24:27], v[56:59]\n"
        "buffer_store_dwordx4 v[68:71], v6, s[40:43], s72 offen sc1\n"
        "v_mfma_f32_16x16x32_bf16 v[60:63], a[16:19], v[24:27], v[60:63]\n"
        "buffer_load_dwordx4 v1, s[40:43], s71 offen offset:1024 sc1 lds\n"
        "s_add_u32 m0, m0, 8192\n"
        "v_mfma_f32_16x16x32_bf16 v[64:67], a[20:23], v[24:27], v[64:67]\n"
        "ds_read_b128 v[24:27], v0 offset:6400\n"
        "s_waitcnt lgkmcnt(2)\n"
        "v_mfma_f32_16x16x32_bf16 v[44:47], a[24:27], v[12:15], v[44:47]\n"
        "s_add_u32 s71, s71, 16384\n"
        "s_add_u32 s66, s72, 147456\n"
        "v_mfma_f32_16x16x32_bf16 v[48:51], a[28:31], v[12:15], v[48:51]\n"
        "buffer_load_dwordx4 v1, s[40:43], s71 offen offset:0 sc1 lds\n"
        "v_mfma_f32_16x16x32_bf16 v[52:55], a[32:35], v[12:15], v[52:55]\n"
        "ds_read_b128 v[12:15], v0 offset:8192\n"
        "v_mfma_f32_16x16x32_bf16 v[56:59], a[24:27], v[16:19], v[56:59]\n"
        "buffer_store_dwordx4 v[72:75], v6, s[40:43], s66 offen sc1\n"
        "v_mfma_f32_16x16x32_bf16 v[60:63], a[28:31], v[16:19], v[60:63]\n"
        "buffer_load_dwordx4 v1, s[40:43], s71 offen offset:1024 sc1 lds\n"
        "s_add_u32 s70, s70, 1\n"
        "v_mfma_f32_16x16x32_bf16 v[64:67], a[32:35], v[16:19], v[64:67]\n"
        "ds_read_b128 v[16:19], v0 offset:8448\n"
        "s_waitcnt lgkmcnt(2)\n"
        "v_mfma_f32_16x16x32_bf16 v[44:47], a[36:39], v[20:23], v[44:47]\n"
        "v_permlane16_swap_b32 v76, v78\n"
        "v_mfma_f32_16x16x32_bf16 v[48:51], a[40:43], v[20:23], v[48:51]\n"
        "v_permlane16_swap_b32 v77, v79\n"
        "v_mfma_f32_16x16x32_bf16 v[52:55], a[44:47], v[20:23], v[52:55]\n"
        "ds_read_b128 v[20:23], v0 offset:10240\n"
        "v_mfma_f32_16x16x32_bf16 v[56:59], a[36:39], v[24:27], v[56:59]\n"
        "buffer_store_dwordx4 v[76:79], v7, s[40:43], s72 offen sc1\n"
        "v_mfma_f32_16x16x32_bf16 v[60:63], a[40:43], v[24:27], v[60:63]\n"
        "v_mfma_f32_16x16x32_bf16 v[64:67], a[44:47], v[24:27], v[64:67]\n"
        "ds_read_b128 v[24:27], v0 offset:10496\n"
        "s_waitcnt lgkmcnt(2)\n"
        "v_mfma_f32_16x16x32_bf16 v[44:47], a[48:51], v[12:15], v[44:47]\n"
        "v_mfma_f32_16x16x32_bf16 v[48:51], a[52:55], v[12:15], v[48:51]\n"
        "v_mfma_f32_16x16x32_bf16 v[52:55], a[56:59], v[12:15], v[52:55]\n"
        "ds_read_b128 v[12:15], v0 offset:12288\n"
        "v_mfma_f32_16x16x32_bf16 v[56:59], a[48:51], v[16:19], v[56:59]\n"
        "v_mfma_f32_16x16x32_bf16 v[60:63], a[52:55], v[16:19], v[60:63]\n"
        "v_mfma_f32_16x16x32_bf16 v[64:67], a[56:59], v[16:19], v[64:67]\n"
        "ds_read_b128 v[16:19], v0 offset:12544\n"
        "s_waitcnt lgkmcnt(2)\n"
        "v_mfma_f32_16x16x32_bf16 v[44:47], a[60:63], v[20:23], v[44:47]\n"
        "v_mfma_f32_16x16x32_bf16 v[48:51], a[64:67], v[20:23], v[48:51]\n"
        "v_mfma_f32_16x16x32_bf16 v[52:55], a[68:71], v[20:23], v[52:55]\n"
        "ds_read_b128 v[20:23], v0 offset:14336\n"
        "v_mfma_f32_16x16x32_bf16 v[56:59], a[60:63], v[24:27], v[56:59]\n"
        "v_mfma_f32_16x16x32_bf16 v[60:63], a[64:67], v[24:27], v[60:63]\n"
        "v_mfma_f32_16x16x32_bf16 v[64:67], a[68:71], v[24:27], v[64:67]\n"
        "ds_read_b128 v[24:27], v0 offset:14592\n"
        "s_waitcnt lgkmcnt(2)\n"
        "v_mfma_f32_16x16x32_bf16 v[44:47], a[72:75], v[12:15], v[44:47]\n"
        "buffer_load_dword v4, v2, s[40:43], s76 offen sc1\n"
        "v_mfma_f32_16x16x32_bf16 v[48:51], a[76:79], v[12:15], v[48:51]\n"
        "v_mfma_f32_16x16x32_bf16 v[52:55], a[80:83], v[12:15], v[52:55]\n"
        "ds_read_b128 v[12:15], v0 offset:16384\n"
        "v_mfma_f32_16x16x32_bf16 v[56:59], a[72:75], v[16:19], v[56:59]\n"
        "v_mfma_f32_16x16x32_bf16 v[60:63], a[76:79], v[16:19], v[60:63]\n"
        "v_mfma_f32_16x16x32_bf16 v[64:67], a[80:83], v[16:19], v[64:67]\n"
        "ds_read_b128 v[16:19], v0 offset:16640\n"
        "s_waitcnt lgkmcnt(2)\n"
        "v_mfma_f32_16x16x32_bf16 v[44:47], a[84:87], v[20:23], v[44:47]\n"
        "v_mfma_f32_16x16x32_bf16 v[48:51], a[88:91], v[20:23], v[48:51]\n"
        "v_mfma_f32_16x16x32_bf16 v[52:55], a[92:95], v[20:23], v[52:55]\n"
        "ds_read_b128 v[20:23], v0 offset:18432\n"
        "v_mfma_f32_16x16x32_bf16 v[56:59], a[84:87], v[24:27], v[56:59]\n"
        "v_mfma_f32_16x16x32_bf16 v[60:63], a[88:91], v[24:27], v[60:63]\n"
        "v_mfma_f32_16x16x32_bf16 v[64:67], a[92:95], v[24:27], v[64:67]\n"
        "ds_read_b128 v[24:27], v0 offset:18688\n"
        "s_waitcnt lgkmcnt(2)\n"
        "v_mfma_f32_16x16x32_bf16 v[44:47], a[96:99], v[12:15], v[44:47]\n"
        "v_mfma_f32_16x16x32_bf16 v[48:51], a[100:103], v[12:15], v[48:51]\n"
        "v_mfma_f32_16x16x32_bf16 v[52:55], a[104:107], v[12:15], v[52:55]\n"
        "ds_read_b128 v[12:15], v0 offset:20480\n"
        "v_mfma_f32_16x16x32_bf16 v[56:59], a[96:99], v[16:19], v[56:59]\n"
        "s_waitcnt vmcnt(1)\n"
        "v_mov_b32 v5, s74\n"
        "v_mfma_f32_16x16x32_bf16 v[60:63], a[100:103], v[16:19], v[60:63]\n"
        "buffer_store_dword v5, v3, s[40:43], s73 offen sc1\n"
        "s_add_u32 s68, s68, 1\n"
        "v_mfma_f32_16x16x32_bf16 v[64:67], a[104:107], v[16:19], v[64:67]\n"
        "ds_read_b128 v[16:19], v0 offset:20736\n"
        "s_waitcnt lgkmcnt(2)\n"
        "v_mfma_f32_16x16x32_bf16 v[44:47], a[108:111], v[20:23], v[44:47]\n"
        "s_lshr_b32 s66, s68, 2\n"
        "s_add_u32 s66, s66, 1\n"
        "v_mfma_f32_16x16x32_bf16 v[48:51], a[112:115], v[20:23], v[48:51]\n"
        "s_bitcmp1_b32 s55, s66\n"
        "s_cselect_b32 s66, 8192, 0\n"
        "v_mfma_f32_16x16x32_bf16 v[52:55], a[116:119], v[20:23], v[52:55]\n"
        "ds_read_b128 v[20:23], v0 offset:22528\n"
        "s_lshl_b32 s72, s68, 19\n"
        "v_mfma_f32_16x16x32_bf16 v[56:59], a[108:111], v[24:27], v[56:59]\n"
        "s_add_u32 s72, s72, s66\n"
        "s_add_u32 s72, s72, s59\n"
        "v_mfma_f32_16x16x32_bf16 v[60:63], a[112:115], v[24:27], v[60:63]\n"
        "s_mov_b32 s74, s68\n"
        "s_mov_b32 s73, s60\n"
        "v_mfma_f32_16x16x32_bf16 v[64:67], a[116:119], v[24:27], v[64:67]\n"
        "ds_read_b128 v[24:27], v0 offset:22784\n"
        "s_waitcnt lgkmcnt(2)\n"
        "v_mfma_f32_16x16x32_bf16 v[44:47], a[120:123], v[12:15], v[44:47]\n"
        "v_mfma_f32_16x16x32_bf16 v[48:51], a[124:127], v[12:15], v[48:51]\n"
        "s_barrier\n"
        "v_mfma_f32_16x16x32_bf16 v[52:55], a[128:131], v[12:15], v[52:55]\n"
        "ds_read_b128 v[12:15], v0 offset:24576\n"
        "v_mfma_f32_16x16x32_bf16 v[56:59], a[120:123], v[16:19], v[56:59]\n"
        "v_mfma_f32_16x16x32_bf16 v[60:63], a[124:127], v[16:19], v[60:63]\n"
        "v_mfma_f32_16x16x32_bf16 v[64:67], a[128:131], v[16:19], v[64:67]\n"
        "ds_read_b128 v[16:19], v0 offset:24832\n"
        "s_waitcnt lgkmcnt(2)\n"
        "v_mfma_f32_16x16x32_bf16 v[44:47], a[132:135], v[20:23], v[44:47]\n"
        "s_waitcnt vmcnt(1)\n"
        "v_cmp_gt_i32 vcc, s70, v4\n"
        "v_mfma_f32_16x16x32_bf16 v[48:51], a[136:139], v[20:23], v[48:51]\n"
        "s_and_b64 vcc, vcc, 63\n"
        "v_mfma_f32_16x16x32_bf16 v[52:55], a[140:143], v[20:23], v[52:55]\n"
        "ds_read_b128 v[20:23], v0 offset:26624\n"
        "v_mfma_f32_16x16x32_bf16 v[56:59], a[132:135], v[24:27], v[56:59]\n"
        "v_mfma_f32_16x16x32_bf16 v[60:63], a[136:139], v[24:27], v[60:63]\n"
        "v_mfma_f32_16x16x32_bf16 v[64:67], a[140:143], v[24:27], v[64:67]\n"
        "ds_read_b128 v[24:27], v0 offset:26880\n"
        "s_waitcnt lgkmcnt(2)\n"
        "s_cbranch_vccnz .Lf2_c10_%=\n"
        "s_branch .Lf2_c11_%=\n"
        ".Lf2_c00_%=:\n"
        "v_mfma_f32_16x16x32_bf16 v[44:47], a[0:3], v[12:15], 0\n"
        "v_cvt_pk_bf16_f32 v74, v60, v61\n"
        "v_cvt_pk_bf16_f32 v75, v62, v63\n"
        "v_mfma_f32_16x16x32_bf16 v[48:51], a[4:7], v[12:15], 0\n"
        "v_cvt_pk_bf16_f32 v78, v64, v65\n"
        "v_cvt_pk_bf16_f32 v79, v66, v67\n"
        "v_mfma_f32_16x16x32_bf16 v[52:55], a[8:11], v[12:15], 0\n"
        "ds_read_b128 v[12:15], v0 offset:4096\n"
        "v_mfma_f32_16x16x32_bf16 v[56:59], a[0:3], v[16:19], 0\n"
        "buffer_store_dwordx4 v[68:71], v6, s[40:43], s72 offen sc1\n"
        "v_mfma_f32_16x16x32_bf16 v[60:63], a[4:7], v[16:19], 0\n"
        "s_add_u32 s66, s72, 147456\n"
        "v_mfma_f32_16x16x32_bf16 v[64:67], a[8:11], v[16:19], 0\n"
        "ds_read_b128 v[16:19], v0 offset:4352\n"
        "s_waitcnt lgkmcnt(2)\n"
        "v_mfma_f32_16x16x32_bf16 v[44:47], a[12:15], v[20:23], v[44:47]\n"
        "buffer_store_dwordx4 v[72:75], v6, s[40:43], s66 offen sc1\n"
        "v_mfma_f32_16x16x32_bf16 v[48:51], a[16:19], v[20:23], v[48:51]\n"
        "v_permlane16_swap_b32 v76, v78\n"
        "v_mfma_f32_16x16x32_bf16 v[52:55], a[20:23], v[20:23], v[52:55]\n"
        "ds_read_b128 v[20:23], v0 offset:6144\n"
        "v_mfma_f32_16x16x32_bf16 v[56:59], a[12:15], v[24:27], v[56:59]\n"
        "v_permlane16_swap_b32 v77, v79\n"
        "v_mfma_f32_16x16x32_bf16 v[60:63], a[16:19], v[24:27], v[60:63]\n"
        "buffer_store_dwordx4 v[76:79], v7, s[40:43], s72 offen sc1\n"
        "v_mfma_f32_16x16x32_bf16 v[64:67], a[20:23], v[24:27], v[64:67]\n"
        "ds_read_b128 v[24:27], v0 offset:6400\n"
        "s_waitcnt lgkmcnt(2)\n"
        "v_mfma_f32_16x16x32_bf16 v[44:47], a[24:27], v[12:15], v[44:47]\n"
        "v_mfma_f32_16x16x32_bf16 v[48:51], a[28:31], v[12:15], v[48:51]\n"
        "v_mfma_f32_16x16x32_bf16 v[52:55], a[32:35], v[12:15], v[52:55]\n"
        "ds_read_b128 v[12:15], v0 offset:8192\n"
        "v_mfma_f32_16x16x32_bf16 v[56:59], a[24:27], v[16:19], v[56:59]\n"
        "v_mfma_f32_16x16x32_bf16 v[60:63], a[28:31], v[16:19], v[60:63]\n"
        "v_mfma_f32_16x16x32_bf16 v[64:67], a[32:35], v[16:19], v[64:67]\n"
        "ds_read_b128 v[16:19], v0 offset:8448\n"
        "s_waitcnt lgkmcnt(2)\n"
        "v_mfma_f32_16x16x32_bf16 v[44:47], a[36:39], v[20:23], v[44:47]\n"
        "v_mfma_f32_16x16x32_bf16 v[48:51], a[40:43], v[20:23], v[48:51]\n"
        "v_mfma_f32_16x16x32_bf16 v[52:55], a[44:47], v[20:23], v[52:55]\n"
        "ds_read_b128 v[20:23], v0 offset:10240\n"
        "v_mfma_f32_16x16x32_bf16 v[56:59], a[36:39], v[24:27], v[56:59]\n"
        "v_mfma_f32_16x16x32_bf16 v[60:63], a[40:43], v[24:27], v[60:63]\n"
        "v_mfma_f32_16x16x32_bf16 v[64:67], a[44:47], v[24:27], v[64:67]\n"
        "ds_read_b128 v[24:27], v0 offset:10496\n"
        "s_waitcnt lgkmcnt(2)\n"
        "v_mfma_f32_16x16x32_bf16 v[44:47], a[48:51], v[12:15], v[44:47]\n"
        "v_mfma_f32_16x16x32_bf16 v[48:51], a[52:55], v[12:15], v[48:51]\n"
        "v_mfma_f32_16x16x32_bf16 v[52:55], a[56:59], v[12:15], v[52:55]\n"
        "ds_read_b128 v[12:15], v0 offset:12288\n"
        "v_mfma_f32_16x16x32_bf16 v[56:59], a[48:51], v[16:19], v[56:59]\n"
        "v_mfma_f32_16x16x32_bf16 v[60:63], a[52:55], v[16:19], v[60:63]\n"
        "v_mfma_f32_16x16x32_bf16 v[64:67], a[56:59], v[16:19], v[64:67]\n"
        "ds_read_b128 v[16:19], v0 offset:12544\n"
        "s_waitcnt lgkmcnt(2)\n"
        "v_mfma_f32_16x16x32_bf16 v[44:47], a[60:63], v[20:23], v[44:47]\n"
        "v_mfma_f32_16x16x32_bf16 v[48:51], a[64:67], v[20:23], v[48:51]\n"
        "v_mfma_f32_16x16x32_bf16 v[52:55], a[68:71], v[20:23], v[52:55]\n"
        "ds_read_b128 v[20:23], v0 offset:14336\n"
        "v_mfma_f32_16x16x32_bf16 v[56:59], a[60:63], v[24:27], v[56:59]\n"
        "v_mfma_f32_16x16x32_bf16 v[60:63], a[64:67], v[24:27], v[60:63]\n"
        "v_mfma_f32_16x16x32_bf16 v[64:67], a[68:71], v[24:27], v[64:67]\n"
        "ds_read_b128 v[24:27], v0 offset:14592\n"
        "s_waitcnt lgkmcnt(2)\n"
        "v_mfma_f32_16x16x32_bf16 v[44:47], a[72:75], v[12:15], v[44:47]\n"
        "buffer_load_dword v4, v2, s[40:43], s76 offen sc1\n"
        "v_mfma_f32_16x16x32_bf16 v[48:51], a[76:79], v[12:15], v[48:51]\n"
        "v_mfma_f32_16x16x32_bf16 v[52:55], a[80:83], v[12:15], v[52:55]\n"
        "ds_read_b128 v[12:15], v0 offset:16384\n"
        "v_mfma_f32_16x16x32_bf16 v[56:59], a[72:75], v[16:19], v[56:59]\n"
        "v_mfma_f32_16x16x32_bf16 v[60:63], a[76:79], v[16:19], v[60:63]\n"
        "s_waitcnt vmcnt(1)\n"
        "v_mov_b32 v5, s74\n"
        "v_mfma_f32_16x16x32_bf16 v[64:67], a[80:83], v[16:19], v[64:67]\n"
        "ds_read_b128 v[16:19], v0 offset:16640\n"
        "s_waitcnt lgkmcnt(2)\n"
        "v_mfma_f32_16x16x32_bf16 v[44:47], a[84:87], v[20:23], v[44:47]\n"
        "buffer_store_dword v5, v3, s[40:43], s73 offen sc1\n"
        "s_add_u32 s68, s68, 1\n"
        "v_mfma_f32_16x16x32_bf16 v[48:51], a[88:91], v[20:23], v[48:51]\n"
        "s_lshr_b32 s66, s68, 2\n"
        "s_add_u32 s66, s66, 1\n"
        "v_mfma_f32_16x16x32_bf16 v[52:55], a[92:95], v[20:23], v[52:55]\n"
        "ds_read_b128 v[20:23], v0 offset:18432\n"
        "v_mfma_f32_16x16x32_bf16 v[56:59], a[84:87], v[24:27], v[56:59]\n"
        "s_bitcmp1_b32 s55, s66\n"
        "s_cselect_b32 s66, 8192, 0\n"
        "v_mfma_f32_16x16x32_bf16 v[60:63], a[88:91], v[24:27], v[60:63]\n"
        "s_lshl_b32 s72, s68, 19\n"
        "s_add_u32 s72, s72, s66\n"
        "v_mfma_f32_16x16x32_bf16 v[64:67], a[92:95], v[24:27], v[64:67]\n"
        "ds_read_b128 v[24:27], v0 offset:18688\n"
        "s_waitcnt lgkmcnt(2)\n"
        "v_mfma_f32_16x16x32_bf16 v[44:47], a[96:99], v[12:15], v[44:47]\n"
        "s_add_u32 s72, s72, s59\n"
        "s_mov_b32 s74, s68\n"
        "v_mfma_f32_16x16x32_bf16 v[48:51], a[100:103], v[12:15], v[48:51]\n"
        "s_mov_b32 s73, s60\n"
        "v_mfma_f32_16x16x32_bf16 v[52:55], a[104:107], v[12:15], v[52:55]\n"
        "ds_read_b128 v[12:15], v0 offset:20480\n"
        "v_mfma_f32_16x16x32_bf16 v[56:59], a[96:99], v[16:19], v[56:59]\n"
        "v_mfma_f32_16x16x32_bf16 v[60:63], a[100:103], v[16:19], v[60:63]\n"
        "v_mfma_f32_16x16x32_bf16 v[64:67], a[104:107], v[16:19], v[64:67]\n"
        "ds_read_b128 v[16:19], v0 offset:20736\n"
        "s_waitcnt lgkmcnt(2)\n"
        "v_mfma_f32_16x16x32_bf16 v[44:47], a[108:111], v[20:23], v[44:47]\n"
        "v_mfma_f32_16x16x32_bf16 v[48:51], a[112:115], v[20:23], v[48:51]\n"
        "v_mfma_f32_16x16x32_bf16 v[52:55], a[116:119], v[20:23], v[52:55]\n"
        "ds_read_b128 v[20:23], v0 offset:22528\n"
        "v_mfma_f32_16x16x32_bf16 v[56:59], a[108:111], v[24:27], v[56:59]\n"
        "v_mfma_f32_16x16x32_bf16 v[60:63], a[112:115], v[24:27], v[60:63]\n"
        "v_mfma_f32_16x16x32_bf16 v[64:67], a[116:119], v[24:27], v[64:67]\n"
        "ds_read_b128 v[24:27], v0 offset:22784\n"
        "s_waitcnt lgkmcnt(2)\n"
        "v_mfma_f32_16x16x32_bf16 v[44:47], a[120:123], v[12:15], v[44:47]\n"
        "v_mfma_f32_16x16x32_bf16 v[48:51], a[124:127], v[12:15], v[48:51]\n"
        "v_mfma_f32_16x16x32_bf16 v[52:55], a[128:131], v[12:15], v[52:55]\n"
        "v_mfma_f32_16x16x32_bf16 v[56:59], a[120:123], v[16:19], v[56:59]\n"
        "v_mfma_f32_16x16x32_bf16 v[60:63], a[124:127], v[16:19], v[60:63]\n"
        "v_mfma_f32_16x16x32_bf16 v[64:67], a[128:131], v[16:19], v[64:67]\n"
        "s_waitcnt lgkmcnt(0)\n"
        "v_mfma_f32_16x16x32_bf16 v[44:47], a[132:135], v[20:23], v[44:47]\n"
        "v_mfma_f32_16x16x32_bf16 v[48:51], a[136:139], v[20:23], v[48:51]\n"
        "v_mfma_f32_16x16x32_bf16 v[52:55], a[140:143], v[20:23], v[52:55]\n"
        "v_mfma_f32_16x16x32_bf16 v[56:59], a[132:135], v[24:27], v[56:59]\n"
        "v_mfma_f32_16x16x32_bf16 v[60:63], a[136:139], v[24:27], v[60:63]\n"
        "v_mfma_f32_16x16x32_bf16 v[64:67], a[140:143], v[24:27], v[64:67]\n"
        "s_waitcnt vmcnt(1)\n"
        "v_cmp_gt_i32 vcc, s70, v4\n"
        "s_nop 0\n"
        "s_and_b64 vcc, vcc, 63\n"
        "s_cbranch_vccnz .Lf2_late0_%=\n"
        "s_add_u32 m0, s61, 24576\n"
        "s_add_u32 s71, s71, 16384\n"
        "buffer_load_dwordx4 v1, s[40:43], s71 offen offset:0 sc1 lds\n"
        "buffer_load_dwordx4 v1, s[40:43], s71 offen offset:1024 sc1 lds\n"
        "s_add_u32 m0, m0, 8192\n"
        "s_add_u32 s71, s71, 16384\n"
        "buffer_load_dwordx4 v1, s[40:43], s71 offen offset:0 sc1 lds\n"
        "buffer_load_dwordx4 v1, s[40:43], s71 offen offset:1024 sc1 lds\n"
        "s_add_u32 m0, m0, 8192\n"
        "s_add_u32 s71, s71, 16384\n"
        "buffer_load_dwordx4 v1, s[40:43], s71 offen offset:0 sc1 lds\n"
        "buffer_load_dwordx4 v1, s[40:43], s71 offen offset:1024 sc1 lds\n"
        "s_add_u32 s70, s70, 1\n"
        "s_branch .Lf2_go0_%=\n"
        ".Lf2_late0_%=:\n"
        "s_mov_b32 s64, 0\n"
        ".Lf2_in0_top_%=:\n"
        "buffer_load_dword v4, v2, s[40:43], s76 offen sc1\n"
        "s_waitcnt vmcnt(0)\n"
        "v_cmp_gt_i32 vcc, s70, v4\n"
        "s_nop 0\n"
        "s_and_b64 vcc, vcc, 63\n"
        "s_cbranch_vccz .Lf2_in0_ok_%=\n"
        "s_add_u32 s64, s64, 1\n"
        "s_and_b32 s65, s64, 255\n"
        "s_cbranch_scc1 .Lf2_in0_go_%=\n"
        "s_mov_b32 s65, 40960\n"
        "s_cmp_lt_u32 s64, 1048576\n"
        "s_cbranch_scc1 .Lf2_in0_chk_%=\n"
        "v_mov_b32 v4, 1\n"
        "buffer_store_dword v4, off, s[40:43], s65 sc1\n"
        "s_waitcnt vmcnt(0)\n"
        ".Lf2_in0_chk_%=:\n"
        "buffer_load_dword v4, off, s[40:43], s65 sc1\n"
        "s_waitcnt vmcnt(0)\n"
        "v_readfirstlane_b32 s65, v4\n"
        "s_nop 0\n"
        "s_cmp_lg_u32 s65, 1\n"
        "s_cbranch_scc1 .Lf2_in0_go_%=\n"
        "s_endpgm\n"
        ".Lf2_in0_go_%=:\n"
        "s_sleep 1\n"
        "s_branch .Lf2_in0_top_%=\n"
        ".Lf2_in0_ok_%=:\n"
        "s_add_u32 m0, s61, 24576\n"
        "s_add_u32 s71, s71, 16384\n"
        "buffer_load_dwordx4 v1, s[40:43], s71 offen offset:0 sc1 lds\n"
        "buffer_load_dwordx4 v1, s[40:43], s71 offen offset:1024 sc1 lds\n"
        "s_add_u32 m0, m0, 8192\n"
        "s_add_u32 s71, s71, 16384\n"
        "buffer_load_dwordx4 v1, s[40:43], s71 offen offset:0 sc1 lds\n"
        "buffer_load_dwordx4 v1, s[40:43], s71 offen offset:1024 sc1 lds\n"
        "s_add_u32 m0, m0, 8192\n"
        "s_add_u32 s71, s71, 16384\n"
        "buffer_load_dwordx4 v1, s[40:43], s71 offen offset:0 sc1 lds\n"
        "buffer_load_dwordx4 v1, s[40:43], s71 offen offset:1024 sc1 lds\n"
        "s_add_u32 s70, s70, 1\n"
        ".Lf2_go0_%=:\n"
        "buffer_load_dword v4, v2, s[40:43], s76 offen sc1\n"
        "s_waitcnt vmcnt(0)\n"
        "s_barrier\n"
        "ds_read_b128 v[12:15], v0 offset:24576\n"
        "ds_read_b128 v[16:19], v0 offset:24832\n"
        "ds_read_b128 v[20:23], v0 offset:26624\n"
        "ds_read_b128 v[24:27], v0 offset:26880\n"
        "v_cmp_gt_i32 vcc, s70, v4\n"
        "s_nop 0\n"
        "s_and_b64 vcc, vcc, 63\n"
        "s_waitcnt lgkmcnt(2)\n"
        "s_cbranch_vccnz .Lf2_c10_%=\n"
        "s_branch .Lf2_c11_%=\n"
        ".Lf2_c11_%=:\n"
        "v_mfma_f32_16x16x32_bf16 v[44:47], a[144:147], v[12:15], v[44:47]\n"
        "s_add_u32 m0, s61, 49152\n"
        "s_add_u32 s71, s71, 16384\n"
        "v_mfma_f32_16x16x32_bf16 v[48:51], a[148:151], v[12:15], v[48:51]\n"
        "buffer_load_dwordx4 v1, s[40:43], s71 offen offset:0 sc1 lds\n"
        "v_mfma_f32_16x16x32_bf16 v[52:55], a[152:155], v[12:15], v[52:55]\n"
        "ds_read_b128 v[12:15], v0 offset:28672\n"
        "v_mfma_f32_16x16x32_bf16 v[56:59], a[144:147], v[16:19], v[56:59]\n"
        "v_mfma_f32_16x16x32_bf16 v[60:63], a[148:151], v[16:19], v[60:63]\n"
        "buffer_load_dwordx4 v1, s[40:43], s71 offen offset:1024 sc1 lds\n"
        "s_add_u32 m0, m0, 8192\n"
        "v_mfma_f32_16x16x32_bf16 v[64:67], a[152:155], v[16:19], v[64:67]\n"
        "ds_read_b128 v[16:19], v0 offset:28928\n"
        "s_waitcnt lgkmcnt(2)\n"
        "v_mfma_f32_16x16x32_bf16 v[44:47], a[156:159], v[20:23], v[44:47]\n"
        "s_add_u32 s71, s71, 16384\n"
        "v_mfma_f32_16x16x32_bf16 v[48:51], a[160:163], v[20:23], v[48:51]\n"
        "buffer_load_dwordx4 v1, s[40:43], s71 offen offset:0 sc1 lds\n"
        "v_mfma_f32_16x16x32_bf16 v[52:55], a[164:167], v[20:23], v[52:55]\n"
        "ds_read_b128 v[20:23], v0 offset:30720\n"
        "v_mfma_f32_16x16x32_bf16 v[56:59], a[156:159], v[24:27], v[56:59]\n"
        "v_mfma_f32_16x16x32_bf16 v[60:63], a[160:163], v[24:27], v[60:63]\n"
        "buffer_load_dwordx4 v1, s[40:43], s71 offen offset:1024 sc1 lds\n"
        "s_add_u32 m0, m0, 8192\n"
        "v_mfma_f32_16x16x32_bf16 v[64:67], a[164:167], v[24:27], v[64:67]\n"
        "ds_read_b128 v[24:27], v0 offset:30976\n"
        "s_waitcnt lgkmcnt(2)\n"
        "v_mfma_f32_16x16x32_bf16 v[44:47], a[168:171], v[12:15], v[44:47]\n"
        "s_add_u32 s71, s71, 16384\n"
        "v_mfma_f32_16x16x32_bf16 v[48:51], a[172:175], v[12:15], v[48:51]\n"
        "buffer_load_dwordx4 v1, s[40:43], s71 offen offset:0 sc1 lds\n"
        "v_mfma_f32_16x16x32_bf16 v[52:55], a[176:179], v[12:15], v[52:55]\n"
        "ds_read_b128 v[12:15], v0 offset:32768\n"
        "v_mfma_f32_16x16x32_bf16 v[56:59], a[168:171], v[16:19], v[56:59]\n"
        "v_mfma_f32_16x16x32_bf16 v[60:63], a[172:175], v[16:19], v[60:63]\n"
        "buffer_load_dwordx4 v1, s[40:43], s71 offen offset:1024 sc1 lds\n"
        "s_add_u32 s69, s69, 1\n"
        "v_mfma_f32_16x16x32_bf16 v[64:67], a[176:179], v[16:19], v[64:67]\n"
        "ds_read_b128 v[16:19], v0 offset:33024\n"
        "s_waitcnt lgkmcnt(2)\n"
        "v_mfma_f32_16x16x32_bf16 v[44:47], a[180:183], v[20:23], v[44:47]\n"
        "s_add_u32 s70, s70, 1\n"
        "v_mfma_f32_16x16x32_bf16 v[48:51], a[184:187], v[20:23], v[48:51]\n"
        "v_mfma_f32_16x16x32_bf16 v[52:55], a[188:191], v[20:23], v[52:55]\n"
        "ds_read_b128 v[20:23], v0 offset:34816\n"
        "v_mfma_f32_16x16x32_bf16 v[56:59], a[180:183], v[24:27], v[56:59]\n"
        "v_mfma_f32_16x16x32_bf16 v[60:63], a[184:187], v[24:27], v[60:63]\n"
        "v_mfma_f32_16x16x32_bf16 v[64:67], a[188:191], v[24:27], v[64:67]\n"
        "ds_read_b128 v[24:27], v0 offset:35072\n"
        "s_waitcnt lgkmcnt(2)\n"
        "v_mfma_f32_16x16x32_bf16 v[44:47], a[192:195], v[12:15], v[44:47]\n"
        "v_mfma_f32_16x16x32_bf16 v[48:51], a[196:199], v[12:15], v[48:51]\n"
        "v_mfma_f32_16x16x32_bf16 v[52:55], a[200:203], v[12:15], v[52:55]\n"
        "ds_read_b128 v[12:15], v0 offset:36864\n"
        "v_mfma_f32_16x16x32_bf16 v[56:59], a[192:195], v[16:19], v[56:59]\n"
        "v_mfma_f32_16x16x32_bf16 v[60:63], a[196:199], v[16:19], v[60:63]\n"
        "v_mfma_f32_16x16x32_bf16 v[64:67], a[200:203], v[16:19], v[64:67]\n"
        "ds_read_b128 v[16:19], v0 offset:37120\n"
        "s_waitcnt lgkmcnt(2)\n"
        "v_mfma_f32_16x16x32_bf16 v[44:47], a[204:207], v[20:23], v[44:47]\n"
        "v_mfma_f32_16x16x32_bf16 v[48:51], a[208:211], v[20:23], v[48:51]\n"
        "v_mfma_f32_16x16x32_bf16 v[52:55], a[212:215], v[20:23], v[52:55]\n"
        "ds_read_b128 v[20:23], v0 offset:38912\n"
        "v_mfma_f32_16x16x32_bf16 v[56:59], a[204:207], v[24:27], v[56:59]\n"
        "v_mfma_f32_16x16x32_bf16 v[60:63], a[208:211], v[24:27], v[60:63]\n"
        "v_mfma_f32_16x16x32_bf16 v[64:67], a[212:215], v[24:27], v[64:67]\n"
        "ds_read_b128 v[24:27], v0 offset:39168\n"
        "s_waitcnt lgkmcnt(2)\n"
        "v_mfma_f32_16x16x32_bf16 v[44:47], a[216:219], v[12:15], v[44:47]\n"
        "buffer_load_dword v4, v2, s[40:43], s76 offen sc1\n"
        "v_mfma_f32_16x16x32_bf16 v[48:51], a[220:223], v[12:15], v[48:51]\n"
        "v_mfma_f32_16x16x32_bf16 v[52:55], a[224:227], v[12:15], v[52:55]\n"
        "ds_read_b128 v[12:15], v0 offset:40960\n"
        "v_mfma_f32_16x16x32_bf16 v[56:59], a[216:219], v[16:19], v[56:59]\n"
        "v_mfma_f32_16x16x32_bf16 v[60:63], a[220:223], v[16:19], v[60:63]\n"
        "v_mfma_f32_16x16x32_bf16 v[64:67], a[224:227], v[16:19], v[64:67]\n"
        "ds_read_b128 v[16:19], v0 offset:41216\n"
        "s_waitcnt lgkmcnt(2)\n"
        "v_mfma_f32_16x16x32_bf16 v[44:47], a[228:231], v[20:23], v[44:47]\n"
        "v_mfma_f32_16x16x32_bf16 v[48:51], a[232:235], v[20:23], v[48:51]\n"
        "v_mfma_f32_16x16x32_bf16 v[52:55], a[236:239], v[20:23], v[52:55]\n"
        "ds_read_b128 v[20:23], v0 offset:43008\n"
        "v_mfma_f32_16x16x32_bf16 v[56:59], a[228:231], v[24:27], v[56:59]\n"
        "v_mfma_f32_16x16x32_bf16 v[60:63], a[232:235], v[24:27], v[60:63]\n"
        "v_mfma_f32_16x16x32_bf16 v[64:67], a[236:239], v[24:27], v[64:67]\n"
        "ds_read_b128 v[24:27], v0 offset:43264\n"
        "s_waitcnt lgkmcnt(2)\n"
        "v_mfma_f32_16x16x32_bf16 v[44:47], a[240:243], v[12:15], v[44:47]\n"
        "v_mfma_f32_16x16x32_bf16 v[48:51], a[244:247], v[12:15], v[48:51]\n"
        "v_mfma_f32_16x16x32_bf16 v[52:55], a[248:251], v[12:15], v[52:55]\n"
        "ds_read_b128 v[12:15], v0 offset:45056\n"
        "v_mfma_f32_16x16x32_bf16 v[56:59], a[240:243], v[16:19], v[56:59]\n"
        "v_mfma_f32_16x16x32_bf16 v[60:63], a[244:247], v[16:19], v[60:63]\n"
        "v_mfma_f32_16x16x32_bf16 v[64:67], a[248:251], v[16:19], v[64:67]\n"
        "ds_read_b128 v[16:19], v0 offset:45312\n"
        "s_waitcnt lgkmcnt(2)\n"
        "v_mfma_f32_16x16x32_bf16 v[44:47], a[252:255], v[20:23], v[44:47]\n"
        "v_mfma_f32_16x16x32_bf16 v[48:51], v[80:83], v[20:23], v[48:51]\n"
        "v_mfma_f32_16x16x32_bf16 v[52:55], v[84:87], v[20:23], v[52:55]\n"
        "ds_read_b128 v[20:23], v0 offset:47104\n"
        "v_mfma_f32_16x16x32_bf16 v[56:59], a[252:255], v[24:27], v[56:59]\n"
        "v_mfma_f32_16x16x32_bf16 v[60:63], v[80:83], v[24:27], v[60:63]\n"
        "v_mfma_f32_16x16x32_bf16 v[64:67], v[84:87], v[24:27], v[64:67]\n"
        "ds_read_b128 v[24:27], v0 offset:47360\n"
        "s_waitcnt lgkmcnt(2)\n"
        "v_mfma_f32_16x16x32_bf16 v[44:47], v[88:91], v[12:15], v[44:47]\n"
        "v_mfma_f32_16x16x32_bf16 v[48:51], v[92:95], v[12:15], v[48:51]\n"
        "s_waitcnt vmcnt(1)\n"
        "s_barrier\n"
        "v_mfma_f32_16x16x32_bf16 v[52:55], v[96:99], v[12:15], v[52:55]\n"
        "ds_read_b128 v[12:15], v8 offset:0\n"
        "v_mfma_f32_16x16x32_bf16 v[56:59], v[88:91], v[16:19], v[56:59]\n"
        "v_mfma_f32_16x16x32_bf16 v[60:63], v[92:95], v[16:19], v[60:63]\n"
        "v_mfma_f32_16x16x32_bf16 v[64:67], v[96:99], v[16:19], v[64:67]\n"
        "ds_read_b128 v[16:19], v8 offset:256\n"
        "s_waitcnt lgkmcnt(2)\n"
        "v_mfma_f32_16x16x32_bf16 v[44:47], v[100:103], v[20:23], v[44:47]\n"
        "s_waitcnt vmcnt(0)\n"
        "v_cmp_gt_i32 vcc, s70, v4\n"
        "v_mfma_f32_16x16x32_bf16 v[48:51], v[104:107], v[20:23], v[48:51]\n"
        "s_and_b64 vcc, vcc, 63\n"
        "v_mfma_f32_16x16x32_bf16 v[52:55], v[108:111], v[20:23], v[52:55]\n"
        "ds_read_b128 v[20:23], v8 offset:2048\n"
        "v_mfma_f32_16x16x32_bf16 v[56:59], v[100:103], v[24:27], v[56:59]\n"
        "v_mfma_f32_16x16x32_bf16 v[60:63], v[104:107], v[24:27], v[60:63]\n"
        "v_mfma_f32_16x16x32_bf16 v[64:67], v[108:111], v[24:27], v[64:67]\n"
        "ds_read_b128 v[24:27], v8 offset:2304\n"
        "s_waitcnt lgkmcnt(2)\n"
        "s_cbranch_vccnz .Lf2_c20_%=\n"
        "s_branch .Lf2_c21_%=\n"
        ".Lf2_c10_%=:\n"
        "v_mfma_f32_16x16x32_bf16 v[44:47], a[144:147], v[12:15], v[44:47]\n"
        "v_mfma_f32_16x16x32_bf16 v[48:51], a[148:151], v[12:15], v[48:51]\n"
        "v_mfma_f32_16x16x32_bf16 v[52:55], a[152:155], v[12:15], v[52:55]\n"
        "ds_read_b128 v[12:15], v0 offset:28672\n"
        "v_mfma_f32_16x16x32_bf16 v[56:59], a[144:147], v[16:19], v[56:59]\n"
        "v_mfma_f32_16x16x32_bf16 v[60:63], a[148:151], v[16:19], v[60:63]\n"
        "v_mfma_f32_16x16x32_bf16 v[64:67], a[152:155], v[16:19], v[64:67]\n"
        "ds_read_b128 v[16:19], v0 offset:28928\n"
        "s_waitcnt lgkmcnt(2)\n"
        "v_mfma_f32_16x16x32_bf16 v[44:47], a[156:159], v[20:23], v[44:47]\n"
        "v_mfma_f32_16x16x32_bf16 v[48:51], a[160:163], v[20:23], v[48:51]\n"
        "v_mfma_f32_16x16x32_bf16 v[52:55], a[164:167], v[20:23], v[52:55]\n"
        "ds_read_b128 v[20:23], v0 offset:30720\n"
        "v_mfma_f32_16x16x32_bf16 v[56:59], a[156:159], v[24:27], v[56:59]\n"
        "v_mfma_f32_16x16x32_bf16 v[60:63], a[160:163], v[24:27], v[60:63]\n"
        "v_mfma_f32_16x16x32_bf16 v[64:67], a[164:167], v[24:27], v[64:67]\n"
        "ds_read_b128 v[24:27], v0 offset:30976\n"
        "s_waitcnt lgkmcnt(2)\n"
        "v_mfma_f32_16x16x32_bf16 v[44:47], a[168:171], v[12:15], v[44:47]\n"
        "v_mfma_f32_16x16x32_bf16 v[48:51], a[172:175], v[12:15], v[48:51]\n"
        "v_mfma_f32_16x16x32_bf16 v[52:55], a[176:179], v[12:15], v[52:55]\n"
        "ds_read_b128 v[12:15], v0 offset:32768\n"
        "v_mfma_f32_16x16x32_bf16 v[56:59], a[168:171], v[16:19], v[56:59]\n"
        "v_mfma_f32_16x16x32_bf16 v[60:63], a[172:175], v[16:19], v[60:63]\n"
        "v_mfma_f32_16x16x32_bf16 v[64:67], a[176:179], v[16:19], v[64:67]\n"
        "ds_read_b128 v[16:19], v0 offset:33024\n"
        "s_waitcnt lgkmcnt(2)\n"
        "v_mfma_f32_16x16x32_bf16 v[44:47], a[180:183], v[20:23], v[44:47]\n"
        "v_mfma_f32_16x16x32_bf16 v[48:51], a[184:187], v[20:23], v[48:51]\n"
        "v_mfma_f32_16x16x32_bf16 v[52:55], a[188:191], v[20:23], v[52:55]\n"
        "ds_read_b128 v[20:23], v0 offset:34816\n"
        "v_mfma_f32_16x16x32_bf16 v[56:59], a[180:183], v[24:27], v[56:59]\n"
        "v_mfma_f32_16x16x32_bf16 v[60:63], a[184:187], v[24:27], v[60:63]\n"
        "v_mfma_f32_16x16x32_bf16 v[64:67], a[188:191], v[24:27], v[64:67]\n"
        "ds_read_b128 v[24:27], v0 offset:35072\n"
        "s_waitcnt lgkmcnt(2)\n"
        "v_mfma_f32_16x16x32_bf16 v[44:47], a[192:195], v[12:15], v[44:47]\n"
        "v_mfma_f32_16x16x32_bf16 v[48:51], a[196:199], v[12:15], v[48:51]\n"
        "v_mfma_f32_16x16x32_bf16 v[52:55], a[200:203], v[12:15], v[52:55]\n"
        "ds_read_b128 v[12:15], v0 offset:36864\n"
        "v_mfma_f32_16x16x32_bf16 v[56:59], a[192:195], v[16:19], v[56:59]\n"
        "v_mfma_f32_16x16x32_bf16 v[60:63], a[196:199], v[16:19], v[60:63]\n"
        "v_mfma_f32_16x16x32_bf16 v[64:67], a[200:203], v[16:19], v[64:67]\n"
        "ds_read_b128 v[16:19], v0 offset:37120\n"
        "s_waitcnt lgkmcnt(2)\n"
        "v_mfma_f32_16x16x32_bf16 v[44:47], a[204:207], v[20:23], v[44:47]\n"
        "v_mfma_f32_16x16x32_bf16 v[48:51], a[208:211], v[20:23], v[48:51]\n"
        "v_mfma_f32_16x16x32_bf16 v[52:55], a[212:215], v[20:23], v[52:55]\n"
        "ds_read_b128 v[20:23], v0 offset:38912\n"
        "v_mfma_f32_16x16x32_bf16 v[56:59], a[204:207], v[24:27], v[56:59]\n"
        "v_mfma_f32_16x16x32_bf16 v[60:63], a[208:211], v[24:27], v[60:63]\n"
        "v_mfma_f32_16x16x32_bf16 v[64:67], a[212:215], v[24:27], v[64:67]\n"
        "ds_read_b128 v[24:27], v0 offset:39168\n"
        "s_waitcnt lgkmcnt(2)\n"
        "v_mfma_f32_16x16x32_bf16 v[44:47], a[216:219], v[12:15], v[44:47]\n"
        "buffer_load_dword v4, v2, s[40:43], s76 offen sc1\n"
        "v_mfma_f32_16x16x32_bf16 v[48:51], a[220:223], v[12:15], v[48:51]\n"
        "v_mfma_f32_16x16x32_bf16 v[52:55], a[224:227], v[12:15], v[52:55]\n"
        "ds_read_b128 v[12:15], v0 offset:40960\n"
        "v_mfma_f32_16x16x32_bf16 v[56:59], a[216:219], v[16:19], v[56:59]\n"
        "v_mfma_f32_16x16x32_bf16 v[60:63], a[220:223], v[16:19], v[60:63]\n"
        "v_mfma_f32_16x16x32_bf16 v[64:67], a[224:227], v[16:19], v[64:67]\n"
        "ds_read_b128 v[16:19], v0 offset:41216\n"
        "s_waitcnt lgkmcnt(2)\n"
        "v_mfma_f32_16x16x32_bf16 v[44:47], a[228:231], v[20:23], v[44:47]\n"
        "v_mfma_f32_16x16x32_bf16 v[48:51], a[232:235], v[20:23], v[48:51]\n"
        "v_mfma_f32_16x16x32_bf16 v[52:55], a[236:239], v[20:23], v[52:55]\n"
        "ds_read_b128 v[20:23], v0 offset:43008\n"
        "v_mfma_f32_16x16x32_bf16 v[56:59], a[228:231], v[24:27], v[56:59]\n"
        "v_mfma_f32_16x16x32_bf16 v[60:63], a[232:235], v[24:27], v[60:63]\n"
        "v_mfma_f32_16x16x32_bf16 v[64:67], a[236:239], v[24:27], v[64:67]\n"
        "ds_read_b128 v[24:27], v0 offset:43264\n"
        "s_waitcnt lgkmcnt(2)\n"
        "v_mfma_f32_16x16x32_bf16 v[44:47], a[240:243], v[12:15], v[44:47]\n"
        "v_mfma_f32_16x16x32_bf16 v[48:51], a[244:247], v[12:15], v[48:51]\n"
        "v_mfma_f32_16x16x32_bf16 v[52:55], a[248:251], v[12:15], v[52:55]\n"
        "ds_read_b128 v[12:15], v0 offset:45056\n"
        "v_mfma_f32_16x16x32_bf16 v[56:59], a[240:243], v[16:19], v[56:59]\n"
        "v_mfma_f32_16x16x32_bf16 v[60:63], a[244:247], v[16:19], v[60:63]\n"
        "v_mfma_f32_16x16x32_bf16 v[64:67], a[248:251], v[16:19], v[64:67]\n"
        "ds_read_b128 v[16:19], v0 offset:45312\n"
        "s_waitcnt lgkmcnt(2)\n"
        "v_mfma_f32_16x16x32_bf16 v[44:47], a[252:255], v[20:23], v[44:47]\n"
        "v_mfma_f32_16x16x32_bf16 v[48:51], v[80:83], v[20:23], v[48:51]\n"
        "v_mfma_f32_16x16x32_bf16 v[52:55], v[84:87], v[20:23], v[52:55]\n"
        "ds_read_b128 v[20:23], v0 offset:47104\n"
        "v_mfma_f32_16x16x32_bf16 v[56:59], a[252:255], v[24:27], v[56:59]\n"
        "v_mfma_f32_16x16x32_bf16 v[60:63], v[80:83], v[24:27], v[60:63]\n"
        "v_mfma_f32_16x16x32_bf16 v[64:67], v[84:87], v[24:27], v[64:67]\n"
        "ds_read_b128 v[24:27], v0 offset:47360\n"
        "s_waitcnt lgkmcnt(2)\n"
        "v_mfma_f32_16x16x32_bf16 v[44:47], v[88:91], v[12:15], v[44:47]\n"
        "v_mfma_f32_16x16x32_bf16 v[48:51], v[92:95], v[12:15], v[48:51]\n"
        "v_mfma_f32_16x16x32_bf16 v[52:55], v[96:99], v[12:15], v[52:55]\n"
        "v_mfma_f32_16x16x32_bf16 v[56:59], v[88:91], v[16:19], v[56:59]\n"
        "v_mfma_f32_16x16x32_bf16 v[60:63], v[92:95], v[16:19], v[60:63]\n"
        "v_mfma_f32_16x16x32_bf16 v[64:67], v[96:99], v[16:19], v[64:67]\n"
        "s_waitcnt lgkmcnt(0)\n"
        "v_mfma_f32_16x16x32_bf16 v[44:47], v[100:103], v[20:23], v[44:47]\n"
        "v_mfma_f32_16x16x32_bf16 v[48:51], v[104:107], v[20:23], v[48:51]\n"
        "v_mfma_f32_16x16x32_bf16 v[52:55], v[108:111], v[20:23], v[52:55]\n"
        "v_mfma_f32_16x16x32_bf16 v[56:59], v[100:103], v[24:27], v[56:59]\n"
        "v_mfma_f32_16x16x32_bf16 v[60:63], v[104:107], v[24:27], v[60:63]\n"
        "v_mfma_f32_16x16x32_bf16 v[64:67], v[108:111], v[24:27], v[64:67]\n"
        "s_waitcnt vmcnt(0)\n"
        "v_cmp_gt_i32 vcc, s70, v4\n"
        "s_nop 0\n"
        "s_and_b64 vcc, vcc, 63\n"
        "s_cbranch_vccnz .Lf2_late1_%=\n"
        "s_add_u32 m0, s61, 49152\n"
        "s_add_u32 s71, s71, 16384\n"
        "buffer_load_dwordx4 v1, s[40:43], s71 offen offset:0 sc1 lds\n"
        "buffer_load_dwordx4 v1, s[40:43], s71 offen offset:1024 sc1 lds\n"
        "s_add_u32 m0, m0, 8192\n"
        "s_add_u32 s71, s71, 16384\n"
        "buffer_load_dwordx4 v1, s[40:43], s71 offen offset:0 sc1 lds\n"
        "buffer_load_dwordx4 v1, s[40:43], s71 offen offset:1024 sc1 lds\n"
        "s_add_u32 m0, m0, 8192\n"
        "s_add_u32 s71, s71, 16384\n"
        "buffer_load_dwordx4 v1, s[40:43], s71 offen offset:0 sc1 lds\n"
        "buffer_load_dwordx4 v1, s[40:43], s71 offen offset:1024 sc1 lds\n"
        "s_add_u32 s69, s69, 1\n"
        "s_add_u32 s70, s70, 1\n"
        "s_branch .Lf2_go1_%=\n"
        ".Lf2_late1_%=:\n"
        "s_mov_b32 s64, 0\n"
        ".Lf2_in1_top_%=:\n"
        "buffer_load_dword v4, v2, s[40:43], s76 offen sc1\n"
        "s_waitcnt vmcnt(0)\n"
        "v_cmp_gt_i32 vcc, s70, v4\n"
        "s_nop 0\n"
        "s_and_b64 vcc, vcc, 63\n"
        "s_cbranch_vccz .Lf2_in1_ok_%=\n"
        "s_add_u32 s64, s64, 1\n"
        "s_and_b32 s65, s64, 255\n"
        "s_cbranch_scc1 .Lf2_in1_go_%=\n"
        "s_mov_b32 s65, 40960\n"
        "s_cmp_lt_u32 s64, 1048576\n"
        "s_cbranch_scc1 .Lf2_in1_chk_%=\n"
        "v_mov_b32 v4, 1\n"
        "buffer_store_dword v4, off, s[40:43], s65 sc1\n"
        "s_waitcnt vmcnt(0)\n"
        ".Lf2_in1_chk_%=:\n"
        "buffer_load_dword v4, off, s[40:43], s65 sc1\n"
        "s_waitcnt vmcnt(0)\n"
        "v_readfirstlane_b32 s65, v4\n"
        "s_nop 0\n"
        "s_cmp_lg_u32 s65, 1\n"
        "s_cbranch_scc1 .Lf2_in1_go_%=\n"
        "s_endpgm\n"
        ".Lf2_in1_go_%=:\n"
        "s_sleep 1\n"
        "s_branch .Lf2_in1_top_%=\n"
        ".Lf2_in1_ok_%=:\n"
        "s_add_u32 m0, s61, 49152\n"
        "s_add_u32 s71, s71, 16384\n"
        "buffer_load_dwordx4 v1, s[40:43], s71 offen offset:0 sc1 lds\n"
        "buffer_load_dwordx4 v1, s[40:43], s71 offen offset:1024 sc1 lds\n"
        "s_add_u32 m0, m0, 8192\n"
        "s_add_u32 s71, s71, 16384\n"
        "buffer_load_dwordx4 v1, s[40:43], s71 offen offset:0 sc1 lds\n"
        "buffer_load_dwordx4 v1, s[40:43], s71 offen offset:1024 sc1 lds\n"
        "s_add_u32 m0, m0, 8192\n"
        "s_add_u32 s71, s71, 16384\n"
        "buffer_load_dwordx4 v1, s[40:43], s71 offen offset:0 sc1 lds\n"
        "buffer_load_dwordx4 v1, s[40:43], s71 offen offset:1024 sc1 lds\n"
        "s_add_u32 s69, s69, 1\n"
        "s_add_u32 s70, s70, 1\n"
        ".Lf2_go1_%=:\n"
        "buffer_load_dword v4, v2, s[40:43], s76 offen sc1\n"
        "s_waitcnt vmcnt(0)\n"
        "s_barrier\n"
        "ds_read_b128 v[12:15], v8 offset:0\n"
        "ds_read_b128 v[16:19], v8 offset:256\n"
        "ds_read_b128 v[20:23], v8 offset:2048\n"
        "ds_read_b128 v[24:27], v8 offset:2304\n"
        "v_cmp_gt_i32 vcc, s70, v4\n"
        "s_nop 0\n"
        "s_and_b64 vcc, vcc, 63\n"
        "s_waitcnt lgkmcnt(2)\n"
        "s_cbranch_vccnz .Lf2_c20_%=\n"
        "s_branch .Lf2_c21_%=\n"
        ".Lf2_c21_%=:\n"
        "v_mfma_f32_16x16x32_bf16 v[44:47], v[112:115], v[12:15], v[44:47]\n"
        "s_add_u32 m0, s61, 0\n"
        "s_lshr_b32 s65, s69, 1\n"
        "v_mfma_f32_16x16x32_bf16 v[48:51], v[116:119], v[12:15], v[48:51]\n"
        "s_bitcmp1_b32 s54, s65\n"
        "s_cselect_b32 s65, 8192, 0\n"
        "v_mfma_f32_16x16x32_bf16 v[52:55], v[120:123], v[12:15], v[52:55]\n"
        "ds_read_b128 v[12:15], v8 offset:4096\n"
        "s_lshl_b32 s71, s69, 20\n"
        "v_mfma_f32_16x16x32_bf16 v[56:59], v[112:115], v[16:19], v[56:59]\n"
        "s_add_u32 s71, s71, s65\n"
        "s_add_u32 s71, s71, s58\n"
        "v_mfma_f32_16x16x32_bf16 v[60:63], v[116:119], v[16:19], v[60:63]\n"
        "buffer_load_dwordx4 v1, s[40:43], s71 offen offset:0 sc1 lds\n"
        "buffer_load_dwordx4 v1, s[40:43], s71 offen offset:1024 sc1 lds\n"
        "v_mfma_f32_16x16x32_bf16 v[64:67], v[120:123], v[16:19], v[64:67]\n"
        "ds_read_b128 v[16:19], v8 offset:4352\n"
        "s_waitcnt lgkmcnt(2)\n"
        "v_mfma_f32_16x16x32_bf16 v[44:47], v[124:127], v[20:23], v[44:47]\n"
        "s_add_u32 m0, m0, 8192\n"
        "s_add_u32 s71, s71, 16384\n"
        "v_mfma_f32_16x16x32_bf16 v[48:51], v[128:131], v[20:23], v[48:51]\n"
        "buffer_load_dwordx4 v1, s[40:43], s71 offen offset:0 sc1 lds\n"
        "v_mfma_f32_16x16x32_bf16 v[52:55], v[132:135], v[20:23], v[52:55]\n"
        "ds_read_b128 v[20:23], v8 offset:6144\n"
        "v_mfma_f32_16x16x32_bf16 v[56:59], v[124:127], v[24:27], v[56:59]\n"
        "v_mfma_f32_16x16x32_bf16 v[60:63], v[128:131], v[24:27], v[60:63]\n"
        "buffer_load_dwordx4 v1, s[40:43], s71 offen offset:1024 sc1 lds\n"
        "s_add_u32 m0, m0, 8192\n"
        "v_mfma_f32_16x16x32_bf16 v[64:67], v[132:135], v[24:27], v[64:67]\n"
        "ds_read_b128 v[24:27], v8 offset:6400\n"
        "s_waitcnt lgkmcnt(2)\n"
        "v_mfma_f32_16x16x32_bf16 v[44:47], v[136:139], v[12:15], v[44:47]\n"
        "s_add_u32 s71, s71, 16384\n"
        "v_mfma_f32_16x16x32_bf16 v[48:51], v[140:143], v[12:15], v[48:51]\n"
        "buffer_load_dwordx4 v1, s[40:43], s71 offen offset:0 sc1 lds\n"
        "v_mfma_f32_16x16x32_bf16 v[52:55], v[144:147], v[12:15], v[52:55]\n"
        "ds_read_b128 v[12:15], v8 offset:8192\n"
        "v_mfma_f32_16x16x32_bf16 v[56:59], v[136:139], v[16:19], v[56:59]\n"
        "v_mfma_f32_16x16x32_bf16 v[60:63], v[140:143], v[16:19], v[60:63]\n"
        "buffer_load_dwordx4 v1, s[40:43], s71 offen offset:1024 sc1 lds\n"
        "s_add_u32 s70, s70, 1\n"
        "v_mfma_f32_16x16x32_bf16 v[64:67], v[144:147], v[16:19], v[64:67]\n"
        "ds_read_b128 v[16:19], v8 offset:8448\n"
        "s_waitcnt lgkmcnt(2)\n"
        "v_mfma_f32_16x16x32_bf16 v[44:47], v[148:151], v[20:23], v[44:47]\n"
        "v_mfma_f32_16x16x32_bf16 v[48:51], v[152:155], v[20:23], v[48:51]\n"
        "v_mfma_f32_16x16x32_bf16 v[52:55], v[156:159], v[20:23], v[52:55]\n"
        "ds_read_b128 v[20:23], v8 offset:10240\n"
        "v_mfma_f32_16x16x32_bf16 v[56:59], v[148:151], v[24:27], v[56:59]\n"
        "v_mfma_f32_16x16x32_bf16 v[60:63], v[152:155], v[24:27], v[60:63]\n"
        "v_mfma_f32_16x16x32_bf16 v[64:67], v[156:159], v[24:27], v[64:67]\n"
        "ds_read_b128 v[24:27], v8 offset:10496\n"
        "s_waitcnt lgkmcnt(2)\n"
        "v_mfma_f32_16x16x32_bf16 v[44:47], v[160:163], v[12:15], v[44:47]\n"
        "v_mfma_f32_16x16x32_bf16 v[48:51], v[164:167], v[12:15], v[48:51]\n"
        "v_mfma_f32_16x16x32_bf16 v[52:55], v[168:171], v[12:15], v[52:55]\n"
        "ds_read_b128 v[12:15], v8 offset:12288\n"
        "v_mfma_f32_16x16x32_bf16 v[56:59], v[160:163], v[16:19], v[56:59]\n"
        "v_mfma_f32_16x16x32_bf16 v[60:63], v[164:167], v[16:19], v[60:63]\n"
        "v_mfma_f32_16x16x32_bf16 v[64:67], v[168:171], v[16:19], v[64:67]\n"
        "ds_read_b128 v[16:19], v8 offset:12544\n"
        "s_waitcnt lgkmcnt(2)\n"
        "v_mfma_f32_16x16x32_bf16 v[44:47], v[172:175], v[20:23], v[44:47]\n"
        "v_mfma_f32_16x16x32_bf16 v[48:51], v[176:179], v[20:23], v[48:51]\n"
        "v_mfma_f32_16x16x32_bf16 v[52:55], v[180:183], v[20:23], v[52:55]\n"
        "ds_read_b128 v[20:23], v8 offset:14336\n"
        "v_mfma_f32_16x16x32_bf16 v[56:59], v[172:175], v[24:27], v[56:59]\n"
        "v_mfma_f32_16x16x32_bf16 v[60:63], v[176:179], v[24:27], v[60:63]\n"
        "v_mfma_f32_16x16x32_bf16 v[64:67], v[180:183], v[24:27], v[64:67]\n"
        "ds_read_b128 v[24:27], v8 offset:14592\n"
        "s_waitcnt lgkmcnt(2)\n"
        "v_mfma_f32_16x16x32_bf16 v[44:47], v[184:187], v[12:15], v[44:47]\n"
        "buffer_load_dword v4, v2, s[40:43], s76 offen sc1\n"
        "v_mfma_f32_16x16x32_bf16 v[48:51], v[188:191], v[12:15], v[48:51]\n"
        "v_mfma_f32_16x16x32_bf16 v[52:55], v[192:195], v[12:15], v[52:55]\n"
        "ds_read_b128 v[12:15], v8 offset:16384\n"
        "v_mfma_f32_16x16x32_bf16 v[56:59], v[184:187], v[16:19], v[56:59]\n"
        "v_mfma_f32_16x16x32_bf16 v[60:63], v[188:191], v[16:19], v[60:63]\n"
        "v_mfma_f32_16x16x32_bf16 v[64:67], v[192:195], v[16:19], v[64:67]\n"
        "ds_read_b128 v[16:19], v8 offset:16640\n"
        "s_waitcnt lgkmcnt(2)\n"
        "v_mfma_f32_16x16x32_bf16 v[44:47], v[196:199], v[20:23], v[44:47]\n"
        "v_mfma_f32_16x16x32_bf16 v[48:51], v[200:203], v[20:23], v[48:51]\n"
        "v_mfma_f32_16x16x32_bf16 v[52:55], v[204:207], v[20:23], v[52:55]\n"
        "ds_read_b128 v[20:23], v8 offset:18432\n"
        "v_mfma_f32_16x16x32_bf16 v[56:59], v[196:199], v[24:27], v[56:59]\n"
        "v_mfma_f32_16x16x32_bf16 v[60:63], v[200:203], v[24:27], v[60:63]\n"
        "v_mfma_f32_16x16x32_bf16 v[64:67], v[204:207], v[24:27], v[64:67]\n"
        "ds_read_b128 v[24:27], v8 offset:18688\n"
        "s_waitcnt lgkmcnt(2)\n"
        "v_mfma_f32_16x16x32_bf16 v[44:47], v[208:211], v[12:15], v[44:47]\n"
        "v_mfma_f32_16x16x32_bf16 v[48:51], v[212:215], v[12:15], v[48:51]\n"
        "v_mfma_f32_16x16x32_bf16 v[52:55], v[216:219], v[12:15], v[52:55]\n"
        "ds_read_b128 v[12:15], v8 offset:20480\n"
        "v_mfma_f32_16x16x32_bf16 v[56:59], v[208:211], v[16:19], v[56:59]\n"
        "v_mfma_f32_16x16x32_bf16 v[60:63], v[212:215], v[16:19], v[60:63]\n"
        "v_mfma_f32_16x16x32_bf16 v[64:67], v[216:219], v[16:19], v[64:67]\n"
        "ds_read_b128 v[16:19], v8 offset:20736\n"
        "s_waitcnt lgkmcnt(2)\n"
        "v_mfma_f32_16x16x32_bf16 v[44:47], v[220:223], v[20:23], v[44:47]\n"
        "v_mfma_f32_16x16x32_bf16 v[48:51], v[224:227], v[20:23], v[48:51]\n"
        "v_mfma_f32_16x16x32_bf16 v[52:55], v[228:231], v[20:23], v[52:55]\n"
        "ds_read_b128 v[20:23], v8 offset:22528\n"
        "v_mfma_f32_16x16x32_bf16 v[56:59], v[220:223], v[24:27], v[56:59]\n"
        "v_mfma_f32_16x16x32_bf16 v[60:63], v[224:227], v[24:27], v[60:63]\n"
        "v_mfma_f32_16x16x32_bf16 v[64:67], v[228:231], v[24:27], v[64:67]\n"
        "ds_read_b128 v[24:27], v8 offset:22784\n"
        "s_waitcnt lgkmcnt(2)\n"
        "v_mfma_f32_16x16x32_bf16 v[44:47], v[232:235], v[12:15], v[44:47]\n"
        "v_mfma_f32_16x16x32_bf16 v[48:51], v[236:239], v[12:15], v[48:51]\n"
        "s_waitcnt vmcnt(1)\n"
        "s_barrier\n"
        "v_mfma_f32_16x16x32_bf16 v[52:55], v[240:243], v[12:15], v[52:55]\n"
        "ds_read_b128 v[12:15], v0 offset:0\n"
        "v_mfma_f32_16x16x32_bf16 v[56:59], v[232:235], v[16:19], v[56:59]\n"
        "v_mfma_f32_16x16x32_bf16 v[60:63], v[236:239], v[16:19], v[60:63]\n"
        "v_mfma_f32_16x16x32_bf16 v[64:67], v[240:243], v[16:19], v[64:67]\n"
        "ds_read_b128 v[16:19], v0 offset:256\n"
        "s_waitcnt lgkmcnt(2)\n"
        "v_mfma_f32_16x16x32_bf16 v[44:47], v[244:247], v[20:23], v[44:47]\n"
        "s_waitcnt vmcnt(0)\n"
        "v_cmp_gt_i32 vcc, s70, v4\n"
        "v_mfma_f32_16x16x32_bf16 v[48:51], v[248:251], v[20:23], v[48:51]\n"
        "s_and_b64 vcc, vcc, 63\n"
        "v_mfma_f32_16x16x32_bf16 v[52:55], v[252:255], v[20:23], v[52:55]\n"
        "ds_read_b128 v[20:23], v0 offset:2048\n"
        "v_cvt_pk_bf16_f32 v68, v44, v45\n"
        "v_cvt_pk_bf16_f32 v69, v46, v47\n"
        "v_mfma_f32_16x16x32_bf16 v[56:59], v[244:247], v[24:27], v[56:59]\n"
        "v_cvt_pk_bf16_f32 v70, v48, v49\n"
        "v_cvt_pk_bf16_f32 v71, v50, v51\n"
        "v_mfma_f32_16x16x32_bf16 v[60:63], v[248:251], v[24:27], v[60:63]\n"
        "v_cvt_pk_bf16_f32 v76, v52, v53\n"
        "v_cvt_pk_bf16_f32 v77, v54, v55\n"
        "v_mfma_f32_16x16x32_bf16 v[64:67], v[252:255], v[24:27], v[64:67]\n"
        "ds_read_b128 v[24:27], v0 offset:2304\n"
        "s_waitcnt lgkmcnt(2)\n"
        "v_cvt_pk_bf16_f32 v72, v56, v57\n"
        "v_cvt_pk_bf16_f32 v73, v58, v59\n"
        "s_cbranch_vccnz .Lf2_c00_%=\n"
        "s_branch .Lf2_c01_%=\n"
        ".Lf2_c20_%=:\n"
        "v_mfma_f32_16x16x32_bf16 v[44:47], v[112:115], v[12:15], v[44:47]\n"
        "v_mfma_f32_16x16x32_bf16 v[48:51], v[116:119], v[12:15], v[48:51]\n"
        "v_mfma_f32_16x16x32_bf16 v[52:55], v[120:123], v[12:15], v[52:55]\n"
        "ds_read_b128 v[12:15], v8 offset:4096\n"
        "v_mfma_f32_16x16x32_bf16 v[56:59], v[112:115], v[16:19], v[56:59]\n"
        "v_mfma_f32_16x16x32_bf16 v[60:63], v[116:119], v[16:19], v[60:63]\n"
        "v_mfma_f32_16x16x32_bf16 v[64:67], v[120:123], v[16:19], v[64:67]\n"
        "ds_read_b128 v[16:19], v8 offset:4352\n"
        "s_waitcnt lgkmcnt(2)\n"
        "v_mfma_f32_16x16x32_bf16 v[44:47], v[124:127], v[20:23], v[44:47]\n"
        "v_mfma_f32_16x16x32_bf16 v[48:51], v[128:131], v[20:23], v[48:51]\n"
        "v_mfma_f32_16x16x32_bf16 v[52:55], v[132:135], v[20:23], v[52:55]\n"
        "ds_read_b128 v[20:23], v8 offset:6144\n"
        "v_mfma_f32_16x16x32_bf16 v[56:59], v[124:127], v[24:27], v[56:59]\n"
        "v_mfma_f32_16x16x32_bf16 v[60:63], v[128:131], v[24:27], v[60:63]\n"
        "v_mfma_f32_16x16x32_bf16 v[64:67], v[132:135], v[24:27], v[64:67]\n"
        "ds_read_b128 v[24:27], v8 offset:6400\n"
        "s_waitcnt lgkmcnt(2)\n"
        "v_mfma_f32_16x16x32_bf16 v[44:47], v[136:139], v[12:15], v[44:47]\n"
        "v_mfma_f32_16x16x32_bf16 v[48:51], v[140:143], v[12:15], v[48:51]\n"
        "v_mfma_f32_16x16x32_bf16 v[52:55], v[144:147], v[12:15], v[52:55]\n"
        "ds_read_b128 v[12:15], v8 offset:8192\n"
        "v_mfma_f32_16x16x32_bf16 v[56:59], v[136:139], v[16:19], v[56:59]\n"
        "v_mfma_f32_16x16x32_bf16 v[60:63], v[140:143], v[16:19], v[60:63]\n"
        "v_mfma_f32_16x16x32_bf16 v[64:67], v[144:147], v[16:19], v[64:67]\n"
        "ds_read_b128 v[16:19], v8 offset:8448\n"
        "s_waitcnt lgkmcnt(2)\n"
        "v_mfma_f32_16x16x32_bf16 v[44:47], v[148:151], v[20:23], v[44:47]\n"
        "v_mfma_f32_16x16x32_bf16 v[48:51], v[152:155], v[20:23], v[48:51]\n"
        "v_mfma_f32_16x16x32_bf16 v[52:55], v[156:159], v[20:23], v[52:55]\n"
        "ds_read_b128 v[20:23], v8 offset:10240\n"
        "v_mfma_f32_16x16x32_bf16 v[56:59], v[148:151], v[24:27], v[56:59]\n"
        "v_mfma_f32_16x16x32_bf16 v[60:63], v[152:155], v[24:27], v[60:63]\n"
        "v_mfma_f32_16x16x32_bf16 v[64:67], v[156:159], v[24:27], v[64:67]\n"
        "ds_read_b128 v[24:27], v8 offset:10496\n"
        "s_waitcnt lgkmcnt(2)\n"
        "v_mfma_f32_16x16x32_bf16 v[44:47], v[160:163], v[12:15], v[44:47]\n"
        "v_mfma_f32_16x16x32_bf16 v[48:51], v[164:167], v[12:15], v[48:51]\n"
        "v_mfma_f32_16x16x32_bf16 v[52:55], v[168:171], v[12:15], v[52:55]\n"
        "ds_read_b128 v[12:15], v8 offset:12288\n"
        "v_mfma_f32_16x16x32_bf16 v[56:59], v[160:163], v[16:19], v[56:59]\n"
        "v_mfma_f32_16x16x32_bf16 v[60:63], v[164:167], v[16:19], v[60:63]\n"
        "v_mfma_f32_16x16x32_bf16 v[64:67], v[168:171], v[16:19], v[64:67]\n"
        "ds_read_b128 v[16:19], v8 offset:12544\n"
        "s_waitcnt lgkmcnt(2)\n"
        "v_mfma_f32_16x16x32_bf16 v[44:47], v[172:175], v[20:23], v[44:47]\n"
        "v_mfma_f32_16x16x32_bf16 v[48:51], v[176:179], v[20:23], v[48:51]\n"
        "v_mfma_f32_16x16x32_bf16 v[52:55], v[180:183], v[20:23], v[52:55]\n"
        "ds_read_b128 v[20:23], v8 offset:14336\n"
        "v_mfma_f32_16x16x32_bf16 v[56:59], v[172:175], v[24:27], v[56:59]\n"
        "v_mfma_f32_16x16x32_bf16 v[60:63], v[176:179], v[24:27], v[60:63]\n"
        "v_mfma_f32_16x16x32_bf16 v[64:67], v[180:183], v[24:27], v[64:67]\n"
        "ds_read_b128 v[24:27], v8 offset:14592\n"
        "s_waitcnt lgkmcnt(2)\n"
        "v_mfma_f32_16x16x32_bf16 v[44:47], v[184:187], v[12:15], v[44:47]\n"
        "buffer_load_dword v4, v2, s[40:43], s76 offen sc1\n"
        "v_mfma_f32_16x16x32_bf16 v[48:51], v[188:191], v[12:15], v[48:51]\n"
        "v_mfma_f32_16x16x32_bf16 v[52:55], v[192:195], v[12:15], v[52:55]\n"
        "ds_read_b128 v[12:15], v8 offset:16384\n"
        "v_mfma_f32_16x16x32_bf16 v[56:59], v[184:187], v[16:19], v[56:59]\n"
        "v_mfma_f32_16x16x32_bf16 v[60:63], v[188:191], v[16:19], v[60:63]\n"
        "v_mfma_f32_16x16x32_bf16 v[64:67], v[192:195], v[16:19], v[64:67]\n"
        "ds_read_b128 v[16:19], v8 offset:16640\n"
        "s_waitcnt lgkmcnt(2)\n"
        "v_mfma_f32_16x16x32_bf16 v[44:47], v[196:199], v[20:23], v[44:47]\n"
        "v_mfma_f32_16x16x32_bf16 v[48:51], v[200:203], v[20:23], v[48:51]\n"
        "v_mfma_f32_16x16x32_bf16 v[52:55], v[204:207], v[20:23], v[52:55]\n"
        "ds_read_b128 v[20:23], v8 offset:18432\n"
        "v_mfma_f32_16x16x32_bf16 v[56:59], v[196:199], v[24:27], v[56:59]\n"
        "v_mfma_f32_16x16x32_bf16 v[60:63], v[200:203], v[24:27], v[60:63]\n"
        "v_mfma_f32_16x16x32_bf16 v[64:67], v[204:207], v[24:27], v[64:67]\n"
        "ds_read_b128 v[24:27], v8 offset:18688\n"
        "s_waitcnt lgkmcnt(2)\n"
        "v_mfma_f32_16x16x32_bf16 v[44:47], v[208:211], v[12:15], v[44:47]\n"
        "v_mfma_f32_16x16x32_bf16 v[48:51], v[212:215], v[12:15], v[48:51]\n"
        "v_mfma_f32_16x16x32_bf16 v[52:55], v[216:219], v[12:15], v[52:55]\n"
        "ds_read_b128 v[12:15], v8 offset:20480\n"
        "v_mfma_f32_16x16x32_bf16 v[56:59], v[208:211], v[16:19], v[56:59]\n"
        "v_mfma_f32_16x16x32_bf16 v[60:63], v[212:215], v[16:19], v[60:63]\n"
        "v_mfma_f32_16x16x32_bf16 v[64:67], v[216:219], v[16:19], v[64:67]\n"
        "ds_read_b128 v[16:19], v8 offset:20736\n"
        "s_waitcnt lgkmcnt(2)\n"
        "v_mfma_f32_16x16x32_bf16 v[44:47], v[220:223], v[20:23], v[44:47]\n"
        "v_mfma_f32_16x16x32_bf16 v[48:51], v[224:227], v[20:23], v[48:51]\n"
        "v_mfma_f32_16x16x32_bf16 v[52:55], v[228:231], v[20:23], v[52:55]\n"
        "ds_read_b128 v[20:23], v8 offset:22528\n"
        "v_mfma_f32_16x16x32_bf16 v[56:59], v[220:223], v[24:27], v[56:59]\n"
        "v_mfma_f32_16x16x32_bf16 v[60:63], v[224:227], v[24:27], v[60:63]\n"
        "v_mfma_f32_16x16x32_bf16 v[64:67], v[228:231], v[24:27], v[64:67]\n"
        "ds_read_b128 v[24:27], v8 offset:22784\n"
        "s_waitcnt lgkmcnt(2)\n"
        "v_mfma_f32_16x16x32_bf16 v[44:47], v[232:235], v[12:15], v[44:47]\n"
        "v_mfma_f32_16x16x32_bf16 v[48:51], v[236:239], v[12:15], v[48:51]\n"
        "v_mfma_f32_16x16x32_bf16 v[52:55], v[240:243], v[12:15], v[52:55]\n"
        "v_mfma_f32_16x16x32_bf16 v[56:59], v[232:235], v[16:19], v[56:59]\n"
        "v_mfma_f32_16x16x32_bf16 v[60:63], v[236:239], v[16:19], v[60:63]\n"
        "v_mfma_f32_16x16x32_bf16 v[64:67], v[240:243], v[16:19], v[64:67]\n"
        "s_waitcnt lgkmcnt(0)\n"
        "v_mfma_f32_16x16x32_bf16 v[44:47], v[244:247], v[20:23], v[44:47]\n"
        "v_mfma_f32_16x16x32_bf16 v[48:51], v[248:251], v[20:23], v[48:51]\n"
        "v_mfma_f32_16x16x32_bf16 v[52:55], v[252:255], v[20:23], v[52:55]\n"
        "v_cvt_pk_bf16_f32 v68, v44, v45\n"
        "v_cvt_pk_bf16_f32 v69, v46, v47\n"
        "v_mfma_f32_16x16x32_bf16 v[56:59], v[244:247], v[24:27], v[56:59]\n"
        "v_cvt_pk_bf16_f32 v70, v48, v49\n"
        "v_cvt_pk_bf16_f32 v71, v50, v51\n"
        "v_mfma_f32_16x16x32_bf16 v[60:63], v[248:251], v[24:27], v[60:63]\n"
        "v_cvt_pk_bf16_f32 v76, v52, v53\n"
        "v_cvt_pk_bf16_f32 v77, v54, v55\n"
        "v_mfma_f32_16x16x32_bf16 v[64:67], v[252:255], v[24:27], v[64:67]\n"
        "v_cvt_pk_bf16_f32 v72, v56, v57\n"
        "v_cvt_pk_bf16_f32 v73, v58, v59\n"
        "s_nop 5\n"
        "v_cvt_pk_bf16_f32 v74, v60, v61\n"
        "v_cvt_pk_bf16_f32 v75, v62, v63\n"
        "v_cvt_pk_bf16_f32 v78, v64, v65\n"
        "v_cvt_pk_bf16_f32 v79, v66, v67\n"
        "buffer_store_dwordx4 v[68:71], v6, s[40:43], s72 offen sc1\n"
        "s_add_u32 s66, s72, 147456\n"
        "buffer_store_dwordx4 v[72:75], v6, s[40:43], s66 offen sc1\n"
        "v_permlane16_swap_b32 v76, v78\n"
        "v_permlane16_swap_b32 v77, v79\n"
        "buffer_store_dwordx4 v[76:79], v7, s[40:43], s72 offen sc1\n"
        "s_waitcnt vmcnt(3)\n"
        "v_cmp_gt_i32 vcc, s70, v4\n"
        "s_nop 0\n"
        "s_and_b64 vcc, vcc, 63\n"
        "s_cbranch_vccnz .Lf2_late2_%=\n"
        "s_add_u32 m0, s61, 0\n"
        "s_lshr_b32 s65, s69, 1\n"
        "s_bitcmp1_b32 s54, s65\n"
        "s_cselect_b32 s65, 8192, 0\n"
        "s_lshl_b32 s71, s69, 20\n"
        "s_add_u32 s71, s71, s65\n"
        "s_add_u32 s71, s71, s58\n"
        "buffer_load_dwordx4 v1, s[40:43], s71 offen offset:0 sc1 lds\n"
        "buffer_load_dwordx4 v1, s[40:43], s71 offen offset:1024 sc1 lds\n"
        "s_add_u32 m0, m0, 8192\n"
        "s_add_u32 s71, s71, 16384\n"
        "buffer_load_dwordx4 v1, s[40:43], s71 offen offset:0 sc1 lds\n"
        "buffer_load_dwordx4 v1, s[40:43], s71 offen offset:1024 sc1 lds\n"
        "s_add_u32 m0, m0, 8192\n"
        "s_add_u32 s71, s71, 16384\n"
        "buffer_load_dwordx4 v1, s[40:43], s71 offen offset:0 sc1 lds\n"
        "buffer_load_dwordx4 v1, s[40:43], s71 offen offset:1024 sc1 lds\n"
        "s_add_u32 s70, s70, 1\n"
        "s_waitcnt vmcnt(6)\n"
        "v_mov_b32 v5, s74\n"
        "buffer_store_dword v5, v3, s[40:43], s73 offen sc1\n"
        "s_branch .Lf2_go2_%=\n"
        ".Lf2_late2_%=:\n"
        "s_waitcnt vmcnt(0)\n"
        "v_mov_b32 v5, s74\n"
        "buffer_store_dword v5, v3, s[40:43], s73 offen sc1\n"
        "s_cmp_eq_u32 s68, 63\n"
        "s_cbranch_scc0 .Lf2_wait2b_%=\n"
        "s_endpgm\n"
        ".Lf2_wait2_%=:\n"
        "s_mov_b32 s70, 0\n"
        ".Lf2_wait2b_%=:\n"
        "s_mov_b32 s64, 0\n"
        ".Lf2_in2_top_%=:\n"
        "buffer_load_dword v4, v2, s[40:43], s76 offen sc1\n"
        "s_waitcnt vmcnt(0)\n"
        "v_cmp_gt_i32 vcc, s70, v4\n"
        "s_nop 0\n"
        "s_and_b64 vcc, vcc, 63\n"
        "s_cbranch_vccz .Lf2_in2_ok_%=\n"
        "s_add_u32 s64, s64, 1\n"
        "s_and_b32 s65, s64, 255\n"
        "s_cbranch_scc1 .Lf2_in2_go_%=\n"
        "s_mov_b32 s65, 40960\n"
        "s_cmp_lt_u32 s64, 1048576\n"
        "s_cbranch_scc1 .Lf2_in2_chk_%=\n"
        "v_mov_b32 v4, 1\n"
        "buffer_store_dword v4, off, s[40:43], s65 sc1\n"
        "s_waitcnt vmcnt(0)\n"
        ".Lf2_in2_chk_%=:\n"
        "buffer_load_dword v4, off, s[40:43], s65 sc1\n"
        "s_waitcnt vmcnt(0)\n"
        "v_readfirstlane_b32 s65, v4\n"
        "s_nop 0\n"
        "s_cmp_lg_u32 s65, 1\n"
        "s_cbranch_scc1 .Lf2_in2_go_%=\n"
        "s_endpgm\n"
        ".Lf2_in2_go_%=:\n"
        "s_sleep 1\n"
        "s_branch .Lf2_in2_top_%=\n"
        ".Lf2_in2_ok_%=:\n"
        "s_add_u32 m0, s61, 0\n"
        "s_lshr_b32 s65, s69, 1\n"
        "s_bitcmp1_b32 s54, s65\n"
        "s_cselect_b32 s65, 8192, 0\n"
        "s_lshl_b32 s71, s69, 20\n"
        "s_add_u32 s71, s71, s65\n"
        "s_add_u32 s71, s71, s58\n"
        "buffer_load_dwordx4 v1, s[40:43], s71 offen offset:0 sc1 lds\n"
        "buffer_load_dwordx4 v1, s[40:43], s71 offen offset:1024 sc1 lds\n"
        "s_add_u32 m0, m0, 8192\n"
        "s_add_u32 s71, s71, 16384\n"
        "buffer_load_dwordx4 v1, s[40:43], s71 offen offset:0 sc1 lds\n"
        "buffer_load_dwordx4 v1, s[40:43], s71 offen offset:1024 sc1 lds\n"
        "s_add_u32 m0, m0, 8192\n"
        "s_add_u32 s71, s71, 16384\n"
        "buffer_load_dwordx4 v1, s[40:43], s71 offen offset:0 sc1 lds\n"
        "buffer_load_dwordx4 v1, s[40:43], s71 offen offset:1024 sc1 lds\n"
        "s_add_u32 s70, s70, 1\n"
        ".Lf2_go2_%=:\n"
        "s_mov_b32 s72, 241172480\n"
        "s_mov_b32 s73, 241172480\n"
        "buffer_load_dword v4, v2, s[40:43], s76 offen sc1\n"
        "s_waitcnt vmcnt(0)\n"
        "s_barrier\n"
        "ds_read_b128 v[12:15], v0 offset:0\n"
        "ds_read_b128 v[16:19], v0 offset:256\n"
        "ds_read_b128 v[20:23], v0 offset:2048\n"
        "ds_read_b128 v[24:27], v0 offset:2304\n"
        "v_cmp_gt_i32 vcc, s70, v4\n"
        "s_nop 0\n"
        "s_and_b64 vcc, vcc, 63\n"
        "s_waitcnt lgkmcnt(2)\n"
        "s_cbranch_vccnz .Lf2_c00_%=\n"
        "s_branch .Lf2_c01_%=\n"
            :: "s"(p.ws), "s"(p.w2), "s"(p.w2), "s"(p.w2), "s"(pair(w, delay)), "s"(pair(x4, pr)),
               "s"(pair(65 * PAGE + spread(B_H + kap * 73728) + w * 2048 + XF2L, 97 * PAGE + spread(C_P5) + XF2S)), "s"(pair(flag(F_F2 + fl), w * 2048)),
               "v"(pair((g >> 1) * 1024 + (g & 1) * 512 + i * 16, lane * 16)), "v"(pair(lane < 6 ? flag(F_F1 + 128 * nu + 24 * kap + 8 * (lane >> 1) + 2 * w + (lane & 1)) : OOB, fo4)), "v"(pair(tr + 64 + 8 * pp, 0)),
               "v"(pair(s01, s2)), "v"(pair(vo[0], vo[1])), "v"(pair(vo[2], tr + 16 * pp))
            : "v0", "v1", "v2", "v3", "v4", "v5", "v6", "v7", "v8", "v9", "v10", "v11", "v12", "v13", "v14", "v15", "v16", "v17", "v18", "v19", "v20", "v21", "v22", "v23", "v24", "v25", "v26", "v27", "v28", "v29", "v30", "v31", "v32", "v33", "v34", "v35", "v36", "v37", "v38", "v39", "v40", "v41", "v42", "v43", "v44", "v45", "v46", "v47", "v48", "v49", "v50", "v51", "v52", "v53", "v54", "v55", "v56", "v57", "v58", "v59", "v60", "v61", "v62", "v63", "v64", "v65", "v66", "v67", "v68", "v69", "v70", "v71", "v72", "v73", "v74", "v75", "v76", "v77", "v78", "v79", "v255", "a255", "s30", "s31", "s32", "s33", "s34", "s35", "s36", "s37", "s38", "s39", "s40", "s41", "s42", "s43", "s44", "s45", "s46", "s47", "s48", "s49", "s50", "s51", "s52", "s53", "s54", "s55", "s56", "s57", "s58", "s59", "s60", "s61", "s62", "s63", "s64", "s65", "s66", "s67", "s68", "s69", "s70", "s71", "s72", "s73", "s74", "s75", "s76", "s77", "s78", "s79", "s80", "s81", "s82", "s83", "s84", "s85", "s86", "s87", "s88", "s89", "s90", "s91", "s92", "s93", "s94", "s95", "s96", "s97", "s98", "s99", "vcc", "scc", "memory");
    }
    __builtin_unreachable();
}

__device__ __forceinline__ void role_attn(const Params& p, int h0, int x4, int pr) {
    const unsigned lane = threadIdx.x & 63, i = lane & 15, g = lane >> 4;
    const int w = rfl(threadIdx.x >> 6), h = h0 + w;
    asm volatile(
        "s_nop 4\n"
        "s_mov_b64 s[40:41], %0\n"
        "s_and_b32 s41, s41, 0xffff\n"
        "s_mov_b32 s42, 236978176\n"
        "s_mov_b32 s43, 0x20000\n"
        "s_mov_b64 s[56:57], %1\n"
        "s_mov_b64 s[58:59], %2\n"
        "s_mov_b64 s[60:61], %3\n"
        "v_mov_b64 v[0:1], %4\n"
        "v_mov_b64 v[2:3], %5\n"
        "v_mov_b64 v[4:5], %6\n"
        "v_cmp_ge_i32 s[76:77], 0, v5\n"
        "v_cmp_ge_i32 s[78:79], -1, v5\n"
        "v_cmp_ge_i32 s[80:81], -2, v5\n"
        "v_cmp_ge_i32 s[82:83], -3, v5\n"
        "s_mov_b32 s84, 0\n"
        "s_mov_b32 s85, -1\n"
        "s_mov_b32 s75, 0x3e0293ee\n"
        "v_lshrrev_b32 v12, 4, v0\n"
        "s_lshl_b32 s66, s57, 7\n"
        "s_mov_b32 s64, 0\n"
        ".Lat_pair_top_%=:\n"
        "s_mov_b64 exec, 1\n"
        "buffer_load_dword v13, off, s[40:43], s66 sc1\n"
        "s_mov_b64 exec, -1\n"
        "s_waitcnt vmcnt(0)\n"
        "v_cmp_gt_i32 vcc, 0, v13\n"
        "s_nop 0\n"
        "s_and_b64 vcc, vcc, 1\n"
        "s_cbranch_vccz .Lat_pair_ok_%=\n"
        "s_add_u32 s64, s64, 1\n"
        "s_and_b32 s65, s64, 255\n"
        "s_cbranch_scc1 .Lat_pair_go_%=\n"
        "s_mov_b32 s65, 40960\n"
        "s_cmp_lt_u32 s64, 1048576\n"
        "s_cbranch_scc1 .Lat_pair_chk_%=\n"
        "v_mov_b32 v13, 1\n"
        "buffer_store_dword v13, off, s[40:43], s65 sc1\n"
        "s_waitcnt vmcnt(0)\n"
        ".Lat_pair_chk_%=:\n"
        "buffer_load_dword v13, off, s[40:43], s65 sc1\n"
        "s_waitcnt vmcnt(0)\n"
        "v_readfirstlane_b32 s65, v13\n"
        "s_nop 0\n"
        "s_cmp_lg_u32 s65, 1\n"
        "s_cbranch_scc1 .Lat_pair_go_%=\n"
        "s_endpgm\n"
        ".Lat_pair_go_%=:\n"
        "s_sleep 2\n"
        "s_branch .Lat_pair_top_%=\n"
        ".Lat_pair_ok_%=:\n"
        "v_readfirstlane_b32 s50, v13\n"
        "s_nop 0\n"
        "s_xor_b32 s50, s50, s56\n"
        "v_lshlrev_b32 v14, 7, v12\n"
        "s_sub_u32 s66, 0, s50\n"
        "s_mov_b32 s52, 24576\n"
        "s_mov_b32 s64, 0\n"
        ".Lat_pg0_top_%=:\n"
        "buffer_load_dword v13, v14, s[40:43], s52 offen sc1\n"
        "s_waitcnt vmcnt(0)\n"
        "v_cmp_gt_i32 vcc, 0, v13\n"
        "s_cbranch_vccz .Lat_pg0_ok_%=\n"
        "s_add_u32 s64, s64, 1\n"
        "s_and_b32 s65, s64, 255\n"
        "s_cbranch_scc1 .Lat_pg0_go_%=\n"
        "s_mov_b32 s65, 40960\n"
        "s_cmp_lt_u32 s64, 1048576\n"
        "s_cbranch_scc1 .Lat_pg0_chk_%=\n"
        "v_mov_b32 v13, 1\n"
        "buffer_store_dword v13, off, s[40:43], s65 sc1\n"
        "s_waitcnt vmcnt(0)\n"
        ".Lat_pg0_chk_%=:\n"
        "buffer_load_dword v13, off, s[40:43], s65 sc1\n"
        "s_waitcnt vmcnt(0)\n"
        "v_readfirstlane_b32 s65, v13\n"
        "s_nop 0\n"
        "s_cmp_lg_u32 s65, 1\n"
        "s_cbranch_scc1 .Lat_pg0_go_%=\n"
        "s_endpgm\n"
        ".Lat_pg0_go_%=:\n"
        "s_sleep 2\n"
        "s_branch .Lat_pg0_top_%=\n"
        ".Lat_pg0_ok_%=:\n"
        "v_cmp_eq_u32 s[52:53], 1, v13\n"
        "s_nop 1\n"
        "s_mov_b32 s54, 32768\n"
        "s_mov_b32 s64, 0\n"
        ".Lat_pg1_top_%=:\n"
        "buffer_load_dword v13, v14, s[40:43], s54 offen sc1\n"
        "s_waitcnt vmcnt(0)\n"
        "v_cmp_gt_i32 vcc, 0, v13\n"
        "s_cbranch_vccz .Lat_pg1_ok_%=\n"
        "s_add_u32 s64, s64, 1\n"
        "s_and_b32 s65, s64, 255\n"
        "s_cbranch_scc1 .Lat_pg1_go_%=\n"
        "s_mov_b32 s65, 40960\n"
        "s_cmp_lt_u32 s64, 1048576\n"
        "s_cbranch_scc1 .Lat_pg1_chk_%=\n"
        "v_mov_b32 v13, 1\n"
        "buffer_store_dword v13, off, s[40:43], s65 sc1\n"
        "s_waitcnt vmcnt(0)\n"
        ".Lat_pg1_chk_%=:\n"
        "buffer_load_dword v13, off, s[40:43], s65 sc1\n"
        "s_waitcnt vmcnt(0)\n"
        "v_readfirstlane_b32 s65, v13\n"
        "s_nop 0\n"
        "s_cmp_lg_u32 s65, 1\n"
        "s_cbranch_scc1 .Lat_pg1_go_%=\n"
        "s_endpgm\n"
        ".Lat_pg1_go_%=:\n"
        "s_sleep 2\n"
        "s_branch .Lat_pg1_top_%=\n"
        ".Lat_pg1_ok_%=:\n"
        "v_cmp_eq_u32 s[54:55], 1, v13\n"
        "s_nop 1\n"
        "s_mov_b32 vcc_lo, 0x55555555\n"
        "s_mov_b32 vcc_hi, 0x55555555\n"
        "s_xor_b64 vcc, vcc, s[52:53]\n"
        "s_bcnt1_i32_b64 s65, vcc\n"
        "s_sub_u32 s64, s65, 23\n"
        "s_cmp_lt_u32 s64, 19\n"
        "s_cbranch_scc1 .Lat_rawmap_%=\n"
        "s_cmp_lt_u32 s65, 32\n"
        "s_cselect_b32 s65, 0, -1\n"
        "s_xor_b32 s52, s65, 0x55555555\n"
        "s_xor_b32 s53, s65, 0x55555555\n"
        "s_xor_b32 s54, s65, 0x55555555\n"
        "s_xor_b32 s55, s65, 0xaaaaaaaa\n"
        ".Lat_rawmap_%=:\n"
        "s_xor_b32 s52, s52, s66\n"
        "s_xor_b32 s53, s53, s66\n"
        "s_xor_b32 s54, s54, s66\n"
        "s_xor_b32 s55, s55, s66\n"
        "s_and_b32 s71, s55, 1\n"
        "s_lshl_b32 s71, s71, 13\n"
        "s_add_u32 s60, s60, s71\n"
        "v_mov_b64 v[166:167], 0\n"
        "v_mov_b64 v[172:173], 0\n"
        "v_mov_b64 v[176:177], 0\n"
        "v_mov_b64 v[178:179], 0\n"
        "v_mov_b64 v[182:183], 0\n"
        "v_mov_b32 v10, 0xff61b1e6\n"
        "s_mov_b32 s68, 0\n"
        "s_mov_b32 s72, 0\n"
        "s_bitcmp1_b64 s[52:53], s68\n"
        "s_cselect_b32 s65, 8192, 0\n"
        "s_lshl_b32 s70, s68, 21\n"
        "s_add_u32 s70, s70, s65\n"
        ".Lat_top_%=:\n"
        "s_mov_b32 s69, s70\n"
        "s_mul_i32 s67, s68, 3\n"
        "s_mov_b32 s64, 0\n"
        ".Lat_more_%=:\n"
        "buffer_load_dword v5, v1, s[40:43], s71 offen sc1\n"
        "s_mov_b32 s86, s72\n"
        "s_waitcnt vmcnt(0)\n"
        "v_min_i32_dpp v5, v5, v5 quad_perm:[1,0,3,2] row_mask:0xf bank_mask:0xf\n"
        "s_nop 1\n"
        "v_min_i32_dpp v5, v5, v5 quad_perm:[2,3,0,1] row_mask:0xf bank_mask:0xf\n"
        "s_nop 0\n"
        "v_readlane_b32 s73, v5, 0\n"
        "v_readlane_b32 s74, v5, 4\n"
        "s_nop 0\n"
        "s_bitcmp1_b32 s72, 0\n"
        "s_cbranch_scc1 .Lat_hasK_%=\n"
        "s_add_u32 s65, s67, 0\n"
        "s_cmp_ge_i32 s73, s65\n"
        "s_cbranch_scc0 .Lat_hasK_%=\n"
        "s_add_u32 s65, s69, s58\n"
        "s_add_u32 s65, s65, 524288\n"
        "s_add_u32 s66, s65, 4096\n"
        "buffer_load_dwordx4 v[48:51], v0, s[40:43], s65 offen offset:0 sc1\n"
        "buffer_load_dwordx4 v[52:55], v0, s[40:43], s65 offen offset:1024 sc1\n"
        "buffer_load_dwordx4 v[56:59], v0, s[40:43], s65 offen offset:2048 sc1\n"
        "buffer_load_dwordx4 v[60:63], v0, s[40:43], s65 offen offset:3072 sc1\n"
        "buffer_load_dwordx4 v[64:67], v0, s[40:43], s66 offen offset:0 sc1\n"
        "buffer_load_dwordx4 v[68:71], v0, s[40:43], s66 offen offset:1024 sc1\n"
        "buffer_load_dwordx4 v[72:75], v0, s[40:43], s66 offen offset:2048 sc1\n"
        "buffer_load_dwordx4 v[76:79], v0, s[40:43], s66 offen offset:3072 sc1\n"
        "s_or_b32 s72, s72, 1\n"
        ".Lat_hasK_%=:\n"
        "s_bitcmp1_b32 s72, 1\n"
        "s_cbranch_scc1 .Lat_hasV_%=\n"
        "s_add_u32 s65, s67, 1\n"
        "s_cmp_ge_i32 s73, s65\n"
        "s_cbranch_scc0 .Lat_hasV_%=\n"
        "s_add_u32 s65, s69, s58\n"
        "s_add_u32 s65, s65, 1048576\n"
        "s_add_u32 s66, s65, 4096\n"
        "buffer_load_dwordx4 v[80:83], v0, s[40:43], s65 offen offset:0 sc1\n"
        "buffer_load_dwordx4 v[84:87], v0, s[40:43], s65 offen offset:1024 sc1\n"
        "buffer_load_dwordx4 v[88:91], v0, s[40:43], s65 offen offset:2048 sc1\n"
        "buffer_load_dwordx4 v[92:95], v0, s[40:43], s65 offen offset:3072 sc1\n"
        "buffer_load_dwordx4 v[96:99], v0, s[40:43], s66 offen offset:0 sc1\n"
        "buffer_load_dwordx4 v[100:103], v0, s[40:43], s66 offen offset:1024 sc1\n"
        "buffer_load_dwordx4 v[104:107], v0, s[40:43], s66 offen offset:2048 sc1\n"
        "buffer_load_dwordx4 v[108:111], v0, s[40:43], s66 offen offset:3072 sc1\n"
        "s_or_b32 s72, s72, 2\n"
        ".Lat_hasV_%=:\n"
        "s_bitcmp1_b32 s72, 3\n"
        "s_cbranch_scc1 .Lat_hasM_%=\n"
        "s_cmp_ge_i32 s74, s68\n"
        "s_cbranch_scc0 .Lat_hasM_%=\n"
        "s_add_u32 s65, s69, s59\n"
        "buffer_load_dwordx4 v[112:115], v3, s[40:43], s65 offen offset:0 sc1\n"
        "buffer_load_dwordx4 v[116:119], v3, s[40:43], s65 offen offset:512 sc1\n"
        "buffer_load_dwordx4 v[120:123], v3, s[40:43], s65 offen offset:1024 sc1\n"
        "buffer_load_dwordx4 v[124:127], v3, s[40:43], s65 offen offset:1536 sc1\n"
        "buffer_load_dwordx4 v[128:131], v4, s[40:43], s65 offen offset:2048 sc1\n"
        "buffer_load_dwordx4 v[132:135], v4, s[40:43], s65 offen offset:2560 sc1\n"
        "buffer_load_dwordx4 v[136:139], v4, s[40:43], s65 offen offset:3072 sc1\n"
        "buffer_load_dwordx4 v[140:143], v4, s[40:43], s65 offen offset:3584 sc1\n"
        "s_or_b32 s72, s72, 8\n"
        ".Lat_hasM_%=:\n"
        "s_bitcmp1_b32 s72, 2\n"
        "s_cbranch_scc1 .Lat_hasQ_%=\n"
        "s_add_u32 s65, s67, 2\n"
        "s_cmp_ge_i32 s73, s65\n"
        "s_cbranch_scc0 .Lat_hasQ_%=\n"
        "s_add_u32 s65, s69, s58\n"
        "s_add_u32 s65, s65, 0\n"
        "s_add_u32 s66, s65, 4096\n"
        "buffer_load_dwordx4 v[16:19], v0, s[40:43], s65 offen offset:0 sc1\n"
        "buffer_load_dwordx4 v[20:23], v0, s[40:43], s65 offen offset:1024 sc1\n"
        "buffer_load_dwordx4 v[24:27], v0, s[40:43], s65 offen offset:2048 sc1\n"
        "buffer_load_dwordx4 v[28:31], v0, s[40:43], s65 offen offset:3072 sc1\n"
        "buffer_load_dwordx4 v[32:35], v0, s[40:43], s66 offen offset:0 sc1\n"
        "buffer_load_dwordx4 v[36:39], v0, s[40:43], s66 offen offset:1024 sc1\n"
        "buffer_load_dwordx4 v[40:43], v0, s[40:43], s66 offen offset:2048 sc1\n"
        "buffer_load_dwordx4 v[44:47], v0, s[40:43], s66 offen offset:3072 sc1\n"
        "s_or_b32 s72, s72, 4\n"
        ".Lat_hasQ_%=:\n"
        "s_and_b32 s65, s72, 7\n"
        "s_cmp_eq_u32 s65, 7\n"
        "s_cbranch_scc1 .Lat_T_%=\n"
        "s_add_u32 s64, s64, 1\n"
        "s_and_b32 s65, s64, 255\n"
        "s_cbranch_scc1 .Lat_sp_go_%=\n"
        "s_mov_b32 s65, 40960\n"
        "s_cmp_lt_u32 s64, 1048576\n"
        "s_cbranch_scc1 .Lat_sp_chk_%=\n"
        "v_mov_b32 v6, 1\n"
        "buffer_store_dword v6, off, s[40:43], s65 sc1\n"
        "s_waitcnt vmcnt(0)\n"
        ".Lat_sp_chk_%=:\n"
        "buffer_load_dword v6, off, s[40:43], s65 sc1\n"
        "s_waitcnt vmcnt(0)\n"
        "v_readfirstlane_b32 s65, v6\n"
        "s_nop 0\n"
        "s_cmp_lg_u32 s65, 1\n"
        "s_cbranch_scc1 .Lat_sp_go_%=\n"
        "s_endpgm\n"
        ".Lat_sp_go_%=:\n"
        "s_branch .Lat_more_%=\n"
        ".Lat_T_%=:\n"
        "s_add_u32 s66, s68, 1\n"
        "s_bitcmp1_b64 s[52:53], s66\n"
        "s_cselect_b32 s65, 8192, 0\n"
        "s_lshl_b32 s70, s66, 21\n"
        "s_add_u32 s70, s70, s65\n"
        "s_add_u32 s67, s67, 3\n"
        "s_cmp_eq_u32 s72, 15\n"
        "s_cbranch_scc1 .Lat_T2_%=\n"
        "s_waitcnt vmcnt(0)\n"
        "buffer_load_dword v5, v1, s[40:43], s71 offen sc1\n"
        "s_branch .Lat_T3_%=\n"
        ".Lat_T2_%=:\n"
        "s_bitcmp1_b32 s86, 2\n"
        "s_cbranch_scc1 .Lat_T3_%=\n"
        "s_waitcnt vmcnt(0)\n"
        ".Lat_T3_%=:\n"
        "v_mfma_f32_16x16x32_bf16 v[144:147], v[48:51], v[16:19], 0\n"
        "v_mfma_f32_16x16x32_bf16 v[152:155], v[52:55], v[20:23], 0\n"
        "v_mfma_f32_16x16x32_bf16 v[148:151], v[48:51], v[20:23], 0\n"
        "v_mfma_f32_16x16x32_bf16 v[144:147], v[56:59], v[24:27], v[144:147]\n"
        "v_mfma_f32_16x16x32_bf16 v[152:155], v[60:63], v[28:31], v[152:155]\n"
        "v_mfma_f32_16x16x32_bf16 v[148:151], v[56:59], v[28:31], v[148:151]\n"
        "v_mfma_f32_16x16x32_bf16 v[144:147], v[64:67], v[32:35], v[144:147]\n"
        "v_mfma_f32_16x16x32_bf16 v[152:155], v[68:71], v[36:39], v[152:155]\n"
        "v_mfma_f32_16x16x32_bf16 v[148:151], v[64:67], v[36:39], v[148:151]\n"
        "v_mfma_f32_16x16x32_bf16 v[144:147], v[72:75], v[40:43], v[144:147]\n"
        "v_mfma_f32_16x16x32_bf16 v[152:155], v[76:79], v[44:47], v[152:155]\n"
        "v_mfma_f32_16x16x32_bf16 v[148:151], v[72:75], v[44:47], v[148:151]\n"
        "v_cndmask_b32 v144, v10, v144, s[76:77]\n"
        "v_cndmask_b32 v145, v10, v145, s[78:79]\n"
        "v_cndmask_b32 v146, v10, v146, s[80:81]\n"
        "v_cndmask_b32 v147, v10, v147, s[82:83]\n"
        "v_cndmask_b32 v152, v10, v152, s[76:77]\n"
        "v_cndmask_b32 v153, v10, v153, s[78:79]\n"
        "v_cndmask_b32 v154, v10, v154, s[80:81]\n"
        "v_cndmask_b32 v155, v10, v155, s[82:83]\n"
        "v_max3_f32 v8, v144, v145, v146\n"
        "v_max3_f32 v9, v152, v153, v154\n"
        "v_max_f32 v8, v8, v147\n"
        "v_max3_f32 v9, v9, v155, v148\n"
        "v_max3_f32 v9, v9, v149, v150\n"
        "v_max_f32 v9, v9, v151\n"
        "v_mul_f32 v144, s75, v144\n"
        "v_mul_f32 v145, s75, v145\n"
        "v_mul_f32 v146, s75, v146\n"
        "v_mul_f32 v147, s75, v147\n"
        "v_mul_f32 v148, s75, v148\n"
        "v_mul_f32 v149, s75, v149\n"
        "v_mul_f32 v150, s75, v150\n"
        "v_mul_f32 v151, s75, v151\n"
        "v_mul_f32 v152, s75, v152\n"
        "v_mul_f32 v153, s75, v153\n"
        "v_mul_f32 v154, s75, v154\n"
        "v_mul_f32 v155, s75, v155\n"
        "s_cmp_eq_u32 s72, 15\n"
        "s_cbranch_scc1 .Lat_hasm_%=\n"
        "s_mov_b32 s64, 0\n"
        ".Lat_wm_%=:\n"
        "s_waitcnt vmcnt(0)\n"
        "v_min_i32_dpp v5, v5, v5 quad_perm:[1,0,3,2] row_mask:0xf bank_mask:0xf\n"
        "s_nop 1\n"
        "v_min_i32_dpp v5, v5, v5 quad_perm:[2,3,0,1] row_mask:0xf bank_mask:0xf\n"
        "s_nop 0\n"
        "v_readlane_b32 s73, v5, 0\n"
        "v_readlane_b32 s74, v5, 4\n"
        "s_nop 0\n"
        "s_cmp_ge_i32 s74, s68\n"
        "s_cbranch_scc1 .Lat_gm_%=\n"
        "s_add_u32 s64, s64, 1\n"
        "s_and_b32 s65, s64, 255\n"
        "s_cbranch_scc1 .Lat_sq_go_%=\n"
        "s_mov_b32 s65, 40960\n"
        "s_cmp_lt_u32 s64, 1048576\n"
        "s_cbranch_scc1 .Lat_sq_chk_%=\n"
        "v_mov_b32 v6, 1\n"
        "buffer_store_dword v6, off, s[40:43], s65 sc1\n"
        "s_waitcnt vmcnt(0)\n"
        ".Lat_sq_chk_%=:\n"
        "buffer_load_dword v6, off, s[40:43], s65 sc1\n"
        "s_waitcnt vmcnt(0)\n"
        "v_readfirstlane_b32 s65, v6\n"
        "s_nop 0\n"
        "s_cmp_lg_u32 s65, 1\n"
        "s_cbranch_scc1 .Lat_sq_go_%=\n"
        "s_endpgm\n"
        ".Lat_sq_go_%=:\n"
        "buffer_load_dword v5, v1, s[40:43], s71 offen sc1\n"
        "s_branch .Lat_wm_%=\n"
        ".Lat_gm_%=:\n"
        "s_add_u32 s65, s69, s59\n"
        "buffer_load_dwordx4 v[112:115], v3, s[40:43], s65 offen offset:0 sc1\n"
        "buffer_load_dwordx4 v[116:119], v3, s[40:43], s65 offen offset:512 sc1\n"
        "buffer_load_dwordx4 v[120:123], v3, s[40:43], s65 offen offset:1024 sc1\n"
        "buffer_load_dwordx4 v[124:127], v3, s[40:43], s65 offen offset:1536 sc1\n"
        "buffer_load_dwordx4 v[128:131], v4, s[40:43], s65 offen offset:2048 sc1\n"
        "buffer_load_dwordx4 v[132:135], v4, s[40:43], s65 offen offset:2560 sc1\n"
        "buffer_load_dwordx4 v[136:139], v4, s[40:43], s65 offen offset:3072 sc1\n"
        "buffer_load_dwordx4 v[140:143], v4, s[40:43], s65 offen offset:3584 sc1\n"
        ".Lat_hasm_%=:\n"
        "s_mov_b32 s72, 0\n"
        "s_add_u32 s65, s70, s58\n"
        "s_add_u32 s65, s65, 524288\n"
        "s_add_u32 s66, s67, 0\n"
        "s_cmp_ge_i32 s73, s66\n"
        "s_cselect_b32 s65, s65, 241172480\n"
        "s_cselect_b32 s66, 1, 0\n"
        "s_or_b32 s72, s72, s66\n"
        "s_add_u32 s66, s65, 4096\n"
        "buffer_load_dwordx4 v[48:51], v0, s[40:43], s65 offen offset:0 sc1\n"
        "buffer_load_dwordx4 v[52:55], v0, s[40:43], s65 offen offset:1024 sc1\n"
        "buffer_load_dwordx4 v[56:59], v0, s[40:43], s65 offen offset:2048 sc1\n"
        "buffer_load_dwordx4 v[60:63], v0, s[40:43], s65 offen offset:3072 sc1\n"
        "buffer_load_dwordx4 v[64:67], v0, s[40:43], s66 offen offset:0 sc1\n"
        "buffer_load_dwordx4 v[68:71], v0, s[40:43], s66 offen offset:1024 sc1\n"
        "buffer_load_dwordx4 v[72:75], v0, s[40:43], s66 offen offset:2048 sc1\n"
        "buffer_load_dwordx4 v[76:79], v0, s[40:43], s66 offen offset:3072 sc1\n"
        "s_add_u32 s65, s70, s58\n"
        "s_add_u32 s65, s65, 0\n"
        "s_add_u32 s66, s67, 2\n"
        "s_cmp_ge_i32 s73, s66\n"
        "s_cselect_b32 s65, s65, 241172480\n"
        "s_cselect_b32 s66, 4, 0\n"
        "s_or_b32 s72, s72, s66\n"
        "s_add_u32 s66, s65, 4096\n"
        "s_waitcnt vmcnt(15)\n"
        "v_mfma_f32_16x16x32_bf16 v[156:159], v[112:115], v[16:19], 0\n"
        "s_waitcnt vmcnt(14)\n"
        "v_mfma_f32_16x16x32_bf16 v[156:159], v[116:119], v[24:27], v[156:159]\n"
        "s_waitcnt vmcnt(13)\n"
        "v_mfma_f32_16x16x32_bf16 v[156:159], v[120:123], v[32:35], v[156:159]\n"
        "s_waitcnt vmcnt(12)\n"
        "v_mfma_f32_16x16x32_bf16 v[156:159], v[124:127], v[40:43], v[156:159]\n"
        "v_mfma_f32_16x16x32_bf16 v[160:163], v[112:115], v[20:23], 0\n"
        "v_mfma_f32_16x16x32_bf16 v[160:163], v[116:119], v[28:31], v[160:163]\n"
        "v_mfma_f32_16x16x32_bf16 v[160:163], v[120:123], v[36:39], v[160:163]\n"
        "v_mfma_f32_16x16x32_bf16 v[160:163], v[124:127], v[44:47], v[160:163]\n"
        "v_max3_f32 v8, v8, v156, v157\n"
        "buffer_load_dwordx4 v[16:19], v0, s[40:43], s65 offen offset:0 sc1\n"
        "v_max3_f32 v216, v8, v158, v159\n"
        "v_max3_f32 v217, v8, v158, v159\n"
        "v_mul_f32 v156, s75, v156\n"
        "v_mul_f32 v157, s75, v157\n"
        "v_permlane16_swap_b32 v216, v217\n"
        "v_max_f32 v8, v216, v217\n"
        "buffer_load_dwordx4 v[20:23], v0, s[40:43], s65 offen offset:1024 sc1\n"
        "v_max_f32 v216, v216, v217\n"
        "v_mul_f32 v158, s75, v158\n"
        "v_mul_f32 v159, s75, v159\n"
        "v_permlane32_swap_b32 v8, v216\n"
        "v_max_f32 v8, v8, v216\n"
        "v_mul_f32 v8, s75, v8\n"
        "buffer_load_dwordx4 v[24:27], v0, s[40:43], s65 offen offset:2048 sc1\n"
        "v_cndmask_b32 v216, v8, -v10, s[84:85]\n"
        "v_sub_f32 v144, v144, v8\n"
        "v_exp_f32 v144, v144\n"
        "v_sub_f32 v145, v145, v8\n"
        "v_exp_f32 v145, v145\n"
        "v_sub_f32 v146, v146, v8\n"
        "buffer_load_dwordx4 v[28:31], v0, s[40:43], s65 offen offset:3072 sc1\n"
        "v_exp_f32 v146, v146\n"
        "v_sub_f32 v147, v147, v8\n"
        "v_exp_f32 v147, v147\n"
        "v_sub_f32 v156, v156, v216\n"
        "v_exp_f32 v156, v156\n"
        "v_sub_f32 v157, v157, v216\n"
        "buffer_load_dwordx4 v[32:35], v0, s[40:43], s66 offen offset:0 sc1\n"
        "v_exp_f32 v157, v157\n"
        "v_sub_f32 v158, v158, v216\n"
        "v_exp_f32 v158, v158\n"
        "v_sub_f32 v159, v159, v216\n"
        "v_exp_f32 v159, v159\n"
        "v_add_f32 v216, v144, v145\n"
        "buffer_load_dwordx4 v[36:39], v0, s[40:43], s66 offen offset:1024 sc1\n"
        "v_add_f32 v217, v146, v147\n"
        "v_add_f32 v216, v216, v156\n"
        "v_add_f32 v217, v217, v157\n"
        "v_add_f32 v216, v216, v158\n"
        "v_add_f32 v217, v217, v159\n"
        "v_add_f32 v8, v216, v217\n"
        "buffer_load_dwordx4 v[40:43], v0, s[40:43], s66 offen offset:2048 sc1\n"
        "v_add_f32 v216, v216, v217\n"
        "s_nop 1\n"
        "v_permlane16_swap_b32 v8, v216\n"
        "v_add_f32 v217, v8, v216\n"
        "v_add_f32 v216, v8, v216\n"
        "s_nop 1\n"
        "buffer_load_dwordx4 v[44:47], v0, s[40:43], s66 offen offset:3072 sc1\n"
        "v_permlane32_swap_b32 v216, v217\n"
        "v_add_f32 v8, v216, v217\n"
        "v_rcp_f32 v8, v8\n"
        "s_nop 0\n"
        "v_mul_f32 v156, v156, v8\n"
        "v_mul_f32 v157, v157, v8\n"
        "v_mul_f32 v158, v158, v8\n"
        "v_mul_f32 v159, v159, v8\n"
        "v_cvt_pk_bf16_f32 v174, v156, v157\n"
        "v_cvt_pk_bf16_f32 v175, v158, v159\n"
        "v_mul_f32 v144, v144, v8\n"
        "v_mul_f32 v145, v145, v8\n"
        "v_cvt_pk_bf16_f32 v164, v144, v145\n"
        "v_mul_f32 v146, v146, v8\n"
        "v_mul_f32 v147, v147, v8\n"
        "v_cvt_pk_bf16_f32 v165, v146, v147\n"
        "buffer_load_dword v5, v1, s[40:43], s71 offen sc1\n"
        "s_add_u32 s65, s69, s58\n"
        "s_add_u32 s66, s65, 4096\n"
        "s_waitcnt vmcnt(17)\n"
        "v_mfma_f32_16x16x32_bf16 v[184:187], v[128:131], v[174:177], 0\n"
        "v_max3_f32 v9, v9, v160, v161\n"
        "v_max3_f32 v220, v9, v162, v163\n"
        "v_max3_f32 v221, v9, v162, v163\n"
        "v_mul_f32 v160, s75, v160\n"
        "v_mul_f32 v161, s75, v161\n"
        "v_mfma_f32_16x16x32_bf16 v[188:191], v[128:131], v[172:175], 0\n"
        "v_permlane16_swap_b32 v220, v221\n"
        "v_max_f32 v9, v220, v221\n"
        "v_max_f32 v220, v220, v221\n"
        "v_mul_f32 v162, s75, v162\n"
        "v_mul_f32 v163, s75, v163\n"
        "v_mfma_f32_16x16x32_bf16 v[184:187], v[80:83], v[164:167], v[184:187]\n"
        "v_permlane32_swap_b32 v9, v220\n"
        "v_max_f32 v9, v9, v220\n"
        "v_mul_f32 v9, s75, v9\n"
        "v_cndmask_b32 v220, v9, -v10, s[84:85]\n"
        "v_sub_f32 v148, v148, v9\n"
        "v_mfma_f32_16x16x32_bf16 v[188:191], v[84:87], v[164:167], v[188:191]\n"
        "v_exp_f32 v148, v148\n"
        "v_sub_f32 v149, v149, v9\n"
        "v_exp_f32 v149, v149\n"
        "v_sub_f32 v150, v150, v9\n"
        "v_mfma_f32_16x16x32_bf16 v[192:195], v[132:135], v[174:177], 0\n"
        "v_exp_f32 v150, v150\n"
        "v_sub_f32 v151, v151, v9\n"
        "v_exp_f32 v151, v151\n"
        "v_sub_f32 v152, v152, v9\n"
        "v_mfma_f32_16x16x32_bf16 v[196:199], v[132:135], v[172:175], 0\n"
        "v_cvt_pk_bf16_f32 v184, v184, v185\n"
        "v_cvt_pk_bf16_f32 v185, v186, v187\n"
        "v_cvt_pk_bf16_f32 v186, v188, v189\n"
        "v_cvt_pk_bf16_f32 v187, v190, v191\n"
        "buffer_store_dwordx4 v[184:187], v0, s[40:43], s65 offen offset:0 sc1\n"
        "v_mfma_f32_16x16x32_bf16 v[192:195], v[88:91], v[164:167], v[192:195]\n"
        "v_exp_f32 v152, v152\n"
        "v_sub_f32 v153, v153, v9\n"
        "v_exp_f32 v153, v153\n"
        "v_sub_f32 v154, v154, v9\n"
        "v_mfma_f32_16x16x32_bf16 v[196:199], v[92:95], v[164:167], v[196:199]\n"
        "v_exp_f32 v154, v154\n"
        "v_sub_f32 v155, v155, v9\n"
        "v_exp_f32 v155, v155\n"
        "v_sub_f32 v160, v160, v220\n"
        "v_mfma_f32_16x16x32_bf16 v[200:203], v[136:139], v[174:177], 0\n"
        "v_exp_f32 v160, v160\n"
        "v_sub_f32 v161, v161, v220\n"
        "v_exp_f32 v161, v161\n"
        "v_sub_f32 v162, v162, v220\n"
        "v_mfma_f32_16x16x32_bf16 v[204:207], v[136:139], v[172:175], 0\n"
        "v_cvt_pk_bf16_f32 v192, v192, v193\n"
        "v_cvt_pk_bf16_f32 v193, v194, v195\n"
        "v_cvt_pk_bf16_f32 v194, v196, v197\n"
        "v_cvt_pk_bf16_f32 v195, v198, v199\n"
        "buffer_store_dwordx4 v[192:195], v0, s[40:43], s65 offen offset:2048 sc1\n"
        "v_mfma_f32_16x16x32_bf16 v[200:203], v[96:99], v[164:167], v[200:203]\n"
        "v_exp_f32 v162, v162\n"
        "v_sub_f32 v163, v163, v220\n"
        "v_exp_f32 v163, v163\n"
        "v_add_f32 v220, v148, v149\n"
        "v_mfma_f32_16x16x32_bf16 v[204:207], v[100:103], v[164:167], v[204:207]\n"
        "v_add_f32 v221, v150, v151\n"
        "v_add_f32 v220, v220, v152\n"
        "v_add_f32 v221, v221, v153\n"
        "v_add_f32 v220, v220, v154\n"
        "v_add_f32 v221, v221, v155\n"
        "v_add_f32 v220, v220, v160\n"
        "v_mfma_f32_16x16x32_bf16 v[208:211], v[140:143], v[174:177], 0\n"
        "v_add_f32 v221, v221, v161\n"
        "v_add_f32 v220, v220, v162\n"
        "v_add_f32 v221, v221, v163\n"
        "v_add_f32 v9, v220, v221\n"
        "v_add_f32 v220, v220, v221\n"
        "v_mfma_f32_16x16x32_bf16 v[212:215], v[140:143], v[172:175], 0\n"
        "v_cvt_pk_bf16_f32 v200, v200, v201\n"
        "v_cvt_pk_bf16_f32 v201, v202, v203\n"
        "v_cvt_pk_bf16_f32 v202, v204, v205\n"
        "v_cvt_pk_bf16_f32 v203, v206, v207\n"
        "buffer_store_dwordx4 v[200:203], v0, s[40:43], s66 offen offset:0 sc1\n"
        "v_mfma_f32_16x16x32_bf16 v[208:211], v[104:107], v[164:167], v[208:211]\n"
        "s_nop 1\n"
        "v_permlane16_swap_b32 v9, v220\n"
        "v_add_f32 v221, v9, v220\n"
        "v_add_f32 v220, v9, v220\n"
        "v_mfma_f32_16x16x32_bf16 v[212:215], v[108:111], v[164:167], v[212:215]\n"
        "s_nop 1\n"
        "v_permlane32_swap_b32 v220, v221\n"
        "v_add_f32 v9, v220, v221\n"
        "v_rcp_f32 v9, v9\n"
        "s_nop 0\n"
        "v_mul_f32 v160, v160, v9\n"
        "v_mul_f32 v161, v161, v9\n"
        "v_mul_f32 v162, v162, v9\n"
        "v_mul_f32 v163, v163, v9\n"
        "v_cvt_pk_bf16_f32 v180, v160, v161\n"
        "v_cvt_pk_bf16_f32 v181, v162, v163\n"
        "v_mul_f32 v148, v148, v9\n"
        "v_mul_f32 v149, v149, v9\n"
        "v_cvt_pk_bf16_f32 v168, v148, v149\n"
        "v_mul_f32 v150, v150, v9\n"
        "v_mul_f32 v151, v151, v9\n"
        "v_cvt_pk_bf16_f32 v169, v150, v151\n"
        "v_mul_f32 v152, v152, v9\n"
        "v_mul_f32 v153, v153, v9\n"
        "v_cvt_pk_bf16_f32 v170, v152, v153\n"
        "v_mul_f32 v154, v154, v9\n"
        "v_mul_f32 v155, v155, v9\n"
        "v_cvt_pk_bf16_f32 v171, v154, v155\n"
        "v_cvt_pk_bf16_f32 v208, v208, v209\n"
        "v_cvt_pk_bf16_f32 v209, v210, v211\n"
        "v_cvt_pk_bf16_f32 v210, v212, v213\n"
        "v_cvt_pk_bf16_f32 v211, v214, v215\n"
        "buffer_store_dwordx4 v[208:211], v0, s[40:43], s66 offen offset:2048 sc1\n"
        "v_mfma_f32_16x16x32_bf16 v[184:187], v[128:131], v[180:183], 0\n"
        "v_mfma_f32_16x16x32_bf16 v[188:191], v[128:131], v[178:181], 0\n"
        "v_mfma_f32_16x16x32_bf16 v[184:187], v[80:83], v[168:171], v[184:187]\n"
        "v_mfma_f32_16x16x32_bf16 v[188:191], v[84:87], v[168:171], v[188:191]\n"
        "v_mfma_f32_16x16x32_bf16 v[192:195], v[132:135], v[180:183], 0\n"
        "v_mfma_f32_16x16x32_bf16 v[196:199], v[132:135], v[178:181], 0\n"
        "v_cvt_pk_bf16_f32 v184, v184, v185\n"
        "v_cvt_pk_bf16_f32 v185, v186, v187\n"
        "v_cvt_pk_bf16_f32 v186, v188, v189\n"
        "v_mfma_f32_16x16x32_bf16 v[192:195], v[88:91], v[168:171], v[192:195]\n"
        "v_cvt_pk_bf16_f32 v187, v190, v191\n"
        "buffer_store_dwordx4 v[184:187], v0, s[40:43], s65 offen offset:1024 sc1\n"
        "v_mfma_f32_16x16x32_bf16 v[196:199], v[92:95], v[168:171], v[196:199]\n"
        "v_mfma_f32_16x16x32_bf16 v[200:203], v[136:139], v[180:183], 0\n"
        "v_mfma_f32_16x16x32_bf16 v[204:207], v[136:139], v[178:181], 0\n"
        "v_cvt_pk_bf16_f32 v192, v192, v193\n"
        "v_cvt_pk_bf16_f32 v193, v194, v195\n"
        "v_cvt_pk_bf16_f32 v194, v196, v197\n"
        "v_mfma_f32_16x16x32_bf16 v[200:203], v[96:99], v[168:171], v[200:203]\n"
        "v_cvt_pk_bf16_f32 v195, v198, v199\n"
        "buffer_store_dwordx4 v[192:195], v0, s[40:43], s65 offen offset:3072 sc1\n"
        "v_mfma_f32_16x16x32_bf16 v[204:207], v[100:103], v[168:171], v[204:207]\n"
        "v_mfma_f32_16x16x32_bf16 v[208:211], v[140:143], v[180:183], 0\n"
        "v_mfma_f32_16x16x32_bf16 v[212:215], v[140:143], v[178:181], 0\n"
        "v_cvt_pk_bf16_f32 v200, v200, v201\n"
        "v_cvt_pk_bf16_f32 v201, v202, v203\n"
        "v_cvt_pk_bf16_f32 v202, v204, v205\n"
        "v_mfma_f32_16x16x32_bf16 v[208:211], v[104:107], v[168:171], v[208:211]\n"
        "v_cvt_pk_bf16_f32 v203, v206, v207\n"
        "buffer_store_dwordx4 v[200:203], v0, s[40:43], s66 offen offset:1024 sc1\n"
        "v_mfma_f32_16x16x32_bf16 v[212:215], v[108:111], v[168:171], v[212:215]\n"
        "s_waitcnt vmcnt(7)\n"
        "v_min_i32_dpp v5, v5, v5 quad_perm:[1,0,3,2] row_mask:0xf bank_mask:0xf\n"
        "s_nop 1\n"
        "v_min_i32_dpp v5, v5, v5 quad_perm:[2,3,0,1] row_mask:0xf bank_mask:0xf\n"
        "s_nop 0\n"
        "v_readlane_b32 s73, v5, 0\n"
        "v_readlane_b32 s74, v5, 4\n"
        "s_nop 0\n"
        "s_nop 0\n"
        "v_cvt_pk_bf16_f32 v208, v208, v209\n"
        "v_cvt_pk_bf16_f32 v209, v210, v211\n"
        "v_cvt_pk_bf16_f32 v210, v212, v213\n"
        "v_cvt_pk_bf16_f32 v211, v214, v215\n"
        "buffer_store_dwordx4 v[208:211], v0, s[40:43], s66 offen offset:3072 sc1\n"
        "s_add_u32 s65, s70, s58\n"
        "s_add_u32 s65, s65, 1048576\n"
        "s_add_u32 s66, s67, 1\n"
        "s_cmp_ge_i32 s73, s66\n"
        "s_cselect_b32 s65, s65, 241172480\n"
        "s_cselect_b32 s66, 2, 0\n"
        "s_or_b32 s72, s72, s66\n"
        "s_add_u32 s66, s65, 4096\n"
        "buffer_load_dwordx4 v[80:83], v0, s[40:43], s65 offen offset:0 sc1\n"
        "buffer_load_dwordx4 v[84:87], v0, s[40:43], s65 offen offset:1024 sc1\n"
        "buffer_load_dwordx4 v[88:91], v0, s[40:43], s65 offen offset:2048 sc1\n"
        "buffer_load_dwordx4 v[92:95], v0, s[40:43], s65 offen offset:3072 sc1\n"
        "buffer_load_dwordx4 v[96:99], v0, s[40:43], s66 offen offset:0 sc1\n"
        "buffer_load_dwordx4 v[100:103], v0, s[40:43], s66 offen offset:1024 sc1\n"
        "buffer_load_dwordx4 v[104:107], v0, s[40:43], s66 offen offset:2048 sc1\n"
        "buffer_load_dwordx4 v[108:111], v0, s[40:43], s66 offen offset:3072 sc1\n"
        "v_mov_b32 v6, s68\n"
        "s_waitcnt vmcnt(8)\n"
        "buffer_store_dword v6, v2, s[40:43], s60 offen sc1\n"
        "s_add_u32 s68, s68, 1\n"
        "s_cmp_lt_u32 s68, 64\n"
        "s_cbranch_scc1 .Lat_top_%=\n"
        "s_endpgm\n"
        :: "s"(p.ws), "s"(pair(x4, pr)), "s"(pair(PAGE + h * 16384, PAGE + spread(A_KM) + (h >> 1) * 16384 + (h & 1) * 4096)), "s"(pair(0, w)),
           "v"(pair(lane * 16, lane < 8 ? flag(((lane & 4) ? F_CH : F_S1 + 128) + 4 * h + (lane & 3)) : OOB)), "v"(pair(lane < 6 ? flag(F_AT + 6 * h + lane) : OOB, (g * 8 + (i & 7)) * 16)),
           "v"(pair((g & 1) * 256 + i * 16, 4 * g - i))
        : "v0", "v1", "v2", "v3", "v4", "v5", "v6", "v7", "v8", "v9", "v10", "v11", "v12", "v13", "v14", "v15", "v16", "v17", "v18", "v19", "v20", "v21", "v22", "v23", "v24", "v25", "v26", "v27", "v28", "v29", "v30", "v31", "v32", "v33", "v34", "v35", "v36", "v37", "v38", "v39", "v40", "v41", "v42", "v43", "v44", "v45", "v46", "v47", "v48", "v49", "v50", "v51", "v52", "v53", "v54", "v55", "v56", "v57", "v58", "v59", "v60", "v61", "v62", "v63", "v64", "v65", "v66", "v67", "v68", "v69", "v70", "v71", "v72", "v73", "v74", "v75", "v76", "v77", "v78", "v79", "v255", "a255", "s30", "s31", "s32", "s33", "s34", "s35", "s36", "s37", "s38", "s39", "s40", "s41", "s42", "s43", "s44", "s45", "s46", "s47", "s48", "s49", "s50", "s51", "s52", "s53", "s54", "s55", "s56", "s57", "s58", "s59", "s60", "s61", "s62", "s63", "s64", "s65", "s66", "s67", "s68", "s69", "s70", "s71", "s72", "s73", "s74", "s75", "s76", "s77", "s78", "s79", "s80", "s81", "s82", "s83", "s84", "s85", "s86", "s87", "s88", "s89", "s90", "s91", "s92", "s93", "s94", "s95", "s96", "s97", "s98", "s99", "vcc", "scc", "memory");
    __builtin_unreachable();
}

template <bool R5>
__device__ __forceinline__ void role_red(const Params& p, int a, int x4, int pr) {
    const unsigned lane = threadIdx.x & 63, i = lane & 15, g = lane >> 4;
    const int w = rfl(threadIdx.x >> 6);
    if (w == 3) return;
    unsigned to[3], po[3], one[4];
#pragma unroll
    for (int n = 0; n < 3; ++n) { const unsigned b = 3 * w + n; to[n] = b < 8 ? a * 16384 + b * 1024 : 65536 + a * 1024; po[n] = part_off(a, 0, 8 * b); }
#pragma unroll
    for (int e = 0; e < 4; ++e) one[e] = ((i & 7) == 2 * e) ? 0x3f80u : (((i & 7) == 2 * e + 1) ? 0x3f800000u : 0u);
    const u64 o0 = pair(one[0], one[1]), o1 = pair(one[2], one[3]);
    const bool lo = i < 8, r0 = g == 2 * (i >> 3), r1 = g == 1 + 2 * (i >> 3);
    const unsigned fin = lane < 16 ? flag((R5 ? F_F2 : F_OP) + a * 64 + w * 16 + lane) : OOB;
    const unsigned fout = (w == 0 && lane < (R5 ? 32 : 24)) ? flag((R5 ? F_XN : F_R3) + 4 * lane + a) : OOB;
#define RED_ARGS :: "s"(p.ws), "s"(p.x), "s"(p.out), "s"(pair(x4, pr)), "s"(pair(to[0], to[1])), "s"(pair(to[2], po[0])), "s"(pair(po[1], po[2])), \
        "v"(pair(lane * 16, fin)), "v"(pair(fout, g * 16384 + i * 16)), "v"(pair(g * (w == 2 ? 1024 : 16384) + i * 16, (8 * a + (lane & 7)) * 1152 + w * 384 + (lane >> 3) * 16)), \
        "v"(pair(w * 64 + (lane & 7) * 8, (lane & 7) * 8)), "v"(lo ? o0 : 0), "v"(lo ? o1 : 0), "v"(lo ? 0 : o0), "v"(lo ? 0 : o1), \
        "v"(r0 ? o0 : 0), "v"(r0 ? o1 : 0), "v"(r1 ? o0 : 0), "v"(r1 ? o1 : 0)
    if constexpr (R5) {
        asm volatile(
        "s_nop 4\n"
        "s_mov_b64 s[40:41], %0\n"
        "s_and_b32 s41, s41, 0xffff\n"
        "s_mov_b32 s42, 236978176\n"
        "s_mov_b32 s43, 0x20000\n"
        "s_mov_b64 s[44:45], %1\n"
        "s_and_b32 s45, s45, 0xffff\n"
        "s_mov_b32 s46, 2359296\n"
        "s_mov_b32 s47, 0x20000\n"
        "s_mov_b64 s[56:57], %3\n"
        "s_mov_b64 s[58:59], %4\n"
        "s_mov_b64 s[60:61], %5\n"
        "s_mov_b64 s[62:63], %6\n"
        "v_mov_b64 v[0:1], %7\n"
        "v_mov_b64 v[2:3], %8\n"
        "v_mov_b64 v[4:5], %9\n"
        "v_mov_b64 v[6:7], %10\n"
        "v_mov_b64 v[8:9], %11\n"
        "v_mov_b64 v[10:11], %12\n"
        "v_mov_b64 v[12:13], %13\n"
        "v_mov_b64 v[14:15], %14\n"
        "v_mov_b64 v[16:17], %15\n"
        "v_mov_b64 v[18:19], %16\n"
        "v_mov_b64 v[20:21], %17\n"
        "v_mov_b64 v[22:23], %18\n"
        "s_mov_b32 s68, 0\n"
        "s_mul_i32 s65, s68, 36864\n"
        "buffer_load_dwordx4 v[80:83], v5, s[44:47], s65 offen offset:0\n"
        "buffer_load_dwordx4 v[84:87], v5, s[44:47], s65 offen offset:128\n"
        "buffer_load_dwordx4 v[88:91], v5, s[44:47], s65 offen offset:256\n"
        "v_lshrrev_b32 v26, 4, v0\n"
        "s_lshl_b32 s66, s57, 7\n"
        "s_mov_b32 s64, 0\n"
        ".Lr5_pair_top_%=:\n"
        "s_mov_b64 exec, 1\n"
        "buffer_load_dword v27, off, s[40:43], s66 sc1\n"
        "s_mov_b64 exec, -1\n"
        "s_waitcnt vmcnt(0)\n"
        "v_cmp_gt_i32 vcc, 0, v27\n"
        "s_nop 0\n"
        "s_and_b64 vcc, vcc, 1\n"
        "s_cbranch_vccz .Lr5_pair_ok_%=\n"
        "s_add_u32 s64, s64, 1\n"
        "s_and_b32 s65, s64, 255\n"
        "s_cbranch_scc1 .Lr5_pair_go_%=\n"
        "s_mov_b32 s65, 40960\n"
        "s_cmp_lt_u32 s64, 1048576\n"
        "s_cbranch_scc1 .Lr5_pair_chk_%=\n"
        "v_mov_b32 v27, 1\n"
        "buffer_store_dword v27, off, s[40:43], s65 sc1\n"
        "s_waitcnt vmcnt(0)\n"
        ".Lr5_pair_chk_%=:\n"
        "buffer_load_dword v27, off, s[40:43], s65 sc1\n"
        "s_waitcnt vmcnt(0)\n"
        "v_readfirstlane_b32 s65, v27\n"
        "s_nop 0\n"
        "s_cmp_lg_u32 s65, 1\n"
        "s_cbranch_scc1 .Lr5_pair_go_%=\n"
        "s_endpgm\n"
        ".Lr5_pair_go_%=:\n"
        "s_sleep 2\n"
        "s_branch .Lr5_pair_top_%=\n"
        ".Lr5_pair_ok_%=:\n"
        "v_readfirstlane_b32 s50, v27\n"
        "s_nop 0\n"
        "s_xor_b32 s50, s50, s56\n"
        "v_lshlrev_b32 v28, 7, v26\n"
        "s_sub_u32 s66, 0, s50\n"
        "s_mov_b32 s52, 24576\n"
        "s_mov_b32 s64, 0\n"
        ".Lr5_pg0_top_%=:\n"
        "buffer_load_dword v27, v28, s[40:43], s52 offen sc1\n"
        "s_waitcnt vmcnt(0)\n"
        "v_cmp_gt_i32 vcc, 0, v27\n"
        "s_cbranch_vccz .Lr5_pg0_ok_%=\n"
        "s_add_u32 s64, s64, 1\n"
        "s_and_b32 s65, s64, 255\n"
        "s_cbranch_scc1 .Lr5_pg0_go_%=\n"
        "s_mov_b32 s65, 40960\n"
        "s_cmp_lt_u32 s64, 1048576\n"
        "s_cbranch_scc1 .Lr5_pg0_chk_%=\n"
        "v_mov_b32 v27, 1\n"
        "buffer_store_dword v27, off, s[40:43], s65 sc1\n"
        "s_waitcnt vmcnt(0)\n"
        ".Lr5_pg0_chk_%=:\n"
        "buffer_load_dword v27, off, s[40:43], s65 sc1\n"
        "s_waitcnt vmcnt(0)\n"
        "v_readfirstlane_b32 s65, v27\n"
        "s_nop 0\n"
        "s_cmp_lg_u32 s65, 1\n"
        "s_cbranch_scc1 .Lr5_pg0_go_%=\n"
        "s_endpgm\n"
        ".Lr5_pg0_go_%=:\n"
        "s_sleep 2\n"
        "s_branch .Lr5_pg0_top_%=\n"
        ".Lr5_pg0_ok_%=:\n"
        "v_cmp_eq_u32 s[52:53], 1, v27\n"
        "s_nop 1\n"
        "s_mov_b32 s54, 32768\n"
        "s_mov_b32 s64, 0\n"
        ".Lr5_pg1_top_%=:\n"
        "buffer_load_dword v27, v28, s[40:43], s54 offen sc1\n"
        "s_waitcnt vmcnt(0)\n"
        "v_cmp_gt_i32 vcc, 0, v27\n"
        "s_cbranch_vccz .Lr5_pg1_ok_%=\n"
        "s_add_u32 s64, s64, 1\n"
        "s_and_b32 s65, s64, 255\n"
        "s_cbranch_scc1 .Lr5_pg1_go_%=\n"
        "s_mov_b32 s65, 40960\n"
        "s_cmp_lt_u32 s64, 1048576\n"
        "s_cbranch_scc1 .Lr5_pg1_chk_%=\n"
        "v_mov_b32 v27, 1\n"
        "buffer_store_dword v27, off, s[40:43], s65 sc1\n"
        "s_waitcnt vmcnt(0)\n"
        ".Lr5_pg1_chk_%=:\n"
        "buffer_load_dword v27, off, s[40:43], s65 sc1\n"
        "s_waitcnt vmcnt(0)\n"
        "v_readfirstlane_b32 s65, v27\n"
        "s_nop 0\n"
        "s_cmp_lg_u32 s65, 1\n"
        "s_cbranch_scc1 .Lr5_pg1_go_%=\n"
        "s_endpgm\n"
        ".Lr5_pg1_go_%=:\n"
        "s_sleep 2\n"
        "s_branch .Lr5_pg1_top_%=\n"
        ".Lr5_pg1_ok_%=:\n"
        "v_cmp_eq_u32 s[54:55], 1, v27\n"
        "s_nop 1\n"
        "s_mov_b32 vcc_lo, 0x55555555\n"
        "s_mov_b32 vcc_hi, 0x55555555\n"
        "s_xor_b64 vcc, vcc, s[52:53]\n"
        "s_bcnt1_i32_b64 s65, vcc\n"
        "s_sub_u32 s64, s65, 23\n"
        "s_cmp_lt_u32 s64, 19\n"
        "s_cbranch_scc1 .Lr5_rawmap_%=\n"
        "s_cmp_lt_u32 s65, 32\n"
        "s_cselect_b32 s65, 0, -1\n"
        "s_xor_b32 s52, s65, 0x55555555\n"
        "s_xor_b32 s53, s65, 0x55555555\n"
        "s_xor_b32 s54, s65, 0x55555555\n"
        "s_xor_b32 s55, s65, 0xaaaaaaaa\n"
        ".Lr5_rawmap_%=:\n"
        "s_xor_b32 s52, s52, s66\n"
        "s_xor_b32 s53, s53, s66\n"
        "s_xor_b32 s54, s54, s66\n"
        "s_xor_b32 s55, s55, s66\n"
        "s_and_b32 s69, s55, 1\n"
        "s_lshl_b32 s69, s69, 13\n"
        "s_xor_b32 s70, s69, 8192\n"
        "s_cmp_eq_u32 s50, 0\n"
        "s_cbranch_scc0 .Lr5_l1_%=\n"
        ".Lr5_l0_%=:\n"
        "s_bitcmp1_b64 s[52:53], s68\n"
        "s_cselect_b32 s65, 8192, 0\n"
        "s_lshl_b32 s71, s68, 21\n"
        "s_add_u32 s71, s71, s65\n"
        "s_add_u32 s71, s71, 2097152\n"
        "s_lshr_b32 s65, s68, 2\n"
        "s_add_u32 s65, s65, 1\n"
        "s_bitcmp1_b32 s55, s65\n"
        "s_cselect_b32 s65, 8192, 0\n"
        "s_lshl_b32 s72, s68, 19\n"
        "s_add_u32 s72, s72, s65\n"
        "s_add_u32 s72, s72, 203423744\n"
        "s_add_u32 s76, s72, s61\n"
        "s_add_u32 s79, s71, s58\n"
        "s_add_u32 s79, s79, 1998848\n"
        "s_add_u32 s77, s72, s62\n"
        "s_add_u32 s80, s71, s59\n"
        "s_add_u32 s80, s80, 1998848\n"
        "s_add_u32 s78, s72, s63\n"
        "s_add_u32 s81, s71, s60\n"
        "s_add_u32 s81, s81, 1998848\n"
        "s_mov_b32 s64, 0\n"
        ".Lr5_l0w_top_%=:\n"
        "buffer_load_dword v24, v1, s[40:43], s69 offen sc1\n"
        "s_waitcnt vmcnt(0)\n"
        "v_cmp_gt_i32 vcc, s68, v24\n"
        "s_nop 0\n"
        "s_and_b64 vcc, vcc, 0xffff\n"
        "s_cbranch_vccz .Lr5_l0w_ok_%=\n"
        "s_add_u32 s64, s64, 1\n"
        "s_and_b32 s65, s64, 255\n"
        "s_cbranch_scc1 .Lr5_l0w_go_%=\n"
        "s_mov_b32 s65, 40960\n"
        "s_cmp_lt_u32 s64, 1048576\n"
        "s_cbranch_scc1 .Lr5_l0w_chk_%=\n"
        "v_mov_b32 v24, 1\n"
        "buffer_store_dword v24, off, s[40:43], s65 sc1\n"
        "s_waitcnt vmcnt(0)\n"
        ".Lr5_l0w_chk_%=:\n"
        "buffer_load_dword v24, off, s[40:43], s65 sc1\n"
        "s_waitcnt vmcnt(0)\n"
        "v_readfirstlane_b32 s65, v24\n"
        "s_nop 0\n"
        "s_cmp_lg_u32 s65, 1\n"
        "s_cbranch_scc1 .Lr5_l0w_go_%=\n"
        "s_endpgm\n"
        ".Lr5_l0w_go_%=:\n"
        "s_branch .Lr5_l0w_top_%=\n"
        ".Lr5_l0w_ok_%=:\n"
        "buffer_load_dwordx4 v[32:35], v3, s[40:43], s76 offen offset:0 sc1\n"
        "buffer_load_dwordx4 v[40:43], v3, s[40:43], s76 offen offset:512 sc1\n"
        "buffer_load_dwordx4 v[36:39], v3, s[40:43], s76 offen offset:256 sc1\n"
        "buffer_load_dwordx4 v[44:47], v3, s[40:43], s76 offen offset:768 sc1\n"
        "buffer_load_dwordx4 v[80:83], v0, s[40:43], s79 offen sc1\n"
        "buffer_load_dwordx4 v[48:51], v3, s[40:43], s77 offen offset:0 sc1\n"
        "buffer_load_dwordx4 v[56:59], v3, s[40:43], s77 offen offset:512 sc1\n"
        "buffer_load_dwordx4 v[52:55], v3, s[40:43], s77 offen offset:256 sc1\n"
        "buffer_load_dwordx4 v[60:63], v3, s[40:43], s77 offen offset:768 sc1\n"
        "buffer_load_dwordx4 v[84:87], v0, s[40:43], s80 offen sc1\n"
        "buffer_load_dwordx4 v[64:67], v4, s[40:43], s78 offen offset:0 sc1\n"
        "buffer_load_dwordx4 v[72:75], v4, s[40:43], s78 offen offset:512 sc1\n"
        "buffer_load_dwordx4 v[68:71], v4, s[40:43], s78 offen offset:256 sc1\n"
        "buffer_load_dwordx4 v[76:79], v4, s[40:43], s78 offen offset:768 sc1\n"
        "buffer_load_dwordx4 v[88:91], v0, s[40:43], s81 offen sc1\n"
        "s_xor_b32 s71, s71, 8192\n"
        "s_add_u32 s72, s71, 1835008\n"
        "s_add_u32 s71, s71, 1916928\n"
        "s_add_u32 s82, s72, s58\n"
        "s_add_u32 s85, s71, s58\n"
        "s_add_u32 s83, s72, s59\n"
        "s_add_u32 s86, s71, s59\n"
        "s_add_u32 s84, s72, s60\n"
        "s_add_u32 s87, s71, s60\n"
        "s_waitcnt vmcnt(14)\n"
        "v_mfma_f32_16x16x32_bf16 v[92:95], v[8:11], v[32:35], 0\n"
        "s_waitcnt vmcnt(13)\n"
        "v_mfma_f32_16x16x32_bf16 v[92:95], v[12:15], v[40:43], v[92:95]\n"
        "s_waitcnt vmcnt(12)\n"
        "v_mfma_f32_16x16x32_bf16 v[96:99], v[8:11], v[36:39], 0\n"
        "s_waitcnt vmcnt(11)\n"
        "v_mfma_f32_16x16x32_bf16 v[96:99], v[12:15], v[44:47], v[96:99]\n"
        "s_waitcnt vmcnt(10)\n"
        "v_mfma_f32_16x16x32_bf16 v[92:95], v[16:19], v[80:83], v[92:95]\n"
        "v_mfma_f32_16x16x32_bf16 v[96:99], v[20:23], v[80:83], v[96:99]\n"
        "s_waitcnt vmcnt(9)\n"
        "v_mfma_f32_16x16x32_bf16 v[100:103], v[8:11], v[48:51], 0\n"
        "s_waitcnt vmcnt(8)\n"
        "v_mfma_f32_16x16x32_bf16 v[100:103], v[12:15], v[56:59], v[100:103]\n"
        "s_waitcnt vmcnt(7)\n"
        "v_mfma_f32_16x16x32_bf16 v[104:107], v[8:11], v[52:55], 0\n"
        "s_waitcnt vmcnt(6)\n"
        "v_mfma_f32_16x16x32_bf16 v[104:107], v[12:15], v[60:63], v[104:107]\n"
        "s_waitcnt vmcnt(5)\n"
        "v_mfma_f32_16x16x32_bf16 v[100:103], v[16:19], v[84:87], v[100:103]\n"
        "v_mfma_f32_16x16x32_bf16 v[104:107], v[20:23], v[84:87], v[104:107]\n"
        "s_waitcnt vmcnt(4)\n"
        "v_mfma_f32_16x16x32_bf16 v[108:111], v[8:11], v[64:67], 0\n"
        "s_waitcnt vmcnt(3)\n"
        "v_mfma_f32_16x16x32_bf16 v[108:111], v[12:15], v[72:75], v[108:111]\n"
        "s_waitcnt vmcnt(2)\n"
        "v_mfma_f32_16x16x32_bf16 v[112:115], v[8:11], v[68:71], 0\n"
        "s_waitcnt vmcnt(1)\n"
        "v_mfma_f32_16x16x32_bf16 v[112:115], v[12:15], v[76:79], v[112:115]\n"
        "s_waitcnt vmcnt(0)\n"
        "v_mfma_f32_16x16x32_bf16 v[108:111], v[16:19], v[88:91], v[108:111]\n"
        "v_mfma_f32_16x16x32_bf16 v[112:115], v[20:23], v[88:91], v[112:115]\n"
        "s_add_u32 s68, s68, 1\n"
        "s_sub_u32 s68, s68, 1\n"
        "v_pk_add_f32 v[164:165], v[92:93], v[94:95]\n"
        "v_pk_add_f32 v[166:167], v[96:97], v[98:99]\n"
        "v_pk_add_f32 v[168:169], v[100:101], v[102:103]\n"
        "v_pk_add_f32 v[170:171], v[104:105], v[106:107]\n"
        "v_pk_add_f32 v[164:165], v[164:165], v[166:167]\n"
        "v_pk_add_f32 v[168:169], v[168:169], v[170:171]\n"
        "v_pk_add_f32 v[164:165], v[164:165], v[168:169]\n"
        "s_nop 0\n"
        "v_pk_add_f32 v[166:167], v[108:109], v[110:111]\n"
        "v_pk_add_f32 v[168:169], v[112:113], v[114:115]\n"
        "v_pk_add_f32 v[164:165], v[164:165], v[166:167]\n"
        "v_pk_add_f32 v[164:165], v[164:165], v[168:169]\n"
        "v_add_f32 v164, v164, v165\n"
        "v_cvt_pk_bf16_f32 v140, v92, v93\n"
        "v_cvt_pk_bf16_f32 v141, v94, v95\n"
        "v_add_f32_dpp v166, v164, v164 row_ror:8 row_mask:0xf bank_mask:0xf\n"
        "v_add_f32_dpp v167, v164, v164 row_ror:8 row_mask:0xf bank_mask:0xf\n"
        "v_cvt_pk_bf16_f32 v142, v96, v97\n"
        "v_cvt_pk_bf16_f32 v143, v98, v99\n"
        "v_permlane16_swap_b32 v166, v167\n"
        "v_add_f32 v164, v166, v167\n"
        "v_add_f32 v166, v166, v167\n"
        "v_cvt_pk_bf16_f32 v144, v100, v101\n"
        "v_cvt_pk_bf16_f32 v145, v102, v103\n"
        "v_permlane32_swap_b32 v164, v166\n"
        "v_add_f32 v164, v164, v166\n"
        "v_mul_f32 v164, 0x3baaaaab, v164\n"
        "v_pk_add_f32 v[116:117], v[92:93], v[164:165] op_sel_hi:[1,0] neg_lo:[0,1] neg_hi:[0,1]\n"
        "v_pk_add_f32 v[118:119], v[94:95], v[164:165] op_sel_hi:[1,0] neg_lo:[0,1] neg_hi:[0,1]\n"
        "v_pk_add_f32 v[120:121], v[96:97], v[164:165] op_sel_hi:[1,0] neg_lo:[0,1] neg_hi:[0,1]\n"
        "v_pk_add_f32 v[122:123], v[98:99], v[164:165] op_sel_hi:[1,0] neg_lo:[0,1] neg_hi:[0,1]\n"
        "v_pk_add_f32 v[124:125], v[100:101], v[164:165] op_sel_hi:[1,0] neg_lo:[0,1] neg_hi:[0,1]\n"
        "v_pk_add_f32 v[126:127], v[102:103], v[164:165] op_sel_hi:[1,0] neg_lo:[0,1] neg_hi:[0,1]\n"
        "v_pk_add_f32 v[128:129], v[104:105], v[164:165] op_sel_hi:[1,0] neg_lo:[0,1] neg_hi:[0,1]\n"
        "v_pk_add_f32 v[130:131], v[106:107], v[164:165] op_sel_hi:[1,0] neg_lo:[0,1] neg_hi:[0,1]\n"
        "v_pk_add_f32 v[132:133], v[108:109], v[164:165] op_sel_hi:[1,0] neg_lo:[0,1] neg_hi:[0,1]\n"
        "v_pk_add_f32 v[134:135], v[110:111], v[164:165] op_sel_hi:[1,0] neg_lo:[0,1] neg_hi:[0,1]\n"
        "v_pk_add_f32 v[136:137], v[112:113], v[164:165] op_sel_hi:[1,0] neg_lo:[0,1] neg_hi:[0,1]\n"
        "v_pk_add_f32 v[138:139], v[114:115], v[164:165] op_sel_hi:[1,0] neg_lo:[0,1] neg_hi:[0,1]\n"
        "v_pk_mul_f32 v[166:167], v[116:117], v[116:117]\n"
        "v_pk_mul_f32 v[168:169], v[118:119], v[118:119]\n"
        "v_pk_fma_f32 v[166:167], v[120:121], v[120:121], v[166:167]\n"
        "v_pk_fma_f32 v[168:169], v[122:123], v[122:123], v[168:169]\n"
        "v_pk_fma_f32 v[166:167], v[124:125], v[124:125], v[166:167]\n"
        "v_pk_fma_f32 v[168:169], v[126:127], v[126:127], v[168:169]\n"
        "v_pk_fma_f32 v[166:167], v[128:129], v[128:129], v[166:167]\n"
        "v_pk_fma_f32 v[168:169], v[130:131], v[130:131], v[168:169]\n"
        "v_pk_fma_f32 v[166:167], v[132:133], v[132:133], v[166:167]\n"
        "v_pk_fma_f32 v[168:169], v[134:135], v[134:135], v[168:169]\n"
        "v_pk_fma_f32 v[166:167], v[136:137], v[136:137], v[166:167]\n"
        "v_pk_fma_f32 v[168:169], v[138:139], v[138:139], v[168:169]\n"
        "v_pk_add_f32 v[166:167], v[166:167], v[168:169]\n"
        "v_add_f32 v165, v166, v167\n"
        "v_cvt_pk_bf16_f32 v146, v104, v105\n"
        "v_cvt_pk_bf16_f32 v147, v106, v107\n"
        "v_add_f32_dpp v166, v165, v165 row_ror:8 row_mask:0xf bank_mask:0xf\n"
        "v_add_f32_dpp v167, v165, v165 row_ror:8 row_mask:0xf bank_mask:0xf\n"
        "v_cvt_pk_bf16_f32 v148, v108, v109\n"
        "v_cvt_pk_bf16_f32 v149, v110, v111\n"
        "v_permlane16_swap_b32 v166, v167\n"
        "v_add_f32 v165, v166, v167\n"
        "v_add_f32 v166, v166, v167\n"
        "v_cvt_pk_bf16_f32 v150, v112, v113\n"
        "v_cvt_pk_bf16_f32 v151, v114, v115\n"
        "v_permlane32_swap_b32 v165, v166\n"
        "v_add_f32 v165, v165, v166\n"
        "ds_write_b64 v6, v[164:165]\n"
        "v_permlane16_swap_b32 v140, v142\n"
        "v_permlane16_swap_b32 v141, v143\n"
        "v_permlane16_swap_b32 v144, v146\n"
        "v_permlane16_swap_b32 v145, v147\n"
        "s_waitcnt lgkmcnt(0)\n"
        "s_barrier\n"
        "ds_read_b64 v[166:167], v7 offset:0\n"
        "ds_read_b64 v[168:169], v7 offset:64\n"
        "ds_read_b64 v[170:171], v7 offset:128\n"
        "v_permlane16_swap_b32 v148, v150\n"
        "v_permlane16_swap_b32 v149, v151\n"
        "buffer_store_dwordx4 v[140:143], v0, s[40:43], s85 offen sc1\n"
        "buffer_store_dwordx4 v[144:147], v0, s[40:43], s86 offen sc1\n"
        "buffer_store_dwordx4 v[148:151], v0, s[40:43], s87 offen sc1\n"
        "s_waitcnt lgkmcnt(0)\n"
        "v_add_f32 v172, v166, v168\n"
        "v_add_f32 v173, v167, v169\n"
        "v_add_f32 v172, v172, v170\n"
        "v_add_f32 v173, v173, v171\n"
        "v_mul_f32 v172, 0x3eaaaaab, v172\n"
        "v_sub_f32 v166, v166, v172\n"
        "v_sub_f32 v168, v168, v172\n"
        "v_sub_f32 v170, v170, v172\n"
        "v_mul_f32 v166, v166, v166\n"
        "v_fmac_f32 v166, v168, v168\n"
        "v_fmac_f32 v166, v170, v170\n"
        "v_fmamk_f32 v173, v166, 0x43400000, v173\n"
        "v_mul_f32 v173, 0x3ae38e39, v173\n"
        "v_cvt_pk_bf16_f32 v172, v172, v173\n"
        "v_and_b32 v173, 0xffff0000, v172\n"
        "v_lshlrev_b32 v172, 16, v172\n"
        "v_add_f32 v173, 0x3727c5ac, v173\n"
        "v_cvt_pk_bf16_f32 v173, v173, v173\n"
        "v_lshlrev_b32 v173, 16, v173\n"
        "v_sqrt_f32 v173, v173\n"
        "v_sub_f32 v172, v164, v172\n"
        "v_cvt_pk_bf16_f32 v173, v173, v173\n"
        "v_lshlrev_b32 v173, 16, v173\n"
        "v_rcp_f32 v173, v173\n"
        "v_mov_b32 v25, s68\n"
        "v_mul_f32 v172, v172, v173\n"
        "v_pk_fma_f32 v[116:117], v[116:117], v[172:173], v[172:173] op_sel:[0,1,0] op_sel_hi:[1,1,0]\n"
        "v_pk_fma_f32 v[118:119], v[118:119], v[172:173], v[172:173] op_sel:[0,1,0] op_sel_hi:[1,1,0]\n"
        "v_pk_fma_f32 v[120:121], v[120:121], v[172:173], v[172:173] op_sel:[0,1,0] op_sel_hi:[1,1,0]\n"
        "v_pk_fma_f32 v[122:123], v[122:123], v[172:173], v[172:173] op_sel:[0,1,0] op_sel_hi:[1,1,0]\n"
        "v_cvt_pk_bf16_f32 v152, v116, v117\n"
        "v_cvt_pk_bf16_f32 v153, v118, v119\n"
        "v_cvt_pk_bf16_f32 v154, v120, v121\n"
        "v_cvt_pk_bf16_f32 v155, v122, v123\n"
        "s_nop 0\n"
        "v_permlane16_swap_b32 v152, v154\n"
        "v_permlane16_swap_b32 v153, v155\n"
        "buffer_store_dwordx4 v[152:155], v0, s[40:43], s82 offen sc1\n"
        "v_pk_fma_f32 v[124:125], v[124:125], v[172:173], v[172:173] op_sel:[0,1,0] op_sel_hi:[1,1,0]\n"
        "v_pk_fma_f32 v[126:127], v[126:127], v[172:173], v[172:173] op_sel:[0,1,0] op_sel_hi:[1,1,0]\n"
        "v_pk_fma_f32 v[128:129], v[128:129], v[172:173], v[172:173] op_sel:[0,1,0] op_sel_hi:[1,1,0]\n"
        "v_pk_fma_f32 v[130:131], v[130:131], v[172:173], v[172:173] op_sel:[0,1,0] op_sel_hi:[1,1,0]\n"
        "v_cvt_pk_bf16_f32 v156, v124, v125\n"
        "v_cvt_pk_bf16_f32 v157, v126, v127\n"
        "v_cvt_pk_bf16_f32 v158, v128, v129\n"
        "v_cvt_pk_bf16_f32 v159, v130, v131\n"
        "s_nop 0\n"
        "v_permlane16_swap_b32 v156, v158\n"
        "v_permlane16_swap_b32 v157, v159\n"
        "buffer_store_dwordx4 v[156:159], v0, s[40:43], s83 offen sc1\n"
        "v_pk_fma_f32 v[132:133], v[132:133], v[172:173], v[172:173] op_sel:[0,1,0] op_sel_hi:[1,1,0]\n"
        "v_pk_fma_f32 v[134:135], v[134:135], v[172:173], v[172:173] op_sel:[0,1,0] op_sel_hi:[1,1,0]\n"
        "v_pk_fma_f32 v[136:137], v[136:137], v[172:173], v[172:173] op_sel:[0,1,0] op_sel_hi:[1,1,0]\n"
        "v_pk_fma_f32 v[138:139], v[138:139], v[172:173], v[172:173] op_sel:[0,1,0] op_sel_hi:[1,1,0]\n"
        "v_cvt_pk_bf16_f32 v160, v132, v133\n"
        "v_cvt_pk_bf16_f32 v161, v134, v135\n"
        "v_cvt_pk_bf16_f32 v162, v136, v137\n"
        "v_cvt_pk_bf16_f32 v163, v138, v139\n"
        "s_nop 0\n"
        "v_permlane16_swap_b32 v160, v162\n"
        "v_permlane16_swap_b32 v161, v163\n"
        "buffer_store_dwordx4 v[160:163], v0, s[40:43], s84 offen sc1\n"
        "s_waitcnt vmcnt(0)\n"
        "s_barrier\n"
        "buffer_store_dword v25, v2, s[40:43], s70 offen sc1\n"
        "s_add_u32 s68, s68, 1\n"
        "s_cmp_lt_u32 s68, 64\n"
        "s_cbranch_scc1 .Lr5_l0_%=\n"
        "s_endpgm\n"
        ".Lr5_l1_%=:\n"
        "s_bitcmp1_b64 s[52:53], s68\n"
        "s_cselect_b32 s65, 8192, 0\n"
        "s_lshl_b32 s71, s68, 21\n"
        "s_add_u32 s71, s71, s65\n"
        "s_add_u32 s71, s71, 2097152\n"
        "s_xor_b32 s71, s71, 8192\n"
        "s_add_u32 s72, s71, 1835008\n"
        "s_add_u32 s71, s71, 1916928\n"
        "s_add_u32 s82, s72, s58\n"
        "s_add_u32 s85, s71, s58\n"
        "s_add_u32 s83, s72, s59\n"
        "s_add_u32 s86, s71, s59\n"
        "s_add_u32 s84, s72, s60\n"
        "s_add_u32 s87, s71, s60\n"
        "s_waitcnt vmcnt(0)\n"
        "v_mfma_f32_16x16x32_bf16 v[92:95], v[16:19], v[80:83], 0\n"
        "v_mfma_f32_16x16x32_bf16 v[96:99], v[20:23], v[80:83], 0\n"
        "v_mfma_f32_16x16x32_bf16 v[100:103], v[16:19], v[84:87], 0\n"
        "v_mfma_f32_16x16x32_bf16 v[104:107], v[20:23], v[84:87], 0\n"
        "v_mfma_f32_16x16x32_bf16 v[108:111], v[16:19], v[88:91], 0\n"
        "v_mfma_f32_16x16x32_bf16 v[112:115], v[20:23], v[88:91], 0\n"
        "s_add_u32 s68, s68, 1\n"
        "s_mul_i32 s65, s68, 36864\n"
        "buffer_load_dwordx4 v[80:83], v5, s[44:47], s65 offen offset:0\n"
        "buffer_load_dwordx4 v[84:87], v5, s[44:47], s65 offen offset:128\n"
        "buffer_load_dwordx4 v[88:91], v5, s[44:47], s65 offen offset:256\n"
        "s_sub_u32 s68, s68, 1\n"
        "v_pk_add_f32 v[164:165], v[92:93], v[94:95]\n"
        "v_pk_add_f32 v[166:167], v[96:97], v[98:99]\n"
        "v_pk_add_f32 v[168:169], v[100:101], v[102:103]\n"
        "v_pk_add_f32 v[170:171], v[104:105], v[106:107]\n"
        "v_pk_add_f32 v[164:165], v[164:165], v[166:167]\n"
        "v_pk_add_f32 v[168:169], v[168:169], v[170:171]\n"
        "v_pk_add_f32 v[164:165], v[164:165], v[168:169]\n"
        "v_pk_add_f32 v[166:167], v[108:109], v[110:111]\n"
        "v_pk_add_f32 v[168:169], v[112:113], v[114:115]\n"
        "v_pk_add_f32 v[164:165], v[164:165], v[166:167]\n"
        "v_pk_add_f32 v[164:165], v[164:165], v[168:169]\n"
        "v_add_f32 v164, v164, v165\n"
        "s_nop 1\n"
        "v_add_f32_dpp v166, v164, v164 row_ror:8 row_mask:0xf bank_mask:0xf\n"
        "v_add_f32_dpp v167, v164, v164 row_ror:8 row_mask:0xf bank_mask:0xf\n"
        "s_nop 1\n"
        "v_permlane16_swap_b32 v166, v167\n"
        "v_add_f32 v164, v166, v167\n"
        "v_add_f32 v166, v166, v167\n"
        "s_nop 1\n"
        "v_permlane32_swap_b32 v164, v166\n"
        "v_add_f32 v164, v164, v166\n"
        "v_mul_f32 v164, 0x3baaaaab, v164\n"
        "v_pk_add_f32 v[116:117], v[92:93], v[164:165] op_sel_hi:[1,0] neg_lo:[0,1] neg_hi:[0,1]\n"
        "v_pk_add_f32 v[118:119], v[94:95], v[164:165] op_sel_hi:[1,0] neg_lo:[0,1] neg_hi:[0,1]\n"
        "v_pk_add_f32 v[120:121], v[96:97], v[164:165] op_sel_hi:[1,0] neg_lo:[0,1] neg_hi:[0,1]\n"
        "v_pk_add_f32 v[122:123], v[98:99], v[164:165] op_sel_hi:[1,0] neg_lo:[0,1] neg_hi:[0,1]\n"
        "v_pk_add_f32 v[124:125], v[100:101], v[164:165] op_sel_hi:[1,0] neg_lo:[0,1] neg_hi:[0,1]\n"
        "v_pk_add_f32 v[126:127], v[102:103], v[164:165] op_sel_hi:[1,0] neg_lo:[0,1] neg_hi:[0,1]\n"
        "v_pk_add_f32 v[128:129], v[104:105], v[164:165] op_sel_hi:[1,0] neg_lo:[0,1] neg_hi:[0,1]\n"
        "v_pk_add_f32 v[130:131], v[106:107], v[164:165] op_sel_hi:[1,0] neg_lo:[0,1] neg_hi:[0,1]\n"
        "v_pk_add_f32 v[132:133], v[108:109], v[164:165] op_sel_hi:[1,0] neg_lo:[0,1] neg_hi:[0,1]\n"
        "v_pk_add_f32 v[134:135], v[110:111], v[164:165] op_sel_hi:[1,0] neg_lo:[0,1] neg_hi:[0,1]\n"
        "v_pk_add_f32 v[136:137], v[112:113], v[164:165] op_sel_hi:[1,0] neg_lo:[0,1] neg_hi:[0,1]\n"
        "v_pk_add_f32 v[138:139], v[114:115], v[164:165] op_sel_hi:[1,0] neg_lo:[0,1] neg_hi:[0,1]\n"
        "v_pk_mul_f32 v[166:167], v[116:117], v[116:117]\n"
        "v_pk_mul_f32 v[168:169], v[118:119], v[118:119]\n"
        "v_pk_fma_f32 v[166:167], v[120:121], v[120:121], v[166:167]\n"
        "v_pk_fma_f32 v[168:169], v[122:123], v[122:123], v[168:169]\n"
        "v_pk_fma_f32 v[166:167], v[124:125], v[124:125], v[166:167]\n"
        "v_pk_fma_f32 v[168:169], v[126:127], v[126:127], v[168:169]\n"
        "v_pk_fma_f32 v[166:167], v[128:129], v[128:129], v[166:167]\n"
        "v_pk_fma_f32 v[168:169], v[130:131], v[130:131], v[168:169]\n"
        "v_pk_fma_f32 v[166:167], v[132:133], v[132:133], v[166:167]\n"
        "v_pk_fma_f32 v[168:169], v[134:135], v[134:135], v[168:169]\n"
        "v_pk_fma_f32 v[166:167], v[136:137], v[136:137], v[166:167]\n"
        "v_pk_fma_f32 v[168:169], v[138:139], v[138:139], v[168:169]\n"
        "v_pk_add_f32 v[166:167], v[166:167], v[168:169]\n"
        "v_add_f32 v165, v166, v167\n"
        "s_nop 1\n"
        "v_add_f32_dpp v166, v165, v165 row_ror:8 row_mask:0xf bank_mask:0xf\n"
        "v_add_f32_dpp v167, v165, v165 row_ror:8 row_mask:0xf bank_mask:0xf\n"
        "s_nop 1\n"
        "v_permlane16_swap_b32 v166, v167\n"
        "v_add_f32 v165, v166, v167\n"
        "v_add_f32 v166, v166, v167\n"
        "s_nop 1\n"
        "v_permlane32_swap_b32 v165, v166\n"
        "v_add_f32 v165, v165, v166\n"
        "ds_write_b64 v6, v[164:165]\n"
        "s_waitcnt lgkmcnt(0)\n"
        "s_barrier\n"
        "ds_read_b64 v[166:167], v7 offset:0\n"
        "ds_read_b64 v[168:169], v7 offset:64\n"
        "ds_read_b64 v[170:171], v7 offset:128\n"
        "s_waitcnt lgkmcnt(0)\n"
        "v_add_f32 v172, v166, v168\n"
        "v_add_f32 v173, v167, v169\n"
        "v_add_f32 v172, v172, v170\n"
        "v_add_f32 v173, v173, v171\n"
        "v_mul_f32 v172, 0x3eaaaaab, v172\n"
        "v_sub_f32 v166, v166, v172\n"
        "v_sub_f32 v168, v168, v172\n"
        "v_sub_f32 v170, v170, v172\n"
        "v_mul_f32 v166, v166, v166\n"
        "v_fmac_f32 v166, v168, v168\n"
        "v_fmac_f32 v166, v170, v170\n"
        "v_fmamk_f32 v173, v166, 0x43400000, v173\n"
        "v_mul_f32 v173, 0x3ae38e39, v173\n"
        "v_cvt_pk_bf16_f32 v172, v172, v173\n"
        "v_and_b32 v173, 0xffff0000, v172\n"
        "v_lshlrev_b32 v172, 16, v172\n"
        "v_add_f32 v173, 0x3727c5ac, v173\n"
        "v_cvt_pk_bf16_f32 v173, v173, v173\n"
        "v_lshlrev_b32 v173, 16, v173\n"
        "v_sqrt_f32 v173, v173\n"
        "v_sub_f32 v172, v164, v172\n"
        "v_cvt_pk_bf16_f32 v173, v173, v173\n"
        "v_lshlrev_b32 v173, 16, v173\n"
        "v_rcp_f32 v173, v173\n"
        "v_mov_b32 v25, s68\n"
        "v_mul_f32 v172, v172, v173\n"
        "v_pk_fma_f32 v[116:117], v[116:117], v[172:173], v[172:173] op_sel:[0,1,0] op_sel_hi:[1,1,0]\n"
        "v_pk_fma_f32 v[118:119], v[118:119], v[172:173], v[172:173] op_sel:[0,1,0] op_sel_hi:[1,1,0]\n"
        "v_pk_fma_f32 v[120:121], v[120:121], v[172:173], v[172:173] op_sel:[0,1,0] op_sel_hi:[1,1,0]\n"
        "v_pk_fma_f32 v[122:123], v[122:123], v[172:173], v[172:173] op_sel:[0,1,0] op_sel_hi:[1,1,0]\n"
        "v_cvt_pk_bf16_f32 v152, v116, v117\n"
        "v_cvt_pk_bf16_f32 v153, v118, v119\n"
        "v_cvt_pk_bf16_f32 v154, v120, v121\n"
        "v_cvt_pk_bf16_f32 v155, v122, v123\n"
        "s_nop 0\n"
        "v_permlane16_swap_b32 v152, v154\n"
        "v_permlane16_swap_b32 v153, v155\n"
        "buffer_store_dwordx4 v[152:155], v0, s[40:43], s82 offen sc1\n"
        "v_pk_fma_f32 v[124:125], v[124:125], v[172:173], v[172:173] op_sel:[0,1,0] op_sel_hi:[1,1,0]\n"
        "v_pk_fma_f32 v[126:127], v[126:127], v[172:173], v[172:173] op_sel:[0,1,0] op_sel_hi:[1,1,0]\n"
        "v_pk_fma_f32 v[128:129], v[128:129], v[172:173], v[172:173] op_sel:[0,1,0] op_sel_hi:[1,1,0]\n"
        "v_pk_fma_f32 v[130:131], v[130:131], v[172:173], v[172:173] op_sel:[0,1,0] op_sel_hi:[1,1,0]\n"
        "v_cvt_pk_bf16_f32 v156, v124, v125\n"
        "v_cvt_pk_bf16_f32 v157, v126, v127\n"
        "v_cvt_pk_bf16_f32 v158, v128, v129\n"
        "v_cvt_pk_bf16_f32 v159, v130, v131\n"
        "s_nop 0\n"
        "v_permlane16_swap_b32 v156, v158\n"
        "v_permlane16_swap_b32 v157, v159\n"
        "buffer_store_dwordx4 v[156:159], v0, s[40:43], s83 offen sc1\n"
        "v_pk_fma_f32 v[132:133], v[132:133], v[172:173], v[172:173] op_sel:[0,1,0] op_sel_hi:[1,1,0]\n"
        "v_pk_fma_f32 v[134:135], v[134:135], v[172:173], v[172:173] op_sel:[0,1,0] op_sel_hi:[1,1,0]\n"
        "v_pk_fma_f32 v[136:137], v[136:137], v[172:173], v[172:173] op_sel:[0,1,0] op_sel_hi:[1,1,0]\n"
        "v_pk_fma_f32 v[138:139], v[138:139], v[172:173], v[172:173] op_sel:[0,1,0] op_sel_hi:[1,1,0]\n"
        "v_cvt_pk_bf16_f32 v160, v132, v133\n"
        "v_cvt_pk_bf16_f32 v161, v134, v135\n"
        "v_cvt_pk_bf16_f32 v162, v136, v137\n"
        "v_cvt_pk_bf16_f32 v163, v138, v139\n"
        "s_nop 0\n"
        "v_permlane16_swap_b32 v160, v162\n"
        "v_permlane16_swap_b32 v161, v163\n"
        "buffer_store_dwordx4 v[160:163], v0, s[40:43], s84 offen sc1\n"
        "s_waitcnt vmcnt(0)\n"
        "s_barrier\n"
        "buffer_store_dword v25, v2, s[40:43], s70 offen sc1\n"
        "s_add_u32 s68, s68, 1\n"
        "s_cmp_lt_u32 s68, 64\n"
        "s_cbranch_scc1 .Lr5_l1_%=\n"
        "s_mov_b32 s68, 0\n"
        "s_mov_b64 s[44:45], %2\n"
        "s_and_b32 s45, s45, 0xffff\n"
        ".Lr5_fin_%=:\n"
        "s_bitcmp1_b64 s[52:53], s68\n"
        "s_cselect_b32 s65, 8192, 0\n"
        "s_lshl_b32 s71, s68, 21\n"
        "s_add_u32 s71, s71, s65\n"
        "s_add_u32 s71, s71, 2097152\n"
        "s_lshr_b32 s65, s68, 2\n"
        "s_add_u32 s65, s65, 1\n"
        "s_bitcmp1_b32 s55, s65\n"
        "s_cselect_b32 s65, 8192, 0\n"
        "s_lshl_b32 s72, s68, 19\n"
        "s_add_u32 s72, s72, s65\n"
        "s_add_u32 s72, s72, 203423744\n"
        "s_add_u32 s76, s72, s61\n"
        "s_add_u32 s79, s71, s58\n"
        "s_add_u32 s79, s79, 1998848\n"
        "s_add_u32 s77, s72, s62\n"
        "s_add_u32 s80, s71, s59\n"
        "s_add_u32 s80, s80, 1998848\n"
        "s_add_u32 s78, s72, s63\n"
        "s_add_u32 s81, s71, s60\n"
        "s_add_u32 s81, s81, 1998848\n"
        "s_mov_b32 s64, 0\n"
        ".Lr5_finw_top_%=:\n"
        "buffer_load_dword v24, v1, s[40:43], s69 offen sc1\n"
        "s_waitcnt vmcnt(0)\n"
        "v_cmp_gt_i32 vcc, s68, v24\n"
        "s_nop 0\n"
        "s_and_b64 vcc, vcc, 0xffff\n"
        "s_cbranch_vccz .Lr5_finw_ok_%=\n"
        "s_add_u32 s64, s64, 1\n"
        "s_and_b32 s65, s64, 255\n"
        "s_cbranch_scc1 .Lr5_finw_go_%=\n"
        "s_mov_b32 s65, 40960\n"
        "s_cmp_lt_u32 s64, 1048576\n"
        "s_cbranch_scc1 .Lr5_finw_chk_%=\n"
        "v_mov_b32 v24, 1\n"
        "buffer_store_dword v24, off, s[40:43], s65 sc1\n"
        "s_waitcnt vmcnt(0)\n"
        ".Lr5_finw_chk_%=:\n"
        "buffer_load_dword v24, off, s[40:43], s65 sc1\n"
        "s_waitcnt vmcnt(0)\n"
        "v_readfirstlane_b32 s65, v24\n"
        "s_nop 0\n"
        "s_cmp_lg_u32 s65, 1\n"
        "s_cbranch_scc1 .Lr5_finw_go_%=\n"
        "s_endpgm\n"
        ".Lr5_finw_go_%=:\n"
        "s_branch .Lr5_finw_top_%=\n"
        ".Lr5_finw_ok_%=:\n"
        "buffer_load_dwordx4 v[32:35], v3, s[40:43], s76 offen offset:0 sc1\n"
        "buffer_load_dwordx4 v[40:43], v3, s[40:43], s76 offen offset:512 sc1\n"
        "buffer_load_dwordx4 v[36:39], v3, s[40:43], s76 offen offset:256 sc1\n"
        "buffer_load_dwordx4 v[44:47], v3, s[40:43], s76 offen offset:768 sc1\n"
        "buffer_load_dwordx4 v[80:83], v0, s[40:43], s79 offen sc1\n"
        "buffer_load_dwordx4 v[48:51], v3, s[40:43], s77 offen offset:0 sc1\n"
        "buffer_load_dwordx4 v[56:59], v3, s[40:43], s77 offen offset:512 sc1\n"
        "buffer_load_dwordx4 v[52:55], v3, s[40:43], s77 offen offset:256 sc1\n"
        "buffer_load_dwordx4 v[60:63], v3, s[40:43], s77 offen offset:768 sc1\n"
        "buffer_load_dwordx4 v[84:87], v0, s[40:43], s80 offen sc1\n"
        "buffer_load_dwordx4 v[64:67], v4, s[40:43], s78 offen offset:0 sc1\n"
        "buffer_load_dwordx4 v[72:75], v4, s[40:43], s78 offen offset:512 sc1\n"
        "buffer_load_dwordx4 v[68:71], v4, s[40:43], s78 offen offset:256 sc1\n"
        "buffer_load_dwordx4 v[76:79], v4, s[40:43], s78 offen offset:768 sc1\n"
        "buffer_load_dwordx4 v[88:91], v0, s[40:43], s81 offen sc1\n"
        "s_waitcnt vmcnt(14)\n"
        "v_mfma_f32_16x16x32_bf16 v[92:95], v[8:11], v[32:35], 0\n"
        "s_waitcnt vmcnt(13)\n"
        "v_mfma_f32_16x16x32_bf16 v[92:95], v[12:15], v[40:43], v[92:95]\n"
        "s_waitcnt vmcnt(12)\n"
        "v_mfma_f32_16x16x32_bf16 v[96:99], v[8:11], v[36:39], 0\n"
        "s_waitcnt vmcnt(11)\n"
        "v_mfma_f32_16x16x32_bf16 v[96:99], v[12:15], v[44:47], v[96:99]\n"
        "s_waitcnt vmcnt(10)\n"
        "v_mfma_f32_16x16x32_bf16 v[92:95], v[16:19], v[80:83], v[92:95]\n"
        "v_mfma_f32_16x16x32_bf16 v[96:99], v[20:23], v[80:83], v[96:99]\n"
        "s_waitcnt vmcnt(9)\n"
        "v_mfma_f32_16x16x32_bf16 v[100:103], v[8:11], v[48:51], 0\n"
        "s_waitcnt vmcnt(8)\n"
        "v_mfma_f32_16x16x32_bf16 v[100:103], v[12:15], v[56:59], v[100:103]\n"
        "s_waitcnt vmcnt(7)\n"
        "v_mfma_f32_16x16x32_bf16 v[104:107], v[8:11], v[52:55], 0\n"
        "s_waitcnt vmcnt(6)\n"
        "v_mfma_f32_16x16x32_bf16 v[104:107], v[12:15], v[60:63], v[104:107]\n"
        "s_waitcnt vmcnt(5)\n"
        "v_mfma_f32_16x16x32_bf16 v[100:103], v[16:19], v[84:87], v[100:103]\n"
        "v_mfma_f32_16x16x32_bf16 v[104:107], v[20:23], v[84:87], v[104:107]\n"
        "s_waitcnt vmcnt(4)\n"
        "v_mfma_f32_16x16x32_bf16 v[108:111], v[8:11], v[64:67], 0\n"
        "s_waitcnt vmcnt(3)\n"
        "v_mfma_f32_16x16x32_bf16 v[108:111], v[12:15], v[72:75], v[108:111]\n"
        "s_waitcnt vmcnt(2)\n"
        "v_mfma_f32_16x16x32_bf16 v[112:115], v[8:11], v[68:71], 0\n"
        "s_waitcnt vmcnt(1)\n"
        "v_mfma_f32_16x16x32_bf16 v[112:115], v[12:15], v[76:79], v[112:115]\n"
        "s_waitcnt vmcnt(0)\n"
        "v_mfma_f32_16x16x32_bf16 v[108:111], v[16:19], v[88:91], v[108:111]\n"
        "v_mfma_f32_16x16x32_bf16 v[112:115], v[20:23], v[88:91], v[112:115]\n"
        "s_mul_i32 s65, s68, 36864\n"
        "v_cvt_pk_bf16_f32 v140, v92, v93\n"
        "v_cvt_pk_bf16_f32 v141, v94, v95\n"
        "v_cvt_pk_bf16_f32 v142, v96, v97\n"
        "v_cvt_pk_bf16_f32 v143, v98, v99\n"
        "s_nop 0\n"
        "v_permlane16_swap_b32 v140, v142\n"
        "v_permlane16_swap_b32 v141, v143\n"
        "buffer_store_dwordx4 v[140:143], v5, s[44:47], s65 offen offset:0\n"
        "v_cvt_pk_bf16_f32 v144, v100, v101\n"
        "v_cvt_pk_bf16_f32 v145, v102, v103\n"
        "v_cvt_pk_bf16_f32 v146, v104, v105\n"
        "v_cvt_pk_bf16_f32 v147, v106, v107\n"
        "s_nop 0\n"
        "v_permlane16_swap_b32 v144, v146\n"
        "v_permlane16_swap_b32 v145, v147\n"
        "buffer_store_dwordx4 v[144:147], v5, s[44:47], s65 offen offset:128\n"
        "s_nop 0\n"
        "v_cvt_pk_bf16_f32 v148, v108, v109\n"
        "v_cvt_pk_bf16_f32 v149, v110, v111\n"
        "v_cvt_pk_bf16_f32 v150, v112, v113\n"
        "v_cvt_pk_bf16_f32 v151, v114, v115\n"
        "s_nop 0\n"
        "v_permlane16_swap_b32 v148, v150\n"
        "v_permlane16_swap_b32 v149, v151\n"
        "buffer_store_dwordx4 v[148:151], v5, s[44:47], s65 offen offset:256\n"
        "s_add_u32 s68, s68, 1\n"
        "s_cmp_lt_u32 s68, 64\n"
        "s_cbranch_scc1 .Lr5_fin_%=\n"
        "s_endpgm\n"
            RED_ARGS : "v0", "v1", "v2", "v3", "v4", "v5", "v6", "v7", "v8", "v9", "v10", "v11", "v12", "v13", "v14", "v15", "v16", "v17", "v18", "v19", "v20", "v21", "v22", "v23", "v24", "v25", "v26", "v27", "v28", "v29", "v30", "v31", "v32", "v33", "v34", "v35", "v36", "v37", "v38", "v39", "v40", "v41", "v42", "v43", "v44", "v45", "v46", "v47", "v48", "v49", "v50", "v51", "v52", "v53", "v54", "v55", "v56", "v57", "v58", "v59", "v60", "v61", "v62", "v63", "v64", "v65", "v66", "v67", "v68", "v69", "v70", "v71", "v72", "v73", "v74", "v75", "v76", "v77", "v78", "v79", "v255", "a255", "s30", "s31", "s32", "s33", "s34", "s35", "s36", "s37", "s38", "s39", "s40", "s41", "s42", "s43", "s44", "s45", "s46", "s47", "s48", "s49", "s50", "s51", "s52", "s53", "s54", "s55", "s56", "s57", "s58", "s59", "s60", "s61", "s62", "s63", "s64", "s65", "s66", "s67", "s68", "s69", "s70", "s71", "s72", "s73", "s74", "s75", "s76", "s77", "s78", "s79", "s80", "s81", "s82", "s83", "s84", "s85", "s86", "s87", "s88", "s89", "s90", "s91", "s92", "s93", "s94", "s95", "s96", "s97", "s98", "s99", "vcc", "scc", "memory");
    } else {
        asm volatile(
        "s_nop 4\n"
        "s_mov_b64 s[40:41], %0\n"
        "s_and_b32 s41, s41, 0xffff\n"
        "s_mov_b32 s42, 236978176\n"
        "s_mov_b32 s43, 0x20000\n"
        "s_mov_b64 s[44:45], %1\n"
        "s_and_b32 s45, s45, 0xffff\n"
        "s_mov_b32 s46, 2359296\n"
        "s_mov_b32 s47, 0x20000\n"
        "s_mov_b64 s[56:57], %3\n"
        "s_mov_b64 s[58:59], %4\n"
        "s_mov_b64 s[60:61], %5\n"
        "s_mov_b64 s[62:63], %6\n"
        "v_mov_b64 v[0:1], %7\n"
        "v_mov_b64 v[2:3], %8\n"
        "v_mov_b64 v[4:5], %9\n"
        "v_mov_b64 v[6:7], %10\n"
        "v_mov_b64 v[8:9], %11\n"
        "v_mov_b64 v[10:11], %12\n"
        "v_mov_b64 v[12:13], %13\n"
        "v_mov_b64 v[14:15], %14\n"
        "v_mov_b64 v[16:17], %15\n"
        "v_mov_b64 v[18:19], %16\n"
        "v_mov_b64 v[20:21], %17\n"
        "v_mov_b64 v[22:23], %18\n"
        "s_mov_b32 s68, 0\n"
        "s_mul_i32 s65, s68, 36864\n"
        "buffer_load_dwordx4 v[80:83], v5, s[44:47], s65 offen offset:0\n"
        "buffer_load_dwordx4 v[84:87], v5, s[44:47], s65 offen offset:128\n"
        "buffer_load_dwordx4 v[88:91], v5, s[44:47], s65 offen offset:256\n"
        "v_lshrrev_b32 v26, 4, v0\n"
        "s_lshl_b32 s66, s57, 7\n"
        "s_mov_b32 s64, 0\n"
        ".Lr3_pair_top_%=:\n"
        "s_mov_b64 exec, 1\n"
        "buffer_load_dword v27, off, s[40:43], s66 sc1\n"
        "s_mov_b64 exec, -1\n"
        "s_waitcnt vmcnt(0)\n"
        "v_cmp_gt_i32 vcc, 0, v27\n"
        "s_nop 0\n"
        "s_and_b64 vcc, vcc, 1\n"
        "s_cbranch_vccz .Lr3_pair_ok_%=\n"
        "s_add_u32 s64, s64, 1\n"
        "s_and_b32 s65, s64, 255\n"
        "s_cbranch_scc1 .Lr3_pair_go_%=\n"
        "s_mov_b32 s65, 40960\n"
        "s_cmp_lt_u32 s64, 1048576\n"
        "s_cbranch_scc1 .Lr3_pair_chk_%=\n"
        "v_mov_b32 v27, 1\n"
        "buffer_store_dword v27, off, s[40:43], s65 sc1\n"
        "s_waitcnt vmcnt(0)\n"
        ".Lr3_pair_chk_%=:\n"
        "buffer_load_dword v27, off, s[40:43], s65 sc1\n"
        "s_waitcnt vmcnt(0)\n"
        "v_readfirstlane_b32 s65, v27\n"
        "s_nop 0\n"
        "s_cmp_lg_u32 s65, 1\n"
        "s_cbranch_scc1 .Lr3_pair_go_%=\n"
        "s_endpgm\n"
        ".Lr3_pair_go_%=:\n"
        "s_sleep 2\n"
        "s_branch .Lr3_pair_top_%=\n"
        ".Lr3_pair_ok_%=:\n"
        "v_readfirstlane_b32 s50, v27\n"
        "s_nop 0\n"
        "s_xor_b32 s50, s50, s56\n"
        "v_lshlrev_b32 v28, 7, v26\n"
        "s_sub_u32 s66, 0, s50\n"
        "s_mov_b32 s52, 24576\n"
        "s_mov_b32 s64, 0\n"
        ".Lr3_pg0_top_%=:\n"
        "buffer_load_dword v27, v28, s[40:43], s52 offen sc1\n"
        "s_waitcnt vmcnt(0)\n"
        "v_cmp_gt_i32 vcc, 0, v27\n"
        "s_cbranch_vccz .Lr3_pg0_ok_%=\n"
        "s_add_u32 s64, s64, 1\n"
        "s_and_b32 s65, s64, 255\n"
        "s_cbranch_scc1 .Lr3_pg0_go_%=\n"
        "s_mov_b32 s65, 40960\n"
        "s_cmp_lt_u32 s64, 1048576\n"
        "s_cbranch_scc1 .Lr3_pg0_chk_%=\n"
        "v_mov_b32 v27, 1\n"
        "buffer_store_dword v27, off, s[40:43], s65 sc1\n"
        "s_waitcnt vmcnt(0)\n"
        ".Lr3_pg0_chk_%=:\n"
        "buffer_load_dword v27, off, s[40:43], s65 sc1\n"
        "s_waitcnt vmcnt(0)\n"
        "v_readfirstlane_b32 s65, v27\n"
        "s_nop 0\n"
        "s_cmp_lg_u32 s65, 1\n"
        "s_cbranch_scc1 .Lr3_pg0_go_%=\n"
        "s_endpgm\n"
        ".Lr3_pg0_go_%=:\n"
        "s_sleep 2\n"
        "s_branch .Lr3_pg0_top_%=\n"
        ".Lr3_pg0_ok_%=:\n"
        "v_cmp_eq_u32 s[52:53], 1, v27\n"
        "s_nop 1\n"
        "s_mov_b32 s54, 32768\n"
        "s_mov_b32 s64, 0\n"
        ".Lr3_pg1_top_%=:\n"
        "buffer_load_dword v27, v28, s[40:43], s54 offen sc1\n"
        "s_waitcnt vmcnt(0)\n"
        "v_cmp_gt_i32 vcc, 0, v27\n"
        "s_cbranch_vccz .Lr3_pg1_ok_%=\n"
        "s_add_u32 s64, s64, 1\n"
        "s_and_b32 s65, s64, 255\n"
        "s_cbranch_scc1 .Lr3_pg1_go_%=\n"
        "s_mov_b32 s65, 40960\n"
        "s_cmp_lt_u32 s64, 1048576\n"
        "s_cbranch_scc1 .Lr3_pg1_chk_%=\n"
        "v_mov_b32 v27, 1\n"
        "buffer_store_dword v27, off, s[40:43], s65 sc1\n"
        "s_waitcnt vmcnt(0)\n"
        ".Lr3_pg1_chk_%=:\n"
        "buffer_load_dword v27, off, s[40:43], s65 sc1\n"
        "s_waitcnt vmcnt(0)\n"
        "v_readfirstlane_b32 s65, v27\n"
        "s_nop 0\n"
        "s_cmp_lg_u32 s65, 1\n"
        "s_cbranch_scc1 .Lr3_pg1_go_%=\n"
        "s_endpgm\n"
        ".Lr3_pg1_go_%=:\n"
        "s_sleep 2\n"
        "s_branch .Lr3_pg1_top_%=\n"
        ".Lr3_pg1_ok_%=:\n"
        "v_cmp_eq_u32 s[54:55], 1, v27\n"
        "s_nop 1\n"
        "s_mov_b32 vcc_lo, 0x55555555\n"
        "s_mov_b32 vcc_hi, 0x55555555\n"
        "s_xor_b64 vcc, vcc, s[52:53]\n"
        "s_bcnt1_i32_b64 s65, vcc\n"
        "s_sub_u32 s64, s65, 23\n"
        "s_cmp_lt_u32 s64, 19\n"
        "s_cbranch_scc1 .Lr3_rawmap_%=\n"
        "s_cmp_lt_u32 s65, 32\n"
        "s_cselect_b32 s65, 0, -1\n"
        "s_xor_b32 s52, s65, 0x55555555\n"
        "s_xor_b32 s53, s65, 0x55555555\n"
        "s_xor_b32 s54, s65, 0x55555555\n"
        "s_xor_b32 s55, s65, 0xaaaaaaaa\n"
        ".Lr3_rawmap_%=:\n"
        "s_xor_b32 s52, s52, s66\n"
        "s_xor_b32 s53, s53, s66\n"
        "s_xor_b32 s54, s54, s66\n"
        "s_xor_b32 s55, s55, s66\n"
        "s_and_b32 s69, s55, 1\n"
        "s_lshl_b32 s69, s69, 13\n"
        "s_xor_b32 s70, s69, 0\n"
        "s_cmp_eq_u32 s50, 0\n"
        "s_cbranch_scc0 .Lr3_l1_%=\n"
        ".Lr3_l0_%=:\n"
        "s_bitcmp1_b64 s[52:53], s68\n"
        "s_cselect_b32 s65, 8192, 0\n"
        "s_lshl_b32 s71, s68, 21\n"
        "s_add_u32 s71, s71, s65\n"
        "s_add_u32 s71, s71, 2097152\n"
        "s_lshr_b32 s65, s68, 1\n"
        "s_bitcmp1_b32 s54, s65\n"
        "s_cselect_b32 s65, 8192, 0\n"
        "s_lshl_b32 s72, s68, 20\n"
        "s_add_u32 s72, s72, s65\n"
        "s_add_u32 s72, s72, 136904704\n"
        "s_add_u32 s76, s72, s61\n"
        "s_add_u32 s79, s71, s58\n"
        "s_add_u32 s79, s79, 1916928\n"
        "s_add_u32 s77, s72, s62\n"
        "s_add_u32 s80, s71, s59\n"
        "s_add_u32 s80, s80, 1916928\n"
        "s_add_u32 s78, s72, s63\n"
        "s_add_u32 s81, s71, s60\n"
        "s_add_u32 s81, s81, 1916928\n"
        "s_mov_b32 s64, 0\n"
        ".Lr3_l0w_top_%=:\n"
        "buffer_load_dword v24, v1, s[40:43], s69 offen sc1\n"
        "s_waitcnt vmcnt(0)\n"
        "v_cmp_gt_i32 vcc, s68, v24\n"
        "s_nop 0\n"
        "s_and_b64 vcc, vcc, 0xffff\n"
        "s_cbranch_vccz .Lr3_l0w_ok_%=\n"
        "s_add_u32 s64, s64, 1\n"
        "s_and_b32 s65, s64, 255\n"
        "s_cbranch_scc1 .Lr3_l0w_go_%=\n"
        "s_mov_b32 s65, 40960\n"
        "s_cmp_lt_u32 s64, 1048576\n"
        "s_cbranch_scc1 .Lr3_l0w_chk_%=\n"
        "v_mov_b32 v24, 1\n"
        "buffer_store_dword v24, off, s[40:43], s65 sc1\n"
        "s_waitcnt vmcnt(0)\n"
        ".Lr3_l0w_chk_%=:\n"
        "buffer_load_dword v24, off, s[40:43], s65 sc1\n"
        "s_waitcnt vmcnt(0)\n"
        "v_readfirstlane_b32 s65, v24\n"
        "s_nop 0\n"
        "s_cmp_lg_u32 s65, 1\n"
        "s_cbranch_scc1 .Lr3_l0w_go_%=\n"
        "s_endpgm\n"
        ".Lr3_l0w_go_%=:\n"
        "s_branch .Lr3_l0w_top_%=\n"
        ".Lr3_l0w_ok_%=:\n"
        "buffer_load_dwordx4 v[32:35], v3, s[40:43], s76 offen offset:0 sc1\n"
        "buffer_load_dwordx4 v[40:43], v3, s[40:43], s76 offen offset:512 sc1\n"
        "buffer_load_dwordx4 v[36:39], v3, s[40:43], s76 offen offset:256 sc1\n"
        "buffer_load_dwordx4 v[44:47], v3, s[40:43], s76 offen offset:768 sc1\n"
        "buffer_load_dwordx4 v[48:51], v3, s[40:43], s77 offen offset:0 sc1\n"
        "buffer_load_dwordx4 v[56:59], v3, s[40:43], s77 offen offset:512 sc1\n"
        "buffer_load_dwordx4 v[52:55], v3, s[40:43], s77 offen offset:256 sc1\n"
        "buffer_load_dwordx4 v[60:63], v3, s[40:43], s77 offen offset:768 sc1\n"
        "buffer_load_dwordx4 v[64:67], v4, s[40:43], s78 offen offset:0 sc1\n"
        "buffer_load_dwordx4 v[72:75], v4, s[40:43], s78 offen offset:512 sc1\n"
        "buffer_load_dwordx4 v[68:71], v4, s[40:43], s78 offen offset:256 sc1\n"
        "buffer_load_dwordx4 v[76:79], v4, s[40:43], s78 offen offset:768 sc1\n"
        "s_add_u32 s72, s72, 294912\n"
        "s_add_u32 s71, s71, 1998848\n"
        "s_add_u32 s82, s72, s58\n"
        "s_add_u32 s85, s71, s58\n"
        "s_add_u32 s83, s72, s59\n"
        "s_add_u32 s86, s71, s59\n"
        "s_add_u32 s84, s72, s60\n"
        "s_add_u32 s87, s71, s60\n"
        "s_waitcnt vmcnt(11)\n"
        "v_mfma_f32_16x16x32_bf16 v[92:95], v[8:11], v[32:35], 0\n"
        "s_waitcnt vmcnt(10)\n"
        "v_mfma_f32_16x16x32_bf16 v[92:95], v[12:15], v[40:43], v[92:95]\n"
        "s_waitcnt vmcnt(9)\n"
        "v_mfma_f32_16x16x32_bf16 v[96:99], v[8:11], v[36:39], 0\n"
        "s_waitcnt vmcnt(8)\n"
        "v_mfma_f32_16x16x32_bf16 v[96:99], v[12:15], v[44:47], v[96:99]\n"
        "v_mfma_f32_16x16x32_bf16 v[92:95], v[16:19], v[80:83], v[92:95]\n"
        "v_mfma_f32_16x16x32_bf16 v[96:99], v[20:23], v[80:83], v[96:99]\n"
        "s_waitcnt vmcnt(7)\n"
        "v_mfma_f32_16x16x32_bf16 v[100:103], v[8:11], v[48:51], 0\n"
        "s_waitcnt vmcnt(6)\n"
        "v_mfma_f32_16x16x32_bf16 v[100:103], v[12:15], v[56:59], v[100:103]\n"
        "s_waitcnt vmcnt(5)\n"
        "v_mfma_f32_16x16x32_bf16 v[104:107], v[8:11], v[52:55], 0\n"
        "s_waitcnt vmcnt(4)\n"
        "v_mfma_f32_16x16x32_bf16 v[104:107], v[12:15], v[60:63], v[104:107]\n"
        "v_mfma_f32_16x16x32_bf16 v[100:103], v[16:19], v[84:87], v[100:103]\n"
        "v_mfma_f32_16x16x32_bf16 v[104:107], v[20:23], v[84:87], v[104:107]\n"
        "s_waitcnt vmcnt(3)\n"
        "v_mfma_f32_16x16x32_bf16 v[108:111], v[8:11], v[64:67], 0\n"
        "s_waitcnt vmcnt(2)\n"
        "v_mfma_f32_16x16x32_bf16 v[108:111], v[12:15], v[72:75], v[108:111]\n"
        "s_waitcnt vmcnt(1)\n"
        "v_mfma_f32_16x16x32_bf16 v[112:115], v[8:11], v[68:71], 0\n"
        "s_waitcnt vmcnt(0)\n"
        "v_mfma_f32_16x16x32_bf16 v[112:115], v[12:15], v[76:79], v[112:115]\n"
        "v_mfma_f32_16x16x32_bf16 v[108:111], v[16:19], v[88:91], v[108:111]\n"
        "v_mfma_f32_16x16x32_bf16 v[112:115], v[20:23], v[88:91], v[112:115]\n"
        "s_add_u32 s68, s68, 1\n"
        "s_mul_i32 s65, s68, 36864\n"
        "buffer_load_dwordx4 v[80:83], v5, s[44:47], s65 offen offset:0\n"
        "buffer_load_dwordx4 v[84:87], v5, s[44:47], s65 offen offset:128\n"
        "buffer_load_dwordx4 v[88:91], v5, s[44:47], s65 offen offset:256\n"
        "s_sub_u32 s68, s68, 1\n"
        "v_pk_add_f32 v[164:165], v[92:93], v[94:95]\n"
        "v_pk_add_f32 v[166:167], v[96:97], v[98:99]\n"
        "v_pk_add_f32 v[168:169], v[100:101], v[102:103]\n"
        "v_pk_add_f32 v[170:171], v[104:105], v[106:107]\n"
        "v_pk_add_f32 v[164:165], v[164:165], v[166:167]\n"
        "v_pk_add_f32 v[168:169], v[168:169], v[170:171]\n"
        "v_pk_add_f32 v[164:165], v[164:165], v[168:169]\n"
        "v_pk_add_f32 v[166:167], v[108:109], v[110:111]\n"
        "v_pk_add_f32 v[168:169], v[112:113], v[114:115]\n"
        "v_pk_add_f32 v[164:165], v[164:165], v[166:167]\n"
        "v_pk_add_f32 v[164:165], v[164:165], v[168:169]\n"
        "v_add_f32 v164, v164, v165\n"
        "v_cvt_pk_bf16_f32 v140, v92, v93\n"
        "v_cvt_pk_bf16_f32 v141, v94, v95\n"
        "v_add_f32_dpp v166, v164, v164 row_ror:8 row_mask:0xf bank_mask:0xf\n"
        "v_add_f32_dpp v167, v164, v164 row_ror:8 row_mask:0xf bank_mask:0xf\n"
        "v_cvt_pk_bf16_f32 v142, v96, v97\n"
        "v_cvt_pk_bf16_f32 v143, v98, v99\n"
        "v_permlane16_swap_b32 v166, v167\n"
        "v_add_f32 v164, v166, v167\n"
        "v_add_f32 v166, v166, v167\n"
        "v_cvt_pk_bf16_f32 v144, v100, v101\n"
        "v_cvt_pk_bf16_f32 v145, v102, v103\n"
        "v_permlane32_swap_b32 v164, v166\n"
        "v_add_f32 v164, v164, v166\n"
        "v_mul_f32 v164, 0x3baaaaab, v164\n"
        "v_pk_add_f32 v[116:117], v[92:93], v[164:165] op_sel_hi:[1,0] neg_lo:[0,1] neg_hi:[0,1]\n"
        "v_pk_add_f32 v[118:119], v[94:95], v[164:165] op_sel_hi:[1,0] neg_lo:[0,1] neg_hi:[0,1]\n"
        "v_pk_add_f32 v[120:121], v[96:97], v[164:165] op_sel_hi:[1,0] neg_lo:[0,1] neg_hi:[0,1]\n"
        "v_pk_add_f32 v[122:123], v[98:99], v[164:165] op_sel_hi:[1,0] neg_lo:[0,1] neg_hi:[0,1]\n"
        "v_pk_add_f32 v[124:125], v[100:101], v[164:165] op_sel_hi:[1,0] neg_lo:[0,1] neg_hi:[0,1]\n"
        "v_pk_add_f32 v[126:127], v[102:103], v[164:165] op_sel_hi:[1,0] neg_lo:[0,1] neg_hi:[0,1]\n"
        "v_pk_add_f32 v[128:129], v[104:105], v[164:165] op_sel_hi:[1,0] neg_lo:[0,1] neg_hi:[0,1]\n"
        "v_pk_add_f32 v[130:131], v[106:107], v[164:165] op_sel_hi:[1,0] neg_lo:[0,1] neg_hi:[0,1]\n"
        "v_pk_add_f32 v[132:133], v[108:109], v[164:165] op_sel_hi:[1,0] neg_lo:[0,1] neg_hi:[0,1]\n"
        "v_pk_add_f32 v[134:135], v[110:111], v[164:165] op_sel_hi:[1,0] neg_lo:[0,1] neg_hi:[0,1]\n"
        "v_pk_add_f32 v[136:137], v[112:113], v[164:165] op_sel_hi:[1,0] neg_lo:[0,1] neg_hi:[0,1]\n"
        "v_pk_add_f32 v[138:139], v[114:115], v[164:165] op_sel_hi:[1,0] neg_lo:[0,1] neg_hi:[0,1]\n"
        "v_pk_mul_f32 v[166:167], v[116:117], v[116:117]\n"
        "v_pk_mul_f32 v[168:169], v[118:119], v[118:119]\n"
        "v_pk_fma_f32 v[166:167], v[120:121], v[120:121], v[166:167]\n"
        "v_pk_fma_f32 v[168:169], v[122:123], v[122:123], v[168:169]\n"
        "v_pk_fma_f32 v[166:167], v[124:125], v[124:125], v[166:167]\n"
        "v_pk_fma_f32 v[168:169], v[126:127], v[126:127], v[168:169]\n"
        "v_pk_fma_f32 v[166:167], v[128:129], v[128:129], v[166:167]\n"
        "v_pk_fma_f32 v[168:169], v[130:131], v[130:131], v[168:169]\n"
        "v_pk_fma_f32 v[166:167], v[132:133], v[132:133], v[166:167]\n"
        "v_pk_fma_f32 v[168:169], v[134:135], v[134:135], v[168:169]\n"
        "v_pk_fma_f32 v[166:167], v[136:137], v[136:137], v[166:167]\n"
        "v_pk_fma_f32 v[168:169], v[138:139], v[138:139], v[168:169]\n"
        "v_pk_add_f32 v[166:167], v[166:167], v[168:169]\n"
        "v_add_f32 v165, v166, v167\n"
        "v_cvt_pk_bf16_f32 v146, v104, v105\n"
        "v_cvt_pk_bf16_f32 v147, v106, v107\n"
        "v_add_f32_dpp v166, v165, v165 row_ror:8 row_mask:0xf bank_mask:0xf\n"
        "v_add_f32_dpp v167, v165, v165 row_ror:8 row_mask:0xf bank_mask:0xf\n"
        "v_cvt_pk_bf16_f32 v148, v108, v109\n"
        "v_cvt_pk_bf16_f32 v149, v110, v111\n"
        "v_permlane16_swap_b32 v166, v167\n"
        "v_add_f32 v165, v166, v167\n"
        "v_add_f32 v166, v166, v167\n"
        "v_cvt_pk_bf16_f32 v150, v112, v113\n"
        "v_cvt_pk_bf16_f32 v151, v114, v115\n"
        "v_permlane32_swap_b32 v165, v166\n"
        "v_add_f32 v165, v165, v166\n"
        "ds_write_b64 v6, v[164:165]\n"
        "v_permlane16_swap_b32 v140, v142\n"
        "v_permlane16_swap_b32 v141, v143\n"
        "v_permlane16_swap_b32 v144, v146\n"
        "v_permlane16_swap_b32 v145, v147\n"
        "s_waitcnt lgkmcnt(0)\n"
        "s_barrier\n"
        "ds_read_b64 v[166:167], v7 offset:0\n"
        "ds_read_b64 v[168:169], v7 offset:64\n"
        "ds_read_b64 v[170:171], v7 offset:128\n"
        "v_permlane16_swap_b32 v148, v150\n"
        "v_permlane16_swap_b32 v149, v151\n"
        "buffer_store_dwordx4 v[140:143], v0, s[40:43], s85 offen sc1\n"
        "buffer_store_dwordx4 v[144:147], v0, s[40:43], s86 offen sc1\n"
        "buffer_store_dwordx4 v[148:151], v0, s[40:43], s87 offen sc1\n"
        "s_waitcnt lgkmcnt(0)\n"
        "v_add_f32 v172, v166, v168\n"
        "v_add_f32 v173, v167, v169\n"
        "v_add_f32 v172, v172, v170\n"
        "v_add_f32 v173, v173, v171\n"
        "v_mul_f32 v172, 0x3eaaaaab, v172\n"
        "v_sub_f32 v166, v166, v172\n"
        "v_sub_f32 v168, v168, v172\n"
        "v_sub_f32 v170, v170, v172\n"
        "v_mul_f32 v166, v166, v166\n"
        "v_fmac_f32 v166, v168, v168\n"
        "v_fmac_f32 v166, v170, v170\n"
        "v_fmamk_f32 v173, v166, 0x43400000, v173\n"
        "v_mul_f32 v173, 0x3ae38e39, v173\n"
        "v_cvt_pk_bf16_f32 v172, v172, v173\n"
        "v_and_b32 v173, 0xffff0000, v172\n"
        "v_lshlrev_b32 v172, 16, v172\n"
        "v_add_f32 v173, 0x3727c5ac, v173\n"
        "v_cvt_pk_bf16_f32 v173, v173, v173\n"
        "v_lshlrev_b32 v173, 16, v173\n"
        "v_sqrt_f32 v173, v173\n"
        "v_sub_f32 v172, v164, v172\n"
        "v_cvt_pk_bf16_f32 v173, v173, v173\n"
        "v_lshlrev_b32 v173, 16, v173\n"
        "v_rcp_f32 v173, v173\n"
        "v_mov_b32 v25, s68\n"
        "v_mul_f32 v172, v172, v173\n"
        "v_pk_fma_f32 v[116:117], v[116:117], v[172:173], v[172:173] op_sel:[0,1,0] op_sel_hi:[1,1,0]\n"
        "v_pk_fma_f32 v[118:119], v[118:119], v[172:173], v[172:173] op_sel:[0,1,0] op_sel_hi:[1,1,0]\n"
        "v_pk_fma_f32 v[120:121], v[120:121], v[172:173], v[172:173] op_sel:[0,1,0] op_sel_hi:[1,1,0]\n"
        "v_pk_fma_f32 v[122:123], v[122:123], v[172:173], v[172:173] op_sel:[0,1,0] op_sel_hi:[1,1,0]\n"
        "v_cvt_pk_bf16_f32 v152, v116, v117\n"
        "v_cvt_pk_bf16_f32 v153, v118, v119\n"
        "v_cvt_pk_bf16_f32 v154, v120, v121\n"
        "v_cvt_pk_bf16_f32 v155, v122, v123\n"
        "s_nop 0\n"
        "v_permlane16_swap_b32 v152, v154\n"
        "v_permlane16_swap_b32 v153, v155\n"
        "buffer_store_dwordx4 v[152:155], v0, s[40:43], s82 offen sc1\n"
        "v_pk_fma_f32 v[124:125], v[124:125], v[172:173], v[172:173] op_sel:[0,1,0] op_sel_hi:[1,1,0]\n"
        "v_pk_fma_f32 v[126:127], v[126:127], v[172:173], v[172:173] op_sel:[0,1,0] op_sel_hi:[1,1,0]\n"
        "v_pk_fma_f32 v[128:129], v[128:129], v[172:173], v[172:173] op_sel:[0,1,0] op_sel_hi:[1,1,0]\n"
        "v_pk_fma_f32 v[130:131], v[130:131], v[172:173], v[172:173] op_sel:[0,1,0] op_sel_hi:[1,1,0]\n"
        "v_cvt_pk_bf16_f32 v156, v124, v125\n"
        "v_cvt_pk_bf16_f32 v157, v126, v127\n"
        "v_cvt_pk_bf16_f32 v158, v128, v129\n"
        "v_cvt_pk_bf16_f32 v159, v130, v131\n"
        "s_nop 0\n"
        "v_permlane16_swap_b32 v156, v158\n"
        "v_permlane16_swap_b32 v157, v159\n"
        "buffer_store_dwordx4 v[156:159], v0, s[40:43], s83 offen sc1\n"
        "v_pk_fma_f32 v[132:133], v[132:133], v[172:173], v[172:173] op_sel:[0,1,0] op_sel_hi:[1,1,0]\n"
        "v_pk_fma_f32 v[134:135], v[134:135], v[172:173], v[172:173] op_sel:[0,1,0] op_sel_hi:[1,1,0]\n"
        "v_pk_fma_f32 v[136:137], v[136:137], v[172:173], v[172:173] op_sel:[0,1,0] op_sel_hi:[1,1,0]\n"
        "v_pk_fma_f32 v[138:139], v[138:139], v[172:173], v[172:173] op_sel:[0,1,0] op_sel_hi:[1,1,0]\n"
        "v_cvt_pk_bf16_f32 v160, v132, v133\n"
        "v_cvt_pk_bf16_f32 v161, v134, v135\n"
        "v_cvt_pk_bf16_f32 v162, v136, v137\n"
        "v_cvt_pk_bf16_f32 v163, v138, v139\n"
        "s_nop 0\n"
        "v_permlane16_swap_b32 v160, v162\n"
        "v_permlane16_swap_b32 v161, v163\n"
        "buffer_store_dwordx4 v[160:163], v0, s[40:43], s84 offen sc1\n"
        "s_waitcnt vmcnt(0)\n"
        "s_barrier\n"
        "buffer_store_dword v25, v2, s[40:43], s70 offen sc1\n"
        "s_add_u32 s68, s68, 1\n"
        "s_cmp_lt_u32 s68, 64\n"
        "s_cbranch_scc1 .Lr3_l0_%=\n"
        "s_endpgm\n"
        ".Lr3_l1_%=:\n"
        "s_bitcmp1_b64 s[52:53], s68\n"
        "s_cselect_b32 s65, 8192, 0\n"
        "s_lshl_b32 s71, s68, 21\n"
        "s_add_u32 s71, s71, s65\n"
        "s_add_u32 s71, s71, 2097152\n"
        "s_lshr_b32 s65, s68, 1\n"
        "s_bitcmp1_b32 s54, s65\n"
        "s_cselect_b32 s65, 8192, 0\n"
        "s_lshl_b32 s72, s68, 20\n"
        "s_add_u32 s72, s72, s65\n"
        "s_add_u32 s72, s72, 136904704\n"
        "s_add_u32 s76, s72, s61\n"
        "s_add_u32 s79, s71, s58\n"
        "s_add_u32 s79, s79, 1916928\n"
        "s_add_u32 s77, s72, s62\n"
        "s_add_u32 s80, s71, s59\n"
        "s_add_u32 s80, s80, 1916928\n"
        "s_add_u32 s78, s72, s63\n"
        "s_add_u32 s81, s71, s60\n"
        "s_add_u32 s81, s81, 1916928\n"
        "s_mov_b32 s64, 0\n"
        ".Lr3_l1w_top_%=:\n"
        "buffer_load_dword v24, v1, s[40:43], s69 offen sc1\n"
        "s_waitcnt vmcnt(0)\n"
        "v_cmp_gt_i32 vcc, s68, v24\n"
        "s_nop 0\n"
        "s_and_b64 vcc, vcc, 0xffff\n"
        "s_cbranch_vccz .Lr3_l1w_ok_%=\n"
        "s_add_u32 s64, s64, 1\n"
        "s_and_b32 s65, s64, 255\n"
        "s_cbranch_scc1 .Lr3_l1w_go_%=\n"
        "s_mov_b32 s65, 40960\n"
        "s_cmp_lt_u32 s64, 1048576\n"
        "s_cbranch_scc1 .Lr3_l1w_chk_%=\n"
        "v_mov_b32 v24, 1\n"
        "buffer_store_dword v24, off, s[40:43], s65 sc1\n"
        "s_waitcnt vmcnt(0)\n"
        ".Lr3_l1w_chk_%=:\n"
        "buffer_load_dword v24, off, s[40:43], s65 sc1\n"
        "s_waitcnt vmcnt(0)\n"
        "v_readfirstlane_b32 s65, v24\n"
        "s_nop 0\n"
        "s_cmp_lg_u32 s65, 1\n"
        "s_cbranch_scc1 .Lr3_l1w_go_%=\n"
        "s_endpgm\n"
        ".Lr3_l1w_go_%=:\n"
        "s_branch .Lr3_l1w_top_%=\n"
        ".Lr3_l1w_ok_%=:\n"
        "buffer_load_dwordx4 v[32:35], v3, s[40:43], s76 offen offset:0 sc1\n"
        "buffer_load_dwordx4 v[40:43], v3, s[40:43], s76 offen offset:512 sc1\n"
        "buffer_load_dwordx4 v[36:39], v3, s[40:43], s76 offen offset:256 sc1\n"
        "buffer_load_dwordx4 v[44:47], v3, s[40:43], s76 offen offset:768 sc1\n"
        "buffer_load_dwordx4 v[80:83], v0, s[40:43], s79 offen sc1\n"
        "buffer_load_dwordx4 v[48:51], v3, s[40:43], s77 offen offset:0 sc1\n"
        "buffer_load_dwordx4 v[56:59], v3, s[40:43], s77 offen offset:512 sc1\n"
        "buffer_load_dwordx4 v[52:55], v3, s[40:43], s77 offen offset:256 sc1\n"
        "buffer_load_dwordx4 v[60:63], v3, s[40:43], s77 offen offset:768 sc1\n"
        "buffer_load_dwordx4 v[84:87], v0, s[40:43], s80 offen sc1\n"
        "buffer_load_dwordx4 v[64:67], v4, s[40:43], s78 offen offset:0 sc1\n"
        "buffer_load_dwordx4 v[72:75], v4, s[40:43], s78 offen offset:512 sc1\n"
        "buffer_load_dwordx4 v[68:71], v4, s[40:43], s78 offen offset:256 sc1\n"
        "buffer_load_dwordx4 v[76:79], v4, s[40:43], s78 offen offset:768 sc1\n"
        "buffer_load_dwordx4 v[88:91], v0, s[40:43], s81 offen sc1\n"
        "s_add_u32 s72, s72, 294912\n"
        "s_add_u32 s71, s71, 1998848\n"
        "s_add_u32 s82, s72, s58\n"
        "s_add_u32 s85, s71, s58\n"
        "s_add_u32 s83, s72, s59\n"
        "s_add_u32 s86, s71, s59\n"
        "s_add_u32 s84, s72, s60\n"
        "s_add_u32 s87, s71, s60\n"
        "s_waitcnt vmcnt(14)\n"
        "v_mfma_f32_16x16x32_bf16 v[92:95], v[8:11], v[32:35], 0\n"
        "s_waitcnt vmcnt(13)\n"
        "v_mfma_f32_16x16x32_bf16 v[92:95], v[12:15], v[40:43], v[92:95]\n"
        "s_waitcnt vmcnt(12)\n"
        "v_mfma_f32_16x16x32_bf16 v[96:99], v[8:11], v[36:39], 0\n"
        "s_waitcnt vmcnt(11)\n"
        "v_mfma_f32_16x16x32_bf16 v[96:99], v[12:15], v[44:47], v[96:99]\n"
        "s_waitcnt vmcnt(10)\n"
        "v_mfma_f32_16x16x32_bf16 v[92:95], v[16:19], v[80:83], v[92:95]\n"
        "v_mfma_f32_16x16x32_bf16 v[96:99], v[20:23], v[80:83], v[96:99]\n"
        "s_waitcnt vmcnt(9)\n"
        "v_mfma_f32_16x16x32_bf16 v[100:103], v[8:11], v[48:51], 0\n"
        "s_waitcnt vmcnt(8)\n"
        "v_mfma_f32_16x16x32_bf16 v[100:103], v[12:15], v[56:59], v[100:103]\n"
        "s_waitcnt vmcnt(7)\n"
        "v_mfma_f32_16x16x32_bf16 v[104:107], v[8:11], v[52:55], 0\n"
        "s_waitcnt vmcnt(6)\n"
        "v_mfma_f32_16x16x32_bf16 v[104:107], v[12:15], v[60:63], v[104:107]\n"
        "s_waitcnt vmcnt(5)\n"
        "v_mfma_f32_16x16x32_bf16 v[100:103], v[16:19], v[84:87], v[100:103]\n"
        "v_mfma_f32_16x16x32_bf16 v[104:107], v[20:23], v[84:87], v[104:107]\n"
        "s_waitcnt vmcnt(4)\n"
        "v_mfma_f32_16x16x32_bf16 v[108:111], v[8:11], v[64:67], 0\n"
        "s_waitcnt vmcnt(3)\n"
        "v_mfma_f32_16x16x32_bf16 v[108:111], v[12:15], v[72:75], v[108:111]\n"
        "s_waitcnt vmcnt(2)\n"
        "v_mfma_f32_16x16x32_bf16 v[112:115], v[8:11], v[68:71], 0\n"
        "s_waitcnt vmcnt(1)\n"
        "v_mfma_f32_16x16x32_bf16 v[112:115], v[12:15], v[76:79], v[112:115]\n"
        "s_waitcnt vmcnt(0)\n"
        "v_mfma_f32_16x16x32_bf16 v[108:111], v[16:19], v[88:91], v[108:111]\n"
        "v_mfma_f32_16x16x32_bf16 v[112:115], v[20:23], v[88:91], v[112:115]\n"
        "s_add_u32 s68, s68, 1\n"
        "s_sub_u32 s68, s68, 1\n"
        "v_pk_add_f32 v[164:165], v[92:93], v[94:95]\n"
        "v_pk_add_f32 v[166:167], v[96:97], v[98:99]\n"
        "v_pk_add_f32 v[168:169], v[100:101], v[102:103]\n"
        "v_pk_add_f32 v[170:171], v[104:105], v[106:107]\n"
        "v_pk_add_f32 v[164:165], v[164:165], v[166:167]\n"
        "v_pk_add_f32 v[168:169], v[168:169], v[170:171]\n"
        "v_pk_add_f32 v[164:165], v[164:165], v[168:169]\n"
        "s_nop 0\n"
        "v_pk_add_f32 v[166:167], v[108:109], v[110:111]\n"
        "v_pk_add_f32 v[168:169], v[112:113], v[114:115]\n"
        "v_pk_add_f32 v[164:165], v[164:165], v[166:167]\n"
        "v_pk_add_f32 v[164:165], v[164:165], v[168:169]\n"
        "v_add_f32 v164, v164, v165\n"
        "v_cvt_pk_bf16_f32 v140, v92, v93\n"
        "v_cvt_pk_bf16_f32 v141, v94, v95\n"
        "v_add_f32_dpp v166, v164, v164 row_ror:8 row_mask:0xf bank_mask:0xf\n"
        "v_add_f32_dpp v167, v164, v164 row_ror:8 row_mask:0xf bank_mask:0xf\n"
        "v_cvt_pk_bf16_f32 v142, v96, v97\n"
        "v_cvt_pk_bf16_f32 v143, v98, v99\n"
        "v_permlane16_swap_b32 v166, v167\n"
        "v_add_f32 v164, v166, v167\n"
        "v_add_f32 v166, v166, v167\n"
        "v_cvt_pk_bf16_f32 v144, v100, v101\n"
        "v_cvt_pk_bf16_f32 v145, v102, v103\n"
        "v_permlane32_swap_b32 v164, v166\n"
        "v_add_f32 v164, v164, v166\n"
        "v_mul_f32 v164, 0x3baaaaab, v164\n"
        "v_pk_add_f32 v[116:117], v[92:93], v[164:165] op_sel_hi:[1,0] neg_lo:[0,1] neg_hi:[0,1]\n"
        "v_pk_add_f32 v[118:119], v[94:95], v[164:165] op_sel_hi:[1,0] neg_lo:[0,1] neg_hi:[0,1]\n"
        "v_pk_add_f32 v[120:121], v[96:97], v[164:165] op_sel_hi:[1,0] neg_lo:[0,1] neg_hi:[0,1]\n"
        "v_pk_add_f32 v[122:123], v[98:99], v[164:165] op_sel_hi:[1,0] neg_lo:[0,1] neg_hi:[0,1]\n"
        "v_pk_add_f32 v[124:125], v[100:101], v[164:165] op_sel_hi:[1,0] neg_lo:[0,1] neg_hi:[0,1]\n"
        "v_pk_add_f32 v[126:127], v[102:103], v[164:165] op_sel_hi:[1,0] neg_lo:[0,1] neg_hi:[0,1]\n"
        "v_pk_add_f32 v[128:129], v[104:105], v[164:165] op_sel_hi:[1,0] neg_lo:[0,1] neg_hi:[0,1]\n"
        "v_pk_add_f32 v[130:131], v[106:107], v[164:165] op_sel_hi:[1,0] neg_lo:[0,1] neg_hi:[0,1]\n"
        "v_pk_add_f32 v[132:133], v[108:109], v[164:165] op_sel_hi:[1,0] neg_lo:[0,1] neg_hi:[0,1]\n"
        "v_pk_add_f32 v[134:135], v[110:111], v[164:165] op_sel_hi:[1,0] neg_lo:[0,1] neg_hi:[0,1]\n"
        "v_pk_add_f32 v[136:137], v[112:113], v[164:165] op_sel_hi:[1,0] neg_lo:[0,1] neg_hi:[0,1]\n"
        "v_pk_add_f32 v[138:139], v[114:115], v[164:165] op_sel_hi:[1,0] neg_lo:[0,1] neg_hi:[0,1]\n"
        "v_pk_mul_f32 v[166:167], v[116:117], v[116:117]\n"
        "v_pk_mul_f32 v[168:169], v[118:119], v[118:119]\n"
        "v_pk_fma_f32 v[166:167], v[120:121], v[120:121], v[166:167]\n"
        "v_pk_fma_f32 v[168:169], v[122:123], v[122:123], v[168:169]\n"
        "v_pk_fma_f32 v[166:167], v[124:125], v[124:125], v[166:167]\n"
        "v_pk_fma_f32 v[168:169], v[126:127], v[126:127], v[168:169]\n"
        "v_pk_fma_f32 v[166:167], v[128:129], v[128:129], v[166:167]\n"
        "v_pk_fma_f32 v[168:169], v[130:131], v[130:131], v[168:169]\n"
        "v_pk_fma_f32 v[166:167], v[132:133], v[132:133], v[166:167]\n"
        "v_pk_fma_f32 v[168:169], v[134:135], v[134:135], v[168:169]\n"
        "v_pk_fma_f32 v[166:167], v[136:137], v[136:137], v[166:167]\n"
        "v_pk_fma_f32 v[168:169], v[138:139], v[138:139], v[168:169]\n"
        "v_pk_add_f32 v[166:167], v[166:167], v[168:169]\n"
        "v_add_f32 v165, v166, v167\n"
        "v_cvt_pk_bf16_f32 v146, v104, v105\n"
        "v_cvt_pk_bf16_f32 v147, v106, v107\n"
        "v_add_f32_dpp v166, v165, v165 row_ror:8 row_mask:0xf bank_mask:0xf\n"
        "v_add_f32_dpp v167, v165, v165 row_ror:8 row_mask:0xf bank_mask:0xf\n"
        "v_cvt_pk_bf16_f32 v148, v108, v109\n"
        "v_cvt_pk_bf16_f32 v149, v110, v111\n"
        "v_permlane16_swap_b32 v166, v167\n"
        "v_add_f32 v165, v166, v167\n"
        "v_add_f32 v166, v166, v167\n"
        "v_cvt_pk_bf16_f32 v150, v112, v113\n"
        "v_cvt_pk_bf16_f32 v151, v114, v115\n"
        "v_permlane32_swap_b32 v165, v166\n"
        "v_add_f32 v165, v165, v166\n"
        "ds_write_b64 v6, v[164:165]\n"
        "v_permlane16_swap_b32 v140, v142\n"
        "v_permlane16_swap_b32 v141, v143\n"
        "v_permlane16_swap_b32 v144, v146\n"
        "v_permlane16_swap_b32 v145, v147\n"
        "s_waitcnt lgkmcnt(0)\n"
        "s_barrier\n"
        "ds_read_b64 v[166:167], v7 offset:0\n"
        "ds_read_b64 v[168:169], v7 offset:64\n"
        "ds_read_b64 v[170:171], v7 offset:128\n"
        "v_permlane16_swap_b32 v148, v150\n"
        "v_permlane16_swap_b32 v149, v151\n"
        "buffer_store_dwordx4 v[140:143], v0, s[40:43], s85 offen sc1\n"
        "buffer_store_dwordx4 v[144:147], v0, s[40:43], s86 offen sc1\n"
        "buffer_store_dwordx4 v[148:151], v0, s[40:43], s87 offen sc1\n"
        "s_waitcnt lgkmcnt(0)\n"
        "v_add_f32 v172, v166, v168\n"
        "v_add_f32 v173, v167, v169\n"
        "v_add_f32 v172, v172, v170\n"
        "v_add_f32 v173, v173, v171\n"
        "v_mul_f32 v172, 0x3eaaaaab, v172\n"
        "v_sub_f32 v166, v166, v172\n"
        "v_sub_f32 v168, v168, v172\n"
        "v_sub_f32 v170, v170, v172\n"
        "v_mul_f32 v166, v166, v166\n"
        "v_fmac_f32 v166, v168, v168\n"
        "v_fmac_f32 v166, v170, v170\n"
        "v_fmamk_f32 v173, v166, 0x43400000, v173\n"
        "v_mul_f32 v173, 0x3ae38e39, v173\n"
        "v_cvt_pk_bf16_f32 v172, v172, v173\n"
        "v_and_b32 v173, 0xffff0000, v172\n"
        "v_lshlrev_b32 v172, 16, v172\n"
        "v_add_f32 v173, 0x3727c5ac, v173\n"
        "v_cvt_pk_bf16_f32 v173, v173, v173\n"
        "v_lshlrev_b32 v173, 16, v173\n"
        "v_sqrt_f32 v173, v173\n"
        "v_sub_f32 v172, v164, v172\n"
        "v_cvt_pk_bf16_f32 v173, v173, v173\n"
        "v_lshlrev_b32 v173, 16, v173\n"
        "v_rcp_f32 v173, v173\n"
        "v_mov_b32 v25, s68\n"
        "v_mul_f32 v172, v172, v173\n"
        "v_pk_fma_f32 v[116:117], v[116:117], v[172:173], v[172:173] op_sel:[0,1,0] op_sel_hi:[1,1,0]\n"
        "v_pk_fma_f32 v[118:119], v[118:119], v[172:173], v[172:173] op_sel:[0,1,0] op_sel_hi:[1,1,0]\n"
        "v_pk_fma_f32 v[120:121], v[120:121], v[172:173], v[172:173] op_sel:[0,1,0] op_sel_hi:[1,1,0]\n"
        "v_pk_fma_f32 v[122:123], v[122:123], v[172:173], v[172:173] op_sel:[0,1,0] op_sel_hi:[1,1,0]\n"
        "v_cvt_pk_bf16_f32 v152, v116, v117\n"
        "v_cvt_pk_bf16_f32 v153, v118, v119\n"
        "v_cvt_pk_bf16_f32 v154, v120, v121\n"
        "v_cvt_pk_bf16_f32 v155, v122, v123\n"
        "s_nop 0\n"
        "v_permlane16_swap_b32 v152, v154\n"
        "v_permlane16_swap_b32 v153, v155\n"
        "buffer_store_dwordx4 v[152:155], v0, s[40:43], s82 offen sc1\n"
        "v_pk_fma_f32 v[124:125], v[124:125], v[172:173], v[172:173] op_sel:[0,1,0] op_sel_hi:[1,1,0]\n"
        "v_pk_fma_f32 v[126:127], v[126:127], v[172:173], v[172:173] op_sel:[0,1,0] op_sel_hi:[1,1,0]\n"
        "v_pk_fma_f32 v[128:129], v[128:129], v[172:173], v[172:173] op_sel:[0,1,0] op_sel_hi:[1,1,0]\n"
        "v_pk_fma_f32 v[130:131], v[130:131], v[172:173], v[172:173] op_sel:[0,1,0] op_sel_hi:[1,1,0]\n"
        "v_cvt_pk_bf16_f32 v156, v124, v125\n"
        "v_cvt_pk_bf16_f32 v157, v126, v127\n"
        "v_cvt_pk_bf16_f32 v158, v128, v129\n"
        "v_cvt_pk_bf16_f32 v159, v130, v131\n"
        "s_nop 0\n"
        "v_permlane16_swap_b32 v156, v158\n"
        "v_permlane16_swap_b32 v157, v159\n"
        "buffer_store_dwordx4 v[156:159], v0, s[40:43], s83 offen sc1\n"
        "v_pk_fma_f32 v[132:133], v[132:133], v[172:173], v[172:173] op_sel:[0,1,0] op_sel_hi:[1,1,0]\n"
        "v_pk_fma_f32 v[134:135], v[134:135], v[172:173], v[172:173] op_sel:[0,1,0] op_sel_hi:[1,1,0]\n"
        "v_pk_fma_f32 v[136:137], v[136:137], v[172:173], v[172:173] op_sel:[0,1,0] op_sel_hi:[1,1,0]\n"
        "v_pk_fma_f32 v[138:139], v[138:139], v[172:173], v[172:173] op_sel:[0,1,0] op_sel_hi:[1,1,0]\n"
        "v_cvt_pk_bf16_f32 v160, v132, v133\n"
        "v_cvt_pk_bf16_f32 v161, v134, v135\n"
        "v_cvt_pk_bf16_f32 v162, v136, v137\n"
        "v_cvt_pk_bf16_f32 v163, v138, v139\n"
        "s_nop 0\n"
        "v_permlane16_swap_b32 v160, v162\n"
        "v_permlane16_swap_b32 v161, v163\n"
        "buffer_store_dwordx4 v[160:163], v0, s[40:43], s84 offen sc1\n"
        "s_waitcnt vmcnt(0)\n"
        "s_barrier\n"
        "buffer_store_dword v25, v2, s[40:43], s70 offen sc1\n"
        "s_add_u32 s68, s68, 1\n"
        "s_cmp_lt_u32 s68, 64\n"
        "s_cbranch_scc1 .Lr3_l1_%=\n"
        "s_endpgm\n"
            RED_ARGS : "v0", "v1", "v2", "v3", "v4", "v5", "v6", "v7", "v8", "v9", "v10", "v11", "v12", "v13", "v14", "v15", "v16", "v17", "v18", "v19", "v20", "v21", "v22", "v23", "v24", "v25", "v26", "v27", "v28", "v29", "v30", "v31", "v32", "v33", "v34", "v35", "v36", "v37", "v38", "v39", "v40", "v41", "v42", "v43", "v44", "v45", "v46", "v47", "v48", "v49", "v50", "v51", "v52", "v53", "v54", "v55", "v56", "v57", "v58", "v59", "v60", "v61", "v62", "v63", "v64", "v65", "v66", "v67", "v68", "v69", "v70", "v71", "v72", "v73", "v74", "v75", "v76", "v77", "v78", "v79", "v255", "a255", "s30", "s31", "s32", "s33", "s34", "s35", "s36", "s37", "s38", "s39", "s40", "s41", "s42", "s43", "s44", "s45", "s46", "s47", "s48", "s49", "s50", "s51", "s52", "s53", "s54", "s55", "s56", "s57", "s58", "s59", "s60", "s61", "s62", "s63", "s64", "s65", "s66", "s67", "s68", "s69", "s70", "s71", "s72", "s73", "s74", "s75", "s76", "s77", "s78", "s79", "s80", "s81", "s82", "s83", "s84", "s85", "s86", "s87", "s88", "s89", "s90", "s91", "s92", "s93", "s94", "s95", "s96", "s97", "s98", "s99", "vcc", "scc", "memory");
    }
    __builtin_unreachable();
}

__global__ __launch_bounds__(256, 1) void mega_kernel(Params p) {
    extern __shared__ __attribute__((aligned(16))) char smem[];
    const rsrc_t rs = __builtin_amdgcn_make_buffer_rsrc(p.ws, 0, WS_USED, 0x00020000);
    const unsigned xb = blockIdx.x & 7;
    const int x4 = xb >> 2, pr = role_tab[(xb & 3) * 32 + (blockIdx.x >> 3)], k = pr >> 2, grp = pr & 3, x = x4 * 4 + grp;
    const int half = my_half();
    if (x4 == 0 && threadIdx.x == 0) st4(rs, (G_PAIR + pr) * 128, half);
    if (half == 1 && k < 28 && !(p.tune2 >> 60)) {
        const unsigned first = (pr * 4 + rfl(threadIdx.x >> 6)) * 1024;
        warm(p.x, 2359296, first); warm(p.Wq, 4718592, first); warm(p.Wk, 4718592, first); warm(p.Wv, 4718592, first); warm(p.Wo, 4718592, first);
        warm(p.w1, 5308416, first); warm(p.w3, 5308416, first); warm(p.w2, 5308416, first);
        DRAIN();
        BAR();
    }
    auto dl = [&](int r) -> int { return (int)(p.tune2 >> (6 * (r + 5 * half))) & 63; };
    if (k < 8) role_chain(p, grp * 8 + k, x4 | dl(0) << 8, pr, smem);
    else if (k < 16) role_s1(p, grp * 8 + k - 8, x4, pr, dl(1));
    else if (k < 22) role_f1(p, grp, k - 16, x4, pr, dl(3));
    else if (k < 25) role_k<true>(p, grp, k - 22, x4, pr, dl(2));
    else if (k < 28) role_k<false>(p, grp, k - 25, x4, pr, dl(4));
    else if (k >= 28) {
        startup(rs, ((k - 28) * 8 + x) * 4 + rfl(threadIdx.x >> 6));
        if (k < 30) role_attn(p, grp * 8 + (k - 28) * 4, x4, pr);
        else if (k == 30) role_red<false>(p, grp, x4, pr);
        else role_red<true>(p, grp, x4, pr);
    }
}

}

extern "C" void kernel_launch(void* const* d_in, const int* in_sizes, int n_in, void* d_out, int out_size,
                              void* d_ws, size_t ws_size, hipStream_t stream) {
    if (ws_size < (size_t)WS_USED + 2 * PAGE) return;
    Params p;
    p.x = (const u16*)d_in[0]; p.mem0 = (const u16*)d_in[1];
    p.Wq = (const u16*)d_in[2]; p.Wk = (const u16*)d_in[3]; p.Wv = (const u16*)d_in[4]; p.Wo = (const u16*)d_in[5];
    p.w1 = (const u16*)d_in[8]; p.w3 = (const u16*)d_in[9]; p.w2 = (const u16*)d_in[10];
    p.out = (u16*)d_out;
    p.ws = (char*)d_ws + ((0 - (size_t)d_ws) & (PAGE - 1));
    p.tune0 = (u64)D1 | (u64)D2 << 32; p.tune1 = (u64)D3 | (u64)D4 << 32;
    p.tune2 = (u64)DL0 | (u64)DL1 << 30;
    (void)hipMemsetAsync(p.ws, 0xFF, FLAG_BYTES, stream);
    (void)hipFuncSetAttribute((const void*)mega_kernel, hipFuncAttributeMaxDynamicSharedMemorySize, LDS_BYTES);
    mega_kernel<<<256, 256, LDS_BYTES, stream>>>(p);
}
```

```cpp
#include <hip/hip_runtime.h>
#include <stdint.h>

typedef unsigned short u16;
typedef unsigned long long u64;
typedef unsigned v4u __attribute__((ext_vector_type(4)));
typedef unsigned v2u __attribute__((ext_vector_type(2)));
typedef short bf16x8 __attribute__((ext_vector_type(8)));
typedef float f32x4 __attribute__((ext_vector_type(4)));
typedef __bf16 bf2_t __attribute__((ext_vector_type(2)));
typedef __amdgpu_buffer_rsrc_t rsrc_t;

#define XF2L 0
#define XF2S 0
#define XF2M 24
#define XFN 1
#define RSLEEP 1
namespace {

constexpr unsigned A_K = 262144u;
constexpr unsigned A_KM = 786432u;
constexpr unsigned A_PROBE = 1044480u;
constexpr unsigned A_Q = 0u;
constexpr unsigned A_ST = 1036288u;
constexpr unsigned A_V = 524288u;
constexpr unsigned A_X1 = 999424u;
constexpr unsigned A_XN = 917504u;
constexpr unsigned A_XR = 958464u;
constexpr unsigned B_H = 0u;
constexpr unsigned B_P3 = 294912u;
constexpr unsigned B_XN1 = 442368u;
constexpr unsigned C_P5 = 0u;
constexpr unsigned DBG = 236978176u;
constexpr unsigned DBG2 = 238026752u;
constexpr unsigned EXB = 294912u;
constexpr unsigned EXL = 270336u;
constexpr unsigned F_AT = 832u;
constexpr unsigned F_CH = 704u;
constexpr unsigned F_F1 = 960u;
constexpr unsigned F_F2 = 1600u;
constexpr unsigned F_LINES = 1856u;
constexpr unsigned F_OP = 1344u;
constexpr unsigned F_R3 = 256u;
constexpr unsigned F_S1 = 448u;
constexpr unsigned F_XN = 0u;
constexpr unsigned G_ABORT = 384u;
constexpr unsigned G_EPOCH = 385u;
constexpr unsigned G_LINES = 512u;
constexpr unsigned G_PG = 256u;
constexpr unsigned G_ROT = 0u;
constexpr unsigned K1 = 1024u;
constexpr unsigned LF0 = 32768u;
constexpr unsigned MEMB = 40960u;
constexpr unsigned MEML = 565248u;
constexpr unsigned NPAGES = 113u;
constexpr unsigned OOB = 2147483648u;
constexpr unsigned PAGE = 2097152u;
constexpr unsigned SPIN_CAP = 1048576u;
constexpr unsigned WS_USED = 236978176u;
constexpr unsigned CH_WB0 = 0u;
constexpr unsigned CH_WB1 = 40960u;
constexpr unsigned CH_WB2 = 81920u;
constexpr unsigned CH_WB3 = 116736u;
constexpr unsigned CH_IMG = 151552u;
constexpr unsigned CH_SO = 156672u;
constexpr unsigned CH_SELT = 160768u;
constexpr unsigned NPROBE = 6u;
constexpr unsigned NCOPY = 4u;
constexpr unsigned DL0 = 407162880u;
constexpr unsigned DL1 = 170434560u;
constexpr unsigned D1 = 672923692u;
constexpr unsigned D2 = 1144258560u;
constexpr unsigned D3 = 0u;
constexpr unsigned D4 = 269352u;
constexpr int LDS_BYTES = 160 * 1024;
constexpr unsigned spread(unsigned o) { return (o >> 13) * 16384u + (o & 8191u); }

__device__ const unsigned char role_tab[128] = {0, 4, 8, 12, 16, 20, 24, 28, 1, 5, 9, 13, 17, 21, 25, 29, 2, 6, 10, 14, 18, 22, 26, 30, 3, 7, 11, 15, 19, 23, 27, 31, 32, 36, 40, 44, 48, 52, 56, 60, 33, 37, 64, 68, 72, 76, 80, 84, 65, 69, 88, 92, 96, 89, 93, 97, 100, 104, 108, 112, 116, 113, 120, 124, 41, 45, 49, 53, 57, 61, 34, 38, 42, 46, 73, 77, 81, 85, 66, 70, 74, 78, 90, 94, 98, 101, 105, 109, 102, 106, 110, 117, 114, 121, 125, 122, 50, 54, 58, 62, 35, 39, 43, 47, 51, 55, 59, 63, 82, 86, 67, 71, 75, 79, 83, 87, 91, 95, 99, 103, 107, 111, 118, 115, 119, 126, 123, 127};

struct Params {
    const u16* x; const u16* mem0; const u16* Wq; const u16* Wk; const u16* Wv; const u16* Wo;
    const u16* w1; const u16* w3; const u16* w2; u16* out; char* ws;
    u64 tune0, tune1, tune2;
};

__device__ __forceinline__ v4u ld16(rsrc_t rs, unsigned voff, unsigned soff) { return __builtin_amdgcn_raw_buffer_load_b128(rs, voff, soff, 16); }
__device__ __forceinline__ int ld4(rsrc_t rs, unsigned off) { return (int)__builtin_amdgcn_raw_buffer_load_b32(rs, off, 0, 16); }
__device__ __forceinline__ void st16(rsrc_t rs, unsigned voff, unsigned soff, v4u v) { __builtin_amdgcn_raw_buffer_store_b128(v, rs, voff, soff, 16); }
__device__ __forceinline__ void st4(rsrc_t rs, unsigned off, int v) { __builtin_amdgcn_raw_buffer_store_b32((unsigned)v, rs, off, 0, 16); }
#define DRAIN() asm volatile("s_waitcnt vmcnt(0)" ::: "memory")
#define BAR() do { asm volatile("s_waitcnt lgkmcnt(0)" ::: "memory"); __builtin_amdgcn_s_barrier(); asm volatile("" ::: "memory"); } while (0)
__device__ __forceinline__ int rfl(int v) { return __builtin_amdgcn_readfirstlane(v); }
__device__ __forceinline__ u64 pair(unsigned lo, unsigned hi) { return (u64)lo | (u64)hi << 32; }
__device__ __forceinline__ unsigned my_xcc() {
    unsigned id;
    asm volatile("s_getreg_b32 %0, hwreg(HW_REG_XCC_ID)" : "=s"(id));
    return id & 7;
}

__device__ __forceinline__ void startup(rsrc_t rs, int hw) {
    const int lane = threadIdx.x & 63;
    const v4u ones = {~0u, ~0u, ~0u, ~0u};
    const unsigned o = (unsigned)(hw < 96 ? hw + 1 : (hw == 96 ? 0 : hw)) * PAGE + spread(A_PROBE);
    unsigned best[2] = {~0u, ~0u};
    if (hw < NPAGES) {
#pragma unroll 1
        for (int r = 0; r < 2 * NPROBE; ++r) {
            const unsigned t0 = (unsigned)__builtin_amdgcn_s_memtime();
            st16(rs, lane < 8 ? lane * 16 : OOB, o + (r & 1) * 8192, ones);
            DRAIN();
            const unsigned t = (unsigned)__builtin_amdgcn_s_memtime() - t0;
            if (r & 1) best[1] = min(best[1], t); else best[0] = min(best[0], t);
        }
    }
#pragma unroll
    for (int q = 0; q < 11; ++q) st16(rs, lane * 16, 2 * LF0 + (hw * 11 + q) * 1024, ones);
    static_assert(2 * LF0 + 128 * 11 * 1024 >= 2 * (MEML + 4 * MEMB) && 2 * LF0 + 128 * 11 * 1024 <= 2 * A_PROBE, "flags and exchange area");
#pragma unroll
    for (int q = 0; q < 4; ++q) st16(rs, lane * 16, (unsigned)(1 + (hw >> 1)) * PAGE + spread(A_ST + q * 1024) + (hw & 1) * 8192, ones);
    DRAIN();
    if (lane == 0) st4(rs, (G_PG + hw) * 128, (best[1] < best[0] ? 1 : 0) ^ (my_xcc() >> 2));
}

__device__ __forceinline__ void warm(const u16* W, unsigned bytes, unsigned first) {
    const rsrc_t r = __builtin_amdgcn_make_buffer_rsrc(const_cast<u16*>(W), 0, bytes, 0x00020000);
    const unsigned vo = (threadIdx.x & 63) * 16;
#pragma unroll 1
    for (unsigned o = first; o < bytes; o += 448 * 1024)
        asm volatile("s_mov_b32 m0, 0\n\ts_nop 0\n\tbuffer_load_dwordx4 %0, %1, %2 offen lds\n\ts_waitcnt vmcnt(40)" :: "v"(vo), "s"(r), "s"(o) : "memory");
}

__device__ __forceinline__ void role_chain(const Params& p, int h, int x4, int pr, char* smem) {
    const unsigned tid = threadIdx.x, lane = tid & 63, c = lane & 15, g = lane >> 4;
    const int w = rfl(tid >> 6);
    const unsigned wb = w == 0 ? CH_WB0 : (w == 1 ? CH_WB1 : (w == 2 ? CH_WB2 : CH_WB3));
    const unsigned slot = (g * 8 + (c & 7)) * 16;
    const unsigned spr = CH_IMG + (c >> 3) * 512 + slot;
    const unsigned gam = lane >> 4, nh = (lane >> 3) & 1, ksl = (lane >> 2) & 1, tau = ((lane ^ gam) >> 1) & 1, rho = lane & 1;
    auto img = [&](unsigned r) -> unsigned { return (2 * (r & 3) + (g & 1)) * 128 + (((g ^ r) & 2) + (r >> 2)) * 16; };
    if (w >= 2) {
        v4u sel;
#pragma unroll
        for (int e = 0; e < 4; ++e) sel[e] = ((c & 7) == 2 * e) ? 0x3f80u : (((c & 7) == 2 * e + 1) ? 0x3f800000u : 0u);
        const unsigned g3 = ksl * 512 + (2 * (gam & 1) + nh) * 128 + (4 * (gam >> 1) + 2 * tau + rho) * 16;
        *reinterpret_cast<v4u*>(smem + wb + 32768 + lane * 16) = sel;
        *reinterpret_cast<v4u*>(smem + wb + 32768 + 1024 + lane * 16) =
            v4u{lane < 8 * NCOPY ? (lane >> 3) * 1024 + (lane & 7) * 16 : OOB, g3, lane < 8 * NCOPY ? (lane >> 3) * 16384 + ((lane & 7) ^ ((h >> 1) & 2)) * 16 : OOB, 0u};
    } else if (w == 0) {
        const bool onq = g == (c & 3), upq = (c & 4) != 0, onk = g == (c >> 2) && (c & 3) < 2, upk = (c & 1) != 0;
        *reinterpret_cast<v4u*>(smem + spr + CH_SELT - CH_IMG) = v4u{onq ? (upq ? 0x3f800000u : 0x3f80u) : 0u, onq ? (upq ? 0x3f80u : 0x3f800000u) : 0u,
                                                                     onk ? (upk ? 0x3f800000u : 0x3f80u) : 0u, onk ? (upk ? 0x3f80u : 0x3f800000u) : 0u};
    } else {
        const unsigned pub = (lane >> 4) * 16384 + ((lane >> 3) & 1) * 4096 + (lane & 7) * 16;
        const unsigned kmo = (c & 3) < 2 ? (g * 8 + 2 * (c >> 2) + (c & 3)) * 16 : OOB;
        const unsigned vmo = (g & 1) ? OOB : (g >> 1) * 256 + c * 16;
        *reinterpret_cast<v4u*>(smem + spr + CH_SELT - CH_IMG + 1024) = v4u{kmo, vmo, pub, CH_SO + img(c & 7) + (c >> 3) * 8};
    }
    if (w == 0) *reinterpret_cast<unsigned*>(smem + spr + CH_SELT - CH_IMG + 2048) = ~0u;
    BAR();
    const unsigned idx = (lane & 2) ? 0xeeeeu : 0x4444u;
    const unsigned va = CH_IMG + img((c >> 2) + 4 * ((((c & 3) + 1) >> 1) & 1));
    const unsigned pp = lane >> 2, row = (pp & 9) | ((pp & 2) << 1) | ((pp & 4) >> 1);
    const unsigned qo = row * 8192 + (128 * h + 32 * w) * 2 + (lane & 3) * 16;
    const unsigned oo = (128 * h + 16 * ((row >> 2) & 1) + 4 * (row >> 3) + (row & 3)) * 1152 + w * 128 + (lane & 3) * 16;
    const unsigned q = c >> 2;
    const unsigned tr = wb + ((q & 1) + 2 * (g & 1) + 8 * (g >> 1)) * 64 + (lane & 3) * 8;
    const unsigned m0 = (gam + 4 * rho) * 1152 + (4 * ksl + 2 * tau + nh) * 16;
    asm volatile(
        "s_nop 4\n"
        "s_mov_b64 s[40:41], %0\n"
        "s_and_b32 s41, s41, 0xffff\n"
        "s_mov_b32 s42, 236978176\n"
        "s_mov_b32 s43, 0x20000\n"
        "s_mov_b64 s[48:49], %6\n"
        "s_mov_b64 s[56:57], %7\n"
        "s_mov_b64 s[60:61], %8\n"
        "s_mov_b64 s[62:63], %9\n"
        "v_mov_b64 v[0:1], %10\n"
        "v_mov_b64 v[2:3], %11\n"
        "v_mov_b64 v[4:5], %12\n"
        "v_mov_b64 v[6:7], %13\n"
        "v_mov_b64 v[8:9], %14\n"
        "v_mov_b64 v[10:11], %15\n"
        "s_mov_b32 s47, 0x20000\n"
        "v_readfirstlane_b32 s82, v2\n"
        "s_lshr_b32 s65, s56, 8\n"
        "s_and_b32 s56, s56, 1\n"
        ".Lch_start_%=:\n"
        "s_sub_u32 s65, s65, 1\n"
        "s_cbranch_scc1 .Lch_start2_%=\n"
        "s_sleep 16\n"
        "s_branch .Lch_start_%=\n"
        ".Lch_start2_%=:\n"
        "s_mov_b64 s[44:45], %5\n"
        "s_and_b32 s45, s45, 0xffff\n"
        "s_mov_b32 s46, 9216\n"
        "s_lshl_b32 s65, s48, 10\n"
        "s_add_u32 m0, s65, 151552\n"
        "s_lshl_b32 s65, s48, 7\n"
        "s_add_u32 s65, s65, 0\n"
        "buffer_load_dwordx4 v9, s[44:47], s65 offen lds\n"
        "s_lshl_b32 s65, s48, 10\n"
        "s_add_u32 m0, s65, 155648\n"
        "s_lshl_b32 s65, s48, 7\n"
        "s_add_u32 s65, s65, 512\n"
        "buffer_load_dwordx4 v9, s[44:47], s65 offen lds\n"
        "s_mov_b32 m0, 159744\n"
        "s_mov_b32 s65, 1024\n"
        "buffer_load_dwordx4 v9, s[44:47], s65 offen lds\n"
        "s_mov_b64 s[44:45], %1\n"
        "s_and_b32 s45, s45, 0xffff\n"
        "s_mov_b32 s46, 4718592\n"
        "s_add_u32 m0, s82, 0\n"
        "s_mov_b32 s65, 0\n"
        "buffer_load_dwordx4 v6, s[44:47], s65 offen lds\n"
        "s_mov_b32 s65, 130048\n"
        "buffer_load_dwordx4 v6, s[44:47], s65 offen offset:1024 lds\n"
        "s_add_u32 m0, s82, 2048\n"
        "s_mov_b32 s65, 262144\n"
        "buffer_load_dwordx4 v6, s[44:47], s65 offen lds\n"
        "s_mov_b32 s65, 392192\n"
        "buffer_load_dwordx4 v6, s[44:47], s65 offen offset:1024 lds\n"
        "s_add_u32 m0, s82, 4096\n"
        "s_mov_b32 s65, 524288\n"
        "buffer_load_dwordx4 v6, s[44:47], s65 offen lds\n"
        "s_mov_b32 s65, 654336\n"
        "buffer_load_dwordx4 v6, s[44:47], s65 offen offset:1024 lds\n"
        "s_add_u32 m0, s82, 6144\n"
        "s_mov_b32 s65, 786432\n"
        "buffer_load_dwordx4 v6, s[44:47], s65 offen lds\n"
        "s_mov_b32 s65, 916480\n"
        "buffer_load_dwordx4 v6, s[44:47], s65 offen offset:1024 lds\n"
        "s_add_u32 m0, s82, 8192\n"
        "s_mov_b32 s65, 1048576\n"
        "buffer_load_dwordx4 v6, s[44:47], s65 offen lds\n"
        "s_mov_b32 s65, 1178624\n"
        "buffer_load_dwordx4 v6, s[44:47], s65 offen offset:1024 lds\n"
        "s_add_u32 m0, s82, 10240\n"
        "s_mov_b32 s65, 1310720\n"
        "buffer_load_dwordx4 v6, s[44:47], s65 offen lds\n"
        "s_mov_b32 s65, 1440768\n"
        "buffer_load_dwordx4 v6, s[44:47], s65 offen offset:1024 lds\n"
        "s_add_u32 m0, s82, 12288\n"
        "s_mov_b32 s65, 1572864\n"
        "buffer_load_dwordx4 v6, s[44:47], s65 offen lds\n"
        "s_mov_b32 s65, 1702912\n"
        "buffer_load_dwordx4 v6, s[44:47], s65 offen offset:1024 lds\n"
        "s_add_u32 m0, s82, 14336\n"
        "s_mov_b32 s65, 1835008\n"
        "buffer_load_dwordx4 v6, s[44:47], s65 offen lds\n"
        "s_mov_b32 s65, 1965056\n"
        "buffer_load_dwordx4 v6, s[44:47], s65 offen offset:1024 lds\n"
        "s_add_u32 m0, s82, 16384\n"
        "s_mov_b32 s65, 2097152\n"
        "buffer_load_dwordx4 v6, s[44:47], s65 offen lds\n"
        "s_mov_b32 s65, 2227200\n"
        "buffer_load_dwordx4 v6, s[44:47], s65 offen offset:1024 lds\n"
        "s_add_u32 m0, s82, 18432\n"
        "s_mov_b32 s65, 2359296\n"
        "buffer_load_dwordx4 v6, s[44:47], s65 offen lds\n"
        "s_mov_b32 s65, 2489344\n"
        "buffer_load_dwordx4 v6, s[44:47], s65 offen offset:1024 lds\n"
        "s_add_u32 m0, s82, 20480\n"
        "s_mov_b32 s65, 2621440\n"
        "buffer_load_dwordx4 v6, s[44:47], s65 offen lds\n"
        "s_mov_b32 s65, 2751488\n"
        "buffer_load_dwordx4 v6, s[44:47], s65 offen offset:1024 lds\n"
        "s_add_u32 m0, s82, 22528\n"
        "s_mov_b32 s65, 2883584\n"
        "buffer_load_dwordx4 v6, s[44:47], s65 offen lds\n"
        "s_mov_b32 s65, 3013632\n"
        "buffer_load_dwordx4 v6, s[44:47], s65 offen offset:1024 lds\n"
        "s_add_u32 m0, s82, 24576\n"
        "s_mov_b32 s65, 3145728\n"
        "buffer_load_dwordx4 v6, s[44:47], s65 offen lds\n"
        "s_mov_b32 s65, 3275776\n"
        "buffer_load_dwordx4 v6, s[44:47], s65 offen offset:1024 lds\n"
        "s_add_u32 m0, s82, 26624\n"
        "s_mov_b32 s65, 3407872\n"
        "buffer_load_dwordx4 v6, s[44:47], s65 offen lds\n"
        "s_mov_b32 s65, 3537920\n"
        "buffer_load_dwordx4 v6, s[44:47], s65 offen offset:1024 lds\n"
        "s_add_u32 m0, s82, 28672\n"
        "s_mov_b32 s65, 3670016\n"
        "buffer_load_dwordx4 v6, s[44:47], s65 offen lds\n"
        "s_mov_b32 s65, 3800064\n"
        "buffer_load_dwordx4 v6, s[44:47], s65 offen offset:1024 lds\n"
        "s_add_u32 m0, s82, 30720\n"
        "s_mov_b32 s65, 3932160\n"
        "buffer_load_dwordx4 v6, s[44:47], s65 offen lds\n"
        "s_mov_b32 s65, 4062208\n"
        "buffer_load_dwordx4 v6, s[44:47], s65 offen offset:1024 lds\n"
        "s_waitcnt vmcnt(28)\n"
        "ds_read_b64_tr_b16 a[0:1], v10 offset:0\n"
        "ds_read_b64_tr_b16 a[2:3], v10 offset:256\n"
        "ds_read_b64_tr_b16 a[4:5], v10 offset:1024\n"
        "ds_read_b64_tr_b16 a[6:7], v10 offset:1280\n"
        "ds_read_b64_tr_b16 a[8:9], v10 offset:32\n"
        "ds_read_b64_tr_b16 a[10:11], v10 offset:288\n"
        "ds_read_b64_tr_b16 a[12:13], v10 offset:1056\n"
        "ds_read_b64_tr_b16 a[14:15], v10 offset:1312\n"
        "s_waitcnt vmcnt(24)\n"
        "ds_read_b64_tr_b16 a[16:17], v10 offset:4096\n"
        "ds_read_b64_tr_b16 a[18:19], v10 offset:4352\n"
        "ds_read_b64_tr_b16 a[20:21], v10 offset:5120\n"
        "ds_read_b64_tr_b16 a[22:23], v10 offset:5376\n"
        "ds_read_b64_tr_b16 a[24:25], v10 offset:4128\n"
        "ds_read_b64_tr_b16 a[26:27], v10 offset:4384\n"
        "ds_read_b64_tr_b16 a[28:29], v10 offset:5152\n"
        "ds_read_b64_tr_b16 a[30:31], v10 offset:5408\n"
        "s_waitcnt lgkmcnt(8)\n"
        "s_add_u32 m0, s82, 0\n"
        "s_mov_b32 s65, 4194304\n"
        "buffer_load_dwordx4 v6, s[44:47], s65 offen lds\n"
        "s_mov_b32 s65, 4324352\n"
        "buffer_load_dwordx4 v6, s[44:47], s65 offen offset:1024 lds\n"
        "s_add_u32 m0, s82, 2048\n"
        "s_mov_b32 s65, 4456448\n"
        "buffer_load_dwordx4 v6, s[44:47], s65 offen lds\n"
        "s_mov_b32 s65, 4586496\n"
        "buffer_load_dwordx4 v6, s[44:47], s65 offen offset:1024 lds\n"
        "s_waitcnt vmcnt(24)\n"
        "ds_read_b64_tr_b16 a[32:33], v10 offset:8192\n"
        "ds_read_b64_tr_b16 a[34:35], v10 offset:8448\n"
        "ds_read_b64_tr_b16 a[36:37], v10 offset:9216\n"
        "ds_read_b64_tr_b16 a[38:39], v10 offset:9472\n"
        "ds_read_b64_tr_b16 a[40:41], v10 offset:8224\n"
        "ds_read_b64_tr_b16 a[42:43], v10 offset:8480\n"
        "ds_read_b64_tr_b16 a[44:45], v10 offset:9248\n"
        "ds_read_b64_tr_b16 a[46:47], v10 offset:9504\n"
        "s_waitcnt lgkmcnt(8)\n"
        "s_mov_b64 s[44:45], %2\n"
        "s_and_b32 s45, s45, 0xffff\n"
        "s_mov_b32 s46, 4718592\n"
        "s_add_u32 m0, s82, 4096\n"
        "s_mov_b32 s65, 0\n"
        "buffer_load_dwordx4 v6, s[44:47], s65 offen lds\n"
        "s_mov_b32 s65, 130048\n"
        "buffer_load_dwordx4 v6, s[44:47], s65 offen offset:1024 lds\n"
        "s_add_u32 m0, s82, 6144\n"
        "s_mov_b32 s65, 262144\n"
        "buffer_load_dwordx4 v6, s[44:47], s65 offen lds\n"
        "s_mov_b32 s65, 392192\n"
        "buffer_load_dwordx4 v6, s[44:47], s65 offen offset:1024 lds\n"
        "s_waitcnt vmcnt(24)\n"
        "ds_read_b64_tr_b16 a[48:49], v10 offset:12288\n"
        "ds_read_b64_tr_b16 a[50:51], v10 offset:12544\n"
        "ds_read_b64_tr_b16 a[52:53], v10 offset:13312\n"
        "ds_read_b64_tr_b16 a[54:55], v10 offset:13568\n"
        "ds_read_b64_tr_b16 a[56:57], v10 offset:12320\n"
        "ds_read_b64_tr_b16 a[58:59], v10 offset:12576\n"
        "ds_read_b64_tr_b16 a[60:61], v10 offset:13344\n"
        "ds_read_b64_tr_b16 a[62:63], v10 offset:13600\n"
        "s_waitcnt lgkmcnt(8)\n"
        "s_add_u32 m0, s82, 8192\n"
        "s_mov_b32 s65, 524288\n"
        "buffer_load_dwordx4 v6, s[44:47], s65 offen lds\n"
        "s_mov_b32 s65, 654336\n"
        "buffer_load_dwordx4 v6, s[44:47], s65 offen offset:1024 lds\n"
        "s_add_u32 m0, s82, 10240\n"
        "s_mov_b32 s65, 786432\n"
        "buffer_load_dwordx4 v6, s[44:47], s65 offen lds\n"
        "s_mov_b32 s65, 916480\n"
        "buffer_load_dwordx4 v6, s[44:47], s65 offen offset:1024 lds\n"
        "s_waitcnt vmcnt(24)\n"
        "ds_read_b64_tr_b16 a[64:65], v10 offset:16384\n"
        "ds_read_b64_tr_b16 a[66:67], v10 offset:16640\n"
        "ds_read_b64_tr_b16 a[68:69], v10 offset:17408\n"
        "ds_read_b64_tr_b16 a[70:71], v10 offset:17664\n"
        "ds_read_b64_tr_b16 a[72:73], v10 offset:16416\n"
        "ds_read_b64_tr_b16 a[74:75], v10 offset:16672\n"
        "ds_read_b64_tr_b16 a[76:77], v10 offset:17440\n"
        "ds_read_b64_tr_b16 a[78:79], v10 offset:17696\n"
        "s_waitcnt lgkmcnt(8)\n"
        "s_add_u32 m0, s82, 12288\n"
        "s_mov_b32 s65, 1048576\n"
        "buffer_load_dwordx4 v6, s[44:47], s65 offen lds\n"
        "s_mov_b32 s65, 1178624\n"
        "buffer_load_dwordx4 v6, s[44:47], s65 offen offset:1024 lds\n"
        "s_add_u32 m0, s82, 14336\n"
        "s_mov_b32 s65, 1310720\n"
        "buffer_load_dwordx4 v6, s[44:47], s65 offen lds\n"
        "s_mov_b32 s65, 1440768\n"
        "buffer_load_dwordx4 v6, s[44:47], s65 offen offset:1024 lds\n"
        "s_waitcnt vmcnt(24)\n"
        "ds_read_b64_tr_b16 a[80:81], v10 offset:20480\n"
        "ds_read_b64_tr_b16 a[82:83], v10 offset:20736\n"
        "ds_read_b64_tr_b16 a[84:85], v10 offset:21504\n"
        "ds_read_b64_tr_b16 a[86:87], v10 offset:21760\n"
        "ds_read_b64_tr_b16 a[88:89], v10 offset:20512\n"
        "ds_read_b64_tr_b16 a[90:91], v10 offset:20768\n"
        "ds_read_b64_tr_b16 a[92:93], v10 offset:21536\n"
        "ds_read_b64_tr_b16 a[94:95], v10 offset:21792\n"
        "s_waitcnt lgkmcnt(8)\n"
        "s_add_u32 m0, s82, 16384\n"
        "s_mov_b32 s65, 1572864\n"
        "buffer_load_dwordx4 v6, s[44:47], s65 offen lds\n"
        "s_mov_b32 s65, 1702912\n"
        "buffer_load_dwordx4 v6, s[44:47], s65 offen offset:1024 lds\n"
        "s_add_u32 m0, s82, 18432\n"
        "s_mov_b32 s65, 1835008\n"
        "buffer_load_dwordx4 v6, s[44:47], s65 offen lds\n"
        "s_mov_b32 s65, 1965056\n"
        "buffer_load_dwordx4 v6, s[44:47], s65 offen offset:1024 lds\n"
        "s_waitcnt vmcnt(24)\n"
        "ds_read_b64_tr_b16 a[96:97], v10 offset:24576\n"
        "ds_read_b64_tr_b16 a[98:99], v10 offset:24832\n"
        "ds_read_b64_tr_b16 a[100:101], v10 offset:25600\n"
        "ds_read_b64_tr_b16 a[102:103], v10 offset:25856\n"
        "ds_read_b64_tr_b16 a[104:105], v10 offset:24608\n"
        "ds_read_b64_tr_b16 a[106:107], v10 offset:24864\n"
        "ds_read_b64_tr_b16 a[108:109], v10 offset:25632\n"
        "ds_read_b64_tr_b16 a[110:111], v10 offset:25888\n"
        "s_waitcnt lgkmcnt(8)\n"
        "s_add_u32 m0, s82, 20480\n"
        "s_mov_b32 s65, 2097152\n"
        "buffer_load_dwordx4 v6, s[44:47], s65 offen lds\n"
        "s_mov_b32 s65, 2227200\n"
        "buffer_load_dwordx4 v6, s[44:47], s65 offen offset:1024 lds\n"
        "s_add_u32 m0, s82, 22528\n"
        "s_mov_b32 s65, 2359296\n"
        "buffer_load_dwordx4 v6, s[44:47], s65 offen lds\n"
        "s_mov_b32 s65, 2489344\n"
        "buffer_load_dwordx4 v6, s[44:47], s65 offen offset:1024 lds\n"
        "s_waitcnt vmcnt(24)\n"
        "ds_read_b64_tr_b16 a[112:113], v10 offset:28672\n"
        "ds_read_b64_tr_b16 a[114:115], v10 offset:28928\n"
        "ds_read_b64_tr_b16 a[116:117], v10 offset:29696\n"
        "ds_read_b64_tr_b16 a[118:119], v10 offset:29952\n"
        "ds_read_b64_tr_b16 a[120:121], v10 offset:28704\n"
        "ds_read_b64_tr_b16 a[122:123], v10 offset:28960\n"
        "ds_read_b64_tr_b16 a[124:125], v10 offset:29728\n"
        "ds_read_b64_tr_b16 a[126:127], v10 offset:29984\n"
        "s_waitcnt lgkmcnt(8)\n"
        "s_add_u32 m0, s82, 24576\n"
        "s_mov_b32 s65, 2621440\n"
        "buffer_load_dwordx4 v6, s[44:47], s65 offen lds\n"
        "s_mov_b32 s65, 2751488\n"
        "buffer_load_dwordx4 v6, s[44:47], s65 offen offset:1024 lds\n"
        "s_add_u32 m0, s82, 26624\n"
        "s_mov_b32 s65, 2883584\n"
        "buffer_load_dwordx4 v6, s[44:47], s65 offen lds\n"
        "s_mov_b32 s65, 3013632\n"
        "buffer_load_dwordx4 v6, s[44:47], s65 offen offset:1024 lds\n"
        "s_waitcnt vmcnt(24)\n"
        "ds_read_b64_tr_b16 a[128:129], v10 offset:0\n"
        "ds_read_b64_tr_b16 a[130:131], v10 offset:256\n"
        "ds_read_b64_tr_b16 a[132:133], v10 offset:1024\n"
        "ds_read_b64_tr_b16 a[134:135], v10 offset:1280\n"
        "ds_read_b64_tr_b16 a[136:137], v10 offset:32\n"
        "ds_read_b64_tr_b16 a[138:139], v10 offset:288\n"
        "ds_read_b64_tr_b16 a[140:141], v10 offset:1056\n"
        "ds_read_b64_tr_b16 a[142:143], v10 offset:1312\n"
        "s_waitcnt lgkmcnt(8)\n"
        "s_add_u32 m0, s82, 28672\n"
        "s_mov_b32 s65, 3145728\n"
        "buffer_load_dwordx4 v6, s[44:47], s65 offen lds\n"
        "s_mov_b32 s65, 3275776\n"
        "buffer_load_dwordx4 v6, s[44:47], s65 offen offset:1024 lds\n"
        "s_add_u32 m0, s82, 30720\n"
        "s_mov_b32 s65, 3407872\n"
        "buffer_load_dwordx4 v6, s[44:47], s65 offen lds\n"
        "s_mov_b32 s65, 3537920\n"
        "buffer_load_dwordx4 v6, s[44:47], s65 offen offset:1024 lds\n"
        "s_waitcnt vmcnt(24)\n"
        "ds_read_b64_tr_b16 a[144:145], v10 offset:4096\n"
        "ds_read_b64_tr_b16 a[146:147], v10 offset:4352\n"
        "ds_read_b64_tr_b16 a[148:149], v10 offset:5120\n"
        "ds_read_b64_tr_b16 a[150:151], v10 offset:5376\n"
        "ds_read_b64_tr_b16 a[152:153], v10 offset:4128\n"
        "ds_read_b64_tr_b16 a[154:155], v10 offset:4384\n"
        "ds_read_b64_tr_b16 a[156:157], v10 offset:5152\n"
        "ds_read_b64_tr_b16 a[158:159], v10 offset:5408\n"
        "s_waitcnt lgkmcnt(8)\n"
        "s_add_u32 m0, s82, 0\n"
        "s_mov_b32 s65, 3670016\n"
        "buffer_load_dwordx4 v6, s[44:47], s65 offen lds\n"
        "s_mov_b32 s65, 3800064\n"
        "buffer_load_dwordx4 v6, s[44:47], s65 offen offset:1024 lds\n"
        "s_add_u32 m0, s82, 2048\n"
        "s_mov_b32 s65, 3932160\n"
        "buffer_load_dwordx4 v6, s[44:47], s65 offen lds\n"
        "s_mov_b32 s65, 4062208\n"
        "buffer_load_dwordx4 v6, s[44:47], s65 offen offset:1024 lds\n"
        "s_waitcnt vmcnt(24)\n"
        "ds_read_b64_tr_b16 a[160:161], v10 offset:8192\n"
        "ds_read_b64_tr_b16 a[162:163], v10 offset:8448\n"
        "ds_read_b64_tr_b16 a[164:165], v10 offset:9216\n"
        "ds_read_b64_tr_b16 a[166:167], v10 offset:9472\n"
        "ds_read_b64_tr_b16 a[168:169], v10 offset:8224\n"
        "ds_read_b64_tr_b16 a[170:171], v10 offset:8480\n"
        "ds_read_b64_tr_b16 a[172:173], v10 offset:9248\n"
        "ds_read_b64_tr_b16 a[174:175], v10 offset:9504\n"
        "s_waitcnt lgkmcnt(8)\n"
        "s_add_u32 m0, s82, 4096\n"
        "s_mov_b32 s65, 4194304\n"
        "buffer_load_dwordx4 v6, s[44:47], s65 offen lds\n"
        "s_mov_b32 s65, 4324352\n"
        "buffer_load_dwordx4 v6, s[44:47], s65 offen offset:1024 lds\n"
        "s_add_u32 m0, s82, 6144\n"
        "s_mov_b32 s65, 4456448\n"
        "buffer_load_dwordx4 v6, s[44:47], s65 offen lds\n"
        "s_mov_b32 s65, 4586496\n"
        "buffer_load_dwordx4 v6, s[44:47], s65 offen offset:1024 lds\n"
        "s_waitcnt vmcnt(24)\n"
        "ds_read_b64_tr_b16 a[176:177], v10 offset:12288\n"
        "ds_read_b64_tr_b16 a[178:179], v10 offset:12544\n"
        "ds_read_b64_tr_b16 a[180:181], v10 offset:13312\n"
        "ds_read_b64_tr_b16 a[182:183], v10 offset:13568\n"
        "ds_read_b64_tr_b16 a[184:185], v10 offset:12320\n"
        "ds_read_b64_tr_b16 a[186:187], v10 offset:12576\n"
        "ds_read_b64_tr_b16 a[188:189], v10 offset:13344\n"
        "ds_read_b64_tr_b16 a[190:191], v10 offset:13600\n"
        "s_waitcnt lgkmcnt(8)\n"
        "s_mov_b64 s[44:45], %4\n"
        "s_and_b32 s45, s45, 0xffff\n"
        "s_mov_b32 s46, 4718592\n"
        "s_add_u32 m0, s82, 8192\n"
        "s_mov_b32 s65, 0\n"
        "buffer_load_dwordx4 v7, s[44:47], s65 offen lds\n"
        "s_mov_b32 s65, 8192\n"
        "buffer_load_dwordx4 v7, s[44:47], s65 offen offset:1024 lds\n"
        "s_add_u32 m0, s82, 10240\n"
        "s_mov_b32 s65, 36864\n"
        "buffer_load_dwordx4 v7, s[44:47], s65 offen lds\n"
        "s_mov_b32 s65, 45056\n"
        "buffer_load_dwordx4 v7, s[44:47], s65 offen offset:1024 lds\n"
        "s_waitcnt vmcnt(24)\n"
        "ds_read_b64_tr_b16 a[192:193], v10 offset:16384\n"
        "ds_read_b64_tr_b16 a[194:195], v10 offset:16640\n"
        "ds_read_b64_tr_b16 a[196:197], v10 offset:17408\n"
        "ds_read_b64_tr_b16 a[198:199], v10 offset:17664\n"
        "ds_read_b64_tr_b16 a[200:201], v10 offset:16416\n"
        "ds_read_b64_tr_b16 a[202:203], v10 offset:16672\n"
        "ds_read_b64_tr_b16 a[204:205], v10 offset:17440\n"
        "ds_read_b64_tr_b16 a[206:207], v10 offset:17696\n"
        "s_waitcnt lgkmcnt(8)\n"
        "s_add_u32 m0, s82, 12288\n"
        "s_mov_b32 s65, 73728\n"
        "buffer_load_dwordx4 v7, s[44:47], s65 offen lds\n"
        "s_mov_b32 s65, 81920\n"
        "buffer_load_dwordx4 v7, s[44:47], s65 offen offset:1024 lds\n"
        "s_add_u32 m0, s82, 14336\n"
        "s_mov_b32 s65, 110592\n"
        "buffer_load_dwordx4 v7, s[44:47], s65 offen lds\n"
        "s_mov_b32 s65, 118784\n"
        "buffer_load_dwordx4 v7, s[44:47], s65 offen offset:1024 lds\n"
        "s_waitcnt vmcnt(24)\n"
        "ds_read_b64_tr_b16 a[208:209], v10 offset:20480\n"
        "ds_read_b64_tr_b16 a[210:211], v10 offset:20736\n"
        "ds_read_b64_tr_b16 a[212:213], v10 offset:21504\n"
        "ds_read_b64_tr_b16 a[214:215], v10 offset:21760\n"
        "ds_read_b64_tr_b16 a[216:217], v10 offset:20512\n"
        "ds_read_b64_tr_b16 a[218:219], v10 offset:20768\n"
        "ds_read_b64_tr_b16 a[220:221], v10 offset:21536\n"
        "ds_read_b64_tr_b16 a[222:223], v10 offset:21792\n"
        "s_waitcnt lgkmcnt(8)\n"
        "s_add_u32 m0, s82, 16384\n"
        "s_mov_b32 s65, 64\n"
        "buffer_load_dwordx4 v7, s[44:47], s65 offen lds\n"
        "s_mov_b32 s65, 8256\n"
        "buffer_load_dwordx4 v7, s[44:47], s65 offen offset:1024 lds\n"
        "s_add_u32 m0, s82, 18432\n"
        "s_mov_b32 s65, 36928\n"
        "buffer_load_dwordx4 v7, s[44:47], s65 offen lds\n"
        "s_mov_b32 s65, 45120\n"
        "buffer_load_dwordx4 v7, s[44:47], s65 offen offset:1024 lds\n"
        "s_waitcnt vmcnt(24)\n"
        "ds_read_b64_tr_b16 a[224:225], v10 offset:24576\n"
        "ds_read_b64_tr_b16 a[226:227], v10 offset:24832\n"
        "ds_read_b64_tr_b16 a[228:229], v10 offset:25600\n"
        "ds_read_b64_tr_b16 a[230:231], v10 offset:25856\n"
        "ds_read_b64_tr_b16 a[232:233], v10 offset:24608\n"
        "ds_read_b64_tr_b16 a[234:235], v10 offset:24864\n"
        "ds_read_b64_tr_b16 a[236:237], v10 offset:25632\n"
        "ds_read_b64_tr_b16 a[238:239], v10 offset:25888\n"
        "s_waitcnt lgkmcnt(8)\n"
        "s_add_u32 m0, s82, 20480\n"
        "s_mov_b32 s65, 73792\n"
        "buffer_load_dwordx4 v7, s[44:47], s65 offen lds\n"
        "s_mov_b32 s65, 81984\n"
        "buffer_load_dwordx4 v7, s[44:47], s65 offen offset:1024 lds\n"
        "s_add_u32 m0, s82, 22528\n"
        "s_mov_b32 s65, 110656\n"
        "buffer_load_dwordx4 v7, s[44:47], s65 offen lds\n"
        "s_mov_b32 s65, 118848\n"
        "buffer_load_dwordx4 v7, s[44:47], s65 offen offset:1024 lds\n"
        "s_waitcnt vmcnt(24)\n"
        "ds_read_b64_tr_b16 a[240:241], v10 offset:28672\n"
        "ds_read_b64_tr_b16 a[242:243], v10 offset:28928\n"
        "ds_read_b64_tr_b16 a[244:245], v10 offset:29696\n"
        "ds_read_b64_tr_b16 a[246:247], v10 offset:29952\n"
        "ds_read_b64_tr_b16 a[248:249], v10 offset:28704\n"
        "ds_read_b64_tr_b16 a[250:251], v10 offset:28960\n"
        "ds_read_b64_tr_b16 a[252:253], v10 offset:29728\n"
        "ds_read_b64_tr_b16 a[254:255], v10 offset:29984\n"
        "s_waitcnt lgkmcnt(8)\n"
        "s_add_u32 m0, s82, 24576\n"
        "s_mov_b32 s65, 512\n"
        "buffer_load_dwordx4 v7, s[44:47], s65 offen lds\n"
        "s_mov_b32 s65, 8704\n"
        "buffer_load_dwordx4 v7, s[44:47], s65 offen offset:1024 lds\n"
        "s_add_u32 m0, s82, 26624\n"
        "s_mov_b32 s65, 37376\n"
        "buffer_load_dwordx4 v7, s[44:47], s65 offen lds\n"
        "s_mov_b32 s65, 45568\n"
        "buffer_load_dwordx4 v7, s[44:47], s65 offen offset:1024 lds\n"
        "s_waitcnt vmcnt(24)\n"
        "ds_read_b64_tr_b16 v[80:81], v10 offset:0\n"
        "ds_read_b64_tr_b16 v[82:83], v10 offset:256\n"
        "ds_read_b64_tr_b16 v[84:85], v10 offset:1024\n"
        "ds_read_b64_tr_b16 v[86:87], v10 offset:1280\n"
        "ds_read_b64_tr_b16 v[88:89], v10 offset:32\n"
        "ds_read_b64_tr_b16 v[90:91], v10 offset:288\n"
        "ds_read_b64_tr_b16 v[92:93], v10 offset:1056\n"
        "ds_read_b64_tr_b16 v[94:95], v10 offset:1312\n"
        "s_waitcnt lgkmcnt(8)\n"
        "s_add_u32 m0, s82, 28672\n"
        "s_mov_b32 s65, 74240\n"
        "buffer_load_dwordx4 v7, s[44:47], s65 offen lds\n"
        "s_mov_b32 s65, 82432\n"
        "buffer_load_dwordx4 v7, s[44:47], s65 offen offset:1024 lds\n"
        "s_add_u32 m0, s82, 30720\n"
        "s_mov_b32 s65, 111104\n"
        "buffer_load_dwordx4 v7, s[44:47], s65 offen lds\n"
        "s_mov_b32 s65, 119296\n"
        "buffer_load_dwordx4 v7, s[44:47], s65 offen offset:1024 lds\n"
        "s_waitcnt vmcnt(24)\n"
        "ds_read_b64_tr_b16 v[96:97], v10 offset:4096\n"
        "ds_read_b64_tr_b16 v[98:99], v10 offset:4352\n"
        "ds_read_b64_tr_b16 v[100:101], v10 offset:5120\n"
        "ds_read_b64_tr_b16 v[102:103], v10 offset:5376\n"
        "ds_read_b64_tr_b16 v[104:105], v10 offset:4128\n"
        "ds_read_b64_tr_b16 v[106:107], v10 offset:4384\n"
        "ds_read_b64_tr_b16 v[108:109], v10 offset:5152\n"
        "ds_read_b64_tr_b16 v[110:111], v10 offset:5408\n"
        "s_waitcnt lgkmcnt(8)\n"
        "s_add_u32 m0, s82, 0\n"
        "s_mov_b32 s65, 576\n"
        "buffer_load_dwordx4 v7, s[44:47], s65 offen lds\n"
        "s_mov_b32 s65, 8768\n"
        "buffer_load_dwordx4 v7, s[44:47], s65 offen offset:1024 lds\n"
        "s_add_u32 m0, s82, 2048\n"
        "s_mov_b32 s65, 37440\n"
        "buffer_load_dwordx4 v7, s[44:47], s65 offen lds\n"
        "s_mov_b32 s65, 45632\n"
        "buffer_load_dwordx4 v7, s[44:47], s65 offen offset:1024 lds\n"
        "s_waitcnt vmcnt(24)\n"
        "ds_read_b64_tr_b16 v[112:113], v8 offset:8192\n"
        "ds_read_b64_tr_b16 v[114:115], v8 offset:8448\n"
        "ds_read_b64_tr_b16 v[116:117], v8 offset:9216\n"
        "ds_read_b64_tr_b16 v[118:119], v8 offset:9472\n"
        "ds_read_b64_tr_b16 v[120:121], v8 offset:10240\n"
        "ds_read_b64_tr_b16 v[122:123], v8 offset:10496\n"
        "ds_read_b64_tr_b16 v[124:125], v8 offset:11264\n"
        "ds_read_b64_tr_b16 v[126:127], v8 offset:11520\n"
        "s_waitcnt lgkmcnt(8)\n"
        "s_add_u32 m0, s82, 4096\n"
        "s_mov_b32 s65, 74304\n"
        "buffer_load_dwordx4 v7, s[44:47], s65 offen lds\n"
        "s_mov_b32 s65, 82496\n"
        "buffer_load_dwordx4 v7, s[44:47], s65 offen offset:1024 lds\n"
        "s_add_u32 m0, s82, 6144\n"
        "s_mov_b32 s65, 111168\n"
        "buffer_load_dwordx4 v7, s[44:47], s65 offen lds\n"
        "s_mov_b32 s65, 119360\n"
        "buffer_load_dwordx4 v7, s[44:47], s65 offen offset:1024 lds\n"
        "s_cmp_lt_u32 s48, 2\n"
        "s_cbranch_scc0 .Lch_noex_%=\n"
        "s_lshl_b32 s66, s48, 6\n"
        "s_add_u32 m0, s82, 32768\n"
        "s_sub_u32 s65, 1024, s66\n"
        "buffer_load_dwordx4 v7, s[44:47], s65 offen lds\n"
        "s_sub_u32 s65, 9216, s66\n"
        "buffer_load_dwordx4 v7, s[44:47], s65 offen offset:1024 lds\n"
        "s_add_u32 m0, s82, 34816\n"
        "s_sub_u32 s65, 37888, s66\n"
        "buffer_load_dwordx4 v7, s[44:47], s65 offen lds\n"
        "s_sub_u32 s65, 46080, s66\n"
        "buffer_load_dwordx4 v7, s[44:47], s65 offen offset:1024 lds\n"
        "s_add_u32 m0, s82, 36864\n"
        "s_sub_u32 s65, 74752, s66\n"
        "buffer_load_dwordx4 v7, s[44:47], s65 offen lds\n"
        "s_sub_u32 s65, 82944, s66\n"
        "buffer_load_dwordx4 v7, s[44:47], s65 offen offset:1024 lds\n"
        "s_add_u32 m0, s82, 38912\n"
        "s_sub_u32 s65, 111616, s66\n"
        "buffer_load_dwordx4 v7, s[44:47], s65 offen lds\n"
        "s_sub_u32 s65, 119808, s66\n"
        "buffer_load_dwordx4 v7, s[44:47], s65 offen offset:1024 lds\n"
        ".Lch_noex_%=:\n"
        "s_waitcnt vmcnt(24)\n"
        "ds_read_b64_tr_b16 v[128:129], v8 offset:12288\n"
        "ds_read_b64_tr_b16 v[130:131], v8 offset:12544\n"
        "ds_read_b64_tr_b16 v[132:133], v8 offset:13312\n"
        "ds_read_b64_tr_b16 v[134:135], v8 offset:13568\n"
        "ds_read_b64_tr_b16 v[136:137], v8 offset:14336\n"
        "ds_read_b64_tr_b16 v[138:139], v8 offset:14592\n"
        "ds_read_b64_tr_b16 v[140:141], v8 offset:15360\n"
        "ds_read_b64_tr_b16 v[142:143], v8 offset:15616\n"
        "s_waitcnt lgkmcnt(8)\n"
        "s_mov_b64 s[44:45], %3\n"
        "s_and_b32 s45, s45, 0xffff\n"
        "s_mov_b32 s46, 4718592\n"
        "s_add_u32 m0, s82, 8192\n"
        "s_mov_b32 s65, 4194304\n"
        "buffer_load_dwordx4 v6, s[44:47], s65 offen lds\n"
        "s_mov_b32 s65, 4324352\n"
        "buffer_load_dwordx4 v6, s[44:47], s65 offen offset:1024 lds\n"
        "s_add_u32 m0, s82, 10240\n"
        "s_mov_b32 s65, 4456448\n"
        "buffer_load_dwordx4 v6, s[44:47], s65 offen lds\n"
        "s_mov_b32 s65, 4586496\n"
        "buffer_load_dwordx4 v6, s[44:47], s65 offen offset:1024 lds\n"
        "s_waitcnt vmcnt(24)\n"
        "ds_read_b64_tr_b16 v[144:145], v8 offset:16384\n"
        "ds_read_b64_tr_b16 v[146:147], v8 offset:16640\n"
        "ds_read_b64_tr_b16 v[148:149], v8 offset:17408\n"
        "ds_read_b64_tr_b16 v[150:151], v8 offset:17664\n"
        "ds_read_b64_tr_b16 v[152:153], v8 offset:18432\n"
        "ds_read_b64_tr_b16 v[154:155], v8 offset:18688\n"
        "ds_read_b64_tr_b16 v[156:157], v8 offset:19456\n"
        "ds_read_b64_tr_b16 v[158:159], v8 offset:19712\n"
        "s_waitcnt lgkmcnt(8)\n"
        "s_add_u32 m0, s82, 12288\n"
        "s_mov_b32 s65, 1572864\n"
        "buffer_load_dwordx4 v6, s[44:47], s65 offen lds\n"
        "s_mov_b32 s65, 1702912\n"
        "buffer_load_dwordx4 v6, s[44:47], s65 offen offset:1024 lds\n"
        "s_add_u32 m0, s82, 14336\n"
        "s_mov_b32 s65, 1835008\n"
        "buffer_load_dwordx4 v6, s[44:47], s65 offen lds\n"
        "s_mov_b32 s65, 1965056\n"
        "buffer_load_dwordx4 v6, s[44:47], s65 offen offset:1024 lds\n"
        "s_waitcnt vmcnt(24)\n"
        "ds_read_b64_tr_b16 v[160:161], v8 offset:20480\n"
        "ds_read_b64_tr_b16 v[162:163], v8 offset:20736\n"
        "ds_read_b64_tr_b16 v[164:165], v8 offset:21504\n"
        "ds_read_b64_tr_b16 v[166:167], v8 offset:21760\n"
        "ds_read_b64_tr_b16 v[168:169], v8 offset:22528\n"
        "ds_read_b64_tr_b16 v[170:171], v8 offset:22784\n"
        "ds_read_b64_tr_b16 v[172:173], v8 offset:23552\n"
        "ds_read_b64_tr_b16 v[174:175], v8 offset:23808\n"
        "s_waitcnt lgkmcnt(8)\n"
        "s_add_u32 m0, s82, 16384\n"
        "s_mov_b32 s65, 2097152\n"
        "buffer_load_dwordx4 v6, s[44:47], s65 offen lds\n"
        "s_mov_b32 s65, 2227200\n"
        "buffer_load_dwordx4 v6, s[44:47], s65 offen offset:1024 lds\n"
        "s_add_u32 m0, s82, 18432\n"
        "s_mov_b32 s65, 2359296\n"
        "buffer_load_dwordx4 v6, s[44:47], s65 offen lds\n"
        "s_mov_b32 s65, 2489344\n"
        "buffer_load_dwordx4 v6, s[44:47], s65 offen offset:1024 lds\n"
        "s_waitcnt vmcnt(24)\n"
        "ds_read_b64_tr_b16 v[176:177], v8 offset:24576\n"
        "ds_read_b64_tr_b16 v[178:179], v8 offset:24832\n"
        "ds_read_b64_tr_b16 v[180:181], v8 offset:25600\n"
        "ds_read_b64_tr_b16 v[182:183], v8 offset:25856\n"
        "ds_read_b64_tr_b16 v[184:185], v8 offset:26624\n"
        "ds_read_b64_tr_b16 v[186:187], v8 offset:26880\n"
        "ds_read_b64_tr_b16 v[188:189], v8 offset:27648\n"
        "ds_read_b64_tr_b16 v[190:191], v8 offset:27904\n"
        "s_waitcnt lgkmcnt(8)\n"
        "s_add_u32 m0, s82, 20480\n"
        "s_mov_b32 s65, 2621440\n"
        "buffer_load_dwordx4 v6, s[44:47], s65 offen lds\n"
        "s_mov_b32 s65, 2751488\n"
        "buffer_load_dwordx4 v6, s[44:47], s65 offen offset:1024 lds\n"
        "s_add_u32 m0, s82, 22528\n"
        "s_mov_b32 s65, 2883584\n"
        "buffer_load_dwordx4 v6, s[44:47], s65 offen lds\n"
        "s_mov_b32 s65, 3013632\n"
        "buffer_load_dwordx4 v6, s[44:47], s65 offen offset:1024 lds\n"
        "s_waitcnt vmcnt(24)\n"
        "ds_read_b64_tr_b16 v[192:193], v8 offset:28672\n"
        "ds_read_b64_tr_b16 v[194:195], v8 offset:28928\n"
        "ds_read_b64_tr_b16 v[196:197], v8 offset:29696\n"
        "ds_read_b64_tr_b16 v[198:199], v8 offset:29952\n"
        "ds_read_b64_tr_b16 v[200:201], v8 offset:30720\n"
        "ds_read_b64_tr_b16 v[202:203], v8 offset:30976\n"
        "ds_read_b64_tr_b16 v[204:205], v8 offset:31744\n"
        "ds_read_b64_tr_b16 v[206:207], v8 offset:32000\n"
        "s_waitcnt lgkmcnt(8)\n"
        "s_add_u32 m0, s82, 24576\n"
        "s_mov_b32 s65, 3145728\n"
        "buffer_load_dwordx4 v6, s[44:47], s65 offen lds\n"
        "s_mov_b32 s65, 3275776\n"
        "buffer_load_dwordx4 v6, s[44:47], s65 offen offset:1024 lds\n"
        "s_add_u32 m0, s82, 26624\n"
        "s_mov_b32 s65, 3407872\n"
        "buffer_load_dwordx4 v6, s[44:47], s65 offen lds\n"
        "s_mov_b32 s65, 3537920\n"
        "buffer_load_dwordx4 v6, s[44:47], s65 offen offset:1024 lds\n"
        "s_waitcnt vmcnt(24)\n"
        "ds_read_b64_tr_b16 v[208:209], v8 offset:0\n"
        "ds_read_b64_tr_b16 v[210:211], v8 offset:256\n"
        "ds_read_b64_tr_b16 v[212:213], v8 offset:1024\n"
        "ds_read_b64_tr_b16 v[214:215], v8 offset:1280\n"
        "ds_read_b64_tr_b16 v[216:217], v8 offset:2048\n"
        "ds_read_b64_tr_b16 v[218:219], v8 offset:2304\n"
        "ds_read_b64_tr_b16 v[220:221], v8 offset:3072\n"
        "ds_read_b64_tr_b16 v[222:223], v8 offset:3328\n"
        "s_waitcnt lgkmcnt(8)\n"
        "s_add_u32 m0, s82, 28672\n"
        "s_mov_b32 s65, 3670016\n"
        "buffer_load_dwordx4 v6, s[44:47], s65 offen lds\n"
        "s_mov_b32 s65, 3800064\n"
        "buffer_load_dwordx4 v6, s[44:47], s65 offen offset:1024 lds\n"
        "s_add_u32 m0, s82, 30720\n"
        "s_mov_b32 s65, 3932160\n"
        "buffer_load_dwordx4 v6, s[44:47], s65 offen lds\n"
        "s_mov_b32 s65, 4062208\n"
        "buffer_load_dwordx4 v6, s[44:47], s65 offen offset:1024 lds\n"
        "s_waitcnt vmcnt(24)\n"
        "ds_read_b64_tr_b16 v[224:225], v8 offset:4096\n"
        "ds_read_b64_tr_b16 v[226:227], v8 offset:4352\n"
        "ds_read_b64_tr_b16 v[228:229], v8 offset:5120\n"
        "ds_read_b64_tr_b16 v[230:231], v8 offset:5376\n"
        "ds_read_b64_tr_b16 v[232:233], v8 offset:6144\n"
        "ds_read_b64_tr_b16 v[234:235], v8 offset:6400\n"
        "ds_read_b64_tr_b16 v[236:237], v8 offset:7168\n"
        "ds_read_b64_tr_b16 v[238:239], v8 offset:7424\n"
        "s_waitcnt lgkmcnt(8)\n"
        "s_add_u32 m0, s82, 0\n"
        "s_mov_b32 s65, 0\n"
        "buffer_load_dwordx4 v6, s[44:47], s65 offen lds\n"
        "s_mov_b32 s65, 130048\n"
        "buffer_load_dwordx4 v6, s[44:47], s65 offen offset:1024 lds\n"
        "s_add_u32 m0, s82, 2048\n"
        "s_mov_b32 s65, 262144\n"
        "buffer_load_dwordx4 v6, s[44:47], s65 offen lds\n"
        "s_mov_b32 s65, 392192\n"
        "buffer_load_dwordx4 v6, s[44:47], s65 offen offset:1024 lds\n"
        "s_waitcnt vmcnt(24)\n"
        "ds_read_b64_tr_b16 v[240:241], v10 offset:8192\n"
        "ds_read_b64_tr_b16 v[242:243], v10 offset:8448\n"
        "ds_read_b64_tr_b16 v[244:245], v10 offset:9216\n"
        "ds_read_b64_tr_b16 v[246:247], v10 offset:9472\n"
        "ds_read_b64_tr_b16 v[248:249], v10 offset:8224\n"
        "ds_read_b64_tr_b16 v[250:251], v10 offset:8480\n"
        "ds_read_b64_tr_b16 v[252:253], v10 offset:9248\n"
        "ds_read_b64_tr_b16 v[254:255], v10 offset:9504\n"
        "s_waitcnt lgkmcnt(8)\n"
        "s_add_u32 m0, s82, 4096\n"
        "s_mov_b32 s65, 524288\n"
        "buffer_load_dwordx4 v6, s[44:47], s65 offen lds\n"
        "s_mov_b32 s65, 654336\n"
        "buffer_load_dwordx4 v6, s[44:47], s65 offen offset:1024 lds\n"
        "s_add_u32 m0, s82, 6144\n"
        "s_mov_b32 s65, 786432\n"
        "buffer_load_dwordx4 v6, s[44:47], s65 offen lds\n"
        "s_mov_b32 s65, 916480\n"
        "buffer_load_dwordx4 v6, s[44:47], s65 offen offset:1024 lds\n"
        "s_waitcnt vmcnt(24)\n"
        "ds_read_b64_tr_b16 v[40:41], v10 offset:12288\n"
        "ds_read_b64_tr_b16 v[42:43], v10 offset:12544\n"
        "ds_read_b64_tr_b16 v[44:45], v10 offset:13312\n"
        "ds_read_b64_tr_b16 v[46:47], v10 offset:13568\n"
        "ds_read_b64_tr_b16 v[48:49], v10 offset:12320\n"
        "ds_read_b64_tr_b16 v[50:51], v10 offset:12576\n"
        "ds_read_b64_tr_b16 v[52:53], v10 offset:13344\n"
        "ds_read_b64_tr_b16 v[54:55], v10 offset:13600\n"
        "s_waitcnt lgkmcnt(8)\n"
        "s_add_u32 m0, s82, 8192\n"
        "s_mov_b32 s65, 1048576\n"
        "buffer_load_dwordx4 v6, s[44:47], s65 offen lds\n"
        "s_mov_b32 s65, 1178624\n"
        "buffer_load_dwordx4 v6, s[44:47], s65 offen offset:1024 lds\n"
        "s_add_u32 m0, s82, 10240\n"
        "s_mov_b32 s65, 1310720\n"
        "buffer_load_dwordx4 v6, s[44:47], s65 offen lds\n"
        "s_mov_b32 s65, 1440768\n"
        "buffer_load_dwordx4 v6, s[44:47], s65 offen offset:1024 lds\n"
        "s_waitcnt vmcnt(24)\n"
        "ds_read_b64_tr_b16 v[24:25], v10 offset:16384\n"
        "ds_read_b64_tr_b16 v[26:27], v10 offset:16640\n"
        "ds_read_b64_tr_b16 v[28:29], v10 offset:17408\n"
        "ds_read_b64_tr_b16 v[30:31], v10 offset:17664\n"
        "ds_read_b64_tr_b16 v[32:33], v10 offset:16416\n"
        "ds_read_b64_tr_b16 v[34:35], v10 offset:16672\n"
        "ds_read_b64_tr_b16 v[36:37], v10 offset:17440\n"
        "ds_read_b64_tr_b16 v[38:39], v10 offset:17696\n"
        "s_waitcnt lgkmcnt(8)\n"
        "ds_write_b128 v2, v[40:43] offset:12288\n"
        "ds_write_b128 v2, v[44:47] offset:13312\n"
        "ds_write_b128 v2, v[48:51] offset:14336\n"
        "ds_write_b128 v2, v[52:55] offset:15360\n"
        "s_waitcnt vmcnt(20)\n"
        "ds_read_b64_tr_b16 v[40:41], v10 offset:20480\n"
        "ds_read_b64_tr_b16 v[42:43], v10 offset:20736\n"
        "ds_read_b64_tr_b16 v[44:45], v10 offset:21504\n"
        "ds_read_b64_tr_b16 v[46:47], v10 offset:21760\n"
        "ds_read_b64_tr_b16 v[48:49], v10 offset:20512\n"
        "ds_read_b64_tr_b16 v[50:51], v10 offset:20768\n"
        "ds_read_b64_tr_b16 v[52:53], v10 offset:21536\n"
        "ds_read_b64_tr_b16 v[54:55], v10 offset:21792\n"
        "s_waitcnt lgkmcnt(8)\n"
        "ds_write_b128 v2, v[24:27] offset:16384\n"
        "ds_write_b128 v2, v[28:31] offset:17408\n"
        "ds_write_b128 v2, v[32:35] offset:18432\n"
        "ds_write_b128 v2, v[36:39] offset:19456\n"
        "s_waitcnt vmcnt(16)\n"
        "ds_read_b64_tr_b16 v[24:25], v10 offset:24576\n"
        "ds_read_b64_tr_b16 v[26:27], v10 offset:24832\n"
        "ds_read_b64_tr_b16 v[28:29], v10 offset:25600\n"
        "ds_read_b64_tr_b16 v[30:31], v10 offset:25856\n"
        "ds_read_b64_tr_b16 v[32:33], v10 offset:24608\n"
        "ds_read_b64_tr_b16 v[34:35], v10 offset:24864\n"
        "ds_read_b64_tr_b16 v[36:37], v10 offset:25632\n"
        "ds_read_b64_tr_b16 v[38:39], v10 offset:25888\n"
        "s_waitcnt lgkmcnt(8)\n"
        "ds_write_b128 v2, v[40:43] offset:20480\n"
        "ds_write_b128 v2, v[44:47] offset:21504\n"
        "ds_write_b128 v2, v[48:51] offset:22528\n"
        "ds_write_b128 v2, v[52:55] offset:23552\n"
        "s_waitcnt vmcnt(12)\n"
        "ds_read_b64_tr_b16 v[40:41], v10 offset:28672\n"
        "ds_read_b64_tr_b16 v[42:43], v10 offset:28928\n"
        "ds_read_b64_tr_b16 v[44:45], v10 offset:29696\n"
        "ds_read_b64_tr_b16 v[46:47], v10 offset:29952\n"
        "ds_read_b64_tr_b16 v[48:49], v10 offset:28704\n"
        "ds_read_b64_tr_b16 v[50:51], v10 offset:28960\n"
        "ds_read_b64_tr_b16 v[52:53], v10 offset:29728\n"
        "ds_read_b64_tr_b16 v[54:55], v10 offset:29984\n"
        "s_waitcnt lgkmcnt(8)\n"
        "ds_write_b128 v2, v[24:27] offset:24576\n"
        "ds_write_b128 v2, v[28:31] offset:25600\n"
        "ds_write_b128 v2, v[32:35] offset:26624\n"
        "ds_write_b128 v2, v[36:39] offset:27648\n"
        "s_waitcnt vmcnt(8)\n"
        "ds_read_b64_tr_b16 v[24:25], v10 offset:0\n"
        "ds_read_b64_tr_b16 v[26:27], v10 offset:256\n"
        "ds_read_b64_tr_b16 v[28:29], v10 offset:1024\n"
        "ds_read_b64_tr_b16 v[30:31], v10 offset:1280\n"
        "ds_read_b64_tr_b16 v[32:33], v10 offset:32\n"
        "ds_read_b64_tr_b16 v[34:35], v10 offset:288\n"
        "ds_read_b64_tr_b16 v[36:37], v10 offset:1056\n"
        "ds_read_b64_tr_b16 v[38:39], v10 offset:1312\n"
        "s_waitcnt lgkmcnt(8)\n"
        "ds_write_b128 v2, v[40:43] offset:28672\n"
        "ds_write_b128 v2, v[44:47] offset:29696\n"
        "ds_write_b128 v2, v[48:51] offset:30720\n"
        "ds_write_b128 v2, v[52:55] offset:31744\n"
        "s_waitcnt vmcnt(4)\n"
        "ds_read_b64_tr_b16 v[40:41], v10 offset:4096\n"
        "ds_read_b64_tr_b16 v[42:43], v10 offset:4352\n"
        "ds_read_b64_tr_b16 v[44:45], v10 offset:5120\n"
        "ds_read_b64_tr_b16 v[46:47], v10 offset:5376\n"
        "ds_read_b64_tr_b16 v[48:49], v10 offset:4128\n"
        "ds_read_b64_tr_b16 v[50:51], v10 offset:4384\n"
        "ds_read_b64_tr_b16 v[52:53], v10 offset:5152\n"
        "ds_read_b64_tr_b16 v[54:55], v10 offset:5408\n"
        "s_waitcnt lgkmcnt(8)\n"
        "ds_write_b128 v2, v[24:27] offset:0\n"
        "ds_write_b128 v2, v[28:31] offset:1024\n"
        "ds_write_b128 v2, v[32:35] offset:2048\n"
        "ds_write_b128 v2, v[36:39] offset:3072\n"
        "s_waitcnt vmcnt(0)\n"
        "ds_read_b64_tr_b16 v[24:25], v10 offset:8192\n"
        "ds_read_b64_tr_b16 v[26:27], v10 offset:8448\n"
        "ds_read_b64_tr_b16 v[28:29], v10 offset:9216\n"
        "ds_read_b64_tr_b16 v[30:31], v10 offset:9472\n"
        "ds_read_b64_tr_b16 v[32:33], v10 offset:8224\n"
        "ds_read_b64_tr_b16 v[34:35], v10 offset:8480\n"
        "ds_read_b64_tr_b16 v[36:37], v10 offset:9248\n"
        "ds_read_b64_tr_b16 v[38:39], v10 offset:9504\n"
        "s_waitcnt lgkmcnt(8)\n"
        "ds_write_b128 v2, v[40:43] offset:4096\n"
        "ds_write_b128 v2, v[44:47] offset:5120\n"
        "ds_write_b128 v2, v[48:51] offset:6144\n"
        "ds_write_b128 v2, v[52:55] offset:7168\n"
        "s_waitcnt lgkmcnt(0)\n"
        "ds_write_b128 v2, v[24:27] offset:8192\n"
        "ds_write_b128 v2, v[28:31] offset:9216\n"
        "ds_write_b128 v2, v[32:35] offset:10240\n"
        "ds_write_b128 v2, v[36:39] offset:11264\n"
        "s_cmp_lt_u32 s48, 2\n"
        "s_cbranch_scc0 .Lch_noex2_%=\n"
        "ds_read_b64_tr_b16 v[40:41], v8 offset:32768\n"
        "ds_read_b64_tr_b16 v[42:43], v8 offset:33024\n"
        "ds_read_b64_tr_b16 v[44:45], v8 offset:33792\n"
        "ds_read_b64_tr_b16 v[46:47], v8 offset:34048\n"
        "ds_read_b64_tr_b16 v[48:49], v8 offset:34816\n"
        "ds_read_b64_tr_b16 v[50:51], v8 offset:35072\n"
        "ds_read_b64_tr_b16 v[52:53], v8 offset:35840\n"
        "ds_read_b64_tr_b16 v[54:55], v8 offset:36096\n"
        "ds_read_b64_tr_b16 v[56:57], v8 offset:36864\n"
        "ds_read_b64_tr_b16 v[58:59], v8 offset:37120\n"
        "ds_read_b64_tr_b16 v[60:61], v8 offset:37888\n"
        "ds_read_b64_tr_b16 v[62:63], v8 offset:38144\n"
        "ds_read_b64_tr_b16 v[64:65], v8 offset:38912\n"
        "ds_read_b64_tr_b16 v[66:67], v8 offset:39168\n"
        "ds_read_b64_tr_b16 v[68:69], v8 offset:39936\n"
        "ds_read_b64_tr_b16 v[70:71], v8 offset:40192\n"
        "s_waitcnt lgkmcnt(0)\n"
        "ds_write_b128 v2, v[40:43] offset:32768\n"
        "ds_write_b128 v2, v[44:47] offset:33792\n"
        "ds_write_b128 v2, v[48:51] offset:34816\n"
        "ds_write_b128 v2, v[52:55] offset:35840\n"
        "ds_write_b128 v2, v[56:59] offset:36864\n"
        "ds_write_b128 v2, v[60:63] offset:37888\n"
        "ds_write_b128 v2, v[64:67] offset:38912\n"
        "ds_write_b128 v2, v[68:71] offset:39936\n"
        ".Lch_noex2_%=:\n"
        "s_waitcnt lgkmcnt(0)\n"
        "v_lshrrev_b32 v12, 4, v3\n"
        "s_mov_b32 s50, s56\n"
        "v_lshlrev_b32 v14, 7, v12\n"
        "s_sub_u32 s66, 0, s50\n"
        "s_mov_b32 s52, 32768\n"
        "s_mov_b32 s64, 0\n"
        ".Lch_pg0_top_%=:\n"
        "buffer_load_dword v13, v14, s[40:43], s52 offen sc1\n"
        "s_waitcnt vmcnt(0)\n"
        "v_cmp_gt_i32 vcc, 0, v13\n"
        "s_cbranch_vccz .Lch_pg0_ok_%=\n"
        "s_add_u32 s64, s64, 1\n"
        "s_and_b32 s65, s64, 255\n"
        "s_cbranch_scc1 .Lch_pg0_go_%=\n"
        "s_mov_b32 s65, 49152\n"
        "s_cmp_lt_u32 s64, 1048576\n"
        "s_cbranch_scc1 .Lch_pg0_chk_%=\n"
        "v_mov_b32 v13, 1\n"
        "buffer_store_dword v13, off, s[40:43], s65 sc1\n"
        "s_waitcnt vmcnt(0)\n"
        ".Lch_pg0_chk_%=:\n"
        "buffer_load_dword v13, off, s[40:43], s65 sc1\n"
        "s_waitcnt vmcnt(0)\n"
        "v_readfirstlane_b32 s65, v13\n"
        "s_nop 0\n"
        "s_cmp_lg_u32 s65, 1\n"
        "s_cbranch_scc1 .Lch_pg0_go_%=\n"
        "s_endpgm\n"
        ".Lch_pg0_go_%=:\n"
        "s_sleep 2\n"
        "s_branch .Lch_pg0_top_%=\n"
        ".Lch_pg0_ok_%=:\n"
        "v_cmp_eq_u32 s[52:53], 1, v13\n"
        "s_nop 1\n"
        "s_mov_b32 s54, 40960\n"
        "s_mov_b32 s64, 0\n"
        ".Lch_pg1_top_%=:\n"
        "buffer_load_dword v13, v14, s[40:43], s54 offen sc1\n"
        "s_waitcnt vmcnt(0)\n"
        "v_cmp_gt_i32 vcc, 0, v13\n"
        "s_cbranch_vccz .Lch_pg1_ok_%=\n"
        "s_add_u32 s64, s64, 1\n"
        "s_and_b32 s65, s64, 255\n"
        "s_cbranch_scc1 .Lch_pg1_go_%=\n"
        "s_mov_b32 s65, 49152\n"
        "s_cmp_lt_u32 s64, 1048576\n"
        "s_cbranch_scc1 .Lch_pg1_chk_%=\n"
        "v_mov_b32 v13, 1\n"
        "buffer_store_dword v13, off, s[40:43], s65 sc1\n"
        "s_waitcnt vmcnt(0)\n"
        ".Lch_pg1_chk_%=:\n"
        "buffer_load_dword v13, off, s[40:43], s65 sc1\n"
        "s_waitcnt vmcnt(0)\n"
        "v_readfirstlane_b32 s65, v13\n"
        "s_nop 0\n"
        "s_cmp_lg_u32 s65, 1\n"
        "s_cbranch_scc1 .Lch_pg1_go_%=\n"
        "s_endpgm\n"
        ".Lch_pg1_go_%=:\n"
        "s_sleep 2\n"
        "s_branch .Lch_pg1_top_%=\n"
        ".Lch_pg1_ok_%=:\n"
        "v_cmp_eq_u32 s[54:55], 1, v13\n"
        "s_nop 1\n"
        "s_mov_b32 vcc_lo, 0x55555555\n"
        "s_mov_b32 vcc_hi, 0x55555555\n"
        "s_xor_b64 vcc, vcc, s[52:53]\n"
        "s_bcnt1_i32_b64 s65, vcc\n"
        "s_sub_u32 s64, s65, 23\n"
        "s_cmp_lt_u32 s64, 19\n"
        "s_cbranch_scc1 .Lch_rawmap_%=\n"
        "s_cmp_lt_u32 s65, 32\n"
        "s_cselect_b32 s65, 0, -1\n"
        "s_xor_b32 s52, s65, 0x55555555\n"
        "s_xor_b32 s53, s65, 0x55555555\n"
        "s_xor_b32 s54, s65, 0x55555555\n"
        "s_xor_b32 s55, s65, 0xaaaaaaaa\n"
        ".Lch_rawmap_%=:\n"
        "s_xor_b32 s52, s52, s66\n"
        "s_xor_b32 s53, s53, s66\n"
        "s_xor_b32 s54, s54, s66\n"
        "s_xor_b32 s55, s55, s66\n"
        "s_mov_b32 s58, 0xff00ff00\n"
        "s_mov_b32 s59, 0xff00ff00\n"
        "s_and_b32 s67, s55, 1\n"
        "s_lshl_b32 s67, s67, 13\n"
        "s_lshl_b32 s65, s49, 2\n"
        "s_add_u32 s65, s65, s48\n"
        "s_lshl_b32 s65, s65, 7\n"
        "s_lshr_b32 s66, s65, 13\n"
        "s_lshl_b32 s66, s66, 13\n"
        "s_add_u32 s66, s66, s65\n"
        "s_add_u32 s66, s66, s67\n"
        "s_add_u32 s78, s66, 180224\n"
        "s_add_u32 s79, s66, 245760\n"
        "s_lshl_b32 s83, s49, 14\n"
        "s_lshl_b32 s65, s48, 11\n"
        "s_add_u32 s83, s83, s65\n"
        "s_add_u32 s83, s83, 524288\n"
        "s_lshl_b32 s65, s49, 12\n"
        "s_lshr_b32 s84, s65, 13\n"
        "s_lshl_b32 s84, s84, 13\n"
        "s_add_u32 s84, s84, s65\n"
        "s_lshl_b32 s65, s48, 9\n"
        "s_add_u32 s84, s84, s65\n"
        "s_add_u32 s84, s84, 1572864\n"
        "s_lshr_b32 s65, s49, 2\n"
        "s_add_u32 s65, s65, 64\n"
        "s_cmp_eq_u32 s48, 2\n"
        "s_cselect_b32 s65, s49, s65\n"
        "s_add_u32 s66, s49, 32\n"
        "s_cmp_eq_u32 s48, 3\n"
        "s_cselect_b32 s65, s66, s65\n"
        "s_lshl_b32 s85, s65, 7\n"
        "s_cmp_eq_u32 s48, 0\n"
        "s_cselect_b32 s66, 57344, 0\n"
        "s_add_u32 s85, s85, s66\n"
        "s_add_u32 s85, s85, s67\n"
        "s_add_u32 s85, s85, 1130496\n"
        "s_lshr_b32 s66, s49, 3\n"
        "s_lshl_b32 s54, s66, 14\n"
        "s_lshl_b32 s55, s66, 10\n"
        "s_and_b32 s66, s48, 1\n"
        "s_lshl_b32 s66, s66, 12\n"
        "s_add_u32 s54, s54, s66\n"
        "s_add_u32 s66, s67, 1130496\n"
        "s_add_u32 s54, s54, s66\n"
        "s_add_u32 s55, s55, s66\n"
        "s_add_u32 s55, s55, 65536\n"
        "s_add_u32 s72, s85, 245760\n"
        "s_lshl_b32 s66, s65, 12\n"
        "s_lshr_b32 s87, s66, 13\n"
        "s_lshl_b32 s87, s87, 13\n"
        "s_add_u32 s87, s87, s66\n"
        "s_and_b32 s65, s49, 3\n"
        "s_or_b32 s88, s65, s48\n"
        "s_lshl_b32 s86, s48, 16\n"
        "s_lshl_b32 s65, s49, 7\n"
        "s_add_u32 s86, s86, s65\n"
        "s_add_u32 s67, s67, 540672\n"
        "s_add_u32 s86, s86, s67\n"
        "s_add_u32 s87, s87, s67\n"
        "s_mov_b32 s73, s86\n"
        "s_add_u32 s74, s73, 262144\n"
        "s_mov_b32 s76, s87\n"
        "s_lshl_b32 s65, s48, 15\n"
        "s_sub_u32 s75, s73, s65\n"
        "s_add_u32 s75, s75, 524288\n"
        "s_lshl_b32 s65, s48, 3\n"
        "s_lshr_b32 s60, s60, s65\n"
        "s_and_b32 s60, s60, 255\n"
        "s_sub_u32 s60, s60, 32\n"
        "s_lshr_b32 s34, s61, s65\n"
        "s_and_b32 s34, s34, 255\n"
        "s_lshr_b32 s61, s62, s65\n"
        "s_and_b32 s61, s61, 255\n"
        "s_mov_b32 s35, 90\n"
        "s_lshl_b32 s33, s48, 10\n"
        "s_lshl_b32 s32, s48, 2\n"
        "s_lshr_b32 s65, s48, 1\n"
        "s_lshl_b32 s31, s65, 10\n"
        "s_and_b32 s65, s48, 1\n"
        "s_lshl_b32 s65, s65, 6\n"
        "s_add_u32 s31, s31, s65\n"
        "s_mov_b32 s51, -1\n"
        "s_mov_b32 s80, -1\n"
        "s_lshl_b32 s62, s49, 7\n"
        "s_cmp_eq_u32 s48, 1\n"
        "s_cselect_b32 s62, s62, 2147483648\n"
        "s_bfe_u32 s56, s63, 0x80008\n"
        "s_lshr_b32 s57, s63, 16\n"
        "s_and_b32 s63, s63, 255\n"
        "s_mov_b32 s44, 0x3f803f80\n"
        "s_mov_b32 s45, 0x3f803f80\n"
        "s_branch .Lch_idle2_%=\n"
        ".Lch_Q_%=:\n"
        "ds_read_b128 v[32:35], v1 offset:0\n"
        "ds_read_b32 v7, v5 offset:0\n"
        "ds_read_b128 v[36:39], v1 offset:1024\n"
        "s_waitcnt lgkmcnt(2)\n"
        "v_smfmac_f32_16x16x64_bf16 v[8:11], v[32:35], a[0:7], v0\n"
        "buffer_load_dwordx4 v[64:67], v3, s[40:43], s30 offen offset:0 sc1\n"
        "v_max3_u32 v6, v6, v32, v33\n"
        "v_smfmac_f32_16x16x64_bf16 v[16:19], v[32:35], a[144:151], v0\n"
        "s_waitcnt lgkmcnt(1)\n"
        "buffer_load_dwordx4 v[68:71], v3, s[40:43], s30 offen offset:1024 sc1\n"
        "v_cmp_eq_u32 vcc, -1, v7\n"
        "v_smfmac_f32_16x16x64_bf16 v[24:27], v[32:35], v[40:47], v0\n"
        "ds_read_b128 v[40:43], v2 offset:6144\n"
        "v_max3_u32 v6, v6, v34, v35\n"
        "v_smfmac_f32_16x16x64_bf16 v[12:15], v[32:35], a[8:15], v0\n"
        "s_cbranch_vccnz .Lch_rt0_%=\n"
        "v_smfmac_f32_16x16x64_bf16 v[20:23], v[32:35], a[152:159], v0\n"
        "s_waitcnt lgkmcnt(1)\n"
        "ds_read_b128 v[44:47], v2 offset:7168\n"
        "v_smfmac_f32_16x16x64_bf16 v[28:31], v[32:35], v[48:55], v0\n"
        "ds_read_b128 v[48:51], v2 offset:8192\n"
        "v_smfmac_f32_16x16x64_bf16 v[8:11], v[36:39], a[16:23], v0\n"
        "ds_read_b128 v[32:35], v1 offset:2048\n"
        "v_max3_u32 v6, v6, v36, v37\n"
        "v_smfmac_f32_16x16x64_bf16 v[16:19], v[36:39], a[160:167], v0\n"
        "ds_read_b128 v[52:55], v2 offset:9216\n"
        "v_smfmac_f32_16x16x64_bf16 v[24:27], v[36:39], v[56:63], v0\n"
        "ds_read_b128 v[56:59], v2 offset:10240\n"
        "v_max3_u32 v6, v6, v38, v39\n"
        "v_smfmac_f32_16x16x64_bf16 v[12:15], v[36:39], a[24:31], v0\n"
        "ds_read_b128 v[60:63], v2 offset:11264\n"
        "v_smfmac_f32_16x16x64_bf16 v[20:23], v[36:39], a[168:175], v0\n"
        "s_waitcnt lgkmcnt(3)\n"
        "v_smfmac_f32_16x16x64_bf16 v[28:31], v[36:39], v[40:47], v0\n"
        "ds_read_b128 v[40:43], v2 offset:12288\n"
        "v_smfmac_f32_16x16x64_bf16 v[8:11], v[32:35], a[32:39], v0\n"
        "ds_read_b128 v[36:39], v1 offset:3072\n"
        "v_max3_u32 v6, v6, v32, v33\n"
        "v_smfmac_f32_16x16x64_bf16 v[16:19], v[32:35], a[176:183], v0\n"
        "s_waitcnt lgkmcnt(4)\n"
        "ds_read_b128 v[44:47], v2 offset:13312\n"
        "v_smfmac_f32_16x16x64_bf16 v[24:27], v[32:35], v[48:55], v0\n"
        "ds_read_b128 v[48:51], v2 offset:14336\n"
        "v_max3_u32 v6, v6, v34, v35\n"
        "v_smfmac_f32_16x16x64_bf16 v[12:15], v[32:35], a[40:47], v0\n"
        "ds_read_b128 v[52:55], v2 offset:15360\n"
        "buffer_load_dwordx4 v[72:75], v3, s[40:43], s89 offen offset:0 sc1\n"
        "v_smfmac_f32_16x16x64_bf16 v[20:23], v[32:35], a[184:191], v0\n"
        "s_waitcnt lgkmcnt(3)\n"
        "s_waitcnt vmcnt(4)\n"
        "s_barrier\n"
        "v_smfmac_f32_16x16x64_bf16 v[28:31], v[32:35], v[56:63], v0\n"
        "ds_read_b128 v[56:59], v2 offset:16384\n"
        "ds_read_b32 v7, v5 offset:4096\n"
        "v_smfmac_f32_16x16x64_bf16 v[8:11], v[36:39], a[48:55], v0\n"
        "ds_read_b128 v[32:35], v1 offset:4096\n"
        "v_max3_u32 v6, v6, v36, v37\n"
        "v_smfmac_f32_16x16x64_bf16 v[16:19], v[36:39], a[192:199], v0\n"
        "s_waitcnt lgkmcnt(5)\n"
        "ds_read_b128 v[60:63], v2 offset:17408\n"
        "v_smfmac_f32_16x16x64_bf16 v[24:27], v[36:39], v[40:47], v0\n"
        "ds_read_b128 v[40:43], v2 offset:18432\n"
        "v_max3_u32 v6, v6, v38, v39\n"
        "v_smfmac_f32_16x16x64_bf16 v[12:15], v[36:39], a[56:63], v0\n"
        "ds_read_b128 v[44:47], v2 offset:19456\n"
        "v_smfmac_f32_16x16x64_bf16 v[20:23], v[36:39], a[200:207], v0\n"
        "s_waitcnt lgkmcnt(3)\n"
        "v_cmp_eq_u32 vcc, -1, v7\n"
        "v_smfmac_f32_16x16x64_bf16 v[28:31], v[36:39], v[48:55], v0\n"
        "s_cbranch_vccnz .Lch_rt1_%=\n"
        ".Lch_ok1_%=:\n"
        "v_smfmac_f32_16x16x64_bf16 v[8:11], v[32:35], a[64:71], v0\n"
        "ds_read_b128 v[36:39], v1 offset:5120\n"
        "v_max3_u32 v6, v6, v32, v33\n"
        "v_smfmac_f32_16x16x64_bf16 v[16:19], v[32:35], a[208:215], v0\n"
        "s_waitcnt lgkmcnt(3)\n"
        "ds_read_b128 v[52:55], v2 offset:21504\n"
        "ds_read_b128 v[48:51], v2 offset:20480\n"
        "v_smfmac_f32_16x16x64_bf16 v[24:27], v[32:35], v[56:63], v0\n"
        "ds_read_b128 v[56:59], v2 offset:22528\n"
        "v_max3_u32 v6, v6, v34, v35\n"
        "v_smfmac_f32_16x16x64_bf16 v[12:15], v[32:35], a[72:79], v0\n"
        "ds_read_b128 v[60:63], v2 offset:23552\n"
        "v_smfmac_f32_16x16x64_bf16 v[20:23], v[32:35], a[216:223], v0\n"
        "s_waitcnt lgkmcnt(4)\n"
        "v_smfmac_f32_16x16x64_bf16 v[28:31], v[32:35], v[40:47], v0\n"
        "ds_read_b128 v[40:43], v2 offset:24576\n"
        "v_smfmac_f32_16x16x64_bf16 v[8:11], v[36:39], a[80:87], v0\n"
        "ds_read_b128 v[32:35], v1 offset:6144\n"
        "v_max3_u32 v6, v6, v36, v37\n"
        "v_smfmac_f32_16x16x64_bf16 v[16:19], v[36:39], a[224:231], v0\n"
        "s_waitcnt lgkmcnt(4)\n"
        "ds_read_b128 v[44:47], v2 offset:25600\n"
        "v_smfmac_f32_16x16x64_bf16 v[24:27], v[36:39], v[48:55], v0\n"
        "ds_read_b128 v[48:51], v2 offset:26624\n"
        "v_max3_u32 v6, v6, v38, v39\n"
        "v_smfmac_f32_16x16x64_bf16 v[12:15], v[36:39], a[88:95], v0\n"
        "ds_read_b128 v[52:55], v2 offset:27648\n"
        "buffer_load_dwordx4 v[76:79], v3, s[40:43], s77 offen offset:1024 sc1\n"
        "v_smfmac_f32_16x16x64_bf16 v[20:23], v[36:39], a[232:239], v0\n"
        "s_waitcnt lgkmcnt(3)\n"
        "v_smfmac_f32_16x16x64_bf16 v[28:31], v[36:39], v[56:63], v0\n"
        "s_waitcnt lgkmcnt(2)\n"
        "ds_read_b128 v[56:59], v2 offset:28672\n"
        "v_smfmac_f32_16x16x64_bf16 v[24:27], v[32:35], v[40:47], v0\n"
        "s_waitcnt lgkmcnt(1)\n"
        "ds_read_b128 v[36:39], v1 offset:7168\n"
        "ds_read_b32 v40, v4 offset:11264\n"
        "v_smfmac_f32_16x16x64_bf16 v[28:31], v[32:35], v[48:55], v0\n"
        "s_waitcnt vmcnt(4)\n"
        "s_barrier\n"
        "ds_read_b32 v7, v5 offset:8192\n"
        "v_smfmac_f32_16x16x64_bf16 v[8:11], v[32:35], a[96:103], v0\n"
        "s_memrealtime s[36:37]\n"
        "ds_read_b128 v[48:51], v1 offset:8192\n"
        "v_max3_u32 v6, v6, v32, v33\n"
        "v_smfmac_f32_16x16x64_bf16 v[16:19], v[32:35], a[240:247], v0\n"
        "ds_read_b128 v[52:55], v4 offset:9216\n"
        "v_max3_u32 v6, v6, v34, v35\n"
        "v_smfmac_f32_16x16x64_bf16 v[12:15], v[32:35], a[104:111], v0\n"
        "v_smfmac_f32_16x16x64_bf16 v[20:23], v[32:35], a[248:255], v0\n"
        "s_waitcnt lgkmcnt(2)\n"
        "s_nop 0\n"
        "v_cmp_eq_u32 vcc, -1, v7\n"
        "v_smfmac_f32_16x16x64_bf16 v[8:11], v[36:39], a[112:119], v0\n"
        "s_waitcnt lgkmcnt(0)\n"
        "s_and_b64 vcc, vcc, 0xffff\n"
        "v_max3_u32 v6, v6, v36, v37\n"
        "v_smfmac_f32_16x16x64_bf16 v[12:15], v[36:39], a[120:127], v0\n"
        "s_cbranch_vccnz .Lch_rt2_%=\n"
        ".Lch_ok2_%=:\n"
        "v_smfmac_f32_16x16x64_bf16 v[8:11], v[48:51], a[128:135], v0\n"
        "s_add_u32 s37, s36, s35\n"
        "ds_read_b128 v[44:47], v2 offset:31744\n"
        "v_readfirstlane_b32 s81, v40\n"
        "v_smfmac_f32_16x16x64_bf16 v[12:15], v[48:51], a[136:143], v0\n"
        "s_cmp_eq_u32 s81, s51\n"
        "v_and_b32 v7, -2, s37\n"
        "v_smfmac_f32_16x16x64_bf16 v[16:19], v[36:39], v[80:87], v0\n"
        "s_cselect_b32 s65, 2147483648, s46\n"
        "v_cvt_pk_bf16_f32 v8, v8, v9\n"
        "v_smfmac_f32_16x16x64_bf16 v[16:19], v[48:51], v[96:103], v0\n"
        "s_mov_b64 exec, 1\n"
        "buffer_store_dword v7, off, s[40:43], s65 sc1\n"
        "s_mov_b64 exec, -1\n"
        "v_cvt_pk_bf16_f32 v9, v10, v11\n"
        "v_smfmac_f32_16x16x64_bf16 v[20:23], v[36:39], v[88:95], v0\n"
        "ds_read_b128 v[60:63], v2 offset:29696\n"
        "v_cvt_pk_bf16_f32 v12, v12, v13\n"
        "v_smfmac_f32_16x16x64_bf16 v[20:23], v[48:51], v[104:111], v0\n"
        "ds_read_b128 v[40:43], v2 offset:30720\n"
        "v_cvt_pk_bf16_f32 v13, v14, v15\n"
        "v_mfma_f32_16x16x16_bf16 v[32:35], v[8:9], v[52:53], 0\n"
        "v_cvt_pk_bf16_f32 v16, v16, v17\n"
        "v_cvt_pk_bf16_f32 v17, v18, v19\n"
        "v_mfma_f32_16x16x16_bf16 v[8:11], v[12:13], v[52:53], 0\n"
        "v_cvt_pk_bf16_f32 v20, v20, v21\n"
        "v_cvt_pk_bf16_f32 v21, v22, v23\n"
        "v_mfma_f32_16x16x16_bf16 v[12:15], v[16:17], v[54:55], 0\n"
        "v_cvt_pk_bf16_f32 v32, v32, v33\n"
        "v_cvt_pk_bf16_f32 v33, v34, v35\n"
        "v_mfma_f32_16x16x16_bf16 v[16:19], v[20:21], v[54:55], 0\n"
        "v_cvt_pk_bf16_f32 v34, v8, v9\n"
        "v_cvt_pk_bf16_f32 v35, v10, v11\n"
        "s_cbranch_scc1 .Lch_slow_%=\n"
        ".Lch_back_%=:\n"
        "s_waitcnt vmcnt(3)\n"
        "v_mfma_f32_16x16x32_bf16 v[8:11], v[64:67], v[32:35], 0\n"
        "v_cvt_pk_bf16_f32 v12, v12, v13\n"
        "v_cvt_pk_bf16_f32 v13, v14, v15\n"
        "v_mfma_f32_16x16x32_bf16 v[20:23], v[68:71], v[32:35], 0\n"
        "v_cvt_pk_bf16_f32 v14, v16, v17\n"
        "v_cvt_pk_bf16_f32 v15, v18, v19\n"
        "v_add_u32 v52, s33, v4\n"
        "v_add_u32 v53, s32, v4\n"
        "v_mfma_f32_16x16x32_bf16 v[16:19], v[12:15], v[32:35], 0\n"
        "v_max3_u32 v6, v6, v38, v39\n"
        "v_max3_u32 v6, v6, v48, v49\n"
        "v_max3_u32 v6, v6, v50, v51\n"
        "v_mov_b32_dpp v8, v20 quad_perm:[0,1,2,3] row_mask:0xf bank_mask:0xc\n"
        "v_mov_b32_dpp v9, v21 quad_perm:[0,1,2,3] row_mask:0xf bank_mask:0xc\n"
        "v_mov_b32_dpp v10, v22 quad_perm:[0,1,2,3] row_mask:0xf bank_mask:0xc\n"
        "v_mov_b32_dpp v11, v23 quad_perm:[0,1,2,3] row_mask:0xf bank_mask:0xc\n"
        "ds_write_b128 v52, v[8:11]\n"
        "v_mov_b32_dpp v16, v17 quad_perm:[0,1,2,3] row_mask:0xf bank_mask:0xc\n"
        "ds_write_b32 v53, v16 offset:4096\n"
        "s_waitcnt lgkmcnt(2)\n"
        "v_smfmac_f32_16x16x64_bf16 v[24:27], v[36:39], v[56:63], v0\n"
        "ds_read_b128 v[68:71], v4 offset:10240\n"
        "v_cmp_eq_u32 vcc, -1, v6\n"
        "v_smfmac_f32_16x16x64_bf16 v[28:31], v[36:39], v[40:47], v0\n"
        "v_mov_b64 v[52:53], s[44:45]\n"
        "v_smfmac_f32_16x16x64_bf16 v[24:27], v[48:51], v[240:247], v0\n"
        "s_cbranch_vccnz .Lch_redo_%=\n"
        "v_smfmac_f32_16x16x64_bf16 v[28:31], v[48:51], v[248:255], v0\n"
        "s_waitcnt lgkmcnt(0)\n"
        "s_barrier\n"
        "ds_read_b128 v[32:35], v4 offset:0\n"
        "ds_read_b128 v[36:39], v4 offset:1024\n"
        "ds_read_b128 v[40:43], v4 offset:2048\n"
        "ds_read_b128 v[44:47], v4 offset:3072\n"
        "ds_read_b128 v[48:51], v4 offset:4096\n"
        "buffer_store_dwordx4 v[12:15], v68, s[40:43], s70 offen sc1\n"
        "v_add_u32 v71, s31, v71\n"
        "v_mov_b64 v[54:55], s[44:45]\n"
        "v_add_f32 v24, v24, v27\n"
        "v_add_f32 v25, v25, v26\n"
        "v_add_f32 v28, v28, v31\n"
        "v_add_f32 v29, v29, v30\n"
        "v_cvt_pk_bf16_f32 v30, v24, v25\n"
        "v_cvt_pk_bf16_f32 v31, v29, v28\n"
        "s_waitcnt lgkmcnt(3)\n"
        "v_pk_add_f32 v[32:33], v[32:33], v[36:37]\n"
        "v_pk_add_f32 v[34:35], v[34:35], v[38:39]\n"
        "s_waitcnt lgkmcnt(1)\n"
        "v_pk_add_f32 v[40:41], v[40:41], v[44:45]\n"
        "v_pk_add_f32 v[42:43], v[42:43], v[46:47]\n"
        "v_pk_add_f32 v[32:33], v[32:33], v[40:41]\n"
        "v_pk_add_f32 v[34:35], v[34:35], v[42:43]\n"
        "s_waitcnt lgkmcnt(0)\n"
        "v_pk_add_f32 v[48:49], v[48:49], v[50:51]\n"
        "v_max3_f32 v36, v32, v33, v34\n"
        "v_add_f32 v48, v48, v49\n"
        "v_max3_f32 v36, v36, v35, v48\n"
        "v_mul_f32 v48, 0x3e0293ee, v48\n"
        "v_mul_f32 v32, 0x3e0293ee, v32\n"
        "v_max_f32_dpp v37, v36, v36 row_ror:8 row_mask:0xf bank_mask:0xf\n"
        "v_max_f32_dpp v38, v36, v36 row_ror:8 row_mask:0xf bank_mask:0xf\n"
        "v_mul_f32 v33, 0x3e0293ee, v33\n"
        "v_mul_f32 v34, 0x3e0293ee, v34\n"
        "v_permlane16_swap_b32 v37, v38\n"
        "v_max_f32 v36, v37, v38\n"
        "v_max_f32 v39, v37, v38\n"
        "v_mul_f32 v35, 0x3e0293ee, v35\n"
        "v_lshlrev_b32 v6, 3, v3\n"
        "v_permlane32_swap_b32 v36, v39\n"
        "v_max_f32 v36, v36, v39\n"
        "v_fmamk_f32 v48, v36, 0xbe0293ee, v48\n"
        "v_exp_f32 v48, v48\n"
        "v_fmamk_f32 v32, v36, 0xbe0293ee, v32\n"
        "v_fmamk_f32 v33, v36, 0xbe0293ee, v33\n"
        "v_mov_b32_dpp v49, v48 row_ror:8 row_mask:0xf bank_mask:0xf\n"
        "v_exp_f32 v32, v32\n"
        "v_cvt_pk_bf16_f32 v49, v48, v49\n"
        "v_exp_f32 v33, v33\n"
        "v_cndmask_b32 v44, v49, 0, s[58:59]\n"
        "v_cndmask_b32 v45, 0, v49, s[58:59]\n"
        "v_fmamk_f32 v34, v36, 0xbe0293ee, v34\n"
        "v_fmamk_f32 v35, v36, 0xbe0293ee, v35\n"
        "v_mfma_f32_16x16x16_bf16 v[56:59], v[52:53], v[44:45], 0\n"
        "v_exp_f32 v34, v34\n"
        "v_exp_f32 v35, v35\n"
        "v_cvt_pk_bf16_f32 v40, v32, v33\n"
        "v_mfma_f32_16x16x16_bf16 v[60:63], v[30:31], v[44:45], 0\n"
        "v_cvt_pk_bf16_f32 v41, v34, v35\n"
        "v_mov_b32_dpp v42, v40 row_ror:8 row_mask:0xf bank_mask:0xf\n"
        "s_waitcnt vmcnt(2)\n"
        "v_mov_b32_dpp v43, v41 row_ror:8 row_mask:0xf bank_mask:0xf\n"
        "v_swap_b32 v76, v78\n"
        "v_swap_b32 v77, v79\n"
        "v_mfma_f32_16x16x32_bf16 v[56:59], v[52:55], v[40:43], v[56:59]\n"
        "v_mov_b64 v[10:11], 0\n"
        "v_mov_b64 v[12:13], 0\n"
        "v_mfma_f32_16x16x32_bf16 v[32:35], v[72:75], v[40:43], v[60:63]\n"
        "s_mov_b32 exec_hi, 0\n"
        "buffer_load_dword v6, v6, s[40:43], s47 offen sc1\n"
        "s_mov_b32 exec_hi, -1\n"
        "v_mfma_f32_16x16x32_bf16 v[48:51], v[76:79], v[40:43], v[60:63]\n"
        "v_rcp_f32 v36, v56\n"
        "v_mov_b64 v[14:15], 0\n"
        "v_mov_b64 v[16:17], 0\n"
        "v_mov_b64 v[18:19], 0\n"
        "v_mul_f32 v32, v32, v36\n"
        "v_mul_f32 v33, v33, v36\n"
        "v_mul_f32 v34, v34, v36\n"
        "v_mul_f32 v35, v35, v36\n"
        "v_mul_f32_dpp v32, v48, v36 quad_perm:[0,1,2,3] row_mask:0xf bank_mask:0xc\n"
        "v_mul_f32_dpp v33, v49, v36 quad_perm:[0,1,2,3] row_mask:0xf bank_mask:0xc\n"
        "v_mul_f32_dpp v34, v50, v36 quad_perm:[0,1,2,3] row_mask:0xf bank_mask:0xc\n"
        "v_mul_f32_dpp v35, v51, v36 quad_perm:[0,1,2,3] row_mask:0xf bank_mask:0xc\n"
        "v_cvt_pk_bf16_f32 v44, v32, v33\n"
        "v_cvt_pk_bf16_f32 v45, v34, v35\n"
        "ds_write_b64 v71, v[44:45]\n"
        "v_and_b32 v7, 0xffffffbf, v1\n"
        "v_cvt_pk_bf16_f32 v24, v24, v25\n"
        "v_cvt_pk_bf16_f32 v26, v28, v29\n"
        "v_mov_b64 v[8:9], 0\n"
        "v_mov_b64 v[20:21], 0\n"
        "v_mov_b64 v[22:23], 0\n"
        "s_waitcnt lgkmcnt(0)\n"
        "s_barrier\n"
        "ds_read_b128 v[28:31], v7 offset:5120\n"
        "ds_read_b128 v[32:35], v7 offset:5184\n"
        "ds_read_b128 v[36:39], v7 offset:6144\n"
        "ds_read_b128 v[40:43], v7 offset:6208\n"
        "v_permlane16_swap_b32 v24, v25\n"
        "v_permlane16_swap_b32 v26, v27\n"
        "buffer_store_dwordx4 v[24:27], v69, s[40:43], s70 offen offset:2048 sc1\n"
        "s_waitcnt lgkmcnt(3)\n"
        "v_smfmac_f32_16x16x64_bf16 v[8:11], v[28:31], v[112:119], v0\n"
        "s_waitcnt lgkmcnt(2)\n"
        "v_smfmac_f32_16x16x64_bf16 v[8:11], v[32:35], v[120:127], v0\n"
        "v_mov_b64 v[24:25], 0\n"
        "s_waitcnt lgkmcnt(1)\n"
        "v_smfmac_f32_16x16x64_bf16 v[8:11], v[36:39], v[128:135], v0\n"
        "v_mov_b64 v[26:27], 0\n"
        "s_waitcnt lgkmcnt(0)\n"
        "v_smfmac_f32_16x16x64_bf16 v[8:11], v[40:43], v[136:143], v0\n"
        "s_barrier\n"
        "v_smfmac_f32_16x16x64_bf16 v[12:15], v[28:31], v[144:151], v0\n"
        "ds_read_b128 v[44:47], v2 offset:32768\n"
        "v_smfmac_f32_16x16x64_bf16 v[12:15], v[32:35], v[152:159], v0\n"
        "ds_read_b128 v[48:51], v2 offset:33792\n"
        "v_cvt_pk_bf16_f32 v8, v8, v9\n"
        "v_smfmac_f32_16x16x64_bf16 v[12:15], v[36:39], v[160:167], v0\n"
        "ds_read_b128 v[52:55], v2 offset:34816\n"
        "v_cvt_pk_bf16_f32 v9, v11, v10\n"
        "v_smfmac_f32_16x16x64_bf16 v[12:15], v[40:43], v[168:175], v0\n"
        "ds_read_b128 v[56:59], v2 offset:35840\n"
        "v_bfi_b32 v8, -2, v8, s51\n"
        "v_smfmac_f32_16x16x64_bf16 v[16:19], v[28:31], v[176:183], v0\n"
        "ds_read_b128 v[60:63], v2 offset:36864\n"
        "v_bfi_b32 v9, -2, v9, s51\n"
        "v_smfmac_f32_16x16x64_bf16 v[16:19], v[32:35], v[184:191], v0\n"
        "v_cvt_pk_bf16_f32 v10, v12, v13\n"
        "v_cvt_pk_bf16_f32 v11, v15, v14\n"
        "v_bfi_b32 v10, -2, v10, s51\n"
        "v_bfi_b32 v11, -2, v11, s51\n"
        "buffer_store_dwordx4 v[8:11], v70, s[40:43], s73 offen sc1\n"
        "v_smfmac_f32_16x16x64_bf16 v[16:19], v[36:39], v[192:199], v0\n"
        "ds_read_b128 v[64:67], v2 offset:37888\n"
        "v_smfmac_f32_16x16x64_bf16 v[16:19], v[40:43], v[200:207], v0\n"
        "ds_read_b128 v[72:75], v2 offset:38912\n"
        "v_smfmac_f32_16x16x64_bf16 v[20:23], v[28:31], v[208:215], v0\n"
        "ds_read_b128 v[76:79], v2 offset:39936\n"
        "v_smfmac_f32_16x16x64_bf16 v[20:23], v[32:35], v[216:223], v0\n"
        "v_cvt_pk_bf16_f32 v16, v16, v17\n"
        "v_smfmac_f32_16x16x64_bf16 v[20:23], v[36:39], v[224:231], v0\n"
        "s_cmp_lt_u32 s48, 2\n"
        "v_cvt_pk_bf16_f32 v17, v19, v18\n"
        "v_smfmac_f32_16x16x64_bf16 v[20:23], v[40:43], v[232:239], v0\n"
        "v_bfi_b32 v16, -2, v16, s51\n"
        "s_cbranch_scc0 .Lch_R23_%=\n"
        "s_waitcnt lgkmcnt(0)\n"
        "v_smfmac_f32_16x16x64_bf16 v[24:27], v[28:31], v[44:51], v0\n"
        "v_bfi_b32 v17, -2, v17, s51\n"
        "v_smfmac_f32_16x16x64_bf16 v[24:27], v[32:35], v[52:59], v0\n"
        "v_cvt_pk_bf16_f32 v18, v20, v21\n"
        "v_cvt_pk_bf16_f32 v19, v23, v22\n"
        "v_bfi_b32 v18, -2, v18, s51\n"
        "v_bfi_b32 v19, -2, v19, s51\n"
        "buffer_store_dwordx4 v[16:19], v70, s[40:43], s74 offen sc1\n"
        "v_smfmac_f32_16x16x64_bf16 v[24:27], v[36:39], v[60:67], v0\n"
        "v_smfmac_f32_16x16x64_bf16 v[24:27], v[40:43], v[72:79], v0\n"
        "v_add_u32 v12, 114688, v3\n"
        "ds_read_b128 v[44:47], v12\n"
        "ds_read_b32 v50, v12 offset:1024\n"
        "s_waitcnt vmcnt(3)\n"
        "s_nop 3\n"
        "v_cvt_pk_bf16_f32 v24, v24, v25\n"
        "v_cvt_pk_bf16_f32 v25, v27, v26\n"
        "v_bfi_b32 v24, -2, v24, s51\n"
        "v_bfi_b32 v25, -2, v25, s51\n"
        "s_nop 0\n"
        "v_permlane32_swap_b32 v24, v26\n"
        "v_permlane32_swap_b32 v25, v27\n"
        "s_mov_b32 exec_hi, 0\n"
        "buffer_store_dwordx4 v[24:27], v70, s[40:43], s75 offen sc1\n"
        "s_memrealtime s[90:91]\n"
        "s_cmp_eq_u32 s88, 0\n"
        "s_cbranch_scc1 .Lch_R_%=\n"
        "s_mov_b32 exec_hi, -1\n"
        "s_waitcnt lgkmcnt(0)\n"
        "s_sub_u32 s65, s90, s36\n"
        "s_min_u32 s65, s65, 120\n"
        "s_lshl_b32 s65, s65, 3\n"
        "s_cmp_eq_u32 s51, 0\n"
        "s_cselect_b32 s38, s65, s38\n"
        "s_sub_u32 s65, s65, s38\n"
        "s_ashr_i32 s65, s65, 2\n"
        "s_add_u32 s38, s38, s65\n"
        "s_lshr_b32 s35, s38, 3\n"
        "s_waitcnt vmcnt(3)\n"
        "v_mov_b32 v12, s51\n"
        "s_mov_b64 exec, 1\n"
        "buffer_store_dword v12, off, s[40:43], s79 sc1\n"
        "s_mov_b64 exec, -1\n"
        "s_mov_b32 s65, s63\n"
        ".Lch_d4_%=:\n"
        "s_sub_u32 s65, s65, 1\n"
        "s_cbranch_scc0 .Lch_d4_%=\n"
        ".Lch_idle2_%=:\n"
        "s_mov_b64 exec, 1\n"
        "buffer_load_dword v7, off, s[40:43], s78 sc1\n"
        "s_mov_b64 exec, -1\n"
        "s_branch .Lch_nog_%=\n"
        ".Lch_R23_%=:\n"
        "s_waitcnt vmcnt(2)\n"
        "v_bfi_b32 v17, -2, v17, s51\n"
        "s_nop 3\n"
        "v_cvt_pk_bf16_f32 v18, v20, v21\n"
        "v_cvt_pk_bf16_f32 v19, v23, v22\n"
        "v_bfi_b32 v18, -2, v18, s51\n"
        "v_bfi_b32 v19, -2, v19, s51\n"
        "buffer_store_dwordx4 v[16:19], v70, s[40:43], s74 offen sc1\n"
        "s_memrealtime s[90:91]\n"
        "s_mov_b32 exec_hi, 0\n"
        ".Lch_R_%=:\n"
        ".Lch_st1_%=:\n"
        "v_cmp_eq_u32 vcc, -1, v6\n"
        "s_cbranch_vccnz .Lch_stwait_%=\n"
        ".Lch_st2_%=:\n"
        "s_waitcnt lgkmcnt(0)\n"
        "s_sub_u32 s65, s90, s60\n"
        "s_memrealtime s[90:91]\n"
        "v_sub_u32 v12, s65, v6\n"
        "v_cmp_lt_u32 vcc, 0xfffffc18, v12\n"
        "s_cbranch_vccnz .Lch_st2_%=\n"
        "s_mov_b32 exec_hi, -1\n"
        ".Lch_rl_%=:\n"
        "buffer_load_dwordx4 v[28:31], v3, s[40:43], s76 offen offset:0 sc1\n"
        "buffer_load_dwordx4 v[32:35], v3, s[40:43], s76 offen offset:1024 sc1\n"
        "buffer_load_dwordx4 v[36:39], v3, s[40:43], s76 offen offset:2048 sc1\n"
        "buffer_load_dwordx4 v[40:43], v3, s[40:43], s76 offen offset:3072 sc1\n"
        "v_mov_b32 v10, s51\n"
        "s_waitcnt vmcnt(6)\n"
        "s_mov_b64 exec, 1\n"
        "buffer_store_dword v10, off, s[40:43], s79 sc1\n"
        "s_mov_b64 exec, -1\n"
        "v_subrev_u32 v8, s36, v6\n"
        "v_subrev_u32 v9, s36, v6\n"
        "s_nop 0\n"
        "v_max_i32_dpp v8, v8, v8 row_ror:8 row_mask:0xf bank_mask:0xf\n"
        "v_min_i32_dpp v9, v9, v9 row_ror:8 row_mask:0xf bank_mask:0xf\n"
        "s_nop 0\n"
        "v_max_i32_dpp v8, v8, v8 row_ror:4 row_mask:0xf bank_mask:0xf\n"
        "v_min_i32_dpp v9, v9, v9 row_ror:4 row_mask:0xf bank_mask:0xf\n"
        "s_nop 0\n"
        "v_max_i32_dpp v8, v8, v8 row_ror:2 row_mask:0xf bank_mask:0xf\n"
        "v_min_i32_dpp v9, v9, v9 row_ror:2 row_mask:0xf bank_mask:0xf\n"
        "s_nop 0\n"
        "v_max_i32_dpp v8, v8, v8 row_ror:1 row_mask:0xf bank_mask:0xf\n"
        "v_min_i32_dpp v9, v9, v9 row_ror:1 row_mask:0xf bank_mask:0xf\n"
        "s_waitcnt lgkmcnt(0)\n"
        "v_readlane_b32 s65, v8, 0\n"
        "v_readlane_b32 s66, v8, 16\n"
        "v_readlane_b32 s90, v9, 0\n"
        "v_readlane_b32 s91, v9, 16\n"
        "s_max_i32 s65, s65, s66\n"
        "s_min_i32 s90, s90, s91\n"
        "s_sub_u32 s90, s65, s90\n"
        "s_add_u32 s65, s65, s36\n"
        "s_add_u32 s65, s65, s34\n"
        "s_cmp_gt_u32 s90, s56\n"
        "s_cselect_b32 s90, s57, 0\n"
        "s_add_u32 s82, s65, s90\n"
        "s_and_b32 s71, s51, 3\n"
        "s_mul_i32 s71, s71, 81920\n"
        "s_add_u32 s39, s71, s85\n"
        "v_mov_b64 v[64:65], -1\n"
        "v_mov_b64 v[66:67], -1\n"
        "s_waitcnt vmcnt(4)\n"
        "v_mfma_f32_16x16x32_bf16 v[12:15], v[28:31], v[44:47], 0\n"
        "v_bitop3_b32 v10, v28, v29, s51 bitop3:0x7e\n"
        "v_bitop3_b32 v11, v30, v31, s51 bitop3:0x7e\n"
        "v_or_b32 v8, v10, v11\n"
        "s_waitcnt vmcnt(3)\n"
        "v_mfma_f32_16x16x32_bf16 v[12:15], v[32:35], v[44:47], v[12:15]\n"
        "v_bitop3_b32 v10, v32, v33, s51 bitop3:0x7e\n"
        "v_bitop3_b32 v11, v34, v35, s51 bitop3:0x7e\n"
        "v_or3_b32 v8, v8, v10, v11\n"
        "s_waitcnt vmcnt(2)\n"
        "v_mfma_f32_16x16x32_bf16 v[12:15], v[36:39], v[44:47], v[12:15]\n"
        "v_bitop3_b32 v10, v36, v37, s51 bitop3:0x7e\n"
        "v_bitop3_b32 v11, v38, v39, s51 bitop3:0x7e\n"
        "v_or3_b32 v8, v8, v10, v11\n"
        "s_waitcnt vmcnt(1)\n"
        "v_mfma_f32_16x16x32_bf16 v[12:15], v[40:43], v[44:47], v[12:15]\n"
        "v_bitop3_b32 v10, v40, v41, s51 bitop3:0x7e\n"
        "v_bitop3_b32 v11, v42, v43, s51 bitop3:0x7e\n"
        "v_or3_b32 v8, v8, v10, v11\n"
        "v_and_b32 v8, 1, v8\n"
        "v_cmp_ne_u32 vcc, 0, v8\n"
        "s_cbranch_vccnz .Lch_rretry_%=\n"
        "s_nop 1\n"
        "v_permlane32_swap_b32 v12, v14\n"
        "v_permlane32_swap_b32 v13, v15\n"
        "v_add_f32 v12, v12, v14\n"
        "v_add_f32 v13, v13, v15\n"
        "v_cvt_pk_bf16_f32 v16, v12, v13\n"
        "v_cvt_pk_bf16_f32 v18, v12, v13\n"
        "s_nop 1\n"
        "v_permlane16_swap_b32 v16, v18\n"
        "s_nop 1\n"
        "v_permlane32_swap_b32 v16, v17\n"
        "v_permlane32_swap_b32 v18, v19\n"
        "buffer_store_dwordx4 v[16:19], v50, s[40:43], s39 offen sc1\n"
        "buffer_store_dwordx4 v[64:67], v50, s[40:43], s72 offen sc1\n"
        "s_mov_b64 exec, 1\n"
        "buffer_load_dword v7, off, s[40:43], s78 sc1\n"
        "s_mov_b64 exec, -1\n"
        "s_mov_b32 s72, s39\n"
        "v_mov_b32 v32, v49\n"
        "s_cmp_eq_u32 s48, 2\n"
        "s_cbranch_scc0 .Lch_nog_%=\n"
        "s_and_b32 s66, s33, 1024\n"
        "s_lshl_b32 s66, s66, 2\n"
        "s_add_u32 m0, s66, 151552\n"
        "s_add_u32 s66, s54, s71\n"
        ".Lch_gwa_%=:\n"
        "s_memrealtime s[90:91]\n"
        "s_waitcnt lgkmcnt(0)\n"
        "s_sub_u32 s65, s90, s82\n"
        "s_cmp_gt_u32 s65, -1000\n"
        "s_cbranch_scc1 .Lch_gwa_%=\n"
        "s_mov_b32 s65, s61\n"
        ".Lch_d2a_%=:\n"
        "s_sub_u32 s65, s65, 1\n"
        "s_cbranch_scc0 .Lch_d2a_%=\n"
        "buffer_load_dwordx4 v3, s[40:43], s66 offen offset:0 sc1 lds\n"
        "buffer_load_dwordx4 v3, s[40:43], s66 offen offset:1024 sc1 lds\n"
        "buffer_load_dwordx4 v3, s[40:43], s66 offen offset:2048 sc1 lds\n"
        "buffer_load_dwordx4 v3, s[40:43], s66 offen offset:3072 sc1 lds\n"
        "s_add_u32 s51, s51, 1\n"
        "s_add_u32 s80, s80, 2\n"
        "s_bitcmp1_b64 s[52:53], s51\n"
        "s_cselect_b32 s65, 8192, 0\n"
        "s_lshl_b32 s68, s51, 21\n"
        "s_add_u32 s68, s68, s65\n"
        "s_add_u32 s68, s68, 2097152\n"
        "s_add_u32 s70, s68, s84\n"
        "s_add_u32 s69, s68, s83\n"
        "s_add_u32 s47, s68, 2068480\n"
        "s_add_u32 s46, s47, s62\n"
        "ds_read_b128 v[40:43], v2 offset:0\n"
        "ds_read_b128 v[44:47], v2 offset:1024\n"
        "ds_read_b128 v[48:51], v2 offset:2048\n"
        "ds_read_b128 v[52:55], v2 offset:3072\n"
        "ds_read_b128 v[56:59], v2 offset:4096\n"
        "ds_read_b128 v[60:63], v2 offset:5120\n"
        "v_mov_b64 v[8:9], 0\n"
        "v_mov_b64 v[10:11], 0\n"
        "v_mov_b64 v[12:13], 0\n"
        "v_mov_b64 v[14:15], 0\n"
        "v_mov_b64 v[16:17], 0\n"
        "v_mov_b64 v[18:19], 0\n"
        "v_mov_b64 v[20:21], 0\n"
        "v_mov_b64 v[22:23], 0\n"
        "v_mov_b64 v[24:25], 0\n"
        "v_mov_b64 v[26:27], 0\n"
        "v_mov_b64 v[28:29], 0\n"
        "v_mov_b64 v[30:31], 0\n"
        "v_mov_b32 v6, 0\n"
        "s_waitcnt vmcnt(4)\n"
        "v_readfirstlane_b32 s65, v7\n"
        "s_cmp_eq_u32 s51, 63\n"
        "s_cselect_b32 s65, -2, s65\n"
        "s_add_u32 s89, s69, 524288\n"
        "s_mov_b32 s30, s69\n"
        "s_mov_b32 s77, s89\n"
        "s_cmp_ge_i32 s65, s80\n"
        "s_cbranch_scc1 .Lch_fasta_%=\n"
        "s_mov_b32 s30, 2147483648\n"
        "s_mov_b32 s89, 2147483648\n"
        "s_sub_u32 s77, s78, 1024\n"
        "v_mov_b32 v7, s51\n"
        "ds_write_b32 v4, v7 offset:11264\n"
        ".Lch_fasta_%=:\n"
        "s_waitcnt vmcnt(0)\n"
        "s_branch .Lch_A_%=\n"
        ".Lch_nog_%=:\n"
        "s_add_u32 s51, s51, 1\n"
        "s_add_u32 s80, s80, 2\n"
        "s_bitcmp1_b64 s[52:53], s51\n"
        "s_cselect_b32 s65, 8192, 0\n"
        "s_lshl_b32 s68, s51, 21\n"
        "s_add_u32 s68, s68, s65\n"
        "s_add_u32 s68, s68, 2097152\n"
        "s_add_u32 s70, s68, s84\n"
        "s_add_u32 s69, s68, s83\n"
        "s_add_u32 s47, s68, 2068480\n"
        "s_add_u32 s46, s47, s62\n"
        "ds_read_b128 v[40:43], v2 offset:0\n"
        "ds_read_b128 v[44:47], v2 offset:1024\n"
        "ds_read_b128 v[48:51], v2 offset:2048\n"
        "ds_read_b128 v[52:55], v2 offset:3072\n"
        "ds_read_b128 v[56:59], v2 offset:4096\n"
        "ds_read_b128 v[60:63], v2 offset:5120\n"
        "v_mov_b64 v[8:9], 0\n"
        "v_mov_b64 v[10:11], 0\n"
        "v_mov_b64 v[12:13], 0\n"
        "v_mov_b64 v[14:15], 0\n"
        "v_mov_b64 v[16:17], 0\n"
        "v_mov_b64 v[18:19], 0\n"
        "v_mov_b64 v[20:21], 0\n"
        "v_mov_b64 v[22:23], 0\n"
        "v_mov_b64 v[24:25], 0\n"
        "v_mov_b64 v[26:27], 0\n"
        "v_mov_b64 v[28:29], 0\n"
        "v_mov_b64 v[30:31], 0\n"
        "v_mov_b32 v6, 0\n"
        "s_cmp_eq_u32 s48, 3\n"
        "s_cselect_b32 s65, s51, 0\n"
        "s_cmp_eq_u32 s65, 0\n"
        "s_cbranch_scc1 .Lch_A2_%=\n"
        "s_and_b32 s66, s33, 1024\n"
        "s_lshl_b32 s66, s66, 2\n"
        "s_add_u32 m0, s66, 151552\n"
        "s_add_u32 s66, s54, s71\n"
        ".Lch_gwb_%=:\n"
        "s_memrealtime s[90:91]\n"
        "s_waitcnt lgkmcnt(0)\n"
        "s_sub_u32 s65, s90, s82\n"
        "s_cmp_gt_u32 s65, -1000\n"
        "s_cbranch_scc1 .Lch_gwb_%=\n"
        "s_mov_b32 s65, s61\n"
        ".Lch_d2b_%=:\n"
        "s_sub_u32 s65, s65, 1\n"
        "s_cbranch_scc0 .Lch_d2b_%=\n"
        "buffer_load_dwordx4 v3, s[40:43], s66 offen offset:0 sc1 lds\n"
        "buffer_load_dwordx4 v3, s[40:43], s66 offen offset:1024 sc1 lds\n"
        "buffer_load_dwordx4 v3, s[40:43], s66 offen offset:2048 sc1 lds\n"
        "buffer_load_dwordx4 v3, s[40:43], s66 offen offset:3072 sc1 lds\n"
        "s_mov_b32 m0, 159744\n"
        "s_add_u32 s66, s71, s55\n"
        "buffer_load_dwordx4 v32, s[40:43], s66 offen sc1 lds\n"
        "s_waitcnt vmcnt(5)\n"
        "s_branch .Lch_A3_%=\n"
        ".Lch_A2_%=:\n"
        "s_waitcnt vmcnt(0)\n"
        ".Lch_A3_%=:\n"
        "v_readfirstlane_b32 s65, v7\n"
        "s_cmp_eq_u32 s51, 63\n"
        "s_cselect_b32 s65, -2, s65\n"
        "s_add_u32 s89, s69, 524288\n"
        "s_mov_b32 s30, s69\n"
        "s_mov_b32 s77, s89\n"
        "s_cmp_ge_i32 s65, s80\n"
        "s_cbranch_scc1 .Lch_fastb_%=\n"
        "s_mov_b32 s30, 2147483648\n"
        "s_mov_b32 s89, 2147483648\n"
        "s_sub_u32 s77, s78, 1024\n"
        "v_mov_b32 v7, s51\n"
        "ds_write_b32 v4, v7 offset:11264\n"
        ".Lch_fastb_%=:\n"
        ".Lch_A_%=:\n"
        "s_waitcnt lgkmcnt(0)\n"
        "s_barrier\n"
        "s_branch .Lch_Q_%=\n"
        ".Lch_rretry_%=:\n"
        "s_waitcnt vmcnt(0)\n"
        "s_add_u32 s64, s64, 1\n"
        "s_and_b32 s65, s64, 255\n"
        "s_cbranch_scc1 .Lch_rr_go_%=\n"
        "s_mov_b32 s65, 49152\n"
        "s_cmp_lt_u32 s64, 1048576\n"
        "s_cbranch_scc1 .Lch_rr_chk_%=\n"
        "v_mov_b32 v8, 1\n"
        "buffer_store_dword v8, off, s[40:43], s65 sc1\n"
        "s_waitcnt vmcnt(0)\n"
        ".Lch_rr_chk_%=:\n"
        "buffer_load_dword v8, off, s[40:43], s65 sc1\n"
        "s_waitcnt vmcnt(0)\n"
        "v_readfirstlane_b32 s65, v8\n"
        "s_nop 0\n"
        "s_cmp_lg_u32 s65, 1\n"
        "s_cbranch_scc1 .Lch_rr_go_%=\n"
        "s_endpgm\n"
        ".Lch_rr_go_%=:\n"
        "s_branch .Lch_rl_%=\n"
        ".Lch_stwait_%=:\n"
        "s_add_u32 s64, s64, 1\n"
        "s_and_b32 s65, s64, 255\n"
        "s_cbranch_scc1 .Lch_sw_go_%=\n"
        "s_mov_b32 s65, 49152\n"
        "s_cmp_lt_u32 s64, 1048576\n"
        "s_cbranch_scc1 .Lch_sw_chk_%=\n"
        "v_mov_b32 v12, 1\n"
        "buffer_store_dword v12, off, s[40:43], s65 sc1\n"
        "s_waitcnt vmcnt(0)\n"
        ".Lch_sw_chk_%=:\n"
        "buffer_load_dword v12, off, s[40:43], s65 sc1\n"
        "s_waitcnt vmcnt(0)\n"
        "v_readfirstlane_b32 s65, v12\n"
        "s_nop 0\n"
        "s_cmp_lg_u32 s65, 1\n"
        "s_cbranch_scc1 .Lch_sw_go_%=\n"
        "s_endpgm\n"
        ".Lch_sw_go_%=:\n"
        "s_sleep 1\n"
        "v_lshlrev_b32 v6, 3, v3\n"
        "buffer_load_dword v6, v6, s[40:43], s47 offen sc1\n"
        "s_waitcnt vmcnt(0)\n"
        "s_branch .Lch_st1_%=\n"
        ".Lch_rt0_%=:\n"
        "s_waitcnt lgkmcnt(0)\n"
        "s_barrier\n"
        "s_add_u32 s64, s64, 1\n"
        "s_and_b32 s65, s64, 255\n"
        "s_cbranch_scc1 .Lch_rt0_go_%=\n"
        "s_mov_b32 s65, 49152\n"
        "s_cmp_lt_u32 s64, 1048576\n"
        "s_cbranch_scc1 .Lch_rt0_chk_%=\n"
        "v_mov_b32 v7, 1\n"
        "buffer_store_dword v7, off, s[40:43], s65 sc1\n"
        "s_waitcnt vmcnt(0)\n"
        ".Lch_rt0_chk_%=:\n"
        "buffer_load_dword v7, off, s[40:43], s65 sc1\n"
        "s_waitcnt vmcnt(0)\n"
        "v_readfirstlane_b32 s65, v7\n"
        "s_nop 0\n"
        "s_cmp_lg_u32 s65, 1\n"
        "s_cbranch_scc1 .Lch_rt0_go_%=\n"
        "s_endpgm\n"
        ".Lch_rt0_go_%=:\n"
        "s_cmp_eq_u32 s48, 2\n"
        "s_cbranch_scc0 .Lch_rw0_%=\n"
        "s_and_b32 s66, s33, 1024\n"
        "s_lshl_b32 s66, s66, 2\n"
        "s_add_u32 m0, s66, 151552\n"
        "s_add_u32 s66, s54, s71\n"
        "buffer_load_dwordx4 v3, s[40:43], s66 offen offset:0 sc1 lds\n"
        "buffer_load_dwordx4 v3, s[40:43], s66 offen offset:1024 sc1 lds\n"
        "buffer_load_dwordx4 v3, s[40:43], s66 offen offset:2048 sc1 lds\n"
        "buffer_load_dwordx4 v3, s[40:43], s66 offen offset:3072 sc1 lds\n"
        "s_waitcnt vmcnt(0)\n"
        ".Lch_rw0_%=:\n"
        "s_waitcnt vmcnt(0)\n"
        "ds_read_b128 v[40:43], v2 offset:0\n"
        "ds_read_b128 v[44:47], v2 offset:1024\n"
        "ds_read_b128 v[48:51], v2 offset:2048\n"
        "ds_read_b128 v[52:55], v2 offset:3072\n"
        "ds_read_b128 v[56:59], v2 offset:4096\n"
        "ds_read_b128 v[60:63], v2 offset:5120\n"
        "v_mov_b64 v[8:9], 0\n"
        "v_mov_b64 v[10:11], 0\n"
        "v_mov_b64 v[12:13], 0\n"
        "v_mov_b64 v[14:15], 0\n"
        "v_mov_b64 v[16:17], 0\n"
        "v_mov_b64 v[18:19], 0\n"
        "v_mov_b64 v[20:21], 0\n"
        "v_mov_b64 v[22:23], 0\n"
        "v_mov_b64 v[24:25], 0\n"
        "v_mov_b64 v[26:27], 0\n"
        "v_mov_b64 v[28:29], 0\n"
        "v_mov_b64 v[30:31], 0\n"
        "v_mov_b32 v6, 0\n"
        "s_branch .Lch_A_%=\n"
        ".Lch_rt1_%=:\n"
        "s_waitcnt lgkmcnt(0)\n"
        "s_barrier\n"
        "s_add_u32 s64, s64, 1\n"
        "s_and_b32 s65, s64, 255\n"
        "s_cbranch_scc1 .Lch_rt1_go_%=\n"
        "s_mov_b32 s65, 49152\n"
        "s_cmp_lt_u32 s64, 1048576\n"
        "s_cbranch_scc1 .Lch_rt1_chk_%=\n"
        "v_mov_b32 v7, 1\n"
        "buffer_store_dword v7, off, s[40:43], s65 sc1\n"
        "s_waitcnt vmcnt(0)\n"
        ".Lch_rt1_chk_%=:\n"
        "buffer_load_dword v7, off, s[40:43], s65 sc1\n"
        "s_waitcnt vmcnt(0)\n"
        "v_readfirstlane_b32 s65, v7\n"
        "s_nop 0\n"
        "s_cmp_lg_u32 s65, 1\n"
        "s_cbranch_scc1 .Lch_rt1_go_%=\n"
        "s_endpgm\n"
        ".Lch_rt1_go_%=:\n"
        "s_cmp_eq_u32 s48, 3\n"
        "s_cbranch_scc0 .Lch_rw1_%=\n"
        "s_and_b32 s66, s33, 1024\n"
        "s_lshl_b32 s66, s66, 2\n"
        "s_add_u32 m0, s66, 151552\n"
        "s_add_u32 s66, s54, s71\n"
        "buffer_load_dwordx4 v3, s[40:43], s66 offen offset:0 sc1 lds\n"
        "buffer_load_dwordx4 v3, s[40:43], s66 offen offset:1024 sc1 lds\n"
        "buffer_load_dwordx4 v3, s[40:43], s66 offen offset:2048 sc1 lds\n"
        "buffer_load_dwordx4 v3, s[40:43], s66 offen offset:3072 sc1 lds\n"
        "s_waitcnt vmcnt(0)\n"
        ".Lch_rw1_%=:\n"
        "s_barrier\n"
        "ds_read_b32 v7, v5 offset:4096\n"
        "ds_read_b128 v[32:35], v1 offset:4096\n"
        "s_waitcnt lgkmcnt(0)\n"
        "v_cmp_eq_u32 vcc, -1, v7\n"
        "s_cbranch_vccnz .Lch_rt1_%=\n"
        "s_branch .Lch_ok1_%=\n"
        ".Lch_rt2_%=:\n"
        "s_waitcnt lgkmcnt(0)\n"
        "s_barrier\n"
        "s_add_u32 s64, s64, 1\n"
        "s_and_b32 s65, s64, 255\n"
        "s_cbranch_scc1 .Lch_rt2_go_%=\n"
        "s_mov_b32 s65, 49152\n"
        "s_cmp_lt_u32 s64, 1048576\n"
        "s_cbranch_scc1 .Lch_rt2_chk_%=\n"
        "v_mov_b32 v7, 1\n"
        "buffer_store_dword v7, off, s[40:43], s65 sc1\n"
        "s_waitcnt vmcnt(0)\n"
        ".Lch_rt2_chk_%=:\n"
        "buffer_load_dword v7, off, s[40:43], s65 sc1\n"
        "s_waitcnt vmcnt(0)\n"
        "v_readfirstlane_b32 s65, v7\n"
        "s_nop 0\n"
        "s_cmp_lg_u32 s65, 1\n"
        "s_cbranch_scc1 .Lch_rt2_go_%=\n"
        "s_endpgm\n"
        ".Lch_rt2_go_%=:\n"
        "s_cmp_eq_u32 s48, 3\n"
        "s_cbranch_scc0 .Lch_rw2_%=\n"
        "ds_read_b32 v7, v2 offset:33796\n"
        "s_waitcnt lgkmcnt(0)\n"
        "s_mov_b32 m0, 159744\n"
        "s_add_u32 s66, s71, s55\n"
        "buffer_load_dwordx4 v7, s[40:43], s66 offen sc1 lds\n"
        "s_waitcnt vmcnt(0)\n"
        ".Lch_rw2_%=:\n"
        "s_barrier\n"
        "ds_read_b32 v7, v5 offset:8192\n"
        "ds_read_b128 v[48:51], v1 offset:8192\n"
        "s_memrealtime s[36:37]\n"
        "s_waitcnt lgkmcnt(0)\n"
        "v_cmp_eq_u32 vcc, -1, v7\n"
        "s_nop 0\n"
        "s_and_b64 vcc, vcc, 0xffff\n"
        "s_cbranch_vccnz .Lch_rt2_%=\n"
        "s_branch .Lch_ok2_%=\n"
        ".Lch_redo_%=:\n"
        "s_waitcnt vmcnt(0) lgkmcnt(0)\n"
        "s_barrier\n"
        "s_add_u32 s64, s64, 1\n"
        "s_and_b32 s65, s64, 255\n"
        "s_cbranch_scc1 .Lch_rd_go_%=\n"
        "s_mov_b32 s65, 49152\n"
        "s_cmp_lt_u32 s64, 1048576\n"
        "s_cbranch_scc1 .Lch_rd_chk_%=\n"
        "v_mov_b32 v7, 1\n"
        "buffer_store_dword v7, off, s[40:43], s65 sc1\n"
        "s_waitcnt vmcnt(0)\n"
        ".Lch_rd_chk_%=:\n"
        "buffer_load_dword v7, off, s[40:43], s65 sc1\n"
        "s_waitcnt vmcnt(0)\n"
        "v_readfirstlane_b32 s65, v7\n"
        "s_nop 0\n"
        "s_cmp_lg_u32 s65, 1\n"
        "s_cbranch_scc1 .Lch_rd_go_%=\n"
        "s_endpgm\n"
        ".Lch_rd_go_%=:\n"
        "s_cmp_lt_u32 s48, 2\n"
        "s_cbranch_scc1 .Lch_rd2_%=\n"
        "s_and_b32 s66, s33, 1024\n"
        "s_lshl_b32 s66, s66, 2\n"
        "s_add_u32 m0, s66, 151552\n"
        "s_add_u32 s66, s54, s71\n"
        "buffer_load_dwordx4 v3, s[40:43], s66 offen offset:0 sc1 lds\n"
        "buffer_load_dwordx4 v3, s[40:43], s66 offen offset:1024 sc1 lds\n"
        "buffer_load_dwordx4 v3, s[40:43], s66 offen offset:2048 sc1 lds\n"
        "buffer_load_dwordx4 v3, s[40:43], s66 offen offset:3072 sc1 lds\n"
        "ds_read_b32 v7, v2 offset:33796\n"
        "s_waitcnt lgkmcnt(0)\n"
        "s_mov_b32 m0, 159744\n"
        "s_add_u32 s66, s71, s55\n"
        "buffer_load_dwordx4 v7, s[40:43], s66 offen sc1 lds\n"
        ".Lch_rd2_%=:\n"
        "s_nop 7\n"
        "ds_read_b128 v[40:43], v2 offset:0\n"
        "ds_read_b128 v[44:47], v2 offset:1024\n"
        "ds_read_b128 v[48:51], v2 offset:2048\n"
        "ds_read_b128 v[52:55], v2 offset:3072\n"
        "ds_read_b128 v[56:59], v2 offset:4096\n"
        "ds_read_b128 v[60:63], v2 offset:5120\n"
        "v_mov_b64 v[8:9], 0\n"
        "v_mov_b64 v[10:11], 0\n"
        "v_mov_b64 v[12:13], 0\n"
        "v_mov_b64 v[14:15], 0\n"
        "v_mov_b64 v[16:17], 0\n"
        "v_mov_b64 v[18:19], 0\n"
        "v_mov_b64 v[20:21], 0\n"
        "v_mov_b64 v[22:23], 0\n"
        "v_mov_b64 v[24:25], 0\n"
        "v_mov_b64 v[26:27], 0\n"
        "v_mov_b64 v[28:29], 0\n"
        "v_mov_b64 v[30:31], 0\n"
        "v_mov_b32 v6, 0\n"
        "s_waitcnt vmcnt(0)\n"
        "s_branch .Lch_A_%=\n"
        ".Lch_slow_%=:\n"
        "v_max3_u32 v6, v6, v38, v39\n"
        "v_max3_u32 v6, v6, v48, v49\n"
        "v_max3_u32 v6, v6, v50, v51\n"
        "v_cmp_eq_u32 vcc, -1, v6\n"
        "s_cbranch_vccnz .Lch_redo_%=\n"
        "s_waitcnt vmcnt(0) lgkmcnt(0)\n"
        "s_cmp_eq_u32 s30, s69\n"
        "s_cbranch_scc1 .Lch_slow3_%=\n"
        "v_readfirstlane_b32 s65, v76\n"
        "s_cmp_eq_u32 s51, 63\n"
        "s_cselect_b32 s65, -2, s65\n"
        "s_cmp_ge_i32 s65, s80\n"
        "s_cbranch_scc1 .Lch_kvw_ok_%=\n"
        "ds_read_b128 v[68:71], v4 offset:10240\n"
        "v_mov_b64 v[72:73], v[24:25]\n"
        "v_mov_b64 v[74:75], v[26:27]\n"
        "v_mov_b64 v[76:77], v[28:29]\n"
        "v_mov_b64 v[78:79], v[30:31]\n"
        "v_cvt_pk_bf16_f32 v64, v12, v13\n"
        "v_cvt_pk_bf16_f32 v65, v14, v15\n"
        "v_cvt_pk_bf16_f32 v66, v16, v17\n"
        "v_cvt_pk_bf16_f32 v67, v18, v19\n"
        "v_smfmac_f32_16x16x64_bf16 v[72:75], v[36:39], v[56:63], v0\n"
        "v_smfmac_f32_16x16x64_bf16 v[76:79], v[36:39], v[40:47], v0\n"
        "v_smfmac_f32_16x16x64_bf16 v[72:75], v[48:51], v[240:247], v0\n"
        "v_smfmac_f32_16x16x64_bf16 v[76:79], v[48:51], v[248:255], v0\n"
        "s_waitcnt lgkmcnt(0)\n"
        "buffer_store_dwordx4 v[64:67], v68, s[40:43], s70 offen sc1\n"
        "s_nop 5\n"
        "v_add_f32 v72, v72, v75\n"
        "v_add_f32 v73, v73, v74\n"
        "v_add_f32 v76, v76, v79\n"
        "v_add_f32 v77, v77, v78\n"
        "v_cvt_pk_bf16_f32 v72, v72, v73\n"
        "v_cvt_pk_bf16_f32 v74, v76, v77\n"
        "s_nop 1\n"
        "v_permlane16_swap_b32 v72, v73\n"
        "v_permlane16_swap_b32 v74, v75\n"
        "buffer_store_dwordx4 v[72:75], v69, s[40:43], s70 offen offset:2048 sc1\n"
        "s_mov_b64 exec, 1\n"
        "buffer_load_dword v64, off, s[40:43], s78 sc1\n"
        "s_mov_b64 exec, -1\n"
        "s_waitcnt vmcnt(0)\n"
        "v_mov_b32 v7, s51\n"
        "s_mov_b64 exec, 1\n"
        "buffer_store_dword v7, off, s[40:43], s79 sc1\n"
        "s_mov_b64 exec, -1\n"
        "s_cmp_eq_u32 s51, 63\n"
        "s_cbranch_scc0 .Lch_slow2_%=\n"
        "s_endpgm\n"
        ".Lch_slow2_%=:\n"
        "s_mov_b32 s70, 241172480\n"
        "v_readfirstlane_b32 s65, v64\n"
        "s_nop 0\n"
        "s_cmp_ge_i32 s65, s80\n"
        "s_cbranch_scc1 .Lch_kvw_ok_%=\n"
        "s_mov_b32 s64, 0\n"
        ".Lch_kvw_top_%=:\n"
        "s_mov_b64 exec, 1\n"
        "buffer_load_dword v7, off, s[40:43], s78 sc1\n"
        "s_mov_b64 exec, -1\n"
        "s_waitcnt vmcnt(0)\n"
        "v_cmp_gt_i32 vcc, s80, v7\n"
        "s_nop 0\n"
        "s_and_b64 vcc, vcc, 1\n"
        "s_cbranch_vccz .Lch_kvw_ok_%=\n"
        "s_add_u32 s64, s64, 1\n"
        "s_and_b32 s65, s64, 255\n"
        "s_cbranch_scc1 .Lch_kvw_go_%=\n"
        "s_mov_b32 s65, 49152\n"
        "s_cmp_lt_u32 s64, 1048576\n"
        "s_cbranch_scc1 .Lch_kvw_chk_%=\n"
        "v_mov_b32 v7, 1\n"
        "buffer_store_dword v7, off, s[40:43], s65 sc1\n"
        "s_waitcnt vmcnt(0)\n"
        ".Lch_kvw_chk_%=:\n"
        "buffer_load_dword v7, off, s[40:43], s65 sc1\n"
        "s_waitcnt vmcnt(0)\n"
        "v_readfirstlane_b32 s65, v7\n"
        "s_nop 0\n"
        "s_cmp_lg_u32 s65, 1\n"
        "s_cbranch_scc1 .Lch_kvw_go_%=\n"
        "s_endpgm\n"
        ".Lch_kvw_go_%=:\n"
        "s_branch .Lch_kvw_top_%=\n"
        ".Lch_kvw_ok_%=:\n"
        "s_add_u32 s89, s69, 524288\n"
        "buffer_load_dwordx4 v[68:71], v3, s[40:43], s69 offen offset:1024 sc1\n"
        "buffer_load_dwordx4 v[64:67], v3, s[40:43], s69 offen sc1\n"
        "buffer_load_dwordx4 v[72:75], v3, s[40:43], s89 offen sc1\n"
        "buffer_load_dwordx4 v[76:79], v3, s[40:43], s89 offen offset:1024 sc1\n"
        "s_mov_b32 s77, s89\n"
        ".Lch_slow3_%=:\n"
        "s_barrier\n"
        "s_memrealtime s[36:37]\n"
        "s_waitcnt lgkmcnt(0)\n"
        "s_add_u32 s36, s36, 12\n"
        "s_add_u32 s37, s36, s35\n"
        "v_and_b32 v7, -2, s37\n"
        "s_mov_b64 exec, 1\n"
        "buffer_store_dword v7, off, s[40:43], s46 sc1\n"
        "s_mov_b64 exec, -1\n"
        "s_mov_b32 s30, s69\n"
        "s_branch .Lch_back_%=\n"
        :: "s"(p.ws), "s"(p.Wq), "s"(p.Wk), "s"(p.Wv), "s"(p.Wo), "s"(p.mem0), "s"(pair(w, h)), "s"(pair(x4, pr)), "s"(p.tune0), "s"(p.tune1),
           "v"(pair(idx, va + (lane & 2) * 32)), "v"(pair(wb + lane * 16, lane * 16)), "v"(pair(spr, CH_IMG + lane * 64 + c * 4)),
           "v"(pair(qo, oo)), "v"(pair(tr + (q >> 1) * 32, m0)), "v"(pair(tr + (q >> 1) * 2048, 0))
        : "v0", "v1", "v2", "v3", "v4", "v5", "v6", "v7", "v8", "v9", "v10", "v11", "v12", "v13", "v14", "v15", "v16", "v17", "v18", "v19", "v20", "v21", "v22", "v23", "v24", "v25", "v26", "v27", "v28", "v29", "v30", "v31", "v32", "v33", "v34", "v35", "v36", "v37", "v38", "v39", "v40", "v41", "v42", "v43", "v44", "v45", "v46", "v47", "v48", "v49", "v50", "v51", "v52", "v53", "v54", "v55", "v56", "v57", "v58", "v59", "v60", "v61", "v62", "v63", "v64", "v65", "v66", "v67", "v68", "v69", "v70", "v71", "v72", "v73", "v74", "v75", "v76", "v77", "v78", "v79", "v255", "a255", "s30", "s31", "s32", "s33", "s34", "s35", "s36", "s37", "s38", "s39", "s40", "s41", "s42", "s43", "s44", "s45", "s46", "s47", "s48", "s49", "s50", "s51", "s52", "s53", "s54", "s55", "s56", "s57", "s58", "s59", "s60", "s61", "s62", "s63", "s64", "s65", "s66", "s67", "s68", "s69", "s70", "s71", "s72", "s73", "s74", "s75", "s76", "s77", "s78", "s79", "s80", "s81", "s82", "s83", "s84", "s85", "s86", "s87", "s88", "s89", "s90", "s91", "s92", "s93", "s94", "s95", "s96", "s97", "s98", "s99", "vcc", "scc", "memory");
    __builtin_unreachable();
}

constexpr unsigned flag(unsigned i) { return spread(LF0 + i * 128); }
#define GEMM_HEAD const unsigned lane = threadIdx.x & 63, i = lane & 15, g = lane >> 4, q = i >> 2, pp = lane & 3; const int w = rfl(threadIdx.x >> 6)
#define TILE_ARGS(base) "s"(pair(base + (w >> 1) * 16384 + (w & 1) * 4096, out1)), "s"(pair(fout, ((w >> 1) * 9216 + (w & 1) * 4096) | ((w >> 1) * 9216 + 8192) << 16)), \
        "s"(pair(base + 65536 + (w >> 1) * 1024, out2)), "v"(pair((i >> 3) * 9216 + g * 128 + (i & 7) * 16, lane * 16))
__device__ __forceinline__ void role_s1(const Params& p, int h, int x4, int pr, int delay) {
    GEMM_HEAD;
    const unsigned vo = (8 * g + q) * 8192 + (128 * h + 32 * w) * 2 + (pp ^ (2 * (g & 1))) * 16;
    const unsigned tr = w * 40960 + (4 * g + q) * 64 + pp * 8;
    const unsigned out1 = PAGE + h * 16384 + w * 2048, out2 = 0, fout = flag(F_S1 + 4 * h + w);
    asm volatile(
        "s_nop 4\n"
        "s_mov_b64 s[40:41], %0\n"
        "s_and_b32 s41, s41, 0xffff\n"
        "s_mov_b32 s42, 236978176\n"
        "s_mov_b32 s43, 0x20000\n"
        "s_mov_b64 s[48:49], %4\n"
        "s_mov_b64 s[56:57], %5\n"
        "s_mov_b64 s[58:59], %6\n"
        "s_mov_b64 s[60:61], %7\n"
        "s_mov_b64 s[62:63], %8\n"
        "v_mov_b64 v[0:1], %9\n"
        "v_mov_b64 v[2:3], %10\n"
        "v_mov_b64 v[4:5], %11\n"
        "v_mov_b64 v[6:7], %12\n"
        "v_mov_b64 v[8:9], %13\n"
        "v_mov_b64 v[10:11], %14\n"
        "s_mov_b32 s47, 0x20000\n"
        "s_mul_i32 s79, s48, 40960\n"
        ".Ls1_start_%=:\n"
        "s_sub_u32 s49, s49, 1\n"
        "s_cbranch_scc1 .Ls1_start2_%=\n"
        "s_sleep 16\n"
        "s_branch .Ls1_start_%=\n"
        ".Ls1_start2_%=:\n"
        "s_mov_b64 s[44:45], %3\n"
        "s_and_b32 s45, s45, 0xffff\n"
        "s_mov_b32 s46, 4718592\n"
        "s_add_u32 m0, s79, 0\n"
        "s_mov_b32 s65, 0\n"
        "buffer_load_dwordx4 v8, s[44:47], s65 offen lds\n"
        "s_mov_b32 s65, 31744\n"
        "buffer_load_dwordx4 v8, s[44:47], s65 offen offset:1024 lds\n"
        "s_add_u32 m0, s79, 2048\n"
        "s_mov_b32 s65, 262144\n"
        "buffer_load_dwordx4 v8, s[44:47], s65 offen lds\n"
        "s_mov_b32 s65, 293888\n"
        "buffer_load_dwordx4 v8, s[44:47], s65 offen offset:1024 lds\n"
        "s_add_u32 m0, s79, 4096\n"
        "s_mov_b32 s65, 524288\n"
        "buffer_load_dwordx4 v8, s[44:47], s65 offen lds\n"
        "s_mov_b32 s65, 556032\n"
        "buffer_load_dwordx4 v8, s[44:47], s65 offen offset:1024 lds\n"
        "s_add_u32 m0, s79, 6144\n"
        "s_mov_b32 s65, 786432\n"
        "buffer_load_dwordx4 v8, s[44:47], s65 offen lds\n"
        "s_mov_b32 s65, 818176\n"
        "buffer_load_dwordx4 v8, s[44:47], s65 offen offset:1024 lds\n"
        "s_add_u32 m0, s79, 8192\n"
        "s_mov_b32 s65, 1048576\n"
        "buffer_load_dwordx4 v8, s[44:47], s65 offen lds\n"
        "s_mov_b32 s65, 1080320\n"
        "buffer_load_dwordx4 v8, s[44:47], s65 offen offset:1024 lds\n"
        "s_add_u32 m0, s79, 10240\n"
        "s_mov_b32 s65, 1310720\n"
        "buffer_load_dwordx4 v8, s[44:47], s65 offen lds\n"
        "s_mov_b32 s65, 1342464\n"
        "buffer_load_dwordx4 v8, s[44:47], s65 offen offset:1024 lds\n"
        "s_add_u32 m0, s79, 12288\n"
        "s_mov_b32 s65, 1572864\n"
        "buffer_load_dwordx4 v8, s[44:47], s65 offen lds\n"
        "s_mov_b32 s65, 1604608\n"
        "buffer_load_dwordx4 v8, s[44:47], s65 offen offset:1024 lds\n"
        "s_add_u32 m0, s79, 14336\n"
        "s_mov_b32 s65, 1835008\n"
        "buffer_load_dwordx4 v8, s[44:47], s65 offen lds\n"
        "s_mov_b32 s65, 1866752\n"
        "buffer_load_dwordx4 v8, s[44:47], s65 offen offset:1024 lds\n"
        "s_add_u32 m0, s79, 16384\n"
        "s_mov_b32 s65, 2097152\n"
        "buffer_load_dwordx4 v8, s[44:47], s65 offen lds\n"
        "s_mov_b32 s65, 2128896\n"
        "buffer_load_dwordx4 v8, s[44:47], s65 offen offset:1024 lds\n"
        "s_add_u32 m0, s79, 18432\n"
        "s_mov_b32 s65, 2359296\n"
        "buffer_load_dwordx4 v8, s[44:47], s65 offen lds\n"
        "s_mov_b32 s65, 2391040\n"
        "buffer_load_dwordx4 v8, s[44:47], s65 offen offset:1024 lds\n"
        "s_add_u32 m0, s79, 20480\n"
        "s_mov_b32 s65, 2621440\n"
        "buffer_load_dwordx4 v8, s[44:47], s65 offen lds\n"
        "s_mov_b32 s65, 2653184\n"
        "buffer_load_dwordx4 v8, s[44:47], s65 offen offset:1024 lds\n"
        "s_add_u32 m0, s79, 22528\n"
        "s_mov_b32 s65, 2883584\n"
        "buffer_load_dwordx4 v8, s[44:47], s65 offen lds\n"
        "s_mov_b32 s65, 2915328\n"
        "buffer_load_dwordx4 v8, s[44:47], s65 offen offset:1024 lds\n"
        "s_add_u32 m0, s79, 24576\n"
        "s_mov_b32 s65, 3145728\n"
        "buffer_load_dwordx4 v8, s[44:47], s65 offen lds\n"
        "s_mov_b32 s65, 3177472\n"
        "buffer_load_dwordx4 v8, s[44:47], s65 offen offset:1024 lds\n"
        "s_add_u32 m0, s79, 26624\n"
        "s_mov_b32 s65, 3407872\n"
        "buffer_load_dwordx4 v8, s[44:47], s65 offen lds\n"
        "s_mov_b32 s65, 3439616\n"
        "buffer_load_dwordx4 v8, s[44:47], s65 offen offset:1024 lds\n"
        "s_add_u32 m0, s79, 28672\n"
        "s_mov_b32 s65, 3670016\n"
        "buffer_load_dwordx4 v8, s[44:47], s65 offen lds\n"
        "s_mov_b32 s65, 3701760\n"
        "buffer_load_dwordx4 v8, s[44:47], s65 offen offset:1024 lds\n"
        "s_add_u32 m0, s79, 30720\n"
        "s_mov_b32 s65, 3932160\n"
        "buffer_load_dwordx4 v8, s[44:47], s65 offen lds\n"
        "s_mov_b32 s65, 3963904\n"
        "buffer_load_dwordx4 v8, s[44:47], s65 offen offset:1024 lds\n"
        "s_waitcnt vmcnt(30)\n"
        "ds_read_b64_tr_b16 a[16:17], v10 offset:0\n"
        "ds_read_b64_tr_b16 a[18:19], v10 offset:1024\n"
        "ds_read_b64_tr_b16 a[20:21], v11 offset:0\n"
        "ds_read_b64_tr_b16 a[22:23], v11 offset:1024\n"
        "s_waitcnt vmcnt(28)\n"
        "ds_read_b64_tr_b16 a[40:41], v10 offset:2048\n"
        "ds_read_b64_tr_b16 a[42:43], v10 offset:3072\n"
        "ds_read_b64_tr_b16 a[44:45], v11 offset:2048\n"
        "ds_read_b64_tr_b16 a[46:47], v11 offset:3072\n"
        "s_waitcnt lgkmcnt(4)\n"
        "s_add_u32 m0, s79, 0\n"
        "s_mov_b32 s65, 4194304\n"
        "buffer_load_dwordx4 v8, s[44:47], s65 offen lds\n"
        "s_mov_b32 s65, 4226048\n"
        "buffer_load_dwordx4 v8, s[44:47], s65 offen offset:1024 lds\n"
        "s_waitcnt vmcnt(28)\n"
        "ds_read_b64_tr_b16 a[64:65], v10 offset:4096\n"
        "ds_read_b64_tr_b16 a[66:67], v10 offset:5120\n"
        "ds_read_b64_tr_b16 a[68:69], v11 offset:4096\n"
        "ds_read_b64_tr_b16 a[70:71], v11 offset:5120\n"
        "s_waitcnt lgkmcnt(4)\n"
        "s_add_u32 m0, s79, 2048\n"
        "s_mov_b32 s65, 4456448\n"
        "buffer_load_dwordx4 v8, s[44:47], s65 offen lds\n"
        "s_mov_b32 s65, 4488192\n"
        "buffer_load_dwordx4 v8, s[44:47], s65 offen offset:1024 lds\n"
        "s_waitcnt vmcnt(28)\n"
        "ds_read_b64_tr_b16 a[88:89], v10 offset:6144\n"
        "ds_read_b64_tr_b16 a[90:91], v10 offset:7168\n"
        "ds_read_b64_tr_b16 a[92:93], v11 offset:6144\n"
        "ds_read_b64_tr_b16 a[94:95], v11 offset:7168\n"
        "s_waitcnt lgkmcnt(4)\n"
        "s_mov_b64 s[44:45], %1\n"
        "s_and_b32 s45, s45, 0xffff\n"
        "s_mov_b32 s46, 4718592\n"
        "s_add_u32 m0, s79, 4096\n"
        "s_mov_b32 s65, 0\n"
        "buffer_load_dwordx4 v8, s[44:47], s65 offen lds\n"
        "s_mov_b32 s65, 31744\n"
        "buffer_load_dwordx4 v8, s[44:47], s65 offen offset:1024 lds\n"
        "s_waitcnt vmcnt(28)\n"
        "ds_read_b64_tr_b16 a[112:113], v10 offset:8192\n"
        "ds_read_b64_tr_b16 a[114:115], v10 offset:9216\n"
        "ds_read_b64_tr_b16 a[116:117], v11 offset:8192\n"
        "ds_read_b64_tr_b16 a[118:119], v11 offset:9216\n"
        "s_waitcnt lgkmcnt(4)\n"
        "s_add_u32 m0, s79, 6144\n"
        "s_mov_b32 s65, 262144\n"
        "buffer_load_dwordx4 v8, s[44:47], s65 offen lds\n"
        "s_mov_b32 s65, 293888\n"
        "buffer_load_dwordx4 v8, s[44:47], s65 offen offset:1024 lds\n"
        "s_waitcnt vmcnt(28)\n"
        "ds_read_b64_tr_b16 a[136:137], v10 offset:10240\n"
        "ds_read_b64_tr_b16 a[138:139], v10 offset:11264\n"
        "ds_read_b64_tr_b16 a[140:141], v11 offset:10240\n"
        "ds_read_b64_tr_b16 a[142:143], v11 offset:11264\n"
        "s_waitcnt lgkmcnt(4)\n"
        "s_add_u32 m0, s79, 8192\n"
        "s_mov_b32 s65, 524288\n"
        "buffer_load_dwordx4 v8, s[44:47], s65 offen lds\n"
        "s_mov_b32 s65, 556032\n"
        "buffer_load_dwordx4 v8, s[44:47], s65 offen offset:1024 lds\n"
        "s_waitcnt vmcnt(28)\n"
        "ds_read_b64_tr_b16 a[160:161], v10 offset:12288\n"
        "ds_read_b64_tr_b16 a[162:163], v10 offset:13312\n"
        "ds_read_b64_tr_b16 a[164:165], v11 offset:12288\n"
        "ds_read_b64_tr_b16 a[166:167], v11 offset:13312\n"
        "s_waitcnt lgkmcnt(4)\n"
        "s_add_u32 m0, s79, 10240\n"
        "s_mov_b32 s65, 786432\n"
        "buffer_load_dwordx4 v8, s[44:47], s65 offen lds\n"
        "s_mov_b32 s65, 818176\n"
        "buffer_load_dwordx4 v8, s[44:47], s65 offen offset:1024 lds\n"
        "s_waitcnt vmcnt(28)\n"
        "ds_read_b64_tr_b16 a[184:185], v10 offset:14336\n"
        "ds_read_b64_tr_b16 a[186:187], v10 offset:15360\n"
        "ds_read_b64_tr_b16 a[188:189], v11 offset:14336\n"
        "ds_read_b64_tr_b16 a[190:191], v11 offset:15360\n"
        "s_waitcnt lgkmcnt(4)\n"
        "s_add_u32 m0, s79, 12288\n"
        "s_mov_b32 s65, 1048576\n"
        "buffer_load_dwordx4 v8, s[44:47], s65 offen lds\n"
        "s_mov_b32 s65, 1080320\n"
        "buffer_load_dwordx4 v8, s[44:47], s65 offen offset:1024 lds\n"
        "s_waitcnt vmcnt(28)\n"
        "ds_read_b64_tr_b16 a[208:209], v10 offset:16384\n"
        "ds_read_b64_tr_b16 a[210:211], v10 offset:17408\n"
        "ds_read_b64_tr_b16 a[212:213], v11 offset:16384\n"
        "ds_read_b64_tr_b16 a[214:215], v11 offset:17408\n"
        "s_waitcnt lgkmcnt(4)\n"
        "s_add_u32 m0, s79, 14336\n"
        "s_mov_b32 s65, 1310720\n"
        "buffer_load_dwordx4 v8, s[44:47], s65 offen lds\n"
        "s_mov_b32 s65, 1342464\n"
        "buffer_load_dwordx4 v8, s[44:47], s65 offen offset:1024 lds\n"
        "s_waitcnt vmcnt(28)\n"
        "ds_read_b64_tr_b16 a[232:233], v10 offset:18432\n"
        "ds_read_b64_tr_b16 a[234:235], v10 offset:19456\n"
        "ds_read_b64_tr_b16 a[236:237], v11 offset:18432\n"
        "ds_read_b64_tr_b16 a[238:239], v11 offset:19456\n"
        "s_waitcnt lgkmcnt(4)\n"
        "s_add_u32 m0, s79, 16384\n"
        "s_mov_b32 s65, 1572864\n"
        "buffer_load_dwordx4 v8, s[44:47], s65 offen lds\n"
        "s_mov_b32 s65, 1604608\n"
        "buffer_load_dwordx4 v8, s[44:47], s65 offen offset:1024 lds\n"
        "s_waitcnt vmcnt(28)\n"
        "ds_read_b64_tr_b16 v[80:81], v10 offset:20480\n"
        "ds_read_b64_tr_b16 v[82:83], v10 offset:21504\n"
        "ds_read_b64_tr_b16 v[84:85], v11 offset:20480\n"
        "ds_read_b64_tr_b16 v[86:87], v11 offset:21504\n"
        "s_waitcnt lgkmcnt(4)\n"
        "s_add_u32 m0, s79, 18432\n"
        "s_mov_b32 s65, 1835008\n"
        "buffer_load_dwordx4 v8, s[44:47], s65 offen lds\n"
        "s_mov_b32 s65, 1866752\n"
        "buffer_load_dwordx4 v8, s[44:47], s65 offen offset:1024 lds\n"
        "s_waitcnt vmcnt(28)\n"
        "ds_read_b64_tr_b16 v[104:105], v10 offset:22528\n"
        "ds_read_b64_tr_b16 v[106:107], v10 offset:23552\n"
        "ds_read_b64_tr_b16 v[108:109], v11 offset:22528\n"
        "ds_read_b64_tr_b16 v[110:111], v11 offset:23552\n"
        "s_waitcnt lgkmcnt(4)\n"
        "s_add_u32 m0, s79, 20480\n"
        "s_mov_b32 s65, 2097152\n"
        "buffer_load_dwordx4 v8, s[44:47], s65 offen lds\n"
        "s_mov_b32 s65, 2128896\n"
        "buffer_load_dwordx4 v8, s[44:47], s65 offen offset:1024 lds\n"
        "s_waitcnt vmcnt(28)\n"
        "ds_read_b64_tr_b16 v[128:129], v10 offset:24576\n"
        "ds_read_b64_tr_b16 v[130:131], v10 offset:25600\n"
        "ds_read_b64_tr_b16 v[132:133], v11 offset:24576\n"
        "ds_read_b64_tr_b16 v[134:135], v11 offset:25600\n"
        "s_waitcnt lgkmcnt(4)\n"
        "s_add_u32 m0, s79, 22528\n"
        "s_mov_b32 s65, 2359296\n"
        "buffer_load_dwordx4 v8, s[44:47], s65 offen lds\n"
        "s_mov_b32 s65, 2391040\n"
        "buffer_load_dwordx4 v8, s[44:47], s65 offen offset:1024 lds\n"
        "s_waitcnt vmcnt(28)\n"
        "ds_read_b64_tr_b16 v[152:153], v10 offset:26624\n"
        "ds_read_b64_tr_b16 v[154:155], v10 offset:27648\n"
        "ds_read_b64_tr_b16 v[156:157], v11 offset:26624\n"
        "ds_read_b64_tr_b16 v[158:159], v11 offset:27648\n"
        "s_waitcnt lgkmcnt(4)\n"
        "s_add_u32 m0, s79, 24576\n"
        "s_mov_b32 s65, 2621440\n"
        "buffer_load_dwordx4 v8, s[44:47], s65 offen lds\n"
        "s_mov_b32 s65, 2653184\n"
        "buffer_load_dwordx4 v8, s[44:47], s65 offen offset:1024 lds\n"
        "s_waitcnt vmcnt(28)\n"
        "ds_read_b64_tr_b16 v[176:177], v10 offset:28672\n"
        "ds_read_b64_tr_b16 v[178:179], v10 offset:29696\n"
        "ds_read_b64_tr_b16 v[180:181], v11 offset:28672\n"
        "ds_read_b64_tr_b16 v[182:183], v11 offset:29696\n"
        "s_waitcnt lgkmcnt(4)\n"
        "s_add_u32 m0, s79, 26624\n"
        "s_mov_b32 s65, 2883584\n"
        "buffer_load_dwordx4 v8, s[44:47], s65 offen lds\n"
        "s_mov_b32 s65, 2915328\n"
        "buffer_load_dwordx4 v8, s[44:47], s65 offen offset:1024 lds\n"
        "s_waitcnt vmcnt(28)\n"
        "ds_read_b64_tr_b16 v[200:201], v10 offset:30720\n"
        "ds_read_b64_tr_b16 v[202:203], v10 offset:31744\n"
        "ds_read_b64_tr_b16 v[204:205], v11 offset:30720\n"
        "ds_read_b64_tr_b16 v[206:207], v11 offset:31744\n"
        "s_waitcnt lgkmcnt(4)\n"
        "s_add_u32 m0, s79, 28672\n"
        "s_mov_b32 s65, 3145728\n"
        "buffer_load_dwordx4 v8, s[44:47], s65 offen lds\n"
        "s_mov_b32 s65, 3177472\n"
        "buffer_load_dwordx4 v8, s[44:47], s65 offen offset:1024 lds\n"
        "s_waitcnt vmcnt(28)\n"
        "ds_read_b64_tr_b16 v[224:225], v10 offset:0\n"
        "ds_read_b64_tr_b16 v[226:227], v10 offset:1024\n"
        "ds_read_b64_tr_b16 v[228:229], v11 offset:0\n"
        "ds_read_b64_tr_b16 v[230:231], v11 offset:1024\n"
        "s_waitcnt lgkmcnt(4)\n"
        "s_add_u32 m0, s79, 30720\n"
        "s_mov_b32 s65, 3407872\n"
        "buffer_load_dwordx4 v8, s[44:47], s65 offen lds\n"
        "s_mov_b32 s65, 3439616\n"
        "buffer_load_dwordx4 v8, s[44:47], s65 offen offset:1024 lds\n"
        "s_waitcnt vmcnt(28)\n"
        "ds_read_b64_tr_b16 v[248:249], v10 offset:2048\n"
        "ds_read_b64_tr_b16 v[250:251], v10 offset:3072\n"
        "ds_read_b64_tr_b16 v[252:253], v11 offset:2048\n"
        "ds_read_b64_tr_b16 v[254:255], v11 offset:3072\n"
        "s_waitcnt lgkmcnt(4)\n"
        "s_add_u32 m0, s79, 0\n"
        "s_mov_b32 s65, 3670016\n"
        "buffer_load_dwordx4 v8, s[44:47], s65 offen lds\n"
        "s_mov_b32 s65, 3701760\n"
        "buffer_load_dwordx4 v8, s[44:47], s65 offen offset:1024 lds\n"
        "s_waitcnt vmcnt(28)\n"
        "ds_read_b64_tr_b16 a[0:1], v10 offset:4096\n"
        "ds_read_b64_tr_b16 a[2:3], v10 offset:5120\n"
        "ds_read_b64_tr_b16 a[4:5], v11 offset:4096\n"
        "ds_read_b64_tr_b16 a[6:7], v11 offset:5120\n"
        "s_waitcnt lgkmcnt(4)\n"
        "s_add_u32 m0, s79, 2048\n"
        "s_mov_b32 s65, 3932160\n"
        "buffer_load_dwordx4 v8, s[44:47], s65 offen lds\n"
        "s_mov_b32 s65, 3963904\n"
        "buffer_load_dwordx4 v8, s[44:47], s65 offen offset:1024 lds\n"
        "s_waitcnt vmcnt(28)\n"
        "ds_read_b64_tr_b16 a[24:25], v10 offset:6144\n"
        "ds_read_b64_tr_b16 a[26:27], v10 offset:7168\n"
        "ds_read_b64_tr_b16 a[28:29], v11 offset:6144\n"
        "ds_read_b64_tr_b16 a[30:31], v11 offset:7168\n"
        "s_waitcnt lgkmcnt(4)\n"
        "s_add_u32 m0, s79, 4096\n"
        "s_mov_b32 s65, 4194304\n"
        "buffer_load_dwordx4 v8, s[44:47], s65 offen lds\n"
        "s_mov_b32 s65, 4226048\n"
        "buffer_load_dwordx4 v8, s[44:47], s65 offen offset:1024 lds\n"
        "s_waitcnt vmcnt(28)\n"
        "ds_read_b64_tr_b16 a[48:49], v10 offset:8192\n"
        "ds_read_b64_tr_b16 a[50:51], v10 offset:9216\n"
        "ds_read_b64_tr_b16 a[52:53], v11 offset:8192\n"
        "ds_read_b64_tr_b16 a[54:55], v11 offset:9216\n"
        "s_waitcnt lgkmcnt(4)\n"
        "s_add_u32 m0, s79, 6144\n"
        "s_mov_b32 s65, 4456448\n"
        "buffer_load_dwordx4 v8, s[44:47], s65 offen lds\n"
        "s_mov_b32 s65, 4488192\n"
        "buffer_load_dwordx4 v8, s[44:47], s65 offen offset:1024 lds\n"
        "s_waitcnt vmcnt(28)\n"
        "ds_read_b64_tr_b16 a[72:73], v10 offset:10240\n"
        "ds_read_b64_tr_b16 a[74:75], v10 offset:11264\n"
        "ds_read_b64_tr_b16 a[76:77], v11 offset:10240\n"
        "ds_read_b64_tr_b16 a[78:79], v11 offset:11264\n"
        "s_waitcnt lgkmcnt(4)\n"
        "s_mov_b64 s[44:45], %2\n"
        "s_and_b32 s45, s45, 0xffff\n"
        "s_mov_b32 s46, 4718592\n"
        "s_add_u32 m0, s79, 8192\n"
        "s_mov_b32 s65, 0\n"
        "buffer_load_dwordx4 v8, s[44:47], s65 offen lds\n"
        "s_mov_b32 s65, 31744\n"
        "buffer_load_dwordx4 v8, s[44:47], s65 offen offset:1024 lds\n"
        "s_waitcnt vmcnt(28)\n"
        "ds_read_b64_tr_b16 a[96:97], v10 offset:12288\n"
        "ds_read_b64_tr_b16 a[98:99], v10 offset:13312\n"
        "ds_read_b64_tr_b16 a[100:101], v11 offset:12288\n"
        "ds_read_b64_tr_b16 a[102:103], v11 offset:13312\n"
        "s_waitcnt lgkmcnt(4)\n"
        "s_add_u32 m0, s79, 10240\n"
        "s_mov_b32 s65, 262144\n"
        "buffer_load_dwordx4 v8, s[44:47], s65 offen lds\n"
        "s_mov_b32 s65, 293888\n"
        "buffer_load_dwordx4 v8, s[44:47], s65 offen offset:1024 lds\n"
        "s_waitcnt vmcnt(28)\n"
        "ds_read_b64_tr_b16 a[120:121], v10 offset:14336\n"
        "ds_read_b64_tr_b16 a[122:123], v10 offset:15360\n"
        "ds_read_b64_tr_b16 a[124:125], v11 offset:14336\n"
        "ds_read_b64_tr_b16 a[126:127], v11 offset:15360\n"
        "s_waitcnt lgkmcnt(4)\n"
        "s_add_u32 m0, s79, 12288\n"
        "s_mov_b32 s65, 524288\n"
        "buffer_load_dwordx4 v8, s[44:47], s65 offen lds\n"
        "s_mov_b32 s65, 556032\n"
        "buffer_load_dwordx4 v8, s[44:47], s65 offen offset:1024 lds\n"
        "s_waitcnt vmcnt(28)\n"
        "ds_read_b64_tr_b16 a[144:145], v10 offset:16384\n"
        "ds_read_b64_tr_b16 a[146:147], v10 offset:17408\n"
        "ds_read_b64_tr_b16 a[148:149], v11 offset:16384\n"
        "ds_read_b64_tr_b16 a[150:151], v11 offset:17408\n"
        "s_waitcnt lgkmcnt(4)\n"
        "s_add_u32 m0, s79, 14336\n"
        "s_mov_b32 s65, 786432\n"
        "buffer_load_dwordx4 v8, s[44:47], s65 offen lds\n"
        "s_mov_b32 s65, 818176\n"
        "buffer_load_dwordx4 v8, s[44:47], s65 offen offset:1024 lds\n"
        "s_waitcnt vmcnt(28)\n"
        "ds_read_b64_tr_b16 a[168:169], v10 offset:18432\n"
        "ds_read_b64_tr_b16 a[170:171], v10 offset:19456\n"
        "ds_read_b64_tr_b16 a[172:173], v11 offset:18432\n"
        "ds_read_b64_tr_b16 a[174:175], v11 offset:19456\n"
        "s_waitcnt lgkmcnt(4)\n"
        "s_add_u32 m0, s79, 16384\n"
        "s_mov_b32 s65, 1048576\n"
        "buffer_load_dwordx4 v8, s[44:47], s65 offen lds\n"
        "s_mov_b32 s65, 1080320\n"
        "buffer_load_dwordx4 v8, s[44:47], s65 offen offset:1024 lds\n"
        "s_waitcnt vmcnt(28)\n"
        "ds_read_b64_tr_b16 a[192:193], v10 offset:20480\n"
        "ds_read_b64_tr_b16 a[194:195], v10 offset:21504\n"
        "ds_read_b64_tr_b16 a[196:197], v11 offset:20480\n"
        "ds_read_b64_tr_b16 a[198:199], v11 offset:21504\n"
        "s_waitcnt lgkmcnt(4)\n"
        "s_add_u32 m0, s79, 18432\n"
        "s_mov_b32 s65, 1310720\n"
        "buffer_load_dwordx4 v8, s[44:47], s65 offen lds\n"
        "s_mov_b32 s65, 1342464\n"
        "buffer_load_dwordx4 v8, s[44:47], s65 offen offset:1024 lds\n"
        "s_waitcnt vmcnt(28)\n"
        "ds_read_b64_tr_b16 a[216:217], v10 offset:22528\n"
        "ds_read_b64_tr_b16 a[218:219], v10 offset:23552\n"
        "ds_read_b64_tr_b16 a[220:221], v11 offset:22528\n"
        "ds_read_b64_tr_b16 a[222:223], v11 offset:23552\n"
        "s_waitcnt lgkmcnt(4)\n"
        "s_add_u32 m0, s79, 20480\n"
        "s_mov_b32 s65, 1572864\n"
        "buffer_load_dwordx4 v8, s[44:47], s65 offen lds\n"
        "s_mov_b32 s65, 1604608\n"
        "buffer_load_dwordx4 v8, s[44:47], s65 offen offset:1024 lds\n"
        "s_waitcnt vmcnt(28)\n"
        "ds_read_b64_tr_b16 a[240:241], v10 offset:24576\n"
        "ds_read_b64_tr_b16 a[242:243], v10 offset:25600\n"
        "ds_read_b64_tr_b16 a[244:245], v11 offset:24576\n"
        "ds_read_b64_tr_b16 a[246:247], v11 offset:25600\n"
        "s_waitcnt lgkmcnt(4)\n"
        "s_add_u32 m0, s79, 22528\n"
        "s_mov_b32 s65, 1835008\n"
        "buffer_load_dwordx4 v8, s[44:47], s65 offen lds\n"
        "s_mov_b32 s65, 1866752\n"
        "buffer_load_dwordx4 v8, s[44:47], s65 offen offset:1024 lds\n"
        "s_waitcnt vmcnt(28)\n"
        "ds_read_b64_tr_b16 v[88:89], v10 offset:26624\n"
        "ds_read_b64_tr_b16 v[90:91], v10 offset:27648\n"
        "ds_read_b64_tr_b16 v[92:93], v11 offset:26624\n"
        "ds_read_b64_tr_b16 v[94:95], v11 offset:27648\n"
        "s_waitcnt lgkmcnt(4)\n"
        "s_add_u32 m0, s79, 24576\n"
        "s_mov_b32 s65, 2097152\n"
        "buffer_load_dwordx4 v8, s[44:47], s65 offen lds\n"
        "s_mov_b32 s65, 2128896\n"
        "buffer_load_dwordx4 v8, s[44:47], s65 offen offset:1024 lds\n"
        "s_waitcnt vmcnt(28)\n"
        "ds_read_b64_tr_b16 v[112:113], v10 offset:28672\n"
        "ds_read_b64_tr_b16 v[114:115], v10 offset:29696\n"
        "ds_read_b64_tr_b16 v[116:117], v11 offset:28672\n"
        "ds_read_b64_tr_b16 v[118:119], v11 offset:29696\n"
        "s_waitcnt lgkmcnt(4)\n"
        "s_add_u32 m0, s79, 26624\n"
        "s_mov_b32 s65, 2359296\n"
        "buffer_load_dwordx4 v8, s[44:47], s65 offen lds\n"
        "s_mov_b32 s65, 2391040\n"
        "buffer_load_dwordx4 v8, s[44:47], s65 offen offset:1024 lds\n"
        "s_waitcnt vmcnt(28)\n"
        "ds_read_b64_tr_b16 v[136:137], v10 offset:30720\n"
        "ds_read_b64_tr_b16 v[138:139], v10 offset:31744\n"
        "ds_read_b64_tr_b16 v[140:141], v11 offset:30720\n"
        "ds_read_b64_tr_b16 v[142:143], v11 offset:31744\n"
        "s_waitcnt lgkmcnt(4)\n"
        "s_add_u32 m0, s79, 28672\n"
        "s_mov_b32 s65, 2621440\n"
        "buffer_load_dwordx4 v8, s[44:47], s65 offen lds\n"
        "s_mov_b32 s65, 2653184\n"
        "buffer_load_dwordx4 v8, s[44:47], s65 offen offset:1024 lds\n"
        "s_waitcnt vmcnt(28)\n"
        "ds_read_b64_tr_b16 v[160:161], v10 offset:0\n"
        "ds_read_b64_tr_b16 v[162:163], v10 offset:1024\n"
        "ds_read_b64_tr_b16 v[164:165], v11 offset:0\n"
        "ds_read_b64_tr_b16 v[166:167], v11 offset:1024\n"
        "s_waitcnt lgkmcnt(4)\n"
        "s_add_u32 m0, s79, 30720\n"
        "s_mov_b32 s65, 2883584\n"
        "buffer_load_dwordx4 v8, s[44:47], s65 offen lds\n"
        "s_mov_b32 s65, 2915328\n"
        "buffer_load_dwordx4 v8, s[44:47], s65 offen offset:1024 lds\n"
        "s_waitcnt vmcnt(28)\n"
        "ds_read_b64_tr_b16 v[184:185], v10 offset:2048\n"
        "ds_read_b64_tr_b16 v[186:187], v10 offset:3072\n"
        "ds_read_b64_tr_b16 v[188:189], v11 offset:2048\n"
        "ds_read_b64_tr_b16 v[190:191], v11 offset:3072\n"
        "s_waitcnt lgkmcnt(4)\n"
        "s_add_u32 m0, s79, 0\n"
        "s_mov_b32 s65, 3145728\n"
        "buffer_load_dwordx4 v8, s[44:47], s65 offen lds\n"
        "s_mov_b32 s65, 3177472\n"
        "buffer_load_dwordx4 v8, s[44:47], s65 offen offset:1024 lds\n"
        "s_waitcnt vmcnt(28)\n"
        "ds_read_b64_tr_b16 v[208:209], v10 offset:4096\n"
        "ds_read_b64_tr_b16 v[210:211], v10 offset:5120\n"
        "ds_read_b64_tr_b16 v[212:213], v11 offset:4096\n"
        "ds_read_b64_tr_b16 v[214:215], v11 offset:5120\n"
        "s_waitcnt lgkmcnt(4)\n"
        "s_add_u32 m0, s79, 2048\n"
        "s_mov_b32 s65, 3407872\n"
        "buffer_load_dwordx4 v8, s[44:47], s65 offen lds\n"
        "s_mov_b32 s65, 3439616\n"
        "buffer_load_dwordx4 v8, s[44:47], s65 offen offset:1024 lds\n"
        "s_waitcnt vmcnt(28)\n"
        "ds_read_b64_tr_b16 v[232:233], v10 offset:6144\n"
        "ds_read_b64_tr_b16 v[234:235], v10 offset:7168\n"
        "ds_read_b64_tr_b16 v[236:237], v11 offset:6144\n"
        "ds_read_b64_tr_b16 v[238:239], v11 offset:7168\n"
        "s_waitcnt lgkmcnt(4)\n"
        "s_add_u32 m0, s79, 4096\n"
        "s_mov_b32 s65, 3670016\n"
        "buffer_load_dwordx4 v8, s[44:47], s65 offen lds\n"
        "s_mov_b32 s65, 3701760\n"
        "buffer_load_dwordx4 v8, s[44:47], s65 offen offset:1024 lds\n"
        "s_waitcnt vmcnt(28)\n"
        "ds_read_b64_tr_b16 a[8:9], v10 offset:8192\n"
        "ds_read_b64_tr_b16 a[10:11], v10 offset:9216\n"
        "ds_read_b64_tr_b16 a[12:13], v11 offset:8192\n"
        "ds_read_b64_tr_b16 a[14:15], v11 offset:9216\n"
        "s_waitcnt lgkmcnt(4)\n"
        "s_add_u32 m0, s79, 6144\n"
        "s_mov_b32 s65, 3932160\n"
        "buffer_load_dwordx4 v8, s[44:47], s65 offen lds\n"
        "s_mov_b32 s65, 3963904\n"
        "buffer_load_dwordx4 v8, s[44:47], s65 offen offset:1024 lds\n"
        "s_waitcnt vmcnt(28)\n"
        "ds_read_b64_tr_b16 a[32:33], v10 offset:10240\n"
        "ds_read_b64_tr_b16 a[34:35], v10 offset:11264\n"
        "ds_read_b64_tr_b16 a[36:37], v11 offset:10240\n"
        "ds_read_b64_tr_b16 a[38:39], v11 offset:11264\n"
        "s_waitcnt lgkmcnt(4)\n"
        "s_add_u32 m0, s79, 8192\n"
        "s_mov_b32 s65, 4194304\n"
        "buffer_load_dwordx4 v8, s[44:47], s65 offen lds\n"
        "s_mov_b32 s65, 4226048\n"
        "buffer_load_dwordx4 v8, s[44:47], s65 offen offset:1024 lds\n"
        "s_waitcnt vmcnt(28)\n"
        "ds_read_b64_tr_b16 a[56:57], v10 offset:12288\n"
        "ds_read_b64_tr_b16 a[58:59], v10 offset:13312\n"
        "ds_read_b64_tr_b16 a[60:61], v11 offset:12288\n"
        "ds_read_b64_tr_b16 a[62:63], v11 offset:13312\n"
        "s_waitcnt lgkmcnt(4)\n"
        "s_add_u32 m0, s79, 10240\n"
        "s_mov_b32 s65, 4456448\n"
        "buffer_load_dwordx4 v8, s[44:47], s65 offen lds\n"
        "s_mov_b32 s65, 4488192\n"
        "buffer_load_dwordx4 v8, s[44:47], s65 offen offset:1024 lds\n"
        "s_waitcnt vmcnt(28)\n"
        "ds_read_b64_tr_b16 a[80:81], v10 offset:14336\n"
        "ds_read_b64_tr_b16 a[82:83], v10 offset:15360\n"
        "ds_read_b64_tr_b16 a[84:85], v11 offset:14336\n"
        "ds_read_b64_tr_b16 a[86:87], v11 offset:15360\n"
        "s_waitcnt vmcnt(26)\n"
        "ds_read_b64_tr_b16 a[104:105], v10 offset:16384\n"
        "ds_read_b64_tr_b16 a[106:107], v10 offset:17408\n"
        "ds_read_b64_tr_b16 a[108:109], v11 offset:16384\n"
        "ds_read_b64_tr_b16 a[110:111], v11 offset:17408\n"
        "s_waitcnt vmcnt(24)\n"
        "ds_read_b64_tr_b16 a[128:129], v10 offset:18432\n"
        "ds_read_b64_tr_b16 a[130:131], v10 offset:19456\n"
        "ds_read_b64_tr_b16 a[132:133], v11 offset:18432\n"
        "ds_read_b64_tr_b16 a[134:135], v11 offset:19456\n"
        "s_waitcnt vmcnt(22)\n"
        "ds_read_b64_tr_b16 a[152:153], v10 offset:20480\n"
        "ds_read_b64_tr_b16 a[154:155], v10 offset:21504\n"
        "ds_read_b64_tr_b16 a[156:157], v11 offset:20480\n"
        "ds_read_b64_tr_b16 a[158:159], v11 offset:21504\n"
        "s_waitcnt vmcnt(20)\n"
        "ds_read_b64_tr_b16 a[176:177], v10 offset:22528\n"
        "ds_read_b64_tr_b16 a[178:179], v10 offset:23552\n"
        "ds_read_b64_tr_b16 a[180:181], v11 offset:22528\n"
        "ds_read_b64_tr_b16 a[182:183], v11 offset:23552\n"
        "s_waitcnt vmcnt(18)\n"
        "ds_read_b64_tr_b16 a[200:201], v10 offset:24576\n"
        "ds_read_b64_tr_b16 a[202:203], v10 offset:25600\n"
        "ds_read_b64_tr_b16 a[204:205], v11 offset:24576\n"
        "ds_read_b64_tr_b16 a[206:207], v11 offset:25600\n"
        "s_waitcnt vmcnt(16)\n"
        "ds_read_b64_tr_b16 a[224:225], v10 offset:26624\n"
        "ds_read_b64_tr_b16 a[226:227], v10 offset:27648\n"
        "ds_read_b64_tr_b16 a[228:229], v11 offset:26624\n"
        "ds_read_b64_tr_b16 a[230:231], v11 offset:27648\n"
        "s_waitcnt vmcnt(14)\n"
        "ds_read_b64_tr_b16 a[248:249], v10 offset:28672\n"
        "ds_read_b64_tr_b16 a[250:251], v10 offset:29696\n"
        "ds_read_b64_tr_b16 a[252:253], v11 offset:28672\n"
        "ds_read_b64_tr_b16 a[254:255], v11 offset:29696\n"
        "s_waitcnt vmcnt(12)\n"
        "ds_read_b64_tr_b16 v[96:97], v10 offset:30720\n"
        "ds_read_b64_tr_b16 v[98:99], v10 offset:31744\n"
        "ds_read_b64_tr_b16 v[100:101], v11 offset:30720\n"
        "ds_read_b64_tr_b16 v[102:103], v11 offset:31744\n"
        "s_waitcnt vmcnt(10)\n"
        "ds_read_b64_tr_b16 v[120:121], v10 offset:0\n"
        "ds_read_b64_tr_b16 v[122:123], v10 offset:1024\n"
        "ds_read_b64_tr_b16 v[124:125], v11 offset:0\n"
        "ds_read_b64_tr_b16 v[126:127], v11 offset:1024\n"
        "s_waitcnt vmcnt(8)\n"
        "ds_read_b64_tr_b16 v[144:145], v10 offset:2048\n"
        "ds_read_b64_tr_b16 v[146:147], v10 offset:3072\n"
        "ds_read_b64_tr_b16 v[148:149], v11 offset:2048\n"
        "ds_read_b64_tr_b16 v[150:151], v11 offset:3072\n"
        "s_waitcnt vmcnt(6)\n"
        "ds_read_b64_tr_b16 v[168:169], v10 offset:4096\n"
        "ds_read_b64_tr_b16 v[170:171], v10 offset:5120\n"
        "ds_read_b64_tr_b16 v[172:173], v11 offset:4096\n"
        "ds_read_b64_tr_b16 v[174:175], v11 offset:5120\n"
        "s_waitcnt vmcnt(4)\n"
        "ds_read_b64_tr_b16 v[192:193], v10 offset:6144\n"
        "ds_read_b64_tr_b16 v[194:195], v10 offset:7168\n"
        "ds_read_b64_tr_b16 v[196:197], v11 offset:6144\n"
        "ds_read_b64_tr_b16 v[198:199], v11 offset:7168\n"
        "s_waitcnt vmcnt(2)\n"
        "ds_read_b64_tr_b16 v[216:217], v10 offset:8192\n"
        "ds_read_b64_tr_b16 v[218:219], v10 offset:9216\n"
        "ds_read_b64_tr_b16 v[220:221], v11 offset:8192\n"
        "ds_read_b64_tr_b16 v[222:223], v11 offset:9216\n"
        "s_waitcnt vmcnt(0)\n"
        "ds_read_b64_tr_b16 v[240:241], v10 offset:10240\n"
        "ds_read_b64_tr_b16 v[242:243], v10 offset:11264\n"
        "ds_read_b64_tr_b16 v[244:245], v11 offset:10240\n"
        "ds_read_b64_tr_b16 v[246:247], v11 offset:11264\n"
        "s_waitcnt lgkmcnt(0)\n"
        "v_lshrrev_b32 v20, 4, v1\n"
        "s_mov_b32 s50, s56\n"
        "v_lshlrev_b32 v22, 7, v20\n"
        "s_sub_u32 s66, 0, s50\n"
        "s_mov_b32 s52, 32768\n"
        "s_mov_b32 s64, 0\n"
        ".Ls1_pg0_top_%=:\n"
        "buffer_load_dword v21, v22, s[40:43], s52 offen sc1\n"
        "s_waitcnt vmcnt(0)\n"
        "v_cmp_gt_i32 vcc, 0, v21\n"
        "s_cbranch_vccz .Ls1_pg0_ok_%=\n"
        "s_add_u32 s64, s64, 1\n"
        "s_and_b32 s65, s64, 255\n"
        "s_cbranch_scc1 .Ls1_pg0_go_%=\n"
        "s_mov_b32 s65, 49152\n"
        "s_cmp_lt_u32 s64, 1048576\n"
        "s_cbranch_scc1 .Ls1_pg0_chk_%=\n"
        "v_mov_b32 v21, 1\n"
        "buffer_store_dword v21, off, s[40:43], s65 sc1\n"
        "s_waitcnt vmcnt(0)\n"
        ".Ls1_pg0_chk_%=:\n"
        "buffer_load_dword v21, off, s[40:43], s65 sc1\n"
        "s_waitcnt vmcnt(0)\n"
        "v_readfirstlane_b32 s65, v21\n"
        "s_nop 0\n"
        "s_cmp_lg_u32 s65, 1\n"
        "s_cbranch_scc1 .Ls1_pg0_go_%=\n"
        "s_endpgm\n"
        ".Ls1_pg0_go_%=:\n"
        "s_sleep 2\n"
        "s_branch .Ls1_pg0_top_%=\n"
        ".Ls1_pg0_ok_%=:\n"
        "v_cmp_eq_u32 s[52:53], 1, v21\n"
        "s_nop 1\n"
        "s_mov_b32 s54, 40960\n"
        "s_mov_b32 s64, 0\n"
        ".Ls1_pg1_top_%=:\n"
        "buffer_load_dword v21, v22, s[40:43], s54 offen sc1\n"
        "s_waitcnt vmcnt(0)\n"
        "v_cmp_gt_i32 vcc, 0, v21\n"
        "s_cbranch_vccz .Ls1_pg1_ok_%=\n"
        "s_add_u32 s64, s64, 1\n"
        "s_and_b32 s65, s64, 255\n"
        "s_cbranch_scc1 .Ls1_pg1_go_%=\n"
        "s_mov_b32 s65, 49152\n"
        "s_cmp_lt_u32 s64, 1048576\n"
        "s_cbranch_scc1 .Ls1_pg1_chk_%=\n"
        "v_mov_b32 v21, 1\n"
        "buffer_store_dword v21, off, s[40:43], s65 sc1\n"
        "s_waitcnt vmcnt(0)\n"
        ".Ls1_pg1_chk_%=:\n"
        "buffer_load_dword v21, off, s[40:43], s65 sc1\n"
        "s_waitcnt vmcnt(0)\n"
        "v_readfirstlane_b32 s65, v21\n"
        "s_nop 0\n"
        "s_cmp_lg_u32 s65, 1\n"
        "s_cbranch_scc1 .Ls1_pg1_go_%=\n"
        "s_endpgm\n"
        ".Ls1_pg1_go_%=:\n"
        "s_sleep 2\n"
        "s_branch .Ls1_pg1_top_%=\n"
        ".Ls1_pg1_ok_%=:\n"
        "v_cmp_eq_u32 s[54:55], 1, v21\n"
        "s_nop 1\n"
        "s_mov_b32 vcc_lo, 0x55555555\n"
        "s_mov_b32 vcc_hi, 0x55555555\n"
        "s_xor_b64 vcc, vcc, s[52:53]\n"
        "s_bcnt1_i32_b64 s65, vcc\n"
        "s_sub_u32 s64, s65, 23\n"
        "s_cmp_lt_u32 s64, 19\n"
        "s_cbranch_scc1 .Ls1_rawmap_%=\n"
        "s_cmp_lt_u32 s65, 32\n"
        "s_cselect_b32 s65, 0, -1\n"
        "s_xor_b32 s52, s65, 0x55555555\n"
        "s_xor_b32 s53, s65, 0x55555555\n"
        "s_xor_b32 s54, s65, 0x55555555\n"
        "s_xor_b32 s55, s65, 0xaaaaaaaa\n"
        ".Ls1_rawmap_%=:\n"
        "s_xor_b32 s52, s52, s66\n"
        "s_xor_b32 s53, s53, s66\n"
        "s_xor_b32 s54, s54, s66\n"
        "s_xor_b32 s55, s55, s66\n"
        "s_and_b32 s76, s55, 1\n"
        "s_lshl_b32 s76, s76, 13\n"
        "s_add_u32 s60, s60, s76\n"
        "s_barrier\n"
        "s_mov_b32 s68, -1\n"
        "s_mov_b32 s69, 0\n"
        "s_mov_b32 s78, 0\n"
        "s_mov_b32 s74, -1\n"
        "s_mov_b32 s86, s76\n"
        "s_lshr_b32 s85, s61, 16\n"
        "s_and_b32 s61, s61, 0xffff\n"
        "s_mov_b32 s89, 241172480\n"
        "s_mov_b32 s84, 241172480\n"
        "v_mov_b32 v6, v0\n"
        "s_mov_b32 s80, 0xbfb8aa3b\n"
        "s_lshr_b32 s67, s69, 1\n"
        "s_bitcmp1_b64 s[52:53], s67\n"
        "s_cselect_b32 s65, 8192, 0\n"
        "s_lshl_b32 s77, s67, 21\n"
        "s_add_u32 s77, s77, s65\n"
        "s_add_u32 s71, s77, s58\n"
        "s_add_u32 s81, s77, s62\n"
        "s_add_u32 s78, s78, 18432\n"
        "s_cmp_eq_u32 s78, 55296\n"
        "s_cselect_b32 s78, 0, s78\n"
        "s_add_u32 m0, s78, s61\n"
        "s_lshr_b32 s70, s69, 1\n"
        "s_branch .Ls1_wait1_%=\n"
        ".Ls1_i0_%=:\n"
        "v_mfma_f32_16x16x32_bf16 v[24:27], a[0:3], v[8:11], 0\n"
        "s_add_u32 s86, s76, 512\n"
        "buffer_load_dword v4, v2, s[40:43], s86 offen sc1\n"
        "v_mfma_f32_16x16x32_bf16 v[28:31], a[4:7], v[8:11], 0\n"
        "s_add_u32 s71, s77, s58\n"
        "v_cvt_pk_bf16_f32 v48, v48, v49\n"
        "v_cvt_pk_bf16_f32 v49, v50, v51\n"
        "v_mfma_f32_16x16x32_bf16 v[32:35], v[8:11], a[8:11], 0\n"
        "s_add_u32 s71, s71, 32768\n"
        "v_cvt_pk_bf16_f32 v50, v52, v53\n"
        "v_cvt_pk_bf16_f32 v51, v54, v55\n"
        "v_mfma_f32_16x16x32_bf16 v[36:39], v[8:11], a[12:15], 0\n"
        "s_add_u32 s81, s77, s62\n"
        "s_add_u32 s66, s72, 524288\n"
        "v_mfma_f32_16x16x32_bf16 v[40:43], a[16:19], v[8:11], 0\n"
        "s_add_u32 s81, s81, 2048\n"
        "buffer_store_dwordx4 v[48:51], v1, s[40:43], s66 offen offset:1024 sc1\n"
        "v_mfma_f32_16x16x32_bf16 v[44:47], a[20:23], v[8:11], 0\n"
        "s_waitcnt lgkmcnt(1)\n"
        "ds_read_b128 v[8:11], v0 offset:1536\n"
        "v_cvt_pk_bf16_f32 v74, v56, v57\n"
        "v_mfma_f32_16x16x32_bf16 v[24:27], a[24:27], v[12:15], v[24:27]\n"
        "s_add_u32 s78, s78, 18432\n"
        "v_cvt_pk_bf16_f32 v75, v58, v59\n"
        "s_add_u32 s66, s72, 1048576\n"
        "v_mfma_f32_16x16x32_bf16 v[28:31], a[28:31], v[12:15], v[28:31]\n"
        "s_cmp_eq_u32 s78, 55296\n"
        "s_cselect_b32 s78, 0, s78\n"
        "v_mfma_f32_16x16x32_bf16 v[32:35], v[12:15], a[32:35], v[32:35]\n"
        "s_add_u32 m0, s78, s61\n"
        "v_mfma_f32_16x16x32_bf16 v[36:39], v[12:15], a[36:39], v[36:39]\n"
        "s_lshr_b32 s70, s69, 1\n"
        "v_mfma_f32_16x16x32_bf16 v[40:43], a[40:43], v[12:15], v[40:43]\n"
        "v_mfma_f32_16x16x32_bf16 v[44:47], a[44:47], v[12:15], v[44:47]\n"
        "s_waitcnt lgkmcnt(1)\n"
        "ds_read_b128 v[12:15], v0 offset:2048\n"
        "v_mfma_f32_16x16x32_bf16 v[24:27], a[48:51], v[16:19], v[24:27]\n"
        "buffer_store_dwordx4 v[72:75], v1, s[40:43], s66 offen offset:0 sc1\n"
        "v_mfma_f32_16x16x32_bf16 v[28:31], a[52:55], v[16:19], v[28:31]\n"
        "v_cvt_pk_bf16_f32 v78, v60, v61\n"
        "v_cvt_pk_bf16_f32 v79, v62, v63\n"
        "v_mfma_f32_16x16x32_bf16 v[32:35], v[16:19], a[56:59], v[32:35]\n"
        "s_waitcnt vmcnt(3)\n"
        "v_mfma_f32_16x16x32_bf16 v[36:39], v[16:19], a[60:63], v[36:39]\n"
        "buffer_store_dword v5, v3, s[40:43], s84 offen sc1\n"
        "v_mfma_f32_16x16x32_bf16 v[40:43], a[64:67], v[16:19], v[40:43]\n"
        "v_mfma_f32_16x16x32_bf16 v[44:47], a[68:71], v[16:19], v[44:47]\n"
        "s_waitcnt lgkmcnt(1)\n"
        "ds_read_b128 v[16:19], v0 offset:2560\n"
        "v_mfma_f32_16x16x32_bf16 v[24:27], a[72:75], v[8:11], v[24:27]\n"
        "v_mfma_f32_16x16x32_bf16 v[28:31], a[76:79], v[8:11], v[28:31]\n"
        "buffer_store_dwordx4 v[76:79], v1, s[40:43], s66 offen offset:1024 sc1\n"
        "v_mfma_f32_16x16x32_bf16 v[32:35], v[8:11], a[80:83], v[32:35]\n"
        "v_cvt_pk_bf16_f32 v64, v64, v65\n"
        "v_cvt_pk_bf16_f32 v65, v66, v67\n"
        "v_mfma_f32_16x16x32_bf16 v[36:39], v[8:11], a[84:87], v[36:39]\n"
        "v_cvt_pk_bf16_f32 v66, v68, v69\n"
        "v_cvt_pk_bf16_f32 v67, v70, v71\n"
        "v_mfma_f32_16x16x32_bf16 v[40:43], a[88:91], v[8:11], v[40:43]\n"
        "v_mfma_f32_16x16x32_bf16 v[44:47], a[92:95], v[8:11], v[44:47]\n"
        "s_waitcnt lgkmcnt(1)\n"
        "ds_read_b128 v[8:11], v0 offset:3072\n"
        "v_mfma_f32_16x16x32_bf16 v[24:27], a[96:99], v[12:15], v[24:27]\n"
        "v_mfma_f32_16x16x32_bf16 v[28:31], a[100:103], v[12:15], v[28:31]\n"
        "v_mfma_f32_16x16x32_bf16 v[32:35], v[12:15], a[104:107], v[32:35]\n"
        "buffer_store_dwordx4 v[64:67], v1, s[40:43], s72 offen offset:1024 sc1\n"
        "v_mfma_f32_16x16x32_bf16 v[36:39], v[12:15], a[108:111], v[36:39]\n"
        "v_mov_b32 v5, s74\n"
        "v_mfma_f32_16x16x32_bf16 v[40:43], a[112:115], v[12:15], v[40:43]\n"
        "s_waitcnt vmcnt(5)\n"
        "v_cmp_gt_i32 vcc, s70, v4\n"
        "v_mfma_f32_16x16x32_bf16 v[44:47], a[116:119], v[12:15], v[44:47]\n"
        "s_waitcnt lgkmcnt(1)\n"
        "ds_read_b128 v[12:15], v0 offset:3584\n"
        "v_mfma_f32_16x16x32_bf16 v[24:27], a[120:123], v[16:19], v[24:27]\n"
        "s_and_b64 vcc, vcc, 1\n"
        "v_mfma_f32_16x16x32_bf16 v[28:31], a[124:127], v[16:19], v[28:31]\n"
        "s_cbranch_vccnz .Ls1_i00_%=\n"
        "v_mfma_f32_16x16x32_bf16 v[32:35], v[16:19], a[128:131], v[32:35]\n"
        "buffer_load_dwordx4 v1, s[40:43], s71 offen offset:0 sc1 lds\n"
        "v_mfma_f32_16x16x32_bf16 v[36:39], v[16:19], a[132:135], v[36:39]\n"
        "v_mfma_f32_16x16x32_bf16 v[40:43], a[136:139], v[16:19], v[40:43]\n"
        "v_mfma_f32_16x16x32_bf16 v[44:47], a[140:143], v[16:19], v[44:47]\n"
        "s_waitcnt lgkmcnt(1)\n"
        "ds_read_b128 v[16:19], v0 offset:4096\n"
        "v_mfma_f32_16x16x32_bf16 v[24:27], a[144:147], v[8:11], v[24:27]\n"
        "buffer_load_dwordx4 v1, s[40:43], s71 offen offset:1024 sc1 lds\n"
        "v_mfma_f32_16x16x32_bf16 v[28:31], a[148:151], v[8:11], v[28:31]\n"
        "v_mfma_f32_16x16x32_bf16 v[32:35], v[8:11], a[152:155], v[32:35]\n"
        "v_mfma_f32_16x16x32_bf16 v[36:39], v[8:11], a[156:159], v[36:39]\n"
        "v_mfma_f32_16x16x32_bf16 v[40:43], a[160:163], v[8:11], v[40:43]\n"
        "buffer_load_dwordx4 v1, s[40:43], s71 offen offset:2048 sc1 lds\n"
        "v_mfma_f32_16x16x32_bf16 v[44:47], a[164:167], v[8:11], v[44:47]\n"
        "s_waitcnt lgkmcnt(1)\n"
        "ds_read_b128 v[8:11], v0 offset:4608\n"
        "v_mfma_f32_16x16x32_bf16 v[24:27], a[168:171], v[12:15], v[24:27]\n"
        "v_mfma_f32_16x16x32_bf16 v[28:31], a[172:175], v[12:15], v[28:31]\n"
        "v_mfma_f32_16x16x32_bf16 v[32:35], v[12:15], a[176:179], v[32:35]\n"
        "buffer_load_dwordx4 v1, s[40:43], s71 offen offset:3072 sc1 lds\n"
        "s_add_u32 m0, s78, s85\n"
        "v_mfma_f32_16x16x32_bf16 v[36:39], v[12:15], a[180:183], v[36:39]\n"
        "s_add_u32 s69, s69, 1\n"
        "v_mfma_f32_16x16x32_bf16 v[40:43], a[184:187], v[12:15], v[40:43]\n"
        "v_mfma_f32_16x16x32_bf16 v[44:47], a[188:191], v[12:15], v[44:47]\n"
        "s_waitcnt lgkmcnt(1)\n"
        "ds_read_b128 v[12:15], v0 offset:5120\n"
        "v_mfma_f32_16x16x32_bf16 v[24:27], a[192:195], v[16:19], v[24:27]\n"
        "buffer_load_dwordx4 v1, s[40:43], s81 offen offset:0 sc1 lds\n"
        "v_mfma_f32_16x16x32_bf16 v[28:31], a[196:199], v[16:19], v[28:31]\n"
        "v_mfma_f32_16x16x32_bf16 v[32:35], v[16:19], a[200:203], v[32:35]\n"
        "s_getreg_b32 s88, hwreg(HW_REG_IB_STS, 0, 4)\n"
        "v_mfma_f32_16x16x32_bf16 v[36:39], v[16:19], a[204:207], v[36:39]\n"
        "s_cmp_le_u32 s88, 5\n"
        "s_cselect_b32 s88, s73, s89\n"
        "s_cselect_b32 s73, s89, s73\n"
        "v_mfma_f32_16x16x32_bf16 v[40:43], a[208:211], v[16:19], v[40:43]\n"
        "buffer_store_dword v5, v3, s[40:43], s88 offen sc1\n"
        "v_mfma_f32_16x16x32_bf16 v[44:47], a[212:215], v[16:19], v[44:47]\n"
        "s_waitcnt lgkmcnt(1)\n"
        "ds_read_b128 v[16:19], v0 offset:5632\n"
        "v_mfma_f32_16x16x32_bf16 v[24:27], a[216:219], v[8:11], v[24:27]\n"
        "v_mfma_f32_16x16x32_bf16 v[28:31], a[220:223], v[8:11], v[28:31]\n"
        "v_mfma_f32_16x16x32_bf16 v[32:35], v[8:11], a[224:227], v[32:35]\n"
        "v_mfma_f32_16x16x32_bf16 v[36:39], v[8:11], a[228:231], v[36:39]\n"
        "v_mfma_f32_16x16x32_bf16 v[40:43], a[232:235], v[8:11], v[40:43]\n"
        "s_getreg_b32 s88, hwreg(HW_REG_IB_STS, 0, 4)\n"
        "v_mfma_f32_16x16x32_bf16 v[44:47], a[236:239], v[8:11], v[44:47]\n"
        "s_waitcnt lgkmcnt(1)\n"
        "ds_read_b128 v[8:11], v0 offset:6144\n"
        "v_mfma_f32_16x16x32_bf16 v[24:27], a[240:243], v[12:15], v[24:27]\n"
        "s_cmp_le_u32 s88, 6\n"
        "s_cselect_b32 s88, s73, s89\n"
        "s_cselect_b32 s73, s89, s73\n"
        "v_mfma_f32_16x16x32_bf16 v[28:31], a[244:247], v[12:15], v[28:31]\n"
        "buffer_store_dword v5, v3, s[40:43], s88 offen sc1\n"
        "v_mfma_f32_16x16x32_bf16 v[32:35], v[12:15], a[248:251], v[32:35]\n"
        "v_mfma_f32_16x16x32_bf16 v[36:39], v[12:15], a[252:255], v[36:39]\n"
        "v_mfma_f32_16x16x32_bf16 v[40:43], v[80:83], v[12:15], v[40:43]\n"
        "v_mfma_f32_16x16x32_bf16 v[44:47], v[84:87], v[12:15], v[44:47]\n"
        "s_waitcnt lgkmcnt(1)\n"
        "ds_read_b128 v[12:15], v0 offset:6656\n"
        "v_mfma_f32_16x16x32_bf16 v[24:27], v[88:91], v[16:19], v[24:27]\n"
        "s_getreg_b32 s88, hwreg(HW_REG_IB_STS, 0, 4)\n"
        "v_mfma_f32_16x16x32_bf16 v[28:31], v[92:95], v[16:19], v[28:31]\n"
        "s_cmp_le_u32 s88, 7\n"
        "s_cselect_b32 s88, s73, s89\n"
        "s_cselect_b32 s73, s89, s73\n"
        "v_mfma_f32_16x16x32_bf16 v[32:35], v[16:19], v[96:99], v[32:35]\n"
        "buffer_store_dword v5, v3, s[40:43], s88 offen sc1\n"
        "v_mfma_f32_16x16x32_bf16 v[36:39], v[16:19], v[100:103], v[36:39]\n"
        "v_mfma_f32_16x16x32_bf16 v[40:43], v[104:107], v[16:19], v[40:43]\n"
        "v_mfma_f32_16x16x32_bf16 v[44:47], v[108:111], v[16:19], v[44:47]\n"
        "s_waitcnt lgkmcnt(1)\n"
        "ds_read_b128 v[16:19], v0 offset:7168\n"
        "v_mfma_f32_16x16x32_bf16 v[24:27], v[112:115], v[8:11], v[24:27]\n"
        "v_mfma_f32_16x16x32_bf16 v[28:31], v[116:119], v[8:11], v[28:31]\n"
        "v_mfma_f32_16x16x32_bf16 v[32:35], v[8:11], v[120:123], v[32:35]\n"
        "v_mfma_f32_16x16x32_bf16 v[36:39], v[8:11], v[124:127], v[36:39]\n"
        "v_mfma_f32_16x16x32_bf16 v[40:43], v[128:131], v[8:11], v[40:43]\n"
        "v_mfma_f32_16x16x32_bf16 v[44:47], v[132:135], v[8:11], v[44:47]\n"
        "s_waitcnt lgkmcnt(1)\n"
        "ds_read_b128 v[8:11], v0 offset:7680\n"
        "v_mfma_f32_16x16x32_bf16 v[24:27], v[136:139], v[12:15], v[24:27]\n"
        "v_mfma_f32_16x16x32_bf16 v[28:31], v[140:143], v[12:15], v[28:31]\n"
        "v_mfma_f32_16x16x32_bf16 v[32:35], v[12:15], v[144:147], v[32:35]\n"
        "s_waitcnt vmcnt(8)\n"
        "buffer_store_dword v5, v3, s[40:43], s73 offen sc1\n"
        "s_add_u32 s74, s74, 1\n"
        "v_mfma_f32_16x16x32_bf16 v[36:39], v[12:15], v[148:151], v[36:39]\n"
        "s_mov_b32 s84, s89\n"
        "s_mov_b32 s73, s60\n"
        "s_add_u32 s68, s68, 1\n"
        "v_mfma_f32_16x16x32_bf16 v[40:43], v[152:155], v[12:15], v[40:43]\n"
        "s_bitcmp1_b64 s[52:53], s68\n"
        "s_cselect_b32 s66, 8192, 0\n"
        "s_lshl_b32 s75, s68, 21\n"
        "v_mfma_f32_16x16x32_bf16 v[44:47], v[156:159], v[12:15], v[44:47]\n"
        "s_waitcnt lgkmcnt(1)\n"
        "ds_read_b128 v[12:15], v0 offset:8192\n"
        "s_add_u32 s75, s75, s66\n"
        "v_mfma_f32_16x16x32_bf16 v[24:27], v[160:163], v[16:19], v[24:27]\n"
        "s_add_u32 s72, s75, s59\n"
        "v_mfma_f32_16x16x32_bf16 v[28:31], v[164:167], v[16:19], v[28:31]\n"
        "v_mfma_f32_16x16x32_bf16 v[32:35], v[16:19], v[168:171], v[32:35]\n"
        "v_mfma_f32_16x16x32_bf16 v[36:39], v[16:19], v[172:175], v[36:39]\n"
        "v_mfma_f32_16x16x32_bf16 v[40:43], v[176:179], v[16:19], v[40:43]\n"
        "v_mfma_f32_16x16x32_bf16 v[44:47], v[180:183], v[16:19], v[44:47]\n"
        "s_waitcnt lgkmcnt(1)\n"
        "ds_read_b128 v[16:19], v0 offset:8704\n"
        "v_mfma_f32_16x16x32_bf16 v[24:27], v[184:187], v[8:11], v[24:27]\n"
        "v_mfma_f32_16x16x32_bf16 v[28:31], v[188:191], v[8:11], v[28:31]\n"
        "v_mfma_f32_16x16x32_bf16 v[32:35], v[8:11], v[192:195], v[32:35]\n"
        "v_mfma_f32_16x16x32_bf16 v[36:39], v[8:11], v[196:199], v[36:39]\n"
        "v_mfma_f32_16x16x32_bf16 v[40:43], v[200:203], v[8:11], v[40:43]\n"
        "s_waitcnt vmcnt(4)\n"
        "s_barrier\n"
        "v_add_u32 v0, s78, v6\n"
        "v_mfma_f32_16x16x32_bf16 v[44:47], v[204:207], v[8:11], v[44:47]\n"
        "s_waitcnt lgkmcnt(1)\n"
        "ds_read_b128 v[8:11], v0 offset:0\n"
        "v_mfma_f32_16x16x32_bf16 v[24:27], v[208:211], v[12:15], v[24:27]\n"
        "v_mfma_f32_16x16x32_bf16 v[28:31], v[212:215], v[12:15], v[28:31]\n"
        "v_mfma_f32_16x16x32_bf16 v[32:35], v[12:15], v[216:219], v[32:35]\n"
        "v_mfma_f32_16x16x32_bf16 v[36:39], v[12:15], v[220:223], v[36:39]\n"
        "v_mfma_f32_16x16x32_bf16 v[40:43], v[224:227], v[12:15], v[40:43]\n"
        "v_mfma_f32_16x16x32_bf16 v[44:47], v[228:231], v[12:15], v[44:47]\n"
        "s_waitcnt lgkmcnt(1)\n"
        "ds_read_b128 v[12:15], v0 offset:512\n"
        "v_mfma_f32_16x16x32_bf16 v[24:27], v[232:235], v[16:19], v[24:27]\n"
        "v_mfma_f32_16x16x32_bf16 v[28:31], v[236:239], v[16:19], v[28:31]\n"
        "v_mfma_f32_16x16x32_bf16 v[32:35], v[16:19], v[240:243], v[32:35]\n"
        "v_mfma_f32_16x16x32_bf16 v[36:39], v[16:19], v[244:247], v[36:39]\n"
        "v_mfma_f32_16x16x32_bf16 v[40:43], v[248:251], v[16:19], v[40:43]\n"
        "v_mfma_f32_16x16x32_bf16 v[44:47], v[252:255], v[16:19], v[44:47]\n"
        "s_waitcnt lgkmcnt(1)\n"
        "ds_read_b128 v[16:19], v0 offset:1024\n"
        "s_branch .Ls1_i1_%=\n"
        ".Ls1_i00_%=:\n"
        "v_mfma_f32_16x16x32_bf16 v[32:35], v[16:19], a[128:131], v[32:35]\n"
        "buffer_load_dword v4, v2, s[40:43], s86 offen sc1\n"
        "v_mfma_f32_16x16x32_bf16 v[36:39], v[16:19], a[132:135], v[36:39]\n"
        "v_mfma_f32_16x16x32_bf16 v[40:43], a[136:139], v[16:19], v[40:43]\n"
        "v_mfma_f32_16x16x32_bf16 v[44:47], a[140:143], v[16:19], v[44:47]\n"
        "s_waitcnt lgkmcnt(1)\n"
        "ds_read_b128 v[16:19], v0 offset:4096\n"
        "v_mfma_f32_16x16x32_bf16 v[24:27], a[144:147], v[8:11], v[24:27]\n"
        "v_mfma_f32_16x16x32_bf16 v[28:31], a[148:151], v[8:11], v[28:31]\n"
        "v_mfma_f32_16x16x32_bf16 v[32:35], v[8:11], a[152:155], v[32:35]\n"
        "v_mfma_f32_16x16x32_bf16 v[36:39], v[8:11], a[156:159], v[36:39]\n"
        "v_mfma_f32_16x16x32_bf16 v[40:43], a[160:163], v[8:11], v[40:43]\n"
        "v_mfma_f32_16x16x32_bf16 v[44:47], a[164:167], v[8:11], v[44:47]\n"
        "s_waitcnt lgkmcnt(1)\n"
        "ds_read_b128 v[8:11], v0 offset:4608\n"
        "v_mfma_f32_16x16x32_bf16 v[24:27], a[168:171], v[12:15], v[24:27]\n"
        "v_mfma_f32_16x16x32_bf16 v[28:31], a[172:175], v[12:15], v[28:31]\n"
        "v_mfma_f32_16x16x32_bf16 v[32:35], v[12:15], a[176:179], v[32:35]\n"
        "v_mfma_f32_16x16x32_bf16 v[36:39], v[12:15], a[180:183], v[36:39]\n"
        "v_mfma_f32_16x16x32_bf16 v[40:43], a[184:187], v[12:15], v[40:43]\n"
        "v_mfma_f32_16x16x32_bf16 v[44:47], a[188:191], v[12:15], v[44:47]\n"
        "s_waitcnt lgkmcnt(1)\n"
        "ds_read_b128 v[12:15], v0 offset:5120\n"
        "v_mfma_f32_16x16x32_bf16 v[24:27], a[192:195], v[16:19], v[24:27]\n"
        "v_mfma_f32_16x16x32_bf16 v[28:31], a[196:199], v[16:19], v[28:31]\n"
        "v_mfma_f32_16x16x32_bf16 v[32:35], v[16:19], a[200:203], v[32:35]\n"
        "s_getreg_b32 s88, hwreg(HW_REG_IB_STS, 0, 4)\n"
        "v_mfma_f32_16x16x32_bf16 v[36:39], v[16:19], a[204:207], v[36:39]\n"
        "s_cmp_le_u32 s88, 1\n"
        "s_cselect_b32 s88, s73, s89\n"
        "s_cselect_b32 s73, s89, s73\n"
        "v_mfma_f32_16x16x32_bf16 v[40:43], a[208:211], v[16:19], v[40:43]\n"
        "buffer_store_dword v5, v3, s[40:43], s88 offen sc1\n"
        "v_mfma_f32_16x16x32_bf16 v[44:47], a[212:215], v[16:19], v[44:47]\n"
        "s_waitcnt lgkmcnt(1)\n"
        "ds_read_b128 v[16:19], v0 offset:5632\n"
        "v_mfma_f32_16x16x32_bf16 v[24:27], a[216:219], v[8:11], v[24:27]\n"
        "v_mfma_f32_16x16x32_bf16 v[28:31], a[220:223], v[8:11], v[28:31]\n"
        "v_mfma_f32_16x16x32_bf16 v[32:35], v[8:11], a[224:227], v[32:35]\n"
        "v_mfma_f32_16x16x32_bf16 v[36:39], v[8:11], a[228:231], v[36:39]\n"
        "v_mfma_f32_16x16x32_bf16 v[40:43], a[232:235], v[8:11], v[40:43]\n"
        "s_getreg_b32 s88, hwreg(HW_REG_IB_STS, 0, 4)\n"
        "v_mfma_f32_16x16x32_bf16 v[44:47], a[236:239], v[8:11], v[44:47]\n"
        "s_waitcnt vmcnt(1)\n"
        "v_cmp_gt_i32 vcc, s70, v4\n"
        "s_waitcnt lgkmcnt(1)\n"
        "ds_read_b128 v[8:11], v0 offset:6144\n"
        "v_mfma_f32_16x16x32_bf16 v[24:27], a[240:243], v[12:15], v[24:27]\n"
        "s_cmp_le_u32 s88, 2\n"
        "s_cselect_b32 s88, s73, s89\n"
        "s_cselect_b32 s73, s89, s73\n"
        "v_mfma_f32_16x16x32_bf16 v[28:31], a[244:247], v[12:15], v[28:31]\n"
        "buffer_store_dword v5, v3, s[40:43], s88 offen sc1\n"
        "s_and_b64 vcc, vcc, 1\n"
        "v_mfma_f32_16x16x32_bf16 v[32:35], v[12:15], a[248:251], v[32:35]\n"
        "s_cbranch_vccnz .Ls1_i01_%=\n"
        "v_mfma_f32_16x16x32_bf16 v[36:39], v[12:15], a[252:255], v[36:39]\n"
        "buffer_load_dwordx4 v1, s[40:43], s71 offen offset:0 sc1 lds\n"
        "v_mfma_f32_16x16x32_bf16 v[40:43], v[80:83], v[12:15], v[40:43]\n"
        "v_mfma_f32_16x16x32_bf16 v[44:47], v[84:87], v[12:15], v[44:47]\n"
        "s_waitcnt lgkmcnt(1)\n"
        "ds_read_b128 v[12:15], v0 offset:6656\n"
        "v_mfma_f32_16x16x32_bf16 v[24:27], v[88:91], v[16:19], v[24:27]\n"
        "s_getreg_b32 s88, hwreg(HW_REG_IB_STS, 0, 4)\n"
        "buffer_load_dwordx4 v1, s[40:43], s71 offen offset:1024 sc1 lds\n"
        "v_mfma_f32_16x16x32_bf16 v[28:31], v[92:95], v[16:19], v[28:31]\n"
        "s_cmp_le_u32 s88, 15\n"
        "s_cselect_b32 s88, s73, s89\n"
        "s_cselect_b32 s73, s89, s73\n"
        "v_mfma_f32_16x16x32_bf16 v[32:35], v[16:19], v[96:99], v[32:35]\n"
        "buffer_store_dword v5, v3, s[40:43], s88 offen sc1\n"
        "v_mfma_f32_16x16x32_bf16 v[36:39], v[16:19], v[100:103], v[36:39]\n"
        "buffer_load_dwordx4 v1, s[40:43], s71 offen offset:2048 sc1 lds\n"
        "v_mfma_f32_16x16x32_bf16 v[40:43], v[104:107], v[16:19], v[40:43]\n"
        "v_mfma_f32_16x16x32_bf16 v[44:47], v[108:111], v[16:19], v[44:47]\n"
        "s_waitcnt lgkmcnt(1)\n"
        "ds_read_b128 v[16:19], v0 offset:7168\n"
        "v_mfma_f32_16x16x32_bf16 v[24:27], v[112:115], v[8:11], v[24:27]\n"
        "buffer_load_dwordx4 v1, s[40:43], s71 offen offset:3072 sc1 lds\n"
        "s_add_u32 m0, s78, s85\n"
        "v_mfma_f32_16x16x32_bf16 v[28:31], v[116:119], v[8:11], v[28:31]\n"
        "s_add_u32 s69, s69, 1\n"
        "v_mfma_f32_16x16x32_bf16 v[32:35], v[8:11], v[120:123], v[32:35]\n"
        "v_mfma_f32_16x16x32_bf16 v[36:39], v[8:11], v[124:127], v[36:39]\n"
        "buffer_load_dwordx4 v1, s[40:43], s81 offen offset:0 sc1 lds\n"
        "v_mfma_f32_16x16x32_bf16 v[40:43], v[128:131], v[8:11], v[40:43]\n"
        "v_mfma_f32_16x16x32_bf16 v[44:47], v[132:135], v[8:11], v[44:47]\n"
        "s_waitcnt lgkmcnt(1)\n"
        "ds_read_b128 v[8:11], v0 offset:7680\n"
        "v_mfma_f32_16x16x32_bf16 v[24:27], v[136:139], v[12:15], v[24:27]\n"
        "v_mfma_f32_16x16x32_bf16 v[28:31], v[140:143], v[12:15], v[28:31]\n"
        "v_mfma_f32_16x16x32_bf16 v[32:35], v[12:15], v[144:147], v[32:35]\n"
        "buffer_store_dword v5, v3, s[40:43], s73 offen sc1\n"
        "s_add_u32 s74, s74, 1\n"
        "v_mfma_f32_16x16x32_bf16 v[36:39], v[12:15], v[148:151], v[36:39]\n"
        "s_mov_b32 s84, s89\n"
        "s_mov_b32 s73, s60\n"
        "s_add_u32 s68, s68, 1\n"
        "v_mfma_f32_16x16x32_bf16 v[40:43], v[152:155], v[12:15], v[40:43]\n"
        "s_bitcmp1_b64 s[52:53], s68\n"
        "s_cselect_b32 s66, 8192, 0\n"
        "s_lshl_b32 s75, s68, 21\n"
        "v_mfma_f32_16x16x32_bf16 v[44:47], v[156:159], v[12:15], v[44:47]\n"
        "s_waitcnt lgkmcnt(1)\n"
        "ds_read_b128 v[12:15], v0 offset:8192\n"
        "s_add_u32 s75, s75, s66\n"
        "v_mfma_f32_16x16x32_bf16 v[24:27], v[160:163], v[16:19], v[24:27]\n"
        "s_add_u32 s72, s75, s59\n"
        "v_mfma_f32_16x16x32_bf16 v[28:31], v[164:167], v[16:19], v[28:31]\n"
        "v_mfma_f32_16x16x32_bf16 v[32:35], v[16:19], v[168:171], v[32:35]\n"
        "v_mfma_f32_16x16x32_bf16 v[36:39], v[16:19], v[172:175], v[36:39]\n"
        "v_mfma_f32_16x16x32_bf16 v[40:43], v[176:179], v[16:19], v[40:43]\n"
        "v_mfma_f32_16x16x32_bf16 v[44:47], v[180:183], v[16:19], v[44:47]\n"
        "s_waitcnt lgkmcnt(1)\n"
        "ds_read_b128 v[16:19], v0 offset:8704\n"
        "v_mfma_f32_16x16x32_bf16 v[24:27], v[184:187], v[8:11], v[24:27]\n"
        "v_mfma_f32_16x16x32_bf16 v[28:31], v[188:191], v[8:11], v[28:31]\n"
        "v_mfma_f32_16x16x32_bf16 v[32:35], v[8:11], v[192:195], v[32:35]\n"
        "v_mfma_f32_16x16x32_bf16 v[36:39], v[8:11], v[196:199], v[36:39]\n"
        "v_mfma_f32_16x16x32_bf16 v[40:43], v[200:203], v[8:11], v[40:43]\n"
        "s_waitcnt vmcnt(1)\n"
        "s_barrier\n"
        "v_add_u32 v0, s78, v6\n"
        "v_mfma_f32_16x16x32_bf16 v[44:47], v[204:207], v[8:11], v[44:47]\n"
        "s_waitcnt lgkmcnt(1)\n"
        "ds_read_b128 v[8:11], v0 offset:0\n"
        "v_mfma_f32_16x16x32_bf16 v[24:27], v[208:211], v[12:15], v[24:27]\n"
        "v_mfma_f32_16x16x32_bf16 v[28:31], v[212:215], v[12:15], v[28:31]\n"
        "v_mfma_f32_16x16x32_bf16 v[32:35], v[12:15], v[216:219], v[32:35]\n"
        "v_mfma_f32_16x16x32_bf16 v[36:39], v[12:15], v[220:223], v[36:39]\n"
        "v_mfma_f32_16x16x32_bf16 v[40:43], v[224:227], v[12:15], v[40:43]\n"
        "v_mfma_f32_16x16x32_bf16 v[44:47], v[228:231], v[12:15], v[44:47]\n"
        "s_waitcnt lgkmcnt(1)\n"
        "ds_read_b128 v[12:15], v0 offset:512\n"
        "v_mfma_f32_16x16x32_bf16 v[24:27], v[232:235], v[16:19], v[24:27]\n"
        "v_mfma_f32_16x16x32_bf16 v[28:31], v[236:239], v[16:19], v[28:31]\n"
        "v_mfma_f32_16x16x32_bf16 v[32:35], v[16:19], v[240:243], v[32:35]\n"
        "v_mfma_f32_16x16x32_bf16 v[36:39], v[16:19], v[244:247], v[36:39]\n"
        "v_mfma_f32_16x16x32_bf16 v[40:43], v[248:251], v[16:19], v[40:43]\n"
        "v_mfma_f32_16x16x32_bf16 v[44:47], v[252:255], v[16:19], v[44:47]\n"
        "s_waitcnt lgkmcnt(1)\n"
        "ds_read_b128 v[16:19], v0 offset:1024\n"
        "s_branch .Ls1_i1_%=\n"
        ".Ls1_i01_%=:\n"
        "v_mfma_f32_16x16x32_bf16 v[36:39], v[12:15], a[252:255], v[36:39]\n"
        "buffer_load_dword v4, v2, s[40:43], s86 offen sc1\n"
        "v_mfma_f32_16x16x32_bf16 v[40:43], v[80:83], v[12:15], v[40:43]\n"
        "v_mfma_f32_16x16x32_bf16 v[44:47], v[84:87], v[12:15], v[44:47]\n"
        "s_waitcnt lgkmcnt(1)\n"
        "ds_read_b128 v[12:15], v0 offset:6656\n"
        "v_mfma_f32_16x16x32_bf16 v[24:27], v[88:91], v[16:19], v[24:27]\n"
        "s_getreg_b32 s88, hwreg(HW_REG_IB_STS, 0, 4)\n"
        "v_mfma_f32_16x16x32_bf16 v[28:31], v[92:95], v[16:19], v[28:31]\n"
        "s_cmp_le_u32 s88, 15\n"
        "s_cselect_b32 s88, s73, s89\n"
        "s_cselect_b32 s73, s89, s73\n"
        "v_mfma_f32_16x16x32_bf16 v[32:35], v[16:19], v[96:99], v[32:35]\n"
        "buffer_store_dword v5, v3, s[40:43], s88 offen sc1\n"
        "v_mfma_f32_16x16x32_bf16 v[36:39], v[16:19], v[100:103], v[36:39]\n"
        "v_mfma_f32_16x16x32_bf16 v[40:43], v[104:107], v[16:19], v[40:43]\n"
        "v_mfma_f32_16x16x32_bf16 v[44:47], v[108:111], v[16:19], v[44:47]\n"
        "s_waitcnt lgkmcnt(1)\n"
        "ds_read_b128 v[16:19], v0 offset:7168\n"
        "v_mfma_f32_16x16x32_bf16 v[24:27], v[112:115], v[8:11], v[24:27]\n"
        "v_mfma_f32_16x16x32_bf16 v[28:31], v[116:119], v[8:11], v[28:31]\n"
        "v_mfma_f32_16x16x32_bf16 v[32:35], v[8:11], v[120:123], v[32:35]\n"
        "v_mfma_f32_16x16x32_bf16 v[36:39], v[8:11], v[124:127], v[36:39]\n"
        "v_mfma_f32_16x16x32_bf16 v[40:43], v[128:131], v[8:11], v[40:43]\n"
        "v_mfma_f32_16x16x32_bf16 v[44:47], v[132:135], v[8:11], v[44:47]\n"
        "s_waitcnt lgkmcnt(1)\n"
        "ds_read_b128 v[8:11], v0 offset:7680\n"
        "v_mfma_f32_16x16x32_bf16 v[24:27], v[136:139], v[12:15], v[24:27]\n"
        "v_mfma_f32_16x16x32_bf16 v[28:31], v[140:143], v[12:15], v[28:31]\n"
        "v_mfma_f32_16x16x32_bf16 v[32:35], v[12:15], v[144:147], v[32:35]\n"
        "buffer_store_dword v5, v3, s[40:43], s73 offen sc1\n"
        "s_add_u32 s74, s74, 1\n"
        "v_mfma_f32_16x16x32_bf16 v[36:39], v[12:15], v[148:151], v[36:39]\n"
        "s_mov_b32 s84, s89\n"
        "s_mov_b32 s73, s60\n"
        "s_add_u32 s68, s68, 1\n"
        "v_mfma_f32_16x16x32_bf16 v[40:43], v[152:155], v[12:15], v[40:43]\n"
        "s_bitcmp1_b64 s[52:53], s68\n"
        "s_cselect_b32 s66, 8192, 0\n"
        "s_lshl_b32 s75, s68, 21\n"
        "v_mfma_f32_16x16x32_bf16 v[44:47], v[156:159], v[12:15], v[44:47]\n"
        "s_waitcnt lgkmcnt(1)\n"
        "ds_read_b128 v[12:15], v0 offset:8192\n"
        "s_add_u32 s75, s75, s66\n"
        "v_mfma_f32_16x16x32_bf16 v[24:27], v[160:163], v[16:19], v[24:27]\n"
        "s_add_u32 s72, s75, s59\n"
        "v_mfma_f32_16x16x32_bf16 v[28:31], v[164:167], v[16:19], v[28:31]\n"
        "v_mfma_f32_16x16x32_bf16 v[32:35], v[16:19], v[168:171], v[32:35]\n"
        "v_mfma_f32_16x16x32_bf16 v[36:39], v[16:19], v[172:175], v[36:39]\n"
        "v_mfma_f32_16x16x32_bf16 v[40:43], v[176:179], v[16:19], v[40:43]\n"
        "v_mfma_f32_16x16x32_bf16 v[44:47], v[180:183], v[16:19], v[44:47]\n"
        "s_waitcnt lgkmcnt(1)\n"
        "ds_read_b128 v[16:19], v0 offset:8704\n"
        "v_mfma_f32_16x16x32_bf16 v[24:27], v[184:187], v[8:11], v[24:27]\n"
        "s_waitcnt vmcnt(2)\n"
        "v_cmp_gt_i32 vcc, s70, v4\n"
        "v_mfma_f32_16x16x32_bf16 v[28:31], v[188:191], v[8:11], v[28:31]\n"
        "v_mfma_f32_16x16x32_bf16 v[32:35], v[8:11], v[192:195], v[32:35]\n"
        "s_and_b64 vcc, vcc, 1\n"
        "v_mfma_f32_16x16x32_bf16 v[36:39], v[8:11], v[196:199], v[36:39]\n"
        "s_cbranch_vccnz .Ls1_i02_%=\n"
        "v_mfma_f32_16x16x32_bf16 v[40:43], v[200:203], v[8:11], v[40:43]\n"
        "buffer_load_dwordx4 v1, s[40:43], s71 offen offset:0 sc1 lds\n"
        "v_mfma_f32_16x16x32_bf16 v[44:47], v[204:207], v[8:11], v[44:47]\n"
        "s_waitcnt lgkmcnt(1)\n"
        "v_mfma_f32_16x16x32_bf16 v[24:27], v[208:211], v[12:15], v[24:27]\n"
        "buffer_load_dwordx4 v1, s[40:43], s71 offen offset:1024 sc1 lds\n"
        "v_mfma_f32_16x16x32_bf16 v[28:31], v[212:215], v[12:15], v[28:31]\n"
        "v_mfma_f32_16x16x32_bf16 v[32:35], v[12:15], v[216:219], v[32:35]\n"
        "buffer_load_dwordx4 v1, s[40:43], s71 offen offset:2048 sc1 lds\n"
        "v_mfma_f32_16x16x32_bf16 v[36:39], v[12:15], v[220:223], v[36:39]\n"
        "v_mfma_f32_16x16x32_bf16 v[40:43], v[224:227], v[12:15], v[40:43]\n"
        "buffer_load_dwordx4 v1, s[40:43], s71 offen offset:3072 sc1 lds\n"
        "s_add_u32 m0, s78, s85\n"
        "s_add_u32 s69, s69, 1\n"
        "v_mfma_f32_16x16x32_bf16 v[44:47], v[228:231], v[12:15], v[44:47]\n"
        "s_waitcnt lgkmcnt(0)\n"
        "v_mfma_f32_16x16x32_bf16 v[24:27], v[232:235], v[16:19], v[24:27]\n"
        "buffer_load_dwordx4 v1, s[40:43], s81 offen offset:0 sc1 lds\n"
        "v_mfma_f32_16x16x32_bf16 v[28:31], v[236:239], v[16:19], v[28:31]\n"
        "v_mfma_f32_16x16x32_bf16 v[32:35], v[16:19], v[240:243], v[32:35]\n"
        "v_mfma_f32_16x16x32_bf16 v[36:39], v[16:19], v[244:247], v[36:39]\n"
        "v_mfma_f32_16x16x32_bf16 v[40:43], v[248:251], v[16:19], v[40:43]\n"
        "v_mfma_f32_16x16x32_bf16 v[44:47], v[252:255], v[16:19], v[44:47]\n"
        "v_cvt_pk_bf16_f32 v24, v24, v25\n"
        "v_cvt_pk_bf16_f32 v25, v26, v27\n"
        "v_cvt_pk_bf16_f32 v26, v28, v29\n"
        "v_cvt_pk_bf16_f32 v27, v30, v31\n"
        "s_add_u32 s66, s72, 524288\n"
        "buffer_store_dwordx4 v[24:27], v1, s[40:43], s66 offen offset:0 sc1\n"
        "v_cvt_pk_bf16_f32 v72, v32, v33\n"
        "v_cvt_pk_bf16_f32 v73, v34, v35\n"
        "v_cvt_pk_bf16_f32 v76, v36, v37\n"
        "v_cvt_pk_bf16_f32 v77, v38, v39\n"
        "v_cvt_pk_bf16_f32 v40, v40, v41\n"
        "v_cvt_pk_bf16_f32 v41, v42, v43\n"
        "v_cvt_pk_bf16_f32 v42, v44, v45\n"
        "v_cvt_pk_bf16_f32 v43, v46, v47\n"
        "buffer_store_dwordx4 v[40:43], v1, s[40:43], s72 offen offset:0 sc1\n"
        "v_mov_b32 v5, s74\n"
        "s_mov_b32 s84, s60\n"
        "s_waitcnt vmcnt(2)\n"
        "s_branch .Ls1_go0_%=\n"
        ".Ls1_i02_%=:\n"
        "v_mfma_f32_16x16x32_bf16 v[40:43], v[200:203], v[8:11], v[40:43]\n"
        "buffer_load_dword v4, v2, s[40:43], s86 offen sc1\n"
        "v_mfma_f32_16x16x32_bf16 v[44:47], v[204:207], v[8:11], v[44:47]\n"
        "s_waitcnt lgkmcnt(1)\n"
        "v_mfma_f32_16x16x32_bf16 v[24:27], v[208:211], v[12:15], v[24:27]\n"
        "v_mfma_f32_16x16x32_bf16 v[28:31], v[212:215], v[12:15], v[28:31]\n"
        "v_mfma_f32_16x16x32_bf16 v[32:35], v[12:15], v[216:219], v[32:35]\n"
        "v_mfma_f32_16x16x32_bf16 v[36:39], v[12:15], v[220:223], v[36:39]\n"
        "v_mfma_f32_16x16x32_bf16 v[40:43], v[224:227], v[12:15], v[40:43]\n"
        "v_mfma_f32_16x16x32_bf16 v[44:47], v[228:231], v[12:15], v[44:47]\n"
        "s_waitcnt lgkmcnt(0)\n"
        "v_mfma_f32_16x16x32_bf16 v[24:27], v[232:235], v[16:19], v[24:27]\n"
        "v_mfma_f32_16x16x32_bf16 v[28:31], v[236:239], v[16:19], v[28:31]\n"
        "v_mfma_f32_16x16x32_bf16 v[32:35], v[16:19], v[240:243], v[32:35]\n"
        "v_mfma_f32_16x16x32_bf16 v[36:39], v[16:19], v[244:247], v[36:39]\n"
        "v_mfma_f32_16x16x32_bf16 v[40:43], v[248:251], v[16:19], v[40:43]\n"
        "v_mfma_f32_16x16x32_bf16 v[44:47], v[252:255], v[16:19], v[44:47]\n"
        "v_cvt_pk_bf16_f32 v24, v24, v25\n"
        "v_cvt_pk_bf16_f32 v25, v26, v27\n"
        "v_cvt_pk_bf16_f32 v26, v28, v29\n"
        "v_cvt_pk_bf16_f32 v27, v30, v31\n"
        "s_add_u32 s66, s72, 524288\n"
        "buffer_store_dwordx4 v[24:27], v1, s[40:43], s66 offen offset:0 sc1\n"
        "v_cvt_pk_bf16_f32 v72, v32, v33\n"
        "v_cvt_pk_bf16_f32 v73, v34, v35\n"
        "v_cvt_pk_bf16_f32 v76, v36, v37\n"
        "v_cvt_pk_bf16_f32 v77, v38, v39\n"
        "v_cvt_pk_bf16_f32 v40, v40, v41\n"
        "v_cvt_pk_bf16_f32 v41, v42, v43\n"
        "v_cvt_pk_bf16_f32 v42, v44, v45\n"
        "v_cvt_pk_bf16_f32 v43, v46, v47\n"
        "buffer_store_dwordx4 v[40:43], v1, s[40:43], s72 offen offset:0 sc1\n"
        "s_waitcnt vmcnt(2)\n"
        "v_cmp_gt_i32 vcc, s70, v4\n"
        "s_nop 0\n"
        "s_and_b64 vcc, vcc, 1\n"
        "s_cbranch_vccnz .Ls1_late0_%=\n"
        "buffer_load_dwordx4 v1, s[40:43], s71 offen offset:0 sc1 lds\n"
        "buffer_load_dwordx4 v1, s[40:43], s71 offen offset:1024 sc1 lds\n"
        "buffer_load_dwordx4 v1, s[40:43], s71 offen offset:2048 sc1 lds\n"
        "buffer_load_dwordx4 v1, s[40:43], s71 offen offset:3072 sc1 lds\n"
        "s_add_u32 m0, s78, s85\n"
        "s_add_u32 s69, s69, 1\n"
        "buffer_load_dwordx4 v1, s[40:43], s81 offen offset:0 sc1 lds\n"
        "s_waitcnt vmcnt(5)\n"
        "v_mov_b32 v5, s74\n"
        "buffer_store_dword v5, v3, s[40:43], s73 offen sc1\n"
        "s_waitcnt vmcnt(1)\n"
        "s_branch .Ls1_go0_%=\n"
        ".Ls1_late0_%=:\n"
        "s_waitcnt vmcnt(0)\n"
        "v_mov_b32 v5, s74\n"
        "buffer_store_dword v5, v3, s[40:43], s73 offen sc1\n"
        "s_mov_b32 s64, 0\n"
        ".Ls1_in0_top_%=:\n"
        "buffer_load_dword v4, v2, s[40:43], s86 offen sc1\n"
        "s_waitcnt vmcnt(0)\n"
        "v_cmp_gt_i32 vcc, s70, v4\n"
        "s_nop 0\n"
        "s_and_b64 vcc, vcc, 1\n"
        "s_cbranch_vccz .Ls1_in0_ok_%=\n"
        "s_add_u32 s64, s64, 1\n"
        "s_and_b32 s65, s64, 255\n"
        "s_cbranch_scc1 .Ls1_in0_go_%=\n"
        "s_mov_b32 s65, 49152\n"
        "s_cmp_lt_u32 s64, 1048576\n"
        "s_cbranch_scc1 .Ls1_in0_chk_%=\n"
        "v_mov_b32 v4, 1\n"
        "buffer_store_dword v4, off, s[40:43], s65 sc1\n"
        "s_waitcnt vmcnt(0)\n"
        ".Ls1_in0_chk_%=:\n"
        "buffer_load_dword v4, off, s[40:43], s65 sc1\n"
        "s_waitcnt vmcnt(0)\n"
        "v_readfirstlane_b32 s65, v4\n"
        "s_nop 0\n"
        "s_cmp_lg_u32 s65, 1\n"
        "s_cbranch_scc1 .Ls1_in0_go_%=\n"
        "s_endpgm\n"
        ".Ls1_in0_go_%=:\n"
        "s_branch .Ls1_in0_top_%=\n"
        ".Ls1_in0_ok_%=:\n"
        "buffer_load_dwordx4 v1, s[40:43], s71 offen offset:0 sc1 lds\n"
        "buffer_load_dwordx4 v1, s[40:43], s71 offen offset:1024 sc1 lds\n"
        "buffer_load_dwordx4 v1, s[40:43], s71 offen offset:2048 sc1 lds\n"
        "buffer_load_dwordx4 v1, s[40:43], s71 offen offset:3072 sc1 lds\n"
        "s_add_u32 m0, s78, s85\n"
        "s_add_u32 s69, s69, 1\n"
        "buffer_load_dwordx4 v1, s[40:43], s81 offen offset:0 sc1 lds\n"
        "s_waitcnt vmcnt(0)\n"
        ".Ls1_go0_%=:\n"
        "s_mov_b32 s72, 241172480\n"
        "s_mov_b32 s83, 241172480\n"
        "s_mov_b32 s73, 241172480\n"
        "v_add_u32 v0, s78, v6\n"
        "s_barrier\n"
        "ds_read_b128 v[8:11], v0 offset:0\n"
        "ds_read_b128 v[12:15], v0 offset:512\n"
        "ds_read_b128 v[16:19], v0 offset:1024\n"
        "s_waitcnt lgkmcnt(2)\n"
        "s_branch .Ls1_i1_%=\n"
        ".Ls1_i1_%=:\n"
        "v_mfma_f32_16x16x32_bf16 v[48:51], a[0:3], v[8:11], 0\n"
        "s_add_u32 s86, s76, 0\n"
        "buffer_load_dword v4, v2, s[40:43], s86 offen sc1\n"
        "v_mfma_f32_16x16x32_bf16 v[52:55], a[4:7], v[8:11], 0\n"
        "s_lshr_b32 s67, s69, 1\n"
        "v_cvt_pk_bf16_f32 v24, v24, v25\n"
        "v_cvt_pk_bf16_f32 v25, v26, v27\n"
        "v_mfma_f32_16x16x32_bf16 v[56:59], v[8:11], a[8:11], 0\n"
        "s_bitcmp1_b64 s[52:53], s67\n"
        "s_cselect_b32 s65, 8192, 0\n"
        "v_cvt_pk_bf16_f32 v26, v28, v29\n"
        "v_mfma_f32_16x16x32_bf16 v[60:63], v[8:11], a[12:15], 0\n"
        "s_lshl_b32 s77, s67, 21\n"
        "v_cvt_pk_bf16_f32 v27, v30, v31\n"
        "s_add_u32 s66, s72, 524288\n"
        "v_mfma_f32_16x16x32_bf16 v[64:67], a[16:19], v[8:11], 0\n"
        "s_add_u32 s77, s77, s65\n"
        "buffer_store_dwordx4 v[24:27], v1, s[40:43], s66 offen offset:0 sc1\n"
        "v_mfma_f32_16x16x32_bf16 v[68:71], a[20:23], v[8:11], 0\n"
        "s_waitcnt lgkmcnt(1)\n"
        "ds_read_b128 v[8:11], v0 offset:1536\n"
        "v_cvt_pk_bf16_f32 v72, v32, v33\n"
        "v_mfma_f32_16x16x32_bf16 v[48:51], a[24:27], v[12:15], v[48:51]\n"
        "s_add_u32 s71, s77, s58\n"
        "v_cvt_pk_bf16_f32 v73, v34, v35\n"
        "v_cvt_pk_bf16_f32 v76, v36, v37\n"
        "v_mfma_f32_16x16x32_bf16 v[52:55], a[28:31], v[12:15], v[52:55]\n"
        "s_add_u32 s81, s77, s62\n"
        "v_cvt_pk_bf16_f32 v77, v38, v39\n"
        "v_cvt_pk_bf16_f32 v40, v40, v41\n"
        "v_mfma_f32_16x16x32_bf16 v[56:59], v[12:15], a[32:35], v[56:59]\n"
        "s_add_u32 s78, s78, 18432\n"
        "v_cvt_pk_bf16_f32 v41, v42, v43\n"
        "v_cvt_pk_bf16_f32 v42, v44, v45\n"
        "v_mfma_f32_16x16x32_bf16 v[60:63], v[12:15], a[36:39], v[60:63]\n"
        "s_cmp_eq_u32 s78, 55296\n"
        "s_cselect_b32 s78, 0, s78\n"
        "v_cvt_pk_bf16_f32 v43, v46, v47\n"
        "v_mfma_f32_16x16x32_bf16 v[64:67], a[40:43], v[12:15], v[64:67]\n"
        "s_add_u32 m0, s78, s61\n"
        "v_mfma_f32_16x16x32_bf16 v[68:71], a[44:47], v[12:15], v[68:71]\n"
        "s_waitcnt lgkmcnt(1)\n"
        "ds_read_b128 v[12:15], v0 offset:2048\n"
        "v_mfma_f32_16x16x32_bf16 v[48:51], a[48:51], v[16:19], v[48:51]\n"
        "s_lshr_b32 s70, s69, 1\n"
        "buffer_store_dwordx4 v[40:43], v1, s[40:43], s72 offen offset:0 sc1\n"
        "v_mfma_f32_16x16x32_bf16 v[52:55], a[52:55], v[16:19], v[52:55]\n"
        "v_mfma_f32_16x16x32_bf16 v[56:59], v[16:19], a[56:59], v[56:59]\n"
        "s_waitcnt vmcnt(3)\n"
        "v_mfma_f32_16x16x32_bf16 v[60:63], v[16:19], a[60:63], v[60:63]\n"
        "buffer_store_dword v5, v3, s[40:43], s84 offen sc1\n"
        "v_mfma_f32_16x16x32_bf16 v[64:67], a[64:67], v[16:19], v[64:67]\n"
        "v_mov_b32 v5, s74\n"
        "v_mfma_f32_16x16x32_bf16 v[68:71], a[68:71], v[16:19], v[68:71]\n"
        "s_waitcnt lgkmcnt(1)\n"
        "ds_read_b128 v[16:19], v0 offset:2560\n"
        "v_mfma_f32_16x16x32_bf16 v[48:51], a[72:75], v[8:11], v[48:51]\n"
        "v_mfma_f32_16x16x32_bf16 v[52:55], a[76:79], v[8:11], v[52:55]\n"
        "v_mfma_f32_16x16x32_bf16 v[56:59], v[8:11], a[80:83], v[56:59]\n"
        "v_mfma_f32_16x16x32_bf16 v[60:63], v[8:11], a[84:87], v[60:63]\n"
        "v_mfma_f32_16x16x32_bf16 v[64:67], a[88:91], v[8:11], v[64:67]\n"
        "v_mfma_f32_16x16x32_bf16 v[68:71], a[92:95], v[8:11], v[68:71]\n"
        "s_waitcnt lgkmcnt(1)\n"
        "ds_read_b128 v[8:11], v0 offset:3072\n"
        "v_mfma_f32_16x16x32_bf16 v[48:51], a[96:99], v[12:15], v[48:51]\n"
        "v_mfma_f32_16x16x32_bf16 v[52:55], a[100:103], v[12:15], v[52:55]\n"
        "v_mfma_f32_16x16x32_bf16 v[56:59], v[12:15], a[104:107], v[56:59]\n"
        "v_mfma_f32_16x16x32_bf16 v[60:63], v[12:15], a[108:111], v[60:63]\n"
        "v_mfma_f32_16x16x32_bf16 v[64:67], a[112:115], v[12:15], v[64:67]\n"
        "s_waitcnt vmcnt(3)\n"
        "v_cmp_gt_i32 vcc, s70, v4\n"
        "v_mfma_f32_16x16x32_bf16 v[68:71], a[116:119], v[12:15], v[68:71]\n"
        "s_waitcnt lgkmcnt(1)\n"
        "ds_read_b128 v[12:15], v0 offset:3584\n"
        "v_mfma_f32_16x16x32_bf16 v[48:51], a[120:123], v[16:19], v[48:51]\n"
        "s_and_b64 vcc, vcc, 1\n"
        "v_mfma_f32_16x16x32_bf16 v[52:55], a[124:127], v[16:19], v[52:55]\n"
        "s_cbranch_vccnz .Ls1_i10_%=\n"
        "v_mfma_f32_16x16x32_bf16 v[56:59], v[16:19], a[128:131], v[56:59]\n"
        "buffer_load_dwordx4 v1, s[40:43], s71 offen offset:0 sc1 lds\n"
        "v_mfma_f32_16x16x32_bf16 v[60:63], v[16:19], a[132:135], v[60:63]\n"
        "v_mfma_f32_16x16x32_bf16 v[64:67], a[136:139], v[16:19], v[64:67]\n"
        "v_mfma_f32_16x16x32_bf16 v[68:71], a[140:143], v[16:19], v[68:71]\n"
        "s_waitcnt lgkmcnt(1)\n"
        "ds_read_b128 v[16:19], v0 offset:4096\n"
        "v_mfma_f32_16x16x32_bf16 v[48:51], a[144:147], v[8:11], v[48:51]\n"
        "s_getreg_b32 s88, hwreg(HW_REG_IB_STS, 0, 4)\n"
        "buffer_load_dwordx4 v1, s[40:43], s71 offen offset:1024 sc1 lds\n"
        "v_mfma_f32_16x16x32_bf16 v[52:55], a[148:151], v[8:11], v[52:55]\n"
        "s_cmp_le_u32 s88, 2\n"
        "s_cselect_b32 s88, s73, s89\n"
        "s_cselect_b32 s73, s89, s73\n"
        "v_mfma_f32_16x16x32_bf16 v[56:59], v[8:11], a[152:155], v[56:59]\n"
        "buffer_store_dword v5, v3, s[40:43], s88 offen sc1\n"
        "v_mfma_f32_16x16x32_bf16 v[60:63], v[8:11], a[156:159], v[60:63]\n"
        "v_mfma_f32_16x16x32_bf16 v[64:67], a[160:163], v[8:11], v[64:67]\n"
        "buffer_load_dwordx4 v1, s[40:43], s71 offen offset:2048 sc1 lds\n"
        "v_mfma_f32_16x16x32_bf16 v[68:71], a[164:167], v[8:11], v[68:71]\n"
        "s_waitcnt lgkmcnt(1)\n"
        "ds_read_b128 v[8:11], v0 offset:4608\n"
        "v_mfma_f32_16x16x32_bf16 v[48:51], a[168:171], v[12:15], v[48:51]\n"
        "v_mfma_f32_16x16x32_bf16 v[52:55], a[172:175], v[12:15], v[52:55]\n"
        "v_mfma_f32_16x16x32_bf16 v[56:59], v[12:15], a[176:179], v[56:59]\n"
        "s_getreg_b32 s88, hwreg(HW_REG_IB_STS, 0, 4)\n"
        "buffer_load_dwordx4 v1, s[40:43], s71 offen offset:3072 sc1 lds\n"
        "s_add_u32 m0, s78, s85\n"
        "v_mfma_f32_16x16x32_bf16 v[60:63], v[12:15], a[180:183], v[60:63]\n"
        "s_cmp_le_u32 s88, 5\n"
        "s_cselect_b32 s88, s73, s89\n"
        "s_cselect_b32 s73, s89, s73\n"
        "v_mfma_f32_16x16x32_bf16 v[64:67], a[184:187], v[12:15], v[64:67]\n"
        "s_add_u32 s69, s69, 1\n"
        "buffer_store_dword v5, v3, s[40:43], s88 offen sc1\n"
        "v_mfma_f32_16x16x32_bf16 v[68:71], a[188:191], v[12:15], v[68:71]\n"
        "s_waitcnt lgkmcnt(1)\n"
        "ds_read_b128 v[12:15], v0 offset:5120\n"
        "v_mfma_f32_16x16x32_bf16 v[48:51], a[192:195], v[16:19], v[48:51]\n"
        "buffer_load_dwordx4 v1, s[40:43], s81 offen offset:0 sc1 lds\n"
        "v_mfma_f32_16x16x32_bf16 v[52:55], a[196:199], v[16:19], v[52:55]\n"
        "v_mfma_f32_16x16x32_bf16 v[56:59], v[16:19], a[200:203], v[56:59]\n"
        "v_mfma_f32_16x16x32_bf16 v[60:63], v[16:19], a[204:207], v[60:63]\n"
        "v_mfma_f32_16x16x32_bf16 v[64:67], a[208:211], v[16:19], v[64:67]\n"
        "s_getreg_b32 s88, hwreg(HW_REG_IB_STS, 0, 4)\n"
        "v_mfma_f32_16x16x32_bf16 v[68:71], a[212:215], v[16:19], v[68:71]\n"
        "s_waitcnt lgkmcnt(1)\n"
        "ds_read_b128 v[16:19], v0 offset:5632\n"
        "v_mfma_f32_16x16x32_bf16 v[48:51], a[216:219], v[8:11], v[48:51]\n"
        "s_cmp_le_u32 s88, 8\n"
        "s_cselect_b32 s88, s73, s89\n"
        "s_cselect_b32 s73, s89, s73\n"
        "v_mfma_f32_16x16x32_bf16 v[52:55], a[220:223], v[8:11], v[52:55]\n"
        "buffer_store_dword v5, v3, s[40:43], s88 offen sc1\n"
        "v_mfma_f32_16x16x32_bf16 v[56:59], v[8:11], a[224:227], v[56:59]\n"
        "v_mfma_f32_16x16x32_bf16 v[60:63], v[8:11], a[228:231], v[60:63]\n"
        "v_mfma_f32_16x16x32_bf16 v[64:67], a[232:235], v[8:11], v[64:67]\n"
        "v_mfma_f32_16x16x32_bf16 v[68:71], a[236:239], v[8:11], v[68:71]\n"
        "s_waitcnt lgkmcnt(1)\n"
        "ds_read_b128 v[8:11], v0 offset:6144\n"
        "v_mfma_f32_16x16x32_bf16 v[48:51], a[240:243], v[12:15], v[48:51]\n"
        "v_mfma_f32_16x16x32_bf16 v[52:55], a[244:247], v[12:15], v[52:55]\n"
        "v_mfma_f32_16x16x32_bf16 v[56:59], v[12:15], a[248:251], v[56:59]\n"
        "v_mfma_f32_16x16x32_bf16 v[60:63], v[12:15], a[252:255], v[60:63]\n"
        "v_mfma_f32_16x16x32_bf16 v[64:67], v[80:83], v[12:15], v[64:67]\n"
        "v_mfma_f32_16x16x32_bf16 v[68:71], v[84:87], v[12:15], v[68:71]\n"
        "s_waitcnt lgkmcnt(1)\n"
        "ds_read_b128 v[12:15], v0 offset:6656\n"
        "v_mfma_f32_16x16x32_bf16 v[48:51], v[88:91], v[16:19], v[48:51]\n"
        "s_waitcnt vmcnt(9)\n"
        "buffer_store_dword v5, v3, s[40:43], s73 offen sc1\n"
        "s_add_u32 s74, s74, 1\n"
        "v_mfma_f32_16x16x32_bf16 v[52:55], v[92:95], v[16:19], v[52:55]\n"
        "s_mov_b32 s84, s89\n"
        "s_mov_b32 s73, s60\n"
        "s_add_u32 s72, s75, s59\n"
        "v_mfma_f32_16x16x32_bf16 v[56:59], v[16:19], v[96:99], v[56:59]\n"
        "v_mfma_f32_16x16x32_bf16 v[60:63], v[16:19], v[100:103], v[60:63]\n"
        "v_mfma_f32_16x16x32_bf16 v[64:67], v[104:107], v[16:19], v[64:67]\n"
        "v_mfma_f32_16x16x32_bf16 v[68:71], v[108:111], v[16:19], v[68:71]\n"
        "s_waitcnt lgkmcnt(1)\n"
        "ds_read_b128 v[16:19], v0 offset:7168\n"
        "v_mfma_f32_16x16x32_bf16 v[48:51], v[112:115], v[8:11], v[48:51]\n"
        "v_mfma_f32_16x16x32_bf16 v[52:55], v[116:119], v[8:11], v[52:55]\n"
        "v_mfma_f32_16x16x32_bf16 v[56:59], v[8:11], v[120:123], v[56:59]\n"
        "v_mfma_f32_16x16x32_bf16 v[60:63], v[8:11], v[124:127], v[60:63]\n"
        "v_mfma_f32_16x16x32_bf16 v[64:67], v[128:131], v[8:11], v[64:67]\n"
        "v_mfma_f32_16x16x32_bf16 v[68:71], v[132:135], v[8:11], v[68:71]\n"
        "s_waitcnt lgkmcnt(1)\n"
        "ds_read_b128 v[8:11], v0 offset:7680\n"
        "v_mfma_f32_16x16x32_bf16 v[48:51], v[136:139], v[12:15], v[48:51]\n"
        "v_mfma_f32_16x16x32_bf16 v[52:55], v[140:143], v[12:15], v[52:55]\n"
        "v_mfma_f32_16x16x32_bf16 v[56:59], v[12:15], v[144:147], v[56:59]\n"
        "v_mfma_f32_16x16x32_bf16 v[60:63], v[12:15], v[148:151], v[60:63]\n"
        "v_mfma_f32_16x16x32_bf16 v[64:67], v[152:155], v[12:15], v[64:67]\n"
        "v_mfma_f32_16x16x32_bf16 v[68:71], v[156:159], v[12:15], v[68:71]\n"
        "s_waitcnt lgkmcnt(1)\n"
        "ds_read_b128 v[12:15], v0 offset:8192\n"
        "v_mfma_f32_16x16x32_bf16 v[48:51], v[160:163], v[16:19], v[48:51]\n"
        "v_mfma_f32_16x16x32_bf16 v[52:55], v[164:167], v[16:19], v[52:55]\n"
        "v_mfma_f32_16x16x32_bf16 v[56:59], v[16:19], v[168:171], v[56:59]\n"
        "v_mfma_f32_16x16x32_bf16 v[60:63], v[16:19], v[172:175], v[60:63]\n"
        "v_mfma_f32_16x16x32_bf16 v[64:67], v[176:179], v[16:19], v[64:67]\n"
        "v_mfma_f32_16x16x32_bf16 v[68:71], v[180:183], v[16:19], v[68:71]\n"
        "s_waitcnt lgkmcnt(1)\n"
        "ds_read_b128 v[16:19], v0 offset:8704\n"
        "v_mfma_f32_16x16x32_bf16 v[48:51], v[184:187], v[8:11], v[48:51]\n"
        "v_mfma_f32_16x16x32_bf16 v[52:55], v[188:191], v[8:11], v[52:55]\n"
        "v_mfma_f32_16x16x32_bf16 v[56:59], v[8:11], v[192:195], v[56:59]\n"
        "v_mfma_f32_16x16x32_bf16 v[60:63], v[8:11], v[196:199], v[60:63]\n"
        "v_mfma_f32_16x16x32_bf16 v[64:67], v[200:203], v[8:11], v[64:67]\n"
        "s_waitcnt vmcnt(2)\n"
        "s_barrier\n"
        "v_add_u32 v0, s78, v6\n"
        "v_mfma_f32_16x16x32_bf16 v[68:71], v[204:207], v[8:11], v[68:71]\n"
        "s_waitcnt lgkmcnt(1)\n"
        "ds_read_b128 v[8:11], v0 offset:0\n"
        "v_mfma_f32_16x16x32_bf16 v[48:51], v[208:211], v[12:15], v[48:51]\n"
        "v_mfma_f32_16x16x32_bf16 v[52:55], v[212:215], v[12:15], v[52:55]\n"
        "v_mfma_f32_16x16x32_bf16 v[56:59], v[12:15], v[216:219], v[56:59]\n"
        "v_mfma_f32_16x16x32_bf16 v[60:63], v[12:15], v[220:223], v[60:63]\n"
        "v_mfma_f32_16x16x32_bf16 v[64:67], v[224:227], v[12:15], v[64:67]\n"
        "v_mfma_f32_16x16x32_bf16 v[68:71], v[228:231], v[12:15], v[68:71]\n"
        "s_waitcnt lgkmcnt(1)\n"
        "ds_read_b128 v[12:15], v0 offset:512\n"
        "v_mfma_f32_16x16x32_bf16 v[48:51], v[232:235], v[16:19], v[48:51]\n"
        "v_mfma_f32_16x16x32_bf16 v[52:55], v[236:239], v[16:19], v[52:55]\n"
        "v_mfma_f32_16x16x32_bf16 v[56:59], v[16:19], v[240:243], v[56:59]\n"
        "v_mfma_f32_16x16x32_bf16 v[60:63], v[16:19], v[244:247], v[60:63]\n"
        "v_mfma_f32_16x16x32_bf16 v[64:67], v[248:251], v[16:19], v[64:67]\n"
        "v_mfma_f32_16x16x32_bf16 v[68:71], v[252:255], v[16:19], v[68:71]\n"
        "s_waitcnt lgkmcnt(1)\n"
        "ds_read_b128 v[16:19], v0 offset:1024\n"
        "s_branch .Ls1_i0_%=\n"
        ".Ls1_i10_%=:\n"
        "v_mfma_f32_16x16x32_bf16 v[56:59], v[16:19], a[128:131], v[56:59]\n"
        "buffer_load_dword v4, v2, s[40:43], s86 offen sc1\n"
        "v_mfma_f32_16x16x32_bf16 v[60:63], v[16:19], a[132:135], v[60:63]\n"
        "v_mfma_f32_16x16x32_bf16 v[64:67], a[136:139], v[16:19], v[64:67]\n"
        "v_mfma_f32_16x16x32_bf16 v[68:71], a[140:143], v[16:19], v[68:71]\n"
        "s_waitcnt lgkmcnt(1)\n"
        "ds_read_b128 v[16:19], v0 offset:4096\n"
        "v_mfma_f32_16x16x32_bf16 v[48:51], a[144:147], v[8:11], v[48:51]\n"
        "s_getreg_b32 s88, hwreg(HW_REG_IB_STS, 0, 4)\n"
        "v_mfma_f32_16x16x32_bf16 v[52:55], a[148:151], v[8:11], v[52:55]\n"
        "s_cmp_le_u32 s88, 2\n"
        "s_cselect_b32 s88, s73, s89\n"
        "s_cselect_b32 s73, s89, s73\n"
        "v_mfma_f32_16x16x32_bf16 v[56:59], v[8:11], a[152:155], v[56:59]\n"
        "buffer_store_dword v5, v3, s[40:43], s88 offen sc1\n"
        "v_mfma_f32_16x16x32_bf16 v[60:63], v[8:11], a[156:159], v[60:63]\n"
        "v_mfma_f32_16x16x32_bf16 v[64:67], a[160:163], v[8:11], v[64:67]\n"
        "v_mfma_f32_16x16x32_bf16 v[68:71], a[164:167], v[8:11], v[68:71]\n"
        "s_waitcnt lgkmcnt(1)\n"
        "ds_read_b128 v[8:11], v0 offset:4608\n"
        "v_mfma_f32_16x16x32_bf16 v[48:51], a[168:171], v[12:15], v[48:51]\n"
        "v_mfma_f32_16x16x32_bf16 v[52:55], a[172:175], v[12:15], v[52:55]\n"
        "v_mfma_f32_16x16x32_bf16 v[56:59], v[12:15], a[176:179], v[56:59]\n"
        "s_getreg_b32 s88, hwreg(HW_REG_IB_STS, 0, 4)\n"
        "v_mfma_f32_16x16x32_bf16 v[60:63], v[12:15], a[180:183], v[60:63]\n"
        "s_cmp_le_u32 s88, 3\n"
        "s_cselect_b32 s88, s73, s89\n"
        "s_cselect_b32 s73, s89, s73\n"
        "v_mfma_f32_16x16x32_bf16 v[64:67], a[184:187], v[12:15], v[64:67]\n"
        "buffer_store_dword v5, v3, s[40:43], s88 offen sc1\n"
        "v_mfma_f32_16x16x32_bf16 v[68:71], a[188:191], v[12:15], v[68:71]\n"
        "s_waitcnt lgkmcnt(1)\n"
        "ds_read_b128 v[12:15], v0 offset:5120\n"
        "v_mfma_f32_16x16x32_bf16 v[48:51], a[192:195], v[16:19], v[48:51]\n"
        "v_mfma_f32_16x16x32_bf16 v[52:55], a[196:199], v[16:19], v[52:55]\n"
        "v_mfma_f32_16x16x32_bf16 v[56:59], v[16:19], a[200:203], v[56:59]\n"
        "v_mfma_f32_16x16x32_bf16 v[60:63], v[16:19], a[204:207], v[60:63]\n"
        "v_mfma_f32_16x16x32_bf16 v[64:67], a[208:211], v[16:19], v[64:67]\n"
        "s_getreg_b32 s88, hwreg(HW_REG_IB_STS, 0, 4)\n"
        "v_mfma_f32_16x16x32_bf16 v[68:71], a[212:215], v[16:19], v[68:71]\n"
        "s_waitcnt lgkmcnt(1)\n"
        "ds_read_b128 v[16:19], v0 offset:5632\n"
        "v_mfma_f32_16x16x32_bf16 v[48:51], a[216:219], v[8:11], v[48:51]\n"
        "s_cmp_le_u32 s88, 4\n"
        "s_cselect_b32 s88, s73, s89\n"
        "s_cselect_b32 s73, s89, s73\n"
        "v_mfma_f32_16x16x32_bf16 v[52:55], a[220:223], v[8:11], v[52:55]\n"
        "buffer_store_dword v5, v3, s[40:43], s88 offen sc1\n"
        "v_mfma_f32_16x16x32_bf16 v[56:59], v[8:11], a[224:227], v[56:59]\n"
        "v_mfma_f32_16x16x32_bf16 v[60:63], v[8:11], a[228:231], v[60:63]\n"
        "v_mfma_f32_16x16x32_bf16 v[64:67], a[232:235], v[8:11], v[64:67]\n"
        "v_mfma_f32_16x16x32_bf16 v[68:71], a[236:239], v[8:11], v[68:71]\n"
        "s_waitcnt vmcnt(3)\n"
        "v_cmp_gt_i32 vcc, s70, v4\n"
        "s_waitcnt lgkmcnt(1)\n"
        "ds_read_b128 v[8:11], v0 offset:6144\n"
        "v_mfma_f32_16x16x32_bf16 v[48:51], a[240:243], v[12:15], v[48:51]\n"
        "v_mfma_f32_16x16x32_bf16 v[52:55], a[244:247], v[12:15], v[52:55]\n"
        "s_and_b64 vcc, vcc, 1\n"
        "v_mfma_f32_16x16x32_bf16 v[56:59], v[12:15], a[248:251], v[56:59]\n"
        "s_cbranch_vccnz .Ls1_i11_%=\n"
        "v_mfma_f32_16x16x32_bf16 v[60:63], v[12:15], a[252:255], v[60:63]\n"
        "buffer_load_dwordx4 v1, s[40:43], s71 offen offset:0 sc1 lds\n"
        "v_mfma_f32_16x16x32_bf16 v[64:67], v[80:83], v[12:15], v[64:67]\n"
        "v_mfma_f32_16x16x32_bf16 v[68:71], v[84:87], v[12:15], v[68:71]\n"
        "s_waitcnt lgkmcnt(1)\n"
        "ds_read_b128 v[12:15], v0 offset:6656\n"
        "v_mfma_f32_16x16x32_bf16 v[48:51], v[88:91], v[16:19], v[48:51]\n"
        "buffer_store_dword v5, v3, s[40:43], s73 offen sc1\n"
        "s_add_u32 s74, s74, 1\n"
        "buffer_load_dwordx4 v1, s[40:43], s71 offen offset:1024 sc1 lds\n"
        "v_mfma_f32_16x16x32_bf16 v[52:55], v[92:95], v[16:19], v[52:55]\n"
        "s_mov_b32 s84, s89\n"
        "s_mov_b32 s73, s60\n"
        "s_add_u32 s72, s75, s59\n"
        "v_mfma_f32_16x16x32_bf16 v[56:59], v[16:19], v[96:99], v[56:59]\n"
        "v_mfma_f32_16x16x32_bf16 v[60:63], v[16:19], v[100:103], v[60:63]\n"
        "buffer_load_dwordx4 v1, s[40:43], s71 offen offset:2048 sc1 lds\n"
        "v_mfma_f32_16x16x32_bf16 v[64:67], v[104:107], v[16:19], v[64:67]\n"
        "v_mfma_f32_16x16x32_bf16 v[68:71], v[108:111], v[16:19], v[68:71]\n"
        "s_waitcnt lgkmcnt(1)\n"
        "ds_read_b128 v[16:19], v0 offset:7168\n"
        "v_mfma_f32_16x16x32_bf16 v[48:51], v[112:115], v[8:11], v[48:51]\n"
        "buffer_load_dwordx4 v1, s[40:43], s71 offen offset:3072 sc1 lds\n"
        "s_add_u32 m0, s78, s85\n"
        "v_mfma_f32_16x16x32_bf16 v[52:55], v[116:119], v[8:11], v[52:55]\n"
        "s_add_u32 s69, s69, 1\n"
        "v_mfma_f32_16x16x32_bf16 v[56:59], v[8:11], v[120:123], v[56:59]\n"
        "v_mfma_f32_16x16x32_bf16 v[60:63], v[8:11], v[124:127], v[60:63]\n"
        "buffer_load_dwordx4 v1, s[40:43], s81 offen offset:0 sc1 lds\n"
        "v_mfma_f32_16x16x32_bf16 v[64:67], v[128:131], v[8:11], v[64:67]\n"
        "v_mfma_f32_16x16x32_bf16 v[68:71], v[132:135], v[8:11], v[68:71]\n"
        "s_waitcnt lgkmcnt(1)\n"
        "ds_read_b128 v[8:11], v0 offset:7680\n"
        "v_mfma_f32_16x16x32_bf16 v[48:51], v[136:139], v[12:15], v[48:51]\n"
        "v_mfma_f32_16x16x32_bf16 v[52:55], v[140:143], v[12:15], v[52:55]\n"
        "v_mfma_f32_16x16x32_bf16 v[56:59], v[12:15], v[144:147], v[56:59]\n"
        "v_mfma_f32_16x16x32_bf16 v[60:63], v[12:15], v[148:151], v[60:63]\n"
        "v_mfma_f32_16x16x32_bf16 v[64:67], v[152:155], v[12:15], v[64:67]\n"
        "v_mfma_f32_16x16x32_bf16 v[68:71], v[156:159], v[12:15], v[68:71]\n"
        "s_waitcnt lgkmcnt(1)\n"
        "ds_read_b128 v[12:15], v0 offset:8192\n"
        "v_mfma_f32_16x16x32_bf16 v[48:51], v[160:163], v[16:19], v[48:51]\n"
        "v_mfma_f32_16x16x32_bf16 v[52:55], v[164:167], v[16:19], v[52:55]\n"
        "v_mfma_f32_16x16x32_bf16 v[56:59], v[16:19], v[168:171], v[56:59]\n"
        "v_mfma_f32_16x16x32_bf16 v[60:63], v[16:19], v[172:175], v[60:63]\n"
        "v_mfma_f32_16x16x32_bf16 v[64:67], v[176:179], v[16:19], v[64:67]\n"
        "v_mfma_f32_16x16x32_bf16 v[68:71], v[180:183], v[16:19], v[68:71]\n"
        "s_waitcnt lgkmcnt(1)\n"
        "ds_read_b128 v[16:19], v0 offset:8704\n"
        "v_mfma_f32_16x16x32_bf16 v[48:51], v[184:187], v[8:11], v[48:51]\n"
        "v_mfma_f32_16x16x32_bf16 v[52:55], v[188:191], v[8:11], v[52:55]\n"
        "v_mfma_f32_16x16x32_bf16 v[56:59], v[8:11], v[192:195], v[56:59]\n"
        "v_mfma_f32_16x16x32_bf16 v[60:63], v[8:11], v[196:199], v[60:63]\n"
        "v_mfma_f32_16x16x32_bf16 v[64:67], v[200:203], v[8:11], v[64:67]\n"
        "s_waitcnt vmcnt(0)\n"
        "s_barrier\n"
        "v_add_u32 v0, s78, v6\n"
        "v_mfma_f32_16x16x32_bf16 v[68:71], v[204:207], v[8:11], v[68:71]\n"
        "s_waitcnt lgkmcnt(1)\n"
        "ds_read_b128 v[8:11], v0 offset:0\n"
        "v_mfma_f32_16x16x32_bf16 v[48:51], v[208:211], v[12:15], v[48:51]\n"
        "v_mfma_f32_16x16x32_bf16 v[52:55], v[212:215], v[12:15], v[52:55]\n"
        "v_mfma_f32_16x16x32_bf16 v[56:59], v[12:15], v[216:219], v[56:59]\n"
        "v_mfma_f32_16x16x32_bf16 v[60:63], v[12:15], v[220:223], v[60:63]\n"
        "v_mfma_f32_16x16x32_bf16 v[64:67], v[224:227], v[12:15], v[64:67]\n"
        "v_mfma_f32_16x16x32_bf16 v[68:71], v[228:231], v[12:15], v[68:71]\n"
        "s_waitcnt lgkmcnt(1)\n"
        "ds_read_b128 v[12:15], v0 offset:512\n"
        "v_mfma_f32_16x16x32_bf16 v[48:51], v[232:235], v[16:19], v[48:51]\n"
        "v_mfma_f32_16x16x32_bf16 v[52:55], v[236:239], v[16:19], v[52:55]\n"
        "v_mfma_f32_16x16x32_bf16 v[56:59], v[16:19], v[240:243], v[56:59]\n"
        "v_mfma_f32_16x16x32_bf16 v[60:63], v[16:19], v[244:247], v[60:63]\n"
        "v_mfma_f32_16x16x32_bf16 v[64:67], v[248:251], v[16:19], v[64:67]\n"
        "v_mfma_f32_16x16x32_bf16 v[68:71], v[252:255], v[16:19], v[68:71]\n"
        "s_waitcnt lgkmcnt(1)\n"
        "ds_read_b128 v[16:19], v0 offset:1024\n"
        "s_branch .Ls1_i0_%=\n"
        ".Ls1_i11_%=:\n"
        "v_mfma_f32_16x16x32_bf16 v[60:63], v[12:15], a[252:255], v[60:63]\n"
        "buffer_load_dword v4, v2, s[40:43], s86 offen sc1\n"
        "v_mfma_f32_16x16x32_bf16 v[64:67], v[80:83], v[12:15], v[64:67]\n"
        "v_mfma_f32_16x16x32_bf16 v[68:71], v[84:87], v[12:15], v[68:71]\n"
        "s_waitcnt lgkmcnt(1)\n"
        "ds_read_b128 v[12:15], v0 offset:6656\n"
        "v_mfma_f32_16x16x32_bf16 v[48:51], v[88:91], v[16:19], v[48:51]\n"
        "buffer_store_dword v5, v3, s[40:43], s73 offen sc1\n"
        "s_add_u32 s74, s74, 1\n"
        "v_mfma_f32_16x16x32_bf16 v[52:55], v[92:95], v[16:19], v[52:55]\n"
        "s_mov_b32 s84, s89\n"
        "s_mov_b32 s73, s60\n"
        "s_add_u32 s72, s75, s59\n"
        "v_mfma_f32_16x16x32_bf16 v[56:59], v[16:19], v[96:99], v[56:59]\n"
        "v_mfma_f32_16x16x32_bf16 v[60:63], v[16:19], v[100:103], v[60:63]\n"
        "v_mfma_f32_16x16x32_bf16 v[64:67], v[104:107], v[16:19], v[64:67]\n"
        "v_mfma_f32_16x16x32_bf16 v[68:71], v[108:111], v[16:19], v[68:71]\n"
        "s_waitcnt lgkmcnt(1)\n"
        "ds_read_b128 v[16:19], v0 offset:7168\n"
        "v_mfma_f32_16x16x32_bf16 v[48:51], v[112:115], v[8:11], v[48:51]\n"
        "v_mfma_f32_16x16x32_bf16 v[52:55], v[116:119], v[8:11], v[52:55]\n"
        "v_mfma_f32_16x16x32_bf16 v[56:59], v[8:11], v[120:123], v[56:59]\n"
        "v_mfma_f32_16x16x32_bf16 v[60:63], v[8:11], v[124:127], v[60:63]\n"
        "v_mfma_f32_16x16x32_bf16 v[64:67], v[128:131], v[8:11], v[64:67]\n"
        "v_mfma_f32_16x16x32_bf16 v[68:71], v[132:135], v[8:11], v[68:71]\n"
        "s_waitcnt lgkmcnt(1)\n"
        "ds_read_b128 v[8:11], v0 offset:7680\n"
        "v_mfma_f32_16x16x32_bf16 v[48:51], v[136:139], v[12:15], v[48:51]\n"
        "v_mfma_f32_16x16x32_bf16 v[52:55], v[140:143], v[12:15], v[52:55]\n"
        "v_mfma_f32_16x16x32_bf16 v[56:59], v[12:15], v[144:147], v[56:59]\n"
        "v_mfma_f32_16x16x32_bf16 v[60:63], v[12:15], v[148:151], v[60:63]\n"
        "v_mfma_f32_16x16x32_bf16 v[64:67], v[152:155], v[12:15], v[64:67]\n"
        "v_mfma_f32_16x16x32_bf16 v[68:71], v[156:159], v[12:15], v[68:71]\n"
        "s_waitcnt lgkmcnt(1)\n"
        "ds_read_b128 v[12:15], v0 offset:8192\n"
        "v_mfma_f32_16x16x32_bf16 v[48:51], v[160:163], v[16:19], v[48:51]\n"
        "v_mfma_f32_16x16x32_bf16 v[52:55], v[164:167], v[16:19], v[52:55]\n"
        "v_mfma_f32_16x16x32_bf16 v[56:59], v[16:19], v[168:171], v[56:59]\n"
        "v_mfma_f32_16x16x32_bf16 v[60:63], v[16:19], v[172:175], v[60:63]\n"
        "v_mfma_f32_16x16x32_bf16 v[64:67], v[176:179], v[16:19], v[64:67]\n"
        "v_mfma_f32_16x16x32_bf16 v[68:71], v[180:183], v[16:19], v[68:71]\n"
        "s_waitcnt lgkmcnt(1)\n"
        "ds_read_b128 v[16:19], v0 offset:8704\n"
        "v_mfma_f32_16x16x32_bf16 v[48:51], v[184:187], v[8:11], v[48:51]\n"
        "s_waitcnt vmcnt(1)\n"
        "v_cmp_gt_i32 vcc, s70, v4\n"
        "v_mfma_f32_16x16x32_bf16 v[52:55], v[188:191], v[8:11], v[52:55]\n"
        "v_mfma_f32_16x16x32_bf16 v[56:59], v[8:11], v[192:195], v[56:59]\n"
        "s_and_b64 vcc, vcc, 1\n"
        "v_mfma_f32_16x16x32_bf16 v[60:63], v[8:11], v[196:199], v[60:63]\n"
        "s_cbranch_vccnz .Ls1_i12_%=\n"
        "v_mfma_f32_16x16x32_bf16 v[64:67], v[200:203], v[8:11], v[64:67]\n"
        "buffer_load_dwordx4 v1, s[40:43], s71 offen offset:0 sc1 lds\n"
        "v_mfma_f32_16x16x32_bf16 v[68:71], v[204:207], v[8:11], v[68:71]\n"
        "s_waitcnt lgkmcnt(1)\n"
        "v_mfma_f32_16x16x32_bf16 v[48:51], v[208:211], v[12:15], v[48:51]\n"
        "buffer_load_dwordx4 v1, s[40:43], s71 offen offset:1024 sc1 lds\n"
        "v_mfma_f32_16x16x32_bf16 v[52:55], v[212:215], v[12:15], v[52:55]\n"
        "v_mfma_f32_16x16x32_bf16 v[56:59], v[12:15], v[216:219], v[56:59]\n"
        "buffer_load_dwordx4 v1, s[40:43], s71 offen offset:2048 sc1 lds\n"
        "v_mfma_f32_16x16x32_bf16 v[60:63], v[12:15], v[220:223], v[60:63]\n"
        "v_mfma_f32_16x16x32_bf16 v[64:67], v[224:227], v[12:15], v[64:67]\n"
        "buffer_load_dwordx4 v1, s[40:43], s71 offen offset:3072 sc1 lds\n"
        "s_add_u32 m0, s78, s85\n"
        "s_add_u32 s69, s69, 1\n"
        "v_mfma_f32_16x16x32_bf16 v[68:71], v[228:231], v[12:15], v[68:71]\n"
        "s_waitcnt lgkmcnt(0)\n"
        "v_mfma_f32_16x16x32_bf16 v[48:51], v[232:235], v[16:19], v[48:51]\n"
        "buffer_load_dwordx4 v1, s[40:43], s81 offen offset:0 sc1 lds\n"
        "v_mfma_f32_16x16x32_bf16 v[52:55], v[236:239], v[16:19], v[52:55]\n"
        "v_mfma_f32_16x16x32_bf16 v[56:59], v[16:19], v[240:243], v[56:59]\n"
        "v_mfma_f32_16x16x32_bf16 v[60:63], v[16:19], v[244:247], v[60:63]\n"
        "v_mfma_f32_16x16x32_bf16 v[64:67], v[248:251], v[16:19], v[64:67]\n"
        "v_mfma_f32_16x16x32_bf16 v[68:71], v[252:255], v[16:19], v[68:71]\n"
        "v_cvt_pk_bf16_f32 v48, v48, v49\n"
        "v_cvt_pk_bf16_f32 v49, v50, v51\n"
        "v_cvt_pk_bf16_f32 v50, v52, v53\n"
        "v_cvt_pk_bf16_f32 v51, v54, v55\n"
        "s_add_u32 s66, s72, 524288\n"
        "buffer_store_dwordx4 v[48:51], v1, s[40:43], s66 offen offset:1024 sc1\n"
        "v_cvt_pk_bf16_f32 v74, v56, v57\n"
        "v_cvt_pk_bf16_f32 v75, v58, v59\n"
        "s_add_u32 s66, s72, 1048576\n"
        "buffer_store_dwordx4 v[72:75], v1, s[40:43], s66 offen offset:0 sc1\n"
        "v_cvt_pk_bf16_f32 v78, v60, v61\n"
        "v_cvt_pk_bf16_f32 v79, v62, v63\n"
        "buffer_store_dwordx4 v[76:79], v1, s[40:43], s66 offen offset:1024 sc1\n"
        "v_cvt_pk_bf16_f32 v64, v64, v65\n"
        "v_cvt_pk_bf16_f32 v65, v66, v67\n"
        "v_cvt_pk_bf16_f32 v66, v68, v69\n"
        "v_cvt_pk_bf16_f32 v67, v70, v71\n"
        "buffer_store_dwordx4 v[64:67], v1, s[40:43], s72 offen offset:1024 sc1\n"
        "v_mov_b32 v5, s74\n"
        "s_mov_b32 s84, s60\n"
        "s_waitcnt vmcnt(4)\n"
        "s_branch .Ls1_go1_%=\n"
        ".Ls1_i12_%=:\n"
        "v_mfma_f32_16x16x32_bf16 v[64:67], v[200:203], v[8:11], v[64:67]\n"
        "buffer_load_dword v4, v2, s[40:43], s86 offen sc1\n"
        "v_mfma_f32_16x16x32_bf16 v[68:71], v[204:207], v[8:11], v[68:71]\n"
        "s_waitcnt lgkmcnt(1)\n"
        "v_mfma_f32_16x16x32_bf16 v[48:51], v[208:211], v[12:15], v[48:51]\n"
        "v_mfma_f32_16x16x32_bf16 v[52:55], v[212:215], v[12:15], v[52:55]\n"
        "v_mfma_f32_16x16x32_bf16 v[56:59], v[12:15], v[216:219], v[56:59]\n"
        "v_mfma_f32_16x16x32_bf16 v[60:63], v[12:15], v[220:223], v[60:63]\n"
        "v_mfma_f32_16x16x32_bf16 v[64:67], v[224:227], v[12:15], v[64:67]\n"
        "v_mfma_f32_16x16x32_bf16 v[68:71], v[228:231], v[12:15], v[68:71]\n"
        "s_waitcnt lgkmcnt(0)\n"
        "v_mfma_f32_16x16x32_bf16 v[48:51], v[232:235], v[16:19], v[48:51]\n"
        "v_mfma_f32_16x16x32_bf16 v[52:55], v[236:239], v[16:19], v[52:55]\n"
        "v_mfma_f32_16x16x32_bf16 v[56:59], v[16:19], v[240:243], v[56:59]\n"
        "v_mfma_f32_16x16x32_bf16 v[60:63], v[16:19], v[244:247], v[60:63]\n"
        "v_mfma_f32_16x16x32_bf16 v[64:67], v[248:251], v[16:19], v[64:67]\n"
        "v_mfma_f32_16x16x32_bf16 v[68:71], v[252:255], v[16:19], v[68:71]\n"
        "v_cvt_pk_bf16_f32 v48, v48, v49\n"
        "v_cvt_pk_bf16_f32 v49, v50, v51\n"
        "v_cvt_pk_bf16_f32 v50, v52, v53\n"
        "v_cvt_pk_bf16_f32 v51, v54, v55\n"
        "s_add_u32 s66, s72, 524288\n"
        "buffer_store_dwordx4 v[48:51], v1, s[40:43], s66 offen offset:1024 sc1\n"
        "v_cvt_pk_bf16_f32 v74, v56, v57\n"
        "v_cvt_pk_bf16_f32 v75, v58, v59\n"
        "s_add_u32 s66, s72, 1048576\n"
        "buffer_store_dwordx4 v[72:75], v1, s[40:43], s66 offen offset:0 sc1\n"
        "v_cvt_pk_bf16_f32 v78, v60, v61\n"
        "v_cvt_pk_bf16_f32 v79, v62, v63\n"
        "buffer_store_dwordx4 v[76:79], v1, s[40:43], s66 offen offset:1024 sc1\n"
        "v_cvt_pk_bf16_f32 v64, v64, v65\n"
        "v_cvt_pk_bf16_f32 v65, v66, v67\n"
        "v_cvt_pk_bf16_f32 v66, v68, v69\n"
        "v_cvt_pk_bf16_f32 v67, v70, v71\n"
        "buffer_store_dwordx4 v[64:67], v1, s[40:43], s72 offen offset:1024 sc1\n"
        "s_waitcnt vmcnt(4)\n"
        "v_cmp_gt_i32 vcc, s70, v4\n"
        "s_nop 0\n"
        "s_and_b64 vcc, vcc, 1\n"
        "s_cbranch_vccnz .Ls1_late1_%=\n"
        "buffer_load_dwordx4 v1, s[40:43], s71 offen offset:0 sc1 lds\n"
        "buffer_load_dwordx4 v1, s[40:43], s71 offen offset:1024 sc1 lds\n"
        "buffer_load_dwordx4 v1, s[40:43], s71 offen offset:2048 sc1 lds\n"
        "buffer_load_dwordx4 v1, s[40:43], s71 offen offset:3072 sc1 lds\n"
        "s_add_u32 m0, s78, s85\n"
        "s_add_u32 s69, s69, 1\n"
        "buffer_load_dwordx4 v1, s[40:43], s81 offen offset:0 sc1 lds\n"
        "s_waitcnt vmcnt(5)\n"
        "v_mov_b32 v5, s74\n"
        "buffer_store_dword v5, v3, s[40:43], s73 offen sc1\n"
        "s_waitcnt vmcnt(1)\n"
        "s_branch .Ls1_go1_%=\n"
        ".Ls1_late1_%=:\n"
        "s_waitcnt vmcnt(0)\n"
        "v_mov_b32 v5, s74\n"
        "buffer_store_dword v5, v3, s[40:43], s73 offen sc1\n"
        "s_cmp_eq_u32 s69, 128\n"
        "s_cbranch_scc0 .Ls1_wait1_%=\n"
        "s_endpgm\n"
        ".Ls1_wait1_%=:\n"
        "s_mov_b32 s64, 0\n"
        ".Ls1_in1_top_%=:\n"
        "buffer_load_dword v4, v2, s[40:43], s86 offen sc1\n"
        "s_waitcnt vmcnt(0)\n"
        "v_cmp_gt_i32 vcc, s70, v4\n"
        "s_nop 0\n"
        "s_and_b64 vcc, vcc, 1\n"
        "s_cbranch_vccz .Ls1_in1_ok_%=\n"
        "s_add_u32 s64, s64, 1\n"
        "s_and_b32 s65, s64, 255\n"
        "s_cbranch_scc1 .Ls1_in1_go_%=\n"
        "s_mov_b32 s65, 49152\n"
        "s_cmp_lt_u32 s64, 1048576\n"
        "s_cbranch_scc1 .Ls1_in1_chk_%=\n"
        "v_mov_b32 v4, 1\n"
        "buffer_store_dword v4, off, s[40:43], s65 sc1\n"
        "s_waitcnt vmcnt(0)\n"
        ".Ls1_in1_chk_%=:\n"
        "buffer_load_dword v4, off, s[40:43], s65 sc1\n"
        "s_waitcnt vmcnt(0)\n"
        "v_readfirstlane_b32 s65, v4\n"
        "s_nop 0\n"
        "s_cmp_lg_u32 s65, 1\n"
        "s_cbranch_scc1 .Ls1_in1_go_%=\n"
        "s_endpgm\n"
        ".Ls1_in1_go_%=:\n"
        "s_branch .Ls1_in1_top_%=\n"
        ".Ls1_in1_ok_%=:\n"
        "buffer_load_dwordx4 v1, s[40:43], s71 offen offset:0 sc1 lds\n"
        "buffer_load_dwordx4 v1, s[40:43], s71 offen offset:1024 sc1 lds\n"
        "buffer_load_dwordx4 v1, s[40:43], s71 offen offset:2048 sc1 lds\n"
        "buffer_load_dwordx4 v1, s[40:43], s71 offen offset:3072 sc1 lds\n"
        "s_add_u32 m0, s78, s85\n"
        "s_add_u32 s69, s69, 1\n"
        "buffer_load_dwordx4 v1, s[40:43], s81 offen offset:0 sc1 lds\n"
        "s_waitcnt vmcnt(0)\n"
        ".Ls1_go1_%=:\n"
        "s_mov_b32 s72, 241172480\n"
        "s_mov_b32 s83, 241172480\n"
        "s_mov_b32 s73, 241172480\n"
        "v_add_u32 v0, s78, v6\n"
        "s_barrier\n"
        "ds_read_b128 v[8:11], v0 offset:0\n"
        "ds_read_b128 v[12:15], v0 offset:512\n"
        "ds_read_b128 v[16:19], v0 offset:1024\n"
        "s_waitcnt lgkmcnt(2)\n"
        "s_branch .Ls1_i0_%=\n"
        :: "s"(p.ws), "s"(p.Wk), "s"(p.Wv), "s"(p.Wq), "s"(pair(w, delay)), "s"(pair(x4, pr)), TILE_ARGS(PAGE + spread(A_XN)),
           "v"(pair(lane == 0 ? flag(F_XN + 8 * h + w) : OOB, lane < 2 ? lane * 32768 : OOB)), "v"(pair(0, 0)), "v"(pair(0, 0)),
           "v"(pair(vo, 0)), "v"(pair(tr + (g & 1) * 32, tr + (1 - (g & 1)) * 32))
        : "v0", "v1", "v2", "v3", "v4", "v5", "v6", "v7", "v8", "v9", "v10", "v11", "v12", "v13", "v14", "v15", "v16", "v17", "v18", "v19", "v20", "v21", "v22", "v23", "v24", "v25", "v26", "v27", "v28", "v29", "v30", "v31", "v32", "v33", "v34", "v35", "v36", "v37", "v38", "v39", "v40", "v41", "v42", "v43", "v44", "v45", "v46", "v47", "v48", "v49", "v50", "v51", "v52", "v53", "v54", "v55", "v56", "v57", "v58", "v59", "v60", "v61", "v62", "v63", "v64", "v65", "v66", "v67", "v68", "v69", "v70", "v71", "v72", "v73", "v74", "v75", "v76", "v77", "v78", "v79", "v255", "a255", "s30", "s31", "s32", "s33", "s34", "s35", "s36", "s37", "s38", "s39", "s40", "s41", "s42", "s43", "s44", "s45", "s46", "s47", "s48", "s49", "s50", "s51", "s52", "s53", "s54", "s55", "s56", "s57", "s58", "s59", "s60", "s61", "s62", "s63", "s64", "s65", "s66", "s67", "s68", "s69", "s70", "s71", "s72", "s73", "s74", "s75", "s76", "s77", "s78", "s79", "s80", "s81", "s82", "s83", "s84", "s85", "s86", "s87", "s88", "s89", "s90", "s91", "s92", "s93", "s94", "s95", "s96", "s97", "s98", "s99", "vcc", "scc", "memory");
    __builtin_unreachable();
}
__device__ __forceinline__ void role_f1(const Params& p, int kap, int jj, int x4, int pr, int delay) {
    GEMM_HEAD;
    const unsigned u = jj * 4 + w;
    unsigned vo[3];
#pragma unroll
    for (int n = 0; n < 3; ++n) {
        const unsigned ps = (64 * n + lane) / 6;
        vo[n] = (8 * ((ps >> 2) & 3) + 4 * (ps >> 4) + (ps & 3)) * 9216 + (1152 * kap + 48 * u) * 2 + ((64 * n + lane) % 6) * 16 - 1024 * n;
    }
    const unsigned hb = 65 * PAGE + spread(B_H + kap * 73728);
    const unsigned out1 = hb + spread(u * 1024), out2 = hb + (6 + (u >> 3)) * 16384 + (u & 7) * 512, fout = flag(F_F1 + 24 * kap + u);
    asm volatile(
        "s_nop 4\n"
        "s_mov_b64 s[40:41], %0\n"
        "s_and_b32 s41, s41, 0xffff\n"
        "s_mov_b32 s42, 236978176\n"
        "s_mov_b32 s43, 0x20000\n"
        "s_mov_b64 s[48:49], %4\n"
        "s_mov_b64 s[56:57], %5\n"
        "s_mov_b64 s[58:59], %6\n"
        "s_mov_b64 s[60:61], %7\n"
        "s_mov_b64 s[62:63], %8\n"
        "v_mov_b64 v[0:1], %9\n"
        "v_mov_b64 v[2:3], %10\n"
        "v_mov_b64 v[4:5], %11\n"
        "v_mov_b64 v[6:7], %12\n"
        "v_mov_b64 v[8:9], %13\n"
        "v_mov_b64 v[10:11], %14\n"
        "s_mov_b32 s47, 0x20000\n"
        "s_mul_i32 s79, s48, 40960\n"
        ".Lf1_start_%=:\n"
        "s_sub_u32 s49, s49, 1\n"
        "s_cbranch_scc1 .Lf1_start2_%=\n"
        "s_sleep 16\n"
        "s_branch .Lf1_start_%=\n"
        ".Lf1_start2_%=:\n"
        "s_mov_b64 s[44:45], %1\n"
        "s_and_b32 s45, s45, 0xffff\n"
        "s_mov_b32 s46, 5308416\n"
        "s_add_u32 m0, s79, 0\n"
        "s_mov_b32 s65, 0\n"
        "buffer_load_dwordx4 v8, s[44:47], s65 offen lds\n"
        "buffer_load_dwordx4 v9, s[44:47], s65 offen offset:1024 lds\n"
        "buffer_load_dwordx4 v10, s[44:47], s65 offen offset:2048 lds\n"
        "s_add_u32 m0, s79, 3072\n"
        "s_mov_b32 s65, 294912\n"
        "buffer_load_dwordx4 v8, s[44:47], s65 offen lds\n"
        "buffer_load_dwordx4 v9, s[44:47], s65 offen offset:1024 lds\n"
        "buffer_load_dwordx4 v10, s[44:47], s65 offen offset:2048 lds\n"
        "s_add_u32 m0, s79, 6144\n"
        "s_mov_b32 s65, 589824\n"
        "buffer_load_dwordx4 v8, s[44:47], s65 offen lds\n"
        "buffer_load_dwordx4 v9, s[44:47], s65 offen offset:1024 lds\n"
        "buffer_load_dwordx4 v10, s[44:47], s65 offen offset:2048 lds\n"
        "s_add_u32 m0, s79, 9216\n"
        "s_mov_b32 s65, 884736\n"
        "buffer_load_dwordx4 v8, s[44:47], s65 offen lds\n"
        "buffer_load_dwordx4 v9, s[44:47], s65 offen offset:1024 lds\n"
        "buffer_load_dwordx4 v10, s[44:47], s65 offen offset:2048 lds\n"
        "s_add_u32 m0, s79, 12288\n"
        "s_mov_b32 s65, 1179648\n"
        "buffer_load_dwordx4 v8, s[44:47], s65 offen lds\n"
        "buffer_load_dwordx4 v9, s[44:47], s65 offen offset:1024 lds\n"
        "buffer_load_dwordx4 v10, s[44:47], s65 offen offset:2048 lds\n"
        "s_add_u32 m0, s79, 15360\n"
        "s_mov_b32 s65, 1474560\n"
        "buffer_load_dwordx4 v8, s[44:47], s65 offen lds\n"
        "buffer_load_dwordx4 v9, s[44:47], s65 offen offset:1024 lds\n"
        "buffer_load_dwordx4 v10, s[44:47], s65 offen offset:2048 lds\n"
        "s_add_u32 m0, s79, 18432\n"
        "s_mov_b32 s65, 1769472\n"
        "buffer_load_dwordx4 v8, s[44:47], s65 offen lds\n"
        "buffer_load_dwordx4 v9, s[44:47], s65 offen offset:1024 lds\n"
        "buffer_load_dwordx4 v10, s[44:47], s65 offen offset:2048 lds\n"
        "s_add_u32 m0, s79, 21504\n"
        "s_mov_b32 s65, 2064384\n"
        "buffer_load_dwordx4 v8, s[44:47], s65 offen lds\n"
        "buffer_load_dwordx4 v9, s[44:47], s65 offen offset:1024 lds\n"
        "buffer_load_dwordx4 v10, s[44:47], s65 offen offset:2048 lds\n"
        "s_add_u32 m0, s79, 24576\n"
        "s_mov_b32 s65, 2359296\n"
        "buffer_load_dwordx4 v8, s[44:47], s65 offen lds\n"
        "buffer_load_dwordx4 v9, s[44:47], s65 offen offset:1024 lds\n"
        "buffer_load_dwordx4 v10, s[44:47], s65 offen offset:2048 lds\n"
        "s_add_u32 m0, s79, 27648\n"
        "s_mov_b32 s65, 2654208\n"
        "buffer_load_dwordx4 v8, s[44:47], s65 offen lds\n"
        "buffer_load_dwordx4 v9, s[44:47], s65 offen offset:1024 lds\n"
        "buffer_load_dwordx4 v10, s[44:47], s65 offen offset:2048 lds\n"
        "s_add_u32 m0, s79, 30720\n"
        "s_mov_b32 s65, 2949120\n"
        "buffer_load_dwordx4 v8, s[44:47], s65 offen lds\n"
        "buffer_load_dwordx4 v9, s[44:47], s65 offen offset:1024 lds\n"
        "buffer_load_dwordx4 v10, s[44:47], s65 offen offset:2048 lds\n"
        "s_add_u32 m0, s79, 33792\n"
        "s_mov_b32 s65, 3244032\n"
        "buffer_load_dwordx4 v8, s[44:47], s65 offen lds\n"
        "buffer_load_dwordx4 v9, s[44:47], s65 offen offset:1024 lds\n"
        "buffer_load_dwordx4 v10, s[44:47], s65 offen offset:2048 lds\n"
        "s_waitcnt vmcnt(33)\n"
        "ds_read_b64_tr_b16 a[0:1], v11 offset:0\n"
        "ds_read_b64_tr_b16 a[2:3], v11 offset:1536\n"
        "ds_read_b64_tr_b16 a[8:9], v11 offset:32\n"
        "ds_read_b64_tr_b16 a[10:11], v11 offset:1568\n"
        "ds_read_b64_tr_b16 a[16:17], v11 offset:64\n"
        "ds_read_b64_tr_b16 a[18:19], v11 offset:1600\n"
        "s_waitcnt vmcnt(30)\n"
        "ds_read_b64_tr_b16 a[24:25], v11 offset:3072\n"
        "ds_read_b64_tr_b16 a[26:27], v11 offset:4608\n"
        "ds_read_b64_tr_b16 a[32:33], v11 offset:3104\n"
        "ds_read_b64_tr_b16 a[34:35], v11 offset:4640\n"
        "ds_read_b64_tr_b16 a[40:41], v11 offset:3136\n"
        "ds_read_b64_tr_b16 a[42:43], v11 offset:4672\n"
        "s_waitcnt lgkmcnt(6)\n"
        "s_add_u32 m0, s79, 0\n"
        "s_mov_b32 s65, 3538944\n"
        "buffer_load_dwordx4 v8, s[44:47], s65 offen lds\n"
        "buffer_load_dwordx4 v9, s[44:47], s65 offen offset:1024 lds\n"
        "buffer_load_dwordx4 v10, s[44:47], s65 offen offset:2048 lds\n"
        "s_waitcnt vmcnt(30)\n"
        "ds_read_b64_tr_b16 a[48:49], v11 offset:6144\n"
        "ds_read_b64_tr_b16 a[50:51], v11 offset:7680\n"
        "ds_read_b64_tr_b16 a[56:57], v11 offset:6176\n"
        "ds_read_b64_tr_b16 a[58:59], v11 offset:7712\n"
        "ds_read_b64_tr_b16 a[64:65], v11 offset:6208\n"
        "ds_read_b64_tr_b16 a[66:67], v11 offset:7744\n"
        "s_waitcnt lgkmcnt(6)\n"
        "s_add_u32 m0, s79, 3072\n"
        "s_mov_b32 s65, 3833856\n"
        "buffer_load_dwordx4 v8, s[44:47], s65 offen lds\n"
        "buffer_load_dwordx4 v9, s[44:47], s65 offen offset:1024 lds\n"
        "buffer_load_dwordx4 v10, s[44:47], s65 offen offset:2048 lds\n"
        "s_waitcnt vmcnt(30)\n"
        "ds_read_b64_tr_b16 a[72:73], v11 offset:9216\n"
        "ds_read_b64_tr_b16 a[74:75], v11 offset:10752\n"
        "ds_read_b64_tr_b16 a[80:81], v11 offset:9248\n"
        "ds_read_b64_tr_b16 a[82:83], v11 offset:10784\n"
        "ds_read_b64_tr_b16 a[88:89], v11 offset:9280\n"
        "ds_read_b64_tr_b16 a[90:91], v11 offset:10816\n"
        "s_waitcnt lgkmcnt(6)\n"
        "s_add_u32 m0, s79, 6144\n"
        "s_mov_b32 s65, 4128768\n"
        "buffer_load_dwordx4 v8, s[44:47], s65 offen lds\n"
        "buffer_load_dwordx4 v9, s[44:47], s65 offen offset:1024 lds\n"
        "buffer_load_dwordx4 v10, s[44:47], s65 offen offset:2048 lds\n"
        "s_waitcnt vmcnt(30)\n"
        "ds_read_b64_tr_b16 a[96:97], v11 offset:12288\n"
        "ds_read_b64_tr_b16 a[98:99], v11 offset:13824\n"
        "ds_read_b64_tr_b16 a[104:105], v11 offset:12320\n"
        "ds_read_b64_tr_b16 a[106:107], v11 offset:13856\n"
        "ds_read_b64_tr_b16 a[112:113], v11 offset:12352\n"
        "ds_read_b64_tr_b16 a[114:115], v11 offset:13888\n"
        "s_waitcnt lgkmcnt(6)\n"
        "s_add_u32 m0, s79, 9216\n"
        "s_mov_b32 s65, 4423680\n"
        "buffer_load_dwordx4 v8, s[44:47], s65 offen lds\n"
        "buffer_load_dwordx4 v9, s[44:47], s65 offen offset:1024 lds\n"
        "buffer_load_dwordx4 v10, s[44:47], s65 offen offset:2048 lds\n"
        "s_waitcnt vmcnt(30)\n"
        "ds_read_b64_tr_b16 a[120:121], v11 offset:15360\n"
        "ds_read_b64_tr_b16 a[122:123], v11 offset:16896\n"
        "ds_read_b64_tr_b16 a[128:129], v11 offset:15392\n"
        "ds_read_b64_tr_b16 a[130:131], v11 offset:16928\n"
        "ds_read_b64_tr_b16 a[136:137], v11 offset:15424\n"
        "ds_read_b64_tr_b16 a[138:139], v11 offset:16960\n"
        "s_waitcnt lgkmcnt(6)\n"
        "s_add_u32 m0, s79, 12288\n"
        "s_mov_b32 s65, 4718592\n"
        "buffer_load_dwordx4 v8, s[44:47], s65 offen lds\n"
        "buffer_load_dwordx4 v9, s[44:47], s65 offen offset:1024 lds\n"
        "buffer_load_dwordx4 v10, s[44:47], s65 offen offset:2048 lds\n"
        "s_waitcnt vmcnt(30)\n"
        "ds_read_b64_tr_b16 a[144:145], v11 offset:18432\n"
        "ds_read_b64_tr_b16 a[146:147], v11 offset:19968\n"
        "ds_read_b64_tr_b16 a[152:153], v11 offset:18464\n"
        "ds_read_b64_tr_b16 a[154:155], v11 offset:20000\n"
        "ds_read_b64_tr_b16 a[160:161], v11 offset:18496\n"
        "ds_read_b64_tr_b16 a[162:163], v11 offset:20032\n"
        "s_waitcnt lgkmcnt(6)\n"
        "s_add_u32 m0, s79, 15360\n"
        "s_mov_b32 s65, 5013504\n"
        "buffer_load_dwordx4 v8, s[44:47], s65 offen lds\n"
        "buffer_load_dwordx4 v9, s[44:47], s65 offen offset:1024 lds\n"
        "buffer_load_dwordx4 v10, s[44:47], s65 offen offset:2048 lds\n"
        "s_waitcnt vmcnt(30)\n"
        "ds_read_b64_tr_b16 a[168:169], v11 offset:21504\n"
        "ds_read_b64_tr_b16 a[170:171], v11 offset:23040\n"
        "ds_read_b64_tr_b16 a[176:177], v11 offset:21536\n"
        "ds_read_b64_tr_b16 a[178:179], v11 offset:23072\n"
        "ds_read_b64_tr_b16 a[184:185], v11 offset:21568\n"
        "ds_read_b64_tr_b16 a[186:187], v11 offset:23104\n"
        "s_waitcnt lgkmcnt(6)\n"
        "s_mov_b64 s[44:45], %2\n"
        "s_and_b32 s45, s45, 0xffff\n"
        "s_mov_b32 s46, 5308416\n"
        "s_add_u32 m0, s79, 18432\n"
        "s_mov_b32 s65, 0\n"
        "buffer_load_dwordx4 v8, s[44:47], s65 offen lds\n"
        "buffer_load_dwordx4 v9, s[44:47], s65 offen offset:1024 lds\n"
        "buffer_load_dwordx4 v10, s[44:47], s65 offen offset:2048 lds\n"
        "s_waitcnt vmcnt(30)\n"
        "ds_read_b64_tr_b16 a[192:193], v11 offset:24576\n"
        "ds_read_b64_tr_b16 a[194:195], v11 offset:26112\n"
        "ds_read_b64_tr_b16 a[200:201], v11 offset:24608\n"
        "ds_read_b64_tr_b16 a[202:203], v11 offset:26144\n"
        "ds_read_b64_tr_b16 a[208:209], v11 offset:24640\n"
        "ds_read_b64_tr_b16 a[210:211], v11 offset:26176\n"
        "s_waitcnt lgkmcnt(6)\n"
        "s_add_u32 m0, s79, 21504\n"
        "s_mov_b32 s65, 294912\n"
        "buffer_load_dwordx4 v8, s[44:47], s65 offen lds\n"
        "buffer_load_dwordx4 v9, s[44:47], s65 offen offset:1024 lds\n"
        "buffer_load_dwordx4 v10, s[44:47], s65 offen offset:2048 lds\n"
        "s_waitcnt vmcnt(30)\n"
        "ds_read_b64_tr_b16 a[216:217], v11 offset:27648\n"
        "ds_read_b64_tr_b16 a[218:219], v11 offset:29184\n"
        "ds_read_b64_tr_b16 a[224:225], v11 offset:27680\n"
        "ds_read_b64_tr_b16 a[226:227], v11 offset:29216\n"
        "ds_read_b64_tr_b16 a[232:233], v11 offset:27712\n"
        "ds_read_b64_tr_b16 a[234:235], v11 offset:29248\n"
        "s_waitcnt lgkmcnt(6)\n"
        "s_add_u32 m0, s79, 24576\n"
        "s_mov_b32 s65, 589824\n"
        "buffer_load_dwordx4 v8, s[44:47], s65 offen lds\n"
        "buffer_load_dwordx4 v9, s[44:47], s65 offen offset:1024 lds\n"
        "buffer_load_dwordx4 v10, s[44:47], s65 offen offset:2048 lds\n"
        "s_waitcnt vmcnt(30)\n"
        "ds_read_b64_tr_b16 a[240:241], v11 offset:30720\n"
        "ds_read_b64_tr_b16 a[242:243], v11 offset:32256\n"
        "ds_read_b64_tr_b16 a[248:249], v11 offset:30752\n"
        "ds_read_b64_tr_b16 a[250:251], v11 offset:32288\n"
        "ds_read_b64_tr_b16 v[80:81], v11 offset:30784\n"
        "ds_read_b64_tr_b16 v[82:83], v11 offset:32320\n"
        "s_waitcnt lgkmcnt(6)\n"
        "s_add_u32 m0, s79, 27648\n"
        "s_mov_b32 s65, 884736\n"
        "buffer_load_dwordx4 v8, s[44:47], s65 offen lds\n"
        "buffer_load_dwordx4 v9, s[44:47], s65 offen offset:1024 lds\n"
        "buffer_load_dwordx4 v10, s[44:47], s65 offen offset:2048 lds\n"
        "s_waitcnt vmcnt(30)\n"
        "ds_read_b64_tr_b16 v[88:89], v11 offset:33792\n"
        "ds_read_b64_tr_b16 v[90:91], v11 offset:35328\n"
        "ds_read_b64_tr_b16 v[96:97], v11 offset:33824\n"
        "ds_read_b64_tr_b16 v[98:99], v11 offset:35360\n"
        "ds_read_b64_tr_b16 v[104:105], v11 offset:33856\n"
        "ds_read_b64_tr_b16 v[106:107], v11 offset:35392\n"
        "s_waitcnt lgkmcnt(6)\n"
        "s_add_u32 m0, s79, 30720\n"
        "s_mov_b32 s65, 1179648\n"
        "buffer_load_dwordx4 v8, s[44:47], s65 offen lds\n"
        "buffer_load_dwordx4 v9, s[44:47], s65 offen offset:1024 lds\n"
        "buffer_load_dwordx4 v10, s[44:47], s65 offen offset:2048 lds\n"
        "s_waitcnt vmcnt(30)\n"
        "ds_read_b64_tr_b16 v[112:113], v11 offset:0\n"
        "ds_read_b64_tr_b16 v[114:115], v11 offset:1536\n"
        "ds_read_b64_tr_b16 v[120:121], v11 offset:32\n"
        "ds_read_b64_tr_b16 v[122:123], v11 offset:1568\n"
        "ds_read_b64_tr_b16 v[128:129], v11 offset:64\n"
        "ds_read_b64_tr_b16 v[130:131], v11 offset:1600\n"
        "s_waitcnt lgkmcnt(6)\n"
        "s_add_u32 m0, s79, 33792\n"
        "s_mov_b32 s65, 1474560\n"
        "buffer_load_dwordx4 v8, s[44:47], s65 offen lds\n"
        "buffer_load_dwordx4 v9, s[44:47], s65 offen offset:1024 lds\n"
        "buffer_load_dwordx4 v10, s[44:47], s65 offen offset:2048 lds\n"
        "s_waitcnt vmcnt(30)\n"
        "ds_read_b64_tr_b16 v[136:137], v11 offset:3072\n"
        "ds_read_b64_tr_b16 v[138:139], v11 offset:4608\n"
        "ds_read_b64_tr_b16 v[144:145], v11 offset:3104\n"
        "ds_read_b64_tr_b16 v[146:147], v11 offset:4640\n"
        "ds_read_b64_tr_b16 v[152:153], v11 offset:3136\n"
        "ds_read_b64_tr_b16 v[154:155], v11 offset:4672\n"
        "s_waitcnt lgkmcnt(6)\n"
        "s_add_u32 m0, s79, 0\n"
        "s_mov_b32 s65, 1769472\n"
        "buffer_load_dwordx4 v8, s[44:47], s65 offen lds\n"
        "buffer_load_dwordx4 v9, s[44:47], s65 offen offset:1024 lds\n"
        "buffer_load_dwordx4 v10, s[44:47], s65 offen offset:2048 lds\n"
        "s_waitcnt vmcnt(30)\n"
        "ds_read_b64_tr_b16 v[160:161], v11 offset:6144\n"
        "ds_read_b64_tr_b16 v[162:163], v11 offset:7680\n"
        "ds_read_b64_tr_b16 v[168:169], v11 offset:6176\n"
        "ds_read_b64_tr_b16 v[170:171], v11 offset:7712\n"
        "ds_read_b64_tr_b16 v[176:177], v11 offset:6208\n"
        "ds_read_b64_tr_b16 v[178:179], v11 offset:7744\n"
        "s_waitcnt lgkmcnt(6)\n"
        "s_add_u32 m0, s79, 3072\n"
        "s_mov_b32 s65, 2064384\n"
        "buffer_load_dwordx4 v8, s[44:47], s65 offen lds\n"
        "buffer_load_dwordx4 v9, s[44:47], s65 offen offset:1024 lds\n"
        "buffer_load_dwordx4 v10, s[44:47], s65 offen offset:2048 lds\n"
        "s_waitcnt vmcnt(30)\n"
        "ds_read_b64_tr_b16 v[184:185], v11 offset:9216\n"
        "ds_read_b64_tr_b16 v[186:187], v11 offset:10752\n"
        "ds_read_b64_tr_b16 v[192:193], v11 offset:9248\n"
        "ds_read_b64_tr_b16 v[194:195], v11 offset:10784\n"
        "ds_read_b64_tr_b16 v[200:201], v11 offset:9280\n"
        "ds_read_b64_tr_b16 v[202:203], v11 offset:10816\n"
        "s_waitcnt lgkmcnt(6)\n"
        "s_add_u32 m0, s79, 6144\n"
        "s_mov_b32 s65, 2359296\n"
        "buffer_load_dwordx4 v8, s[44:47], s65 offen lds\n"
        "buffer_load_dwordx4 v9, s[44:47], s65 offen offset:1024 lds\n"
        "buffer_load_dwordx4 v10, s[44:47], s65 offen offset:2048 lds\n"
        "s_waitcnt vmcnt(30)\n"
        "ds_read_b64_tr_b16 v[208:209], v11 offset:12288\n"
        "ds_read_b64_tr_b16 v[210:211], v11 offset:13824\n"
        "ds_read_b64_tr_b16 v[216:217], v11 offset:12320\n"
        "ds_read_b64_tr_b16 v[218:219], v11 offset:13856\n"
        "ds_read_b64_tr_b16 v[224:225], v11 offset:12352\n"
        "ds_read_b64_tr_b16 v[226:227], v11 offset:13888\n"
        "s_waitcnt lgkmcnt(6)\n"
        "s_add_u32 m0, s79, 9216\n"
        "s_mov_b32 s65, 2654208\n"
        "buffer_load_dwordx4 v8, s[44:47], s65 offen lds\n"
        "buffer_load_dwordx4 v9, s[44:47], s65 offen offset:1024 lds\n"
        "buffer_load_dwordx4 v10, s[44:47], s65 offen offset:2048 lds\n"
        "s_waitcnt vmcnt(30)\n"
        "ds_read_b64_tr_b16 v[232:233], v11 offset:15360\n"
        "ds_read_b64_tr_b16 v[234:235], v11 offset:16896\n"
        "ds_read_b64_tr_b16 v[240:241], v11 offset:15392\n"
        "ds_read_b64_tr_b16 v[242:243], v11 offset:16928\n"
        "ds_read_b64_tr_b16 v[248:249], v11 offset:15424\n"
        "ds_read_b64_tr_b16 v[250:251], v11 offset:16960\n"
        "s_waitcnt lgkmcnt(6)\n"
        "s_add_u32 m0, s79, 12288\n"
        "s_mov_b32 s65, 2949120\n"
        "buffer_load_dwordx4 v8, s[44:47], s65 offen lds\n"
        "buffer_load_dwordx4 v9, s[44:47], s65 offen offset:1024 lds\n"
        "buffer_load_dwordx4 v10, s[44:47], s65 offen offset:2048 lds\n"
        "s_waitcnt vmcnt(30)\n"
        "ds_read_b64_tr_b16 a[4:5], v11 offset:18432\n"
        "ds_read_b64_tr_b16 a[6:7], v11 offset:19968\n"
        "ds_read_b64_tr_b16 a[12:13], v11 offset:18464\n"
        "ds_read_b64_tr_b16 a[14:15], v11 offset:20000\n"
        "ds_read_b64_tr_b16 a[20:21], v11 offset:18496\n"
        "ds_read_b64_tr_b16 a[22:23], v11 offset:20032\n"
        "s_waitcnt lgkmcnt(6)\n"
        "s_add_u32 m0, s79, 15360\n"
        "s_mov_b32 s65, 3244032\n"
        "buffer_load_dwordx4 v8, s[44:47], s65 offen lds\n"
        "buffer_load_dwordx4 v9, s[44:47], s65 offen offset:1024 lds\n"
        "buffer_load_dwordx4 v10, s[44:47], s65 offen offset:2048 lds\n"
        "s_waitcnt vmcnt(30)\n"
        "ds_read_b64_tr_b16 a[28:29], v11 offset:21504\n"
        "ds_read_b64_tr_b16 a[30:31], v11 offset:23040\n"
        "ds_read_b64_tr_b16 a[36:37], v11 offset:21536\n"
        "ds_read_b64_tr_b16 a[38:39], v11 offset:23072\n"
        "ds_read_b64_tr_b16 a[44:45], v11 offset:21568\n"
        "ds_read_b64_tr_b16 a[46:47], v11 offset:23104\n"
        "s_waitcnt lgkmcnt(6)\n"
        "s_add_u32 m0, s79, 18432\n"
        "s_mov_b32 s65, 3538944\n"
        "buffer_load_dwordx4 v8, s[44:47], s65 offen lds\n"
        "buffer_load_dwordx4 v9, s[44:47], s65 offen offset:1024 lds\n"
        "buffer_load_dwordx4 v10, s[44:47], s65 offen offset:2048 lds\n"
        "s_waitcnt vmcnt(30)\n"
        "ds_read_b64_tr_b16 a[52:53], v11 offset:24576\n"
        "ds_read_b64_tr_b16 a[54:55], v11 offset:26112\n"
        "ds_read_b64_tr_b16 a[60:61], v11 offset:24608\n"
        "ds_read_b64_tr_b16 a[62:63], v11 offset:26144\n"
        "ds_read_b64_tr_b16 a[68:69], v11 offset:24640\n"
        "ds_read_b64_tr_b16 a[70:71], v11 offset:26176\n"
        "s_waitcnt lgkmcnt(6)\n"
        "s_add_u32 m0, s79, 21504\n"
        "s_mov_b32 s65, 3833856\n"
        "buffer_load_dwordx4 v8, s[44:47], s65 offen lds\n"
        "buffer_load_dwordx4 v9, s[44:47], s65 offen offset:1024 lds\n"
        "buffer_load_dwordx4 v10, s[44:47], s65 offen offset:2048 lds\n"
        "s_waitcnt vmcnt(30)\n"
        "ds_read_b64_tr_b16 a[76:77], v11 offset:27648\n"
        "ds_read_b64_tr_b16 a[78:79], v11 offset:29184\n"
        "ds_read_b64_tr_b16 a[84:85], v11 offset:27680\n"
        "ds_read_b64_tr_b16 a[86:87], v11 offset:29216\n"
        "ds_read_b64_tr_b16 a[92:93], v11 offset:27712\n"
        "ds_read_b64_tr_b16 a[94:95], v11 offset:29248\n"
        "s_waitcnt lgkmcnt(6)\n"
        "s_add_u32 m0, s79, 24576\n"
        "s_mov_b32 s65, 4128768\n"
        "buffer_load_dwordx4 v8, s[44:47], s65 offen lds\n"
        "buffer_load_dwordx4 v9, s[44:47], s65 offen offset:1024 lds\n"
        "buffer_load_dwordx4 v10, s[44:47], s65 offen offset:2048 lds\n"
        "s_waitcnt vmcnt(30)\n"
        "ds_read_b64_tr_b16 a[100:101], v11 offset:30720\n"
        "ds_read_b64_tr_b16 a[102:103], v11 offset:32256\n"
        "ds_read_b64_tr_b16 a[108:109], v11 offset:30752\n"
        "ds_read_b64_tr_b16 a[110:111], v11 offset:32288\n"
        "ds_read_b64_tr_b16 a[116:117], v11 offset:30784\n"
        "ds_read_b64_tr_b16 a[118:119], v11 offset:32320\n"
        "s_waitcnt lgkmcnt(6)\n"
        "s_add_u32 m0, s79, 27648\n"
        "s_mov_b32 s65, 4423680\n"
        "buffer_load_dwordx4 v8, s[44:47], s65 offen lds\n"
        "buffer_load_dwordx4 v9, s[44:47], s65 offen offset:1024 lds\n"
        "buffer_load_dwordx4 v10, s[44:47], s65 offen offset:2048 lds\n"
        "s_waitcnt vmcnt(30)\n"
        "ds_read_b64_tr_b16 a[124:125], v11 offset:33792\n"
        "ds_read_b64_tr_b16 a[126:127], v11 offset:35328\n"
        "ds_read_b64_tr_b16 a[132:133], v11 offset:33824\n"
        "ds_read_b64_tr_b16 a[134:135], v11 offset:35360\n"
        "ds_read_b64_tr_b16 a[140:141], v11 offset:33856\n"
        "ds_read_b64_tr_b16 a[142:143], v11 offset:35392\n"
        "s_waitcnt lgkmcnt(6)\n"
        "s_add_u32 m0, s79, 30720\n"
        "s_mov_b32 s65, 4718592\n"
        "buffer_load_dwordx4 v8, s[44:47], s65 offen lds\n"
        "buffer_load_dwordx4 v9, s[44:47], s65 offen offset:1024 lds\n"
        "buffer_load_dwordx4 v10, s[44:47], s65 offen offset:2048 lds\n"
        "s_waitcnt vmcnt(30)\n"
        "ds_read_b64_tr_b16 a[148:149], v11 offset:0\n"
        "ds_read_b64_tr_b16 a[150:151], v11 offset:1536\n"
        "ds_read_b64_tr_b16 a[156:157], v11 offset:32\n"
        "ds_read_b64_tr_b16 a[158:159], v11 offset:1568\n"
        "ds_read_b64_tr_b16 a[164:165], v11 offset:64\n"
        "ds_read_b64_tr_b16 a[166:167], v11 offset:1600\n"
        "s_waitcnt lgkmcnt(6)\n"
        "s_add_u32 m0, s79, 33792\n"
        "s_mov_b32 s65, 5013504\n"
        "buffer_load_dwordx4 v8, s[44:47], s65 offen lds\n"
        "buffer_load_dwordx4 v9, s[44:47], s65 offen offset:1024 lds\n"
        "buffer_load_dwordx4 v10, s[44:47], s65 offen offset:2048 lds\n"
        "s_waitcnt vmcnt(30)\n"
        "ds_read_b64_tr_b16 a[172:173], v11 offset:3072\n"
        "ds_read_b64_tr_b16 a[174:175], v11 offset:4608\n"
        "ds_read_b64_tr_b16 a[180:181], v11 offset:3104\n"
        "ds_read_b64_tr_b16 a[182:183], v11 offset:4640\n"
        "ds_read_b64_tr_b16 a[188:189], v11 offset:3136\n"
        "ds_read_b64_tr_b16 a[190:191], v11 offset:4672\n"
        "s_waitcnt vmcnt(27)\n"
        "ds_read_b64_tr_b16 a[196:197], v11 offset:6144\n"
        "ds_read_b64_tr_b16 a[198:199], v11 offset:7680\n"
        "ds_read_b64_tr_b16 a[204:205], v11 offset:6176\n"
        "ds_read_b64_tr_b16 a[206:207], v11 offset:7712\n"
        "ds_read_b64_tr_b16 a[212:213], v11 offset:6208\n"
        "ds_read_b64_tr_b16 a[214:215], v11 offset:7744\n"
        "s_waitcnt vmcnt(24)\n"
        "ds_read_b64_tr_b16 a[220:221], v11 offset:9216\n"
        "ds_read_b64_tr_b16 a[222:223], v11 offset:10752\n"
        "ds_read_b64_tr_b16 a[228:229], v11 offset:9248\n"
        "ds_read_b64_tr_b16 a[230:231], v11 offset:10784\n"
        "ds_read_b64_tr_b16 a[236:237], v11 offset:9280\n"
        "ds_read_b64_tr_b16 a[238:239], v11 offset:10816\n"
        "s_waitcnt vmcnt(21)\n"
        "ds_read_b64_tr_b16 a[244:245], v11 offset:12288\n"
        "ds_read_b64_tr_b16 a[246:247], v11 offset:13824\n"
        "ds_read_b64_tr_b16 a[252:253], v11 offset:12320\n"
        "ds_read_b64_tr_b16 a[254:255], v11 offset:13856\n"
        "ds_read_b64_tr_b16 v[84:85], v11 offset:12352\n"
        "ds_read_b64_tr_b16 v[86:87], v11 offset:13888\n"
        "s_waitcnt vmcnt(18)\n"
        "ds_read_b64_tr_b16 v[92:93], v11 offset:15360\n"
        "ds_read_b64_tr_b16 v[94:95], v11 offset:16896\n"
        "ds_read_b64_tr_b16 v[100:101], v11 offset:15392\n"
        "ds_read_b64_tr_b16 v[102:103], v11 offset:16928\n"
        "ds_read_b64_tr_b16 v[108:109], v11 offset:15424\n"
        "ds_read_b64_tr_b16 v[110:111], v11 offset:16960\n"
        "s_waitcnt vmcnt(15)\n"
        "ds_read_b64_tr_b16 v[116:117], v11 offset:18432\n"
        "ds_read_b64_tr_b16 v[118:119], v11 offset:19968\n"
        "ds_read_b64_tr_b16 v[124:125], v11 offset:18464\n"
        "ds_read_b64_tr_b16 v[126:127], v11 offset:20000\n"
        "ds_read_b64_tr_b16 v[132:133], v11 offset:18496\n"
        "ds_read_b64_tr_b16 v[134:135], v11 offset:20032\n"
        "s_waitcnt vmcnt(12)\n"
        "ds_read_b64_tr_b16 v[140:141], v11 offset:21504\n"
        "ds_read_b64_tr_b16 v[142:143], v11 offset:23040\n"
        "ds_read_b64_tr_b16 v[148:149], v11 offset:21536\n"
        "ds_read_b64_tr_b16 v[150:151], v11 offset:23072\n"
        "ds_read_b64_tr_b16 v[156:157], v11 offset:21568\n"
        "ds_read_b64_tr_b16 v[158:159], v11 offset:23104\n"
        "s_waitcnt vmcnt(9)\n"
        "ds_read_b64_tr_b16 v[164:165], v11 offset:24576\n"
        "ds_read_b64_tr_b16 v[166:167], v11 offset:26112\n"
        "ds_read_b64_tr_b16 v[172:173], v11 offset:24608\n"
        "ds_read_b64_tr_b16 v[174:175], v11 offset:26144\n"
        "ds_read_b64_tr_b16 v[180:181], v11 offset:24640\n"
        "ds_read_b64_tr_b16 v[182:183], v11 offset:26176\n"
        "s_waitcnt vmcnt(6)\n"
        "ds_read_b64_tr_b16 v[188:189], v11 offset:27648\n"
        "ds_read_b64_tr_b16 v[190:191], v11 offset:29184\n"
        "ds_read_b64_tr_b16 v[196:197], v11 offset:27680\n"
        "ds_read_b64_tr_b16 v[198:199], v11 offset:29216\n"
        "ds_read_b64_tr_b16 v[204:205], v11 offset:27712\n"
        "ds_read_b64_tr_b16 v[206:207], v11 offset:29248\n"
        "s_waitcnt vmcnt(3)\n"
        "ds_read_b64_tr_b16 v[212:213], v11 offset:30720\n"
        "ds_read_b64_tr_b16 v[214:215], v11 offset:32256\n"
        "ds_read_b64_tr_b16 v[220:221], v11 offset:30752\n"
        "ds_read_b64_tr_b16 v[222:223], v11 offset:32288\n"
        "ds_read_b64_tr_b16 v[228:229], v11 offset:30784\n"
        "ds_read_b64_tr_b16 v[230:231], v11 offset:32320\n"
        "s_waitcnt vmcnt(0)\n"
        "ds_read_b64_tr_b16 v[236:237], v11 offset:33792\n"
        "ds_read_b64_tr_b16 v[238:239], v11 offset:35328\n"
        "ds_read_b64_tr_b16 v[244:245], v11 offset:33824\n"
        "ds_read_b64_tr_b16 v[246:247], v11 offset:35360\n"
        "ds_read_b64_tr_b16 v[252:253], v11 offset:33856\n"
        "ds_read_b64_tr_b16 v[254:255], v11 offset:35392\n"
        "s_waitcnt lgkmcnt(0)\n"
        "v_lshrrev_b32 v20, 4, v1\n"
        "s_mov_b32 s50, s56\n"
        "v_lshlrev_b32 v22, 7, v20\n"
        "s_sub_u32 s66, 0, s50\n"
        "s_mov_b32 s52, 32768\n"
        "s_mov_b32 s64, 0\n"
        ".Lf1_pg0_top_%=:\n"
        "buffer_load_dword v21, v22, s[40:43], s52 offen sc1\n"
        "s_waitcnt vmcnt(0)\n"
        "v_cmp_gt_i32 vcc, 0, v21\n"
        "s_cbranch_vccz .Lf1_pg0_ok_%=\n"
        "s_add_u32 s64, s64, 1\n"
        "s_and_b32 s65, s64, 255\n"
        "s_cbranch_scc1 .Lf1_pg0_go_%=\n"
        "s_mov_b32 s65, 49152\n"
        "s_cmp_lt_u32 s64, 1048576\n"
        "s_cbranch_scc1 .Lf1_pg0_chk_%=\n"
        "v_mov_b32 v21, 1\n"
        "buffer_store_dword v21, off, s[40:43], s65 sc1\n"
        "s_waitcnt vmcnt(0)\n"
        ".Lf1_pg0_chk_%=:\n"
        "buffer_load_dword v21, off, s[40:43], s65 sc1\n"
        "s_waitcnt vmcnt(0)\n"
        "v_readfirstlane_b32 s65, v21\n"
        "s_nop 0\n"
        "s_cmp_lg_u32 s65, 1\n"
        "s_cbranch_scc1 .Lf1_pg0_go_%=\n"
        "s_endpgm\n"
        ".Lf1_pg0_go_%=:\n"
        "s_sleep 2\n"
        "s_branch .Lf1_pg0_top_%=\n"
        ".Lf1_pg0_ok_%=:\n"
        "v_cmp_eq_u32 s[52:53], 1, v21\n"
        "s_nop 1\n"
        "s_mov_b32 s54, 40960\n"
        "s_mov_b32 s64, 0\n"
        ".Lf1_pg1_top_%=:\n"
        "buffer_load_dword v21, v22, s[40:43], s54 offen sc1\n"
        "s_waitcnt vmcnt(0)\n"
        "v_cmp_gt_i32 vcc, 0, v21\n"
        "s_cbranch_vccz .Lf1_pg1_ok_%=\n"
        "s_add_u32 s64, s64, 1\n"
        "s_and_b32 s65, s64, 255\n"
        "s_cbranch_scc1 .Lf1_pg1_go_%=\n"
        "s_mov_b32 s65, 49152\n"
        "s_cmp_lt_u32 s64, 1048576\n"
        "s_cbranch_scc1 .Lf1_pg1_chk_%=\n"
        "v_mov_b32 v21, 1\n"
        "buffer_store_dword v21, off, s[40:43], s65 sc1\n"
        "s_waitcnt vmcnt(0)\n"
        ".Lf1_pg1_chk_%=:\n"
        "buffer_load_dword v21, off, s[40:43], s65 sc1\n"
        "s_waitcnt vmcnt(0)\n"
        "v_readfirstlane_b32 s65, v21\n"
        "s_nop 0\n"
        "s_cmp_lg_u32 s65, 1\n"
        "s_cbranch_scc1 .Lf1_pg1_go_%=\n"
        "s_endpgm\n"
        ".Lf1_pg1_go_%=:\n"
        "s_sleep 2\n"
        "s_branch .Lf1_pg1_top_%=\n"
        ".Lf1_pg1_ok_%=:\n"
        "v_cmp_eq_u32 s[54:55], 1, v21\n"
        "s_nop 1\n"
        "s_mov_b32 vcc_lo, 0x55555555\n"
        "s_mov_b32 vcc_hi, 0x55555555\n"
        "s_xor_b64 vcc, vcc, s[52:53]\n"
        "s_bcnt1_i32_b64 s65, vcc\n"
        "s_sub_u32 s64, s65, 23\n"
        "s_cmp_lt_u32 s64, 19\n"
        "s_cbranch_scc1 .Lf1_rawmap_%=\n"
        "s_cmp_lt_u32 s65, 32\n"
        "s_cselect_b32 s65, 0, -1\n"
        "s_xor_b32 s52, s65, 0x55555555\n"
        "s_xor_b32 s53, s65, 0x55555555\n"
        "s_xor_b32 s54, s65, 0x55555555\n"
        "s_xor_b32 s55, s65, 0xaaaaaaaa\n"
        ".Lf1_rawmap_%=:\n"
        "s_xor_b32 s52, s52, s66\n"
        "s_xor_b32 s53, s53, s66\n"
        "s_xor_b32 s54, s54, s66\n"
        "s_xor_b32 s55, s55, s66\n"
        "s_and_b32 s76, s55, 1\n"
        "s_lshl_b32 s76, s76, 13\n"
        "s_add_u32 s60, s60, s76\n"
        "s_barrier\n"
        "s_mov_b32 s68, -1\n"
        "s_mov_b32 s69, 0\n"
        "s_mov_b32 s78, 0\n"
        "s_mov_b32 s74, -1\n"
        "s_mov_b32 s86, s76\n"
        "s_lshr_b32 s85, s61, 16\n"
        "s_and_b32 s61, s61, 0xffff\n"
        "s_mov_b32 s89, 241172480\n"
        "s_mov_b32 s84, 241172480\n"
        "v_mov_b32 v6, v0\n"
        "s_mov_b32 s80, 0xbfb8aa3b\n"
        "s_lshr_b32 s67, s69, 1\n"
        "s_lshr_b32 s65, s67, 1\n"
        "s_bitcmp1_b32 s54, s65\n"
        "s_cselect_b32 s65, 8192, 0\n"
        "s_lshl_b32 s77, s67, 20\n"
        "s_add_u32 s77, s77, s65\n"
        "s_add_u32 s71, s77, s58\n"
        "s_add_u32 s81, s77, s62\n"
        "s_add_u32 s78, s78, 18432\n"
        "s_cmp_eq_u32 s78, 55296\n"
        "s_cselect_b32 s78, 0, s78\n"
        "s_add_u32 m0, s78, s61\n"
        "s_lshr_b32 s70, s69, 1\n"
        "s_branch .Lf1_wait1_%=\n"
        ".Lf1_i0_%=:\n"
        "v_mfma_f32_16x16x32_bf16 v[24:27], a[0:3], v[8:11], 0\n"
        "s_add_u32 s86, s76, 512\n"
        "buffer_load_dword v4, v2, s[40:43], s86 offen sc1\n"
        "v_mfma_f32_16x16x32_bf16 v[28:31], a[4:7], v[8:11], 0\n"
        "s_add_u32 s71, s77, s58\n"
        "v_mul_f32 v72, s80, v48\n"
        "v_mul_f32 v73, s80, v49\n"
        "v_mfma_f32_16x16x32_bf16 v[32:35], a[8:11], v[8:11], 0\n"
        "s_add_u32 s71, s71, 32768\n"
        "v_mul_f32 v74, s80, v50\n"
        "v_mul_f32 v75, s80, v51\n"
        "v_mfma_f32_16x16x32_bf16 v[36:39], a[12:15], v[8:11], 0\n"
        "s_add_u32 s81, s77, s62\n"
        "v_exp_f32 v72, v72\n"
        "v_mfma_f32_16x16x32_bf16 v[40:43], a[16:19], v[8:11], 0\n"
        "s_add_u32 s81, s81, 2048\n"
        "v_exp_f32 v73, v73\n"
        "v_mfma_f32_16x16x32_bf16 v[44:47], a[20:23], v[8:11], 0\n"
        "s_waitcnt lgkmcnt(1)\n"
        "ds_read_b128 v[8:11], v0 offset:1536\n"
        "v_mul_f32 v76, s80, v56\n"
        "v_mfma_f32_16x16x32_bf16 v[24:27], a[24:27], v[12:15], v[24:27]\n"
        "s_add_u32 s78, s78, 18432\n"
        "v_exp_f32 v74, v74\n"
        "v_mfma_f32_16x16x32_bf16 v[28:31], a[28:31], v[12:15], v[28:31]\n"
        "s_cmp_eq_u32 s78, 55296\n"
        "s_cselect_b32 s78, 0, s78\n"
        "v_mul_f32 v77, s80, v57\n"
        "v_mfma_f32_16x16x32_bf16 v[32:35], a[32:35], v[12:15], v[32:35]\n"
        "s_add_u32 m0, s78, s61\n"
        "v_exp_f32 v75, v75\n"
        "v_mfma_f32_16x16x32_bf16 v[36:39], a[36:39], v[12:15], v[36:39]\n"
        "s_lshr_b32 s70, s69, 1\n"
        "v_mul_f32 v48, v48, v52\n"
        "v_mul_f32 v49, v49, v53\n"
        "v_mfma_f32_16x16x32_bf16 v[40:43], a[40:43], v[12:15], v[40:43]\n"
        "v_mul_f32 v50, v50, v54\n"
        "v_mul_f32 v51, v51, v55\n"
        "v_mfma_f32_16x16x32_bf16 v[44:47], a[44:47], v[12:15], v[44:47]\n"
        "s_waitcnt lgkmcnt(1)\n"
        "ds_read_b128 v[12:15], v0 offset:2048\n"
        "v_add_f32 v72, 1.0, v72\n"
        "v_mfma_f32_16x16x32_bf16 v[24:27], a[48:51], v[16:19], v[24:27]\n"
        "v_add_f32 v73, 1.0, v73\n"
        "v_add_f32 v74, 1.0, v74\n"
        "v_mfma_f32_16x16x32_bf16 v[28:31], a[52:55], v[16:19], v[28:31]\n"
        "v_add_f32 v75, 1.0, v75\n"
        "v_mul_f32 v78, s80, v58\n"
        "v_mfma_f32_16x16x32_bf16 v[32:35], a[56:59], v[16:19], v[32:35]\n"
        "s_waitcnt vmcnt(3)\n"
        "v_rcp_f32 v72, v72\n"
        "v_mfma_f32_16x16x32_bf16 v[36:39], a[60:63], v[16:19], v[36:39]\n"
        "buffer_store_dword v5, v3, s[40:43], s84 offen sc1\n"
        "v_mul_f32 v79, s80, v59\n"
        "v_mfma_f32_16x16x32_bf16 v[40:43], a[64:67], v[16:19], v[40:43]\n"
        "v_rcp_f32 v73, v73\n"
        "v_mfma_f32_16x16x32_bf16 v[44:47], a[68:71], v[16:19], v[44:47]\n"
        "s_waitcnt lgkmcnt(1)\n"
        "ds_read_b128 v[16:19], v0 offset:2560\n"
        "v_mfma_f32_16x16x32_bf16 v[24:27], a[72:75], v[8:11], v[24:27]\n"
        "v_rcp_f32 v74, v74\n"
        "v_mfma_f32_16x16x32_bf16 v[28:31], a[76:79], v[8:11], v[28:31]\n"
        "v_rcp_f32 v75, v75\n"
        "v_mfma_f32_16x16x32_bf16 v[32:35], a[80:83], v[8:11], v[32:35]\n"
        "v_mul_f32 v48, v48, v72\n"
        "v_mul_f32 v49, v49, v73\n"
        "v_mfma_f32_16x16x32_bf16 v[36:39], a[84:87], v[8:11], v[36:39]\n"
        "v_mul_f32 v50, v50, v74\n"
        "v_mul_f32 v51, v51, v75\n"
        "v_mfma_f32_16x16x32_bf16 v[40:43], a[88:91], v[8:11], v[40:43]\n"
        "v_cvt_pk_bf16_f32 v52, v48, v49\n"
        "v_cvt_pk_bf16_f32 v53, v50, v51\n"
        "v_mfma_f32_16x16x32_bf16 v[44:47], a[92:95], v[8:11], v[44:47]\n"
        "s_waitcnt lgkmcnt(1)\n"
        "ds_read_b128 v[8:11], v0 offset:3072\n"
        "v_mul_f32 v72, s80, v64\n"
        "v_mfma_f32_16x16x32_bf16 v[24:27], a[96:99], v[12:15], v[24:27]\n"
        "v_exp_f32 v76, v76\n"
        "v_mfma_f32_16x16x32_bf16 v[28:31], a[100:103], v[12:15], v[28:31]\n"
        "v_exp_f32 v77, v77\n"
        "v_mfma_f32_16x16x32_bf16 v[32:35], a[104:107], v[12:15], v[32:35]\n"
        "v_exp_f32 v78, v78\n"
        "v_mfma_f32_16x16x32_bf16 v[36:39], a[108:111], v[12:15], v[36:39]\n"
        "v_exp_f32 v79, v79\n"
        "v_mfma_f32_16x16x32_bf16 v[40:43], a[112:115], v[12:15], v[40:43]\n"
        "s_waitcnt vmcnt(1)\n"
        "v_cmp_gt_i32 vcc, s70, v4\n"
        "v_mul_f32 v56, v56, v60\n"
        "v_mfma_f32_16x16x32_bf16 v[44:47], a[116:119], v[12:15], v[44:47]\n"
        "s_waitcnt lgkmcnt(1)\n"
        "ds_read_b128 v[12:15], v0 offset:3584\n"
        "v_mul_f32 v57, v57, v61\n"
        "v_mfma_f32_16x16x32_bf16 v[24:27], a[120:123], v[16:19], v[24:27]\n"
        "v_mul_f32 v58, v58, v62\n"
        "s_and_b64 vcc, vcc, 1\n"
        "v_mfma_f32_16x16x32_bf16 v[28:31], a[124:127], v[16:19], v[28:31]\n"
        "s_cbranch_vccnz .Lf1_i00_%=\n"
        "v_mfma_f32_16x16x32_bf16 v[32:35], a[128:131], v[16:19], v[32:35]\n"
        "v_mul_f32 v59, v59, v63\n"
        "buffer_load_dwordx4 v1, s[40:43], s71 offen offset:0 sc1 lds\n"
        "v_mfma_f32_16x16x32_bf16 v[36:39], a[132:135], v[16:19], v[36:39]\n"
        "v_add_f32 v76, 1.0, v76\n"
        "v_add_f32 v77, 1.0, v77\n"
        "v_mfma_f32_16x16x32_bf16 v[40:43], a[136:139], v[16:19], v[40:43]\n"
        "v_add_f32 v78, 1.0, v78\n"
        "v_add_f32 v79, 1.0, v79\n"
        "v_mfma_f32_16x16x32_bf16 v[44:47], a[140:143], v[16:19], v[44:47]\n"
        "s_waitcnt lgkmcnt(1)\n"
        "ds_read_b128 v[16:19], v0 offset:4096\n"
        "v_mul_f32 v73, s80, v65\n"
        "v_mfma_f32_16x16x32_bf16 v[24:27], a[144:147], v[8:11], v[24:27]\n"
        "v_rcp_f32 v76, v76\n"
        "buffer_load_dwordx4 v1, s[40:43], s71 offen offset:1024 sc1 lds\n"
        "v_mfma_f32_16x16x32_bf16 v[28:31], a[148:151], v[8:11], v[28:31]\n"
        "v_rcp_f32 v77, v77\n"
        "v_mfma_f32_16x16x32_bf16 v[32:35], a[152:155], v[8:11], v[32:35]\n"
        "v_rcp_f32 v78, v78\n"
        "v_mfma_f32_16x16x32_bf16 v[36:39], a[156:159], v[8:11], v[36:39]\n"
        "v_rcp_f32 v79, v79\n"
        "v_mfma_f32_16x16x32_bf16 v[40:43], a[160:163], v[8:11], v[40:43]\n"
        "v_mul_f32 v56, v56, v76\n"
        "v_mul_f32 v57, v57, v77\n"
        "buffer_load_dwordx4 v1, s[40:43], s71 offen offset:2048 sc1 lds\n"
        "v_mfma_f32_16x16x32_bf16 v[44:47], a[164:167], v[8:11], v[44:47]\n"
        "s_waitcnt lgkmcnt(1)\n"
        "ds_read_b128 v[8:11], v0 offset:4608\n"
        "v_mul_f32 v58, v58, v78\n"
        "v_mfma_f32_16x16x32_bf16 v[24:27], a[168:171], v[12:15], v[24:27]\n"
        "v_mul_f32 v59, v59, v79\n"
        "v_cvt_pk_bf16_f32 v54, v56, v57\n"
        "v_mfma_f32_16x16x32_bf16 v[28:31], a[172:175], v[12:15], v[28:31]\n"
        "v_cvt_pk_bf16_f32 v55, v58, v59\n"
        "v_mul_f32 v74, s80, v66\n"
        "v_mfma_f32_16x16x32_bf16 v[32:35], a[176:179], v[12:15], v[32:35]\n"
        "buffer_store_dwordx4 v[52:55], v1, s[40:43], s72 offen offset:0 sc1\n"
        "v_mfma_f32_16x16x32_bf16 v[36:39], a[180:183], v[12:15], v[36:39]\n"
        "v_mul_f32 v75, s80, v67\n"
        "buffer_load_dwordx4 v1, s[40:43], s71 offen offset:3072 sc1 lds\n"
        "v_mfma_f32_16x16x32_bf16 v[40:43], a[184:187], v[12:15], v[40:43]\n"
        "s_add_u32 m0, s78, s85\n"
        "v_exp_f32 v72, v72\n"
        "v_mfma_f32_16x16x32_bf16 v[44:47], a[188:191], v[12:15], v[44:47]\n"
        "s_waitcnt lgkmcnt(1)\n"
        "ds_read_b128 v[12:15], v0 offset:5120\n"
        "s_add_u32 s69, s69, 1\n"
        "v_mfma_f32_16x16x32_bf16 v[24:27], a[192:195], v[16:19], v[24:27]\n"
        "v_exp_f32 v73, v73\n"
        "buffer_load_dwordx4 v1, s[40:43], s81 offen offset:0 sc1 lds\n"
        "v_mfma_f32_16x16x32_bf16 v[28:31], a[196:199], v[16:19], v[28:31]\n"
        "v_exp_f32 v74, v74\n"
        "v_mfma_f32_16x16x32_bf16 v[32:35], a[200:203], v[16:19], v[32:35]\n"
        "v_exp_f32 v75, v75\n"
        "v_mfma_f32_16x16x32_bf16 v[36:39], a[204:207], v[16:19], v[36:39]\n"
        "v_mul_f32 v64, v64, v68\n"
        "v_mul_f32 v65, v65, v69\n"
        "v_mfma_f32_16x16x32_bf16 v[40:43], a[208:211], v[16:19], v[40:43]\n"
        "v_mul_f32 v66, v66, v70\n"
        "v_mul_f32 v67, v67, v71\n"
        "v_mfma_f32_16x16x32_bf16 v[44:47], a[212:215], v[16:19], v[44:47]\n"
        "s_waitcnt lgkmcnt(1)\n"
        "ds_read_b128 v[16:19], v0 offset:5632\n"
        "v_add_f32 v72, 1.0, v72\n"
        "v_mfma_f32_16x16x32_bf16 v[24:27], a[216:219], v[8:11], v[24:27]\n"
        "v_add_f32 v73, 1.0, v73\n"
        "v_add_f32 v74, 1.0, v74\n"
        "v_mfma_f32_16x16x32_bf16 v[28:31], a[220:223], v[8:11], v[28:31]\n"
        "v_add_f32 v75, 1.0, v75\n"
        "v_mfma_f32_16x16x32_bf16 v[32:35], a[224:227], v[8:11], v[32:35]\n"
        "v_rcp_f32 v72, v72\n"
        "v_mfma_f32_16x16x32_bf16 v[36:39], a[228:231], v[8:11], v[36:39]\n"
        "v_rcp_f32 v73, v73\n"
        "v_mfma_f32_16x16x32_bf16 v[40:43], a[232:235], v[8:11], v[40:43]\n"
        "v_rcp_f32 v74, v74\n"
        "v_mfma_f32_16x16x32_bf16 v[44:47], a[236:239], v[8:11], v[44:47]\n"
        "s_waitcnt lgkmcnt(1)\n"
        "ds_read_b128 v[8:11], v0 offset:6144\n"
        "v_mfma_f32_16x16x32_bf16 v[24:27], a[240:243], v[12:15], v[24:27]\n"
        "v_rcp_f32 v75, v75\n"
        "v_mfma_f32_16x16x32_bf16 v[28:31], a[244:247], v[12:15], v[28:31]\n"
        "v_mul_f32 v64, v64, v72\n"
        "v_mfma_f32_16x16x32_bf16 v[32:35], a[248:251], v[12:15], v[32:35]\n"
        "v_mfma_f32_16x16x32_bf16 v[36:39], a[252:255], v[12:15], v[36:39]\n"
        "v_mul_f32 v65, v65, v73\n"
        "v_mfma_f32_16x16x32_bf16 v[40:43], v[80:83], v[12:15], v[40:43]\n"
        "v_mul_f32 v66, v66, v74\n"
        "v_mul_f32 v67, v67, v75\n"
        "v_mfma_f32_16x16x32_bf16 v[44:47], v[84:87], v[12:15], v[44:47]\n"
        "s_waitcnt lgkmcnt(1)\n"
        "ds_read_b128 v[12:15], v0 offset:6656\n"
        "v_cvt_pk_bf16_f32 v68, v64, v65\n"
        "v_mfma_f32_16x16x32_bf16 v[24:27], v[88:91], v[16:19], v[24:27]\n"
        "v_cvt_pk_bf16_f32 v69, v66, v67\n"
        "s_nop 0\n"
        "v_mfma_f32_16x16x32_bf16 v[28:31], v[92:95], v[16:19], v[28:31]\n"
        "v_permlane16_swap_b32 v68, v70\n"
        "v_mfma_f32_16x16x32_bf16 v[32:35], v[96:99], v[16:19], v[32:35]\n"
        "v_permlane16_swap_b32 v69, v71\n"
        "v_mfma_f32_16x16x32_bf16 v[36:39], v[100:103], v[16:19], v[36:39]\n"
        "buffer_store_dwordx4 v[68:71], v7, s[40:43], s83 offen offset:0 sc1\n"
        "v_mfma_f32_16x16x32_bf16 v[40:43], v[104:107], v[16:19], v[40:43]\n"
        "v_mov_b32 v5, s74\n"
        "v_mfma_f32_16x16x32_bf16 v[44:47], v[108:111], v[16:19], v[44:47]\n"
        "s_waitcnt lgkmcnt(1)\n"
        "ds_read_b128 v[16:19], v0 offset:7168\n"
        "v_mfma_f32_16x16x32_bf16 v[24:27], v[112:115], v[8:11], v[24:27]\n"
        "v_mfma_f32_16x16x32_bf16 v[28:31], v[116:119], v[8:11], v[28:31]\n"
        "v_mfma_f32_16x16x32_bf16 v[32:35], v[120:123], v[8:11], v[32:35]\n"
        "v_mfma_f32_16x16x32_bf16 v[36:39], v[124:127], v[8:11], v[36:39]\n"
        "v_mfma_f32_16x16x32_bf16 v[40:43], v[128:131], v[8:11], v[40:43]\n"
        "v_mfma_f32_16x16x32_bf16 v[44:47], v[132:135], v[8:11], v[44:47]\n"
        "s_waitcnt lgkmcnt(1)\n"
        "ds_read_b128 v[8:11], v0 offset:7680\n"
        "v_mfma_f32_16x16x32_bf16 v[24:27], v[136:139], v[12:15], v[24:27]\n"
        "v_mfma_f32_16x16x32_bf16 v[28:31], v[140:143], v[12:15], v[28:31]\n"
        "v_mfma_f32_16x16x32_bf16 v[32:35], v[144:147], v[12:15], v[32:35]\n"
        "v_mfma_f32_16x16x32_bf16 v[36:39], v[148:151], v[12:15], v[36:39]\n"
        "v_mfma_f32_16x16x32_bf16 v[40:43], v[152:155], v[12:15], v[40:43]\n"
        "v_mfma_f32_16x16x32_bf16 v[44:47], v[156:159], v[12:15], v[44:47]\n"
        "s_waitcnt lgkmcnt(1)\n"
        "ds_read_b128 v[12:15], v0 offset:8192\n"
        "v_mfma_f32_16x16x32_bf16 v[24:27], v[160:163], v[16:19], v[24:27]\n"
        "v_mfma_f32_16x16x32_bf16 v[28:31], v[164:167], v[16:19], v[28:31]\n"
        "v_mfma_f32_16x16x32_bf16 v[32:35], v[168:171], v[16:19], v[32:35]\n"
        "v_mfma_f32_16x16x32_bf16 v[36:39], v[172:175], v[16:19], v[36:39]\n"
        "v_mfma_f32_16x16x32_bf16 v[40:43], v[176:179], v[16:19], v[40:43]\n"
        "v_mfma_f32_16x16x32_bf16 v[44:47], v[180:183], v[16:19], v[44:47]\n"
        "s_waitcnt lgkmcnt(1)\n"
        "ds_read_b128 v[16:19], v0 offset:8704\n"
        "v_mfma_f32_16x16x32_bf16 v[24:27], v[184:187], v[8:11], v[24:27]\n"
        "v_mfma_f32_16x16x32_bf16 v[28:31], v[188:191], v[8:11], v[28:31]\n"
        "s_getreg_b32 s88, hwreg(HW_REG_IB_STS, 0, 4)\n"
        "v_mfma_f32_16x16x32_bf16 v[32:35], v[192:195], v[8:11], v[32:35]\n"
        "v_mfma_f32_16x16x32_bf16 v[36:39], v[196:199], v[8:11], v[36:39]\n"
        "v_mfma_f32_16x16x32_bf16 v[40:43], v[200:203], v[8:11], v[40:43]\n"
        "s_waitcnt vmcnt(1)\n"
        "s_barrier\n"
        "v_add_u32 v0, s78, v6\n"
        "v_mfma_f32_16x16x32_bf16 v[44:47], v[204:207], v[8:11], v[44:47]\n"
        "s_waitcnt lgkmcnt(1)\n"
        "ds_read_b128 v[8:11], v0 offset:0\n"
        "v_mfma_f32_16x16x32_bf16 v[24:27], v[208:211], v[12:15], v[24:27]\n"
        "s_cmp_le_u32 s88, 0\n"
        "s_cselect_b32 s88, s73, s89\n"
        "s_cselect_b32 s73, s89, s73\n"
        "v_mfma_f32_16x16x32_bf16 v[28:31], v[212:215], v[12:15], v[28:31]\n"
        "buffer_store_dword v5, v3, s[40:43], s88 offen sc1\n"
        "v_mfma_f32_16x16x32_bf16 v[32:35], v[216:219], v[12:15], v[32:35]\n"
        "s_getreg_b32 s88, hwreg(HW_REG_IB_STS, 0, 4)\n"
        "v_mfma_f32_16x16x32_bf16 v[36:39], v[220:223], v[12:15], v[36:39]\n"
        "s_cmp_le_u32 s88, 1\n"
        "s_cselect_b32 s88, s73, s89\n"
        "s_cselect_b32 s73, s89, s73\n"
        "v_mfma_f32_16x16x32_bf16 v[40:43], v[224:227], v[12:15], v[40:43]\n"
        "buffer_store_dword v5, v3, s[40:43], s88 offen sc1\n"
        "s_add_u32 s74, s74, 1\n"
        "v_mfma_f32_16x16x32_bf16 v[44:47], v[228:231], v[12:15], v[44:47]\n"
        "s_waitcnt lgkmcnt(1)\n"
        "ds_read_b128 v[12:15], v0 offset:512\n"
        "s_mov_b32 s84, s73\n"
        "v_mfma_f32_16x16x32_bf16 v[24:27], v[232:235], v[16:19], v[24:27]\n"
        "s_mov_b32 s73, s60\n"
        "s_add_u32 s68, s68, 1\n"
        "s_lshr_b32 s66, s68, 1\n"
        "v_mfma_f32_16x16x32_bf16 v[28:31], v[236:239], v[16:19], v[28:31]\n"
        "s_bitcmp1_b32 s54, s66\n"
        "s_cselect_b32 s66, 8192, 0\n"
        "s_lshl_b32 s75, s68, 20\n"
        "v_mfma_f32_16x16x32_bf16 v[32:35], v[240:243], v[16:19], v[32:35]\n"
        "s_add_u32 s75, s75, s66\n"
        "s_add_u32 s72, s75, s59\n"
        "s_add_u32 s83, s75, s63\n"
        "v_mfma_f32_16x16x32_bf16 v[36:39], v[244:247], v[16:19], v[36:39]\n"
        "v_mfma_f32_16x16x32_bf16 v[40:43], v[248:251], v[16:19], v[40:43]\n"
        "v_mfma_f32_16x16x32_bf16 v[44:47], v[252:255], v[16:19], v[44:47]\n"
        "s_waitcnt lgkmcnt(1)\n"
        "ds_read_b128 v[16:19], v0 offset:1024\n"
        "s_branch .Lf1_i1_%=\n"
        ".Lf1_i00_%=:\n"
        "v_mfma_f32_16x16x32_bf16 v[32:35], a[128:131], v[16:19], v[32:35]\n"
        "v_mul_f32 v59, v59, v63\n"
        "buffer_load_dword v4, v2, s[40:43], s86 offen sc1\n"
        "v_mfma_f32_16x16x32_bf16 v[36:39], a[132:135], v[16:19], v[36:39]\n"
        "v_add_f32 v76, 1.0, v76\n"
        "v_add_f32 v77, 1.0, v77\n"
        "v_mfma_f32_16x16x32_bf16 v[40:43], a[136:139], v[16:19], v[40:43]\n"
        "v_add_f32 v78, 1.0, v78\n"
        "v_add_f32 v79, 1.0, v79\n"
        "v_mfma_f32_16x16x32_bf16 v[44:47], a[140:143], v[16:19], v[44:47]\n"
        "s_waitcnt lgkmcnt(1)\n"
        "ds_read_b128 v[16:19], v0 offset:4096\n"
        "v_mul_f32 v73, s80, v65\n"
        "v_mfma_f32_16x16x32_bf16 v[24:27], a[144:147], v[8:11], v[24:27]\n"
        "v_rcp_f32 v76, v76\n"
        "v_mfma_f32_16x16x32_bf16 v[28:31], a[148:151], v[8:11], v[28:31]\n"
        "v_rcp_f32 v77, v77\n"
        "v_mfma_f32_16x16x32_bf16 v[32:35], a[152:155], v[8:11], v[32:35]\n"
        "v_rcp_f32 v78, v78\n"
        "v_mfma_f32_16x16x32_bf16 v[36:39], a[156:159], v[8:11], v[36:39]\n"
        "v_rcp_f32 v79, v79\n"
        "v_mfma_f32_16x16x32_bf16 v[40:43], a[160:163], v[8:11], v[40:43]\n"
        "v_mul_f32 v56, v56, v76\n"
        "v_mul_f32 v57, v57, v77\n"
        "v_mfma_f32_16x16x32_bf16 v[44:47], a[164:167], v[8:11], v[44:47]\n"
        "s_waitcnt lgkmcnt(1)\n"
        "ds_read_b128 v[8:11], v0 offset:4608\n"
        "v_mul_f32 v58, v58, v78\n"
        "v_mfma_f32_16x16x32_bf16 v[24:27], a[168:171], v[12:15], v[24:27]\n"
        "v_mul_f32 v59, v59, v79\n"
        "v_cvt_pk_bf16_f32 v54, v56, v57\n"
        "v_mfma_f32_16x16x32_bf16 v[28:31], a[172:175], v[12:15], v[28:31]\n"
        "v_cvt_pk_bf16_f32 v55, v58, v59\n"
        "v_mul_f32 v74, s80, v66\n"
        "v_mfma_f32_16x16x32_bf16 v[32:35], a[176:179], v[12:15], v[32:35]\n"
        "buffer_store_dwordx4 v[52:55], v1, s[40:43], s72 offen offset:0 sc1\n"
        "v_mfma_f32_16x16x32_bf16 v[36:39], a[180:183], v[12:15], v[36:39]\n"
        "v_mul_f32 v75, s80, v67\n"
        "v_mfma_f32_16x16x32_bf16 v[40:43], a[184:187], v[12:15], v[40:43]\n"
        "v_exp_f32 v72, v72\n"
        "v_mfma_f32_16x16x32_bf16 v[44:47], a[188:191], v[12:15], v[44:47]\n"
        "s_waitcnt lgkmcnt(1)\n"
        "ds_read_b128 v[12:15], v0 offset:5120\n"
        "v_mfma_f32_16x16x32_bf16 v[24:27], a[192:195], v[16:19], v[24:27]\n"
        "v_exp_f32 v73, v73\n"
        "v_mfma_f32_16x16x32_bf16 v[28:31], a[196:199], v[16:19], v[28:31]\n"
        "v_exp_f32 v74, v74\n"
        "v_mfma_f32_16x16x32_bf16 v[32:35], a[200:203], v[16:19], v[32:35]\n"
        "v_exp_f32 v75, v75\n"
        "v_mfma_f32_16x16x32_bf16 v[36:39], a[204:207], v[16:19], v[36:39]\n"
        "v_mul_f32 v64, v64, v68\n"
        "v_mul_f32 v65, v65, v69\n"
        "v_mfma_f32_16x16x32_bf16 v[40:43], a[208:211], v[16:19], v[40:43]\n"
        "v_mul_f32 v66, v66, v70\n"
        "v_mul_f32 v67, v67, v71\n"
        "v_mfma_f32_16x16x32_bf16 v[44:47], a[212:215], v[16:19], v[44:47]\n"
        "s_waitcnt lgkmcnt(1)\n"
        "ds_read_b128 v[16:19], v0 offset:5632\n"
        "v_add_f32 v72, 1.0, v72\n"
        "v_mfma_f32_16x16x32_bf16 v[24:27], a[216:219], v[8:11], v[24:27]\n"
        "v_add_f32 v73, 1.0, v73\n"
        "v_add_f32 v74, 1.0, v74\n"
        "v_mfma_f32_16x16x32_bf16 v[28:31], a[220:223], v[8:11], v[28:31]\n"
        "v_add_f32 v75, 1.0, v75\n"
        "v_mfma_f32_16x16x32_bf16 v[32:35], a[224:227], v[8:11], v[32:35]\n"
        "v_rcp_f32 v72, v72\n"
        "v_mfma_f32_16x16x32_bf16 v[36:39], a[228:231], v[8:11], v[36:39]\n"
        "v_rcp_f32 v73, v73\n"
        "v_mfma_f32_16x16x32_bf16 v[40:43], a[232:235], v[8:11], v[40:43]\n"
        "v_rcp_f32 v74, v74\n"
        "v_mfma_f32_16x16x32_bf16 v[44:47], a[236:239], v[8:11], v[44:47]\n"
        "s_waitcnt vmcnt(1)\n"
        "v_cmp_gt_i32 vcc, s70, v4\n"
        "s_waitcnt lgkmcnt(1)\n"
        "ds_read_b128 v[8:11], v0 offset:6144\n"
        "v_mfma_f32_16x16x32_bf16 v[24:27], a[240:243], v[12:15], v[24:27]\n"
        "v_rcp_f32 v75, v75\n"
        "v_mfma_f32_16x16x32_bf16 v[28:31], a[244:247], v[12:15], v[28:31]\n"
        "v_mul_f32 v64, v64, v72\n"
        "s_and_b64 vcc, vcc, 1\n"
        "v_mfma_f32_16x16x32_bf16 v[32:35], a[248:251], v[12:15], v[32:35]\n"
        "s_cbranch_vccnz .Lf1_i01_%=\n"
        "v_mfma_f32_16x16x32_bf16 v[36:39], a[252:255], v[12:15], v[36:39]\n"
        "v_mul_f32 v65, v65, v73\n"
        "buffer_load_dwordx4 v1, s[40:43], s71 offen offset:0 sc1 lds\n"
        "v_mfma_f32_16x16x32_bf16 v[40:43], v[80:83], v[12:15], v[40:43]\n"
        "v_mul_f32 v66, v66, v74\n"
        "v_mul_f32 v67, v67, v75\n"
        "v_mfma_f32_16x16x32_bf16 v[44:47], v[84:87], v[12:15], v[44:47]\n"
        "s_waitcnt lgkmcnt(1)\n"
        "ds_read_b128 v[12:15], v0 offset:6656\n"
        "v_cvt_pk_bf16_f32 v68, v64, v65\n"
        "v_mfma_f32_16x16x32_bf16 v[24:27], v[88:91], v[16:19], v[24:27]\n"
        "v_cvt_pk_bf16_f32 v69, v66, v67\n"
        "s_nop 0\n"
        "buffer_load_dwordx4 v1, s[40:43], s71 offen offset:1024 sc1 lds\n"
        "v_mfma_f32_16x16x32_bf16 v[28:31], v[92:95], v[16:19], v[28:31]\n"
        "v_permlane16_swap_b32 v68, v70\n"
        "v_mfma_f32_16x16x32_bf16 v[32:35], v[96:99], v[16:19], v[32:35]\n"
        "v_permlane16_swap_b32 v69, v71\n"
        "v_mfma_f32_16x16x32_bf16 v[36:39], v[100:103], v[16:19], v[36:39]\n"
        "buffer_store_dwordx4 v[68:71], v7, s[40:43], s83 offen offset:0 sc1\n"
        "v_mfma_f32_16x16x32_bf16 v[40:43], v[104:107], v[16:19], v[40:43]\n"
        "v_mov_b32 v5, s74\n"
        "buffer_load_dwordx4 v1, s[40:43], s71 offen offset:2048 sc1 lds\n"
        "v_mfma_f32_16x16x32_bf16 v[44:47], v[108:111], v[16:19], v[44:47]\n"
        "s_waitcnt lgkmcnt(1)\n"
        "ds_read_b128 v[16:19], v0 offset:7168\n"
        "v_mfma_f32_16x16x32_bf16 v[24:27], v[112:115], v[8:11], v[24:27]\n"
        "buffer_load_dwordx4 v1, s[40:43], s71 offen offset:3072 sc1 lds\n"
        "s_add_u32 m0, s78, s85\n"
        "v_mfma_f32_16x16x32_bf16 v[28:31], v[116:119], v[8:11], v[28:31]\n"
        "s_add_u32 s69, s69, 1\n"
        "v_mfma_f32_16x16x32_bf16 v[32:35], v[120:123], v[8:11], v[32:35]\n"
        "v_mfma_f32_16x16x32_bf16 v[36:39], v[124:127], v[8:11], v[36:39]\n"
        "buffer_load_dwordx4 v1, s[40:43], s81 offen offset:0 sc1 lds\n"
        "v_mfma_f32_16x16x32_bf16 v[40:43], v[128:131], v[8:11], v[40:43]\n"
        "v_mfma_f32_16x16x32_bf16 v[44:47], v[132:135], v[8:11], v[44:47]\n"
        "s_waitcnt lgkmcnt(1)\n"
        "ds_read_b128 v[8:11], v0 offset:7680\n"
        "v_mfma_f32_16x16x32_bf16 v[24:27], v[136:139], v[12:15], v[24:27]\n"
        "v_mfma_f32_16x16x32_bf16 v[28:31], v[140:143], v[12:15], v[28:31]\n"
        "v_mfma_f32_16x16x32_bf16 v[32:35], v[144:147], v[12:15], v[32:35]\n"
        "v_mfma_f32_16x16x32_bf16 v[36:39], v[148:151], v[12:15], v[36:39]\n"
        "v_mfma_f32_16x16x32_bf16 v[40:43], v[152:155], v[12:15], v[40:43]\n"
        "v_mfma_f32_16x16x32_bf16 v[44:47], v[156:159], v[12:15], v[44:47]\n"
        "s_waitcnt lgkmcnt(1)\n"
        "ds_read_b128 v[12:15], v0 offset:8192\n"
        "v_mfma_f32_16x16x32_bf16 v[24:27], v[160:163], v[16:19], v[24:27]\n"
        "v_mfma_f32_16x16x32_bf16 v[28:31], v[164:167], v[16:19], v[28:31]\n"
        "v_mfma_f32_16x16x32_bf16 v[32:35], v[168:171], v[16:19], v[32:35]\n"
        "v_mfma_f32_16x16x32_bf16 v[36:39], v[172:175], v[16:19], v[36:39]\n"
        "v_mfma_f32_16x16x32_bf16 v[40:43], v[176:179], v[16:19], v[40:43]\n"
        "v_mfma_f32_16x16x32_bf16 v[44:47], v[180:183], v[16:19], v[44:47]\n"
        "s_waitcnt lgkmcnt(1)\n"
        "ds_read_b128 v[16:19], v0 offset:8704\n"
        "v_mfma_f32_16x16x32_bf16 v[24:27], v[184:187], v[8:11], v[24:27]\n"
        "v_mfma_f32_16x16x32_bf16 v[28:31], v[188:191], v[8:11], v[28:31]\n"
        "s_getreg_b32 s88, hwreg(HW_REG_IB_STS, 0, 4)\n"
        "v_mfma_f32_16x16x32_bf16 v[32:35], v[192:195], v[8:11], v[32:35]\n"
        "v_mfma_f32_16x16x32_bf16 v[36:39], v[196:199], v[8:11], v[36:39]\n"
        "v_mfma_f32_16x16x32_bf16 v[40:43], v[200:203], v[8:11], v[40:43]\n"
        "s_waitcnt vmcnt(0)\n"
        "s_barrier\n"
        "v_add_u32 v0, s78, v6\n"
        "v_mfma_f32_16x16x32_bf16 v[44:47], v[204:207], v[8:11], v[44:47]\n"
        "s_waitcnt lgkmcnt(1)\n"
        "ds_read_b128 v[8:11], v0 offset:0\n"
        "v_mfma_f32_16x16x32_bf16 v[24:27], v[208:211], v[12:15], v[24:27]\n"
        "s_cmp_le_u32 s88, 3\n"
        "s_cselect_b32 s88, s73, s89\n"
        "s_cselect_b32 s73, s89, s73\n"
        "v_mfma_f32_16x16x32_bf16 v[28:31], v[212:215], v[12:15], v[28:31]\n"
        "buffer_store_dword v5, v3, s[40:43], s88 offen sc1\n"
        "v_mfma_f32_16x16x32_bf16 v[32:35], v[216:219], v[12:15], v[32:35]\n"
        "s_getreg_b32 s88, hwreg(HW_REG_IB_STS, 0, 4)\n"
        "v_mfma_f32_16x16x32_bf16 v[36:39], v[220:223], v[12:15], v[36:39]\n"
        "s_cmp_le_u32 s88, 15\n"
        "s_cselect_b32 s88, s73, s89\n"
        "s_cselect_b32 s73, s89, s73\n"
        "v_mfma_f32_16x16x32_bf16 v[40:43], v[224:227], v[12:15], v[40:43]\n"
        "buffer_store_dword v5, v3, s[40:43], s88 offen sc1\n"
        "s_add_u32 s74, s74, 1\n"
        "v_mfma_f32_16x16x32_bf16 v[44:47], v[228:231], v[12:15], v[44:47]\n"
        "s_waitcnt lgkmcnt(1)\n"
        "ds_read_b128 v[12:15], v0 offset:512\n"
        "s_mov_b32 s84, s73\n"
        "v_mfma_f32_16x16x32_bf16 v[24:27], v[232:235], v[16:19], v[24:27]\n"
        "s_mov_b32 s73, s60\n"
        "s_add_u32 s68, s68, 1\n"
        "s_lshr_b32 s66, s68, 1\n"
        "v_mfma_f32_16x16x32_bf16 v[28:31], v[236:239], v[16:19], v[28:31]\n"
        "s_bitcmp1_b32 s54, s66\n"
        "s_cselect_b32 s66, 8192, 0\n"
        "s_lshl_b32 s75, s68, 20\n"
        "v_mfma_f32_16x16x32_bf16 v[32:35], v[240:243], v[16:19], v[32:35]\n"
        "s_add_u32 s75, s75, s66\n"
        "s_add_u32 s72, s75, s59\n"
        "s_add_u32 s83, s75, s63\n"
        "v_mfma_f32_16x16x32_bf16 v[36:39], v[244:247], v[16:19], v[36:39]\n"
        "v_mfma_f32_16x16x32_bf16 v[40:43], v[248:251], v[16:19], v[40:43]\n"
        "v_mfma_f32_16x16x32_bf16 v[44:47], v[252:255], v[16:19], v[44:47]\n"
        "s_waitcnt lgkmcnt(1)\n"
        "ds_read_b128 v[16:19], v0 offset:1024\n"
        "s_branch .Lf1_i1_%=\n"
        ".Lf1_i01_%=:\n"
        "v_mfma_f32_16x16x32_bf16 v[36:39], a[252:255], v[12:15], v[36:39]\n"
        "v_mul_f32 v65, v65, v73\n"
        "buffer_load_dword v4, v2, s[40:43], s86 offen sc1\n"
        "v_mfma_f32_16x16x32_bf16 v[40:43], v[80:83], v[12:15], v[40:43]\n"
        "v_mul_f32 v66, v66, v74\n"
        "v_mul_f32 v67, v67, v75\n"
        "v_mfma_f32_16x16x32_bf16 v[44:47], v[84:87], v[12:15], v[44:47]\n"
        "s_waitcnt lgkmcnt(1)\n"
        "ds_read_b128 v[12:15], v0 offset:6656\n"
        "v_cvt_pk_bf16_f32 v68, v64, v65\n"
        "v_mfma_f32_16x16x32_bf16 v[24:27], v[88:91], v[16:19], v[24:27]\n"
        "v_cvt_pk_bf16_f32 v69, v66, v67\n"
        "s_nop 0\n"
        "v_mfma_f32_16x16x32_bf16 v[28:31], v[92:95], v[16:19], v[28:31]\n"
        "v_permlane16_swap_b32 v68, v70\n"
        "v_mfma_f32_16x16x32_bf16 v[32:35], v[96:99], v[16:19], v[32:35]\n"
        "v_permlane16_swap_b32 v69, v71\n"
        "v_mfma_f32_16x16x32_bf16 v[36:39], v[100:103], v[16:19], v[36:39]\n"
        "buffer_store_dwordx4 v[68:71], v7, s[40:43], s83 offen offset:0 sc1\n"
        "v_mfma_f32_16x16x32_bf16 v[40:43], v[104:107], v[16:19], v[40:43]\n"
        "v_mov_b32 v5, s74\n"
        "v_mfma_f32_16x16x32_bf16 v[44:47], v[108:111], v[16:19], v[44:47]\n"
        "s_waitcnt lgkmcnt(1)\n"
        "ds_read_b128 v[16:19], v0 offset:7168\n"
        "v_mfma_f32_16x16x32_bf16 v[24:27], v[112:115], v[8:11], v[24:27]\n"
        "v_mfma_f32_16x16x32_bf16 v[28:31], v[116:119], v[8:11], v[28:31]\n"
        "v_mfma_f32_16x16x32_bf16 v[32:35], v[120:123], v[8:11], v[32:35]\n"
        "v_mfma_f32_16x16x32_bf16 v[36:39], v[124:127], v[8:11], v[36:39]\n"
        "v_mfma_f32_16x16x32_bf16 v[40:43], v[128:131], v[8:11], v[40:43]\n"
        "v_mfma_f32_16x16x32_bf16 v[44:47], v[132:135], v[8:11], v[44:47]\n"
        "s_waitcnt lgkmcnt(1)\n"
        "ds_read_b128 v[8:11], v0 offset:7680\n"
        "v_mfma_f32_16x16x32_bf16 v[24:27], v[136:139], v[12:15], v[24:27]\n"
        "v_mfma_f32_16x16x32_bf16 v[28:31], v[140:143], v[12:15], v[28:31]\n"
        "v_mfma_f32_16x16x32_bf16 v[32:35], v[144:147], v[12:15], v[32:35]\n"
        "v_mfma_f32_16x16x32_bf16 v[36:39], v[148:151], v[12:15], v[36:39]\n"
        "v_mfma_f32_16x16x32_bf16 v[40:43], v[152:155], v[12:15], v[40:43]\n"
        "v_mfma_f32_16x16x32_bf16 v[44:47], v[156:159], v[12:15], v[44:47]\n"
        "s_waitcnt lgkmcnt(1)\n"
        "ds_read_b128 v[12:15], v0 offset:8192\n"
        "v_mfma_f32_16x16x32_bf16 v[24:27], v[160:163], v[16:19], v[24:27]\n"
        "v_mfma_f32_16x16x32_bf16 v[28:31], v[164:167], v[16:19], v[28:31]\n"
        "v_mfma_f32_16x16x32_bf16 v[32:35], v[168:171], v[16:19], v[32:35]\n"
        "v_mfma_f32_16x16x32_bf16 v[36:39], v[172:175], v[16:19], v[36:39]\n"
        "v_mfma_f32_16x16x32_bf16 v[40:43], v[176:179], v[16:19], v[40:43]\n"
        "v_mfma_f32_16x16x32_bf16 v[44:47], v[180:183], v[16:19], v[44:47]\n"
        "s_waitcnt lgkmcnt(1)\n"
        "ds_read_b128 v[16:19], v0 offset:8704\n"
        "v_mfma_f32_16x16x32_bf16 v[24:27], v[184:187], v[8:11], v[24:27]\n"
        "s_waitcnt vmcnt(1)\n"
        "v_cmp_gt_i32 vcc, s70, v4\n"
        "v_mfma_f32_16x16x32_bf16 v[28:31], v[188:191], v[8:11], v[28:31]\n"
        "s_getreg_b32 s88, hwreg(HW_REG_IB_STS, 0, 4)\n"
        "v_mfma_f32_16x16x32_bf16 v[32:35], v[192:195], v[8:11], v[32:35]\n"
        "s_and_b64 vcc, vcc, 1\n"
        "v_mfma_f32_16x16x32_bf16 v[36:39], v[196:199], v[8:11], v[36:39]\n"
        "s_cbranch_vccnz .Lf1_i02_%=\n"
        "v_mfma_f32_16x16x32_bf16 v[40:43], v[200:203], v[8:11], v[40:43]\n"
        "buffer_load_dwordx4 v1, s[40:43], s71 offen offset:0 sc1 lds\n"
        "v_mfma_f32_16x16x32_bf16 v[44:47], v[204:207], v[8:11], v[44:47]\n"
        "s_waitcnt lgkmcnt(1)\n"
        "v_mfma_f32_16x16x32_bf16 v[24:27], v[208:211], v[12:15], v[24:27]\n"
        "s_cmp_le_u32 s88, 0\n"
        "s_cselect_b32 s88, s73, s89\n"
        "s_cselect_b32 s73, s89, s73\n"
        "buffer_load_dwordx4 v1, s[40:43], s71 offen offset:1024 sc1 lds\n"
        "v_mfma_f32_16x16x32_bf16 v[28:31], v[212:215], v[12:15], v[28:31]\n"
        "buffer_store_dword v5, v3, s[40:43], s88 offen sc1\n"
        "v_mfma_f32_16x16x32_bf16 v[32:35], v[216:219], v[12:15], v[32:35]\n"
        "s_getreg_b32 s88, hwreg(HW_REG_IB_STS, 0, 4)\n"
        "buffer_load_dwordx4 v1, s[40:43], s71 offen offset:2048 sc1 lds\n"
        "v_mfma_f32_16x16x32_bf16 v[36:39], v[220:223], v[12:15], v[36:39]\n"
        "s_cmp_le_u32 s88, 3\n"
        "s_cselect_b32 s88, s73, s89\n"
        "s_cselect_b32 s73, s89, s73\n"
        "v_mfma_f32_16x16x32_bf16 v[40:43], v[224:227], v[12:15], v[40:43]\n"
        "buffer_store_dword v5, v3, s[40:43], s88 offen sc1\n"
        "s_add_u32 s74, s74, 1\n"
        "buffer_load_dwordx4 v1, s[40:43], s71 offen offset:3072 sc1 lds\n"
        "s_add_u32 m0, s78, s85\n"
        "s_add_u32 s69, s69, 1\n"
        "v_mfma_f32_16x16x32_bf16 v[44:47], v[228:231], v[12:15], v[44:47]\n"
        "s_waitcnt lgkmcnt(0)\n"
        "s_mov_b32 s84, s73\n"
        "v_mfma_f32_16x16x32_bf16 v[24:27], v[232:235], v[16:19], v[24:27]\n"
        "s_mov_b32 s73, s60\n"
        "s_add_u32 s68, s68, 1\n"
        "s_lshr_b32 s66, s68, 1\n"
        "buffer_load_dwordx4 v1, s[40:43], s81 offen offset:0 sc1 lds\n"
        "v_mfma_f32_16x16x32_bf16 v[28:31], v[236:239], v[16:19], v[28:31]\n"
        "s_bitcmp1_b32 s54, s66\n"
        "s_cselect_b32 s66, 8192, 0\n"
        "s_lshl_b32 s75, s68, 20\n"
        "v_mfma_f32_16x16x32_bf16 v[32:35], v[240:243], v[16:19], v[32:35]\n"
        "s_add_u32 s75, s75, s66\n"
        "s_add_u32 s72, s75, s59\n"
        "s_add_u32 s83, s75, s63\n"
        "v_mfma_f32_16x16x32_bf16 v[36:39], v[244:247], v[16:19], v[36:39]\n"
        "v_mfma_f32_16x16x32_bf16 v[40:43], v[248:251], v[16:19], v[40:43]\n"
        "v_mfma_f32_16x16x32_bf16 v[44:47], v[252:255], v[16:19], v[44:47]\n"
        "v_mul_f32 v72, s80, v24\n"
        "v_mul_f32 v73, s80, v25\n"
        "v_mul_f32 v74, s80, v26\n"
        "v_mul_f32 v75, s80, v27\n"
        "v_exp_f32 v72, v72\n"
        "v_exp_f32 v73, v73\n"
        "v_exp_f32 v74, v74\n"
        "v_exp_f32 v75, v75\n"
        "v_mul_f32 v24, v24, v28\n"
        "v_mul_f32 v25, v25, v29\n"
        "v_mul_f32 v26, v26, v30\n"
        "v_mul_f32 v27, v27, v31\n"
        "v_add_f32 v72, 1.0, v72\n"
        "v_add_f32 v73, 1.0, v73\n"
        "v_add_f32 v74, 1.0, v74\n"
        "v_add_f32 v75, 1.0, v75\n"
        "v_rcp_f32 v72, v72\n"
        "v_rcp_f32 v73, v73\n"
        "v_rcp_f32 v74, v74\n"
        "v_rcp_f32 v75, v75\n"
        "v_mul_f32 v24, v24, v72\n"
        "v_mul_f32 v25, v25, v73\n"
        "v_mul_f32 v26, v26, v74\n"
        "v_mul_f32 v27, v27, v75\n"
        "v_cvt_pk_bf16_f32 v28, v24, v25\n"
        "v_cvt_pk_bf16_f32 v29, v26, v27\n"
        "s_waitcnt vmcnt(7)\n"
        "buffer_store_dword v5, v3, s[40:43], s84 offen sc1\n"
        "s_mov_b32 s84, s89\n"
        "v_mul_f32 v76, s80, v32\n"
        "v_mul_f32 v77, s80, v33\n"
        "v_mul_f32 v78, s80, v34\n"
        "v_mul_f32 v79, s80, v35\n"
        "v_exp_f32 v76, v76\n"
        "v_exp_f32 v77, v77\n"
        "v_exp_f32 v78, v78\n"
        "v_exp_f32 v79, v79\n"
        "v_mul_f32 v32, v32, v36\n"
        "v_mul_f32 v33, v33, v37\n"
        "v_mul_f32 v34, v34, v38\n"
        "v_mul_f32 v35, v35, v39\n"
        "v_add_f32 v76, 1.0, v76\n"
        "v_add_f32 v77, 1.0, v77\n"
        "v_add_f32 v78, 1.0, v78\n"
        "v_add_f32 v79, 1.0, v79\n"
        "v_rcp_f32 v76, v76\n"
        "v_rcp_f32 v77, v77\n"
        "v_rcp_f32 v78, v78\n"
        "v_rcp_f32 v79, v79\n"
        "v_mul_f32 v32, v32, v76\n"
        "v_mul_f32 v33, v33, v77\n"
        "v_mul_f32 v34, v34, v78\n"
        "v_mul_f32 v35, v35, v79\n"
        "v_cvt_pk_bf16_f32 v30, v32, v33\n"
        "v_cvt_pk_bf16_f32 v31, v34, v35\n"
        "buffer_store_dwordx4 v[28:31], v1, s[40:43], s72 offen offset:0 sc1\n"
        "v_mul_f32 v72, s80, v40\n"
        "v_mul_f32 v73, s80, v41\n"
        "v_mul_f32 v74, s80, v42\n"
        "v_mul_f32 v75, s80, v43\n"
        "v_exp_f32 v72, v72\n"
        "v_exp_f32 v73, v73\n"
        "v_exp_f32 v74, v74\n"
        "v_exp_f32 v75, v75\n"
        "v_mul_f32 v40, v40, v44\n"
        "v_mul_f32 v41, v41, v45\n"
        "v_mul_f32 v42, v42, v46\n"
        "v_mul_f32 v43, v43, v47\n"
        "v_add_f32 v72, 1.0, v72\n"
        "v_add_f32 v73, 1.0, v73\n"
        "v_add_f32 v74, 1.0, v74\n"
        "v_add_f32 v75, 1.0, v75\n"
        "v_rcp_f32 v72, v72\n"
        "v_rcp_f32 v73, v73\n"
        "v_rcp_f32 v74, v74\n"
        "v_rcp_f32 v75, v75\n"
        "v_mul_f32 v40, v40, v72\n"
        "v_mul_f32 v41, v41, v73\n"
        "v_mul_f32 v42, v42, v74\n"
        "v_mul_f32 v43, v43, v75\n"
        "v_cvt_pk_bf16_f32 v44, v40, v41\n"
        "v_cvt_pk_bf16_f32 v45, v42, v43\n"
        "s_nop 0\n"
        "v_permlane16_swap_b32 v44, v46\n"
        "v_permlane16_swap_b32 v45, v47\n"
        "buffer_store_dwordx4 v[44:47], v7, s[40:43], s83 offen offset:0 sc1\n"
        "v_mov_b32 v5, s74\n"
        "s_mov_b32 s84, s60\n"
        "buffer_store_dword v5, v3, s[40:43], s89 offen sc1\n"
        "buffer_store_dword v5, v3, s[40:43], s89 offen sc1\n"
        "s_waitcnt vmcnt(5)\n"
        "s_branch .Lf1_go0_%=\n"
        ".Lf1_i02_%=:\n"
        "v_mfma_f32_16x16x32_bf16 v[40:43], v[200:203], v[8:11], v[40:43]\n"
        "buffer_load_dword v4, v2, s[40:43], s86 offen sc1\n"
        "v_mfma_f32_16x16x32_bf16 v[44:47], v[204:207], v[8:11], v[44:47]\n"
        "s_waitcnt lgkmcnt(1)\n"
        "v_mfma_f32_16x16x32_bf16 v[24:27], v[208:211], v[12:15], v[24:27]\n"
        "s_cmp_le_u32 s88, 0\n"
        "s_cselect_b32 s88, s73, s89\n"
        "s_cselect_b32 s73, s89, s73\n"
        "v_mfma_f32_16x16x32_bf16 v[28:31], v[212:215], v[12:15], v[28:31]\n"
        "buffer_store_dword v5, v3, s[40:43], s88 offen sc1\n"
        "v_mfma_f32_16x16x32_bf16 v[32:35], v[216:219], v[12:15], v[32:35]\n"
        "s_getreg_b32 s88, hwreg(HW_REG_IB_STS, 0, 4)\n"
        "v_mfma_f32_16x16x32_bf16 v[36:39], v[220:223], v[12:15], v[36:39]\n"
        "s_cmp_le_u32 s88, 2\n"
        "s_cselect_b32 s88, s73, s89\n"
        "s_cselect_b32 s73, s89, s73\n"
        "v_mfma_f32_16x16x32_bf16 v[40:43], v[224:227], v[12:15], v[40:43]\n"
        "buffer_store_dword v5, v3, s[40:43], s88 offen sc1\n"
        "s_add_u32 s74, s74, 1\n"
        "v_mfma_f32_16x16x32_bf16 v[44:47], v[228:231], v[12:15], v[44:47]\n"
        "s_waitcnt lgkmcnt(0)\n"
        "s_mov_b32 s84, s73\n"
        "v_mfma_f32_16x16x32_bf16 v[24:27], v[232:235], v[16:19], v[24:27]\n"
        "s_mov_b32 s73, s60\n"
        "s_add_u32 s68, s68, 1\n"
        "s_lshr_b32 s66, s68, 1\n"
        "v_mfma_f32_16x16x32_bf16 v[28:31], v[236:239], v[16:19], v[28:31]\n"
        "s_bitcmp1_b32 s54, s66\n"
        "s_cselect_b32 s66, 8192, 0\n"
        "s_lshl_b32 s75, s68, 20\n"
        "v_mfma_f32_16x16x32_bf16 v[32:35], v[240:243], v[16:19], v[32:35]\n"
        "s_add_u32 s75, s75, s66\n"
        "s_add_u32 s72, s75, s59\n"
        "s_add_u32 s83, s75, s63\n"
        "v_mfma_f32_16x16x32_bf16 v[36:39], v[244:247], v[16:19], v[36:39]\n"
        "v_mfma_f32_16x16x32_bf16 v[40:43], v[248:251], v[16:19], v[40:43]\n"
        "v_mfma_f32_16x16x32_bf16 v[44:47], v[252:255], v[16:19], v[44:47]\n"
        "v_mul_f32 v72, s80, v24\n"
        "v_mul_f32 v73, s80, v25\n"
        "v_mul_f32 v74, s80, v26\n"
        "v_mul_f32 v75, s80, v27\n"
        "v_exp_f32 v72, v72\n"
        "v_exp_f32 v73, v73\n"
        "v_exp_f32 v74, v74\n"
        "v_exp_f32 v75, v75\n"
        "v_mul_f32 v24, v24, v28\n"
        "v_mul_f32 v25, v25, v29\n"
        "v_mul_f32 v26, v26, v30\n"
        "v_mul_f32 v27, v27, v31\n"
        "v_add_f32 v72, 1.0, v72\n"
        "v_add_f32 v73, 1.0, v73\n"
        "v_add_f32 v74, 1.0, v74\n"
        "v_add_f32 v75, 1.0, v75\n"
        "v_rcp_f32 v72, v72\n"
        "v_rcp_f32 v73, v73\n"
        "v_rcp_f32 v74, v74\n"
        "v_rcp_f32 v75, v75\n"
        "v_mul_f32 v24, v24, v72\n"
        "v_mul_f32 v25, v25, v73\n"
        "v_mul_f32 v26, v26, v74\n"
        "v_mul_f32 v27, v27, v75\n"
        "v_cvt_pk_bf16_f32 v28, v24, v25\n"
        "v_cvt_pk_bf16_f32 v29, v26, v27\n"
        "s_waitcnt vmcnt(3)\n"
        "buffer_store_dword v5, v3, s[40:43], s84 offen sc1\n"
        "s_mov_b32 s84, s89\n"
        "v_mul_f32 v76, s80, v32\n"
        "v_mul_f32 v77, s80, v33\n"
        "v_mul_f32 v78, s80, v34\n"
        "v_mul_f32 v79, s80, v35\n"
        "v_exp_f32 v76, v76\n"
        "v_exp_f32 v77, v77\n"
        "v_exp_f32 v78, v78\n"
        "v_exp_f32 v79, v79\n"
        "v_mul_f32 v32, v32, v36\n"
        "v_mul_f32 v33, v33, v37\n"
        "v_mul_f32 v34, v34, v38\n"
        "v_mul_f32 v35, v35, v39\n"
        "v_add_f32 v76, 1.0, v76\n"
        "v_add_f32 v77, 1.0, v77\n"
        "v_add_f32 v78, 1.0, v78\n"
        "v_add_f32 v79, 1.0, v79\n"
        "v_rcp_f32 v76, v76\n"
        "v_rcp_f32 v77, v77\n"
        "v_rcp_f32 v78, v78\n"
        "v_rcp_f32 v79, v79\n"
        "v_mul_f32 v32, v32, v76\n"
        "v_mul_f32 v33, v33, v77\n"
        "v_mul_f32 v34, v34, v78\n"
        "v_mul_f32 v35, v35, v79\n"
        "v_cvt_pk_bf16_f32 v30, v32, v33\n"
        "v_cvt_pk_bf16_f32 v31, v34, v35\n"
        "buffer_store_dwordx4 v[28:31], v1, s[40:43], s72 offen offset:0 sc1\n"
        "v_mul_f32 v72, s80, v40\n"
        "v_mul_f32 v73, s80, v41\n"
        "v_mul_f32 v74, s80, v42\n"
        "v_mul_f32 v75, s80, v43\n"
        "v_exp_f32 v72, v72\n"
        "v_exp_f32 v73, v73\n"
        "v_exp_f32 v74, v74\n"
        "v_exp_f32 v75, v75\n"
        "v_mul_f32 v40, v40, v44\n"
        "v_mul_f32 v41, v41, v45\n"
        "v_mul_f32 v42, v42, v46\n"
        "v_mul_f32 v43, v43, v47\n"
        "v_add_f32 v72, 1.0, v72\n"
        "v_add_f32 v73, 1.0, v73\n"
        "v_add_f32 v74, 1.0, v74\n"
        "v_add_f32 v75, 1.0, v75\n"
        "v_rcp_f32 v72, v72\n"
        "v_rcp_f32 v73, v73\n"
        "v_rcp_f32 v74, v74\n"
        "v_rcp_f32 v75, v75\n"
        "v_mul_f32 v40, v40, v72\n"
        "v_mul_f32 v41, v41, v73\n"
        "v_mul_f32 v42, v42, v74\n"
        "v_mul_f32 v43, v43, v75\n"
        "v_cvt_pk_bf16_f32 v44, v40, v41\n"
        "v_cvt_pk_bf16_f32 v45, v42, v43\n"
        "s_nop 0\n"
        "v_permlane16_swap_b32 v44, v46\n"
        "v_permlane16_swap_b32 v45, v47\n"
        "buffer_store_dwordx4 v[44:47], v7, s[40:43], s83 offen offset:0 sc1\n"
        "s_waitcnt vmcnt(5)\n"
        "v_cmp_gt_i32 vcc, s70, v4\n"
        "s_nop 0\n"
        "s_and_b64 vcc, vcc, 1\n"
        "s_cbranch_vccnz .Lf1_late0_%=\n"
        "buffer_load_dwordx4 v1, s[40:43], s71 offen offset:0 sc1 lds\n"
        "buffer_load_dwordx4 v1, s[40:43], s71 offen offset:1024 sc1 lds\n"
        "buffer_load_dwordx4 v1, s[40:43], s71 offen offset:2048 sc1 lds\n"
        "buffer_load_dwordx4 v1, s[40:43], s71 offen offset:3072 sc1 lds\n"
        "s_add_u32 m0, s78, s85\n"
        "s_add_u32 s69, s69, 1\n"
        "buffer_load_dwordx4 v1, s[40:43], s81 offen offset:0 sc1 lds\n"
        "s_waitcnt vmcnt(5)\n"
        "v_mov_b32 v5, s74\n"
        "buffer_store_dword v5, v3, s[40:43], s73 offen sc1\n"
        "s_waitcnt vmcnt(1)\n"
        "s_branch .Lf1_go0_%=\n"
        ".Lf1_late0_%=:\n"
        "s_waitcnt vmcnt(0)\n"
        "v_mov_b32 v5, s74\n"
        "buffer_store_dword v5, v3, s[40:43], s73 offen sc1\n"
        "s_mov_b32 s64, 0\n"
        ".Lf1_in0_top_%=:\n"
        "buffer_load_dword v4, v2, s[40:43], s86 offen sc1\n"
        "s_waitcnt vmcnt(0)\n"
        "v_cmp_gt_i32 vcc, s70, v4\n"
        "s_nop 0\n"
        "s_and_b64 vcc, vcc, 1\n"
        "s_cbranch_vccz .Lf1_in0_ok_%=\n"
        "s_add_u32 s64, s64, 1\n"
        "s_and_b32 s65, s64, 255\n"
        "s_cbranch_scc1 .Lf1_in0_go_%=\n"
        "s_mov_b32 s65, 49152\n"
        "s_cmp_lt_u32 s64, 1048576\n"
        "s_cbranch_scc1 .Lf1_in0_chk_%=\n"
        "v_mov_b32 v4, 1\n"
        "buffer_store_dword v4, off, s[40:43], s65 sc1\n"
        "s_waitcnt vmcnt(0)\n"
        ".Lf1_in0_chk_%=:\n"
        "buffer_load_dword v4, off, s[40:43], s65 sc1\n"
        "s_waitcnt vmcnt(0)\n"
        "v_readfirstlane_b32 s65, v4\n"
        "s_nop 0\n"
        "s_cmp_lg_u32 s65, 1\n"
        "s_cbranch_scc1 .Lf1_in0_go_%=\n"
        "s_endpgm\n"
        ".Lf1_in0_go_%=:\n"
        "s_branch .Lf1_in0_top_%=\n"
        ".Lf1_in0_ok_%=:\n"
        "buffer_load_dwordx4 v1, s[40:43], s71 offen offset:0 sc1 lds\n"
        "buffer_load_dwordx4 v1, s[40:43], s71 offen offset:1024 sc1 lds\n"
        "buffer_load_dwordx4 v1, s[40:43], s71 offen offset:2048 sc1 lds\n"
        "buffer_load_dwordx4 v1, s[40:43], s71 offen offset:3072 sc1 lds\n"
        "s_add_u32 m0, s78, s85\n"
        "s_add_u32 s69, s69, 1\n"
        "buffer_load_dwordx4 v1, s[40:43], s81 offen offset:0 sc1 lds\n"
        "s_waitcnt vmcnt(0)\n"
        ".Lf1_go0_%=:\n"
        "s_mov_b32 s72, 241172480\n"
        "s_mov_b32 s83, 241172480\n"
        "s_mov_b32 s73, 241172480\n"
        "v_add_u32 v0, s78, v6\n"
        "s_barrier\n"
        "ds_read_b128 v[8:11], v0 offset:0\n"
        "ds_read_b128 v[12:15], v0 offset:512\n"
        "ds_read_b128 v[16:19], v0 offset:1024\n"
        "s_waitcnt lgkmcnt(2)\n"
        "s_branch .Lf1_i1_%=\n"
        ".Lf1_i1_%=:\n"
        "v_mfma_f32_16x16x32_bf16 v[48:51], a[0:3], v[8:11], 0\n"
        "s_add_u32 s86, s76, 0\n"
        "buffer_load_dword v4, v2, s[40:43], s86 offen sc1\n"
        "v_mfma_f32_16x16x32_bf16 v[52:55], a[4:7], v[8:11], 0\n"
        "s_lshr_b32 s67, s69, 1\n"
        "v_mul_f32 v72, s80, v24\n"
        "v_mul_f32 v73, s80, v25\n"
        "v_mfma_f32_16x16x32_bf16 v[56:59], a[8:11], v[8:11], 0\n"
        "s_lshr_b32 s65, s67, 1\n"
        "v_mul_f32 v74, s80, v26\n"
        "v_mul_f32 v75, s80, v27\n"
        "v_mfma_f32_16x16x32_bf16 v[60:63], a[12:15], v[8:11], 0\n"
        "s_bitcmp1_b32 s54, s65\n"
        "s_cselect_b32 s65, 8192, 0\n"
        "v_mul_f32 v76, s80, v32\n"
        "v_mfma_f32_16x16x32_bf16 v[64:67], a[16:19], v[8:11], 0\n"
        "s_lshl_b32 s77, s67, 20\n"
        "v_exp_f32 v72, v72\n"
        "v_mfma_f32_16x16x32_bf16 v[68:71], a[20:23], v[8:11], 0\n"
        "s_waitcnt lgkmcnt(1)\n"
        "ds_read_b128 v[8:11], v0 offset:1536\n"
        "v_mul_f32 v77, s80, v33\n"
        "v_mfma_f32_16x16x32_bf16 v[48:51], a[24:27], v[12:15], v[48:51]\n"
        "s_add_u32 s77, s77, s65\n"
        "v_exp_f32 v73, v73\n"
        "v_mfma_f32_16x16x32_bf16 v[52:55], a[28:31], v[12:15], v[52:55]\n"
        "s_add_u32 s71, s77, s58\n"
        "v_exp_f32 v74, v74\n"
        "v_mfma_f32_16x16x32_bf16 v[56:59], a[32:35], v[12:15], v[56:59]\n"
        "s_add_u32 s81, s77, s62\n"
        "v_exp_f32 v75, v75\n"
        "v_mfma_f32_16x16x32_bf16 v[60:63], a[36:39], v[12:15], v[60:63]\n"
        "s_add_u32 s78, s78, 18432\n"
        "v_mul_f32 v24, v24, v28\n"
        "v_mul_f32 v25, v25, v29\n"
        "v_mfma_f32_16x16x32_bf16 v[64:67], a[40:43], v[12:15], v[64:67]\n"
        "s_cmp_eq_u32 s78, 55296\n"
        "s_cselect_b32 s78, 0, s78\n"
        "v_mul_f32 v26, v26, v30\n"
        "v_mfma_f32_16x16x32_bf16 v[68:71], a[44:47], v[12:15], v[68:71]\n"
        "s_waitcnt lgkmcnt(1)\n"
        "ds_read_b128 v[12:15], v0 offset:2048\n"
        "v_mul_f32 v27, v27, v31\n"
        "v_mfma_f32_16x16x32_bf16 v[48:51], a[48:51], v[16:19], v[48:51]\n"
        "s_add_u32 m0, s78, s61\n"
        "v_add_f32 v72, 1.0, v72\n"
        "v_add_f32 v73, 1.0, v73\n"
        "v_mfma_f32_16x16x32_bf16 v[52:55], a[52:55], v[16:19], v[52:55]\n"
        "s_lshr_b32 s70, s69, 1\n"
        "v_add_f32 v74, 1.0, v74\n"
        "v_add_f32 v75, 1.0, v75\n"
        "v_mfma_f32_16x16x32_bf16 v[56:59], a[56:59], v[16:19], v[56:59]\n"
        "s_waitcnt vmcnt(3)\n"
        "v_rcp_f32 v72, v72\n"
        "v_mfma_f32_16x16x32_bf16 v[60:63], a[60:63], v[16:19], v[60:63]\n"
        "buffer_store_dword v5, v3, s[40:43], s84 offen sc1\n"
        "v_mul_f32 v78, s80, v34\n"
        "v_mfma_f32_16x16x32_bf16 v[64:67], a[64:67], v[16:19], v[64:67]\n"
        "v_rcp_f32 v73, v73\n"
        "v_mfma_f32_16x16x32_bf16 v[68:71], a[68:71], v[16:19], v[68:71]\n"
        "s_waitcnt lgkmcnt(1)\n"
        "ds_read_b128 v[16:19], v0 offset:2560\n"
        "v_mul_f32 v79, s80, v35\n"
        "v_mfma_f32_16x16x32_bf16 v[48:51], a[72:75], v[8:11], v[48:51]\n"
        "v_rcp_f32 v74, v74\n"
        "v_mfma_f32_16x16x32_bf16 v[52:55], a[76:79], v[8:11], v[52:55]\n"
        "v_rcp_f32 v75, v75\n"
        "v_mfma_f32_16x16x32_bf16 v[56:59], a[80:83], v[8:11], v[56:59]\n"
        "v_mul_f32 v24, v24, v72\n"
        "v_mul_f32 v25, v25, v73\n"
        "v_mfma_f32_16x16x32_bf16 v[60:63], a[84:87], v[8:11], v[60:63]\n"
        "v_mul_f32 v26, v26, v74\n"
        "v_mul_f32 v27, v27, v75\n"
        "v_mfma_f32_16x16x32_bf16 v[64:67], a[88:91], v[8:11], v[64:67]\n"
        "v_cvt_pk_bf16_f32 v28, v24, v25\n"
        "v_cvt_pk_bf16_f32 v29, v26, v27\n"
        "v_mfma_f32_16x16x32_bf16 v[68:71], a[92:95], v[8:11], v[68:71]\n"
        "s_waitcnt lgkmcnt(1)\n"
        "ds_read_b128 v[8:11], v0 offset:3072\n"
        "v_mul_f32 v72, s80, v40\n"
        "v_mfma_f32_16x16x32_bf16 v[48:51], a[96:99], v[12:15], v[48:51]\n"
        "v_exp_f32 v76, v76\n"
        "v_mfma_f32_16x16x32_bf16 v[52:55], a[100:103], v[12:15], v[52:55]\n"
        "v_exp_f32 v77, v77\n"
        "v_mfma_f32_16x16x32_bf16 v[56:59], a[104:107], v[12:15], v[56:59]\n"
        "v_exp_f32 v78, v78\n"
        "v_mfma_f32_16x16x32_bf16 v[60:63], a[108:111], v[12:15], v[60:63]\n"
        "v_exp_f32 v79, v79\n"
        "v_mfma_f32_16x16x32_bf16 v[64:67], a[112:115], v[12:15], v[64:67]\n"
        "s_waitcnt vmcnt(1)\n"
        "v_cmp_gt_i32 vcc, s70, v4\n"
        "v_mul_f32 v32, v32, v36\n"
        "v_mfma_f32_16x16x32_bf16 v[68:71], a[116:119], v[12:15], v[68:71]\n"
        "s_waitcnt lgkmcnt(1)\n"
        "ds_read_b128 v[12:15], v0 offset:3584\n"
        "v_mul_f32 v33, v33, v37\n"
        "v_mfma_f32_16x16x32_bf16 v[48:51], a[120:123], v[16:19], v[48:51]\n"
        "v_mul_f32 v34, v34, v38\n"
        "s_and_b64 vcc, vcc, 1\n"
        "v_mfma_f32_16x16x32_bf16 v[52:55], a[124:127], v[16:19], v[52:55]\n"
        "s_cbranch_vccnz .Lf1_i10_%=\n"
        "v_mfma_f32_16x16x32_bf16 v[56:59], a[128:131], v[16:19], v[56:59]\n"
        "v_mul_f32 v35, v35, v39\n"
        "buffer_load_dwordx4 v1, s[40:43], s71 offen offset:0 sc1 lds\n"
        "v_mfma_f32_16x16x32_bf16 v[60:63], a[132:135], v[16:19], v[60:63]\n"
        "v_add_f32 v76, 1.0, v76\n"
        "v_add_f32 v77, 1.0, v77\n"
        "v_mfma_f32_16x16x32_bf16 v[64:67], a[136:139], v[16:19], v[64:67]\n"
        "v_add_f32 v78, 1.0, v78\n"
        "v_add_f32 v79, 1.0, v79\n"
        "v_mfma_f32_16x16x32_bf16 v[68:71], a[140:143], v[16:19], v[68:71]\n"
        "s_waitcnt lgkmcnt(1)\n"
        "ds_read_b128 v[16:19], v0 offset:4096\n"
        "v_mul_f32 v73, s80, v41\n"
        "v_mfma_f32_16x16x32_bf16 v[48:51], a[144:147], v[8:11], v[48:51]\n"
        "v_rcp_f32 v76, v76\n"
        "buffer_load_dwordx4 v1, s[40:43], s71 offen offset:1024 sc1 lds\n"
        "v_mfma_f32_16x16x32_bf16 v[52:55], a[148:151], v[8:11], v[52:55]\n"
        "v_rcp_f32 v77, v77\n"
        "v_mfma_f32_16x16x32_bf16 v[56:59], a[152:155], v[8:11], v[56:59]\n"
        "v_rcp_f32 v78, v78\n"
        "v_mfma_f32_16x16x32_bf16 v[60:63], a[156:159], v[8:11], v[60:63]\n"
        "v_rcp_f32 v79, v79\n"
        "v_mfma_f32_16x16x32_bf16 v[64:67], a[160:163], v[8:11], v[64:67]\n"
        "v_mul_f32 v32, v32, v76\n"
        "v_mul_f32 v33, v33, v77\n"
        "buffer_load_dwordx4 v1, s[40:43], s71 offen offset:2048 sc1 lds\n"
        "v_mfma_f32_16x16x32_bf16 v[68:71], a[164:167], v[8:11], v[68:71]\n"
        "s_waitcnt lgkmcnt(1)\n"
        "ds_read_b128 v[8:11], v0 offset:4608\n"
        "v_mul_f32 v34, v34, v78\n"
        "v_mfma_f32_16x16x32_bf16 v[48:51], a[168:171], v[12:15], v[48:51]\n"
        "v_mul_f32 v35, v35, v79\n"
        "v_cvt_pk_bf16_f32 v30, v32, v33\n"
        "v_mfma_f32_16x16x32_bf16 v[52:55], a[172:175], v[12:15], v[52:55]\n"
        "v_cvt_pk_bf16_f32 v31, v34, v35\n"
        "v_mul_f32 v74, s80, v42\n"
        "v_mfma_f32_16x16x32_bf16 v[56:59], a[176:179], v[12:15], v[56:59]\n"
        "buffer_store_dwordx4 v[28:31], v1, s[40:43], s72 offen offset:0 sc1\n"
        "v_mfma_f32_16x16x32_bf16 v[60:63], a[180:183], v[12:15], v[60:63]\n"
        "v_mul_f32 v75, s80, v43\n"
        "buffer_load_dwordx4 v1, s[40:43], s71 offen offset:3072 sc1 lds\n"
        "v_mfma_f32_16x16x32_bf16 v[64:67], a[184:187], v[12:15], v[64:67]\n"
        "s_add_u32 m0, s78, s85\n"
        "v_exp_f32 v72, v72\n"
        "v_mfma_f32_16x16x32_bf16 v[68:71], a[188:191], v[12:15], v[68:71]\n"
        "s_waitcnt lgkmcnt(1)\n"
        "ds_read_b128 v[12:15], v0 offset:5120\n"
        "s_add_u32 s69, s69, 1\n"
        "v_mfma_f32_16x16x32_bf16 v[48:51], a[192:195], v[16:19], v[48:51]\n"
        "v_exp_f32 v73, v73\n"
        "buffer_load_dwordx4 v1, s[40:43], s81 offen offset:0 sc1 lds\n"
        "v_mfma_f32_16x16x32_bf16 v[52:55], a[196:199], v[16:19], v[52:55]\n"
        "v_exp_f32 v74, v74\n"
        "v_mfma_f32_16x16x32_bf16 v[56:59], a[200:203], v[16:19], v[56:59]\n"
        "v_exp_f32 v75, v75\n"
        "v_mfma_f32_16x16x32_bf16 v[60:63], a[204:207], v[16:19], v[60:63]\n"
        "v_mul_f32 v40, v40, v44\n"
        "v_mul_f32 v41, v41, v45\n"
        "v_mfma_f32_16x16x32_bf16 v[64:67], a[208:211], v[16:19], v[64:67]\n"
        "v_mul_f32 v42, v42, v46\n"
        "v_mul_f32 v43, v43, v47\n"
        "v_mfma_f32_16x16x32_bf16 v[68:71], a[212:215], v[16:19], v[68:71]\n"
        "s_waitcnt lgkmcnt(1)\n"
        "ds_read_b128 v[16:19], v0 offset:5632\n"
        "v_add_f32 v72, 1.0, v72\n"
        "v_mfma_f32_16x16x32_bf16 v[48:51], a[216:219], v[8:11], v[48:51]\n"
        "v_add_f32 v73, 1.0, v73\n"
        "v_add_f32 v74, 1.0, v74\n"
        "v_mfma_f32_16x16x32_bf16 v[52:55], a[220:223], v[8:11], v[52:55]\n"
        "v_add_f32 v75, 1.0, v75\n"
        "v_mfma_f32_16x16x32_bf16 v[56:59], a[224:227], v[8:11], v[56:59]\n"
        "v_rcp_f32 v72, v72\n"
        "v_mfma_f32_16x16x32_bf16 v[60:63], a[228:231], v[8:11], v[60:63]\n"
        "v_rcp_f32 v73, v73\n"
        "v_mfma_f32_16x16x32_bf16 v[64:67], a[232:235], v[8:11], v[64:67]\n"
        "v_rcp_f32 v74, v74\n"
        "v_mfma_f32_16x16x32_bf16 v[68:71], a[236:239], v[8:11], v[68:71]\n"
        "s_waitcnt lgkmcnt(1)\n"
        "ds_read_b128 v[8:11], v0 offset:6144\n"
        "v_mfma_f32_16x16x32_bf16 v[48:51], a[240:243], v[12:15], v[48:51]\n"
        "v_rcp_f32 v75, v75\n"
        "v_mfma_f32_16x16x32_bf16 v[52:55], a[244:247], v[12:15], v[52:55]\n"
        "v_mul_f32 v40, v40, v72\n"
        "v_mfma_f32_16x16x32_bf16 v[56:59], a[248:251], v[12:15], v[56:59]\n"
        "v_mfma_f32_16x16x32_bf16 v[60:63], a[252:255], v[12:15], v[60:63]\n"
        "v_mul_f32 v41, v41, v73\n"
        "v_mfma_f32_16x16x32_bf16 v[64:67], v[80:83], v[12:15], v[64:67]\n"
        "v_mul_f32 v42, v42, v74\n"
        "v_mul_f32 v43, v43, v75\n"
        "v_mfma_f32_16x16x32_bf16 v[68:71], v[84:87], v[12:15], v[68:71]\n"
        "s_waitcnt lgkmcnt(1)\n"
        "ds_read_b128 v[12:15], v0 offset:6656\n"
        "v_cvt_pk_bf16_f32 v44, v40, v41\n"
        "v_mfma_f32_16x16x32_bf16 v[48:51], v[88:91], v[16:19], v[48:51]\n"
        "v_cvt_pk_bf16_f32 v45, v42, v43\n"
        "s_nop 0\n"
        "v_mfma_f32_16x16x32_bf16 v[52:55], v[92:95], v[16:19], v[52:55]\n"
        "v_permlane16_swap_b32 v44, v46\n"
        "v_mfma_f32_16x16x32_bf16 v[56:59], v[96:99], v[16:19], v[56:59]\n"
        "v_permlane16_swap_b32 v45, v47\n"
        "v_mfma_f32_16x16x32_bf16 v[60:63], v[100:103], v[16:19], v[60:63]\n"
        "buffer_store_dwordx4 v[44:47], v7, s[40:43], s83 offen offset:0 sc1\n"
        "v_mfma_f32_16x16x32_bf16 v[64:67], v[104:107], v[16:19], v[64:67]\n"
        "v_mov_b32 v5, s74\n"
        "v_mfma_f32_16x16x32_bf16 v[68:71], v[108:111], v[16:19], v[68:71]\n"
        "s_waitcnt lgkmcnt(1)\n"
        "ds_read_b128 v[16:19], v0 offset:7168\n"
        "v_mfma_f32_16x16x32_bf16 v[48:51], v[112:115], v[8:11], v[48:51]\n"
        "v_mfma_f32_16x16x32_bf16 v[52:55], v[116:119], v[8:11], v[52:55]\n"
        "v_mfma_f32_16x16x32_bf16 v[56:59], v[120:123], v[8:11], v[56:59]\n"
        "v_mfma_f32_16x16x32_bf16 v[60:63], v[124:127], v[8:11], v[60:63]\n"
        "v_mfma_f32_16x16x32_bf16 v[64:67], v[128:131], v[8:11], v[64:67]\n"
        "v_mfma_f32_16x16x32_bf16 v[68:71], v[132:135], v[8:11], v[68:71]\n"
        "s_waitcnt lgkmcnt(1)\n"
        "ds_read_b128 v[8:11], v0 offset:7680\n"
        "v_mfma_f32_16x16x32_bf16 v[48:51], v[136:139], v[12:15], v[48:51]\n"
        "v_mfma_f32_16x16x32_bf16 v[52:55], v[140:143], v[12:15], v[52:55]\n"
        "v_mfma_f32_16x16x32_bf16 v[56:59], v[144:147], v[12:15], v[56:59]\n"
        "v_mfma_f32_16x16x32_bf16 v[60:63], v[148:151], v[12:15], v[60:63]\n"
        "v_mfma_f32_16x16x32_bf16 v[64:67], v[152:155], v[12:15], v[64:67]\n"
        "v_mfma_f32_16x16x32_bf16 v[68:71], v[156:159], v[12:15], v[68:71]\n"
        "s_waitcnt lgkmcnt(1)\n"
        "ds_read_b128 v[12:15], v0 offset:8192\n"
        "v_mfma_f32_16x16x32_bf16 v[48:51], v[160:163], v[16:19], v[48:51]\n"
        "v_mfma_f32_16x16x32_bf16 v[52:55], v[164:167], v[16:19], v[52:55]\n"
        "v_mfma_f32_16x16x32_bf16 v[56:59], v[168:171], v[16:19], v[56:59]\n"
        "v_mfma_f32_16x16x32_bf16 v[60:63], v[172:175], v[16:19], v[60:63]\n"
        "v_mfma_f32_16x16x32_bf16 v[64:67], v[176:179], v[16:19], v[64:67]\n"
        "v_mfma_f32_16x16x32_bf16 v[68:71], v[180:183], v[16:19], v[68:71]\n"
        "s_waitcnt lgkmcnt(1)\n"
        "ds_read_b128 v[16:19], v0 offset:8704\n"
        "v_mfma_f32_16x16x32_bf16 v[48:51], v[184:187], v[8:11], v[48:51]\n"
        "v_mfma_f32_16x16x32_bf16 v[52:55], v[188:191], v[8:11], v[52:55]\n"
        "s_getreg_b32 s88, hwreg(HW_REG_IB_STS, 0, 4)\n"
        "v_mfma_f32_16x16x32_bf16 v[56:59], v[192:195], v[8:11], v[56:59]\n"
        "v_mfma_f32_16x16x32_bf16 v[60:63], v[196:199], v[8:11], v[60:63]\n"
        "v_mfma_f32_16x16x32_bf16 v[64:67], v[200:203], v[8:11], v[64:67]\n"
        "s_waitcnt vmcnt(1)\n"
        "s_barrier\n"
        "v_add_u32 v0, s78, v6\n"
        "v_mfma_f32_16x16x32_bf16 v[68:71], v[204:207], v[8:11], v[68:71]\n"
        "s_waitcnt lgkmcnt(1)\n"
        "ds_read_b128 v[8:11], v0 offset:0\n"
        "v_mfma_f32_16x16x32_bf16 v[48:51], v[208:211], v[12:15], v[48:51]\n"
        "s_cmp_le_u32 s88, 0\n"
        "s_cselect_b32 s88, s73, s89\n"
        "s_cselect_b32 s73, s89, s73\n"
        "v_mfma_f32_16x16x32_bf16 v[52:55], v[212:215], v[12:15], v[52:55]\n"
        "buffer_store_dword v5, v3, s[40:43], s88 offen sc1\n"
        "v_mfma_f32_16x16x32_bf16 v[56:59], v[216:219], v[12:15], v[56:59]\n"
        "s_getreg_b32 s88, hwreg(HW_REG_IB_STS, 0, 4)\n"
        "v_mfma_f32_16x16x32_bf16 v[60:63], v[220:223], v[12:15], v[60:63]\n"
        "s_cmp_le_u32 s88, 1\n"
        "s_cselect_b32 s88, s73, s89\n"
        "s_cselect_b32 s73, s89, s73\n"
        "v_mfma_f32_16x16x32_bf16 v[64:67], v[224:227], v[12:15], v[64:67]\n"
        "buffer_store_dword v5, v3, s[40:43], s88 offen sc1\n"
        "s_add_u32 s74, s74, 1\n"
        "v_mfma_f32_16x16x32_bf16 v[68:71], v[228:231], v[12:15], v[68:71]\n"
        "s_waitcnt lgkmcnt(1)\n"
        "ds_read_b128 v[12:15], v0 offset:512\n"
        "s_mov_b32 s84, s73\n"
        "v_mfma_f32_16x16x32_bf16 v[48:51], v[232:235], v[16:19], v[48:51]\n"
        "s_mov_b32 s73, s60\n"
        "s_add_u32 s72, s75, s59\n"
        "s_add_u32 s83, s75, s63\n"
        "v_mfma_f32_16x16x32_bf16 v[52:55], v[236:239], v[16:19], v[52:55]\n"
        "s_add_u32 s72, s72, 49152\n"
        "s_add_u32 s83, s83, 4096\n"
        "v_mfma_f32_16x16x32_bf16 v[56:59], v[240:243], v[16:19], v[56:59]\n"
        "v_mfma_f32_16x16x32_bf16 v[60:63], v[244:247], v[16:19], v[60:63]\n"
        "v_mfma_f32_16x16x32_bf16 v[64:67], v[248:251], v[16:19], v[64:67]\n"
        "v_mfma_f32_16x16x32_bf16 v[68:71], v[252:255], v[16:19], v[68:71]\n"
        "s_waitcnt lgkmcnt(1)\n"
        "ds_read_b128 v[16:19], v0 offset:1024\n"
        "s_branch .Lf1_i0_%=\n"
        ".Lf1_i10_%=:\n"
        "v_mfma_f32_16x16x32_bf16 v[56:59], a[128:131], v[16:19], v[56:59]\n"
        "v_mul_f32 v35, v35, v39\n"
        "buffer_load_dword v4, v2, s[40:43], s86 offen sc1\n"
        "v_mfma_f32_16x16x32_bf16 v[60:63], a[132:135], v[16:19], v[60:63]\n"
        "v_add_f32 v76, 1.0, v76\n"
        "v_add_f32 v77, 1.0, v77\n"
        "v_mfma_f32_16x16x32_bf16 v[64:67], a[136:139], v[16:19], v[64:67]\n"
        "v_add_f32 v78, 1.0, v78\n"
        "v_add_f32 v79, 1.0, v79\n"
        "v_mfma_f32_16x16x32_bf16 v[68:71], a[140:143], v[16:19], v[68:71]\n"
        "s_waitcnt lgkmcnt(1)\n"
        "ds_read_b128 v[16:19], v0 offset:4096\n"
        "v_mul_f32 v73, s80, v41\n"
        "v_mfma_f32_16x16x32_bf16 v[48:51], a[144:147], v[8:11], v[48:51]\n"
        "v_rcp_f32 v76, v76\n"
        "v_mfma_f32_16x16x32_bf16 v[52:55], a[148:151], v[8:11], v[52:55]\n"
        "v_rcp_f32 v77, v77\n"
        "v_mfma_f32_16x16x32_bf16 v[56:59], a[152:155], v[8:11], v[56:59]\n"
        "v_rcp_f32 v78, v78\n"
        "v_mfma_f32_16x16x32_bf16 v[60:63], a[156:159], v[8:11], v[60:63]\n"
        "v_rcp_f32 v79, v79\n"
        "v_mfma_f32_16x16x32_bf16 v[64:67], a[160:163], v[8:11], v[64:67]\n"
        "v_mul_f32 v32, v32, v76\n"
        "v_mul_f32 v33, v33, v77\n"
        "v_mfma_f32_16x16x32_bf16 v[68:71], a[164:167], v[8:11], v[68:71]\n"
        "s_waitcnt lgkmcnt(1)\n"
        "ds_read_b128 v[8:11], v0 offset:4608\n"
        "v_mul_f32 v34, v34, v78\n"
        "v_mfma_f32_16x16x32_bf16 v[48:51], a[168:171], v[12:15], v[48:51]\n"
        "v_mul_f32 v35, v35, v79\n"
        "v_cvt_pk_bf16_f32 v30, v32, v33\n"
        "v_mfma_f32_16x16x32_bf16 v[52:55], a[172:175], v[12:15], v[52:55]\n"
        "v_cvt_pk_bf16_f32 v31, v34, v35\n"
        "v_mul_f32 v74, s80, v42\n"
        "v_mfma_f32_16x16x32_bf16 v[56:59], a[176:179], v[12:15], v[56:59]\n"
        "buffer_store_dwordx4 v[28:31], v1, s[40:43], s72 offen offset:0 sc1\n"
        "v_mfma_f32_16x16x32_bf16 v[60:63], a[180:183], v[12:15], v[60:63]\n"
        "v_mul_f32 v75, s80, v43\n"
        "v_mfma_f32_16x16x32_bf16 v[64:67], a[184:187], v[12:15], v[64:67]\n"
        "v_exp_f32 v72, v72\n"
        "v_mfma_f32_16x16x32_bf16 v[68:71], a[188:191], v[12:15], v[68:71]\n"
        "s_waitcnt lgkmcnt(1)\n"
        "ds_read_b128 v[12:15], v0 offset:5120\n"
        "v_mfma_f32_16x16x32_bf16 v[48:51], a[192:195], v[16:19], v[48:51]\n"
        "v_exp_f32 v73, v73\n"
        "v_mfma_f32_16x16x32_bf16 v[52:55], a[196:199], v[16:19], v[52:55]\n"
        "v_exp_f32 v74, v74\n"
        "v_mfma_f32_16x16x32_bf16 v[56:59], a[200:203], v[16:19], v[56:59]\n"
        "v_exp_f32 v75, v75\n"
        "v_mfma_f32_16x16x32_bf16 v[60:63], a[204:207], v[16:19], v[60:63]\n"
        "v_mul_f32 v40, v40, v44\n"
        "v_mul_f32 v41, v41, v45\n"
        "v_mfma_f32_16x16x32_bf16 v[64:67], a[208:211], v[16:19], v[64:67]\n"
        "v_mul_f32 v42, v42, v46\n"
        "v_mul_f32 v43, v43, v47\n"
        "v_mfma_f32_16x16x32_bf16 v[68:71], a[212:215], v[16:19], v[68:71]\n"
        "s_waitcnt lgkmcnt(1)\n"
        "ds_read_b128 v[16:19], v0 offset:5632\n"
        "v_add_f32 v72, 1.0, v72\n"
        "v_mfma_f32_16x16x32_bf16 v[48:51], a[216:219], v[8:11], v[48:51]\n"
        "v_add_f32 v73, 1.0, v73\n"
        "v_add_f32 v74, 1.0, v74\n"
        "v_mfma_f32_16x16x32_bf16 v[52:55], a[220:223], v[8:11], v[52:55]\n"
        "v_add_f32 v75, 1.0, v75\n"
        "v_mfma_f32_16x16x32_bf16 v[56:59], a[224:227], v[8:11], v[56:59]\n"
        "v_rcp_f32 v72, v72\n"
        "v_mfma_f32_16x16x32_bf16 v[60:63], a[228:231], v[8:11], v[60:63]\n"
        "v_rcp_f32 v73, v73\n"
        "v_mfma_f32_16x16x32_bf16 v[64:67], a[232:235], v[8:11], v[64:67]\n"
        "v_rcp_f32 v74, v74\n"
        "v_mfma_f32_16x16x32_bf16 v[68:71], a[236:239], v[8:11], v[68:71]\n"
        "s_waitcnt vmcnt(1)\n"
        "v_cmp_gt_i32 vcc, s70, v4\n"
        "s_waitcnt lgkmcnt(1)\n"
        "ds_read_b128 v[8:11], v0 offset:6144\n"
        "v_mfma_f32_16x16x32_bf16 v[48:51], a[240:243], v[12:15], v[48:51]\n"
        "v_rcp_f32 v75, v75\n"
        "v_mfma_f32_16x16x32_bf16 v[52:55], a[244:247], v[12:15], v[52:55]\n"
        "v_mul_f32 v40, v40, v72\n"
        "s_and_b64 vcc, vcc, 1\n"
        "v_mfma_f32_16x16x32_bf16 v[56:59], a[248:251], v[12:15], v[56:59]\n"
        "s_cbranch_vccnz .Lf1_i11_%=\n"
        "v_mfma_f32_16x16x32_bf16 v[60:63], a[252:255], v[12:15], v[60:63]\n"
        "v_mul_f32 v41, v41, v73\n"
        "buffer_load_dwordx4 v1, s[40:43], s71 offen offset:0 sc1 lds\n"
        "v_mfma_f32_16x16x32_bf16 v[64:67], v[80:83], v[12:15], v[64:67]\n"
        "v_mul_f32 v42, v42, v74\n"
        "v_mul_f32 v43, v43, v75\n"
        "v_mfma_f32_16x16x32_bf16 v[68:71], v[84:87], v[12:15], v[68:71]\n"
        "s_waitcnt lgkmcnt(1)\n"
        "ds_read_b128 v[12:15], v0 offset:6656\n"
        "v_cvt_pk_bf16_f32 v44, v40, v41\n"
        "v_mfma_f32_16x16x32_bf16 v[48:51], v[88:91], v[16:19], v[48:51]\n"
        "v_cvt_pk_bf16_f32 v45, v42, v43\n"
        "s_nop 0\n"
        "buffer_load_dwordx4 v1, s[40:43], s71 offen offset:1024 sc1 lds\n"
        "v_mfma_f32_16x16x32_bf16 v[52:55], v[92:95], v[16:19], v[52:55]\n"
        "v_permlane16_swap_b32 v44, v46\n"
        "v_mfma_f32_16x16x32_bf16 v[56:59], v[96:99], v[16:19], v[56:59]\n"
        "v_permlane16_swap_b32 v45, v47\n"
        "v_mfma_f32_16x16x32_bf16 v[60:63], v[100:103], v[16:19], v[60:63]\n"
        "buffer_store_dwordx4 v[44:47], v7, s[40:43], s83 offen offset:0 sc1\n"
        "v_mfma_f32_16x16x32_bf16 v[64:67], v[104:107], v[16:19], v[64:67]\n"
        "v_mov_b32 v5, s74\n"
        "buffer_load_dwordx4 v1, s[40:43], s71 offen offset:2048 sc1 lds\n"
        "v_mfma_f32_16x16x32_bf16 v[68:71], v[108:111], v[16:19], v[68:71]\n"
        "s_waitcnt lgkmcnt(1)\n"
        "ds_read_b128 v[16:19], v0 offset:7168\n"
        "v_mfma_f32_16x16x32_bf16 v[48:51], v[112:115], v[8:11], v[48:51]\n"
        "buffer_load_dwordx4 v1, s[40:43], s71 offen offset:3072 sc1 lds\n"
        "s_add_u32 m0, s78, s85\n"
        "v_mfma_f32_16x16x32_bf16 v[52:55], v[116:119], v[8:11], v[52:55]\n"
        "s_add_u32 s69, s69, 1\n"
        "v_mfma_f32_16x16x32_bf16 v[56:59], v[120:123], v[8:11], v[56:59]\n"
        "v_mfma_f32_16x16x32_bf16 v[60:63], v[124:127], v[8:11], v[60:63]\n"
        "buffer_load_dwordx4 v1, s[40:43], s81 offen offset:0 sc1 lds\n"
        "v_mfma_f32_16x16x32_bf16 v[64:67], v[128:131], v[8:11], v[64:67]\n"
        "v_mfma_f32_16x16x32_bf16 v[68:71], v[132:135], v[8:11], v[68:71]\n"
        "s_waitcnt lgkmcnt(1)\n"
        "ds_read_b128 v[8:11], v0 offset:7680\n"
        "v_mfma_f32_16x16x32_bf16 v[48:51], v[136:139], v[12:15], v[48:51]\n"
        "v_mfma_f32_16x16x32_bf16 v[52:55], v[140:143], v[12:15], v[52:55]\n"
        "v_mfma_f32_16x16x32_bf16 v[56:59], v[144:147], v[12:15], v[56:59]\n"
        "v_mfma_f32_16x16x32_bf16 v[60:63], v[148:151], v[12:15], v[60:63]\n"
        "v_mfma_f32_16x16x32_bf16 v[64:67], v[152:155], v[12:15], v[64:67]\n"
        "v_mfma_f32_16x16x32_bf16 v[68:71], v[156:159], v[12:15], v[68:71]\n"
        "s_waitcnt lgkmcnt(1)\n"
        "ds_read_b128 v[12:15], v0 offset:8192\n"
        "v_mfma_f32_16x16x32_bf16 v[48:51], v[160:163], v[16:19], v[48:51]\n"
        "v_mfma_f32_16x16x32_bf16 v[52:55], v[164:167], v[16:19], v[52:55]\n"
        "v_mfma_f32_16x16x32_bf16 v[56:59], v[168:171], v[16:19], v[56:59]\n"
        "v_mfma_f32_16x16x32_bf16 v[60:63], v[172:175], v[16:19], v[60:63]\n"
        "v_mfma_f32_16x16x32_bf16 v[64:67], v[176:179], v[16:19], v[64:67]\n"
        "v_mfma_f32_16x16x32_bf16 v[68:71], v[180:183], v[16:19], v[68:71]\n"
        "s_waitcnt lgkmcnt(1)\n"
        "ds_read_b128 v[16:19], v0 offset:8704\n"
        "v_mfma_f32_16x16x32_bf16 v[48:51], v[184:187], v[8:11], v[48:51]\n"
        "v_mfma_f32_16x16x32_bf16 v[52:55], v[188:191], v[8:11], v[52:55]\n"
        "s_getreg_b32 s88, hwreg(HW_REG_IB_STS, 0, 4)\n"
        "v_mfma_f32_16x16x32_bf16 v[56:59], v[192:195], v[8:11], v[56:59]\n"
        "v_mfma_f32_16x16x32_bf16 v[60:63], v[196:199], v[8:11], v[60:63]\n"
        "v_mfma_f32_16x16x32_bf16 v[64:67], v[200:203], v[8:11], v[64:67]\n"
        "s_waitcnt vmcnt(0)\n"
        "s_barrier\n"
        "v_add_u32 v0, s78, v6\n"
        "v_mfma_f32_16x16x32_bf16 v[68:71], v[204:207], v[8:11], v[68:71]\n"
        "s_waitcnt lgkmcnt(1)\n"
        "ds_read_b128 v[8:11], v0 offset:0\n"
        "v_mfma_f32_16x16x32_bf16 v[48:51], v[208:211], v[12:15], v[48:51]\n"
        "s_cmp_le_u32 s88, 3\n"
        "s_cselect_b32 s88, s73, s89\n"
        "s_cselect_b32 s73, s89, s73\n"
        "v_mfma_f32_16x16x32_bf16 v[52:55], v[212:215], v[12:15], v[52:55]\n"
        "buffer_store_dword v5, v3, s[40:43], s88 offen sc1\n"
        "v_mfma_f32_16x16x32_bf16 v[56:59], v[216:219], v[12:15], v[56:59]\n"
        "s_getreg_b32 s88, hwreg(HW_REG_IB_STS, 0, 4)\n"
        "v_mfma_f32_16x16x32_bf16 v[60:63], v[220:223], v[12:15], v[60:63]\n"
        "s_cmp_le_u32 s88, 15\n"
        "s_cselect_b32 s88, s73, s89\n"
        "s_cselect_b32 s73, s89, s73\n"
        "v_mfma_f32_16x16x32_bf16 v[64:67], v[224:227], v[12:15], v[64:67]\n"
        "buffer_store_dword v5, v3, s[40:43], s88 offen sc1\n"
        "s_add_u32 s74, s74, 1\n"
        "v_mfma_f32_16x16x32_bf16 v[68:71], v[228:231], v[12:15], v[68:71]\n"
        "s_waitcnt lgkmcnt(1)\n"
        "ds_read_b128 v[12:15], v0 offset:512\n"
        "s_mov_b32 s84, s73\n"
        "v_mfma_f32_16x16x32_bf16 v[48:51], v[232:235], v[16:19], v[48:51]\n"
        "s_mov_b32 s73, s60\n"
        "s_add_u32 s72, s75, s59\n"
        "s_add_u32 s83, s75, s63\n"
        "v_mfma_f32_16x16x32_bf16 v[52:55], v[236:239], v[16:19], v[52:55]\n"
        "s_add_u32 s72, s72, 49152\n"
        "s_add_u32 s83, s83, 4096\n"
        "v_mfma_f32_16x16x32_bf16 v[56:59], v[240:243], v[16:19], v[56:59]\n"
        "v_mfma_f32_16x16x32_bf16 v[60:63], v[244:247], v[16:19], v[60:63]\n"
        "v_mfma_f32_16x16x32_bf16 v[64:67], v[248:251], v[16:19], v[64:67]\n"
        "v_mfma_f32_16x16x32_bf16 v[68:71], v[252:255], v[16:19], v[68:71]\n"
        "s_waitcnt lgkmcnt(1)\n"
        "ds_read_b128 v[16:19], v0 offset:1024\n"
        "s_branch .Lf1_i0_%=\n"
        ".Lf1_i11_%=:\n"
        "v_mfma_f32_16x16x32_bf16 v[60:63], a[252:255], v[12:15], v[60:63]\n"
        "v_mul_f32 v41, v41, v73\n"
        "buffer_load_dword v4, v2, s[40:43], s86 offen sc1\n"
        "v_mfma_f32_16x16x32_bf16 v[64:67], v[80:83], v[12:15], v[64:67]\n"
        "v_mul_f32 v42, v42, v74\n"
        "v_mul_f32 v43, v43, v75\n"
        "v_mfma_f32_16x16x32_bf16 v[68:71], v[84:87], v[12:15], v[68:71]\n"
        "s_waitcnt lgkmcnt(1)\n"
        "ds_read_b128 v[12:15], v0 offset:6656\n"
        "v_cvt_pk_bf16_f32 v44, v40, v41\n"
        "v_mfma_f32_16x16x32_bf16 v[48:51], v[88:91], v[16:19], v[48:51]\n"
        "v_cvt_pk_bf16_f32 v45, v42, v43\n"
        "s_nop 0\n"
        "v_mfma_f32_16x16x32_bf16 v[52:55], v[92:95], v[16:19], v[52:55]\n"
        "v_permlane16_swap_b32 v44, v46\n"
        "v_mfma_f32_16x16x32_bf16 v[56:59], v[96:99], v[16:19], v[56:59]\n"
        "v_permlane16_swap_b32 v45, v47\n"
        "v_mfma_f32_16x16x32_bf16 v[60:63], v[100:103], v[16:19], v[60:63]\n"
        "buffer_store_dwordx4 v[44:47], v7, s[40:43], s83 offen offset:0 sc1\n"
        "v_mfma_f32_16x16x32_bf16 v[64:67], v[104:107], v[16:19], v[64:67]\n"
        "v_mov_b32 v5, s74\n"
        "v_mfma_f32_16x16x32_bf16 v[68:71], v[108:111], v[16:19], v[68:71]\n"
        "s_waitcnt lgkmcnt(1)\n"
        "ds_read_b128 v[16:19], v0 offset:7168\n"
        "v_mfma_f32_16x16x32_bf16 v[48:51], v[112:115], v[8:11], v[48:51]\n"
        "v_mfma_f32_16x16x32_bf16 v[52:55], v[116:119], v[8:11], v[52:55]\n"
        "v_mfma_f32_16x16x32_bf16 v[56:59], v[120:123], v[8:11], v[56:59]\n"
        "v_mfma_f32_16x16x32_bf16 v[60:63], v[124:127], v[8:11], v[60:63]\n"
        "v_mfma_f32_16x16x32_bf16 v[64:67], v[128:131], v[8:11], v[64:67]\n"
        "v_mfma_f32_16x16x32_bf16 v[68:71], v[132:135], v[8:11], v[68:71]\n"
        "s_waitcnt lgkmcnt(1)\n"
        "ds_read_b128 v[8:11], v0 offset:7680\n"
        "v_mfma_f32_16x16x32_bf16 v[48:51], v[136:139], v[12:15], v[48:51]\n"
        "v_mfma_f32_16x16x32_bf16 v[52:55], v[140:143], v[12:15], v[52:55]\n"
        "v_mfma_f32_16x16x32_bf16 v[56:59], v[144:147], v[12:15], v[56:59]\n"
        "v_mfma_f32_16x16x32_bf16 v[60:63], v[148:151], v[12:15], v[60:63]\n"
        "v_mfma_f32_16x16x32_bf16 v[64:67], v[152:155], v[12:15], v[64:67]\n"
        "v_mfma_f32_16x16x32_bf16 v[68:71], v[156:159], v[12:15], v[68:71]\n"
        "s_waitcnt lgkmcnt(1)\n"
        "ds_read_b128 v[12:15], v0 offset:8192\n"
        "v_mfma_f32_16x16x32_bf16 v[48:51], v[160:163], v[16:19], v[48:51]\n"
        "v_mfma_f32_16x16x32_bf16 v[52:55], v[164:167], v[16:19], v[52:55]\n"
        "v_mfma_f32_16x16x32_bf16 v[56:59], v[168:171], v[16:19], v[56:59]\n"
        "v_mfma_f32_16x16x32_bf16 v[60:63], v[172:175], v[16:19], v[60:63]\n"
        "v_mfma_f32_16x16x32_bf16 v[64:67], v[176:179], v[16:19], v[64:67]\n"
        "v_mfma_f32_16x16x32_bf16 v[68:71], v[180:183], v[16:19], v[68:71]\n"
        "s_waitcnt lgkmcnt(1)\n"
        "ds_read_b128 v[16:19], v0 offset:8704\n"
        "v_mfma_f32_16x16x32_bf16 v[48:51], v[184:187], v[8:11], v[48:51]\n"
        "s_waitcnt vmcnt(1)\n"
        "v_cmp_gt_i32 vcc, s70, v4\n"
        "v_mfma_f32_16x16x32_bf16 v[52:55], v[188:191], v[8:11], v[52:55]\n"
        "s_getreg_b32 s88, hwreg(HW_REG_IB_STS, 0, 4)\n"
        "v_mfma_f32_16x16x32_bf16 v[56:59], v[192:195], v[8:11], v[56:59]\n"
        "s_and_b64 vcc, vcc, 1\n"
        "v_mfma_f32_16x16x32_bf16 v[60:63], v[196:199], v[8:11], v[60:63]\n"
        "s_cbranch_vccnz .Lf1_i12_%=\n"
        "v_mfma_f32_16x16x32_bf16 v[64:67], v[200:203], v[8:11], v[64:67]\n"
        "buffer_load_dwordx4 v1, s[40:43], s71 offen offset:0 sc1 lds\n"
        "v_mfma_f32_16x16x32_bf16 v[68:71], v[204:207], v[8:11], v[68:71]\n"
        "s_waitcnt lgkmcnt(1)\n"
        "v_mfma_f32_16x16x32_bf16 v[48:51], v[208:211], v[12:15], v[48:51]\n"
        "s_cmp_le_u32 s88, 0\n"
        "s_cselect_b32 s88, s73, s89\n"
        "s_cselect_b32 s73, s89, s73\n"
        "buffer_load_dwordx4 v1, s[40:43], s71 offen offset:1024 sc1 lds\n"
        "v_mfma_f32_16x16x32_bf16 v[52:55], v[212:215], v[12:15], v[52:55]\n"
        "buffer_store_dword v5, v3, s[40:43], s88 offen sc1\n"
        "v_mfma_f32_16x16x32_bf16 v[56:59], v[216:219], v[12:15], v[56:59]\n"
        "s_getreg_b32 s88, hwreg(HW_REG_IB_STS, 0, 4)\n"
        "buffer_load_dwordx4 v1, s[40:43], s71 offen offset:2048 sc1 lds\n"
        "v_mfma_f32_16x16x32_bf16 v[60:63], v[220:223], v[12:15], v[60:63]\n"
        "s_cmp_le_u32 s88, 3\n"
        "s_cselect_b32 s88, s73, s89\n"
        "s_cselect_b32 s73, s89, s73\n"
        "v_mfma_f32_16x16x32_bf16 v[64:67], v[224:227], v[12:15], v[64:67]\n"
        "buffer_store_dword v5, v3, s[40:43], s88 offen sc1\n"
        "s_add_u32 s74, s74, 1\n"
        "buffer_load_dwordx4 v1, s[40:43], s71 offen offset:3072 sc1 lds\n"
        "s_add_u32 m0, s78, s85\n"
        "s_add_u32 s69, s69, 1\n"
        "v_mfma_f32_16x16x32_bf16 v[68:71], v[228:231], v[12:15], v[68:71]\n"
        "s_waitcnt lgkmcnt(0)\n"
        "s_mov_b32 s84, s73\n"
        "v_mfma_f32_16x16x32_bf16 v[48:51], v[232:235], v[16:19], v[48:51]\n"
        "s_mov_b32 s73, s60\n"
        "s_add_u32 s72, s75, s59\n"
        "s_add_u32 s83, s75, s63\n"
        "buffer_load_dwordx4 v1, s[40:43], s81 offen offset:0 sc1 lds\n"
        "v_mfma_f32_16x16x32_bf16 v[52:55], v[236:239], v[16:19], v[52:55]\n"
        "s_add_u32 s72, s72, 49152\n"
        "s_add_u32 s83, s83, 4096\n"
        "v_mfma_f32_16x16x32_bf16 v[56:59], v[240:243], v[16:19], v[56:59]\n"
        "v_mfma_f32_16x16x32_bf16 v[60:63], v[244:247], v[16:19], v[60:63]\n"
        "v_mfma_f32_16x16x32_bf16 v[64:67], v[248:251], v[16:19], v[64:67]\n"
        "v_mfma_f32_16x16x32_bf16 v[68:71], v[252:255], v[16:19], v[68:71]\n"
        "v_mul_f32 v72, s80, v48\n"
        "v_mul_f32 v73, s80, v49\n"
        "v_mul_f32 v74, s80, v50\n"
        "v_mul_f32 v75, s80, v51\n"
        "v_exp_f32 v72, v72\n"
        "v_exp_f32 v73, v73\n"
        "v_exp_f32 v74, v74\n"
        "v_exp_f32 v75, v75\n"
        "v_mul_f32 v48, v48, v52\n"
        "v_mul_f32 v49, v49, v53\n"
        "v_mul_f32 v50, v50, v54\n"
        "v_mul_f32 v51, v51, v55\n"
        "v_add_f32 v72, 1.0, v72\n"
        "v_add_f32 v73, 1.0, v73\n"
        "v_add_f32 v74, 1.0, v74\n"
        "v_add_f32 v75, 1.0, v75\n"
        "v_rcp_f32 v72, v72\n"
        "v_rcp_f32 v73, v73\n"
        "v_rcp_f32 v74, v74\n"
        "v_rcp_f32 v75, v75\n"
        "v_mul_f32 v48, v48, v72\n"
        "v_mul_f32 v49, v49, v73\n"
        "v_mul_f32 v50, v50, v74\n"
        "v_mul_f32 v51, v51, v75\n"
        "v_cvt_pk_bf16_f32 v52, v48, v49\n"
        "v_cvt_pk_bf16_f32 v53, v50, v51\n"
        "s_waitcnt vmcnt(7)\n"
        "buffer_store_dword v5, v3, s[40:43], s84 offen sc1\n"
        "s_mov_b32 s84, s89\n"
        "v_mul_f32 v76, s80, v56\n"
        "v_mul_f32 v77, s80, v57\n"
        "v_mul_f32 v78, s80, v58\n"
        "v_mul_f32 v79, s80, v59\n"
        "v_exp_f32 v76, v76\n"
        "v_exp_f32 v77, v77\n"
        "v_exp_f32 v78, v78\n"
        "v_exp_f32 v79, v79\n"
        "v_mul_f32 v56, v56, v60\n"
        "v_mul_f32 v57, v57, v61\n"
        "v_mul_f32 v58, v58, v62\n"
        "v_mul_f32 v59, v59, v63\n"
        "v_add_f32 v76, 1.0, v76\n"
        "v_add_f32 v77, 1.0, v77\n"
        "v_add_f32 v78, 1.0, v78\n"
        "v_add_f32 v79, 1.0, v79\n"
        "v_rcp_f32 v76, v76\n"
        "v_rcp_f32 v77, v77\n"
        "v_rcp_f32 v78, v78\n"
        "v_rcp_f32 v79, v79\n"
        "v_mul_f32 v56, v56, v76\n"
        "v_mul_f32 v57, v57, v77\n"
        "v_mul_f32 v58, v58, v78\n"
        "v_mul_f32 v59, v59, v79\n"
        "v_cvt_pk_bf16_f32 v54, v56, v57\n"
        "v_cvt_pk_bf16_f32 v55, v58, v59\n"
        "buffer_store_dwordx4 v[52:55], v1, s[40:43], s72 offen offset:0 sc1\n"
        "v_mul_f32 v72, s80, v64\n"
        "v_mul_f32 v73, s80, v65\n"
        "v_mul_f32 v74, s80, v66\n"
        "v_mul_f32 v75, s80, v67\n"
        "v_exp_f32 v72, v72\n"
        "v_exp_f32 v73, v73\n"
        "v_exp_f32 v74, v74\n"
        "v_exp_f32 v75, v75\n"
        "v_mul_f32 v64, v64, v68\n"
        "v_mul_f32 v65, v65, v69\n"
        "v_mul_f32 v66, v66, v70\n"
        "v_mul_f32 v67, v67, v71\n"
        "v_add_f32 v72, 1.0, v72\n"
        "v_add_f32 v73, 1.0, v73\n"
        "v_add_f32 v74, 1.0, v74\n"
        "v_add_f32 v75, 1.0, v75\n"
        "v_rcp_f32 v72, v72\n"
        "v_rcp_f32 v73, v73\n"
        "v_rcp_f32 v74, v74\n"
        "v_rcp_f32 v75, v75\n"
        "v_mul_f32 v64, v64, v72\n"
        "v_mul_f32 v65, v65, v73\n"
        "v_mul_f32 v66, v66, v74\n"
        "v_mul_f32 v67, v67, v75\n"
        "v_cvt_pk_bf16_f32 v68, v64, v65\n"
        "v_cvt_pk_bf16_f32 v69, v66, v67\n"
        "s_nop 0\n"
        "v_permlane16_swap_b32 v68, v70\n"
        "v_permlane16_swap_b32 v69, v71\n"
        "buffer_store_dwordx4 v[68:71], v7, s[40:43], s83 offen offset:0 sc1\n"
        "v_mov_b32 v5, s74\n"
        "s_mov_b32 s84, s60\n"
        "buffer_store_dword v5, v3, s[40:43], s89 offen sc1\n"
        "buffer_store_dword v5, v3, s[40:43], s89 offen sc1\n"
        "s_waitcnt vmcnt(5)\n"
        "s_branch .Lf1_go1_%=\n"
        ".Lf1_i12_%=:\n"
        "v_mfma_f32_16x16x32_bf16 v[64:67], v[200:203], v[8:11], v[64:67]\n"
        "buffer_load_dword v4, v2, s[40:43], s86 offen sc1\n"
        "v_mfma_f32_16x16x32_bf16 v[68:71], v[204:207], v[8:11], v[68:71]\n"
        "s_waitcnt lgkmcnt(1)\n"
        "v_mfma_f32_16x16x32_bf16 v[48:51], v[208:211], v[12:15], v[48:51]\n"
        "s_cmp_le_u32 s88, 0\n"
        "s_cselect_b32 s88, s73, s89\n"
        "s_cselect_b32 s73, s89, s73\n"
        "v_mfma_f32_16x16x32_bf16 v[52:55], v[212:215], v[12:15], v[52:55]\n"
        "buffer_store_dword v5, v3, s[40:43], s88 offen sc1\n"
        "v_mfma_f32_16x16x32_bf16 v[56:59], v[216:219], v[12:15], v[56:59]\n"
        "s_getreg_b32 s88, hwreg(HW_REG_IB_STS, 0, 4)\n"
        "v_mfma_f32_16x16x32_bf16 v[60:63], v[220:223], v[12:15], v[60:63]\n"
        "s_cmp_le_u32 s88, 2\n"
        "s_cselect_b32 s88, s73, s89\n"
        "s_cselect_b32 s73, s89, s73\n"
        "v_mfma_f32_16x16x32_bf16 v[64:67], v[224:227], v[12:15], v[64:67]\n"
        "buffer_store_dword v5, v3, s[40:43], s88 offen sc1\n"
        "s_add_u32 s74, s74, 1\n"
        "v_mfma_f32_16x16x32_bf16 v[68:71], v[228:231], v[12:15], v[68:71]\n"
        "s_waitcnt lgkmcnt(0)\n"
        "s_mov_b32 s84, s73\n"
        "v_mfma_f32_16x16x32_bf16 v[48:51], v[232:235], v[16:19], v[48:51]\n"
        "s_mov_b32 s73, s60\n"
        "s_add_u32 s72, s75, s59\n"
        "s_add_u32 s83, s75, s63\n"
        "v_mfma_f32_16x16x32_bf16 v[52:55], v[236:239], v[16:19], v[52:55]\n"
        "s_add_u32 s72, s72, 49152\n"
        "s_add_u32 s83, s83, 4096\n"
        "v_mfma_f32_16x16x32_bf16 v[56:59], v[240:243], v[16:19], v[56:59]\n"
        "v_mfma_f32_16x16x32_bf16 v[60:63], v[244:247], v[16:19], v[60:63]\n"
        "v_mfma_f32_16x16x32_bf16 v[64:67], v[248:251], v[16:19], v[64:67]\n"
        "v_mfma_f32_16x16x32_bf16 v[68:71], v[252:255], v[16:19], v[68:71]\n"
        "v_mul_f32 v72, s80, v48\n"
        "v_mul_f32 v73, s80, v49\n"
        "v_mul_f32 v74, s80, v50\n"
        "v_mul_f32 v75, s80, v51\n"
        "v_exp_f32 v72, v72\n"
        "v_exp_f32 v73, v73\n"
        "v_exp_f32 v74, v74\n"
        "v_exp_f32 v75, v75\n"
        "v_mul_f32 v48, v48, v52\n"
        "v_mul_f32 v49, v49, v53\n"
        "v_mul_f32 v50, v50, v54\n"
        "v_mul_f32 v51, v51, v55\n"
        "v_add_f32 v72, 1.0, v72\n"
        "v_add_f32 v73, 1.0, v73\n"
        "v_add_f32 v74, 1.0, v74\n"
        "v_add_f32 v75, 1.0, v75\n"
        "v_rcp_f32 v72, v72\n"
        "v_rcp_f32 v73, v73\n"
        "v_rcp_f32 v74, v74\n"
        "v_rcp_f32 v75, v75\n"
        "v_mul_f32 v48, v48, v72\n"
        "v_mul_f32 v49, v49, v73\n"
        "v_mul_f32 v50, v50, v74\n"
        "v_mul_f32 v51, v51, v75\n"
        "v_cvt_pk_bf16_f32 v52, v48, v49\n"
        "v_cvt_pk_bf16_f32 v53, v50, v51\n"
        "s_waitcnt vmcnt(3)\n"
        "buffer_store_dword v5, v3, s[40:43], s84 offen sc1\n"
        "s_mov_b32 s84, s89\n"
        "v_mul_f32 v76, s80, v56\n"
        "v_mul_f32 v77, s80, v57\n"
        "v_mul_f32 v78, s80, v58\n"
        "v_mul_f32 v79, s80, v59\n"
        "v_exp_f32 v76, v76\n"
        "v_exp_f32 v77, v77\n"
        "v_exp_f32 v78, v78\n"
        "v_exp_f32 v79, v79\n"
        "v_mul_f32 v56, v56, v60\n"
        "v_mul_f32 v57, v57, v61\n"
        "v_mul_f32 v58, v58, v62\n"
        "v_mul_f32 v59, v59, v63\n"
        "v_add_f32 v76, 1.0, v76\n"
        "v_add_f32 v77, 1.0, v77\n"
        "v_add_f32 v78, 1.0, v78\n"
        "v_add_f32 v79, 1.0, v79\n"
        "v_rcp_f32 v76, v76\n"
        "v_rcp_f32 v77, v77\n"
        "v_rcp_f32 v78, v78\n"
        "v_rcp_f32 v79, v79\n"
        "v_mul_f32 v56, v56, v76\n"
        "v_mul_f32 v57, v57, v77\n"
        "v_mul_f32 v58, v58, v78\n"
        "v_mul_f32 v59, v59, v79\n"
        "v_cvt_pk_bf16_f32 v54, v56, v57\n"
        "v_cvt_pk_bf16_f32 v55, v58, v59\n"
        "buffer_store_dwordx4 v[52:55], v1, s[40:43], s72 offen offset:0 sc1\n"
        "v_mul_f32 v72, s80, v64\n"
        "v_mul_f32 v73, s80, v65\n"
        "v_mul_f32 v74, s80, v66\n"
        "v_mul_f32 v75, s80, v67\n"
        "v_exp_f32 v72, v72\n"
        "v_exp_f32 v73, v73\n"
        "v_exp_f32 v74, v74\n"
        "v_exp_f32 v75, v75\n"
        "v_mul_f32 v64, v64, v68\n"
        "v_mul_f32 v65, v65, v69\n"
        "v_mul_f32 v66, v66, v70\n"
        "v_mul_f32 v67, v67, v71\n"
        "v_add_f32 v72, 1.0, v72\n"
        "v_add_f32 v73, 1.0, v73\n"
        "v_add_f32 v74, 1.0, v74\n"
        "v_add_f32 v75, 1.0, v75\n"
        "v_rcp_f32 v72, v72\n"
        "v_rcp_f32 v73, v73\n"
        "v_rcp_f32 v74, v74\n"
        "v_rcp_f32 v75, v75\n"
        "v_mul_f32 v64, v64, v72\n"
        "v_mul_f32 v65, v65, v73\n"
        "v_mul_f32 v66, v66, v74\n"
        "v_mul_f32 v67, v67, v75\n"
        "v_cvt_pk_bf16_f32 v68, v64, v65\n"
        "v_cvt_pk_bf16_f32 v69, v66, v67\n"
        "s_nop 0\n"
        "v_permlane16_swap_b32 v68, v70\n"
        "v_permlane16_swap_b32 v69, v71\n"
        "buffer_store_dwordx4 v[68:71], v7, s[40:43], s83 offen offset:0 sc1\n"
        "s_waitcnt vmcnt(5)\n"
        "v_cmp_gt_i32 vcc, s70, v4\n"
        "s_nop 0\n"
        "s_and_b64 vcc, vcc, 1\n"
        "s_cbranch_vccnz .Lf1_late1_%=\n"
        "buffer_load_dwordx4 v1, s[40:43], s71 offen offset:0 sc1 lds\n"
        "buffer_load_dwordx4 v1, s[40:43], s71 offen offset:1024 sc1 lds\n"
        "buffer_load_dwordx4 v1, s[40:43], s71 offen offset:2048 sc1 lds\n"
        "buffer_load_dwordx4 v1, s[40:43], s71 offen offset:3072 sc1 lds\n"
        "s_add_u32 m0, s78, s85\n"
        "s_add_u32 s69, s69, 1\n"
        "buffer_load_dwordx4 v1, s[40:43], s81 offen offset:0 sc1 lds\n"
        "s_waitcnt vmcnt(5)\n"
        "v_mov_b32 v5, s74\n"
        "buffer_store_dword v5, v3, s[40:43], s73 offen sc1\n"
        "s_waitcnt vmcnt(1)\n"
        "s_branch .Lf1_go1_%=\n"
        ".Lf1_late1_%=:\n"
        "s_waitcnt vmcnt(0)\n"
        "v_mov_b32 v5, s74\n"
        "buffer_store_dword v5, v3, s[40:43], s73 offen sc1\n"
        "s_cmp_eq_u32 s69, 128\n"
        "s_cbranch_scc0 .Lf1_wait1_%=\n"
        "s_endpgm\n"
        ".Lf1_wait1_%=:\n"
        "s_mov_b32 s64, 0\n"
        ".Lf1_in1_top_%=:\n"
        "buffer_load_dword v4, v2, s[40:43], s86 offen sc1\n"
        "s_waitcnt vmcnt(0)\n"
        "v_cmp_gt_i32 vcc, s70, v4\n"
        "s_nop 0\n"
        "s_and_b64 vcc, vcc, 1\n"
        "s_cbranch_vccz .Lf1_in1_ok_%=\n"
        "s_add_u32 s64, s64, 1\n"
        "s_and_b32 s65, s64, 255\n"
        "s_cbranch_scc1 .Lf1_in1_go_%=\n"
        "s_mov_b32 s65, 49152\n"
        "s_cmp_lt_u32 s64, 1048576\n"
        "s_cbranch_scc1 .Lf1_in1_chk_%=\n"
        "v_mov_b32 v4, 1\n"
        "buffer_store_dword v4, off, s[40:43], s65 sc1\n"
        "s_waitcnt vmcnt(0)\n"
        ".Lf1_in1_chk_%=:\n"
        "buffer_load_dword v4, off, s[40:43], s65 sc1\n"
        "s_waitcnt vmcnt(0)\n"
        "v_readfirstlane_b32 s65, v4\n"
        "s_nop 0\n"
        "s_cmp_lg_u32 s65, 1\n"
        "s_cbranch_scc1 .Lf1_in1_go_%=\n"
        "s_endpgm\n"
        ".Lf1_in1_go_%=:\n"
        "s_branch .Lf1_in1_top_%=\n"
        ".Lf1_in1_ok_%=:\n"
        "buffer_load_dwordx4 v1, s[40:43], s71 offen offset:0 sc1 lds\n"
        "buffer_load_dwordx4 v1, s[40:43], s71 offen offset:1024 sc1 lds\n"
        "buffer_load_dwordx4 v1, s[40:43], s71 offen offset:2048 sc1 lds\n"
        "buffer_load_dwordx4 v1, s[40:43], s71 offen offset:3072 sc1 lds\n"
        "s_add_u32 m0, s78, s85\n"
        "s_add_u32 s69, s69, 1\n"
        "buffer_load_dwordx4 v1, s[40:43], s81 offen offset:0 sc1 lds\n"
        "s_waitcnt vmcnt(0)\n"
        ".Lf1_go1_%=:\n"
        "s_mov_b32 s72, 241172480\n"
        "s_mov_b32 s83, 241172480\n"
        "s_mov_b32 s73, 241172480\n"
        "v_add_u32 v0, s78, v6\n"
        "s_barrier\n"
        "ds_read_b128 v[8:11], v0 offset:0\n"
        "ds_read_b128 v[12:15], v0 offset:512\n"
        "ds_read_b128 v[16:19], v0 offset:1024\n"
        "s_waitcnt lgkmcnt(2)\n"
        "s_branch .Lf1_i0_%=\n"
        :: "s"(p.ws), "s"(p.w1), "s"(p.w3), "s"(p.w3), "s"(pair(w, delay)), "s"(pair(x4, pr)), TILE_ARGS(65 * PAGE + spread(B_XN1)),
           "v"(pair(lane == 0 ? flag(F_R3 + 8 * (6 * kap + jj) + w) : OOB, lane < 3 ? lane * 32768 : OOB)), "v"(pair(0, 0)), "v"(pair(0, (g & 1) ? OOB : (g >> 1) * 256 + i * 16)),
           "v"(pair(vo[0], vo[1])), "v"(pair(vo[2], w * 40960 + (4 * g + q) * 96 + 8 * pp))
        : "v0", "v1", "v2", "v3", "v4", "v5", "v6", "v7", "v8", "v9", "v10", "v11", "v12", "v13", "v14", "v15", "v16", "v17", "v18", "v19", "v20", "v21", "v22", "v23", "v24", "v25", "v26", "v27", "v28", "v29", "v30", "v31", "v32", "v33", "v34", "v35", "v36", "v37", "v38", "v39", "v40", "v41", "v42", "v43", "v44", "v45", "v46", "v47", "v48", "v49", "v50", "v51", "v52", "v53", "v54", "v55", "v56", "v57", "v58", "v59", "v60", "v61", "v62", "v63", "v64", "v65", "v66", "v67", "v68", "v69", "v70", "v71", "v72", "v73", "v74", "v75", "v76", "v77", "v78", "v79", "v255", "a255", "s30", "s31", "s32", "s33", "s34", "s35", "s36", "s37", "s38", "s39", "s40", "s41", "s42", "s43", "s44", "s45", "s46", "s47", "s48", "s49", "s50", "s51", "s52", "s53", "s54", "s55", "s56", "s57", "s58", "s59", "s60", "s61", "s62", "s63", "s64", "s65", "s66", "s67", "s68", "s69", "s70", "s71", "s72", "s73", "s74", "s75", "s76", "s77", "s78", "s79", "s80", "s81", "s82", "s83", "s84", "s85", "s86", "s87", "s88", "s89", "s90", "s91", "s92", "s93", "s94", "s95", "s96", "s97", "s98", "s99", "vcc", "scc", "memory");
    __builtin_unreachable();
}
__device__ __forceinline__ unsigned part_off(unsigned a, unsigned kap, unsigned chunk) {
    const unsigned b = (a & 1) * 4 + kap;
    return (a >> 1) * (9 * 16384) + (chunk < 64 ? b * 16384 + chunk * 128 : 8 * 16384 + b * 1024 + (chunk - 64) * 128);
}
template <bool OP>
__device__ __forceinline__ void role_k(const Params& p, int kap, int nu, int x4, int pr, int delay) {
    GEMM_HEAD;
    unsigned vo[3], vt[3];
#pragma unroll
    for (int n = 0; n < 3; ++n) {
        const unsigned ps = (64 * n + lane) / 6, gg = (ps >> 2) & 3;
        const unsigned col = (192 * nu + 48 * w) * 2 + ((64 * n + lane) % 6) * 16 - 1024 * n;
        vo[n] = ((OP ? 1024 : 1152) * kap + ps) * 1152 + col;
        vt[n] = (1152 * kap + 48 * (gg >> 1) + 32 + 8 * (gg & 1) + 4 * (ps >> 4) + (ps & 3)) * 1152 + col;
    }
    const unsigned tr = w * 40960 + (4 * g + q) * 96;
    const unsigned c0 = 24 * nu + 6 * w;
    const unsigned s01 = part_off(i >> 3, kap, c0 + g) + (i & 7) * 16, s2 = (g & 1) ? OOB : part_off(i >> 3, kap, c0 + 4 + (g >> 1)) + (i & 7) * 16;
    const unsigned fl = nu * 16 + kap * 4 + w, fo4 = lane < 4 ? lane * 16384 : OOB;
#define K_ARGS "v"(pair(0, 0)), "v"(pair(s01, s2)), "v"(pair(vo[0], vo[1])), "v"(pair(vo[2], tr + 16 * pp)), "v"(pair(vt[0], vt[1])), "v"(pair(vt[2], tr + 64 + 8 * pp))
    if constexpr (OP) {
        asm volatile(
        "s_nop 4\n"
        "s_mov_b64 s[40:41], %0\n"
        "s_and_b32 s41, s41, 0xffff\n"
        "s_mov_b32 s42, 236978176\n"
        "s_mov_b32 s43, 0x20000\n"
        "s_mov_b64 s[48:49], %4\n"
        "s_mov_b64 s[56:57], %5\n"
        "s_mov_b64 s[58:59], %6\n"
        "s_mov_b64 s[60:61], %7\n"
        "s_mov_b64 s[62:63], %8\n"
        "v_mov_b64 v[0:1], %9\n"
        "v_mov_b64 v[2:3], %10\n"
        "v_mov_b64 v[4:5], %11\n"
        "v_mov_b64 v[6:7], %12\n"
        "v_mov_b64 v[8:9], %13\n"
        "v_mov_b64 v[10:11], %14\n"
        "v_mov_b64 v[12:13], %15\n"
        "v_mov_b64 v[14:15], %16\n"
        "s_mov_b32 s47, 0x20000\n"
        "s_mul_i32 s79, s48, 40960\n"
        ".Lop_start_%=:\n"
        "s_sub_u32 s49, s49, 1\n"
        "s_cbranch_scc1 .Lop_start2_%=\n"
        "s_sleep 16\n"
        "s_branch .Lop_start_%=\n"
        ".Lop_start2_%=:\n"
        "s_mov_b64 s[44:45], %1\n"
        "s_and_b32 s45, s45, 0xffff\n"
        "s_mov_b32 s46, 4718592\n"
        "s_add_u32 m0, s79, 0\n"
        "s_mov_b32 s65, 0\n"
        "buffer_load_dwordx4 v8, s[44:47], s65 offen lds\n"
        "buffer_load_dwordx4 v9, s[44:47], s65 offen offset:1024 lds\n"
        "buffer_load_dwordx4 v10, s[44:47], s65 offen offset:2048 lds\n"
        "s_add_u32 m0, s79, 3072\n"
        "s_mov_b32 s65, 36864\n"
        "buffer_load_dwordx4 v8, s[44:47], s65 offen lds\n"
        "buffer_load_dwordx4 v9, s[44:47], s65 offen offset:1024 lds\n"
        "buffer_load_dwordx4 v10, s[44:47], s65 offen offset:2048 lds\n"
        "s_add_u32 m0, s79, 6144\n"
        "s_mov_b32 s65, 73728\n"
        "buffer_load_dwordx4 v8, s[44:47], s65 offen lds\n"
        "buffer_load_dwordx4 v9, s[44:47], s65 offen offset:1024 lds\n"
        "buffer_load_dwordx4 v10, s[44:47], s65 offen offset:2048 lds\n"
        "s_add_u32 m0, s79, 9216\n"
        "s_mov_b32 s65, 110592\n"
        "buffer_load_dwordx4 v8, s[44:47], s65 offen lds\n"
        "buffer_load_dwordx4 v9, s[44:47], s65 offen offset:1024 lds\n"
        "buffer_load_dwordx4 v10, s[44:47], s65 offen offset:2048 lds\n"
        "s_add_u32 m0, s79, 12288\n"
        "s_mov_b32 s65, 147456\n"
        "buffer_load_dwordx4 v8, s[44:47], s65 offen lds\n"
        "buffer_load_dwordx4 v9, s[44:47], s65 offen offset:1024 lds\n"
        "buffer_load_dwordx4 v10, s[44:47], s65 offen offset:2048 lds\n"
        "s_add_u32 m0, s79, 15360\n"
        "s_mov_b32 s65, 184320\n"
        "buffer_load_dwordx4 v8, s[44:47], s65 offen lds\n"
        "buffer_load_dwordx4 v9, s[44:47], s65 offen offset:1024 lds\n"
        "buffer_load_dwordx4 v10, s[44:47], s65 offen offset:2048 lds\n"
        "s_add_u32 m0, s79, 18432\n"
        "s_mov_b32 s65, 221184\n"
        "buffer_load_dwordx4 v8, s[44:47], s65 offen lds\n"
        "buffer_load_dwordx4 v9, s[44:47], s65 offen offset:1024 lds\n"
        "buffer_load_dwordx4 v10, s[44:47], s65 offen offset:2048 lds\n"
        "s_add_u32 m0, s79, 21504\n"
        "s_mov_b32 s65, 258048\n"
        "buffer_load_dwordx4 v8, s[44:47], s65 offen lds\n"
        "buffer_load_dwordx4 v9, s[44:47], s65 offen offset:1024 lds\n"
        "buffer_load_dwordx4 v10, s[44:47], s65 offen offset:2048 lds\n"
        "s_add_u32 m0, s79, 24576\n"
        "s_mov_b32 s65, 294912\n"
        "buffer_load_dwordx4 v8, s[44:47], s65 offen lds\n"
        "buffer_load_dwordx4 v9, s[44:47], s65 offen offset:1024 lds\n"
        "buffer_load_dwordx4 v10, s[44:47], s65 offen offset:2048 lds\n"
        "s_add_u32 m0, s79, 27648\n"
        "s_mov_b32 s65, 331776\n"
        "buffer_load_dwordx4 v8, s[44:47], s65 offen lds\n"
        "buffer_load_dwordx4 v9, s[44:47], s65 offen offset:1024 lds\n"
        "buffer_load_dwordx4 v10, s[44:47], s65 offen offset:2048 lds\n"
        "s_add_u32 m0, s79, 30720\n"
        "s_mov_b32 s65, 368640\n"
        "buffer_load_dwordx4 v8, s[44:47], s65 offen lds\n"
        "buffer_load_dwordx4 v9, s[44:47], s65 offen offset:1024 lds\n"
        "buffer_load_dwordx4 v10, s[44:47], s65 offen offset:2048 lds\n"
        "s_add_u32 m0, s79, 33792\n"
        "s_mov_b32 s65, 405504\n"
        "buffer_load_dwordx4 v8, s[44:47], s65 offen lds\n"
        "buffer_load_dwordx4 v9, s[44:47], s65 offen offset:1024 lds\n"
        "buffer_load_dwordx4 v10, s[44:47], s65 offen offset:2048 lds\n"
        "s_waitcnt vmcnt(33)\n"
        "ds_read_b64_tr_b16 a[0:1], v11 offset:0\n"
        "ds_read_b64_tr_b16 a[2:3], v11 offset:1536\n"
        "ds_read_b64_tr_b16 a[4:5], v11 offset:8\n"
        "ds_read_b64_tr_b16 a[6:7], v11 offset:1544\n"
        "ds_read_b64_tr_b16 a[8:9], v15 offset:0\n"
        "ds_read_b64_tr_b16 a[10:11], v15 offset:1536\n"
        "s_waitcnt vmcnt(30)\n"
        "ds_read_b64_tr_b16 a[12:13], v11 offset:3072\n"
        "ds_read_b64_tr_b16 a[14:15], v11 offset:4608\n"
        "ds_read_b64_tr_b16 a[16:17], v11 offset:3080\n"
        "ds_read_b64_tr_b16 a[18:19], v11 offset:4616\n"
        "ds_read_b64_tr_b16 a[20:21], v15 offset:3072\n"
        "ds_read_b64_tr_b16 a[22:23], v15 offset:4608\n"
        "s_waitcnt lgkmcnt(6)\n"
        "s_add_u32 m0, s79, 0\n"
        "s_mov_b32 s65, 442368\n"
        "buffer_load_dwordx4 v8, s[44:47], s65 offen lds\n"
        "buffer_load_dwordx4 v9, s[44:47], s65 offen offset:1024 lds\n"
        "buffer_load_dwordx4 v10, s[44:47], s65 offen offset:2048 lds\n"
        "s_waitcnt vmcnt(30)\n"
        "ds_read_b64_tr_b16 a[24:25], v11 offset:6144\n"
        "ds_read_b64_tr_b16 a[26:27], v11 offset:7680\n"
        "ds_read_b64_tr_b16 a[28:29], v11 offset:6152\n"
        "ds_read_b64_tr_b16 a[30:31], v11 offset:7688\n"
        "ds_read_b64_tr_b16 a[32:33], v15 offset:6144\n"
        "ds_read_b64_tr_b16 a[34:35], v15 offset:7680\n"
        "s_waitcnt lgkmcnt(6)\n"
        "s_add_u32 m0, s79, 3072\n"
        "s_mov_b32 s65, 479232\n"
        "buffer_load_dwordx4 v8, s[44:47], s65 offen lds\n"
        "buffer_load_dwordx4 v9, s[44:47], s65 offen offset:1024 lds\n"
        "buffer_load_dwordx4 v10, s[44:47], s65 offen offset:2048 lds\n"
        "s_waitcnt vmcnt(30)\n"
        "ds_read_b64_tr_b16 a[36:37], v11 offset:9216\n"
        "ds_read_b64_tr_b16 a[38:39], v11 offset:10752\n"
        "ds_read_b64_tr_b16 a[40:41], v11 offset:9224\n"
        "ds_read_b64_tr_b16 a[42:43], v11 offset:10760\n"
        "ds_read_b64_tr_b16 a[44:45], v15 offset:9216\n"
        "ds_read_b64_tr_b16 a[46:47], v15 offset:10752\n"
        "s_waitcnt lgkmcnt(6)\n"
        "s_add_u32 m0, s79, 6144\n"
        "s_mov_b32 s65, 516096\n"
        "buffer_load_dwordx4 v8, s[44:47], s65 offen lds\n"
        "buffer_load_dwordx4 v9, s[44:47], s65 offen offset:1024 lds\n"
        "buffer_load_dwordx4 v10, s[44:47], s65 offen offset:2048 lds\n"
        "s_waitcnt vmcnt(30)\n"
        "ds_read_b64_tr_b16 a[48:49], v11 offset:12288\n"
        "ds_read_b64_tr_b16 a[50:51], v11 offset:13824\n"
        "ds_read_b64_tr_b16 a[52:53], v11 offset:12296\n"
        "ds_read_b64_tr_b16 a[54:55], v11 offset:13832\n"
        "ds_read_b64_tr_b16 a[56:57], v15 offset:12288\n"
        "ds_read_b64_tr_b16 a[58:59], v15 offset:13824\n"
        "s_waitcnt lgkmcnt(6)\n"
        "s_add_u32 m0, s79, 9216\n"
        "s_mov_b32 s65, 552960\n"
        "buffer_load_dwordx4 v8, s[44:47], s65 offen lds\n"
        "buffer_load_dwordx4 v9, s[44:47], s65 offen offset:1024 lds\n"
        "buffer_load_dwordx4 v10, s[44:47], s65 offen offset:2048 lds\n"
        "s_waitcnt vmcnt(30)\n"
        "ds_read_b64_tr_b16 a[60:61], v11 offset:15360\n"
        "ds_read_b64_tr_b16 a[62:63], v11 offset:16896\n"
        "ds_read_b64_tr_b16 a[64:65], v11 offset:15368\n"
        "ds_read_b64_tr_b16 a[66:67], v11 offset:16904\n"
        "ds_read_b64_tr_b16 a[68:69], v15 offset:15360\n"
        "ds_read_b64_tr_b16 a[70:71], v15 offset:16896\n"
        "s_waitcnt lgkmcnt(6)\n"
        "s_add_u32 m0, s79, 12288\n"
        "s_mov_b32 s65, 589824\n"
        "buffer_load_dwordx4 v8, s[44:47], s65 offen lds\n"
        "buffer_load_dwordx4 v9, s[44:47], s65 offen offset:1024 lds\n"
        "buffer_load_dwordx4 v10, s[44:47], s65 offen offset:2048 lds\n"
        "s_waitcnt vmcnt(30)\n"
        "ds_read_b64_tr_b16 a[72:73], v11 offset:18432\n"
        "ds_read_b64_tr_b16 a[74:75], v11 offset:19968\n"
        "ds_read_b64_tr_b16 a[76:77], v11 offset:18440\n"
        "ds_read_b64_tr_b16 a[78:79], v11 offset:19976\n"
        "ds_read_b64_tr_b16 a[80:81], v15 offset:18432\n"
        "ds_read_b64_tr_b16 a[82:83], v15 offset:19968\n"
        "s_waitcnt lgkmcnt(6)\n"
        "s_add_u32 m0, s79, 15360\n"
        "s_mov_b32 s65, 626688\n"
        "buffer_load_dwordx4 v8, s[44:47], s65 offen lds\n"
        "buffer_load_dwordx4 v9, s[44:47], s65 offen offset:1024 lds\n"
        "buffer_load_dwordx4 v10, s[44:47], s65 offen offset:2048 lds\n"
        "s_waitcnt vmcnt(30)\n"
        "ds_read_b64_tr_b16 a[84:85], v11 offset:21504\n"
        "ds_read_b64_tr_b16 a[86:87], v11 offset:23040\n"
        "ds_read_b64_tr_b16 a[88:89], v11 offset:21512\n"
        "ds_read_b64_tr_b16 a[90:91], v11 offset:23048\n"
        "ds_read_b64_tr_b16 a[92:93], v15 offset:21504\n"
        "ds_read_b64_tr_b16 a[94:95], v15 offset:23040\n"
        "s_waitcnt lgkmcnt(6)\n"
        "s_add_u32 m0, s79, 18432\n"
        "s_mov_b32 s65, 663552\n"
        "buffer_load_dwordx4 v8, s[44:47], s65 offen lds\n"
        "buffer_load_dwordx4 v9, s[44:47], s65 offen offset:1024 lds\n"
        "buffer_load_dwordx4 v10, s[44:47], s65 offen offset:2048 lds\n"
        "s_waitcnt vmcnt(30)\n"
        "ds_read_b64_tr_b16 a[96:97], v11 offset:24576\n"
        "ds_read_b64_tr_b16 a[98:99], v11 offset:26112\n"
        "ds_read_b64_tr_b16 a[100:101], v11 offset:24584\n"
        "ds_read_b64_tr_b16 a[102:103], v11 offset:26120\n"
        "ds_read_b64_tr_b16 a[104:105], v15 offset:24576\n"
        "ds_read_b64_tr_b16 a[106:107], v15 offset:26112\n"
        "s_waitcnt lgkmcnt(6)\n"
        "s_add_u32 m0, s79, 21504\n"
        "s_mov_b32 s65, 700416\n"
        "buffer_load_dwordx4 v8, s[44:47], s65 offen lds\n"
        "buffer_load_dwordx4 v9, s[44:47], s65 offen offset:1024 lds\n"
        "buffer_load_dwordx4 v10, s[44:47], s65 offen offset:2048 lds\n"
        "s_waitcnt vmcnt(30)\n"
        "ds_read_b64_tr_b16 a[108:109], v11 offset:27648\n"
        "ds_read_b64_tr_b16 a[110:111], v11 offset:29184\n"
        "ds_read_b64_tr_b16 a[112:113], v11 offset:27656\n"
        "ds_read_b64_tr_b16 a[114:115], v11 offset:29192\n"
        "ds_read_b64_tr_b16 a[116:117], v15 offset:27648\n"
        "ds_read_b64_tr_b16 a[118:119], v15 offset:29184\n"
        "s_waitcnt lgkmcnt(6)\n"
        "s_add_u32 m0, s79, 24576\n"
        "s_mov_b32 s65, 737280\n"
        "buffer_load_dwordx4 v8, s[44:47], s65 offen lds\n"
        "buffer_load_dwordx4 v9, s[44:47], s65 offen offset:1024 lds\n"
        "buffer_load_dwordx4 v10, s[44:47], s65 offen offset:2048 lds\n"
        "s_waitcnt vmcnt(30)\n"
        "ds_read_b64_tr_b16 a[120:121], v11 offset:30720\n"
        "ds_read_b64_tr_b16 a[122:123], v11 offset:32256\n"
        "ds_read_b64_tr_b16 a[124:125], v11 offset:30728\n"
        "ds_read_b64_tr_b16 a[126:127], v11 offset:32264\n"
        "ds_read_b64_tr_b16 a[128:129], v15 offset:30720\n"
        "ds_read_b64_tr_b16 a[130:131], v15 offset:32256\n"
        "s_waitcnt lgkmcnt(6)\n"
        "s_add_u32 m0, s79, 27648\n"
        "s_mov_b32 s65, 774144\n"
        "buffer_load_dwordx4 v8, s[44:47], s65 offen lds\n"
        "buffer_load_dwordx4 v9, s[44:47], s65 offen offset:1024 lds\n"
        "buffer_load_dwordx4 v10, s[44:47], s65 offen offset:2048 lds\n"
        "s_waitcnt vmcnt(30)\n"
        "ds_read_b64_tr_b16 a[132:133], v11 offset:33792\n"
        "ds_read_b64_tr_b16 a[134:135], v11 offset:35328\n"
        "ds_read_b64_tr_b16 a[136:137], v11 offset:33800\n"
        "ds_read_b64_tr_b16 a[138:139], v11 offset:35336\n"
        "ds_read_b64_tr_b16 a[140:141], v15 offset:33792\n"
        "ds_read_b64_tr_b16 a[142:143], v15 offset:35328\n"
        "s_waitcnt lgkmcnt(6)\n"
        "s_add_u32 m0, s79, 30720\n"
        "s_mov_b32 s65, 811008\n"
        "buffer_load_dwordx4 v8, s[44:47], s65 offen lds\n"
        "buffer_load_dwordx4 v9, s[44:47], s65 offen offset:1024 lds\n"
        "buffer_load_dwordx4 v10, s[44:47], s65 offen offset:2048 lds\n"
        "s_waitcnt vmcnt(30)\n"
        "ds_read_b64_tr_b16 a[144:145], v11 offset:0\n"
        "ds_read_b64_tr_b16 a[146:147], v11 offset:1536\n"
        "ds_read_b64_tr_b16 a[148:149], v11 offset:8\n"
        "ds_read_b64_tr_b16 a[150:151], v11 offset:1544\n"
        "ds_read_b64_tr_b16 a[152:153], v15 offset:0\n"
        "ds_read_b64_tr_b16 a[154:155], v15 offset:1536\n"
        "s_waitcnt lgkmcnt(6)\n"
        "s_add_u32 m0, s79, 33792\n"
        "s_mov_b32 s65, 847872\n"
        "buffer_load_dwordx4 v8, s[44:47], s65 offen lds\n"
        "buffer_load_dwordx4 v9, s[44:47], s65 offen offset:1024 lds\n"
        "buffer_load_dwordx4 v10, s[44:47], s65 offen offset:2048 lds\n"
        "s_waitcnt vmcnt(30)\n"
        "ds_read_b64_tr_b16 a[156:157], v11 offset:3072\n"
        "ds_read_b64_tr_b16 a[158:159], v11 offset:4608\n"
        "ds_read_b64_tr_b16 a[160:161], v11 offset:3080\n"
        "ds_read_b64_tr_b16 a[162:163], v11 offset:4616\n"
        "ds_read_b64_tr_b16 a[164:165], v15 offset:3072\n"
        "ds_read_b64_tr_b16 a[166:167], v15 offset:4608\n"
        "s_waitcnt lgkmcnt(6)\n"
        "s_add_u32 m0, s79, 0\n"
        "s_mov_b32 s65, 884736\n"
        "buffer_load_dwordx4 v8, s[44:47], s65 offen lds\n"
        "buffer_load_dwordx4 v9, s[44:47], s65 offen offset:1024 lds\n"
        "buffer_load_dwordx4 v10, s[44:47], s65 offen offset:2048 lds\n"
        "s_waitcnt vmcnt(30)\n"
        "ds_read_b64_tr_b16 a[168:169], v11 offset:6144\n"
        "ds_read_b64_tr_b16 a[170:171], v11 offset:7680\n"
        "ds_read_b64_tr_b16 a[172:173], v11 offset:6152\n"
        "ds_read_b64_tr_b16 a[174:175], v11 offset:7688\n"
        "ds_read_b64_tr_b16 a[176:177], v15 offset:6144\n"
        "ds_read_b64_tr_b16 a[178:179], v15 offset:7680\n"
        "s_waitcnt lgkmcnt(6)\n"
        "s_add_u32 m0, s79, 3072\n"
        "s_mov_b32 s65, 921600\n"
        "buffer_load_dwordx4 v8, s[44:47], s65 offen lds\n"
        "buffer_load_dwordx4 v9, s[44:47], s65 offen offset:1024 lds\n"
        "buffer_load_dwordx4 v10, s[44:47], s65 offen offset:2048 lds\n"
        "s_waitcnt vmcnt(30)\n"
        "ds_read_b64_tr_b16 a[180:181], v11 offset:9216\n"
        "ds_read_b64_tr_b16 a[182:183], v11 offset:10752\n"
        "ds_read_b64_tr_b16 a[184:185], v11 offset:9224\n"
        "ds_read_b64_tr_b16 a[186:187], v11 offset:10760\n"
        "ds_read_b64_tr_b16 a[188:189], v15 offset:9216\n"
        "ds_read_b64_tr_b16 a[190:191], v15 offset:10752\n"
        "s_waitcnt lgkmcnt(6)\n"
        "s_add_u32 m0, s79, 6144\n"
        "s_mov_b32 s65, 958464\n"
        "buffer_load_dwordx4 v8, s[44:47], s65 offen lds\n"
        "buffer_load_dwordx4 v9, s[44:47], s65 offen offset:1024 lds\n"
        "buffer_load_dwordx4 v10, s[44:47], s65 offen offset:2048 lds\n"
        "s_waitcnt vmcnt(30)\n"
        "ds_read_b64_tr_b16 a[192:193], v11 offset:12288\n"
        "ds_read_b64_tr_b16 a[194:195], v11 offset:13824\n"
        "ds_read_b64_tr_b16 a[196:197], v11 offset:12296\n"
        "ds_read_b64_tr_b16 a[198:199], v11 offset:13832\n"
        "ds_read_b64_tr_b16 a[200:201], v15 offset:12288\n"
        "ds_read_b64_tr_b16 a[202:203], v15 offset:13824\n"
        "s_waitcnt lgkmcnt(6)\n"
        "s_add_u32 m0, s79, 9216\n"
        "s_mov_b32 s65, 995328\n"
        "buffer_load_dwordx4 v8, s[44:47], s65 offen lds\n"
        "buffer_load_dwordx4 v9, s[44:47], s65 offen offset:1024 lds\n"
        "buffer_load_dwordx4 v10, s[44:47], s65 offen offset:2048 lds\n"
        "s_waitcnt vmcnt(30)\n"
        "ds_read_b64_tr_b16 a[204:205], v11 offset:15360\n"
        "ds_read_b64_tr_b16 a[206:207], v11 offset:16896\n"
        "ds_read_b64_tr_b16 a[208:209], v11 offset:15368\n"
        "ds_read_b64_tr_b16 a[210:211], v11 offset:16904\n"
        "ds_read_b64_tr_b16 a[212:213], v15 offset:15360\n"
        "ds_read_b64_tr_b16 a[214:215], v15 offset:16896\n"
        "s_waitcnt lgkmcnt(6)\n"
        "s_add_u32 m0, s79, 12288\n"
        "s_mov_b32 s65, 1032192\n"
        "buffer_load_dwordx4 v8, s[44:47], s65 offen lds\n"
        "buffer_load_dwordx4 v9, s[44:47], s65 offen offset:1024 lds\n"
        "buffer_load_dwordx4 v10, s[44:47], s65 offen offset:2048 lds\n"
        "s_waitcnt vmcnt(30)\n"
        "ds_read_b64_tr_b16 a[216:217], v11 offset:18432\n"
        "ds_read_b64_tr_b16 a[218:219], v11 offset:19968\n"
        "ds_read_b64_tr_b16 a[220:221], v11 offset:18440\n"
        "ds_read_b64_tr_b16 a[222:223], v11 offset:19976\n"
        "ds_read_b64_tr_b16 a[224:225], v15 offset:18432\n"
        "ds_read_b64_tr_b16 a[226:227], v15 offset:19968\n"
        "s_waitcnt lgkmcnt(6)\n"
        "s_add_u32 m0, s79, 15360\n"
        "s_mov_b32 s65, 1069056\n"
        "buffer_load_dwordx4 v8, s[44:47], s65 offen lds\n"
        "buffer_load_dwordx4 v9, s[44:47], s65 offen offset:1024 lds\n"
        "buffer_load_dwordx4 v10, s[44:47], s65 offen offset:2048 lds\n"
        "s_waitcnt vmcnt(30)\n"
        "ds_read_b64_tr_b16 a[228:229], v11 offset:21504\n"
        "ds_read_b64_tr_b16 a[230:231], v11 offset:23040\n"
        "ds_read_b64_tr_b16 a[232:233], v11 offset:21512\n"
        "ds_read_b64_tr_b16 a[234:235], v11 offset:23048\n"
        "ds_read_b64_tr_b16 a[236:237], v15 offset:21504\n"
        "ds_read_b64_tr_b16 a[238:239], v15 offset:23040\n"
        "s_waitcnt lgkmcnt(6)\n"
        "s_add_u32 m0, s79, 18432\n"
        "s_mov_b32 s65, 1105920\n"
        "buffer_load_dwordx4 v8, s[44:47], s65 offen lds\n"
        "buffer_load_dwordx4 v9, s[44:47], s65 offen offset:1024 lds\n"
        "buffer_load_dwordx4 v10, s[44:47], s65 offen offset:2048 lds\n"
        "s_waitcnt vmcnt(30)\n"
        "ds_read_b64_tr_b16 a[240:241], v11 offset:24576\n"
        "ds_read_b64_tr_b16 a[242:243], v11 offset:26112\n"
        "ds_read_b64_tr_b16 a[244:245], v11 offset:24584\n"
        "ds_read_b64_tr_b16 a[246:247], v11 offset:26120\n"
        "ds_read_b64_tr_b16 a[248:249], v15 offset:24576\n"
        "ds_read_b64_tr_b16 a[250:251], v15 offset:26112\n"
        "s_waitcnt lgkmcnt(6)\n"
        "s_add_u32 m0, s79, 21504\n"
        "s_mov_b32 s65, 1142784\n"
        "buffer_load_dwordx4 v8, s[44:47], s65 offen lds\n"
        "buffer_load_dwordx4 v9, s[44:47], s65 offen offset:1024 lds\n"
        "buffer_load_dwordx4 v10, s[44:47], s65 offen offset:2048 lds\n"
        "s_waitcnt vmcnt(30)\n"
        "ds_read_b64_tr_b16 a[252:253], v11 offset:27648\n"
        "ds_read_b64_tr_b16 a[254:255], v11 offset:29184\n"
        "ds_read_b64_tr_b16 v[80:81], v11 offset:27656\n"
        "ds_read_b64_tr_b16 v[82:83], v11 offset:29192\n"
        "ds_read_b64_tr_b16 v[84:85], v15 offset:27648\n"
        "ds_read_b64_tr_b16 v[86:87], v15 offset:29184\n"
        "s_waitcnt vmcnt(27)\n"
        "ds_read_b64_tr_b16 v[88:89], v11 offset:30720\n"
        "ds_read_b64_tr_b16 v[90:91], v11 offset:32256\n"
        "ds_read_b64_tr_b16 v[92:93], v11 offset:30728\n"
        "ds_read_b64_tr_b16 v[94:95], v11 offset:32264\n"
        "ds_read_b64_tr_b16 v[96:97], v15 offset:30720\n"
        "ds_read_b64_tr_b16 v[98:99], v15 offset:32256\n"
        "s_waitcnt vmcnt(24)\n"
        "ds_read_b64_tr_b16 v[100:101], v11 offset:33792\n"
        "ds_read_b64_tr_b16 v[102:103], v11 offset:35328\n"
        "ds_read_b64_tr_b16 v[104:105], v11 offset:33800\n"
        "ds_read_b64_tr_b16 v[106:107], v11 offset:35336\n"
        "ds_read_b64_tr_b16 v[108:109], v15 offset:33792\n"
        "ds_read_b64_tr_b16 v[110:111], v15 offset:35328\n"
        "s_waitcnt vmcnt(21)\n"
        "ds_read_b64_tr_b16 v[112:113], v11 offset:0\n"
        "ds_read_b64_tr_b16 v[114:115], v11 offset:1536\n"
        "ds_read_b64_tr_b16 v[116:117], v11 offset:8\n"
        "ds_read_b64_tr_b16 v[118:119], v11 offset:1544\n"
        "ds_read_b64_tr_b16 v[120:121], v15 offset:0\n"
        "ds_read_b64_tr_b16 v[122:123], v15 offset:1536\n"
        "s_waitcnt vmcnt(18)\n"
        "ds_read_b64_tr_b16 v[124:125], v11 offset:3072\n"
        "ds_read_b64_tr_b16 v[126:127], v11 offset:4608\n"
        "ds_read_b64_tr_b16 v[128:129], v11 offset:3080\n"
        "ds_read_b64_tr_b16 v[130:131], v11 offset:4616\n"
        "ds_read_b64_tr_b16 v[132:133], v15 offset:3072\n"
        "ds_read_b64_tr_b16 v[134:135], v15 offset:4608\n"
        "s_waitcnt vmcnt(15)\n"
        "ds_read_b64_tr_b16 v[136:137], v11 offset:6144\n"
        "ds_read_b64_tr_b16 v[138:139], v11 offset:7680\n"
        "ds_read_b64_tr_b16 v[140:141], v11 offset:6152\n"
        "ds_read_b64_tr_b16 v[142:143], v11 offset:7688\n"
        "ds_read_b64_tr_b16 v[144:145], v15 offset:6144\n"
        "ds_read_b64_tr_b16 v[146:147], v15 offset:7680\n"
        "s_waitcnt vmcnt(12)\n"
        "ds_read_b64_tr_b16 v[148:149], v11 offset:9216\n"
        "ds_read_b64_tr_b16 v[150:151], v11 offset:10752\n"
        "ds_read_b64_tr_b16 v[152:153], v11 offset:9224\n"
        "ds_read_b64_tr_b16 v[154:155], v11 offset:10760\n"
        "ds_read_b64_tr_b16 v[156:157], v15 offset:9216\n"
        "ds_read_b64_tr_b16 v[158:159], v15 offset:10752\n"
        "s_waitcnt vmcnt(9)\n"
        "ds_read_b64_tr_b16 v[160:161], v11 offset:12288\n"
        "ds_read_b64_tr_b16 v[162:163], v11 offset:13824\n"
        "ds_read_b64_tr_b16 v[164:165], v11 offset:12296\n"
        "ds_read_b64_tr_b16 v[166:167], v11 offset:13832\n"
        "ds_read_b64_tr_b16 v[168:169], v15 offset:12288\n"
        "ds_read_b64_tr_b16 v[170:171], v15 offset:13824\n"
        "s_waitcnt vmcnt(6)\n"
        "ds_read_b64_tr_b16 v[172:173], v11 offset:15360\n"
        "ds_read_b64_tr_b16 v[174:175], v11 offset:16896\n"
        "ds_read_b64_tr_b16 v[176:177], v11 offset:15368\n"
        "ds_read_b64_tr_b16 v[178:179], v11 offset:16904\n"
        "ds_read_b64_tr_b16 v[180:181], v15 offset:15360\n"
        "ds_read_b64_tr_b16 v[182:183], v15 offset:16896\n"
        "s_waitcnt vmcnt(3)\n"
        "ds_read_b64_tr_b16 v[184:185], v11 offset:18432\n"
        "ds_read_b64_tr_b16 v[186:187], v11 offset:19968\n"
        "ds_read_b64_tr_b16 v[188:189], v11 offset:18440\n"
        "ds_read_b64_tr_b16 v[190:191], v11 offset:19976\n"
        "ds_read_b64_tr_b16 v[192:193], v15 offset:18432\n"
        "ds_read_b64_tr_b16 v[194:195], v15 offset:19968\n"
        "s_waitcnt vmcnt(0)\n"
        "ds_read_b64_tr_b16 v[196:197], v11 offset:21504\n"
        "ds_read_b64_tr_b16 v[198:199], v11 offset:23040\n"
        "ds_read_b64_tr_b16 v[200:201], v11 offset:21512\n"
        "ds_read_b64_tr_b16 v[202:203], v11 offset:23048\n"
        "ds_read_b64_tr_b16 v[204:205], v15 offset:21504\n"
        "ds_read_b64_tr_b16 v[206:207], v15 offset:23040\n"
        "s_waitcnt lgkmcnt(0)\n"
        "v_lshrrev_b32 v20, 4, v1\n"
        "s_mov_b32 s50, s56\n"
        "v_lshlrev_b32 v22, 7, v20\n"
        "s_sub_u32 s66, 0, s50\n"
        "s_mov_b32 s52, 32768\n"
        "s_mov_b32 s64, 0\n"
        ".Lop_pg0_top_%=:\n"
        "buffer_load_dword v21, v22, s[40:43], s52 offen sc1\n"
        "s_waitcnt vmcnt(0)\n"
        "v_cmp_gt_i32 vcc, 0, v21\n"
        "s_cbranch_vccz .Lop_pg0_ok_%=\n"
        "s_add_u32 s64, s64, 1\n"
        "s_and_b32 s65, s64, 255\n"
        "s_cbranch_scc1 .Lop_pg0_go_%=\n"
        "s_mov_b32 s65, 49152\n"
        "s_cmp_lt_u32 s64, 1048576\n"
        "s_cbranch_scc1 .Lop_pg0_chk_%=\n"
        "v_mov_b32 v21, 1\n"
        "buffer_store_dword v21, off, s[40:43], s65 sc1\n"
        "s_waitcnt vmcnt(0)\n"
        ".Lop_pg0_chk_%=:\n"
        "buffer_load_dword v21, off, s[40:43], s65 sc1\n"
        "s_waitcnt vmcnt(0)\n"
        "v_readfirstlane_b32 s65, v21\n"
        "s_nop 0\n"
        "s_cmp_lg_u32 s65, 1\n"
        "s_cbranch_scc1 .Lop_pg0_go_%=\n"
        "s_endpgm\n"
        ".Lop_pg0_go_%=:\n"
        "s_sleep 2\n"
        "s_branch .Lop_pg0_top_%=\n"
        ".Lop_pg0_ok_%=:\n"
        "v_cmp_eq_u32 s[52:53], 1, v21\n"
        "s_nop 1\n"
        "s_mov_b32 s54, 40960\n"
        "s_mov_b32 s64, 0\n"
        ".Lop_pg1_top_%=:\n"
        "buffer_load_dword v21, v22, s[40:43], s54 offen sc1\n"
        "s_waitcnt vmcnt(0)\n"
        "v_cmp_gt_i32 vcc, 0, v21\n"
        "s_cbranch_vccz .Lop_pg1_ok_%=\n"
        "s_add_u32 s64, s64, 1\n"
        "s_and_b32 s65, s64, 255\n"
        "s_cbranch_scc1 .Lop_pg1_go_%=\n"
        "s_mov_b32 s65, 49152\n"
        "s_cmp_lt_u32 s64, 1048576\n"
        "s_cbranch_scc1 .Lop_pg1_chk_%=\n"
        "v_mov_b32 v21, 1\n"
        "buffer_store_dword v21, off, s[40:43], s65 sc1\n"
        "s_waitcnt vmcnt(0)\n"
        ".Lop_pg1_chk_%=:\n"
        "buffer_load_dword v21, off, s[40:43], s65 sc1\n"
        "s_waitcnt vmcnt(0)\n"
        "v_readfirstlane_b32 s65, v21\n"
        "s_nop 0\n"
        "s_cmp_lg_u32 s65, 1\n"
        "s_cbranch_scc1 .Lop_pg1_go_%=\n"
        "s_endpgm\n"
        ".Lop_pg1_go_%=:\n"
        "s_sleep 2\n"
        "s_branch .Lop_pg1_top_%=\n"
        ".Lop_pg1_ok_%=:\n"
        "v_cmp_eq_u32 s[54:55], 1, v21\n"
        "s_nop 1\n"
        "s_mov_b32 vcc_lo, 0x55555555\n"
        "s_mov_b32 vcc_hi, 0x55555555\n"
        "s_xor_b64 vcc, vcc, s[52:53]\n"
        "s_bcnt1_i32_b64 s65, vcc\n"
        "s_sub_u32 s64, s65, 23\n"
        "s_cmp_lt_u32 s64, 19\n"
        "s_cbranch_scc1 .Lop_rawmap_%=\n"
        "s_cmp_lt_u32 s65, 32\n"
        "s_cselect_b32 s65, 0, -1\n"
        "s_xor_b32 s52, s65, 0x55555555\n"
        "s_xor_b32 s53, s65, 0x55555555\n"
        "s_xor_b32 s54, s65, 0x55555555\n"
        "s_xor_b32 s55, s65, 0xaaaaaaaa\n"
        ".Lop_rawmap_%=:\n"
        "s_xor_b32 s52, s52, s66\n"
        "s_xor_b32 s53, s53, s66\n"
        "s_xor_b32 s54, s54, s66\n"
        "s_xor_b32 s55, s55, s66\n"
        "s_and_b32 s76, s55, 1\n"
        "s_lshl_b32 s76, s76, 13\n"
        "s_add_u32 s60, s60, s76\n"
        "s_barrier\n"
        "v_mov_b32 v48, v6\n"
        "s_mov_b32 s68, -1\n"
        "s_mov_b32 s69, 0\n"
        "s_mov_b32 s78, 0\n"
        "s_mov_b32 s74, -1\n"
        "s_mov_b32 s86, s76\n"
        "s_lshr_b32 s85, s61, 16\n"
        "s_and_b32 s61, s61, 0xffff\n"
        "s_mov_b32 s89, 241172480\n"
        "s_mov_b32 s84, 241172480\n"
        "v_mov_b32 v6, v0\n"
        "s_mov_b32 s80, 0xbfb8aa3b\n"
        "s_lshr_b32 s67, s69, 1\n"
        "s_bitcmp1_b64 s[52:53], s67\n"
        "s_cselect_b32 s65, 8192, 0\n"
        "s_lshl_b32 s77, s67, 21\n"
        "s_add_u32 s77, s77, s65\n"
        "s_add_u32 s71, s77, s58\n"
        "s_add_u32 s78, s78, 32768\n"
        "s_cmp_eq_u32 s78, 98304\n"
        "s_cselect_b32 s78, 0, s78\n"
        "s_add_u32 m0, s78, s61\n"
        "s_lshr_b32 s70, s69, 0\n"
        "s_branch .Lop_wait1_%=\n"
        ".Lop_i0_%=:\n"
        "v_mfma_f32_16x16x32_bf16 v[24:27], a[0:3], v[8:11], 0\n"
        "buffer_load_dword v4, v2, s[40:43], s86 offen sc1\n"
        "v_mfma_f32_16x16x32_bf16 v[28:31], a[4:7], v[8:11], 0\n"
        "s_add_u32 s71, s77, s58\n"
        "s_add_u32 s71, s71, 4096\n"
        "s_add_u32 s78, s78, 32768\n"
        "v_mfma_f32_16x16x32_bf16 v[32:35], a[8:11], v[8:11], 0\n"
        "s_cmp_eq_u32 s78, 98304\n"
        "s_cselect_b32 s78, 0, s78\n"
        "ds_read_b128 v[8:11], v0 offset:4096\n"
        "v_mfma_f32_16x16x32_bf16 v[24:27], a[12:15], v[12:15], v[24:27]\n"
        "s_add_u32 m0, s78, s61\n"
        "s_lshr_b32 s70, s69, 0\n"
        "v_cvt_pk_bf16_f32 v36, v36, v37\n"
        "v_mfma_f32_16x16x32_bf16 v[28:31], a[16:19], v[12:15], v[28:31]\n"
        "v_cvt_pk_bf16_f32 v37, v38, v39\n"
        "v_cvt_pk_bf16_f32 v38, v40, v41\n"
        "v_mfma_f32_16x16x32_bf16 v[32:35], a[20:23], v[12:15], v[32:35]\n"
        "s_waitcnt lgkmcnt(1)\n"
        "ds_read_b128 v[12:15], v0 offset:5120\n"
        "v_cvt_pk_bf16_f32 v39, v42, v43\n"
        "v_mfma_f32_16x16x32_bf16 v[24:27], a[24:27], v[16:19], v[24:27]\n"
        "buffer_store_dwordx4 v[36:39], v48, s[40:43], s72 offen offset:0 sc1\n"
        "v_mfma_f32_16x16x32_bf16 v[28:31], a[28:31], v[16:19], v[28:31]\n"
        "v_cvt_pk_bf16_f32 v44, v44, v45\n"
        "v_cvt_pk_bf16_f32 v45, v46, v47\n"
        "v_mfma_f32_16x16x32_bf16 v[32:35], a[32:35], v[16:19], v[32:35]\n"
        "s_nop 0\n"
        "ds_read_b128 v[16:19], v0 offset:6144\n"
        "v_mfma_f32_16x16x32_bf16 v[24:27], a[36:39], v[20:23], v[24:27]\n"
        "v_permlane16_swap_b32 v44, v46\n"
        "v_mfma_f32_16x16x32_bf16 v[28:31], a[40:43], v[20:23], v[28:31]\n"
        "v_permlane16_swap_b32 v45, v47\n"
        "v_mfma_f32_16x16x32_bf16 v[32:35], a[44:47], v[20:23], v[32:35]\n"
        "s_waitcnt lgkmcnt(1)\n"
        "ds_read_b128 v[20:23], v0 offset:7168\n"
        "v_mfma_f32_16x16x32_bf16 v[24:27], a[48:51], v[8:11], v[24:27]\n"
        "v_mfma_f32_16x16x32_bf16 v[28:31], a[52:55], v[8:11], v[28:31]\n"
        "buffer_store_dwordx4 v[44:47], v7, s[40:43], s72 offen offset:0 sc1\n"
        "v_mfma_f32_16x16x32_bf16 v[32:35], a[56:59], v[8:11], v[32:35]\n"
        "ds_read_b128 v[8:11], v0 offset:8192\n"
        "v_mfma_f32_16x16x32_bf16 v[24:27], a[60:63], v[12:15], v[24:27]\n"
        "v_mfma_f32_16x16x32_bf16 v[28:31], a[64:67], v[12:15], v[28:31]\n"
        "v_mfma_f32_16x16x32_bf16 v[32:35], a[68:71], v[12:15], v[32:35]\n"
        "s_waitcnt lgkmcnt(1)\n"
        "ds_read_b128 v[12:15], v0 offset:9216\n"
        "v_mfma_f32_16x16x32_bf16 v[24:27], a[72:75], v[16:19], v[24:27]\n"
        "v_mfma_f32_16x16x32_bf16 v[28:31], a[76:79], v[16:19], v[28:31]\n"
        "s_waitcnt vmcnt(2)\n"
        "v_cmp_gt_i32 vcc, s70, v4\n"
        "v_mfma_f32_16x16x32_bf16 v[32:35], a[80:83], v[16:19], v[32:35]\n"
        "ds_read_b128 v[16:19], v0 offset:10240\n"
        "v_mfma_f32_16x16x32_bf16 v[24:27], a[84:87], v[20:23], v[24:27]\n"
        "s_and_b64 vcc, vcc, 3\n"
        "v_mfma_f32_16x16x32_bf16 v[28:31], a[88:91], v[20:23], v[28:31]\n"
        "s_cbranch_vccnz .Lop_i00_%=\n"
        "v_mfma_f32_16x16x32_bf16 v[32:35], a[92:95], v[20:23], v[32:35]\n"
        "s_waitcnt lgkmcnt(1)\n"
        "ds_read_b128 v[20:23], v0 offset:11264\n"
        "buffer_load_dwordx4 v1, s[40:43], s71 offen offset:0 sc1 lds\n"
        "v_mfma_f32_16x16x32_bf16 v[24:27], a[96:99], v[8:11], v[24:27]\n"
        "v_mfma_f32_16x16x32_bf16 v[28:31], a[100:103], v[8:11], v[28:31]\n"
        "v_mfma_f32_16x16x32_bf16 v[32:35], a[104:107], v[8:11], v[32:35]\n"
        "ds_read_b128 v[8:11], v0 offset:12288\n"
        "v_mfma_f32_16x16x32_bf16 v[24:27], a[108:111], v[12:15], v[24:27]\n"
        "buffer_load_dwordx4 v1, s[40:43], s71 offen offset:1024 sc1 lds\n"
        "v_mfma_f32_16x16x32_bf16 v[28:31], a[112:115], v[12:15], v[28:31]\n"
        "v_mfma_f32_16x16x32_bf16 v[32:35], a[116:119], v[12:15], v[32:35]\n"
        "s_waitcnt lgkmcnt(1)\n"
        "ds_read_b128 v[12:15], v0 offset:13312\n"
        "v_mfma_f32_16x16x32_bf16 v[24:27], a[120:123], v[16:19], v[24:27]\n"
        "v_mfma_f32_16x16x32_bf16 v[28:31], a[124:127], v[16:19], v[28:31]\n"
        "buffer_load_dwordx4 v1, s[40:43], s71 offen offset:2048 sc1 lds\n"
        "v_mfma_f32_16x16x32_bf16 v[32:35], a[128:131], v[16:19], v[32:35]\n"
        "ds_read_b128 v[16:19], v0 offset:14336\n"
        "v_mfma_f32_16x16x32_bf16 v[24:27], a[132:135], v[20:23], v[24:27]\n"
        "v_mfma_f32_16x16x32_bf16 v[28:31], a[136:139], v[20:23], v[28:31]\n"
        "v_mfma_f32_16x16x32_bf16 v[32:35], a[140:143], v[20:23], v[32:35]\n"
        "s_waitcnt lgkmcnt(1)\n"
        "ds_read_b128 v[20:23], v0 offset:15360\n"
        "buffer_load_dwordx4 v1, s[40:43], s71 offen offset:3072 sc1 lds\n"
        "v_mfma_f32_16x16x32_bf16 v[24:27], a[144:147], v[8:11], v[24:27]\n"
        "s_add_u32 m0, m0, 16384\n"
        "s_add_u32 s71, s71, 65536\n"
        "v_mfma_f32_16x16x32_bf16 v[28:31], a[148:151], v[8:11], v[28:31]\n"
        "s_barrier\n"
        "v_mfma_f32_16x16x32_bf16 v[32:35], a[152:155], v[8:11], v[32:35]\n"
        "ds_read_b128 v[8:11], v0 offset:16384\n"
        "buffer_store_dword v5, v3, s[40:43], s84 offen sc1\n"
        "v_mfma_f32_16x16x32_bf16 v[24:27], a[156:159], v[12:15], v[24:27]\n"
        "v_mov_b32 v5, s74\n"
        "buffer_load_dwordx4 v1, s[40:43], s71 offen offset:0 sc1 lds\n"
        "v_mfma_f32_16x16x32_bf16 v[28:31], a[160:163], v[12:15], v[28:31]\n"
        "s_getreg_b32 s88, hwreg(HW_REG_IB_STS, 0, 4)\n"
        "v_mfma_f32_16x16x32_bf16 v[32:35], a[164:167], v[12:15], v[32:35]\n"
        "s_waitcnt lgkmcnt(1)\n"
        "ds_read_b128 v[12:15], v0 offset:17408\n"
        "v_mfma_f32_16x16x32_bf16 v[24:27], a[168:171], v[16:19], v[24:27]\n"
        "s_cmp_le_u32 s88, 6\n"
        "s_cselect_b32 s88, s73, s89\n"
        "s_cselect_b32 s73, s89, s73\n"
        "v_mfma_f32_16x16x32_bf16 v[28:31], a[172:175], v[16:19], v[28:31]\n"
        "buffer_store_dword v5, v3, s[40:43], s88 offen sc1\n"
        "buffer_load_dwordx4 v1, s[40:43], s71 offen offset:1024 sc1 lds\n"
        "v_mfma_f32_16x16x32_bf16 v[32:35], a[176:179], v[16:19], v[32:35]\n"
        "ds_read_b128 v[16:19], v0 offset:18432\n"
        "v_mfma_f32_16x16x32_bf16 v[24:27], a[180:183], v[20:23], v[24:27]\n"
        "s_getreg_b32 s88, hwreg(HW_REG_IB_STS, 0, 4)\n"
        "v_mfma_f32_16x16x32_bf16 v[28:31], a[184:187], v[20:23], v[28:31]\n"
        "s_cmp_le_u32 s88, 8\n"
        "s_cselect_b32 s88, s73, s89\n"
        "s_cselect_b32 s73, s89, s73\n"
        "v_mfma_f32_16x16x32_bf16 v[32:35], a[188:191], v[20:23], v[32:35]\n"
        "s_waitcnt lgkmcnt(1)\n"
        "ds_read_b128 v[20:23], v0 offset:19456\n"
        "buffer_store_dword v5, v3, s[40:43], s88 offen sc1\n"
        "v_mfma_f32_16x16x32_bf16 v[24:27], a[192:195], v[8:11], v[24:27]\n"
        "buffer_load_dwordx4 v1, s[40:43], s71 offen offset:2048 sc1 lds\n"
        "v_mfma_f32_16x16x32_bf16 v[28:31], a[196:199], v[8:11], v[28:31]\n"
        "v_mfma_f32_16x16x32_bf16 v[32:35], a[200:203], v[8:11], v[32:35]\n"
        "ds_read_b128 v[8:11], v0 offset:20480\n"
        "v_mfma_f32_16x16x32_bf16 v[24:27], a[204:207], v[12:15], v[24:27]\n"
        "buffer_load_dwordx4 v1, s[40:43], s71 offen offset:3072 sc1 lds\n"
        "s_add_u32 s69, s69, 1\n"
        "v_mfma_f32_16x16x32_bf16 v[28:31], a[208:211], v[12:15], v[28:31]\n"
        "v_mfma_f32_16x16x32_bf16 v[32:35], a[212:215], v[12:15], v[32:35]\n"
        "s_waitcnt lgkmcnt(1)\n"
        "ds_read_b128 v[12:15], v0 offset:21504\n"
        "v_mfma_f32_16x16x32_bf16 v[24:27], a[216:219], v[16:19], v[24:27]\n"
        "s_getreg_b32 s88, hwreg(HW_REG_IB_STS, 0, 4)\n"
        "v_mfma_f32_16x16x32_bf16 v[28:31], a[220:223], v[16:19], v[28:31]\n"
        "s_cmp_le_u32 s88, 11\n"
        "s_cselect_b32 s88, s73, s89\n"
        "s_cselect_b32 s73, s89, s73\n"
        "v_mfma_f32_16x16x32_bf16 v[32:35], a[224:227], v[16:19], v[32:35]\n"
        "ds_read_b128 v[16:19], v0 offset:22528\n"
        "buffer_store_dword v5, v3, s[40:43], s88 offen sc1\n"
        "v_mfma_f32_16x16x32_bf16 v[24:27], a[228:231], v[20:23], v[24:27]\n"
        "v_mfma_f32_16x16x32_bf16 v[28:31], a[232:235], v[20:23], v[28:31]\n"
        "v_mfma_f32_16x16x32_bf16 v[32:35], a[236:239], v[20:23], v[32:35]\n"
        "s_waitcnt lgkmcnt(1)\n"
        "ds_read_b128 v[20:23], v0 offset:23552\n"
        "v_mfma_f32_16x16x32_bf16 v[24:27], a[240:243], v[8:11], v[24:27]\n"
        "v_mfma_f32_16x16x32_bf16 v[28:31], a[244:247], v[8:11], v[28:31]\n"
        "v_mfma_f32_16x16x32_bf16 v[32:35], a[248:251], v[8:11], v[32:35]\n"
        "ds_read_b128 v[8:11], v0 offset:24576\n"
        "v_mfma_f32_16x16x32_bf16 v[24:27], a[252:255], v[12:15], v[24:27]\n"
        "v_mfma_f32_16x16x32_bf16 v[28:31], v[80:83], v[12:15], v[28:31]\n"
        "v_mfma_f32_16x16x32_bf16 v[32:35], v[84:87], v[12:15], v[32:35]\n"
        "s_waitcnt lgkmcnt(1)\n"
        "ds_read_b128 v[12:15], v0 offset:25600\n"
        "v_mfma_f32_16x16x32_bf16 v[24:27], v[88:91], v[16:19], v[24:27]\n"
        "v_mfma_f32_16x16x32_bf16 v[28:31], v[92:95], v[16:19], v[28:31]\n"
        "s_waitcnt vmcnt(12)\n"
        "buffer_store_dword v5, v3, s[40:43], s73 offen sc1\n"
        "s_add_u32 s74, s74, 1\n"
        "v_mfma_f32_16x16x32_bf16 v[32:35], v[96:99], v[16:19], v[32:35]\n"
        "s_mov_b32 s84, s89\n"
        "ds_read_b128 v[16:19], v0 offset:26624\n"
        "s_mov_b32 s73, s60\n"
        "v_mfma_f32_16x16x32_bf16 v[24:27], v[100:103], v[20:23], v[24:27]\n"
        "s_add_u32 s68, s68, 1\n"
        "s_lshr_b32 s66, s68, 1\n"
        "v_mfma_f32_16x16x32_bf16 v[28:31], v[104:107], v[20:23], v[28:31]\n"
        "s_bitcmp1_b32 s54, s66\n"
        "s_cselect_b32 s66, 8192, 0\n"
        "s_lshl_b32 s75, s68, 20\n"
        "v_mfma_f32_16x16x32_bf16 v[32:35], v[108:111], v[20:23], v[32:35]\n"
        "s_waitcnt lgkmcnt(1)\n"
        "ds_read_b128 v[20:23], v0 offset:27648\n"
        "s_add_u32 s75, s75, s66\n"
        "v_mfma_f32_16x16x32_bf16 v[24:27], v[112:115], v[8:11], v[24:27]\n"
        "s_add_u32 s72, s75, s59\n"
        "v_mfma_f32_16x16x32_bf16 v[28:31], v[116:119], v[8:11], v[28:31]\n"
        "v_mfma_f32_16x16x32_bf16 v[32:35], v[120:123], v[8:11], v[32:35]\n"
        "ds_read_b128 v[8:11], v0 offset:28672\n"
        "v_mfma_f32_16x16x32_bf16 v[24:27], v[124:127], v[12:15], v[24:27]\n"
        "v_mfma_f32_16x16x32_bf16 v[28:31], v[128:131], v[12:15], v[28:31]\n"
        "v_mfma_f32_16x16x32_bf16 v[32:35], v[132:135], v[12:15], v[32:35]\n"
        "s_waitcnt lgkmcnt(1)\n"
        "ds_read_b128 v[12:15], v0 offset:29696\n"
        "v_mfma_f32_16x16x32_bf16 v[24:27], v[136:139], v[16:19], v[24:27]\n"
        "v_mfma_f32_16x16x32_bf16 v[28:31], v[140:143], v[16:19], v[28:31]\n"
        "v_mfma_f32_16x16x32_bf16 v[32:35], v[144:147], v[16:19], v[32:35]\n"
        "ds_read_b128 v[16:19], v0 offset:30720\n"
        "v_mfma_f32_16x16x32_bf16 v[24:27], v[148:151], v[20:23], v[24:27]\n"
        "v_mfma_f32_16x16x32_bf16 v[28:31], v[152:155], v[20:23], v[28:31]\n"
        "v_mfma_f32_16x16x32_bf16 v[32:35], v[156:159], v[20:23], v[32:35]\n"
        "s_waitcnt lgkmcnt(1)\n"
        "ds_read_b128 v[20:23], v0 offset:31744\n"
        "v_mfma_f32_16x16x32_bf16 v[24:27], v[160:163], v[8:11], v[24:27]\n"
        "v_mfma_f32_16x16x32_bf16 v[28:31], v[164:167], v[8:11], v[28:31]\n"
        "s_waitcnt vmcnt(9)\n"
        "s_barrier\n"
        "v_add_u32 v0, s78, v6\n"
        "v_mfma_f32_16x16x32_bf16 v[32:35], v[168:171], v[8:11], v[32:35]\n"
        "ds_read_b128 v[8:11], v0 offset:0\n"
        "v_mfma_f32_16x16x32_bf16 v[24:27], v[172:175], v[12:15], v[24:27]\n"
        "v_mfma_f32_16x16x32_bf16 v[28:31], v[176:179], v[12:15], v[28:31]\n"
        "v_mfma_f32_16x16x32_bf16 v[32:35], v[180:183], v[12:15], v[32:35]\n"
        "s_waitcnt lgkmcnt(1)\n"
        "ds_read_b128 v[12:15], v0 offset:1024\n"
        "v_mfma_f32_16x16x32_bf16 v[24:27], v[184:187], v[16:19], v[24:27]\n"
        "v_mfma_f32_16x16x32_bf16 v[28:31], v[188:191], v[16:19], v[28:31]\n"
        "v_mfma_f32_16x16x32_bf16 v[32:35], v[192:195], v[16:19], v[32:35]\n"
        "ds_read_b128 v[16:19], v0 offset:2048\n"
        "v_mfma_f32_16x16x32_bf16 v[24:27], v[196:199], v[20:23], v[24:27]\n"
        "v_mfma_f32_16x16x32_bf16 v[28:31], v[200:203], v[20:23], v[28:31]\n"
        "v_mfma_f32_16x16x32_bf16 v[32:35], v[204:207], v[20:23], v[32:35]\n"
        "s_waitcnt lgkmcnt(1)\n"
        "ds_read_b128 v[20:23], v0 offset:3072\n"
        "s_branch .Lop_i1_%=\n"
        ".Lop_i00_%=:\n"
        "v_mfma_f32_16x16x32_bf16 v[32:35], a[92:95], v[20:23], v[32:35]\n"
        "s_waitcnt lgkmcnt(1)\n"
        "ds_read_b128 v[20:23], v0 offset:11264\n"
        "buffer_load_dword v4, v2, s[40:43], s86 offen sc1\n"
        "v_mfma_f32_16x16x32_bf16 v[24:27], a[96:99], v[8:11], v[24:27]\n"
        "v_mfma_f32_16x16x32_bf16 v[28:31], a[100:103], v[8:11], v[28:31]\n"
        "v_mfma_f32_16x16x32_bf16 v[32:35], a[104:107], v[8:11], v[32:35]\n"
        "ds_read_b128 v[8:11], v0 offset:12288\n"
        "v_mfma_f32_16x16x32_bf16 v[24:27], a[108:111], v[12:15], v[24:27]\n"
        "v_mfma_f32_16x16x32_bf16 v[28:31], a[112:115], v[12:15], v[28:31]\n"
        "v_mfma_f32_16x16x32_bf16 v[32:35], a[116:119], v[12:15], v[32:35]\n"
        "s_waitcnt lgkmcnt(1)\n"
        "ds_read_b128 v[12:15], v0 offset:13312\n"
        "v_mfma_f32_16x16x32_bf16 v[24:27], a[120:123], v[16:19], v[24:27]\n"
        "v_mfma_f32_16x16x32_bf16 v[28:31], a[124:127], v[16:19], v[28:31]\n"
        "v_mfma_f32_16x16x32_bf16 v[32:35], a[128:131], v[16:19], v[32:35]\n"
        "ds_read_b128 v[16:19], v0 offset:14336\n"
        "v_mfma_f32_16x16x32_bf16 v[24:27], a[132:135], v[20:23], v[24:27]\n"
        "v_mfma_f32_16x16x32_bf16 v[28:31], a[136:139], v[20:23], v[28:31]\n"
        "v_mfma_f32_16x16x32_bf16 v[32:35], a[140:143], v[20:23], v[32:35]\n"
        "s_waitcnt lgkmcnt(1)\n"
        "ds_read_b128 v[20:23], v0 offset:15360\n"
        "v_mfma_f32_16x16x32_bf16 v[24:27], a[144:147], v[8:11], v[24:27]\n"
        "v_mfma_f32_16x16x32_bf16 v[28:31], a[148:151], v[8:11], v[28:31]\n"
        "s_barrier\n"
        "v_mfma_f32_16x16x32_bf16 v[32:35], a[152:155], v[8:11], v[32:35]\n"
        "ds_read_b128 v[8:11], v0 offset:16384\n"
        "buffer_store_dword v5, v3, s[40:43], s84 offen sc1\n"
        "v_mfma_f32_16x16x32_bf16 v[24:27], a[156:159], v[12:15], v[24:27]\n"
        "v_mov_b32 v5, s74\n"
        "v_mfma_f32_16x16x32_bf16 v[28:31], a[160:163], v[12:15], v[28:31]\n"
        "s_getreg_b32 s88, hwreg(HW_REG_IB_STS, 0, 4)\n"
        "v_mfma_f32_16x16x32_bf16 v[32:35], a[164:167], v[12:15], v[32:35]\n"
        "s_waitcnt lgkmcnt(1)\n"
        "ds_read_b128 v[12:15], v0 offset:17408\n"
        "v_mfma_f32_16x16x32_bf16 v[24:27], a[168:171], v[16:19], v[24:27]\n"
        "s_cmp_le_u32 s88, 2\n"
        "s_cselect_b32 s88, s73, s89\n"
        "s_cselect_b32 s73, s89, s73\n"
        "v_mfma_f32_16x16x32_bf16 v[28:31], a[172:175], v[16:19], v[28:31]\n"
        "buffer_store_dword v5, v3, s[40:43], s88 offen sc1\n"
        "v_mfma_f32_16x16x32_bf16 v[32:35], a[176:179], v[16:19], v[32:35]\n"
        "ds_read_b128 v[16:19], v0 offset:18432\n"
        "v_mfma_f32_16x16x32_bf16 v[24:27], a[180:183], v[20:23], v[24:27]\n"
        "s_getreg_b32 s88, hwreg(HW_REG_IB_STS, 0, 4)\n"
        "v_mfma_f32_16x16x32_bf16 v[28:31], a[184:187], v[20:23], v[28:31]\n"
        "s_cmp_le_u32 s88, 3\n"
        "s_cselect_b32 s88, s73, s89\n"
        "s_cselect_b32 s73, s89, s73\n"
        "v_mfma_f32_16x16x32_bf16 v[32:35], a[188:191], v[20:23], v[32:35]\n"
        "s_waitcnt lgkmcnt(1)\n"
        "ds_read_b128 v[20:23], v0 offset:19456\n"
        "buffer_store_dword v5, v3, s[40:43], s88 offen sc1\n"
        "v_mfma_f32_16x16x32_bf16 v[24:27], a[192:195], v[8:11], v[24:27]\n"
        "v_mfma_f32_16x16x32_bf16 v[28:31], a[196:199], v[8:11], v[28:31]\n"
        "s_waitcnt vmcnt(3)\n"
        "v_cmp_gt_i32 vcc, s70, v4\n"
        "v_mfma_f32_16x16x32_bf16 v[32:35], a[200:203], v[8:11], v[32:35]\n"
        "ds_read_b128 v[8:11], v0 offset:20480\n"
        "v_mfma_f32_16x16x32_bf16 v[24:27], a[204:207], v[12:15], v[24:27]\n"
        "s_and_b64 vcc, vcc, 3\n"
        "v_mfma_f32_16x16x32_bf16 v[28:31], a[208:211], v[12:15], v[28:31]\n"
        "s_cbranch_vccnz .Lop_i01_%=\n"
        "v_mfma_f32_16x16x32_bf16 v[32:35], a[212:215], v[12:15], v[32:35]\n"
        "s_waitcnt lgkmcnt(1)\n"
        "ds_read_b128 v[12:15], v0 offset:21504\n"
        "buffer_load_dwordx4 v1, s[40:43], s71 offen offset:0 sc1 lds\n"
        "v_mfma_f32_16x16x32_bf16 v[24:27], a[216:219], v[16:19], v[24:27]\n"
        "s_getreg_b32 s88, hwreg(HW_REG_IB_STS, 0, 4)\n"
        "v_mfma_f32_16x16x32_bf16 v[28:31], a[220:223], v[16:19], v[28:31]\n"
        "s_cmp_le_u32 s88, 15\n"
        "s_cselect_b32 s88, s73, s89\n"
        "s_cselect_b32 s73, s89, s73\n"
        "v_mfma_f32_16x16x32_bf16 v[32:35], a[224:227], v[16:19], v[32:35]\n"
        "ds_read_b128 v[16:19], v0 offset:22528\n"
        "buffer_store_dword v5, v3, s[40:43], s88 offen sc1\n"
        "v_mfma_f32_16x16x32_bf16 v[24:27], a[228:231], v[20:23], v[24:27]\n"
        "buffer_load_dwordx4 v1, s[40:43], s71 offen offset:1024 sc1 lds\n"
        "v_mfma_f32_16x16x32_bf16 v[28:31], a[232:235], v[20:23], v[28:31]\n"
        "v_mfma_f32_16x16x32_bf16 v[32:35], a[236:239], v[20:23], v[32:35]\n"
        "s_waitcnt lgkmcnt(1)\n"
        "ds_read_b128 v[20:23], v0 offset:23552\n"
        "buffer_load_dwordx4 v1, s[40:43], s71 offen offset:2048 sc1 lds\n"
        "v_mfma_f32_16x16x32_bf16 v[24:27], a[240:243], v[8:11], v[24:27]\n"
        "v_mfma_f32_16x16x32_bf16 v[28:31], a[244:247], v[8:11], v[28:31]\n"
        "v_mfma_f32_16x16x32_bf16 v[32:35], a[248:251], v[8:11], v[32:35]\n"
        "ds_read_b128 v[8:11], v0 offset:24576\n"
        "buffer_load_dwordx4 v1, s[40:43], s71 offen offset:3072 sc1 lds\n"
        "v_mfma_f32_16x16x32_bf16 v[24:27], a[252:255], v[12:15], v[24:27]\n"
        "s_add_u32 m0, m0, 16384\n"
        "s_add_u32 s71, s71, 65536\n"
        "v_mfma_f32_16x16x32_bf16 v[28:31], v[80:83], v[12:15], v[28:31]\n"
        "v_mfma_f32_16x16x32_bf16 v[32:35], v[84:87], v[12:15], v[32:35]\n"
        "s_waitcnt lgkmcnt(1)\n"
        "ds_read_b128 v[12:15], v0 offset:25600\n"
        "buffer_load_dwordx4 v1, s[40:43], s71 offen offset:0 sc1 lds\n"
        "v_mfma_f32_16x16x32_bf16 v[24:27], v[88:91], v[16:19], v[24:27]\n"
        "v_mfma_f32_16x16x32_bf16 v[28:31], v[92:95], v[16:19], v[28:31]\n"
        "buffer_store_dword v5, v3, s[40:43], s73 offen sc1\n"
        "s_add_u32 s74, s74, 1\n"
        "v_mfma_f32_16x16x32_bf16 v[32:35], v[96:99], v[16:19], v[32:35]\n"
        "s_mov_b32 s84, s89\n"
        "ds_read_b128 v[16:19], v0 offset:26624\n"
        "s_mov_b32 s73, s60\n"
        "v_mfma_f32_16x16x32_bf16 v[24:27], v[100:103], v[20:23], v[24:27]\n"
        "s_add_u32 s68, s68, 1\n"
        "s_lshr_b32 s66, s68, 1\n"
        "v_mfma_f32_16x16x32_bf16 v[28:31], v[104:107], v[20:23], v[28:31]\n"
        "s_bitcmp1_b32 s54, s66\n"
        "s_cselect_b32 s66, 8192, 0\n"
        "s_lshl_b32 s75, s68, 20\n"
        "v_mfma_f32_16x16x32_bf16 v[32:35], v[108:111], v[20:23], v[32:35]\n"
        "s_waitcnt lgkmcnt(1)\n"
        "ds_read_b128 v[20:23], v0 offset:27648\n"
        "s_add_u32 s75, s75, s66\n"
        "v_mfma_f32_16x16x32_bf16 v[24:27], v[112:115], v[8:11], v[24:27]\n"
        "s_add_u32 s72, s75, s59\n"
        "v_mfma_f32_16x16x32_bf16 v[28:31], v[116:119], v[8:11], v[28:31]\n"
        "buffer_load_dwordx4 v1, s[40:43], s71 offen offset:1024 sc1 lds\n"
        "v_mfma_f32_16x16x32_bf16 v[32:35], v[120:123], v[8:11], v[32:35]\n"
        "ds_read_b128 v[8:11], v0 offset:28672\n"
        "v_mfma_f32_16x16x32_bf16 v[24:27], v[124:127], v[12:15], v[24:27]\n"
        "v_mfma_f32_16x16x32_bf16 v[28:31], v[128:131], v[12:15], v[28:31]\n"
        "v_mfma_f32_16x16x32_bf16 v[32:35], v[132:135], v[12:15], v[32:35]\n"
        "s_waitcnt lgkmcnt(1)\n"
        "ds_read_b128 v[12:15], v0 offset:29696\n"
        "buffer_load_dwordx4 v1, s[40:43], s71 offen offset:2048 sc1 lds\n"
        "v_mfma_f32_16x16x32_bf16 v[24:27], v[136:139], v[16:19], v[24:27]\n"
        "v_mfma_f32_16x16x32_bf16 v[28:31], v[140:143], v[16:19], v[28:31]\n"
        "v_mfma_f32_16x16x32_bf16 v[32:35], v[144:147], v[16:19], v[32:35]\n"
        "ds_read_b128 v[16:19], v0 offset:30720\n"
        "v_mfma_f32_16x16x32_bf16 v[24:27], v[148:151], v[20:23], v[24:27]\n"
        "buffer_load_dwordx4 v1, s[40:43], s71 offen offset:3072 sc1 lds\n"
        "s_add_u32 s69, s69, 1\n"
        "v_mfma_f32_16x16x32_bf16 v[28:31], v[152:155], v[20:23], v[28:31]\n"
        "v_mfma_f32_16x16x32_bf16 v[32:35], v[156:159], v[20:23], v[32:35]\n"
        "s_waitcnt lgkmcnt(1)\n"
        "ds_read_b128 v[20:23], v0 offset:31744\n"
        "v_mfma_f32_16x16x32_bf16 v[24:27], v[160:163], v[8:11], v[24:27]\n"
        "v_mfma_f32_16x16x32_bf16 v[28:31], v[164:167], v[8:11], v[28:31]\n"
        "s_waitcnt vmcnt(5)\n"
        "s_barrier\n"
        "v_add_u32 v0, s78, v6\n"
        "v_mfma_f32_16x16x32_bf16 v[32:35], v[168:171], v[8:11], v[32:35]\n"
        "ds_read_b128 v[8:11], v0 offset:0\n"
        "v_mfma_f32_16x16x32_bf16 v[24:27], v[172:175], v[12:15], v[24:27]\n"
        "v_mfma_f32_16x16x32_bf16 v[28:31], v[176:179], v[12:15], v[28:31]\n"
        "v_mfma_f32_16x16x32_bf16 v[32:35], v[180:183], v[12:15], v[32:35]\n"
        "s_waitcnt lgkmcnt(1)\n"
        "ds_read_b128 v[12:15], v0 offset:1024\n"
        "v_mfma_f32_16x16x32_bf16 v[24:27], v[184:187], v[16:19], v[24:27]\n"
        "v_mfma_f32_16x16x32_bf16 v[28:31], v[188:191], v[16:19], v[28:31]\n"
        "v_mfma_f32_16x16x32_bf16 v[32:35], v[192:195], v[16:19], v[32:35]\n"
        "ds_read_b128 v[16:19], v0 offset:2048\n"
        "v_mfma_f32_16x16x32_bf16 v[24:27], v[196:199], v[20:23], v[24:27]\n"
        "v_mfma_f32_16x16x32_bf16 v[28:31], v[200:203], v[20:23], v[28:31]\n"
        "v_mfma_f32_16x16x32_bf16 v[32:35], v[204:207], v[20:23], v[32:35]\n"
        "s_waitcnt lgkmcnt(1)\n"
        "ds_read_b128 v[20:23], v0 offset:3072\n"
        "s_branch .Lop_i1_%=\n"
        ".Lop_i01_%=:\n"
        "v_mfma_f32_16x16x32_bf16 v[32:35], a[212:215], v[12:15], v[32:35]\n"
        "s_waitcnt lgkmcnt(1)\n"
        "ds_read_b128 v[12:15], v0 offset:21504\n"
        "buffer_load_dword v4, v2, s[40:43], s86 offen sc1\n"
        "v_mfma_f32_16x16x32_bf16 v[24:27], a[216:219], v[16:19], v[24:27]\n"
        "s_getreg_b32 s88, hwreg(HW_REG_IB_STS, 0, 4)\n"
        "v_mfma_f32_16x16x32_bf16 v[28:31], a[220:223], v[16:19], v[28:31]\n"
        "s_cmp_le_u32 s88, 15\n"
        "s_cselect_b32 s88, s73, s89\n"
        "s_cselect_b32 s73, s89, s73\n"
        "v_mfma_f32_16x16x32_bf16 v[32:35], a[224:227], v[16:19], v[32:35]\n"
        "ds_read_b128 v[16:19], v0 offset:22528\n"
        "buffer_store_dword v5, v3, s[40:43], s88 offen sc1\n"
        "v_mfma_f32_16x16x32_bf16 v[24:27], a[228:231], v[20:23], v[24:27]\n"
        "v_mfma_f32_16x16x32_bf16 v[28:31], a[232:235], v[20:23], v[28:31]\n"
        "v_mfma_f32_16x16x32_bf16 v[32:35], a[236:239], v[20:23], v[32:35]\n"
        "s_waitcnt lgkmcnt(1)\n"
        "ds_read_b128 v[20:23], v0 offset:23552\n"
        "v_mfma_f32_16x16x32_bf16 v[24:27], a[240:243], v[8:11], v[24:27]\n"
        "v_mfma_f32_16x16x32_bf16 v[28:31], a[244:247], v[8:11], v[28:31]\n"
        "v_mfma_f32_16x16x32_bf16 v[32:35], a[248:251], v[8:11], v[32:35]\n"
        "ds_read_b128 v[8:11], v0 offset:24576\n"
        "v_mfma_f32_16x16x32_bf16 v[24:27], a[252:255], v[12:15], v[24:27]\n"
        "v_mfma_f32_16x16x32_bf16 v[28:31], v[80:83], v[12:15], v[28:31]\n"
        "v_mfma_f32_16x16x32_bf16 v[32:35], v[84:87], v[12:15], v[32:35]\n"
        "s_waitcnt lgkmcnt(1)\n"
        "ds_read_b128 v[12:15], v0 offset:25600\n"
        "v_mfma_f32_16x16x32_bf16 v[24:27], v[88:91], v[16:19], v[24:27]\n"
        "v_mfma_f32_16x16x32_bf16 v[28:31], v[92:95], v[16:19], v[28:31]\n"
        "buffer_store_dword v5, v3, s[40:43], s73 offen sc1\n"
        "s_add_u32 s74, s74, 1\n"
        "v_mfma_f32_16x16x32_bf16 v[32:35], v[96:99], v[16:19], v[32:35]\n"
        "s_mov_b32 s84, s89\n"
        "ds_read_b128 v[16:19], v0 offset:26624\n"
        "s_mov_b32 s73, s60\n"
        "v_mfma_f32_16x16x32_bf16 v[24:27], v[100:103], v[20:23], v[24:27]\n"
        "s_add_u32 s68, s68, 1\n"
        "s_lshr_b32 s66, s68, 1\n"
        "v_mfma_f32_16x16x32_bf16 v[28:31], v[104:107], v[20:23], v[28:31]\n"
        "s_bitcmp1_b32 s54, s66\n"
        "s_cselect_b32 s66, 8192, 0\n"
        "s_lshl_b32 s75, s68, 20\n"
        "v_mfma_f32_16x16x32_bf16 v[32:35], v[108:111], v[20:23], v[32:35]\n"
        "s_waitcnt lgkmcnt(1)\n"
        "ds_read_b128 v[20:23], v0 offset:27648\n"
        "s_add_u32 s75, s75, s66\n"
        "v_mfma_f32_16x16x32_bf16 v[24:27], v[112:115], v[8:11], v[24:27]\n"
        "s_add_u32 s72, s75, s59\n"
        "v_mfma_f32_16x16x32_bf16 v[28:31], v[116:119], v[8:11], v[28:31]\n"
        "v_mfma_f32_16x16x32_bf16 v[32:35], v[120:123], v[8:11], v[32:35]\n"
        "ds_read_b128 v[8:11], v0 offset:28672\n"
        "v_mfma_f32_16x16x32_bf16 v[24:27], v[124:127], v[12:15], v[24:27]\n"
        "v_mfma_f32_16x16x32_bf16 v[28:31], v[128:131], v[12:15], v[28:31]\n"
        "v_mfma_f32_16x16x32_bf16 v[32:35], v[132:135], v[12:15], v[32:35]\n"
        "s_waitcnt lgkmcnt(1)\n"
        "ds_read_b128 v[12:15], v0 offset:29696\n"
        "v_mfma_f32_16x16x32_bf16 v[24:27], v[136:139], v[16:19], v[24:27]\n"
        "v_mfma_f32_16x16x32_bf16 v[28:31], v[140:143], v[16:19], v[28:31]\n"
        "v_mfma_f32_16x16x32_bf16 v[32:35], v[144:147], v[16:19], v[32:35]\n"
        "ds_read_b128 v[16:19], v0 offset:30720\n"
        "v_mfma_f32_16x16x32_bf16 v[24:27], v[148:151], v[20:23], v[24:27]\n"
        "s_waitcnt vmcnt(2)\n"
        "v_cmp_gt_i32 vcc, s70, v4\n"
        "v_mfma_f32_16x16x32_bf16 v[28:31], v[152:155], v[20:23], v[28:31]\n"
        "v_mfma_f32_16x16x32_bf16 v[32:35], v[156:159], v[20:23], v[32:35]\n"
        "s_waitcnt lgkmcnt(1)\n"
        "ds_read_b128 v[20:23], v0 offset:31744\n"
        "s_and_b64 vcc, vcc, 3\n"
        "v_mfma_f32_16x16x32_bf16 v[24:27], v[160:163], v[8:11], v[24:27]\n"
        "s_cbranch_vccnz .Lop_i02_%=\n"
        "v_mfma_f32_16x16x32_bf16 v[28:31], v[164:167], v[8:11], v[28:31]\n"
        "buffer_load_dwordx4 v1, s[40:43], s71 offen offset:0 sc1 lds\n"
        "v_mfma_f32_16x16x32_bf16 v[32:35], v[168:171], v[8:11], v[32:35]\n"
        "v_mfma_f32_16x16x32_bf16 v[24:27], v[172:175], v[12:15], v[24:27]\n"
        "buffer_load_dwordx4 v1, s[40:43], s71 offen offset:1024 sc1 lds\n"
        "v_mfma_f32_16x16x32_bf16 v[28:31], v[176:179], v[12:15], v[28:31]\n"
        "v_mfma_f32_16x16x32_bf16 v[32:35], v[180:183], v[12:15], v[32:35]\n"
        "s_waitcnt lgkmcnt(0)\n"
        "buffer_load_dwordx4 v1, s[40:43], s71 offen offset:2048 sc1 lds\n"
        "v_mfma_f32_16x16x32_bf16 v[24:27], v[184:187], v[16:19], v[24:27]\n"
        "v_mfma_f32_16x16x32_bf16 v[28:31], v[188:191], v[16:19], v[28:31]\n"
        "buffer_load_dwordx4 v1, s[40:43], s71 offen offset:3072 sc1 lds\n"
        "s_add_u32 m0, m0, 16384\n"
        "s_add_u32 s71, s71, 65536\n"
        "v_mfma_f32_16x16x32_bf16 v[32:35], v[192:195], v[16:19], v[32:35]\n"
        "v_mfma_f32_16x16x32_bf16 v[24:27], v[196:199], v[20:23], v[24:27]\n"
        "buffer_load_dwordx4 v1, s[40:43], s71 offen offset:0 sc1 lds\n"
        "v_mfma_f32_16x16x32_bf16 v[28:31], v[200:203], v[20:23], v[28:31]\n"
        "v_mfma_f32_16x16x32_bf16 v[32:35], v[204:207], v[20:23], v[32:35]\n"
        "buffer_load_dwordx4 v1, s[40:43], s71 offen offset:1024 sc1 lds\n"
        "buffer_load_dwordx4 v1, s[40:43], s71 offen offset:2048 sc1 lds\n"
        "buffer_load_dwordx4 v1, s[40:43], s71 offen offset:3072 sc1 lds\n"
        "s_add_u32 s69, s69, 1\n"
        "s_nop 2\n"
        "v_cvt_pk_bf16_f32 v24, v24, v25\n"
        "v_cvt_pk_bf16_f32 v25, v26, v27\n"
        "v_cvt_pk_bf16_f32 v26, v28, v29\n"
        "v_cvt_pk_bf16_f32 v27, v30, v31\n"
        "buffer_store_dwordx4 v[24:27], v48, s[40:43], s72 offen offset:0 sc1\n"
        "v_cvt_pk_bf16_f32 v32, v32, v33\n"
        "v_cvt_pk_bf16_f32 v33, v34, v35\n"
        "s_nop 0\n"
        "v_permlane16_swap_b32 v32, v34\n"
        "v_permlane16_swap_b32 v33, v35\n"
        "buffer_store_dwordx4 v[32:35], v7, s[40:43], s72 offen offset:0 sc1\n"
        "v_mov_b32 v5, s74\n"
        "s_mov_b32 s84, s60\n"
        "s_waitcnt vmcnt(6)\n"
        "s_branch .Lop_go0_%=\n"
        ".Lop_i02_%=:\n"
        "v_mfma_f32_16x16x32_bf16 v[28:31], v[164:167], v[8:11], v[28:31]\n"
        "buffer_load_dword v4, v2, s[40:43], s86 offen sc1\n"
        "v_mfma_f32_16x16x32_bf16 v[32:35], v[168:171], v[8:11], v[32:35]\n"
        "v_mfma_f32_16x16x32_bf16 v[24:27], v[172:175], v[12:15], v[24:27]\n"
        "v_mfma_f32_16x16x32_bf16 v[28:31], v[176:179], v[12:15], v[28:31]\n"
        "v_mfma_f32_16x16x32_bf16 v[32:35], v[180:183], v[12:15], v[32:35]\n"
        "s_waitcnt lgkmcnt(0)\n"
        "v_mfma_f32_16x16x32_bf16 v[24:27], v[184:187], v[16:19], v[24:27]\n"
        "v_mfma_f32_16x16x32_bf16 v[28:31], v[188:191], v[16:19], v[28:31]\n"
        "v_mfma_f32_16x16x32_bf16 v[32:35], v[192:195], v[16:19], v[32:35]\n"
        "v_mfma_f32_16x16x32_bf16 v[24:27], v[196:199], v[20:23], v[24:27]\n"
        "v_mfma_f32_16x16x32_bf16 v[28:31], v[200:203], v[20:23], v[28:31]\n"
        "v_mfma_f32_16x16x32_bf16 v[32:35], v[204:207], v[20:23], v[32:35]\n"
        "s_nop 2\n"
        "v_cvt_pk_bf16_f32 v24, v24, v25\n"
        "v_cvt_pk_bf16_f32 v25, v26, v27\n"
        "v_cvt_pk_bf16_f32 v26, v28, v29\n"
        "v_cvt_pk_bf16_f32 v27, v30, v31\n"
        "buffer_store_dwordx4 v[24:27], v48, s[40:43], s72 offen offset:0 sc1\n"
        "v_cvt_pk_bf16_f32 v32, v32, v33\n"
        "v_cvt_pk_bf16_f32 v33, v34, v35\n"
        "s_nop 0\n"
        "v_permlane16_swap_b32 v32, v34\n"
        "v_permlane16_swap_b32 v33, v35\n"
        "buffer_store_dwordx4 v[32:35], v7, s[40:43], s72 offen offset:0 sc1\n"
        "s_waitcnt vmcnt(2)\n"
        "v_cmp_gt_i32 vcc, s70, v4\n"
        "s_nop 0\n"
        "s_and_b64 vcc, vcc, 3\n"
        "s_cbranch_vccnz .Lop_late0_%=\n"
        "buffer_load_dwordx4 v1, s[40:43], s71 offen offset:0 sc1 lds\n"
        "buffer_load_dwordx4 v1, s[40:43], s71 offen offset:1024 sc1 lds\n"
        "buffer_load_dwordx4 v1, s[40:43], s71 offen offset:2048 sc1 lds\n"
        "buffer_load_dwordx4 v1, s[40:43], s71 offen offset:3072 sc1 lds\n"
        "s_add_u32 m0, m0, 16384\n"
        "s_add_u32 s71, s71, 65536\n"
        "buffer_load_dwordx4 v1, s[40:43], s71 offen offset:0 sc1 lds\n"
        "buffer_load_dwordx4 v1, s[40:43], s71 offen offset:1024 sc1 lds\n"
        "buffer_load_dwordx4 v1, s[40:43], s71 offen offset:2048 sc1 lds\n"
        "buffer_load_dwordx4 v1, s[40:43], s71 offen offset:3072 sc1 lds\n"
        "s_add_u32 s69, s69, 1\n"
        "s_waitcnt vmcnt(8)\n"
        "v_mov_b32 v5, s74\n"
        "buffer_store_dword v5, v3, s[40:43], s73 offen sc1\n"
        "s_waitcnt vmcnt(5)\n"
        "s_branch .Lop_go0_%=\n"
        ".Lop_late0_%=:\n"
        "s_waitcnt vmcnt(0)\n"
        "v_mov_b32 v5, s74\n"
        "buffer_store_dword v5, v3, s[40:43], s73 offen sc1\n"
        "s_mov_b32 s64, 0\n"
        ".Lop_in0_top_%=:\n"
        "buffer_load_dword v4, v2, s[40:43], s86 offen sc1\n"
        "s_waitcnt vmcnt(0)\n"
        "v_cmp_gt_i32 vcc, s70, v4\n"
        "s_nop 0\n"
        "s_and_b64 vcc, vcc, 3\n"
        "s_cbranch_vccz .Lop_in0_ok_%=\n"
        "s_add_u32 s64, s64, 1\n"
        "s_and_b32 s65, s64, 255\n"
        "s_cbranch_scc1 .Lop_in0_go_%=\n"
        "s_mov_b32 s65, 49152\n"
        "s_cmp_lt_u32 s64, 1048576\n"
        "s_cbranch_scc1 .Lop_in0_chk_%=\n"
        "v_mov_b32 v4, 1\n"
        "buffer_store_dword v4, off, s[40:43], s65 sc1\n"
        "s_waitcnt vmcnt(0)\n"
        ".Lop_in0_chk_%=:\n"
        "buffer_load_dword v4, off, s[40:43], s65 sc1\n"
        "s_waitcnt vmcnt(0)\n"
        "v_readfirstlane_b32 s65, v4\n"
        "s_nop 0\n"
        "s_cmp_lg_u32 s65, 1\n"
        "s_cbranch_scc1 .Lop_in0_go_%=\n"
        "s_endpgm\n"
        ".Lop_in0_go_%=:\n"
        "s_branch .Lop_in0_top_%=\n"
        ".Lop_in0_ok_%=:\n"
        "buffer_load_dwordx4 v1, s[40:43], s71 offen offset:0 sc1 lds\n"
        "buffer_load_dwordx4 v1, s[40:43], s71 offen offset:1024 sc1 lds\n"
        "buffer_load_dwordx4 v1, s[40:43], s71 offen offset:2048 sc1 lds\n"
        "buffer_load_dwordx4 v1, s[40:43], s71 offen offset:3072 sc1 lds\n"
        "s_add_u32 m0, m0, 16384\n"
        "s_add_u32 s71, s71, 65536\n"
        "buffer_load_dwordx4 v1, s[40:43], s71 offen offset:0 sc1 lds\n"
        "buffer_load_dwordx4 v1, s[40:43], s71 offen offset:1024 sc1 lds\n"
        "buffer_load_dwordx4 v1, s[40:43], s71 offen offset:2048 sc1 lds\n"
        "buffer_load_dwordx4 v1, s[40:43], s71 offen offset:3072 sc1 lds\n"
        "s_add_u32 s69, s69, 1\n"
        "s_waitcnt vmcnt(4)\n"
        ".Lop_go0_%=:\n"
        "s_mov_b32 s72, 241172480\n"
        "s_mov_b32 s83, 241172480\n"
        "s_mov_b32 s73, 241172480\n"
        "v_add_u32 v0, s78, v6\n"
        "s_barrier\n"
        "ds_read_b128 v[8:11], v0 offset:0\n"
        "ds_read_b128 v[12:15], v0 offset:1024\n"
        "ds_read_b128 v[16:19], v0 offset:2048\n"
        "ds_read_b128 v[20:23], v0 offset:3072\n"
        "s_waitcnt lgkmcnt(2)\n"
        "s_branch .Lop_i1_%=\n"
        ".Lop_i1_%=:\n"
        "v_mfma_f32_16x16x32_bf16 v[36:39], a[0:3], v[8:11], 0\n"
        "buffer_load_dword v4, v2, s[40:43], s86 offen sc1\n"
        "v_mfma_f32_16x16x32_bf16 v[40:43], a[4:7], v[8:11], 0\n"
        "s_lshr_b32 s67, s69, 1\n"
        "s_bitcmp1_b64 s[52:53], s67\n"
        "s_cselect_b32 s65, 8192, 0\n"
        "v_mfma_f32_16x16x32_bf16 v[44:47], a[8:11], v[8:11], 0\n"
        "s_lshl_b32 s77, s67, 21\n"
        "ds_read_b128 v[8:11], v0 offset:4096\n"
        "s_add_u32 s77, s77, s65\n"
        "v_mfma_f32_16x16x32_bf16 v[36:39], a[12:15], v[12:15], v[36:39]\n"
        "s_add_u32 s71, s77, s58\n"
        "s_add_u32 s78, s78, 32768\n"
        "v_cvt_pk_bf16_f32 v24, v24, v25\n"
        "v_mfma_f32_16x16x32_bf16 v[40:43], a[16:19], v[12:15], v[40:43]\n"
        "s_cmp_eq_u32 s78, 98304\n"
        "s_cselect_b32 s78, 0, s78\n"
        "s_add_u32 m0, s78, s61\n"
        "v_mfma_f32_16x16x32_bf16 v[44:47], a[20:23], v[12:15], v[44:47]\n"
        "s_waitcnt lgkmcnt(1)\n"
        "ds_read_b128 v[12:15], v0 offset:5120\n"
        "s_lshr_b32 s70, s69, 0\n"
        "v_mfma_f32_16x16x32_bf16 v[36:39], a[24:27], v[16:19], v[36:39]\n"
        "v_cvt_pk_bf16_f32 v25, v26, v27\n"
        "v_cvt_pk_bf16_f32 v26, v28, v29\n"
        "v_mfma_f32_16x16x32_bf16 v[40:43], a[28:31], v[16:19], v[40:43]\n"
        "v_cvt_pk_bf16_f32 v27, v30, v31\n"
        "v_mfma_f32_16x16x32_bf16 v[44:47], a[32:35], v[16:19], v[44:47]\n"
        "ds_read_b128 v[16:19], v0 offset:6144\n"
        "v_mfma_f32_16x16x32_bf16 v[36:39], a[36:39], v[20:23], v[36:39]\n"
        "buffer_store_dwordx4 v[24:27], v48, s[40:43], s72 offen offset:0 sc1\n"
        "v_mfma_f32_16x16x32_bf16 v[40:43], a[40:43], v[20:23], v[40:43]\n"
        "v_cvt_pk_bf16_f32 v32, v32, v33\n"
        "v_cvt_pk_bf16_f32 v33, v34, v35\n"
        "v_mfma_f32_16x16x32_bf16 v[44:47], a[44:47], v[20:23], v[44:47]\n"
        "s_waitcnt lgkmcnt(1)\n"
        "ds_read_b128 v[20:23], v0 offset:7168\n"
        "s_nop 0\n"
        "v_mfma_f32_16x16x32_bf16 v[36:39], a[48:51], v[8:11], v[36:39]\n"
        "v_permlane16_swap_b32 v32, v34\n"
        "v_mfma_f32_16x16x32_bf16 v[40:43], a[52:55], v[8:11], v[40:43]\n"
        "v_permlane16_swap_b32 v33, v35\n"
        "v_mfma_f32_16x16x32_bf16 v[44:47], a[56:59], v[8:11], v[44:47]\n"
        "ds_read_b128 v[8:11], v0 offset:8192\n"
        "v_mfma_f32_16x16x32_bf16 v[36:39], a[60:63], v[12:15], v[36:39]\n"
        "v_mfma_f32_16x16x32_bf16 v[40:43], a[64:67], v[12:15], v[40:43]\n"
        "buffer_store_dwordx4 v[32:35], v7, s[40:43], s72 offen offset:0 sc1\n"
        "v_mfma_f32_16x16x32_bf16 v[44:47], a[68:71], v[12:15], v[44:47]\n"
        "s_waitcnt lgkmcnt(1)\n"
        "ds_read_b128 v[12:15], v0 offset:9216\n"
        "v_mfma_f32_16x16x32_bf16 v[36:39], a[72:75], v[16:19], v[36:39]\n"
        "v_mfma_f32_16x16x32_bf16 v[40:43], a[76:79], v[16:19], v[40:43]\n"
        "s_waitcnt vmcnt(2)\n"
        "v_cmp_gt_i32 vcc, s70, v4\n"
        "v_mfma_f32_16x16x32_bf16 v[44:47], a[80:83], v[16:19], v[44:47]\n"
        "ds_read_b128 v[16:19], v0 offset:10240\n"
        "v_mfma_f32_16x16x32_bf16 v[36:39], a[84:87], v[20:23], v[36:39]\n"
        "s_and_b64 vcc, vcc, 3\n"
        "v_mfma_f32_16x16x32_bf16 v[40:43], a[88:91], v[20:23], v[40:43]\n"
        "s_cbranch_vccnz .Lop_i10_%=\n"
        "v_mfma_f32_16x16x32_bf16 v[44:47], a[92:95], v[20:23], v[44:47]\n"
        "s_waitcnt lgkmcnt(1)\n"
        "ds_read_b128 v[20:23], v0 offset:11264\n"
        "buffer_load_dwordx4 v1, s[40:43], s71 offen offset:0 sc1 lds\n"
        "v_mfma_f32_16x16x32_bf16 v[36:39], a[96:99], v[8:11], v[36:39]\n"
        "v_mfma_f32_16x16x32_bf16 v[40:43], a[100:103], v[8:11], v[40:43]\n"
        "v_mfma_f32_16x16x32_bf16 v[44:47], a[104:107], v[8:11], v[44:47]\n"
        "ds_read_b128 v[8:11], v0 offset:12288\n"
        "v_mfma_f32_16x16x32_bf16 v[36:39], a[108:111], v[12:15], v[36:39]\n"
        "buffer_load_dwordx4 v1, s[40:43], s71 offen offset:1024 sc1 lds\n"
        "v_mfma_f32_16x16x32_bf16 v[40:43], a[112:115], v[12:15], v[40:43]\n"
        "v_mfma_f32_16x16x32_bf16 v[44:47], a[116:119], v[12:15], v[44:47]\n"
        "s_waitcnt lgkmcnt(1)\n"
        "ds_read_b128 v[12:15], v0 offset:13312\n"
        "v_mfma_f32_16x16x32_bf16 v[36:39], a[120:123], v[16:19], v[36:39]\n"
        "v_mfma_f32_16x16x32_bf16 v[40:43], a[124:127], v[16:19], v[40:43]\n"
        "buffer_load_dwordx4 v1, s[40:43], s71 offen offset:2048 sc1 lds\n"
        "v_mfma_f32_16x16x32_bf16 v[44:47], a[128:131], v[16:19], v[44:47]\n"
        "ds_read_b128 v[16:19], v0 offset:14336\n"
        "v_mfma_f32_16x16x32_bf16 v[36:39], a[132:135], v[20:23], v[36:39]\n"
        "v_mfma_f32_16x16x32_bf16 v[40:43], a[136:139], v[20:23], v[40:43]\n"
        "v_mfma_f32_16x16x32_bf16 v[44:47], a[140:143], v[20:23], v[44:47]\n"
        "s_waitcnt lgkmcnt(1)\n"
        "ds_read_b128 v[20:23], v0 offset:15360\n"
        "buffer_load_dwordx4 v1, s[40:43], s71 offen offset:3072 sc1 lds\n"
        "v_mfma_f32_16x16x32_bf16 v[36:39], a[144:147], v[8:11], v[36:39]\n"
        "s_add_u32 m0, m0, 16384\n"
        "s_add_u32 s71, s71, 65536\n"
        "v_mfma_f32_16x16x32_bf16 v[40:43], a[148:151], v[8:11], v[40:43]\n"
        "s_barrier\n"
        "v_mfma_f32_16x16x32_bf16 v[44:47], a[152:155], v[8:11], v[44:47]\n"
        "ds_read_b128 v[8:11], v0 offset:16384\n"
        "buffer_store_dword v5, v3, s[40:43], s84 offen sc1\n"
        "v_mfma_f32_16x16x32_bf16 v[36:39], a[156:159], v[12:15], v[36:39]\n"
        "v_mov_b32 v5, s74\n"
        "buffer_load_dwordx4 v1, s[40:43], s71 offen offset:0 sc1 lds\n"
        "v_mfma_f32_16x16x32_bf16 v[40:43], a[160:163], v[12:15], v[40:43]\n"
        "s_getreg_b32 s88, hwreg(HW_REG_IB_STS, 0, 4)\n"
        "v_mfma_f32_16x16x32_bf16 v[44:47], a[164:167], v[12:15], v[44:47]\n"
        "s_waitcnt lgkmcnt(1)\n"
        "ds_read_b128 v[12:15], v0 offset:17408\n"
        "v_mfma_f32_16x16x32_bf16 v[36:39], a[168:171], v[16:19], v[36:39]\n"
        "s_cmp_le_u32 s88, 6\n"
        "s_cselect_b32 s88, s73, s89\n"
        "s_cselect_b32 s73, s89, s73\n"
        "v_mfma_f32_16x16x32_bf16 v[40:43], a[172:175], v[16:19], v[40:43]\n"
        "buffer_store_dword v5, v3, s[40:43], s88 offen sc1\n"
        "buffer_load_dwordx4 v1, s[40:43], s71 offen offset:1024 sc1 lds\n"
        "v_mfma_f32_16x16x32_bf16 v[44:47], a[176:179], v[16:19], v[44:47]\n"
        "ds_read_b128 v[16:19], v0 offset:18432\n"
        "v_mfma_f32_16x16x32_bf16 v[36:39], a[180:183], v[20:23], v[36:39]\n"
        "v_mfma_f32_16x16x32_bf16 v[40:43], a[184:187], v[20:23], v[40:43]\n"
        "v_mfma_f32_16x16x32_bf16 v[44:47], a[188:191], v[20:23], v[44:47]\n"
        "s_waitcnt lgkmcnt(1)\n"
        "ds_read_b128 v[20:23], v0 offset:19456\n"
        "buffer_load_dwordx4 v1, s[40:43], s71 offen offset:2048 sc1 lds\n"
        "v_mfma_f32_16x16x32_bf16 v[36:39], a[192:195], v[8:11], v[36:39]\n"
        "s_getreg_b32 s88, hwreg(HW_REG_IB_STS, 0, 4)\n"
        "v_mfma_f32_16x16x32_bf16 v[40:43], a[196:199], v[8:11], v[40:43]\n"
        "v_mfma_f32_16x16x32_bf16 v[44:47], a[200:203], v[8:11], v[44:47]\n"
        "ds_read_b128 v[8:11], v0 offset:20480\n"
        "v_mfma_f32_16x16x32_bf16 v[36:39], a[204:207], v[12:15], v[36:39]\n"
        "buffer_load_dwordx4 v1, s[40:43], s71 offen offset:3072 sc1 lds\n"
        "s_add_u32 s69, s69, 1\n"
        "v_mfma_f32_16x16x32_bf16 v[40:43], a[208:211], v[12:15], v[40:43]\n"
        "v_mfma_f32_16x16x32_bf16 v[44:47], a[212:215], v[12:15], v[44:47]\n"
        "s_waitcnt lgkmcnt(1)\n"
        "ds_read_b128 v[12:15], v0 offset:21504\n"
        "v_mfma_f32_16x16x32_bf16 v[36:39], a[216:219], v[16:19], v[36:39]\n"
        "s_cmp_le_u32 s88, 9\n"
        "s_cselect_b32 s88, s73, s89\n"
        "s_cselect_b32 s73, s89, s73\n"
        "v_mfma_f32_16x16x32_bf16 v[40:43], a[220:223], v[16:19], v[40:43]\n"
        "buffer_store_dword v5, v3, s[40:43], s88 offen sc1\n"
        "v_mfma_f32_16x16x32_bf16 v[44:47], a[224:227], v[16:19], v[44:47]\n"
        "s_getreg_b32 s88, hwreg(HW_REG_IB_STS, 0, 4)\n"
        "ds_read_b128 v[16:19], v0 offset:22528\n"
        "v_mfma_f32_16x16x32_bf16 v[36:39], a[228:231], v[20:23], v[36:39]\n"
        "s_cmp_le_u32 s88, 11\n"
        "s_cselect_b32 s88, s73, s89\n"
        "s_cselect_b32 s73, s89, s73\n"
        "v_mfma_f32_16x16x32_bf16 v[40:43], a[232:235], v[20:23], v[40:43]\n"
        "buffer_store_dword v5, v3, s[40:43], s88 offen sc1\n"
        "v_mfma_f32_16x16x32_bf16 v[44:47], a[236:239], v[20:23], v[44:47]\n"
        "s_waitcnt lgkmcnt(1)\n"
        "ds_read_b128 v[20:23], v0 offset:23552\n"
        "v_mfma_f32_16x16x32_bf16 v[36:39], a[240:243], v[8:11], v[36:39]\n"
        "v_mfma_f32_16x16x32_bf16 v[40:43], a[244:247], v[8:11], v[40:43]\n"
        "v_mfma_f32_16x16x32_bf16 v[44:47], a[248:251], v[8:11], v[44:47]\n"
        "ds_read_b128 v[8:11], v0 offset:24576\n"
        "v_mfma_f32_16x16x32_bf16 v[36:39], a[252:255], v[12:15], v[36:39]\n"
        "v_mfma_f32_16x16x32_bf16 v[40:43], v[80:83], v[12:15], v[40:43]\n"
        "v_mfma_f32_16x16x32_bf16 v[44:47], v[84:87], v[12:15], v[44:47]\n"
        "s_waitcnt lgkmcnt(1)\n"
        "ds_read_b128 v[12:15], v0 offset:25600\n"
        "v_mfma_f32_16x16x32_bf16 v[36:39], v[88:91], v[16:19], v[36:39]\n"
        "v_mfma_f32_16x16x32_bf16 v[40:43], v[92:95], v[16:19], v[40:43]\n"
        "v_mfma_f32_16x16x32_bf16 v[44:47], v[96:99], v[16:19], v[44:47]\n"
        "ds_read_b128 v[16:19], v0 offset:26624\n"
        "v_mfma_f32_16x16x32_bf16 v[36:39], v[100:103], v[20:23], v[36:39]\n"
        "v_mfma_f32_16x16x32_bf16 v[40:43], v[104:107], v[20:23], v[40:43]\n"
        "s_waitcnt vmcnt(12)\n"
        "buffer_store_dword v5, v3, s[40:43], s73 offen sc1\n"
        "s_add_u32 s74, s74, 1\n"
        "v_mfma_f32_16x16x32_bf16 v[44:47], v[108:111], v[20:23], v[44:47]\n"
        "s_waitcnt lgkmcnt(1)\n"
        "ds_read_b128 v[20:23], v0 offset:27648\n"
        "s_mov_b32 s84, s89\n"
        "v_mfma_f32_16x16x32_bf16 v[36:39], v[112:115], v[8:11], v[36:39]\n"
        "s_mov_b32 s73, s60\n"
        "s_add_u32 s72, s75, s59\n"
        "s_add_u32 s72, s72, 147456\n"
        "v_mfma_f32_16x16x32_bf16 v[40:43], v[116:119], v[8:11], v[40:43]\n"
        "v_mfma_f32_16x16x32_bf16 v[44:47], v[120:123], v[8:11], v[44:47]\n"
        "ds_read_b128 v[8:11], v0 offset:28672\n"
        "v_mfma_f32_16x16x32_bf16 v[36:39], v[124:127], v[12:15], v[36:39]\n"
        "v_mfma_f32_16x16x32_bf16 v[40:43], v[128:131], v[12:15], v[40:43]\n"
        "v_mfma_f32_16x16x32_bf16 v[44:47], v[132:135], v[12:15], v[44:47]\n"
        "s_waitcnt lgkmcnt(1)\n"
        "ds_read_b128 v[12:15], v0 offset:29696\n"
        "v_mfma_f32_16x16x32_bf16 v[36:39], v[136:139], v[16:19], v[36:39]\n"
        "v_mfma_f32_16x16x32_bf16 v[40:43], v[140:143], v[16:19], v[40:43]\n"
        "v_mfma_f32_16x16x32_bf16 v[44:47], v[144:147], v[16:19], v[44:47]\n"
        "ds_read_b128 v[16:19], v0 offset:30720\n"
        "v_mfma_f32_16x16x32_bf16 v[36:39], v[148:151], v[20:23], v[36:39]\n"
        "v_mfma_f32_16x16x32_bf16 v[40:43], v[152:155], v[20:23], v[40:43]\n"
        "v_mfma_f32_16x16x32_bf16 v[44:47], v[156:159], v[20:23], v[44:47]\n"
        "s_waitcnt lgkmcnt(1)\n"
        "ds_read_b128 v[20:23], v0 offset:31744\n"
        "v_mfma_f32_16x16x32_bf16 v[36:39], v[160:163], v[8:11], v[36:39]\n"
        "v_mfma_f32_16x16x32_bf16 v[40:43], v[164:167], v[8:11], v[40:43]\n"
        "s_waitcnt vmcnt(9)\n"
        "s_barrier\n"
        "v_add_u32 v0, s78, v6\n"
        "v_mfma_f32_16x16x32_bf16 v[44:47], v[168:171], v[8:11], v[44:47]\n"
        "ds_read_b128 v[8:11], v0 offset:0\n"
        "v_mfma_f32_16x16x32_bf16 v[36:39], v[172:175], v[12:15], v[36:39]\n"
        "v_mfma_f32_16x16x32_bf16 v[40:43], v[176:179], v[12:15], v[40:43]\n"
        "v_mfma_f32_16x16x32_bf16 v[44:47], v[180:183], v[12:15], v[44:47]\n"
        "s_waitcnt lgkmcnt(1)\n"
        "ds_read_b128 v[12:15], v0 offset:1024\n"
        "v_mfma_f32_16x16x32_bf16 v[36:39], v[184:187], v[16:19], v[36:39]\n"
        "v_mfma_f32_16x16x32_bf16 v[40:43], v[188:191], v[16:19], v[40:43]\n"
        "v_mfma_f32_16x16x32_bf16 v[44:47], v[192:195], v[16:19], v[44:47]\n"
        "ds_read_b128 v[16:19], v0 offset:2048\n"
        "v_mfma_f32_16x16x32_bf16 v[36:39], v[196:199], v[20:23], v[36:39]\n"
        "v_mfma_f32_16x16x32_bf16 v[40:43], v[200:203], v[20:23], v[40:43]\n"
        "v_mfma_f32_16x16x32_bf16 v[44:47], v[204:207], v[20:23], v[44:47]\n"
        "s_waitcnt lgkmcnt(1)\n"
        "ds_read_b128 v[20:23], v0 offset:3072\n"
        "s_branch .Lop_i0_%=\n"
        ".Lop_i10_%=:\n"
        "v_mfma_f32_16x16x32_bf16 v[44:47], a[92:95], v[20:23], v[44:47]\n"
        "s_waitcnt lgkmcnt(1)\n"
        "ds_read_b128 v[20:23], v0 offset:11264\n"
        "buffer_load_dword v4, v2, s[40:43], s86 offen sc1\n"
        "v_mfma_f32_16x16x32_bf16 v[36:39], a[96:99], v[8:11], v[36:39]\n"
        "v_mfma_f32_16x16x32_bf16 v[40:43], a[100:103], v[8:11], v[40:43]\n"
        "v_mfma_f32_16x16x32_bf16 v[44:47], a[104:107], v[8:11], v[44:47]\n"
        "ds_read_b128 v[8:11], v0 offset:12288\n"
        "v_mfma_f32_16x16x32_bf16 v[36:39], a[108:111], v[12:15], v[36:39]\n"
        "v_mfma_f32_16x16x32_bf16 v[40:43], a[112:115], v[12:15], v[40:43]\n"
        "v_mfma_f32_16x16x32_bf16 v[44:47], a[116:119], v[12:15], v[44:47]\n"
        "s_waitcnt lgkmcnt(1)\n"
        "ds_read_b128 v[12:15], v0 offset:13312\n"
        "v_mfma_f32_16x16x32_bf16 v[36:39], a[120:123], v[16:19], v[36:39]\n"
        "v_mfma_f32_16x16x32_bf16 v[40:43], a[124:127], v[16:19], v[40:43]\n"
        "v_mfma_f32_16x16x32_bf16 v[44:47], a[128:131], v[16:19], v[44:47]\n"
        "ds_read_b128 v[16:19], v0 offset:14336\n"
        "v_mfma_f32_16x16x32_bf16 v[36:39], a[132:135], v[20:23], v[36:39]\n"
        "v_mfma_f32_16x16x32_bf16 v[40:43], a[136:139], v[20:23], v[40:43]\n"
        "v_mfma_f32_16x16x32_bf16 v[44:47], a[140:143], v[20:23], v[44:47]\n"
        "s_waitcnt lgkmcnt(1)\n"
        "ds_read_b128 v[20:23], v0 offset:15360\n"
        "v_mfma_f32_16x16x32_bf16 v[36:39], a[144:147], v[8:11], v[36:39]\n"
        "v_mfma_f32_16x16x32_bf16 v[40:43], a[148:151], v[8:11], v[40:43]\n"
        "s_barrier\n"
        "v_mfma_f32_16x16x32_bf16 v[44:47], a[152:155], v[8:11], v[44:47]\n"
        "ds_read_b128 v[8:11], v0 offset:16384\n"
        "buffer_store_dword v5, v3, s[40:43], s84 offen sc1\n"
        "v_mfma_f32_16x16x32_bf16 v[36:39], a[156:159], v[12:15], v[36:39]\n"
        "v_mov_b32 v5, s74\n"
        "v_mfma_f32_16x16x32_bf16 v[40:43], a[160:163], v[12:15], v[40:43]\n"
        "s_getreg_b32 s88, hwreg(HW_REG_IB_STS, 0, 4)\n"
        "v_mfma_f32_16x16x32_bf16 v[44:47], a[164:167], v[12:15], v[44:47]\n"
        "s_waitcnt lgkmcnt(1)\n"
        "ds_read_b128 v[12:15], v0 offset:17408\n"
        "v_mfma_f32_16x16x32_bf16 v[36:39], a[168:171], v[16:19], v[36:39]\n"
        "s_cmp_le_u32 s88, 2\n"
        "s_cselect_b32 s88, s73, s89\n"
        "s_cselect_b32 s73, s89, s73\n"
        "v_mfma_f32_16x16x32_bf16 v[40:43], a[172:175], v[16:19], v[40:43]\n"
        "buffer_store_dword v5, v3, s[40:43], s88 offen sc1\n"
        "v_mfma_f32_16x16x32_bf16 v[44:47], a[176:179], v[16:19], v[44:47]\n"
        "ds_read_b128 v[16:19], v0 offset:18432\n"
        "v_mfma_f32_16x16x32_bf16 v[36:39], a[180:183], v[20:23], v[36:39]\n"
        "v_mfma_f32_16x16x32_bf16 v[40:43], a[184:187], v[20:23], v[40:43]\n"
        "v_mfma_f32_16x16x32_bf16 v[44:47], a[188:191], v[20:23], v[44:47]\n"
        "s_waitcnt lgkmcnt(1)\n"
        "ds_read_b128 v[20:23], v0 offset:19456\n"
        "v_mfma_f32_16x16x32_bf16 v[36:39], a[192:195], v[8:11], v[36:39]\n"
        "s_getreg_b32 s88, hwreg(HW_REG_IB_STS, 0, 4)\n"
        "v_mfma_f32_16x16x32_bf16 v[40:43], a[196:199], v[8:11], v[40:43]\n"
        "s_waitcnt vmcnt(2)\n"
        "v_cmp_gt_i32 vcc, s70, v4\n"
        "v_mfma_f32_16x16x32_bf16 v[44:47], a[200:203], v[8:11], v[44:47]\n"
        "ds_read_b128 v[8:11], v0 offset:20480\n"
        "v_mfma_f32_16x16x32_bf16 v[36:39], a[204:207], v[12:15], v[36:39]\n"
        "s_and_b64 vcc, vcc, 3\n"
        "v_mfma_f32_16x16x32_bf16 v[40:43], a[208:211], v[12:15], v[40:43]\n"
        "s_cbranch_vccnz .Lop_i11_%=\n"
        "v_mfma_f32_16x16x32_bf16 v[44:47], a[212:215], v[12:15], v[44:47]\n"
        "s_waitcnt lgkmcnt(1)\n"
        "ds_read_b128 v[12:15], v0 offset:21504\n"
        "buffer_load_dwordx4 v1, s[40:43], s71 offen offset:0 sc1 lds\n"
        "v_mfma_f32_16x16x32_bf16 v[36:39], a[216:219], v[16:19], v[36:39]\n"
        "s_cmp_le_u32 s88, 3\n"
        "s_cselect_b32 s88, s73, s89\n"
        "s_cselect_b32 s73, s89, s73\n"
        "v_mfma_f32_16x16x32_bf16 v[40:43], a[220:223], v[16:19], v[40:43]\n"
        "buffer_store_dword v5, v3, s[40:43], s88 offen sc1\n"
        "v_mfma_f32_16x16x32_bf16 v[44:47], a[224:227], v[16:19], v[44:47]\n"
        "s_getreg_b32 s88, hwreg(HW_REG_IB_STS, 0, 4)\n"
        "ds_read_b128 v[16:19], v0 offset:22528\n"
        "buffer_load_dwordx4 v1, s[40:43], s71 offen offset:1024 sc1 lds\n"
        "v_mfma_f32_16x16x32_bf16 v[36:39], a[228:231], v[20:23], v[36:39]\n"
        "s_cmp_le_u32 s88, 15\n"
        "s_cselect_b32 s88, s73, s89\n"
        "s_cselect_b32 s73, s89, s73\n"
        "v_mfma_f32_16x16x32_bf16 v[40:43], a[232:235], v[20:23], v[40:43]\n"
        "buffer_store_dword v5, v3, s[40:43], s88 offen sc1\n"
        "v_mfma_f32_16x16x32_bf16 v[44:47], a[236:239], v[20:23], v[44:47]\n"
        "s_waitcnt lgkmcnt(1)\n"
        "ds_read_b128 v[20:23], v0 offset:23552\n"
        "buffer_load_dwordx4 v1, s[40:43], s71 offen offset:2048 sc1 lds\n"
        "v_mfma_f32_16x16x32_bf16 v[36:39], a[240:243], v[8:11], v[36:39]\n"
        "v_mfma_f32_16x16x32_bf16 v[40:43], a[244:247], v[8:11], v[40:43]\n"
        "v_mfma_f32_16x16x32_bf16 v[44:47], a[248:251], v[8:11], v[44:47]\n"
        "ds_read_b128 v[8:11], v0 offset:24576\n"
        "buffer_load_dwordx4 v1, s[40:43], s71 offen offset:3072 sc1 lds\n"
        "v_mfma_f32_16x16x32_bf16 v[36:39], a[252:255], v[12:15], v[36:39]\n"
        "s_add_u32 m0, m0, 16384\n"
        "s_add_u32 s71, s71, 65536\n"
        "v_mfma_f32_16x16x32_bf16 v[40:43], v[80:83], v[12:15], v[40:43]\n"
        "v_mfma_f32_16x16x32_bf16 v[44:47], v[84:87], v[12:15], v[44:47]\n"
        "s_waitcnt lgkmcnt(1)\n"
        "ds_read_b128 v[12:15], v0 offset:25600\n"
        "buffer_load_dwordx4 v1, s[40:43], s71 offen offset:0 sc1 lds\n"
        "v_mfma_f32_16x16x32_bf16 v[36:39], v[88:91], v[16:19], v[36:39]\n"
        "v_mfma_f32_16x16x32_bf16 v[40:43], v[92:95], v[16:19], v[40:43]\n"
        "v_mfma_f32_16x16x32_bf16 v[44:47], v[96:99], v[16:19], v[44:47]\n"
        "ds_read_b128 v[16:19], v0 offset:26624\n"
        "v_mfma_f32_16x16x32_bf16 v[36:39], v[100:103], v[20:23], v[36:39]\n"
        "v_mfma_f32_16x16x32_bf16 v[40:43], v[104:107], v[20:23], v[40:43]\n"
        "buffer_store_dword v5, v3, s[40:43], s73 offen sc1\n"
        "s_add_u32 s74, s74, 1\n"
        "v_mfma_f32_16x16x32_bf16 v[44:47], v[108:111], v[20:23], v[44:47]\n"
        "s_waitcnt lgkmcnt(1)\n"
        "ds_read_b128 v[20:23], v0 offset:27648\n"
        "s_mov_b32 s84, s89\n"
        "v_mfma_f32_16x16x32_bf16 v[36:39], v[112:115], v[8:11], v[36:39]\n"
        "s_mov_b32 s73, s60\n"
        "s_add_u32 s72, s75, s59\n"
        "s_add_u32 s72, s72, 147456\n"
        "v_mfma_f32_16x16x32_bf16 v[40:43], v[116:119], v[8:11], v[40:43]\n"
        "buffer_load_dwordx4 v1, s[40:43], s71 offen offset:1024 sc1 lds\n"
        "v_mfma_f32_16x16x32_bf16 v[44:47], v[120:123], v[8:11], v[44:47]\n"
        "ds_read_b128 v[8:11], v0 offset:28672\n"
        "v_mfma_f32_16x16x32_bf16 v[36:39], v[124:127], v[12:15], v[36:39]\n"
        "v_mfma_f32_16x16x32_bf16 v[40:43], v[128:131], v[12:15], v[40:43]\n"
        "v_mfma_f32_16x16x32_bf16 v[44:47], v[132:135], v[12:15], v[44:47]\n"
        "s_waitcnt lgkmcnt(1)\n"
        "ds_read_b128 v[12:15], v0 offset:29696\n"
        "buffer_load_dwordx4 v1, s[40:43], s71 offen offset:2048 sc1 lds\n"
        "v_mfma_f32_16x16x32_bf16 v[36:39], v[136:139], v[16:19], v[36:39]\n"
        "v_mfma_f32_16x16x32_bf16 v[40:43], v[140:143], v[16:19], v[40:43]\n"
        "v_mfma_f32_16x16x32_bf16 v[44:47], v[144:147], v[16:19], v[44:47]\n"
        "ds_read_b128 v[16:19], v0 offset:30720\n"
        "v_mfma_f32_16x16x32_bf16 v[36:39], v[148:151], v[20:23], v[36:39]\n"
        "buffer_load_dwordx4 v1, s[40:43], s71 offen offset:3072 sc1 lds\n"
        "s_add_u32 s69, s69, 1\n"
        "v_mfma_f32_16x16x32_bf16 v[40:43], v[152:155], v[20:23], v[40:43]\n"
        "v_mfma_f32_16x16x32_bf16 v[44:47], v[156:159], v[20:23], v[44:47]\n"
        "s_waitcnt lgkmcnt(1)\n"
        "ds_read_b128 v[20:23], v0 offset:31744\n"
        "v_mfma_f32_16x16x32_bf16 v[36:39], v[160:163], v[8:11], v[36:39]\n"
        "v_mfma_f32_16x16x32_bf16 v[40:43], v[164:167], v[8:11], v[40:43]\n"
        "s_waitcnt vmcnt(5)\n"
        "s_barrier\n"
        "v_add_u32 v0, s78, v6\n"
        "v_mfma_f32_16x16x32_bf16 v[44:47], v[168:171], v[8:11], v[44:47]\n"
        "ds_read_b128 v[8:11], v0 offset:0\n"
        "v_mfma_f32_16x16x32_bf16 v[36:39], v[172:175], v[12:15], v[36:39]\n"
        "v_mfma_f32_16x16x32_bf16 v[40:43], v[176:179], v[12:15], v[40:43]\n"
        "v_mfma_f32_16x16x32_bf16 v[44:47], v[180:183], v[12:15], v[44:47]\n"
        "s_waitcnt lgkmcnt(1)\n"
        "ds_read_b128 v[12:15], v0 offset:1024\n"
        "v_mfma_f32_16x16x32_bf16 v[36:39], v[184:187], v[16:19], v[36:39]\n"
        "v_mfma_f32_16x16x32_bf16 v[40:43], v[188:191], v[16:19], v[40:43]\n"
        "v_mfma_f32_16x16x32_bf16 v[44:47], v[192:195], v[16:19], v[44:47]\n"
        "ds_read_b128 v[16:19], v0 offset:2048\n"
        "v_mfma_f32_16x16x32_bf16 v[36:39], v[196:199], v[20:23], v[36:39]\n"
        "v_mfma_f32_16x16x32_bf16 v[40:43], v[200:203], v[20:23], v[40:43]\n"
        "v_mfma_f32_16x16x32_bf16 v[44:47], v[204:207], v[20:23], v[44:47]\n"
        "s_waitcnt lgkmcnt(1)\n"
        "ds_read_b128 v[20:23], v0 offset:3072\n"
        "s_branch .Lop_i0_%=\n"
        ".Lop_i11_%=:\n"
        "v_mfma_f32_16x16x32_bf16 v[44:47], a[212:215], v[12:15], v[44:47]\n"
        "s_waitcnt lgkmcnt(1)\n"
        "ds_read_b128 v[12:15], v0 offset:21504\n"
        "buffer_load_dword v4, v2, s[40:43], s86 offen sc1\n"
        "v_mfma_f32_16x16x32_bf16 v[36:39], a[216:219], v[16:19], v[36:39]\n"
        "s_cmp_le_u32 s88, 3\n"
        "s_cselect_b32 s88, s73, s89\n"
        "s_cselect_b32 s73, s89, s73\n"
        "v_mfma_f32_16x16x32_bf16 v[40:43], a[220:223], v[16:19], v[40:43]\n"
        "buffer_store_dword v5, v3, s[40:43], s88 offen sc1\n"
        "v_mfma_f32_16x16x32_bf16 v[44:47], a[224:227], v[16:19], v[44:47]\n"
        "s_getreg_b32 s88, hwreg(HW_REG_IB_STS, 0, 4)\n"
        "ds_read_b128 v[16:19], v0 offset:22528\n"
        "v_mfma_f32_16x16x32_bf16 v[36:39], a[228:231], v[20:23], v[36:39]\n"
        "s_cmp_le_u32 s88, 15\n"
        "s_cselect_b32 s88, s73, s89\n"
        "s_cselect_b32 s73, s89, s73\n"
        "v_mfma_f32_16x16x32_bf16 v[40:43], a[232:235], v[20:23], v[40:43]\n"
        "buffer_store_dword v5, v3, s[40:43], s88 offen sc1\n"
        "v_mfma_f32_16x16x32_bf16 v[44:47], a[236:239], v[20:23], v[44:47]\n"
        "s_waitcnt lgkmcnt(1)\n"
        "ds_read_b128 v[20:23], v0 offset:23552\n"
        "v_mfma_f32_16x16x32_bf16 v[36:39], a[240:243], v[8:11], v[36:39]\n"
        "v_mfma_f32_16x16x32_bf16 v[40:43], a[244:247], v[8:11], v[40:43]\n"
        "v_mfma_f32_16x16x32_bf16 v[44:47], a[248:251], v[8:11], v[44:47]\n"
        "ds_read_b128 v[8:11], v0 offset:24576\n"
        "v_mfma_f32_16x16x32_bf16 v[36:39], a[252:255], v[12:15], v[36:39]\n"
        "v_mfma_f32_16x16x32_bf16 v[40:43], v[80:83], v[12:15], v[40:43]\n"
        "v_mfma_f32_16x16x32_bf16 v[44:47], v[84:87], v[12:15], v[44:47]\n"
        "s_waitcnt lgkmcnt(1)\n"
        "ds_read_b128 v[12:15], v0 offset:25600\n"
        "v_mfma_f32_16x16x32_bf16 v[36:39], v[88:91], v[16:19], v[36:39]\n"
        "v_mfma_f32_16x16x32_bf16 v[40:43], v[92:95], v[16:19], v[40:43]\n"
        "v_mfma_f32_16x16x32_bf16 v[44:47], v[96:99], v[16:19], v[44:47]\n"
        "ds_read_b128 v[16:19], v0 offset:26624\n"
        "v_mfma_f32_16x16x32_bf16 v[36:39], v[100:103], v[20:23], v[36:39]\n"
        "v_mfma_f32_16x16x32_bf16 v[40:43], v[104:107], v[20:23], v[40:43]\n"
        "buffer_store_dword v5, v3, s[40:43], s73 offen sc1\n"
        "s_add_u32 s74, s74, 1\n"
        "v_mfma_f32_16x16x32_bf16 v[44:47], v[108:111], v[20:23], v[44:47]\n"
        "s_waitcnt lgkmcnt(1)\n"
        "ds_read_b128 v[20:23], v0 offset:27648\n"
        "s_mov_b32 s84, s89\n"
        "v_mfma_f32_16x16x32_bf16 v[36:39], v[112:115], v[8:11], v[36:39]\n"
        "s_mov_b32 s73, s60\n"
        "s_add_u32 s72, s75, s59\n"
        "s_add_u32 s72, s72, 147456\n"
        "v_mfma_f32_16x16x32_bf16 v[40:43], v[116:119], v[8:11], v[40:43]\n"
        "v_mfma_f32_16x16x32_bf16 v[44:47], v[120:123], v[8:11], v[44:47]\n"
        "ds_read_b128 v[8:11], v0 offset:28672\n"
        "v_mfma_f32_16x16x32_bf16 v[36:39], v[124:127], v[12:15], v[36:39]\n"
        "v_mfma_f32_16x16x32_bf16 v[40:43], v[128:131], v[12:15], v[40:43]\n"
        "v_mfma_f32_16x16x32_bf16 v[44:47], v[132:135], v[12:15], v[44:47]\n"
        "s_waitcnt lgkmcnt(1)\n"
        "ds_read_b128 v[12:15], v0 offset:29696\n"
        "v_mfma_f32_16x16x32_bf16 v[36:39], v[136:139], v[16:19], v[36:39]\n"
        "v_mfma_f32_16x16x32_bf16 v[40:43], v[140:143], v[16:19], v[40:43]\n"
        "v_mfma_f32_16x16x32_bf16 v[44:47], v[144:147], v[16:19], v[44:47]\n"
        "ds_read_b128 v[16:19], v0 offset:30720\n"
        "v_mfma_f32_16x16x32_bf16 v[36:39], v[148:151], v[20:23], v[36:39]\n"
        "s_waitcnt vmcnt(3)\n"
        "v_cmp_gt_i32 vcc, s70, v4\n"
        "v_mfma_f32_16x16x32_bf16 v[40:43], v[152:155], v[20:23], v[40:43]\n"
        "v_mfma_f32_16x16x32_bf16 v[44:47], v[156:159], v[20:23], v[44:47]\n"
        "s_waitcnt lgkmcnt(1)\n"
        "ds_read_b128 v[20:23], v0 offset:31744\n"
        "s_and_b64 vcc, vcc, 3\n"
        "v_mfma_f32_16x16x32_bf16 v[36:39], v[160:163], v[8:11], v[36:39]\n"
        "s_cbranch_vccnz .Lop_i12_%=\n"
        "v_mfma_f32_16x16x32_bf16 v[40:43], v[164:167], v[8:11], v[40:43]\n"
        "buffer_load_dwordx4 v1, s[40:43], s71 offen offset:0 sc1 lds\n"
        "v_mfma_f32_16x16x32_bf16 v[44:47], v[168:171], v[8:11], v[44:47]\n"
        "v_mfma_f32_16x16x32_bf16 v[36:39], v[172:175], v[12:15], v[36:39]\n"
        "buffer_load_dwordx4 v1, s[40:43], s71 offen offset:1024 sc1 lds\n"
        "v_mfma_f32_16x16x32_bf16 v[40:43], v[176:179], v[12:15], v[40:43]\n"
        "v_mfma_f32_16x16x32_bf16 v[44:47], v[180:183], v[12:15], v[44:47]\n"
        "s_waitcnt lgkmcnt(0)\n"
        "buffer_load_dwordx4 v1, s[40:43], s71 offen offset:2048 sc1 lds\n"
        "v_mfma_f32_16x16x32_bf16 v[36:39], v[184:187], v[16:19], v[36:39]\n"
        "v_mfma_f32_16x16x32_bf16 v[40:43], v[188:191], v[16:19], v[40:43]\n"
        "buffer_load_dwordx4 v1, s[40:43], s71 offen offset:3072 sc1 lds\n"
        "s_add_u32 m0, m0, 16384\n"
        "s_add_u32 s71, s71, 65536\n"
        "v_mfma_f32_16x16x32_bf16 v[44:47], v[192:195], v[16:19], v[44:47]\n"
        "v_mfma_f32_16x16x32_bf16 v[36:39], v[196:199], v[20:23], v[36:39]\n"
        "buffer_load_dwordx4 v1, s[40:43], s71 offen offset:0 sc1 lds\n"
        "v_mfma_f32_16x16x32_bf16 v[40:43], v[200:203], v[20:23], v[40:43]\n"
        "v_mfma_f32_16x16x32_bf16 v[44:47], v[204:207], v[20:23], v[44:47]\n"
        "buffer_load_dwordx4 v1, s[40:43], s71 offen offset:1024 sc1 lds\n"
        "buffer_load_dwordx4 v1, s[40:43], s71 offen offset:2048 sc1 lds\n"
        "buffer_load_dwordx4 v1, s[40:43], s71 offen offset:3072 sc1 lds\n"
        "s_add_u32 s69, s69, 1\n"
        "s_nop 2\n"
        "v_cvt_pk_bf16_f32 v36, v36, v37\n"
        "v_cvt_pk_bf16_f32 v37, v38, v39\n"
        "v_cvt_pk_bf16_f32 v38, v40, v41\n"
        "v_cvt_pk_bf16_f32 v39, v42, v43\n"
        "buffer_store_dwordx4 v[36:39], v48, s[40:43], s72 offen offset:0 sc1\n"
        "v_cvt_pk_bf16_f32 v44, v44, v45\n"
        "v_cvt_pk_bf16_f32 v45, v46, v47\n"
        "s_nop 0\n"
        "v_permlane16_swap_b32 v44, v46\n"
        "v_permlane16_swap_b32 v45, v47\n"
        "buffer_store_dwordx4 v[44:47], v7, s[40:43], s72 offen offset:0 sc1\n"
        "v_mov_b32 v5, s74\n"
        "s_mov_b32 s84, s60\n"
        "s_waitcnt vmcnt(6)\n"
        "s_branch .Lop_go1_%=\n"
        ".Lop_i12_%=:\n"
        "v_mfma_f32_16x16x32_bf16 v[40:43], v[164:167], v[8:11], v[40:43]\n"
        "buffer_load_dword v4, v2, s[40:43], s86 offen sc1\n"
        "v_mfma_f32_16x16x32_bf16 v[44:47], v[168:171], v[8:11], v[44:47]\n"
        "v_mfma_f32_16x16x32_bf16 v[36:39], v[172:175], v[12:15], v[36:39]\n"
        "v_mfma_f32_16x16x32_bf16 v[40:43], v[176:179], v[12:15], v[40:43]\n"
        "v_mfma_f32_16x16x32_bf16 v[44:47], v[180:183], v[12:15], v[44:47]\n"
        "s_waitcnt lgkmcnt(0)\n"
        "v_mfma_f32_16x16x32_bf16 v[36:39], v[184:187], v[16:19], v[36:39]\n"
        "v_mfma_f32_16x16x32_bf16 v[40:43], v[188:191], v[16:19], v[40:43]\n"
        "v_mfma_f32_16x16x32_bf16 v[44:47], v[192:195], v[16:19], v[44:47]\n"
        "v_mfma_f32_16x16x32_bf16 v[36:39], v[196:199], v[20:23], v[36:39]\n"
        "v_mfma_f32_16x16x32_bf16 v[40:43], v[200:203], v[20:23], v[40:43]\n"
        "v_mfma_f32_16x16x32_bf16 v[44:47], v[204:207], v[20:23], v[44:47]\n"
        "s_nop 2\n"
        "v_cvt_pk_bf16_f32 v36, v36, v37\n"
        "v_cvt_pk_bf16_f32 v37, v38, v39\n"
        "v_cvt_pk_bf16_f32 v38, v40, v41\n"
        "v_cvt_pk_bf16_f32 v39, v42, v43\n"
        "buffer_store_dwordx4 v[36:39], v48, s[40:43], s72 offen offset:0 sc1\n"
        "v_cvt_pk_bf16_f32 v44, v44, v45\n"
        "v_cvt_pk_bf16_f32 v45, v46, v47\n"
        "s_nop 0\n"
        "v_permlane16_swap_b32 v44, v46\n"
        "v_permlane16_swap_b32 v45, v47\n"
        "buffer_store_dwordx4 v[44:47], v7, s[40:43], s72 offen offset:0 sc1\n"
        "s_waitcnt vmcnt(2)\n"
        "v_cmp_gt_i32 vcc, s70, v4\n"
        "s_nop 0\n"
        "s_and_b64 vcc, vcc, 3\n"
        "s_cbranch_vccnz .Lop_late1_%=\n"
        "buffer_load_dwordx4 v1, s[40:43], s71 offen offset:0 sc1 lds\n"
        "buffer_load_dwordx4 v1, s[40:43], s71 offen offset:1024 sc1 lds\n"
        "buffer_load_dwordx4 v1, s[40:43], s71 offen offset:2048 sc1 lds\n"
        "buffer_load_dwordx4 v1, s[40:43], s71 offen offset:3072 sc1 lds\n"
        "s_add_u32 m0, m0, 16384\n"
        "s_add_u32 s71, s71, 65536\n"
        "buffer_load_dwordx4 v1, s[40:43], s71 offen offset:0 sc1 lds\n"
        "buffer_load_dwordx4 v1, s[40:43], s71 offen offset:1024 sc1 lds\n"
        "buffer_load_dwordx4 v1, s[40:43], s71 offen offset:2048 sc1 lds\n"
        "buffer_load_dwordx4 v1, s[40:43], s71 offen offset:3072 sc1 lds\n"
        "s_add_u32 s69, s69, 1\n"
        "s_waitcnt vmcnt(8)\n"
        "v_mov_b32 v5, s74\n"
        "buffer_store_dword v5, v3, s[40:43], s73 offen sc1\n"
        "s_waitcnt vmcnt(5)\n"
        "s_branch .Lop_go1_%=\n"
        ".Lop_late1_%=:\n"
        "s_waitcnt vmcnt(0)\n"
        "v_mov_b32 v5, s74\n"
        "buffer_store_dword v5, v3, s[40:43], s73 offen sc1\n"
        "s_cmp_eq_u32 s69, 128\n"
        "s_cbranch_scc0 .Lop_wait1_%=\n"
        "s_endpgm\n"
        ".Lop_wait1_%=:\n"
        "s_mov_b32 s64, 0\n"
        ".Lop_in1_top_%=:\n"
        "buffer_load_dword v4, v2, s[40:43], s86 offen sc1\n"
        "s_waitcnt vmcnt(0)\n"
        "v_cmp_gt_i32 vcc, s70, v4\n"
        "s_nop 0\n"
        "s_and_b64 vcc, vcc, 3\n"
        "s_cbranch_vccz .Lop_in1_ok_%=\n"
        "s_add_u32 s64, s64, 1\n"
        "s_and_b32 s65, s64, 255\n"
        "s_cbranch_scc1 .Lop_in1_go_%=\n"
        "s_mov_b32 s65, 49152\n"
        "s_cmp_lt_u32 s64, 1048576\n"
        "s_cbranch_scc1 .Lop_in1_chk_%=\n"
        "v_mov_b32 v4, 1\n"
        "buffer_store_dword v4, off, s[40:43], s65 sc1\n"
        "s_waitcnt vmcnt(0)\n"
        ".Lop_in1_chk_%=:\n"
        "buffer_load_dword v4, off, s[40:43], s65 sc1\n"
        "s_waitcnt vmcnt(0)\n"
        "v_readfirstlane_b32 s65, v4\n"
        "s_nop 0\n"
        "s_cmp_lg_u32 s65, 1\n"
        "s_cbranch_scc1 .Lop_in1_go_%=\n"
        "s_endpgm\n"
        ".Lop_in1_go_%=:\n"
        "s_branch .Lop_in1_top_%=\n"
        ".Lop_in1_ok_%=:\n"
        "buffer_load_dwordx4 v1, s[40:43], s71 offen offset:0 sc1 lds\n"
        "buffer_load_dwordx4 v1, s[40:43], s71 offen offset:1024 sc1 lds\n"
        "buffer_load_dwordx4 v1, s[40:43], s71 offen offset:2048 sc1 lds\n"
        "buffer_load_dwordx4 v1, s[40:43], s71 offen offset:3072 sc1 lds\n"
        "s_add_u32 m0, m0, 16384\n"
        "s_add_u32 s71, s71, 65536\n"
        "buffer_load_dwordx4 v1, s[40:43], s71 offen offset:0 sc1 lds\n"
        "buffer_load_dwordx4 v1, s[40:43], s71 offen offset:1024 sc1 lds\n"
        "buffer_load_dwordx4 v1, s[40:43], s71 offen offset:2048 sc1 lds\n"
        "buffer_load_dwordx4 v1, s[40:43], s71 offen offset:3072 sc1 lds\n"
        "s_add_u32 s69, s69, 1\n"
        "s_waitcnt vmcnt(4)\n"
        ".Lop_go1_%=:\n"
        "s_mov_b32 s72, 241172480\n"
        "s_mov_b32 s83, 241172480\n"
        "s_mov_b32 s73, 241172480\n"
        "v_add_u32 v0, s78, v6\n"
        "s_barrier\n"
        "ds_read_b128 v[8:11], v0 offset:0\n"
        "ds_read_b128 v[12:15], v0 offset:1024\n"
        "ds_read_b128 v[16:19], v0 offset:2048\n"
        "ds_read_b128 v[20:23], v0 offset:3072\n"
        "s_waitcnt lgkmcnt(2)\n"
        "s_branch .Lop_i0_%=\n"
            :: "s"(p.ws), "s"(p.Wo), "s"(p.Wo), "s"(p.Wo), "s"(pair(w, delay)), "s"(pair(x4, pr)),
               "s"(pair(PAGE + spread(A_Q) + (8 * kap + w) * 16384, 65 * PAGE + spread(B_P3))), "s"(pair(flag(F_OP + fl), w * 4096)), "s"(pair(0, 0)),
               "v"(pair(lane * 16, lane * 16)), "v"(pair(lane < 2 ? flag(F_AT + (8 * kap + w + 4 * lane) * 3 + nu) : OOB, fo4)), K_ARGS
            : "v0", "v1", "v2", "v3", "v4", "v5", "v6", "v7", "v8", "v9", "v10", "v11", "v12", "v13", "v14", "v15", "v16", "v17", "v18", "v19", "v20", "v21", "v22", "v23", "v24", "v25", "v26", "v27", "v28", "v29", "v30", "v31", "v32", "v33", "v34", "v35", "v36", "v37", "v38", "v39", "v40", "v41", "v42", "v43", "v44", "v45", "v46", "v47", "v48", "v49", "v50", "v51", "v52", "v53", "v54", "v55", "v56", "v57", "v58", "v59", "v60", "v61", "v62", "v63", "v64", "v65", "v66", "v67", "v68", "v69", "v70", "v71", "v72", "v73", "v74", "v75", "v76", "v77", "v78", "v79", "v255", "a255", "s30", "s31", "s32", "s33", "s34", "s35", "s36", "s37", "s38", "s39", "s40", "s41", "s42", "s43", "s44", "s45", "s46", "s47", "s48", "s49", "s50", "s51", "s52", "s53", "s54", "s55", "s56", "s57", "s58", "s59", "s60", "s61", "s62", "s63", "s64", "s65", "s66", "s67", "s68", "s69", "s70", "s71", "s72", "s73", "s74", "s75", "s76", "s77", "s78", "s79", "s80", "s81", "s82", "s83", "s84", "s85", "s86", "s87", "s88", "s89", "s90", "s91", "s92", "s93", "s94", "s95", "s96", "s97", "s98", "s99", "vcc", "scc", "memory");
    } else {
        const unsigned hb = 65 * PAGE + spread(B_H + kap * 73728);
        asm volatile(
        "s_nop 4\n"
        "s_mov_b64 s[40:41], %0\n"
        "s_and_b32 s41, s41, 0xffff\n"
        "s_mov_b32 s42, 236978176\n"
        "s_mov_b32 s43, 0x20000\n"
        "s_mov_b64 s[48:49], %4\n"
        "s_mov_b64 s[56:57], %5\n"
        "s_mov_b64 s[58:59], %6\n"
        "s_mov_b64 s[60:61], %7\n"
        "s_mov_b64 s[62:63], %8\n"
        "v_mov_b64 v[0:1], %9\n"
        "v_mov_b64 v[2:3], %10\n"
        "v_mov_b64 v[4:5], %11\n"
        "v_mov_b64 v[6:7], %12\n"
        "v_mov_b64 v[8:9], %13\n"
        "v_mov_b64 v[10:11], %14\n"
        "v_mov_b64 v[12:13], %15\n"
        "v_mov_b64 v[14:15], %16\n"
        "s_mov_b32 s47, 0x20000\n"
        "s_mul_i32 s79, s48, 40960\n"
        ".Lf2_start_%=:\n"
        "s_sub_u32 s49, s49, 1\n"
        "s_cbranch_scc1 .Lf2_start2_%=\n"
        "s_sleep 16\n"
        "s_branch .Lf2_start_%=\n"
        ".Lf2_start2_%=:\n"
        "s_mov_b64 s[44:45], %1\n"
        "s_and_b32 s45, s45, 0xffff\n"
        "s_mov_b32 s46, 5308416\n"
        "s_add_u32 m0, s79, 0\n"
        "s_mov_b32 s65, 0\n"
        "buffer_load_dwordx4 v8, s[44:47], s65 offen lds\n"
        "buffer_load_dwordx4 v9, s[44:47], s65 offen offset:1024 lds\n"
        "buffer_load_dwordx4 v10, s[44:47], s65 offen offset:2048 lds\n"
        "s_add_u32 m0, s79, 3072\n"
        "s_mov_b32 s65, 55296\n"
        "buffer_load_dwordx4 v8, s[44:47], s65 offen lds\n"
        "buffer_load_dwordx4 v9, s[44:47], s65 offen offset:1024 lds\n"
        "buffer_load_dwordx4 v10, s[44:47], s65 offen offset:2048 lds\n"
        "s_add_u32 m0, s79, 6144\n"
        "s_mov_b32 s65, 110592\n"
        "buffer_load_dwordx4 v8, s[44:47], s65 offen lds\n"
        "buffer_load_dwordx4 v9, s[44:47], s65 offen offset:1024 lds\n"
        "buffer_load_dwordx4 v10, s[44:47], s65 offen offset:2048 lds\n"
        "s_add_u32 m0, s79, 9216\n"
        "s_mov_b32 s65, 165888\n"
        "buffer_load_dwordx4 v8, s[44:47], s65 offen lds\n"
        "buffer_load_dwordx4 v9, s[44:47], s65 offen offset:1024 lds\n"
        "buffer_load_dwordx4 v10, s[44:47], s65 offen offset:2048 lds\n"
        "s_add_u32 m0, s79, 12288\n"
        "s_mov_b32 s65, 221184\n"
        "buffer_load_dwordx4 v8, s[44:47], s65 offen lds\n"
        "buffer_load_dwordx4 v9, s[44:47], s65 offen offset:1024 lds\n"
        "buffer_load_dwordx4 v10, s[44:47], s65 offen offset:2048 lds\n"
        "s_add_u32 m0, s79, 15360\n"
        "s_mov_b32 s65, 276480\n"
        "buffer_load_dwordx4 v8, s[44:47], s65 offen lds\n"
        "buffer_load_dwordx4 v9, s[44:47], s65 offen offset:1024 lds\n"
        "buffer_load_dwordx4 v10, s[44:47], s65 offen offset:2048 lds\n"
        "s_add_u32 m0, s79, 18432\n"
        "s_mov_b32 s65, 331776\n"
        "buffer_load_dwordx4 v8, s[44:47], s65 offen lds\n"
        "buffer_load_dwordx4 v9, s[44:47], s65 offen offset:1024 lds\n"
        "buffer_load_dwordx4 v10, s[44:47], s65 offen offset:2048 lds\n"
        "s_add_u32 m0, s79, 21504\n"
        "s_mov_b32 s65, 387072\n"
        "buffer_load_dwordx4 v8, s[44:47], s65 offen lds\n"
        "buffer_load_dwordx4 v9, s[44:47], s65 offen offset:1024 lds\n"
        "buffer_load_dwordx4 v10, s[44:47], s65 offen offset:2048 lds\n"
        "s_add_u32 m0, s79, 24576\n"
        "s_mov_b32 s65, 0\n"
        "buffer_load_dwordx4 v12, s[44:47], s65 offen lds\n"
        "buffer_load_dwordx4 v13, s[44:47], s65 offen offset:1024 lds\n"
        "buffer_load_dwordx4 v14, s[44:47], s65 offen offset:2048 lds\n"
        "s_add_u32 m0, s79, 27648\n"
        "s_mov_b32 s65, 110592\n"
        "buffer_load_dwordx4 v12, s[44:47], s65 offen lds\n"
        "buffer_load_dwordx4 v13, s[44:47], s65 offen offset:1024 lds\n"
        "buffer_load_dwordx4 v14, s[44:47], s65 offen offset:2048 lds\n"
        "s_add_u32 m0, s79, 30720\n"
        "s_mov_b32 s65, 221184\n"
        "buffer_load_dwordx4 v12, s[44:47], s65 offen lds\n"
        "buffer_load_dwordx4 v13, s[44:47], s65 offen offset:1024 lds\n"
        "buffer_load_dwordx4 v14, s[44:47], s65 offen offset:2048 lds\n"
        "s_add_u32 m0, s79, 33792\n"
        "s_mov_b32 s65, 331776\n"
        "buffer_load_dwordx4 v12, s[44:47], s65 offen lds\n"
        "buffer_load_dwordx4 v13, s[44:47], s65 offen offset:1024 lds\n"
        "buffer_load_dwordx4 v14, s[44:47], s65 offen offset:2048 lds\n"
        "s_waitcnt vmcnt(33)\n"
        "ds_read_b64_tr_b16 a[0:1], v11 offset:0\n"
        "ds_read_b64_tr_b16 a[2:3], v11 offset:1536\n"
        "ds_read_b64_tr_b16 a[4:5], v11 offset:8\n"
        "ds_read_b64_tr_b16 a[6:7], v11 offset:1544\n"
        "ds_read_b64_tr_b16 a[8:9], v15 offset:0\n"
        "ds_read_b64_tr_b16 a[10:11], v15 offset:1536\n"
        "s_waitcnt vmcnt(30)\n"
        "ds_read_b64_tr_b16 a[12:13], v11 offset:3072\n"
        "ds_read_b64_tr_b16 a[14:15], v11 offset:4608\n"
        "ds_read_b64_tr_b16 a[16:17], v11 offset:3080\n"
        "ds_read_b64_tr_b16 a[18:19], v11 offset:4616\n"
        "ds_read_b64_tr_b16 a[20:21], v15 offset:3072\n"
        "ds_read_b64_tr_b16 a[22:23], v15 offset:4608\n"
        "s_waitcnt lgkmcnt(6)\n"
        "s_add_u32 m0, s79, 0\n"
        "s_mov_b32 s65, 442368\n"
        "buffer_load_dwordx4 v8, s[44:47], s65 offen lds\n"
        "buffer_load_dwordx4 v9, s[44:47], s65 offen offset:1024 lds\n"
        "buffer_load_dwordx4 v10, s[44:47], s65 offen offset:2048 lds\n"
        "s_waitcnt vmcnt(30)\n"
        "ds_read_b64_tr_b16 a[24:25], v11 offset:6144\n"
        "ds_read_b64_tr_b16 a[26:27], v11 offset:7680\n"
        "ds_read_b64_tr_b16 a[28:29], v11 offset:6152\n"
        "ds_read_b64_tr_b16 a[30:31], v11 offset:7688\n"
        "ds_read_b64_tr_b16 a[32:33], v15 offset:6144\n"
        "ds_read_b64_tr_b16 a[34:35], v15 offset:7680\n"
        "s_waitcnt lgkmcnt(6)\n"
        "s_add_u32 m0, s79, 3072\n"
        "s_mov_b32 s65, 497664\n"
        "buffer_load_dwordx4 v8, s[44:47], s65 offen lds\n"
        "buffer_load_dwordx4 v9, s[44:47], s65 offen offset:1024 lds\n"
        "buffer_load_dwordx4 v10, s[44:47], s65 offen offset:2048 lds\n"
        "s_waitcnt vmcnt(30)\n"
        "ds_read_b64_tr_b16 a[36:37], v11 offset:9216\n"
        "ds_read_b64_tr_b16 a[38:39], v11 offset:10752\n"
        "ds_read_b64_tr_b16 a[40:41], v11 offset:9224\n"
        "ds_read_b64_tr_b16 a[42:43], v11 offset:10760\n"
        "ds_read_b64_tr_b16 a[44:45], v15 offset:9216\n"
        "ds_read_b64_tr_b16 a[46:47], v15 offset:10752\n"
        "s_waitcnt lgkmcnt(6)\n"
        "s_add_u32 m0, s79, 6144\n"
        "s_mov_b32 s65, 552960\n"
        "buffer_load_dwordx4 v8, s[44:47], s65 offen lds\n"
        "buffer_load_dwordx4 v9, s[44:47], s65 offen offset:1024 lds\n"
        "buffer_load_dwordx4 v10, s[44:47], s65 offen offset:2048 lds\n"
        "s_waitcnt vmcnt(30)\n"
        "ds_read_b64_tr_b16 a[48:49], v11 offset:12288\n"
        "ds_read_b64_tr_b16 a[50:51], v11 offset:13824\n"
        "ds_read_b64_tr_b16 a[52:53], v11 offset:12296\n"
        "ds_read_b64_tr_b16 a[54:55], v11 offset:13832\n"
        "ds_read_b64_tr_b16 a[56:57], v15 offset:12288\n"
        "ds_read_b64_tr_b16 a[58:59], v15 offset:13824\n"
        "s_waitcnt lgkmcnt(6)\n"
        "s_add_u32 m0, s79, 9216\n"
        "s_mov_b32 s65, 608256\n"
        "buffer_load_dwordx4 v8, s[44:47], s65 offen lds\n"
        "buffer_load_dwordx4 v9, s[44:47], s65 offen offset:1024 lds\n"
        "buffer_load_dwordx4 v10, s[44:47], s65 offen offset:2048 lds\n"
        "s_waitcnt vmcnt(30)\n"
        "ds_read_b64_tr_b16 a[60:61], v11 offset:15360\n"
        "ds_read_b64_tr_b16 a[62:63], v11 offset:16896\n"
        "ds_read_b64_tr_b16 a[64:65], v11 offset:15368\n"
        "ds_read_b64_tr_b16 a[66:67], v11 offset:16904\n"
        "ds_read_b64_tr_b16 a[68:69], v15 offset:15360\n"
        "ds_read_b64_tr_b16 a[70:71], v15 offset:16896\n"
        "s_waitcnt lgkmcnt(6)\n"
        "s_add_u32 m0, s79, 12288\n"
        "s_mov_b32 s65, 663552\n"
        "buffer_load_dwordx4 v8, s[44:47], s65 offen lds\n"
        "buffer_load_dwordx4 v9, s[44:47], s65 offen offset:1024 lds\n"
        "buffer_load_dwordx4 v10, s[44:47], s65 offen offset:2048 lds\n"
        "s_waitcnt vmcnt(30)\n"
        "ds_read_b64_tr_b16 a[72:73], v11 offset:18432\n"
        "ds_read_b64_tr_b16 a[74:75], v11 offset:19968\n"
        "ds_read_b64_tr_b16 a[76:77], v11 offset:18440\n"
        "ds_read_b64_tr_b16 a[78:79], v11 offset:19976\n"
        "ds_read_b64_tr_b16 a[80:81], v15 offset:18432\n"
        "ds_read_b64_tr_b16 a[82:83], v15 offset:19968\n"
        "s_waitcnt lgkmcnt(6)\n"
        "s_add_u32 m0, s79, 15360\n"
        "s_mov_b32 s65, 718848\n"
        "buffer_load_dwordx4 v8, s[44:47], s65 offen lds\n"
        "buffer_load_dwordx4 v9, s[44:47], s65 offen offset:1024 lds\n"
        "buffer_load_dwordx4 v10, s[44:47], s65 offen offset:2048 lds\n"
        "s_waitcnt vmcnt(30)\n"
        "ds_read_b64_tr_b16 a[84:85], v11 offset:21504\n"
        "ds_read_b64_tr_b16 a[86:87], v11 offset:23040\n"
        "ds_read_b64_tr_b16 a[88:89], v11 offset:21512\n"
        "ds_read_b64_tr_b16 a[90:91], v11 offset:23048\n"
        "ds_read_b64_tr_b16 a[92:93], v15 offset:21504\n"
        "ds_read_b64_tr_b16 a[94:95], v15 offset:23040\n"
        "s_waitcnt lgkmcnt(6)\n"
        "s_add_u32 m0, s79, 18432\n"
        "s_mov_b32 s65, 774144\n"
        "buffer_load_dwordx4 v8, s[44:47], s65 offen lds\n"
        "buffer_load_dwordx4 v9, s[44:47], s65 offen offset:1024 lds\n"
        "buffer_load_dwordx4 v10, s[44:47], s65 offen offset:2048 lds\n"
        "s_waitcnt vmcnt(30)\n"
        "ds_read_b64_tr_b16 a[96:97], v11 offset:24576\n"
        "ds_read_b64_tr_b16 a[98:99], v11 offset:26112\n"
        "ds_read_b64_tr_b16 a[100:101], v11 offset:24584\n"
        "ds_read_b64_tr_b16 a[102:103], v11 offset:26120\n"
        "ds_read_b64_tr_b16 a[104:105], v15 offset:24576\n"
        "ds_read_b64_tr_b16 a[106:107], v15 offset:26112\n"
        "s_waitcnt lgkmcnt(6)\n"
        "s_add_u32 m0, s79, 21504\n"
        "s_mov_b32 s65, 829440\n"
        "buffer_load_dwordx4 v8, s[44:47], s65 offen lds\n"
        "buffer_load_dwordx4 v9, s[44:47], s65 offen offset:1024 lds\n"
        "buffer_load_dwordx4 v10, s[44:47], s65 offen offset:2048 lds\n"
        "s_waitcnt vmcnt(30)\n"
        "ds_read_b64_tr_b16 a[108:109], v11 offset:27648\n"
        "ds_read_b64_tr_b16 a[110:111], v11 offset:29184\n"
        "ds_read_b64_tr_b16 a[112:113], v11 offset:27656\n"
        "ds_read_b64_tr_b16 a[114:115], v11 offset:29192\n"
        "ds_read_b64_tr_b16 a[116:117], v15 offset:27648\n"
        "ds_read_b64_tr_b16 a[118:119], v15 offset:29184\n"
        "s_waitcnt lgkmcnt(6)\n"
        "s_add_u32 m0, s79, 24576\n"
        "s_mov_b32 s65, 442368\n"
        "buffer_load_dwordx4 v12, s[44:47], s65 offen lds\n"
        "buffer_load_dwordx4 v13, s[44:47], s65 offen offset:1024 lds\n"
        "buffer_load_dwordx4 v14, s[44:47], s65 offen offset:2048 lds\n"
        "s_waitcnt vmcnt(30)\n"
        "ds_read_b64_tr_b16 a[120:121], v11 offset:30720\n"
        "ds_read_b64_tr_b16 a[122:123], v11 offset:32256\n"
        "ds_read_b64_tr_b16 a[124:125], v11 offset:30728\n"
        "ds_read_b64_tr_b16 a[126:127], v11 offset:32264\n"
        "ds_read_b64_tr_b16 a[128:129], v15 offset:30720\n"
        "ds_read_b64_tr_b16 a[130:131], v15 offset:32256\n"
        "s_waitcnt lgkmcnt(6)\n"
        "s_add_u32 m0, s79, 27648\n"
        "s_mov_b32 s65, 552960\n"
        "buffer_load_dwordx4 v12, s[44:47], s65 offen lds\n"
        "buffer_load_dwordx4 v13, s[44:47], s65 offen offset:1024 lds\n"
        "buffer_load_dwordx4 v14, s[44:47], s65 offen offset:2048 lds\n"
        "s_waitcnt vmcnt(30)\n"
        "ds_read_b64_tr_b16 a[132:133], v11 offset:33792\n"
        "ds_read_b64_tr_b16 a[134:135], v11 offset:35328\n"
        "ds_read_b64_tr_b16 a[136:137], v11 offset:33800\n"
        "ds_read_b64_tr_b16 a[138:139], v11 offset:35336\n"
        "ds_read_b64_tr_b16 a[140:141], v15 offset:33792\n"
        "ds_read_b64_tr_b16 a[142:143], v15 offset:35328\n"
        "s_waitcnt lgkmcnt(6)\n"
        "s_add_u32 m0, s79, 30720\n"
        "s_mov_b32 s65, 663552\n"
        "buffer_load_dwordx4 v12, s[44:47], s65 offen lds\n"
        "buffer_load_dwordx4 v13, s[44:47], s65 offen offset:1024 lds\n"
        "buffer_load_dwordx4 v14, s[44:47], s65 offen offset:2048 lds\n"
        "s_waitcnt vmcnt(30)\n"
        "ds_read_b64_tr_b16 a[144:145], v11 offset:0\n"
        "ds_read_b64_tr_b16 a[146:147], v11 offset:1536\n"
        "ds_read_b64_tr_b16 a[148:149], v11 offset:8\n"
        "ds_read_b64_tr_b16 a[150:151], v11 offset:1544\n"
        "ds_read_b64_tr_b16 a[152:153], v15 offset:0\n"
        "ds_read_b64_tr_b16 a[154:155], v15 offset:1536\n"
        "s_waitcnt lgkmcnt(6)\n"
        "s_add_u32 m0, s79, 33792\n"
        "s_mov_b32 s65, 774144\n"
        "buffer_load_dwordx4 v12, s[44:47], s65 offen lds\n"
        "buffer_load_dwordx4 v13, s[44:47], s65 offen offset:1024 lds\n"
        "buffer_load_dwordx4 v14, s[44:47], s65 offen offset:2048 lds\n"
        "s_waitcnt vmcnt(30)\n"
        "ds_read_b64_tr_b16 a[156:157], v11 offset:3072\n"
        "ds_read_b64_tr_b16 a[158:159], v11 offset:4608\n"
        "ds_read_b64_tr_b16 a[160:161], v11 offset:3080\n"
        "ds_read_b64_tr_b16 a[162:163], v11 offset:4616\n"
        "ds_read_b64_tr_b16 a[164:165], v15 offset:3072\n"
        "ds_read_b64_tr_b16 a[166:167], v15 offset:4608\n"
        "s_waitcnt lgkmcnt(6)\n"
        "s_add_u32 m0, s79, 0\n"
        "s_mov_b32 s65, 884736\n"
        "buffer_load_dwordx4 v8, s[44:47], s65 offen lds\n"
        "buffer_load_dwordx4 v9, s[44:47], s65 offen offset:1024 lds\n"
        "buffer_load_dwordx4 v10, s[44:47], s65 offen offset:2048 lds\n"
        "s_waitcnt vmcnt(30)\n"
        "ds_read_b64_tr_b16 a[168:169], v11 offset:6144\n"
        "ds_read_b64_tr_b16 a[170:171], v11 offset:7680\n"
        "ds_read_b64_tr_b16 a[172:173], v11 offset:6152\n"
        "ds_read_b64_tr_b16 a[174:175], v11 offset:7688\n"
        "ds_read_b64_tr_b16 a[176:177], v15 offset:6144\n"
        "ds_read_b64_tr_b16 a[178:179], v15 offset:7680\n"
        "s_waitcnt lgkmcnt(6)\n"
        "s_add_u32 m0, s79, 3072\n"
        "s_mov_b32 s65, 940032\n"
        "buffer_load_dwordx4 v8, s[44:47], s65 offen lds\n"
        "buffer_load_dwordx4 v9, s[44:47], s65 offen offset:1024 lds\n"
        "buffer_load_dwordx4 v10, s[44:47], s65 offen offset:2048 lds\n"
        "s_waitcnt vmcnt(30)\n"
        "ds_read_b64_tr_b16 a[180:181], v11 offset:9216\n"
        "ds_read_b64_tr_b16 a[182:183], v11 offset:10752\n"
        "ds_read_b64_tr_b16 a[184:185], v11 offset:9224\n"
        "ds_read_b64_tr_b16 a[186:187], v11 offset:10760\n"
        "ds_read_b64_tr_b16 a[188:189], v15 offset:9216\n"
        "ds_read_b64_tr_b16 a[190:191], v15 offset:10752\n"
        "s_waitcnt lgkmcnt(6)\n"
        "s_add_u32 m0, s79, 6144\n"
        "s_mov_b32 s65, 995328\n"
        "buffer_load_dwordx4 v8, s[44:47], s65 offen lds\n"
        "buffer_load_dwordx4 v9, s[44:47], s65 offen offset:1024 lds\n"
        "buffer_load_dwordx4 v10, s[44:47], s65 offen offset:2048 lds\n"
        "s_waitcnt vmcnt(30)\n"
        "ds_read_b64_tr_b16 a[192:193], v11 offset:12288\n"
        "ds_read_b64_tr_b16 a[194:195], v11 offset:13824\n"
        "ds_read_b64_tr_b16 a[196:197], v11 offset:12296\n"
        "ds_read_b64_tr_b16 a[198:199], v11 offset:13832\n"
        "ds_read_b64_tr_b16 a[200:201], v15 offset:12288\n"
        "ds_read_b64_tr_b16 a[202:203], v15 offset:13824\n"
        "s_waitcnt lgkmcnt(6)\n"
        "s_add_u32 m0, s79, 9216\n"
        "s_mov_b32 s65, 1050624\n"
        "buffer_load_dwordx4 v8, s[44:47], s65 offen lds\n"
        "buffer_load_dwordx4 v9, s[44:47], s65 offen offset:1024 lds\n"
        "buffer_load_dwordx4 v10, s[44:47], s65 offen offset:2048 lds\n"
        "s_waitcnt vmcnt(30)\n"
        "ds_read_b64_tr_b16 a[204:205], v11 offset:15360\n"
        "ds_read_b64_tr_b16 a[206:207], v11 offset:16896\n"
        "ds_read_b64_tr_b16 a[208:209], v11 offset:15368\n"
        "ds_read_b64_tr_b16 a[210:211], v11 offset:16904\n"
        "ds_read_b64_tr_b16 a[212:213], v15 offset:15360\n"
        "ds_read_b64_tr_b16 a[214:215], v15 offset:16896\n"
        "s_waitcnt lgkmcnt(6)\n"
        "s_add_u32 m0, s79, 12288\n"
        "s_mov_b32 s65, 1105920\n"
        "buffer_load_dwordx4 v8, s[44:47], s65 offen lds\n"
        "buffer_load_dwordx4 v9, s[44:47], s65 offen offset:1024 lds\n"
        "buffer_load_dwordx4 v10, s[44:47], s65 offen offset:2048 lds\n"
        "s_waitcnt vmcnt(30)\n"
        "ds_read_b64_tr_b16 a[216:217], v11 offset:18432\n"
        "ds_read_b64_tr_b16 a[218:219], v11 offset:19968\n"
        "ds_read_b64_tr_b16 a[220:221], v11 offset:18440\n"
        "ds_read_b64_tr_b16 a[222:223], v11 offset:19976\n"
        "ds_read_b64_tr_b16 a[224:225], v15 offset:18432\n"
        "ds_read_b64_tr_b16 a[226:227], v15 offset:19968\n"
        "s_waitcnt lgkmcnt(6)\n"
        "s_add_u32 m0, s79, 15360\n"
        "s_mov_b32 s65, 1161216\n"
        "buffer_load_dwordx4 v8, s[44:47], s65 offen lds\n"
        "buffer_load_dwordx4 v9, s[44:47], s65 offen offset:1024 lds\n"
        "buffer_load_dwordx4 v10, s[44:47], s65 offen offset:2048 lds\n"
        "s_waitcnt vmcnt(30)\n"
        "ds_read_b64_tr_b16 a[228:229], v11 offset:21504\n"
        "ds_read_b64_tr_b16 a[230:231], v11 offset:23040\n"
        "ds_read_b64_tr_b16 a[232:233], v11 offset:21512\n"
        "ds_read_b64_tr_b16 a[234:235], v11 offset:23048\n"
        "ds_read_b64_tr_b16 a[236:237], v15 offset:21504\n"
        "ds_read_b64_tr_b16 a[238:239], v15 offset:23040\n"
        "s_waitcnt lgkmcnt(6)\n"
        "s_add_u32 m0, s79, 18432\n"
        "s_mov_b32 s65, 1216512\n"
        "buffer_load_dwordx4 v8, s[44:47], s65 offen lds\n"
        "buffer_load_dwordx4 v9, s[44:47], s65 offen offset:1024 lds\n"
        "buffer_load_dwordx4 v10, s[44:47], s65 offen offset:2048 lds\n"
        "s_waitcnt vmcnt(30)\n"
        "ds_read_b64_tr_b16 a[240:241], v11 offset:24576\n"
        "ds_read_b64_tr_b16 a[242:243], v11 offset:26112\n"
        "ds_read_b64_tr_b16 a[244:245], v11 offset:24584\n"
        "ds_read_b64_tr_b16 a[246:247], v11 offset:26120\n"
        "ds_read_b64_tr_b16 a[248:249], v15 offset:24576\n"
        "ds_read_b64_tr_b16 a[250:251], v15 offset:26112\n"
        "s_waitcnt lgkmcnt(6)\n"
        "s_add_u32 m0, s79, 21504\n"
        "s_mov_b32 s65, 1271808\n"
        "buffer_load_dwordx4 v8, s[44:47], s65 offen lds\n"
        "buffer_load_dwordx4 v9, s[44:47], s65 offen offset:1024 lds\n"
        "buffer_load_dwordx4 v10, s[44:47], s65 offen offset:2048 lds\n"
        "s_waitcnt vmcnt(30)\n"
        "ds_read_b64_tr_b16 a[252:253], v11 offset:27648\n"
        "ds_read_b64_tr_b16 a[254:255], v11 offset:29184\n"
        "ds_read_b64_tr_b16 v[80:81], v11 offset:27656\n"
        "ds_read_b64_tr_b16 v[82:83], v11 offset:29192\n"
        "ds_read_b64_tr_b16 v[84:85], v15 offset:27648\n"
        "ds_read_b64_tr_b16 v[86:87], v15 offset:29184\n"
        "s_waitcnt lgkmcnt(6)\n"
        "s_add_u32 m0, s79, 24576\n"
        "s_mov_b32 s65, 884736\n"
        "buffer_load_dwordx4 v12, s[44:47], s65 offen lds\n"
        "buffer_load_dwordx4 v13, s[44:47], s65 offen offset:1024 lds\n"
        "buffer_load_dwordx4 v14, s[44:47], s65 offen offset:2048 lds\n"
        "s_waitcnt vmcnt(30)\n"
        "ds_read_b64_tr_b16 v[88:89], v11 offset:30720\n"
        "ds_read_b64_tr_b16 v[90:91], v11 offset:32256\n"
        "ds_read_b64_tr_b16 v[92:93], v11 offset:30728\n"
        "ds_read_b64_tr_b16 v[94:95], v11 offset:32264\n"
        "ds_read_b64_tr_b16 v[96:97], v15 offset:30720\n"
        "ds_read_b64_tr_b16 v[98:99], v15 offset:32256\n"
        "s_waitcnt lgkmcnt(6)\n"
        "s_add_u32 m0, s79, 27648\n"
        "s_mov_b32 s65, 995328\n"
        "buffer_load_dwordx4 v12, s[44:47], s65 offen lds\n"
        "buffer_load_dwordx4 v13, s[44:47], s65 offen offset:1024 lds\n"
        "buffer_load_dwordx4 v14, s[44:47], s65 offen offset:2048 lds\n"
        "s_waitcnt vmcnt(30)\n"
        "ds_read_b64_tr_b16 v[100:101], v11 offset:33792\n"
        "ds_read_b64_tr_b16 v[102:103], v11 offset:35328\n"
        "ds_read_b64_tr_b16 v[104:105], v11 offset:33800\n"
        "ds_read_b64_tr_b16 v[106:107], v11 offset:35336\n"
        "ds_read_b64_tr_b16 v[108:109], v15 offset:33792\n"
        "ds_read_b64_tr_b16 v[110:111], v15 offset:35328\n"
        "s_waitcnt lgkmcnt(6)\n"
        "s_add_u32 m0, s79, 30720\n"
        "s_mov_b32 s65, 1105920\n"
        "buffer_load_dwordx4 v12, s[44:47], s65 offen lds\n"
        "buffer_load_dwordx4 v13, s[44:47], s65 offen offset:1024 lds\n"
        "buffer_load_dwordx4 v14, s[44:47], s65 offen offset:2048 lds\n"
        "s_waitcnt vmcnt(30)\n"
        "ds_read_b64_tr_b16 v[112:113], v11 offset:0\n"
        "ds_read_b64_tr_b16 v[114:115], v11 offset:1536\n"
        "ds_read_b64_tr_b16 v[116:117], v11 offset:8\n"
        "ds_read_b64_tr_b16 v[118:119], v11 offset:1544\n"
        "ds_read_b64_tr_b16 v[120:121], v15 offset:0\n"
        "ds_read_b64_tr_b16 v[122:123], v15 offset:1536\n"
        "s_waitcnt lgkmcnt(6)\n"
        "s_add_u32 m0, s79, 33792\n"
        "s_mov_b32 s65, 1216512\n"
        "buffer_load_dwordx4 v12, s[44:47], s65 offen lds\n"
        "buffer_load_dwordx4 v13, s[44:47], s65 offen offset:1024 lds\n"
        "buffer_load_dwordx4 v14, s[44:47], s65 offen offset:2048 lds\n"
        "s_waitcnt vmcnt(30)\n"
        "ds_read_b64_tr_b16 v[124:125], v11 offset:3072\n"
        "ds_read_b64_tr_b16 v[126:127], v11 offset:4608\n"
        "ds_read_b64_tr_b16 v[128:129], v11 offset:3080\n"
        "ds_read_b64_tr_b16 v[130:131], v11 offset:4616\n"
        "ds_read_b64_tr_b16 v[132:133], v15 offset:3072\n"
        "ds_read_b64_tr_b16 v[134:135], v15 offset:4608\n"
        "s_waitcnt vmcnt(27)\n"
        "ds_read_b64_tr_b16 v[136:137], v11 offset:6144\n"
        "ds_read_b64_tr_b16 v[138:139], v11 offset:7680\n"
        "ds_read_b64_tr_b16 v[140:141], v11 offset:6152\n"
        "ds_read_b64_tr_b16 v[142:143], v11 offset:7688\n"
        "ds_read_b64_tr_b16 v[144:145], v15 offset:6144\n"
        "ds_read_b64_tr_b16 v[146:147], v15 offset:7680\n"
        "s_waitcnt vmcnt(24)\n"
        "ds_read_b64_tr_b16 v[148:149], v11 offset:9216\n"
        "ds_read_b64_tr_b16 v[150:151], v11 offset:10752\n"
        "ds_read_b64_tr_b16 v[152:153], v11 offset:9224\n"
        "ds_read_b64_tr_b16 v[154:155], v11 offset:10760\n"
        "ds_read_b64_tr_b16 v[156:157], v15 offset:9216\n"
        "ds_read_b64_tr_b16 v[158:159], v15 offset:10752\n"
        "s_waitcnt vmcnt(21)\n"
        "ds_read_b64_tr_b16 v[160:161], v11 offset:12288\n"
        "ds_read_b64_tr_b16 v[162:163], v11 offset:13824\n"
        "ds_read_b64_tr_b16 v[164:165], v11 offset:12296\n"
        "ds_read_b64_tr_b16 v[166:167], v11 offset:13832\n"
        "ds_read_b64_tr_b16 v[168:169], v15 offset:12288\n"
        "ds_read_b64_tr_b16 v[170:171], v15 offset:13824\n"
        "s_waitcnt vmcnt(18)\n"
        "ds_read_b64_tr_b16 v[172:173], v11 offset:15360\n"
        "ds_read_b64_tr_b16 v[174:175], v11 offset:16896\n"
        "ds_read_b64_tr_b16 v[176:177], v11 offset:15368\n"
        "ds_read_b64_tr_b16 v[178:179], v11 offset:16904\n"
        "ds_read_b64_tr_b16 v[180:181], v15 offset:15360\n"
        "ds_read_b64_tr_b16 v[182:183], v15 offset:16896\n"
        "s_waitcnt vmcnt(15)\n"
        "ds_read_b64_tr_b16 v[184:185], v11 offset:18432\n"
        "ds_read_b64_tr_b16 v[186:187], v11 offset:19968\n"
        "ds_read_b64_tr_b16 v[188:189], v11 offset:18440\n"
        "ds_read_b64_tr_b16 v[190:191], v11 offset:19976\n"
        "ds_read_b64_tr_b16 v[192:193], v15 offset:18432\n"
        "ds_read_b64_tr_b16 v[194:195], v15 offset:19968\n"
        "s_waitcnt vmcnt(12)\n"
        "ds_read_b64_tr_b16 v[196:197], v11 offset:21504\n"
        "ds_read_b64_tr_b16 v[198:199], v11 offset:23040\n"
        "ds_read_b64_tr_b16 v[200:201], v11 offset:21512\n"
        "ds_read_b64_tr_b16 v[202:203], v11 offset:23048\n"
        "ds_read_b64_tr_b16 v[204:205], v15 offset:21504\n"
        "ds_read_b64_tr_b16 v[206:207], v15 offset:23040\n"
        "s_waitcnt vmcnt(9)\n"
        "ds_read_b64_tr_b16 v[208:209], v11 offset:24576\n"
        "ds_read_b64_tr_b16 v[210:211], v11 offset:26112\n"
        "ds_read_b64_tr_b16 v[212:213], v11 offset:24584\n"
        "ds_read_b64_tr_b16 v[214:215], v11 offset:26120\n"
        "ds_read_b64_tr_b16 v[216:217], v15 offset:24576\n"
        "ds_read_b64_tr_b16 v[218:219], v15 offset:26112\n"
        "s_waitcnt vmcnt(6)\n"
        "ds_read_b64_tr_b16 v[220:221], v11 offset:27648\n"
        "ds_read_b64_tr_b16 v[222:223], v11 offset:29184\n"
        "ds_read_b64_tr_b16 v[224:225], v11 offset:27656\n"
        "ds_read_b64_tr_b16 v[226:227], v11 offset:29192\n"
        "ds_read_b64_tr_b16 v[228:229], v15 offset:27648\n"
        "ds_read_b64_tr_b16 v[230:231], v15 offset:29184\n"
        "s_waitcnt vmcnt(3)\n"
        "ds_read_b64_tr_b16 v[232:233], v11 offset:30720\n"
        "ds_read_b64_tr_b16 v[234:235], v11 offset:32256\n"
        "ds_read_b64_tr_b16 v[236:237], v11 offset:30728\n"
        "ds_read_b64_tr_b16 v[238:239], v11 offset:32264\n"
        "ds_read_b64_tr_b16 v[240:241], v15 offset:30720\n"
        "ds_read_b64_tr_b16 v[242:243], v15 offset:32256\n"
        "s_waitcnt vmcnt(0)\n"
        "ds_read_b64_tr_b16 v[244:245], v11 offset:33792\n"
        "ds_read_b64_tr_b16 v[246:247], v11 offset:35328\n"
        "ds_read_b64_tr_b16 v[248:249], v11 offset:33800\n"
        "ds_read_b64_tr_b16 v[250:251], v11 offset:35336\n"
        "ds_read_b64_tr_b16 v[252:253], v15 offset:33792\n"
        "ds_read_b64_tr_b16 v[254:255], v15 offset:35328\n"
        "s_waitcnt lgkmcnt(0)\n"
        "v_lshrrev_b32 v20, 4, v1\n"
        "s_mov_b32 s50, s56\n"
        "v_lshlrev_b32 v22, 7, v20\n"
        "s_sub_u32 s66, 0, s50\n"
        "s_mov_b32 s52, 32768\n"
        "s_mov_b32 s64, 0\n"
        ".Lf2_pg0_top_%=:\n"
        "buffer_load_dword v21, v22, s[40:43], s52 offen sc1\n"
        "s_waitcnt vmcnt(0)\n"
        "v_cmp_gt_i32 vcc, 0, v21\n"
        "s_cbranch_vccz .Lf2_pg0_ok_%=\n"
        "s_add_u32 s64, s64, 1\n"
        "s_and_b32 s65, s64, 255\n"
        "s_cbranch_scc1 .Lf2_pg0_go_%=\n"
        "s_mov_b32 s65, 49152\n"
        "s_cmp_lt_u32 s64, 1048576\n"
        "s_cbranch_scc1 .Lf2_pg0_chk_%=\n"
        "v_mov_b32 v21, 1\n"
        "buffer_store_dword v21, off, s[40:43], s65 sc1\n"
        "s_waitcnt vmcnt(0)\n"
        ".Lf2_pg0_chk_%=:\n"
        "buffer_load_dword v21, off, s[40:43], s65 sc1\n"
        "s_waitcnt vmcnt(0)\n"
        "v_readfirstlane_b32 s65, v21\n"
        "s_nop 0\n"
        "s_cmp_lg_u32 s65, 1\n"
        "s_cbranch_scc1 .Lf2_pg0_go_%=\n"
        "s_endpgm\n"
        ".Lf2_pg0_go_%=:\n"
        "s_sleep 2\n"
        "s_branch .Lf2_pg0_top_%=\n"
        ".Lf2_pg0_ok_%=:\n"
        "v_cmp_eq_u32 s[52:53], 1, v21\n"
        "s_nop 1\n"
        "s_mov_b32 s54, 40960\n"
        "s_mov_b32 s64, 0\n"
        ".Lf2_pg1_top_%=:\n"
        "buffer_load_dword v21, v22, s[40:43], s54 offen sc1\n"
        "s_waitcnt vmcnt(0)\n"
        "v_cmp_gt_i32 vcc, 0, v21\n"
        "s_cbranch_vccz .Lf2_pg1_ok_%=\n"
        "s_add_u32 s64, s64, 1\n"
        "s_and_b32 s65, s64, 255\n"
        "s_cbranch_scc1 .Lf2_pg1_go_%=\n"
        "s_mov_b32 s65, 49152\n"
        "s_cmp_lt_u32 s64, 1048576\n"
        "s_cbranch_scc1 .Lf2_pg1_chk_%=\n"
        "v_mov_b32 v21, 1\n"
        "buffer_store_dword v21, off, s[40:43], s65 sc1\n"
        "s_waitcnt vmcnt(0)\n"
        ".Lf2_pg1_chk_%=:\n"
        "buffer_load_dword v21, off, s[40:43], s65 sc1\n"
        "s_waitcnt vmcnt(0)\n"
        "v_readfirstlane_b32 s65, v21\n"
        "s_nop 0\n"
        "s_cmp_lg_u32 s65, 1\n"
        "s_cbranch_scc1 .Lf2_pg1_go_%=\n"
        "s_endpgm\n"
        ".Lf2_pg1_go_%=:\n"
        "s_sleep 2\n"
        "s_branch .Lf2_pg1_top_%=\n"
        ".Lf2_pg1_ok_%=:\n"
        "v_cmp_eq_u32 s[54:55], 1, v21\n"
        "s_nop 1\n"
        "s_mov_b32 vcc_lo, 0x55555555\n"
        "s_mov_b32 vcc_hi, 0x55555555\n"
        "s_xor_b64 vcc, vcc, s[52:53]\n"
        "s_bcnt1_i32_b64 s65, vcc\n"
        "s_sub_u32 s64, s65, 23\n"
        "s_cmp_lt_u32 s64, 19\n"
        "s_cbranch_scc1 .Lf2_rawmap_%=\n"
        "s_cmp_lt_u32 s65, 32\n"
        "s_cselect_b32 s65, 0, -1\n"
        "s_xor_b32 s52, s65, 0x55555555\n"
        "s_xor_b32 s53, s65, 0x55555555\n"
        "s_xor_b32 s54, s65, 0x55555555\n"
        "s_xor_b32 s55, s65, 0xaaaaaaaa\n"
        ".Lf2_rawmap_%=:\n"
        "s_xor_b32 s52, s52, s66\n"
        "s_xor_b32 s53, s53, s66\n"
        "s_xor_b32 s54, s54, s66\n"
        "s_xor_b32 s55, s55, s66\n"
        "s_and_b32 s76, s55, 1\n"
        "s_lshl_b32 s76, s76, 13\n"
        "s_add_u32 s60, s60, s76\n"
        "s_barrier\n"
        "v_mov_b32 v48, v6\n"
        "s_mov_b32 s68, -1\n"
        "s_mov_b32 s69, 0\n"
        "s_mov_b32 s78, 0\n"
        "s_mov_b32 s74, -1\n"
        "s_mov_b32 s86, s76\n"
        "s_lshr_b32 s85, s61, 16\n"
        "s_and_b32 s61, s61, 0xffff\n"
        "s_mov_b32 s89, 241172480\n"
        "s_mov_b32 s84, 241172480\n"
        "v_mov_b32 v6, v0\n"
        "s_mov_b32 s80, 0xbfb8aa3b\n"
        "s_lshr_b32 s67, s69, 1\n"
        "s_lshr_b32 s65, s67, 1\n"
        "s_bitcmp1_b32 s54, s65\n"
        "s_cselect_b32 s65, 8192, 0\n"
        "s_lshl_b32 s77, s67, 20\n"
        "s_add_u32 s77, s77, s65\n"
        "s_add_u32 s71, s77, s58\n"
        "s_add_u32 s81, s77, s62\n"
        "s_add_u32 s78, s78, 36864\n"
        "s_cmp_eq_u32 s78, 110592\n"
        "s_cselect_b32 s78, 0, s78\n"
        "s_add_u32 m0, s78, s61\n"
        "s_lshr_b32 s70, s69, 0\n"
        "s_branch .Lf2_wait1_%=\n"
        ".Lf2_i0_%=:\n"
        "v_mfma_f32_16x16x32_bf16 v[24:27], a[0:3], v[8:11], 0\n"
        "v_mfma_f32_16x16x32_bf16 v[28:31], a[4:7], v[8:11], 0\n"
        "s_add_u32 s71, s77, s58\n"
        "buffer_load_dword v4, v2, s[40:43], s86 offen sc1\n"
        "s_add_u32 s71, s71, 49152\n"
        "v_mfma_f32_16x16x32_bf16 v[32:35], a[8:11], v[8:11], 0\n"
        "s_add_u32 s81, s77, s62\n"
        "ds_read_b128 v[8:11], v0 offset:4096\n"
        "s_add_u32 s81, s81, 4096\n"
        "v_mfma_f32_16x16x32_bf16 v[24:27], a[12:15], v[12:15], v[24:27]\n"
        "s_add_u32 s78, s78, 36864\n"
        "s_cmp_eq_u32 s78, 110592\n"
        "s_cselect_b32 s78, 0, s78\n"
        "v_mfma_f32_16x16x32_bf16 v[28:31], a[16:19], v[12:15], v[28:31]\n"
        "s_add_u32 m0, s78, s61\n"
        "s_lshr_b32 s70, s69, 0\n"
        "v_cvt_pk_bf16_f32 v36, v36, v37\n"
        "v_mfma_f32_16x16x32_bf16 v[32:35], a[20:23], v[12:15], v[32:35]\n"
        "s_waitcnt lgkmcnt(1)\n"
        "ds_read_b128 v[12:15], v0 offset:5120\n"
        "v_cvt_pk_bf16_f32 v37, v38, v39\n"
        "v_mfma_f32_16x16x32_bf16 v[24:27], a[24:27], v[16:19], v[24:27]\n"
        "v_cvt_pk_bf16_f32 v38, v40, v41\n"
        "v_cvt_pk_bf16_f32 v39, v42, v43\n"
        "v_mfma_f32_16x16x32_bf16 v[28:31], a[28:31], v[16:19], v[28:31]\n"
        "buffer_store_dwordx4 v[36:39], v48, s[40:43], s72 offen offset:0 sc1\n"
        "v_mfma_f32_16x16x32_bf16 v[32:35], a[32:35], v[16:19], v[32:35]\n"
        "ds_read_b128 v[16:19], v0 offset:6144\n"
        "v_cvt_pk_bf16_f32 v44, v44, v45\n"
        "v_mfma_f32_16x16x32_bf16 v[24:27], a[36:39], v[20:23], v[24:27]\n"
        "v_cvt_pk_bf16_f32 v45, v46, v47\n"
        "s_nop 0\n"
        "v_mfma_f32_16x16x32_bf16 v[28:31], a[40:43], v[20:23], v[28:31]\n"
        "v_permlane16_swap_b32 v44, v46\n"
        "v_mfma_f32_16x16x32_bf16 v[32:35], a[44:47], v[20:23], v[32:35]\n"
        "s_waitcnt lgkmcnt(1)\n"
        "ds_read_b128 v[20:23], v0 offset:7168\n"
        "v_mfma_f32_16x16x32_bf16 v[24:27], a[48:51], v[8:11], v[24:27]\n"
        "v_permlane16_swap_b32 v45, v47\n"
        "v_mfma_f32_16x16x32_bf16 v[28:31], a[52:55], v[8:11], v[28:31]\n"
        "v_mfma_f32_16x16x32_bf16 v[32:35], a[56:59], v[8:11], v[32:35]\n"
        "ds_read_b128 v[8:11], v0 offset:8192\n"
        "v_mfma_f32_16x16x32_bf16 v[24:27], a[60:63], v[12:15], v[24:27]\n"
        "buffer_store_dwordx4 v[44:47], v7, s[40:43], s72 offen offset:0 sc1\n"
        "v_mfma_f32_16x16x32_bf16 v[28:31], a[64:67], v[12:15], v[28:31]\n"
        "v_mfma_f32_16x16x32_bf16 v[32:35], a[68:71], v[12:15], v[32:35]\n"
        "s_waitcnt lgkmcnt(1)\n"
        "ds_read_b128 v[12:15], v0 offset:9216\n"
        "v_mfma_f32_16x16x32_bf16 v[24:27], a[72:75], v[16:19], v[24:27]\n"
        "v_mfma_f32_16x16x32_bf16 v[28:31], a[76:79], v[16:19], v[28:31]\n"
        "v_mfma_f32_16x16x32_bf16 v[32:35], a[80:83], v[16:19], v[32:35]\n"
        "ds_read_b128 v[16:19], v0 offset:10240\n"
        "v_mfma_f32_16x16x32_bf16 v[24:27], a[84:87], v[20:23], v[24:27]\n"
        "v_mfma_f32_16x16x32_bf16 v[28:31], a[88:91], v[20:23], v[28:31]\n"
        "v_mfma_f32_16x16x32_bf16 v[32:35], a[92:95], v[20:23], v[32:35]\n"
        "s_waitcnt lgkmcnt(1)\n"
        "ds_read_b128 v[20:23], v0 offset:11264\n"
        "v_mfma_f32_16x16x32_bf16 v[24:27], a[96:99], v[8:11], v[24:27]\n"
        "v_mfma_f32_16x16x32_bf16 v[28:31], a[100:103], v[8:11], v[28:31]\n"
        "s_waitcnt vmcnt(3)\n"
        "s_barrier\n"
        "v_mfma_f32_16x16x32_bf16 v[32:35], a[104:107], v[8:11], v[32:35]\n"
        "ds_read_b128 v[8:11], v0 offset:12288\n"
        "buffer_store_dword v5, v3, s[40:43], s84 offen sc1\n"
        "v_mfma_f32_16x16x32_bf16 v[24:27], a[108:111], v[12:15], v[24:27]\n"
        "v_mov_b32 v5, s74\n"
        "v_mfma_f32_16x16x32_bf16 v[28:31], a[112:115], v[12:15], v[28:31]\n"
        "v_mfma_f32_16x16x32_bf16 v[32:35], a[116:119], v[12:15], v[32:35]\n"
        "s_waitcnt lgkmcnt(1)\n"
        "ds_read_b128 v[12:15], v0 offset:13312\n"
        "v_mfma_f32_16x16x32_bf16 v[24:27], a[120:123], v[16:19], v[24:27]\n"
        "v_mfma_f32_16x16x32_bf16 v[28:31], a[124:127], v[16:19], v[28:31]\n"
        "s_waitcnt vmcnt(3)\n"
        "v_cmp_gt_i32 vcc, s70, v4\n"
        "v_mfma_f32_16x16x32_bf16 v[32:35], a[128:131], v[16:19], v[32:35]\n"
        "ds_read_b128 v[16:19], v0 offset:14336\n"
        "v_mfma_f32_16x16x32_bf16 v[24:27], a[132:135], v[20:23], v[24:27]\n"
        "s_and_b64 vcc, vcc, 63\n"
        "v_mfma_f32_16x16x32_bf16 v[28:31], a[136:139], v[20:23], v[28:31]\n"
        "s_cbranch_vccnz .Lf2_i00_%=\n"
        "v_mfma_f32_16x16x32_bf16 v[32:35], a[140:143], v[20:23], v[32:35]\n"
        "s_waitcnt lgkmcnt(1)\n"
        "ds_read_b128 v[20:23], v0 offset:15360\n"
        "buffer_load_dwordx4 v1, s[40:43], s71 offen offset:0 sc1 lds\n"
        "v_mfma_f32_16x16x32_bf16 v[24:27], a[144:147], v[8:11], v[24:27]\n"
        "v_mfma_f32_16x16x32_bf16 v[28:31], a[148:151], v[8:11], v[28:31]\n"
        "v_mfma_f32_16x16x32_bf16 v[32:35], a[152:155], v[8:11], v[32:35]\n"
        "ds_read_b128 v[8:11], v0 offset:16384\n"
        "v_mfma_f32_16x16x32_bf16 v[24:27], a[156:159], v[12:15], v[24:27]\n"
        "s_getreg_b32 s88, hwreg(HW_REG_IB_STS, 0, 4)\n"
        "buffer_load_dwordx4 v1, s[40:43], s71 offen offset:1024 sc1 lds\n"
        "s_add_u32 m0, m0, s85\n"
        "v_mfma_f32_16x16x32_bf16 v[28:31], a[160:163], v[12:15], v[28:31]\n"
        "s_cmp_le_u32 s88, 2\n"
        "s_cselect_b32 s88, s73, s89\n"
        "s_cselect_b32 s73, s89, s73\n"
        "v_mfma_f32_16x16x32_bf16 v[32:35], a[164:167], v[12:15], v[32:35]\n"
        "s_waitcnt lgkmcnt(1)\n"
        "ds_read_b128 v[12:15], v0 offset:17408\n"
        "buffer_store_dword v5, v3, s[40:43], s88 offen sc1\n"
        "v_mfma_f32_16x16x32_bf16 v[24:27], a[168:171], v[16:19], v[24:27]\n"
        "s_add_u32 s71, s71, 16384\n"
        "v_mfma_f32_16x16x32_bf16 v[28:31], a[172:175], v[16:19], v[28:31]\n"
        "buffer_load_dwordx4 v1, s[40:43], s81 offen offset:0 sc1 lds\n"
        "s_sub_u32 m0, m0, s85\n"
        "v_mfma_f32_16x16x32_bf16 v[32:35], a[176:179], v[16:19], v[32:35]\n"
        "s_add_u32 m0, m0, 12288\n"
        "ds_read_b128 v[16:19], v0 offset:18432\n"
        "s_add_u32 s81, s81, 16384\n"
        "v_mfma_f32_16x16x32_bf16 v[24:27], a[180:183], v[20:23], v[24:27]\n"
        "v_mfma_f32_16x16x32_bf16 v[28:31], a[184:187], v[20:23], v[28:31]\n"
        "v_mfma_f32_16x16x32_bf16 v[32:35], a[188:191], v[20:23], v[32:35]\n"
        "s_waitcnt lgkmcnt(1)\n"
        "ds_read_b128 v[20:23], v0 offset:19456\n"
        "buffer_load_dwordx4 v1, s[40:43], s71 offen offset:0 sc1 lds\n"
        "v_mfma_f32_16x16x32_bf16 v[24:27], a[192:195], v[8:11], v[24:27]\n"
        "s_getreg_b32 s88, hwreg(HW_REG_IB_STS, 0, 4)\n"
        "v_mfma_f32_16x16x32_bf16 v[28:31], a[196:199], v[8:11], v[28:31]\n"
        "s_cmp_le_u32 s88, 6\n"
        "s_cselect_b32 s88, s73, s89\n"
        "s_cselect_b32 s73, s89, s73\n"
        "v_mfma_f32_16x16x32_bf16 v[32:35], a[200:203], v[8:11], v[32:35]\n"
        "ds_read_b128 v[8:11], v0 offset:20480\n"
        "buffer_store_dword v5, v3, s[40:43], s88 offen sc1\n"
        "v_mfma_f32_16x16x32_bf16 v[24:27], a[204:207], v[12:15], v[24:27]\n"
        "buffer_load_dwordx4 v1, s[40:43], s71 offen offset:1024 sc1 lds\n"
        "s_add_u32 m0, m0, s85\n"
        "v_mfma_f32_16x16x32_bf16 v[28:31], a[208:211], v[12:15], v[28:31]\n"
        "s_add_u32 s71, s71, 16384\n"
        "v_mfma_f32_16x16x32_bf16 v[32:35], a[212:215], v[12:15], v[32:35]\n"
        "s_waitcnt lgkmcnt(1)\n"
        "ds_read_b128 v[12:15], v0 offset:21504\n"
        "v_mfma_f32_16x16x32_bf16 v[24:27], a[216:219], v[16:19], v[24:27]\n"
        "v_mfma_f32_16x16x32_bf16 v[28:31], a[220:223], v[16:19], v[28:31]\n"
        "s_getreg_b32 s88, hwreg(HW_REG_IB_STS, 0, 4)\n"
        "buffer_load_dwordx4 v1, s[40:43], s81 offen offset:0 sc1 lds\n"
        "s_sub_u32 m0, m0, s85\n"
        "v_mfma_f32_16x16x32_bf16 v[32:35], a[224:227], v[16:19], v[32:35]\n"
        "s_add_u32 m0, m0, 12288\n"
        "ds_read_b128 v[16:19], v0 offset:22528\n"
        "s_add_u32 s81, s81, 16384\n"
        "v_mfma_f32_16x16x32_bf16 v[24:27], a[228:231], v[20:23], v[24:27]\n"
        "s_cmp_le_u32 s88, 8\n"
        "s_cselect_b32 s88, s73, s89\n"
        "s_cselect_b32 s73, s89, s73\n"
        "v_mfma_f32_16x16x32_bf16 v[28:31], a[232:235], v[20:23], v[28:31]\n"
        "buffer_store_dword v5, v3, s[40:43], s88 offen sc1\n"
        "v_mfma_f32_16x16x32_bf16 v[32:35], a[236:239], v[20:23], v[32:35]\n"
        "s_waitcnt lgkmcnt(1)\n"
        "ds_read_b128 v[20:23], v0 offset:23552\n"
        "buffer_load_dwordx4 v1, s[40:43], s71 offen offset:0 sc1 lds\n"
        "v_mfma_f32_16x16x32_bf16 v[24:27], a[240:243], v[8:11], v[24:27]\n"
        "v_mfma_f32_16x16x32_bf16 v[28:31], a[244:247], v[8:11], v[28:31]\n"
        "v_mfma_f32_16x16x32_bf16 v[32:35], a[248:251], v[8:11], v[32:35]\n"
        "ds_read_b128 v[8:11], v0 offset:24576\n"
        "v_mfma_f32_16x16x32_bf16 v[24:27], a[252:255], v[12:15], v[24:27]\n"
        "buffer_load_dwordx4 v1, s[40:43], s71 offen offset:1024 sc1 lds\n"
        "s_add_u32 m0, m0, s85\n"
        "v_mfma_f32_16x16x32_bf16 v[28:31], v[80:83], v[12:15], v[28:31]\n"
        "s_add_u32 s71, s71, 16384\n"
        "v_mfma_f32_16x16x32_bf16 v[32:35], v[84:87], v[12:15], v[32:35]\n"
        "s_waitcnt lgkmcnt(1)\n"
        "ds_read_b128 v[12:15], v0 offset:25600\n"
        "v_mfma_f32_16x16x32_bf16 v[24:27], v[88:91], v[16:19], v[24:27]\n"
        "v_mfma_f32_16x16x32_bf16 v[28:31], v[92:95], v[16:19], v[28:31]\n"
        "buffer_load_dwordx4 v1, s[40:43], s81 offen offset:0 sc1 lds\n"
        "s_add_u32 s69, s69, 1\n"
        "v_mfma_f32_16x16x32_bf16 v[32:35], v[96:99], v[16:19], v[32:35]\n"
        "ds_read_b128 v[16:19], v0 offset:26624\n"
        "v_mfma_f32_16x16x32_bf16 v[24:27], v[100:103], v[20:23], v[24:27]\n"
        "s_waitcnt vmcnt(13)\n"
        "buffer_store_dword v5, v3, s[40:43], s73 offen sc1\n"
        "s_add_u32 s74, s74, 1\n"
        "v_mfma_f32_16x16x32_bf16 v[28:31], v[104:107], v[20:23], v[28:31]\n"
        "s_mov_b32 s84, s89\n"
        "s_mov_b32 s73, s60\n"
        "s_add_u32 s68, s68, 1\n"
        "v_mfma_f32_16x16x32_bf16 v[32:35], v[108:111], v[20:23], v[32:35]\n"
        "s_waitcnt lgkmcnt(1)\n"
        "ds_read_b128 v[20:23], v0 offset:27648\n"
        "s_lshr_b32 s66, s68, 2\n"
        "v_mfma_f32_16x16x32_bf16 v[24:27], v[112:115], v[8:11], v[24:27]\n"
        "s_add_u32 s66, s66, 1\n"
        "s_bitcmp1_b32 s55, s66\n"
        "s_cselect_b32 s66, 8192, 0\n"
        "v_mfma_f32_16x16x32_bf16 v[28:31], v[116:119], v[8:11], v[28:31]\n"
        "s_lshl_b32 s75, s68, 19\n"
        "s_add_u32 s75, s75, s66\n"
        "s_add_u32 s72, s75, s59\n"
        "v_mfma_f32_16x16x32_bf16 v[32:35], v[120:123], v[8:11], v[32:35]\n"
        "ds_read_b128 v[8:11], v0 offset:28672\n"
        "v_mfma_f32_16x16x32_bf16 v[24:27], v[124:127], v[12:15], v[24:27]\n"
        "v_mfma_f32_16x16x32_bf16 v[28:31], v[128:131], v[12:15], v[28:31]\n"
        "v_mfma_f32_16x16x32_bf16 v[32:35], v[132:135], v[12:15], v[32:35]\n"
        "s_waitcnt lgkmcnt(1)\n"
        "ds_read_b128 v[12:15], v0 offset:29696\n"
        "v_mfma_f32_16x16x32_bf16 v[24:27], v[136:139], v[16:19], v[24:27]\n"
        "v_mfma_f32_16x16x32_bf16 v[28:31], v[140:143], v[16:19], v[28:31]\n"
        "v_mfma_f32_16x16x32_bf16 v[32:35], v[144:147], v[16:19], v[32:35]\n"
        "ds_read_b128 v[16:19], v0 offset:30720\n"
        "v_mfma_f32_16x16x32_bf16 v[24:27], v[148:151], v[20:23], v[24:27]\n"
        "v_mfma_f32_16x16x32_bf16 v[28:31], v[152:155], v[20:23], v[28:31]\n"
        "v_mfma_f32_16x16x32_bf16 v[32:35], v[156:159], v[20:23], v[32:35]\n"
        "s_waitcnt lgkmcnt(1)\n"
        "ds_read_b128 v[20:23], v0 offset:31744\n"
        "v_mfma_f32_16x16x32_bf16 v[24:27], v[160:163], v[8:11], v[24:27]\n"
        "v_mfma_f32_16x16x32_bf16 v[28:31], v[164:167], v[8:11], v[28:31]\n"
        "v_mfma_f32_16x16x32_bf16 v[32:35], v[168:171], v[8:11], v[32:35]\n"
        "ds_read_b128 v[8:11], v0 offset:32768\n"
        "v_mfma_f32_16x16x32_bf16 v[24:27], v[172:175], v[12:15], v[24:27]\n"
        "v_mfma_f32_16x16x32_bf16 v[28:31], v[176:179], v[12:15], v[28:31]\n"
        "v_mfma_f32_16x16x32_bf16 v[32:35], v[180:183], v[12:15], v[32:35]\n"
        "s_waitcnt lgkmcnt(1)\n"
        "ds_read_b128 v[12:15], v0 offset:33792\n"
        "v_mfma_f32_16x16x32_bf16 v[24:27], v[184:187], v[16:19], v[24:27]\n"
        "v_mfma_f32_16x16x32_bf16 v[28:31], v[188:191], v[16:19], v[28:31]\n"
        "v_mfma_f32_16x16x32_bf16 v[32:35], v[192:195], v[16:19], v[32:35]\n"
        "ds_read_b128 v[16:19], v0 offset:34816\n"
        "v_mfma_f32_16x16x32_bf16 v[24:27], v[196:199], v[20:23], v[24:27]\n"
        "v_mfma_f32_16x16x32_bf16 v[28:31], v[200:203], v[20:23], v[28:31]\n"
        "v_mfma_f32_16x16x32_bf16 v[32:35], v[204:207], v[20:23], v[32:35]\n"
        "s_waitcnt lgkmcnt(1)\n"
        "ds_read_b128 v[20:23], v0 offset:35840\n"
        "v_mfma_f32_16x16x32_bf16 v[24:27], v[208:211], v[8:11], v[24:27]\n"
        "v_mfma_f32_16x16x32_bf16 v[28:31], v[212:215], v[8:11], v[28:31]\n"
        "s_waitcnt vmcnt(9)\n"
        "s_barrier\n"
        "v_add_u32 v0, s78, v6\n"
        "v_mfma_f32_16x16x32_bf16 v[32:35], v[216:219], v[8:11], v[32:35]\n"
        "ds_read_b128 v[8:11], v0 offset:0\n"
        "v_mfma_f32_16x16x32_bf16 v[24:27], v[220:223], v[12:15], v[24:27]\n"
        "v_mfma_f32_16x16x32_bf16 v[28:31], v[224:227], v[12:15], v[28:31]\n"
        "v_mfma_f32_16x16x32_bf16 v[32:35], v[228:231], v[12:15], v[32:35]\n"
        "s_waitcnt lgkmcnt(1)\n"
        "ds_read_b128 v[12:15], v0 offset:1024\n"
        "v_mfma_f32_16x16x32_bf16 v[24:27], v[232:235], v[16:19], v[24:27]\n"
        "v_mfma_f32_16x16x32_bf16 v[28:31], v[236:239], v[16:19], v[28:31]\n"
        "v_mfma_f32_16x16x32_bf16 v[32:35], v[240:243], v[16:19], v[32:35]\n"
        "ds_read_b128 v[16:19], v0 offset:2048\n"
        "v_mfma_f32_16x16x32_bf16 v[24:27], v[244:247], v[20:23], v[24:27]\n"
        "v_mfma_f32_16x16x32_bf16 v[28:31], v[248:251], v[20:23], v[28:31]\n"
        "v_mfma_f32_16x16x32_bf16 v[32:35], v[252:255], v[20:23], v[32:35]\n"
        "s_waitcnt lgkmcnt(1)\n"
        "ds_read_b128 v[20:23], v0 offset:3072\n"
        "s_branch .Lf2_i1_%=\n"
        ".Lf2_i00_%=:\n"
        "v_mfma_f32_16x16x32_bf16 v[32:35], a[140:143], v[20:23], v[32:35]\n"
        "s_waitcnt lgkmcnt(1)\n"
        "ds_read_b128 v[20:23], v0 offset:15360\n"
        "buffer_load_dword v4, v2, s[40:43], s86 offen sc1\n"
        "v_mfma_f32_16x16x32_bf16 v[24:27], a[144:147], v[8:11], v[24:27]\n"
        "v_mfma_f32_16x16x32_bf16 v[28:31], a[148:151], v[8:11], v[28:31]\n"
        "v_mfma_f32_16x16x32_bf16 v[32:35], a[152:155], v[8:11], v[32:35]\n"
        "ds_read_b128 v[8:11], v0 offset:16384\n"
        "v_mfma_f32_16x16x32_bf16 v[24:27], a[156:159], v[12:15], v[24:27]\n"
        "s_getreg_b32 s88, hwreg(HW_REG_IB_STS, 0, 4)\n"
        "v_mfma_f32_16x16x32_bf16 v[28:31], a[160:163], v[12:15], v[28:31]\n"
        "s_cmp_le_u32 s88, 2\n"
        "s_cselect_b32 s88, s73, s89\n"
        "s_cselect_b32 s73, s89, s73\n"
        "v_mfma_f32_16x16x32_bf16 v[32:35], a[164:167], v[12:15], v[32:35]\n"
        "s_waitcnt lgkmcnt(1)\n"
        "ds_read_b128 v[12:15], v0 offset:17408\n"
        "buffer_store_dword v5, v3, s[40:43], s88 offen sc1\n"
        "v_mfma_f32_16x16x32_bf16 v[24:27], a[168:171], v[16:19], v[24:27]\n"
        "v_mfma_f32_16x16x32_bf16 v[28:31], a[172:175], v[16:19], v[28:31]\n"
        "v_mfma_f32_16x16x32_bf16 v[32:35], a[176:179], v[16:19], v[32:35]\n"
        "ds_read_b128 v[16:19], v0 offset:18432\n"
        "v_mfma_f32_16x16x32_bf16 v[24:27], a[180:183], v[20:23], v[24:27]\n"
        "v_mfma_f32_16x16x32_bf16 v[28:31], a[184:187], v[20:23], v[28:31]\n"
        "v_mfma_f32_16x16x32_bf16 v[32:35], a[188:191], v[20:23], v[32:35]\n"
        "s_waitcnt lgkmcnt(1)\n"
        "ds_read_b128 v[20:23], v0 offset:19456\n"
        "v_mfma_f32_16x16x32_bf16 v[24:27], a[192:195], v[8:11], v[24:27]\n"
        "s_getreg_b32 s88, hwreg(HW_REG_IB_STS, 0, 4)\n"
        "v_mfma_f32_16x16x32_bf16 v[28:31], a[196:199], v[8:11], v[28:31]\n"
        "s_cmp_le_u32 s88, 3\n"
        "s_cselect_b32 s88, s73, s89\n"
        "s_cselect_b32 s73, s89, s73\n"
        "v_mfma_f32_16x16x32_bf16 v[32:35], a[200:203], v[8:11], v[32:35]\n"
        "ds_read_b128 v[8:11], v0 offset:20480\n"
        "buffer_store_dword v5, v3, s[40:43], s88 offen sc1\n"
        "v_mfma_f32_16x16x32_bf16 v[24:27], a[204:207], v[12:15], v[24:27]\n"
        "v_mfma_f32_16x16x32_bf16 v[28:31], a[208:211], v[12:15], v[28:31]\n"
        "v_mfma_f32_16x16x32_bf16 v[32:35], a[212:215], v[12:15], v[32:35]\n"
        "s_waitcnt lgkmcnt(1)\n"
        "ds_read_b128 v[12:15], v0 offset:21504\n"
        "v_mfma_f32_16x16x32_bf16 v[24:27], a[216:219], v[16:19], v[24:27]\n"
        "v_mfma_f32_16x16x32_bf16 v[28:31], a[220:223], v[16:19], v[28:31]\n"
        "s_getreg_b32 s88, hwreg(HW_REG_IB_STS, 0, 4)\n"
        "v_mfma_f32_16x16x32_bf16 v[32:35], a[224:227], v[16:19], v[32:35]\n"
        "ds_read_b128 v[16:19], v0 offset:22528\n"
        "v_mfma_f32_16x16x32_bf16 v[24:27], a[228:231], v[20:23], v[24:27]\n"
        "s_cmp_le_u32 s88, 4\n"
        "s_cselect_b32 s88, s73, s89\n"
        "s_cselect_b32 s73, s89, s73\n"
        "v_mfma_f32_16x16x32_bf16 v[28:31], a[232:235], v[20:23], v[28:31]\n"
        "buffer_store_dword v5, v3, s[40:43], s88 offen sc1\n"
        "v_mfma_f32_16x16x32_bf16 v[32:35], a[236:239], v[20:23], v[32:35]\n"
        "s_waitcnt lgkmcnt(1)\n"
        "ds_read_b128 v[20:23], v0 offset:23552\n"
        "v_mfma_f32_16x16x32_bf16 v[24:27], a[240:243], v[8:11], v[24:27]\n"
        "v_mfma_f32_16x16x32_bf16 v[28:31], a[244:247], v[8:11], v[28:31]\n"
        "s_waitcnt vmcnt(3)\n"
        "v_cmp_gt_i32 vcc, s70, v4\n"
        "v_mfma_f32_16x16x32_bf16 v[32:35], a[248:251], v[8:11], v[32:35]\n"
        "ds_read_b128 v[8:11], v0 offset:24576\n"
        "v_mfma_f32_16x16x32_bf16 v[24:27], a[252:255], v[12:15], v[24:27]\n"
        "s_and_b64 vcc, vcc, 63\n"
        "v_mfma_f32_16x16x32_bf16 v[28:31], v[80:83], v[12:15], v[28:31]\n"
        "s_cbranch_vccnz .Lf2_i01_%=\n"
        "v_mfma_f32_16x16x32_bf16 v[32:35], v[84:87], v[12:15], v[32:35]\n"
        "s_waitcnt lgkmcnt(1)\n"
        "ds_read_b128 v[12:15], v0 offset:25600\n"
        "buffer_load_dwordx4 v1, s[40:43], s71 offen offset:0 sc1 lds\n"
        "v_mfma_f32_16x16x32_bf16 v[24:27], v[88:91], v[16:19], v[24:27]\n"
        "v_mfma_f32_16x16x32_bf16 v[28:31], v[92:95], v[16:19], v[28:31]\n"
        "v_mfma_f32_16x16x32_bf16 v[32:35], v[96:99], v[16:19], v[32:35]\n"
        "ds_read_b128 v[16:19], v0 offset:26624\n"
        "buffer_load_dwordx4 v1, s[40:43], s71 offen offset:1024 sc1 lds\n"
        "v_mfma_f32_16x16x32_bf16 v[24:27], v[100:103], v[20:23], v[24:27]\n"
        "buffer_store_dword v5, v3, s[40:43], s73 offen sc1\n"
        "s_add_u32 s74, s74, 1\n"
        "v_mfma_f32_16x16x32_bf16 v[28:31], v[104:107], v[20:23], v[28:31]\n"
        "s_mov_b32 s84, s89\n"
        "s_mov_b32 s73, s60\n"
        "s_add_u32 s68, s68, 1\n"
        "v_mfma_f32_16x16x32_bf16 v[32:35], v[108:111], v[20:23], v[32:35]\n"
        "s_waitcnt lgkmcnt(1)\n"
        "ds_read_b128 v[20:23], v0 offset:27648\n"
        "s_lshr_b32 s66, s68, 2\n"
        "v_mfma_f32_16x16x32_bf16 v[24:27], v[112:115], v[8:11], v[24:27]\n"
        "s_add_u32 s66, s66, 1\n"
        "s_bitcmp1_b32 s55, s66\n"
        "s_cselect_b32 s66, 8192, 0\n"
        "v_mfma_f32_16x16x32_bf16 v[28:31], v[116:119], v[8:11], v[28:31]\n"
        "s_lshl_b32 s75, s68, 19\n"
        "s_add_u32 s75, s75, s66\n"
        "s_add_u32 s72, s75, s59\n"
        "v_mfma_f32_16x16x32_bf16 v[32:35], v[120:123], v[8:11], v[32:35]\n"
        "s_add_u32 m0, m0, s85\n"
        "ds_read_b128 v[8:11], v0 offset:28672\n"
        "s_add_u32 s71, s71, 16384\n"
        "v_mfma_f32_16x16x32_bf16 v[24:27], v[124:127], v[12:15], v[24:27]\n"
        "buffer_load_dwordx4 v1, s[40:43], s81 offen offset:0 sc1 lds\n"
        "s_sub_u32 m0, m0, s85\n"
        "v_mfma_f32_16x16x32_bf16 v[28:31], v[128:131], v[12:15], v[28:31]\n"
        "s_add_u32 m0, m0, 12288\n"
        "s_add_u32 s81, s81, 16384\n"
        "buffer_load_dwordx4 v1, s[40:43], s71 offen offset:0 sc1 lds\n"
        "v_mfma_f32_16x16x32_bf16 v[32:35], v[132:135], v[12:15], v[32:35]\n"
        "s_waitcnt lgkmcnt(1)\n"
        "ds_read_b128 v[12:15], v0 offset:29696\n"
        "buffer_load_dwordx4 v1, s[40:43], s71 offen offset:1024 sc1 lds\n"
        "v_mfma_f32_16x16x32_bf16 v[24:27], v[136:139], v[16:19], v[24:27]\n"
        "s_add_u32 m0, m0, s85\n"
        "s_add_u32 s71, s71, 16384\n"
        "v_mfma_f32_16x16x32_bf16 v[28:31], v[140:143], v[16:19], v[28:31]\n"
        "v_mfma_f32_16x16x32_bf16 v[32:35], v[144:147], v[16:19], v[32:35]\n"
        "ds_read_b128 v[16:19], v0 offset:30720\n"
        "v_mfma_f32_16x16x32_bf16 v[24:27], v[148:151], v[20:23], v[24:27]\n"
        "v_mfma_f32_16x16x32_bf16 v[28:31], v[152:155], v[20:23], v[28:31]\n"
        "v_mfma_f32_16x16x32_bf16 v[32:35], v[156:159], v[20:23], v[32:35]\n"
        "s_waitcnt lgkmcnt(1)\n"
        "ds_read_b128 v[20:23], v0 offset:31744\n"
        "v_mfma_f32_16x16x32_bf16 v[24:27], v[160:163], v[8:11], v[24:27]\n"
        "v_mfma_f32_16x16x32_bf16 v[28:31], v[164:167], v[8:11], v[28:31]\n"
        "buffer_load_dwordx4 v1, s[40:43], s81 offen offset:0 sc1 lds\n"
        "s_sub_u32 m0, m0, s85\n"
        "v_mfma_f32_16x16x32_bf16 v[32:35], v[168:171], v[8:11], v[32:35]\n"
        "s_add_u32 m0, m0, 12288\n"
        "ds_read_b128 v[8:11], v0 offset:32768\n"
        "s_add_u32 s81, s81, 16384\n"
        "v_mfma_f32_16x16x32_bf16 v[24:27], v[172:175], v[12:15], v[24:27]\n"
        "v_mfma_f32_16x16x32_bf16 v[28:31], v[176:179], v[12:15], v[28:31]\n"
        "v_mfma_f32_16x16x32_bf16 v[32:35], v[180:183], v[12:15], v[32:35]\n"
        "s_waitcnt lgkmcnt(1)\n"
        "ds_read_b128 v[12:15], v0 offset:33792\n"
        "buffer_load_dwordx4 v1, s[40:43], s71 offen offset:0 sc1 lds\n"
        "v_mfma_f32_16x16x32_bf16 v[24:27], v[184:187], v[16:19], v[24:27]\n"
        "v_mfma_f32_16x16x32_bf16 v[28:31], v[188:191], v[16:19], v[28:31]\n"
        "v_mfma_f32_16x16x32_bf16 v[32:35], v[192:195], v[16:19], v[32:35]\n"
        "ds_read_b128 v[16:19], v0 offset:34816\n"
        "v_mfma_f32_16x16x32_bf16 v[24:27], v[196:199], v[20:23], v[24:27]\n"
        "buffer_load_dwordx4 v1, s[40:43], s71 offen offset:1024 sc1 lds\n"
        "s_add_u32 m0, m0, s85\n"
        "v_mfma_f32_16x16x32_bf16 v[28:31], v[200:203], v[20:23], v[28:31]\n"
        "s_add_u32 s71, s71, 16384\n"
        "v_mfma_f32_16x16x32_bf16 v[32:35], v[204:207], v[20:23], v[32:35]\n"
        "s_waitcnt lgkmcnt(1)\n"
        "ds_read_b128 v[20:23], v0 offset:35840\n"
        "v_mfma_f32_16x16x32_bf16 v[24:27], v[208:211], v[8:11], v[24:27]\n"
        "v_mfma_f32_16x16x32_bf16 v[28:31], v[212:215], v[8:11], v[28:31]\n"
        "s_waitcnt vmcnt(5)\n"
        "s_barrier\n"
        "v_add_u32 v0, s78, v6\n"
        "v_mfma_f32_16x16x32_bf16 v[32:35], v[216:219], v[8:11], v[32:35]\n"
        "ds_read_b128 v[8:11], v0 offset:0\n"
        "buffer_load_dwordx4 v1, s[40:43], s81 offen offset:0 sc1 lds\n"
        "v_mfma_f32_16x16x32_bf16 v[24:27], v[220:223], v[12:15], v[24:27]\n"
        "s_add_u32 s69, s69, 1\n"
        "v_mfma_f32_16x16x32_bf16 v[28:31], v[224:227], v[12:15], v[28:31]\n"
        "v_mfma_f32_16x16x32_bf16 v[32:35], v[228:231], v[12:15], v[32:35]\n"
        "s_waitcnt lgkmcnt(1)\n"
        "ds_read_b128 v[12:15], v0 offset:1024\n"
        "v_mfma_f32_16x16x32_bf16 v[24:27], v[232:235], v[16:19], v[24:27]\n"
        "v_mfma_f32_16x16x32_bf16 v[28:31], v[236:239], v[16:19], v[28:31]\n"
        "v_mfma_f32_16x16x32_bf16 v[32:35], v[240:243], v[16:19], v[32:35]\n"
        "ds_read_b128 v[16:19], v0 offset:2048\n"
        "v_mfma_f32_16x16x32_bf16 v[24:27], v[244:247], v[20:23], v[24:27]\n"
        "v_mfma_f32_16x16x32_bf16 v[28:31], v[248:251], v[20:23], v[28:31]\n"
        "v_mfma_f32_16x16x32_bf16 v[32:35], v[252:255], v[20:23], v[32:35]\n"
        "s_waitcnt lgkmcnt(1)\n"
        "ds_read_b128 v[20:23], v0 offset:3072\n"
        "s_branch .Lf2_i1_%=\n"
        ".Lf2_i01_%=:\n"
        "v_mfma_f32_16x16x32_bf16 v[32:35], v[84:87], v[12:15], v[32:35]\n"
        "s_waitcnt lgkmcnt(1)\n"
        "ds_read_b128 v[12:15], v0 offset:25600\n"
        "buffer_load_dword v4, v2, s[40:43], s86 offen sc1\n"
        "v_mfma_f32_16x16x32_bf16 v[24:27], v[88:91], v[16:19], v[24:27]\n"
        "v_mfma_f32_16x16x32_bf16 v[28:31], v[92:95], v[16:19], v[28:31]\n"
        "v_mfma_f32_16x16x32_bf16 v[32:35], v[96:99], v[16:19], v[32:35]\n"
        "ds_read_b128 v[16:19], v0 offset:26624\n"
        "v_mfma_f32_16x16x32_bf16 v[24:27], v[100:103], v[20:23], v[24:27]\n"
        "buffer_store_dword v5, v3, s[40:43], s73 offen sc1\n"
        "s_add_u32 s74, s74, 1\n"
        "v_mfma_f32_16x16x32_bf16 v[28:31], v[104:107], v[20:23], v[28:31]\n"
        "s_mov_b32 s84, s89\n"
        "s_mov_b32 s73, s60\n"
        "s_add_u32 s68, s68, 1\n"
        "v_mfma_f32_16x16x32_bf16 v[32:35], v[108:111], v[20:23], v[32:35]\n"
        "s_waitcnt lgkmcnt(1)\n"
        "ds_read_b128 v[20:23], v0 offset:27648\n"
        "s_lshr_b32 s66, s68, 2\n"
        "v_mfma_f32_16x16x32_bf16 v[24:27], v[112:115], v[8:11], v[24:27]\n"
        "s_add_u32 s66, s66, 1\n"
        "s_bitcmp1_b32 s55, s66\n"
        "s_cselect_b32 s66, 8192, 0\n"
        "v_mfma_f32_16x16x32_bf16 v[28:31], v[116:119], v[8:11], v[28:31]\n"
        "s_lshl_b32 s75, s68, 19\n"
        "s_add_u32 s75, s75, s66\n"
        "s_add_u32 s72, s75, s59\n"
        "v_mfma_f32_16x16x32_bf16 v[32:35], v[120:123], v[8:11], v[32:35]\n"
        "ds_read_b128 v[8:11], v0 offset:28672\n"
        "v_mfma_f32_16x16x32_bf16 v[24:27], v[124:127], v[12:15], v[24:27]\n"
        "v_mfma_f32_16x16x32_bf16 v[28:31], v[128:131], v[12:15], v[28:31]\n"
        "v_mfma_f32_16x16x32_bf16 v[32:35], v[132:135], v[12:15], v[32:35]\n"
        "s_waitcnt lgkmcnt(1)\n"
        "ds_read_b128 v[12:15], v0 offset:29696\n"
        "v_mfma_f32_16x16x32_bf16 v[24:27], v[136:139], v[16:19], v[24:27]\n"
        "v_mfma_f32_16x16x32_bf16 v[28:31], v[140:143], v[16:19], v[28:31]\n"
        "v_mfma_f32_16x16x32_bf16 v[32:35], v[144:147], v[16:19], v[32:35]\n"
        "ds_read_b128 v[16:19], v0 offset:30720\n"
        "v_mfma_f32_16x16x32_bf16 v[24:27], v[148:151], v[20:23], v[24:27]\n"
        "v_mfma_f32_16x16x32_bf16 v[28:31], v[152:155], v[20:23], v[28:31]\n"
        "v_mfma_f32_16x16x32_bf16 v[32:35], v[156:159], v[20:23], v[32:35]\n"
        "s_waitcnt lgkmcnt(1)\n"
        "ds_read_b128 v[20:23], v0 offset:31744\n"
        "v_mfma_f32_16x16x32_bf16 v[24:27], v[160:163], v[8:11], v[24:27]\n"
        "v_mfma_f32_16x16x32_bf16 v[28:31], v[164:167], v[8:11], v[28:31]\n"
        "v_mfma_f32_16x16x32_bf16 v[32:35], v[168:171], v[8:11], v[32:35]\n"
        "ds_read_b128 v[8:11], v0 offset:32768\n"
        "v_mfma_f32_16x16x32_bf16 v[24:27], v[172:175], v[12:15], v[24:27]\n"
        "v_mfma_f32_16x16x32_bf16 v[28:31], v[176:179], v[12:15], v[28:31]\n"
        "v_mfma_f32_16x16x32_bf16 v[32:35], v[180:183], v[12:15], v[32:35]\n"
        "s_waitcnt lgkmcnt(1)\n"
        "ds_read_b128 v[12:15], v0 offset:33792\n"
        "v_mfma_f32_16x16x32_bf16 v[24:27], v[184:187], v[16:19], v[24:27]\n"
        "v_mfma_f32_16x16x32_bf16 v[28:31], v[188:191], v[16:19], v[28:31]\n"
        "v_mfma_f32_16x16x32_bf16 v[32:35], v[192:195], v[16:19], v[32:35]\n"
        "ds_read_b128 v[16:19], v0 offset:34816\n"
        "v_mfma_f32_16x16x32_bf16 v[24:27], v[196:199], v[20:23], v[24:27]\n"
        "s_waitcnt vmcnt(1)\n"
        "v_cmp_gt_i32 vcc, s70, v4\n"
        "v_mfma_f32_16x16x32_bf16 v[28:31], v[200:203], v[20:23], v[28:31]\n"
        "v_mfma_f32_16x16x32_bf16 v[32:35], v[204:207], v[20:23], v[32:35]\n"
        "s_waitcnt lgkmcnt(1)\n"
        "ds_read_b128 v[20:23], v0 offset:35840\n"
        "s_and_b64 vcc, vcc, 63\n"
        "v_mfma_f32_16x16x32_bf16 v[24:27], v[208:211], v[8:11], v[24:27]\n"
        "s_cbranch_vccnz .Lf2_i02_%=\n"
        "v_mfma_f32_16x16x32_bf16 v[28:31], v[212:215], v[8:11], v[28:31]\n"
        "buffer_load_dwordx4 v1, s[40:43], s71 offen offset:0 sc1 lds\n"
        "v_mfma_f32_16x16x32_bf16 v[32:35], v[216:219], v[8:11], v[32:35]\n"
        "v_mfma_f32_16x16x32_bf16 v[24:27], v[220:223], v[12:15], v[24:27]\n"
        "buffer_load_dwordx4 v1, s[40:43], s71 offen offset:1024 sc1 lds\n"
        "s_add_u32 m0, m0, s85\n"
        "s_add_u32 s71, s71, 16384\n"
        "v_mfma_f32_16x16x32_bf16 v[28:31], v[224:227], v[12:15], v[28:31]\n"
        "v_mfma_f32_16x16x32_bf16 v[32:35], v[228:231], v[12:15], v[32:35]\n"
        "s_waitcnt lgkmcnt(0)\n"
        "buffer_load_dwordx4 v1, s[40:43], s81 offen offset:0 sc1 lds\n"
        "s_sub_u32 m0, m0, s85\n"
        "s_add_u32 m0, m0, 12288\n"
        "s_add_u32 s81, s81, 16384\n"
        "v_mfma_f32_16x16x32_bf16 v[24:27], v[232:235], v[16:19], v[24:27]\n"
        "v_mfma_f32_16x16x32_bf16 v[28:31], v[236:239], v[16:19], v[28:31]\n"
        "buffer_load_dwordx4 v1, s[40:43], s71 offen offset:0 sc1 lds\n"
        "v_mfma_f32_16x16x32_bf16 v[32:35], v[240:243], v[16:19], v[32:35]\n"
        "v_mfma_f32_16x16x32_bf16 v[24:27], v[244:247], v[20:23], v[24:27]\n"
        "buffer_load_dwordx4 v1, s[40:43], s71 offen offset:1024 sc1 lds\n"
        "s_add_u32 m0, m0, s85\n"
        "s_add_u32 s71, s71, 16384\n"
        "v_mfma_f32_16x16x32_bf16 v[28:31], v[248:251], v[20:23], v[28:31]\n"
        "v_mfma_f32_16x16x32_bf16 v[32:35], v[252:255], v[20:23], v[32:35]\n"
        "buffer_load_dwordx4 v1, s[40:43], s81 offen offset:0 sc1 lds\n"
        "s_sub_u32 m0, m0, s85\n"
        "s_add_u32 m0, m0, 12288\n"
        "s_add_u32 s81, s81, 16384\n"
        "buffer_load_dwordx4 v1, s[40:43], s71 offen offset:0 sc1 lds\n"
        "buffer_load_dwordx4 v1, s[40:43], s71 offen offset:1024 sc1 lds\n"
        "s_add_u32 m0, m0, s85\n"
        "s_add_u32 s71, s71, 16384\n"
        "buffer_load_dwordx4 v1, s[40:43], s81 offen offset:0 sc1 lds\n"
        "s_add_u32 s69, s69, 1\n"
        "s_nop 2\n"
        "v_cvt_pk_bf16_f32 v24, v24, v25\n"
        "v_cvt_pk_bf16_f32 v25, v26, v27\n"
        "v_cvt_pk_bf16_f32 v26, v28, v29\n"
        "v_cvt_pk_bf16_f32 v27, v30, v31\n"
        "buffer_store_dwordx4 v[24:27], v48, s[40:43], s72 offen offset:0 sc1\n"
        "v_cvt_pk_bf16_f32 v32, v32, v33\n"
        "v_cvt_pk_bf16_f32 v33, v34, v35\n"
        "s_nop 0\n"
        "v_permlane16_swap_b32 v32, v34\n"
        "v_permlane16_swap_b32 v33, v35\n"
        "buffer_store_dwordx4 v[32:35], v7, s[40:43], s72 offen offset:0 sc1\n"
        "v_mov_b32 v5, s74\n"
        "s_mov_b32 s84, s60\n"
        "s_waitcnt vmcnt(8)\n"
        "s_branch .Lf2_go0_%=\n"
        ".Lf2_i02_%=:\n"
        "v_mfma_f32_16x16x32_bf16 v[28:31], v[212:215], v[8:11], v[28:31]\n"
        "buffer_load_dword v4, v2, s[40:43], s86 offen sc1\n"
        "v_mfma_f32_16x16x32_bf16 v[32:35], v[216:219], v[8:11], v[32:35]\n"
        "v_mfma_f32_16x16x32_bf16 v[24:27], v[220:223], v[12:15], v[24:27]\n"
        "v_mfma_f32_16x16x32_bf16 v[28:31], v[224:227], v[12:15], v[28:31]\n"
        "v_mfma_f32_16x16x32_bf16 v[32:35], v[228:231], v[12:15], v[32:35]\n"
        "s_waitcnt lgkmcnt(0)\n"
        "v_mfma_f32_16x16x32_bf16 v[24:27], v[232:235], v[16:19], v[24:27]\n"
        "v_mfma_f32_16x16x32_bf16 v[28:31], v[236:239], v[16:19], v[28:31]\n"
        "v_mfma_f32_16x16x32_bf16 v[32:35], v[240:243], v[16:19], v[32:35]\n"
        "v_mfma_f32_16x16x32_bf16 v[24:27], v[244:247], v[20:23], v[24:27]\n"
        "v_mfma_f32_16x16x32_bf16 v[28:31], v[248:251], v[20:23], v[28:31]\n"
        "v_mfma_f32_16x16x32_bf16 v[32:35], v[252:255], v[20:23], v[32:35]\n"
        "s_nop 2\n"
        "v_cvt_pk_bf16_f32 v24, v24, v25\n"
        "v_cvt_pk_bf16_f32 v25, v26, v27\n"
        "v_cvt_pk_bf16_f32 v26, v28, v29\n"
        "v_cvt_pk_bf16_f32 v27, v30, v31\n"
        "buffer_store_dwordx4 v[24:27], v48, s[40:43], s72 offen offset:0 sc1\n"
        "v_cvt_pk_bf16_f32 v32, v32, v33\n"
        "v_cvt_pk_bf16_f32 v33, v34, v35\n"
        "s_nop 0\n"
        "v_permlane16_swap_b32 v32, v34\n"
        "v_permlane16_swap_b32 v33, v35\n"
        "buffer_store_dwordx4 v[32:35], v7, s[40:43], s72 offen offset:0 sc1\n"
        "s_waitcnt vmcnt(2)\n"
        "v_cmp_gt_i32 vcc, s70, v4\n"
        "s_nop 0\n"
        "s_and_b64 vcc, vcc, 63\n"
        "s_cbranch_vccnz .Lf2_late0_%=\n"
        "buffer_load_dwordx4 v1, s[40:43], s71 offen offset:0 sc1 lds\n"
        "buffer_load_dwordx4 v1, s[40:43], s71 offen offset:1024 sc1 lds\n"
        "s_add_u32 m0, m0, s85\n"
        "s_add_u32 s71, s71, 16384\n"
        "buffer_load_dwordx4 v1, s[40:43], s81 offen offset:0 sc1 lds\n"
        "s_sub_u32 m0, m0, s85\n"
        "s_add_u32 m0, m0, 12288\n"
        "s_add_u32 s81, s81, 16384\n"
        "buffer_load_dwordx4 v1, s[40:43], s71 offen offset:0 sc1 lds\n"
        "buffer_load_dwordx4 v1, s[40:43], s71 offen offset:1024 sc1 lds\n"
        "s_add_u32 m0, m0, s85\n"
        "s_add_u32 s71, s71, 16384\n"
        "buffer_load_dwordx4 v1, s[40:43], s81 offen offset:0 sc1 lds\n"
        "s_sub_u32 m0, m0, s85\n"
        "s_add_u32 m0, m0, 12288\n"
        "s_add_u32 s81, s81, 16384\n"
        "buffer_load_dwordx4 v1, s[40:43], s71 offen offset:0 sc1 lds\n"
        "buffer_load_dwordx4 v1, s[40:43], s71 offen offset:1024 sc1 lds\n"
        "s_add_u32 m0, m0, s85\n"
        "s_add_u32 s71, s71, 16384\n"
        "buffer_load_dwordx4 v1, s[40:43], s81 offen offset:0 sc1 lds\n"
        "s_add_u32 s69, s69, 1\n"
        "s_waitcnt vmcnt(9)\n"
        "v_mov_b32 v5, s74\n"
        "buffer_store_dword v5, v3, s[40:43], s73 offen sc1\n"
        "s_waitcnt vmcnt(7)\n"
        "s_branch .Lf2_go0_%=\n"
        ".Lf2_late0_%=:\n"
        "s_waitcnt vmcnt(0)\n"
        "v_mov_b32 v5, s74\n"
        "buffer_store_dword v5, v3, s[40:43], s73 offen sc1\n"
        "s_mov_b32 s64, 0\n"
        ".Lf2_in0_top_%=:\n"
        "buffer_load_dword v4, v2, s[40:43], s86 offen sc1\n"
        "s_waitcnt vmcnt(0)\n"
        "v_cmp_gt_i32 vcc, s70, v4\n"
        "s_nop 0\n"
        "s_and_b64 vcc, vcc, 63\n"
        "s_cbranch_vccz .Lf2_in0_ok_%=\n"
        "s_add_u32 s64, s64, 1\n"
        "s_and_b32 s65, s64, 255\n"
        "s_cbranch_scc1 .Lf2_in0_go_%=\n"
        "s_mov_b32 s65, 49152\n"
        "s_cmp_lt_u32 s64, 1048576\n"
        "s_cbranch_scc1 .Lf2_in0_chk_%=\n"
        "v_mov_b32 v4, 1\n"
        "buffer_store_dword v4, off, s[40:43], s65 sc1\n"
        "s_waitcnt vmcnt(0)\n"
        ".Lf2_in0_chk_%=:\n"
        "buffer_load_dword v4, off, s[40:43], s65 sc1\n"
        "s_waitcnt vmcnt(0)\n"
        "v_readfirstlane_b32 s65, v4\n"
        "s_nop 0\n"
        "s_cmp_lg_u32 s65, 1\n"
        "s_cbranch_scc1 .Lf2_in0_go_%=\n"
        "s_endpgm\n"
        ".Lf2_in0_go_%=:\n"
        "s_branch .Lf2_in0_top_%=\n"
        ".Lf2_in0_ok_%=:\n"
        "buffer_load_dwordx4 v1, s[40:43], s71 offen offset:0 sc1 lds\n"
        "buffer_load_dwordx4 v1, s[40:43], s71 offen offset:1024 sc1 lds\n"
        "s_add_u32 m0, m0, s85\n"
        "s_add_u32 s71, s71, 16384\n"
        "buffer_load_dwordx4 v1, s[40:43], s81 offen offset:0 sc1 lds\n"
        "s_sub_u32 m0, m0, s85\n"
        "s_add_u32 m0, m0, 12288\n"
        "s_add_u32 s81, s81, 16384\n"
        "buffer_load_dwordx4 v1, s[40:43], s71 offen offset:0 sc1 lds\n"
        "buffer_load_dwordx4 v1, s[40:43], s71 offen offset:1024 sc1 lds\n"
        "s_add_u32 m0, m0, s85\n"
        "s_add_u32 s71, s71, 16384\n"
        "buffer_load_dwordx4 v1, s[40:43], s81 offen offset:0 sc1 lds\n"
        "s_sub_u32 m0, m0, s85\n"
        "s_add_u32 m0, m0, 12288\n"
        "s_add_u32 s81, s81, 16384\n"
        "buffer_load_dwordx4 v1, s[40:43], s71 offen offset:0 sc1 lds\n"
        "buffer_load_dwordx4 v1, s[40:43], s71 offen offset:1024 sc1 lds\n"
        "s_add_u32 m0, m0, s85\n"
        "s_add_u32 s71, s71, 16384\n"
        "buffer_load_dwordx4 v1, s[40:43], s81 offen offset:0 sc1 lds\n"
        "s_add_u32 s69, s69, 1\n"
        "s_waitcnt vmcnt(6)\n"
        ".Lf2_go0_%=:\n"
        "s_mov_b32 s72, 241172480\n"
        "s_mov_b32 s83, 241172480\n"
        "s_mov_b32 s73, 241172480\n"
        "v_add_u32 v0, s78, v6\n"
        "s_barrier\n"
        "ds_read_b128 v[8:11], v0 offset:0\n"
        "ds_read_b128 v[12:15], v0 offset:1024\n"
        "ds_read_b128 v[16:19], v0 offset:2048\n"
        "ds_read_b128 v[20:23], v0 offset:3072\n"
        "s_waitcnt lgkmcnt(2)\n"
        "s_branch .Lf2_i1_%=\n"
        ".Lf2_i1_%=:\n"
        "v_mfma_f32_16x16x32_bf16 v[36:39], a[0:3], v[8:11], 0\n"
        "v_mfma_f32_16x16x32_bf16 v[40:43], a[4:7], v[8:11], 0\n"
        "s_lshr_b32 s67, s69, 1\n"
        "buffer_load_dword v4, v2, s[40:43], s86 offen sc1\n"
        "s_lshr_b32 s65, s67, 1\n"
        "v_mfma_f32_16x16x32_bf16 v[44:47], a[8:11], v[8:11], 0\n"
        "s_bitcmp1_b32 s54, s65\n"
        "s_cselect_b32 s65, 8192, 0\n"
        "ds_read_b128 v[8:11], v0 offset:4096\n"
        "v_mfma_f32_16x16x32_bf16 v[36:39], a[12:15], v[12:15], v[36:39]\n"
        "s_lshl_b32 s77, s67, 20\n"
        "s_add_u32 s77, s77, s65\n"
        "s_add_u32 s71, s77, s58\n"
        "v_mfma_f32_16x16x32_bf16 v[40:43], a[16:19], v[12:15], v[40:43]\n"
        "s_add_u32 s81, s77, s62\n"
        "s_add_u32 s78, s78, 36864\n"
        "v_cvt_pk_bf16_f32 v24, v24, v25\n"
        "v_mfma_f32_16x16x32_bf16 v[44:47], a[20:23], v[12:15], v[44:47]\n"
        "s_waitcnt lgkmcnt(1)\n"
        "ds_read_b128 v[12:15], v0 offset:5120\n"
        "v_cvt_pk_bf16_f32 v25, v26, v27\n"
        "v_mfma_f32_16x16x32_bf16 v[36:39], a[24:27], v[16:19], v[36:39]\n"
        "s_cmp_eq_u32 s78, 110592\n"
        "s_cselect_b32 s78, 0, s78\n"
        "s_add_u32 m0, s78, s61\n"
        "v_mfma_f32_16x16x32_bf16 v[40:43], a[28:31], v[16:19], v[40:43]\n"
        "s_lshr_b32 s70, s69, 0\n"
        "v_cvt_pk_bf16_f32 v26, v28, v29\n"
        "v_cvt_pk_bf16_f32 v27, v30, v31\n"
        "v_mfma_f32_16x16x32_bf16 v[44:47], a[32:35], v[16:19], v[44:47]\n"
        "ds_read_b128 v[16:19], v0 offset:6144\n"
        "v_mfma_f32_16x16x32_bf16 v[36:39], a[36:39], v[20:23], v[36:39]\n"
        "buffer_store_dwordx4 v[24:27], v48, s[40:43], s72 offen offset:0 sc1\n"
        "v_mfma_f32_16x16x32_bf16 v[40:43], a[40:43], v[20:23], v[40:43]\n"
        "v_cvt_pk_bf16_f32 v32, v32, v33\n"
        "v_cvt_pk_bf16_f32 v33, v34, v35\n"
        "v_mfma_f32_16x16x32_bf16 v[44:47], a[44:47], v[20:23], v[44:47]\n"
        "s_waitcnt lgkmcnt(1)\n"
        "ds_read_b128 v[20:23], v0 offset:7168\n"
        "s_nop 0\n"
        "v_mfma_f32_16x16x32_bf16 v[36:39], a[48:51], v[8:11], v[36:39]\n"
        "v_permlane16_swap_b32 v32, v34\n"
        "v_mfma_f32_16x16x32_bf16 v[40:43], a[52:55], v[8:11], v[40:43]\n"
        "v_permlane16_swap_b32 v33, v35\n"
        "v_mfma_f32_16x16x32_bf16 v[44:47], a[56:59], v[8:11], v[44:47]\n"
        "ds_read_b128 v[8:11], v0 offset:8192\n"
        "v_mfma_f32_16x16x32_bf16 v[36:39], a[60:63], v[12:15], v[36:39]\n"
        "v_mfma_f32_16x16x32_bf16 v[40:43], a[64:67], v[12:15], v[40:43]\n"
        "buffer_store_dwordx4 v[32:35], v7, s[40:43], s72 offen offset:0 sc1\n"
        "v_mfma_f32_16x16x32_bf16 v[44:47], a[68:71], v[12:15], v[44:47]\n"
        "s_waitcnt lgkmcnt(1)\n"
        "ds_read_b128 v[12:15], v0 offset:9216\n"
        "v_mfma_f32_16x16x32_bf16 v[36:39], a[72:75], v[16:19], v[36:39]\n"
        "v_mfma_f32_16x16x32_bf16 v[40:43], a[76:79], v[16:19], v[40:43]\n"
        "v_mfma_f32_16x16x32_bf16 v[44:47], a[80:83], v[16:19], v[44:47]\n"
        "ds_read_b128 v[16:19], v0 offset:10240\n"
        "v_mfma_f32_16x16x32_bf16 v[36:39], a[84:87], v[20:23], v[36:39]\n"
        "v_mfma_f32_16x16x32_bf16 v[40:43], a[88:91], v[20:23], v[40:43]\n"
        "v_mfma_f32_16x16x32_bf16 v[44:47], a[92:95], v[20:23], v[44:47]\n"
        "s_waitcnt lgkmcnt(1)\n"
        "ds_read_b128 v[20:23], v0 offset:11264\n"
        "v_mfma_f32_16x16x32_bf16 v[36:39], a[96:99], v[8:11], v[36:39]\n"
        "v_mfma_f32_16x16x32_bf16 v[40:43], a[100:103], v[8:11], v[40:43]\n"
        "s_waitcnt vmcnt(3)\n"
        "s_barrier\n"
        "v_mfma_f32_16x16x32_bf16 v[44:47], a[104:107], v[8:11], v[44:47]\n"
        "ds_read_b128 v[8:11], v0 offset:12288\n"
        "buffer_store_dword v5, v3, s[40:43], s84 offen sc1\n"
        "v_mfma_f32_16x16x32_bf16 v[36:39], a[108:111], v[12:15], v[36:39]\n"
        "v_mov_b32 v5, s74\n"
        "v_mfma_f32_16x16x32_bf16 v[40:43], a[112:115], v[12:15], v[40:43]\n"
        "v_mfma_f32_16x16x32_bf16 v[44:47], a[116:119], v[12:15], v[44:47]\n"
        "s_waitcnt lgkmcnt(1)\n"
        "ds_read_b128 v[12:15], v0 offset:13312\n"
        "v_mfma_f32_16x16x32_bf16 v[36:39], a[120:123], v[16:19], v[36:39]\n"
        "v_mfma_f32_16x16x32_bf16 v[40:43], a[124:127], v[16:19], v[40:43]\n"
        "s_waitcnt vmcnt(3)\n"
        "v_cmp_gt_i32 vcc, s70, v4\n"
        "v_mfma_f32_16x16x32_bf16 v[44:47], a[128:131], v[16:19], v[44:47]\n"
        "ds_read_b128 v[16:19], v0 offset:14336\n"
        "v_mfma_f32_16x16x32_bf16 v[36:39], a[132:135], v[20:23], v[36:39]\n"
        "s_and_b64 vcc, vcc, 63\n"
        "v_mfma_f32_16x16x32_bf16 v[40:43], a[136:139], v[20:23], v[40:43]\n"
        "s_cbranch_vccnz .Lf2_i10_%=\n"
        "v_mfma_f32_16x16x32_bf16 v[44:47], a[140:143], v[20:23], v[44:47]\n"
        "s_waitcnt lgkmcnt(1)\n"
        "ds_read_b128 v[20:23], v0 offset:15360\n"
        "buffer_load_dwordx4 v1, s[40:43], s71 offen offset:0 sc1 lds\n"
        "v_mfma_f32_16x16x32_bf16 v[36:39], a[144:147], v[8:11], v[36:39]\n"
        "v_mfma_f32_16x16x32_bf16 v[40:43], a[148:151], v[8:11], v[40:43]\n"
        "v_mfma_f32_16x16x32_bf16 v[44:47], a[152:155], v[8:11], v[44:47]\n"
        "ds_read_b128 v[8:11], v0 offset:16384\n"
        "v_mfma_f32_16x16x32_bf16 v[36:39], a[156:159], v[12:15], v[36:39]\n"
        "buffer_load_dwordx4 v1, s[40:43], s71 offen offset:1024 sc1 lds\n"
        "s_add_u32 m0, m0, s85\n"
        "v_mfma_f32_16x16x32_bf16 v[40:43], a[160:163], v[12:15], v[40:43]\n"
        "s_getreg_b32 s88, hwreg(HW_REG_IB_STS, 0, 4)\n"
        "s_add_u32 s71, s71, 16384\n"
        "v_mfma_f32_16x16x32_bf16 v[44:47], a[164:167], v[12:15], v[44:47]\n"
        "s_waitcnt lgkmcnt(1)\n"
        "ds_read_b128 v[12:15], v0 offset:17408\n"
        "v_mfma_f32_16x16x32_bf16 v[36:39], a[168:171], v[16:19], v[36:39]\n"
        "s_cmp_le_u32 s88, 3\n"
        "s_cselect_b32 s88, s73, s89\n"
        "s_cselect_b32 s73, s89, s73\n"
        "v_mfma_f32_16x16x32_bf16 v[40:43], a[172:175], v[16:19], v[40:43]\n"
        "buffer_store_dword v5, v3, s[40:43], s88 offen sc1\n"
        "buffer_load_dwordx4 v1, s[40:43], s81 offen offset:0 sc1 lds\n"
        "v_mfma_f32_16x16x32_bf16 v[44:47], a[176:179], v[16:19], v[44:47]\n"
        "s_sub_u32 m0, m0, s85\n"
        "ds_read_b128 v[16:19], v0 offset:18432\n"
        "s_add_u32 m0, m0, 12288\n"
        "v_mfma_f32_16x16x32_bf16 v[36:39], a[180:183], v[20:23], v[36:39]\n"
        "s_add_u32 s81, s81, 16384\n"
        "v_mfma_f32_16x16x32_bf16 v[40:43], a[184:187], v[20:23], v[40:43]\n"
        "v_mfma_f32_16x16x32_bf16 v[44:47], a[188:191], v[20:23], v[44:47]\n"
        "s_waitcnt lgkmcnt(1)\n"
        "ds_read_b128 v[20:23], v0 offset:19456\n"
        "buffer_load_dwordx4 v1, s[40:43], s71 offen offset:0 sc1 lds\n"
        "v_mfma_f32_16x16x32_bf16 v[36:39], a[192:195], v[8:11], v[36:39]\n"
        "s_getreg_b32 s88, hwreg(HW_REG_IB_STS, 0, 4)\n"
        "v_mfma_f32_16x16x32_bf16 v[40:43], a[196:199], v[8:11], v[40:43]\n"
        "s_cmp_le_u32 s88, 6\n"
        "s_cselect_b32 s88, s73, s89\n"
        "s_cselect_b32 s73, s89, s73\n"
        "v_mfma_f32_16x16x32_bf16 v[44:47], a[200:203], v[8:11], v[44:47]\n"
        "ds_read_b128 v[8:11], v0 offset:20480\n"
        "buffer_store_dword v5, v3, s[40:43], s88 offen sc1\n"
        "v_mfma_f32_16x16x32_bf16 v[36:39], a[204:207], v[12:15], v[36:39]\n"
        "buffer_load_dwordx4 v1, s[40:43], s71 offen offset:1024 sc1 lds\n"
        "s_add_u32 m0, m0, s85\n"
        "v_mfma_f32_16x16x32_bf16 v[40:43], a[208:211], v[12:15], v[40:43]\n"
        "s_add_u32 s71, s71, 16384\n"
        "v_mfma_f32_16x16x32_bf16 v[44:47], a[212:215], v[12:15], v[44:47]\n"
        "s_waitcnt lgkmcnt(1)\n"
        "ds_read_b128 v[12:15], v0 offset:21504\n"
        "v_mfma_f32_16x16x32_bf16 v[36:39], a[216:219], v[16:19], v[36:39]\n"
        "v_mfma_f32_16x16x32_bf16 v[40:43], a[220:223], v[16:19], v[40:43]\n"
        "buffer_load_dwordx4 v1, s[40:43], s81 offen offset:0 sc1 lds\n"
        "s_sub_u32 m0, m0, s85\n"
        "v_mfma_f32_16x16x32_bf16 v[44:47], a[224:227], v[16:19], v[44:47]\n"
        "s_getreg_b32 s88, hwreg(HW_REG_IB_STS, 0, 4)\n"
        "ds_read_b128 v[16:19], v0 offset:22528\n"
        "s_add_u32 m0, m0, 12288\n"
        "v_mfma_f32_16x16x32_bf16 v[36:39], a[228:231], v[20:23], v[36:39]\n"
        "s_cmp_le_u32 s88, 9\n"
        "s_cselect_b32 s88, s73, s89\n"
        "s_cselect_b32 s73, s89, s73\n"
        "v_mfma_f32_16x16x32_bf16 v[40:43], a[232:235], v[20:23], v[40:43]\n"
        "s_add_u32 s81, s81, 16384\n"
        "buffer_store_dword v5, v3, s[40:43], s88 offen sc1\n"
        "v_mfma_f32_16x16x32_bf16 v[44:47], a[236:239], v[20:23], v[44:47]\n"
        "s_waitcnt lgkmcnt(1)\n"
        "ds_read_b128 v[20:23], v0 offset:23552\n"
        "buffer_load_dwordx4 v1, s[40:43], s71 offen offset:0 sc1 lds\n"
        "v_mfma_f32_16x16x32_bf16 v[36:39], a[240:243], v[8:11], v[36:39]\n"
        "v_mfma_f32_16x16x32_bf16 v[40:43], a[244:247], v[8:11], v[40:43]\n"
        "v_mfma_f32_16x16x32_bf16 v[44:47], a[248:251], v[8:11], v[44:47]\n"
        "ds_read_b128 v[8:11], v0 offset:24576\n"
        "v_mfma_f32_16x16x32_bf16 v[36:39], a[252:255], v[12:15], v[36:39]\n"
        "buffer_load_dwordx4 v1, s[40:43], s71 offen offset:1024 sc1 lds\n"
        "s_add_u32 m0, m0, s85\n"
        "v_mfma_f32_16x16x32_bf16 v[40:43], v[80:83], v[12:15], v[40:43]\n"
        "s_add_u32 s71, s71, 16384\n"
        "v_mfma_f32_16x16x32_bf16 v[44:47], v[84:87], v[12:15], v[44:47]\n"
        "s_waitcnt lgkmcnt(1)\n"
        "ds_read_b128 v[12:15], v0 offset:25600\n"
        "v_mfma_f32_16x16x32_bf16 v[36:39], v[88:91], v[16:19], v[36:39]\n"
        "v_mfma_f32_16x16x32_bf16 v[40:43], v[92:95], v[16:19], v[40:43]\n"
        "buffer_load_dwordx4 v1, s[40:43], s81 offen offset:0 sc1 lds\n"
        "s_add_u32 s69, s69, 1\n"
        "v_mfma_f32_16x16x32_bf16 v[44:47], v[96:99], v[16:19], v[44:47]\n"
        "ds_read_b128 v[16:19], v0 offset:26624\n"
        "v_mfma_f32_16x16x32_bf16 v[36:39], v[100:103], v[20:23], v[36:39]\n"
        "v_mfma_f32_16x16x32_bf16 v[40:43], v[104:107], v[20:23], v[40:43]\n"
        "s_waitcnt vmcnt(13)\n"
        "buffer_store_dword v5, v3, s[40:43], s73 offen sc1\n"
        "s_add_u32 s74, s74, 1\n"
        "v_mfma_f32_16x16x32_bf16 v[44:47], v[108:111], v[20:23], v[44:47]\n"
        "s_waitcnt lgkmcnt(1)\n"
        "ds_read_b128 v[20:23], v0 offset:27648\n"
        "s_mov_b32 s84, s89\n"
        "v_mfma_f32_16x16x32_bf16 v[36:39], v[112:115], v[8:11], v[36:39]\n"
        "s_mov_b32 s73, s60\n"
        "s_add_u32 s72, s75, s59\n"
        "s_add_u32 s72, s72, 147456\n"
        "v_mfma_f32_16x16x32_bf16 v[40:43], v[116:119], v[8:11], v[40:43]\n"
        "v_mfma_f32_16x16x32_bf16 v[44:47], v[120:123], v[8:11], v[44:47]\n"
        "ds_read_b128 v[8:11], v0 offset:28672\n"
        "v_mfma_f32_16x16x32_bf16 v[36:39], v[124:127], v[12:15], v[36:39]\n"
        "v_mfma_f32_16x16x32_bf16 v[40:43], v[128:131], v[12:15], v[40:43]\n"
        "v_mfma_f32_16x16x32_bf16 v[44:47], v[132:135], v[12:15], v[44:47]\n"
        "s_waitcnt lgkmcnt(1)\n"
        "ds_read_b128 v[12:15], v0 offset:29696\n"
        "v_mfma_f32_16x16x32_bf16 v[36:39], v[136:139], v[16:19], v[36:39]\n"
        "v_mfma_f32_16x16x32_bf16 v[40:43], v[140:143], v[16:19], v[40:43]\n"
        "v_mfma_f32_16x16x32_bf16 v[44:47], v[144:147], v[16:19], v[44:47]\n"
        "ds_read_b128 v[16:19], v0 offset:30720\n"
        "v_mfma_f32_16x16x32_bf16 v[36:39], v[148:151], v[20:23], v[36:39]\n"
        "v_mfma_f32_16x16x32_bf16 v[40:43], v[152:155], v[20:23], v[40:43]\n"
        "v_mfma_f32_16x16x32_bf16 v[44:47], v[156:159], v[20:23], v[44:47]\n"
        "s_waitcnt lgkmcnt(1)\n"
        "ds_read_b128 v[20:23], v0 offset:31744\n"
        "v_mfma_f32_16x16x32_bf16 v[36:39], v[160:163], v[8:11], v[36:39]\n"
        "v_mfma_f32_16x16x32_bf16 v[40:43], v[164:167], v[8:11], v[40:43]\n"
        "v_mfma_f32_16x16x32_bf16 v[44:47], v[168:171], v[8:11], v[44:47]\n"
        "ds_read_b128 v[8:11], v0 offset:32768\n"
        "v_mfma_f32_16x16x32_bf16 v[36:39], v[172:175], v[12:15], v[36:39]\n"
        "v_mfma_f32_16x16x32_bf16 v[40:43], v[176:179], v[12:15], v[40:43]\n"
        "v_mfma_f32_16x16x32_bf16 v[44:47], v[180:183], v[12:15], v[44:47]\n"
        "s_waitcnt lgkmcnt(1)\n"
        "ds_read_b128 v[12:15], v0 offset:33792\n"
        "v_mfma_f32_16x16x32_bf16 v[36:39], v[184:187], v[16:19], v[36:39]\n"
        "v_mfma_f32_16x16x32_bf16 v[40:43], v[188:191], v[16:19], v[40:43]\n"
        "v_mfma_f32_16x16x32_bf16 v[44:47], v[192:195], v[16:19], v[44:47]\n"
        "ds_read_b128 v[16:19], v0 offset:34816\n"
        "v_mfma_f32_16x16x32_bf16 v[36:39], v[196:199], v[20:23], v[36:39]\n"
        "v_mfma_f32_16x16x32_bf16 v[40:43], v[200:203], v[20:23], v[40:43]\n"
        "v_mfma_f32_16x16x32_bf16 v[44:47], v[204:207], v[20:23], v[44:47]\n"
        "s_waitcnt lgkmcnt(1)\n"
        "ds_read_b128 v[20:23], v0 offset:35840\n"
        "v_mfma_f32_16x16x32_bf16 v[36:39], v[208:211], v[8:11], v[36:39]\n"
        "v_mfma_f32_16x16x32_bf16 v[40:43], v[212:215], v[8:11], v[40:43]\n"
        "s_waitcnt vmcnt(9)\n"
        "s_barrier\n"
        "v_add_u32 v0, s78, v6\n"
        "v_mfma_f32_16x16x32_bf16 v[44:47], v[216:219], v[8:11], v[44:47]\n"
        "ds_read_b128 v[8:11], v0 offset:0\n"
        "v_mfma_f32_16x16x32_bf16 v[36:39], v[220:223], v[12:15], v[36:39]\n"
        "v_mfma_f32_16x16x32_bf16 v[40:43], v[224:227], v[12:15], v[40:43]\n"
        "v_mfma_f32_16x16x32_bf16 v[44:47], v[228:231], v[12:15], v[44:47]\n"
        "s_waitcnt lgkmcnt(1)\n"
        "ds_read_b128 v[12:15], v0 offset:1024\n"
        "v_mfma_f32_16x16x32_bf16 v[36:39], v[232:235], v[16:19], v[36:39]\n"
        "v_mfma_f32_16x16x32_bf16 v[40:43], v[236:239], v[16:19], v[40:43]\n"
        "v_mfma_f32_16x16x32_bf16 v[44:47], v[240:243], v[16:19], v[44:47]\n"
        "ds_read_b128 v[16:19], v0 offset:2048\n"
        "v_mfma_f32_16x16x32_bf16 v[36:39], v[244:247], v[20:23], v[36:39]\n"
        "v_mfma_f32_16x16x32_bf16 v[40:43], v[248:251], v[20:23], v[40:43]\n"
        "v_mfma_f32_16x16x32_bf16 v[44:47], v[252:255], v[20:23], v[44:47]\n"
        "s_waitcnt lgkmcnt(1)\n"
        "ds_read_b128 v[20:23], v0 offset:3072\n"
        "s_branch .Lf2_i0_%=\n"
        ".Lf2_i10_%=:\n"
        "v_mfma_f32_16x16x32_bf16 v[44:47], a[140:143], v[20:23], v[44:47]\n"
        "s_waitcnt lgkmcnt(1)\n"
        "ds_read_b128 v[20:23], v0 offset:15360\n"
        "buffer_load_dword v4, v2, s[40:43], s86 offen sc1\n"
        "v_mfma_f32_16x16x32_bf16 v[36:39], a[144:147], v[8:11], v[36:39]\n"
        "v_mfma_f32_16x16x32_bf16 v[40:43], a[148:151], v[8:11], v[40:43]\n"
        "v_mfma_f32_16x16x32_bf16 v[44:47], a[152:155], v[8:11], v[44:47]\n"
        "ds_read_b128 v[8:11], v0 offset:16384\n"
        "v_mfma_f32_16x16x32_bf16 v[36:39], a[156:159], v[12:15], v[36:39]\n"
        "v_mfma_f32_16x16x32_bf16 v[40:43], a[160:163], v[12:15], v[40:43]\n"
        "s_getreg_b32 s88, hwreg(HW_REG_IB_STS, 0, 4)\n"
        "v_mfma_f32_16x16x32_bf16 v[44:47], a[164:167], v[12:15], v[44:47]\n"
        "s_waitcnt lgkmcnt(1)\n"
        "ds_read_b128 v[12:15], v0 offset:17408\n"
        "v_mfma_f32_16x16x32_bf16 v[36:39], a[168:171], v[16:19], v[36:39]\n"
        "s_cmp_le_u32 s88, 2\n"
        "s_cselect_b32 s88, s73, s89\n"
        "s_cselect_b32 s73, s89, s73\n"
        "v_mfma_f32_16x16x32_bf16 v[40:43], a[172:175], v[16:19], v[40:43]\n"
        "buffer_store_dword v5, v3, s[40:43], s88 offen sc1\n"
        "v_mfma_f32_16x16x32_bf16 v[44:47], a[176:179], v[16:19], v[44:47]\n"
        "ds_read_b128 v[16:19], v0 offset:18432\n"
        "v_mfma_f32_16x16x32_bf16 v[36:39], a[180:183], v[20:23], v[36:39]\n"
        "v_mfma_f32_16x16x32_bf16 v[40:43], a[184:187], v[20:23], v[40:43]\n"
        "v_mfma_f32_16x16x32_bf16 v[44:47], a[188:191], v[20:23], v[44:47]\n"
        "s_waitcnt lgkmcnt(1)\n"
        "ds_read_b128 v[20:23], v0 offset:19456\n"
        "v_mfma_f32_16x16x32_bf16 v[36:39], a[192:195], v[8:11], v[36:39]\n"
        "s_getreg_b32 s88, hwreg(HW_REG_IB_STS, 0, 4)\n"
        "v_mfma_f32_16x16x32_bf16 v[40:43], a[196:199], v[8:11], v[40:43]\n"
        "s_cmp_le_u32 s88, 3\n"
        "s_cselect_b32 s88, s73, s89\n"
        "s_cselect_b32 s73, s89, s73\n"
        "v_mfma_f32_16x16x32_bf16 v[44:47], a[200:203], v[8:11], v[44:47]\n"
        "ds_read_b128 v[8:11], v0 offset:20480\n"
        "buffer_store_dword v5, v3, s[40:43], s88 offen sc1\n"
        "v_mfma_f32_16x16x32_bf16 v[36:39], a[204:207], v[12:15], v[36:39]\n"
        "v_mfma_f32_16x16x32_bf16 v[40:43], a[208:211], v[12:15], v[40:43]\n"
        "v_mfma_f32_16x16x32_bf16 v[44:47], a[212:215], v[12:15], v[44:47]\n"
        "s_waitcnt lgkmcnt(1)\n"
        "ds_read_b128 v[12:15], v0 offset:21504\n"
        "v_mfma_f32_16x16x32_bf16 v[36:39], a[216:219], v[16:19], v[36:39]\n"
        "v_mfma_f32_16x16x32_bf16 v[40:43], a[220:223], v[16:19], v[40:43]\n"
        "v_mfma_f32_16x16x32_bf16 v[44:47], a[224:227], v[16:19], v[44:47]\n"
        "s_getreg_b32 s88, hwreg(HW_REG_IB_STS, 0, 4)\n"
        "ds_read_b128 v[16:19], v0 offset:22528\n"
        "v_mfma_f32_16x16x32_bf16 v[36:39], a[228:231], v[20:23], v[36:39]\n"
        "s_cmp_le_u32 s88, 4\n"
        "s_cselect_b32 s88, s73, s89\n"
        "s_cselect_b32 s73, s89, s73\n"
        "v_mfma_f32_16x16x32_bf16 v[40:43], a[232:235], v[20:23], v[40:43]\n"
        "buffer_store_dword v5, v3, s[40:43], s88 offen sc1\n"
        "v_mfma_f32_16x16x32_bf16 v[44:47], a[236:239], v[20:23], v[44:47]\n"
        "s_waitcnt lgkmcnt(1)\n"
        "ds_read_b128 v[20:23], v0 offset:23552\n"
        "v_mfma_f32_16x16x32_bf16 v[36:39], a[240:243], v[8:11], v[36:39]\n"
        "v_mfma_f32_16x16x32_bf16 v[40:43], a[244:247], v[8:11], v[40:43]\n"
        "s_waitcnt vmcnt(3)\n"
        "v_cmp_gt_i32 vcc, s70, v4\n"
        "v_mfma_f32_16x16x32_bf16 v[44:47], a[248:251], v[8:11], v[44:47]\n"
        "ds_read_b128 v[8:11], v0 offset:24576\n"
        "v_mfma_f32_16x16x32_bf16 v[36:39], a[252:255], v[12:15], v[36:39]\n"
        "s_and_b64 vcc, vcc, 63\n"
        "v_mfma_f32_16x16x32_bf16 v[40:43], v[80:83], v[12:15], v[40:43]\n"
        "s_cbranch_vccnz .Lf2_i11_%=\n"
        "v_mfma_f32_16x16x32_bf16 v[44:47], v[84:87], v[12:15], v[44:47]\n"
        "s_waitcnt lgkmcnt(1)\n"
        "ds_read_b128 v[12:15], v0 offset:25600\n"
        "buffer_load_dwordx4 v1, s[40:43], s71 offen offset:0 sc1 lds\n"
        "v_mfma_f32_16x16x32_bf16 v[36:39], v[88:91], v[16:19], v[36:39]\n"
        "v_mfma_f32_16x16x32_bf16 v[40:43], v[92:95], v[16:19], v[40:43]\n"
        "v_mfma_f32_16x16x32_bf16 v[44:47], v[96:99], v[16:19], v[44:47]\n"
        "ds_read_b128 v[16:19], v0 offset:26624\n"
        "buffer_load_dwordx4 v1, s[40:43], s71 offen offset:1024 sc1 lds\n"
        "v_mfma_f32_16x16x32_bf16 v[36:39], v[100:103], v[20:23], v[36:39]\n"
        "s_add_u32 m0, m0, s85\n"
        "s_add_u32 s71, s71, 16384\n"
        "v_mfma_f32_16x16x32_bf16 v[40:43], v[104:107], v[20:23], v[40:43]\n"
        "buffer_store_dword v5, v3, s[40:43], s73 offen sc1\n"
        "s_add_u32 s74, s74, 1\n"
        "v_mfma_f32_16x16x32_bf16 v[44:47], v[108:111], v[20:23], v[44:47]\n"
        "s_waitcnt lgkmcnt(1)\n"
        "ds_read_b128 v[20:23], v0 offset:27648\n"
        "s_mov_b32 s84, s89\n"
        "buffer_load_dwordx4 v1, s[40:43], s81 offen offset:0 sc1 lds\n"
        "v_mfma_f32_16x16x32_bf16 v[36:39], v[112:115], v[8:11], v[36:39]\n"
        "s_mov_b32 s73, s60\n"
        "s_add_u32 s72, s75, s59\n"
        "s_add_u32 s72, s72, 147456\n"
        "v_mfma_f32_16x16x32_bf16 v[40:43], v[116:119], v[8:11], v[40:43]\n"
        "s_sub_u32 m0, m0, s85\n"
        "s_add_u32 m0, m0, 12288\n"
        "s_add_u32 s81, s81, 16384\n"
        "v_mfma_f32_16x16x32_bf16 v[44:47], v[120:123], v[8:11], v[44:47]\n"
        "ds_read_b128 v[8:11], v0 offset:28672\n"
        "buffer_load_dwordx4 v1, s[40:43], s71 offen offset:0 sc1 lds\n"
        "v_mfma_f32_16x16x32_bf16 v[36:39], v[124:127], v[12:15], v[36:39]\n"
        "v_mfma_f32_16x16x32_bf16 v[40:43], v[128:131], v[12:15], v[40:43]\n"
        "v_mfma_f32_16x16x32_bf16 v[44:47], v[132:135], v[12:15], v[44:47]\n"
        "s_waitcnt lgkmcnt(1)\n"
        "ds_read_b128 v[12:15], v0 offset:29696\n"
        "buffer_load_dwordx4 v1, s[40:43], s71 offen offset:1024 sc1 lds\n"
        "v_mfma_f32_16x16x32_bf16 v[36:39], v[136:139], v[16:19], v[36:39]\n"
        "s_add_u32 m0, m0, s85\n"
        "s_add_u32 s71, s71, 16384\n"
        "v_mfma_f32_16x16x32_bf16 v[40:43], v[140:143], v[16:19], v[40:43]\n"
        "v_mfma_f32_16x16x32_bf16 v[44:47], v[144:147], v[16:19], v[44:47]\n"
        "ds_read_b128 v[16:19], v0 offset:30720\n"
        "v_mfma_f32_16x16x32_bf16 v[36:39], v[148:151], v[20:23], v[36:39]\n"
        "v_mfma_f32_16x16x32_bf16 v[40:43], v[152:155], v[20:23], v[40:43]\n"
        "v_mfma_f32_16x16x32_bf16 v[44:47], v[156:159], v[20:23], v[44:47]\n"
        "s_waitcnt lgkmcnt(1)\n"
        "ds_read_b128 v[20:23], v0 offset:31744\n"
        "v_mfma_f32_16x16x32_bf16 v[36:39], v[160:163], v[8:11], v[36:39]\n"
        "v_mfma_f32_16x16x32_bf16 v[40:43], v[164:167], v[8:11], v[40:43]\n"
        "buffer_load_dwordx4 v1, s[40:43], s81 offen offset:0 sc1 lds\n"
        "s_sub_u32 m0, m0, s85\n"
        "v_mfma_f32_16x16x32_bf16 v[44:47], v[168:171], v[8:11], v[44:47]\n"
        "s_add_u32 m0, m0, 12288\n"
        "ds_read_b128 v[8:11], v0 offset:32768\n"
        "s_add_u32 s81, s81, 16384\n"
        "v_mfma_f32_16x16x32_bf16 v[36:39], v[172:175], v[12:15], v[36:39]\n"
        "v_mfma_f32_16x16x32_bf16 v[40:43], v[176:179], v[12:15], v[40:43]\n"
        "v_mfma_f32_16x16x32_bf16 v[44:47], v[180:183], v[12:15], v[44:47]\n"
        "s_waitcnt lgkmcnt(1)\n"
        "ds_read_b128 v[12:15], v0 offset:33792\n"
        "buffer_load_dwordx4 v1, s[40:43], s71 offen offset:0 sc1 lds\n"
        "v_mfma_f32_16x16x32_bf16 v[36:39], v[184:187], v[16:19], v[36:39]\n"
        "v_mfma_f32_16x16x32_bf16 v[40:43], v[188:191], v[16:19], v[40:43]\n"
        "v_mfma_f32_16x16x32_bf16 v[44:47], v[192:195], v[16:19], v[44:47]\n"
        "ds_read_b128 v[16:19], v0 offset:34816\n"
        "v_mfma_f32_16x16x32_bf16 v[36:39], v[196:199], v[20:23], v[36:39]\n"
        "buffer_load_dwordx4 v1, s[40:43], s71 offen offset:1024 sc1 lds\n"
        "s_add_u32 m0, m0, s85\n"
        "v_mfma_f32_16x16x32_bf16 v[40:43], v[200:203], v[20:23], v[40:43]\n"
        "s_add_u32 s71, s71, 16384\n"
        "v_mfma_f32_16x16x32_bf16 v[44:47], v[204:207], v[20:23], v[44:47]\n"
        "s_waitcnt lgkmcnt(1)\n"
        "ds_read_b128 v[20:23], v0 offset:35840\n"
        "v_mfma_f32_16x16x32_bf16 v[36:39], v[208:211], v[8:11], v[36:39]\n"
        "v_mfma_f32_16x16x32_bf16 v[40:43], v[212:215], v[8:11], v[40:43]\n"
        "s_waitcnt vmcnt(5)\n"
        "s_barrier\n"
        "v_add_u32 v0, s78, v6\n"
        "v_mfma_f32_16x16x32_bf16 v[44:47], v[216:219], v[8:11], v[44:47]\n"
        "ds_read_b128 v[8:11], v0 offset:0\n"
        "buffer_load_dwordx4 v1, s[40:43], s81 offen offset:0 sc1 lds\n"
        "v_mfma_f32_16x16x32_bf16 v[36:39], v[220:223], v[12:15], v[36:39]\n"
        "s_add_u32 s69, s69, 1\n"
        "v_mfma_f32_16x16x32_bf16 v[40:43], v[224:227], v[12:15], v[40:43]\n"
        "v_mfma_f32_16x16x32_bf16 v[44:47], v[228:231], v[12:15], v[44:47]\n"
        "s_waitcnt lgkmcnt(1)\n"
        "ds_read_b128 v[12:15], v0 offset:1024\n"
        "v_mfma_f32_16x16x32_bf16 v[36:39], v[232:235], v[16:19], v[36:39]\n"
        "v_mfma_f32_16x16x32_bf16 v[40:43], v[236:239], v[16:19], v[40:43]\n"
        "v_mfma_f32_16x16x32_bf16 v[44:47], v[240:243], v[16:19], v[44:47]\n"
        "ds_read_b128 v[16:19], v0 offset:2048\n"
        "v_mfma_f32_16x16x32_bf16 v[36:39], v[244:247], v[20:23], v[36:39]\n"
        "v_mfma_f32_16x16x32_bf16 v[40:43], v[248:251], v[20:23], v[40:43]\n"
        "v_mfma_f32_16x16x32_bf16 v[44:47], v[252:255], v[20:23], v[44:47]\n"
        "s_waitcnt lgkmcnt(1)\n"
        "ds_read_b128 v[20:23], v0 offset:3072\n"
        "s_branch .Lf2_i0_%=\n"
        ".Lf2_i11_%=:\n"
        "v_mfma_f32_16x16x32_bf16 v[44:47], v[84:87], v[12:15], v[44:47]\n"
        "s_waitcnt lgkmcnt(1)\n"
        "ds_read_b128 v[12:15], v0 offset:25600\n"
        "buffer_load_dword v4, v2, s[40:43], s86 offen sc1\n"
        "v_mfma_f32_16x16x32_bf16 v[36:39], v[88:91], v[16:19], v[36:39]\n"
        "v_mfma_f32_16x16x32_bf16 v[40:43], v[92:95], v[16:19], v[40:43]\n"
        "v_mfma_f32_16x16x32_bf16 v[44:47], v[96:99], v[16:19], v[44:47]\n"
        "ds_read_b128 v[16:19], v0 offset:26624\n"
        "v_mfma_f32_16x16x32_bf16 v[36:39], v[100:103], v[20:23], v[36:39]\n"
        "v_mfma_f32_16x16x32_bf16 v[40:43], v[104:107], v[20:23], v[40:43]\n"
        "buffer_store_dword v5, v3, s[40:43], s73 offen sc1\n"
        "s_add_u32 s74, s74, 1\n"
        "v_mfma_f32_16x16x32_bf16 v[44:47], v[108:111], v[20:23], v[44:47]\n"
        "s_waitcnt lgkmcnt(1)\n"
        "ds_read_b128 v[20:23], v0 offset:27648\n"
        "s_mov_b32 s84, s89\n"
        "v_mfma_f32_16x16x32_bf16 v[36:39], v[112:115], v[8:11], v[36:39]\n"
        "s_mov_b32 s73, s60\n"
        "s_add_u32 s72, s75, s59\n"
        "s_add_u32 s72, s72, 147456\n"
        "v_mfma_f32_16x16x32_bf16 v[40:43], v[116:119], v[8:11], v[40:43]\n"
        "v_mfma_f32_16x16x32_bf16 v[44:47], v[120:123], v[8:11], v[44:47]\n"
        "ds_read_b128 v[8:11], v0 offset:28672\n"
        "v_mfma_f32_16x16x32_bf16 v[36:39], v[124:127], v[12:15], v[36:39]\n"
        "v_mfma_f32_16x16x32_bf16 v[40:43], v[128:131], v[12:15], v[40:43]\n"
        "v_mfma_f32_16x16x32_bf16 v[44:47], v[132:135], v[12:15], v[44:47]\n"
        "s_waitcnt lgkmcnt(1)\n"
        "ds_read_b128 v[12:15], v0 offset:29696\n"
        "v_mfma_f32_16x16x32_bf16 v[36:39], v[136:139], v[16:19], v[36:39]\n"
        "v_mfma_f32_16x16x32_bf16 v[40:43], v[140:143], v[16:19], v[40:43]\n"
        "v_mfma_f32_16x16x32_bf16 v[44:47], v[144:147], v[16:19], v[44:47]\n"
        "ds_read_b128 v[16:19], v0 offset:30720\n"
        "v_mfma_f32_16x16x32_bf16 v[36:39], v[148:151], v[20:23], v[36:39]\n"
        "v_mfma_f32_16x16x32_bf16 v[40:43], v[152:155], v[20:23], v[40:43]\n"
        "v_mfma_f32_16x16x32_bf16 v[44:47], v[156:159], v[20:23], v[44:47]\n"
        "s_waitcnt lgkmcnt(1)\n"
        "ds_read_b128 v[20:23], v0 offset:31744\n"
        "v_mfma_f32_16x16x32_bf16 v[36:39], v[160:163], v[8:11], v[36:39]\n"
        "v_mfma_f32_16x16x32_bf16 v[40:43], v[164:167], v[8:11], v[40:43]\n"
        "v_mfma_f32_16x16x32_bf16 v[44:47], v[168:171], v[8:11], v[44:47]\n"
        "ds_read_b128 v[8:11], v0 offset:32768\n"
        "v_mfma_f32_16x16x32_bf16 v[36:39], v[172:175], v[12:15], v[36:39]\n"
        "v_mfma_f32_16x16x32_bf16 v[40:43], v[176:179], v[12:15], v[40:43]\n"
        "v_mfma_f32_16x16x32_bf16 v[44:47], v[180:183], v[12:15], v[44:47]\n"
        "s_waitcnt lgkmcnt(1)\n"
        "ds_read_b128 v[12:15], v0 offset:33792\n"
        "v_mfma_f32_16x16x32_bf16 v[36:39], v[184:187], v[16:19], v[36:39]\n"
        "v_mfma_f32_16x16x32_bf16 v[40:43], v[188:191], v[16:19], v[40:43]\n"
        "v_mfma_f32_16x16x32_bf16 v[44:47], v[192:195], v[16:19], v[44:47]\n"
        "ds_read_b128 v[16:19], v0 offset:34816\n"
        "v_mfma_f32_16x16x32_bf16 v[36:39], v[196:199], v[20:23], v[36:39]\n"
        "s_waitcnt vmcnt(1)\n"
        "v_cmp_gt_i32 vcc, s70, v4\n"
        "v_mfma_f32_16x16x32_bf16 v[40:43], v[200:203], v[20:23], v[40:43]\n"
        "v_mfma_f32_16x16x32_bf16 v[44:47], v[204:207], v[20:23], v[44:47]\n"
        "s_waitcnt lgkmcnt(1)\n"
        "ds_read_b128 v[20:23], v0 offset:35840\n"
        "s_and_b64 vcc, vcc, 63\n"
        "v_mfma_f32_16x16x32_bf16 v[36:39], v[208:211], v[8:11], v[36:39]\n"
        "s_cbranch_vccnz .Lf2_i12_%=\n"
        "v_mfma_f32_16x16x32_bf16 v[40:43], v[212:215], v[8:11], v[40:43]\n"
        "buffer_load_dwordx4 v1, s[40:43], s71 offen offset:0 sc1 lds\n"
        "v_mfma_f32_16x16x32_bf16 v[44:47], v[216:219], v[8:11], v[44:47]\n"
        "v_mfma_f32_16x16x32_bf16 v[36:39], v[220:223], v[12:15], v[36:39]\n"
        "buffer_load_dwordx4 v1, s[40:43], s71 offen offset:1024 sc1 lds\n"
        "s_add_u32 m0, m0, s85\n"
        "s_add_u32 s71, s71, 16384\n"
        "v_mfma_f32_16x16x32_bf16 v[40:43], v[224:227], v[12:15], v[40:43]\n"
        "v_mfma_f32_16x16x32_bf16 v[44:47], v[228:231], v[12:15], v[44:47]\n"
        "s_waitcnt lgkmcnt(0)\n"
        "buffer_load_dwordx4 v1, s[40:43], s81 offen offset:0 sc1 lds\n"
        "s_sub_u32 m0, m0, s85\n"
        "s_add_u32 m0, m0, 12288\n"
        "s_add_u32 s81, s81, 16384\n"
        "v_mfma_f32_16x16x32_bf16 v[36:39], v[232:235], v[16:19], v[36:39]\n"
        "v_mfma_f32_16x16x32_bf16 v[40:43], v[236:239], v[16:19], v[40:43]\n"
        "buffer_load_dwordx4 v1, s[40:43], s71 offen offset:0 sc1 lds\n"
        "v_mfma_f32_16x16x32_bf16 v[44:47], v[240:243], v[16:19], v[44:47]\n"
        "v_mfma_f32_16x16x32_bf16 v[36:39], v[244:247], v[20:23], v[36:39]\n"
        "buffer_load_dwordx4 v1, s[40:43], s71 offen offset:1024 sc1 lds\n"
        "s_add_u32 m0, m0, s85\n"
        "s_add_u32 s71, s71, 16384\n"
        "v_mfma_f32_16x16x32_bf16 v[40:43], v[248:251], v[20:23], v[40:43]\n"
        "v_mfma_f32_16x16x32_bf16 v[44:47], v[252:255], v[20:23], v[44:47]\n"
        "buffer_load_dwordx4 v1, s[40:43], s81 offen offset:0 sc1 lds\n"
        "s_sub_u32 m0, m0, s85\n"
        "s_add_u32 m0, m0, 12288\n"
        "s_add_u32 s81, s81, 16384\n"
        "buffer_load_dwordx4 v1, s[40:43], s71 offen offset:0 sc1 lds\n"
        "buffer_load_dwordx4 v1, s[40:43], s71 offen offset:1024 sc1 lds\n"
        "s_add_u32 m0, m0, s85\n"
        "s_add_u32 s71, s71, 16384\n"
        "buffer_load_dwordx4 v1, s[40:43], s81 offen offset:0 sc1 lds\n"
        "s_add_u32 s69, s69, 1\n"
        "s_nop 2\n"
        "v_cvt_pk_bf16_f32 v36, v36, v37\n"
        "v_cvt_pk_bf16_f32 v37, v38, v39\n"
        "v_cvt_pk_bf16_f32 v38, v40, v41\n"
        "v_cvt_pk_bf16_f32 v39, v42, v43\n"
        "buffer_store_dwordx4 v[36:39], v48, s[40:43], s72 offen offset:0 sc1\n"
        "v_cvt_pk_bf16_f32 v44, v44, v45\n"
        "v_cvt_pk_bf16_f32 v45, v46, v47\n"
        "s_nop 0\n"
        "v_permlane16_swap_b32 v44, v46\n"
        "v_permlane16_swap_b32 v45, v47\n"
        "buffer_store_dwordx4 v[44:47], v7, s[40:43], s72 offen offset:0 sc1\n"
        "v_mov_b32 v5, s74\n"
        "s_mov_b32 s84, s60\n"
        "s_waitcnt vmcnt(8)\n"
        "s_branch .Lf2_go1_%=\n"
        ".Lf2_i12_%=:\n"
        "v_mfma_f32_16x16x32_bf16 v[40:43], v[212:215], v[8:11], v[40:43]\n"
        "buffer_load_dword v4, v2, s[40:43], s86 offen sc1\n"
        "v_mfma_f32_16x16x32_bf16 v[44:47], v[216:219], v[8:11], v[44:47]\n"
        "v_mfma_f32_16x16x32_bf16 v[36:39], v[220:223], v[12:15], v[36:39]\n"
        "v_mfma_f32_16x16x32_bf16 v[40:43], v[224:227], v[12:15], v[40:43]\n"
        "v_mfma_f32_16x16x32_bf16 v[44:47], v[228:231], v[12:15], v[44:47]\n"
        "s_waitcnt lgkmcnt(0)\n"
        "v_mfma_f32_16x16x32_bf16 v[36:39], v[232:235], v[16:19], v[36:39]\n"
        "v_mfma_f32_16x16x32_bf16 v[40:43], v[236:239], v[16:19], v[40:43]\n"
        "v_mfma_f32_16x16x32_bf16 v[44:47], v[240:243], v[16:19], v[44:47]\n"
        "v_mfma_f32_16x16x32_bf16 v[36:39], v[244:247], v[20:23], v[36:39]\n"
        "v_mfma_f32_16x16x32_bf16 v[40:43], v[248:251], v[20:23], v[40:43]\n"
        "v_mfma_f32_16x16x32_bf16 v[44:47], v[252:255], v[20:23], v[44:47]\n"
        "s_nop 2\n"
        "v_cvt_pk_bf16_f32 v36, v36, v37\n"
        "v_cvt_pk_bf16_f32 v37, v38, v39\n"
        "v_cvt_pk_bf16_f32 v38, v40, v41\n"
        "v_cvt_pk_bf16_f32 v39, v42, v43\n"
        "buffer_store_dwordx4 v[36:39], v48, s[40:43], s72 offen offset:0 sc1\n"
        "v_cvt_pk_bf16_f32 v44, v44, v45\n"
        "v_cvt_pk_bf16_f32 v45, v46, v47\n"
        "s_nop 0\n"
        "v_permlane16_swap_b32 v44, v46\n"
        "v_permlane16_swap_b32 v45, v47\n"
        "buffer_store_dwordx4 v[44:47], v7, s[40:43], s72 offen offset:0 sc1\n"
        "s_waitcnt vmcnt(2)\n"
        "v_cmp_gt_i32 vcc, s70, v4\n"
        "s_nop 0\n"
        "s_and_b64 vcc, vcc, 63\n"
        "s_cbranch_vccnz .Lf2_late1_%=\n"
        "buffer_load_dwordx4 v1, s[40:43], s71 offen offset:0 sc1 lds\n"
        "buffer_load_dwordx4 v1, s[40:43], s71 offen offset:1024 sc1 lds\n"
        "s_add_u32 m0, m0, s85\n"
        "s_add_u32 s71, s71, 16384\n"
        "buffer_load_dwordx4 v1, s[40:43], s81 offen offset:0 sc1 lds\n"
        "s_sub_u32 m0, m0, s85\n"
        "s_add_u32 m0, m0, 12288\n"
        "s_add_u32 s81, s81, 16384\n"
        "buffer_load_dwordx4 v1, s[40:43], s71 offen offset:0 sc1 lds\n"
        "buffer_load_dwordx4 v1, s[40:43], s71 offen offset:1024 sc1 lds\n"
        "s_add_u32 m0, m0, s85\n"
        "s_add_u32 s71, s71, 16384\n"
        "buffer_load_dwordx4 v1, s[40:43], s81 offen offset:0 sc1 lds\n"
        "s_sub_u32 m0, m0, s85\n"
        "s_add_u32 m0, m0, 12288\n"
        "s_add_u32 s81, s81, 16384\n"
        "buffer_load_dwordx4 v1, s[40:43], s71 offen offset:0 sc1 lds\n"
        "buffer_load_dwordx4 v1, s[40:43], s71 offen offset:1024 sc1 lds\n"
        "s_add_u32 m0, m0, s85\n"
        "s_add_u32 s71, s71, 16384\n"
        "buffer_load_dwordx4 v1, s[40:43], s81 offen offset:0 sc1 lds\n"
        "s_add_u32 s69, s69, 1\n"
        "s_waitcnt vmcnt(9)\n"
        "v_mov_b32 v5, s74\n"
        "buffer_store_dword v5, v3, s[40:43], s73 offen sc1\n"
        "s_waitcnt vmcnt(7)\n"
        "s_branch .Lf2_go1_%=\n"
        ".Lf2_late1_%=:\n"
        "s_waitcnt vmcnt(0)\n"
        "v_mov_b32 v5, s74\n"
        "buffer_store_dword v5, v3, s[40:43], s73 offen sc1\n"
        "s_cmp_eq_u32 s69, 128\n"
        "s_cbranch_scc0 .Lf2_wait1_%=\n"
        "s_endpgm\n"
        ".Lf2_wait1_%=:\n"
        "s_mov_b32 s64, 0\n"
        ".Lf2_in1_top_%=:\n"
        "buffer_load_dword v4, v2, s[40:43], s86 offen sc1\n"
        "s_waitcnt vmcnt(0)\n"
        "v_cmp_gt_i32 vcc, s70, v4\n"
        "s_nop 0\n"
        "s_and_b64 vcc, vcc, 63\n"
        "s_cbranch_vccz .Lf2_in1_ok_%=\n"
        "s_add_u32 s64, s64, 1\n"
        "s_and_b32 s65, s64, 255\n"
        "s_cbranch_scc1 .Lf2_in1_go_%=\n"
        "s_mov_b32 s65, 49152\n"
        "s_cmp_lt_u32 s64, 1048576\n"
        "s_cbranch_scc1 .Lf2_in1_chk_%=\n"
        "v_mov_b32 v4, 1\n"
        "buffer_store_dword v4, off, s[40:43], s65 sc1\n"
        "s_waitcnt vmcnt(0)\n"
        ".Lf2_in1_chk_%=:\n"
        "buffer_load_dword v4, off, s[40:43], s65 sc1\n"
        "s_waitcnt vmcnt(0)\n"
        "v_readfirstlane_b32 s65, v4\n"
        "s_nop 0\n"
        "s_cmp_lg_u32 s65, 1\n"
        "s_cbranch_scc1 .Lf2_in1_go_%=\n"
        "s_endpgm\n"
        ".Lf2_in1_go_%=:\n"
        "s_branch .Lf2_in1_top_%=\n"
        ".Lf2_in1_ok_%=:\n"
        "buffer_load_dwordx4 v1, s[40:43], s71 offen offset:0 sc1 lds\n"
        "buffer_load_dwordx4 v1, s[40:43], s71 offen offset:1024 sc1 lds\n"
        "s_add_u32 m0, m0, s85\n"
        "s_add_u32 s71, s71, 16384\n"
        "buffer_load_dwordx4 v1, s[40:43], s81 offen offset:0 sc1 lds\n"
        "s_sub_u32 m0, m0, s85\n"
        "s_add_u32 m0, m0, 12288\n"
        "s_add_u32 s81, s81, 16384\n"
        "buffer_load_dwordx4 v1, s[40:43], s71 offen offset:0 sc1 lds\n"
        "buffer_load_dwordx4 v1, s[40:43], s71 offen offset:1024 sc1 lds\n"
        "s_add_u32 m0, m0, s85\n"
        "s_add_u32 s71, s71, 16384\n"
        "buffer_load_dwordx4 v1, s[40:43], s81 offen offset:0 sc1 lds\n"
        "s_sub_u32 m0, m0, s85\n"
        "s_add_u32 m0, m0, 12288\n"
        "s_add_u32 s81, s81, 16384\n"
        "buffer_load_dwordx4 v1, s[40:43], s71 offen offset:0 sc1 lds\n"
        "buffer_load_dwordx4 v1, s[40:43], s71 offen offset:1024 sc1 lds\n"
        "s_add_u32 m0, m0, s85\n"
        "s_add_u32 s71, s71, 16384\n"
        "buffer_load_dwordx4 v1, s[40:43], s81 offen offset:0 sc1 lds\n"
        "s_add_u32 s69, s69, 1\n"
        "s_waitcnt vmcnt(6)\n"
        ".Lf2_go1_%=:\n"
        "s_mov_b32 s72, 241172480\n"
        "s_mov_b32 s83, 241172480\n"
        "s_mov_b32 s73, 241172480\n"
        "v_add_u32 v0, s78, v6\n"
        "s_barrier\n"
        "ds_read_b128 v[8:11], v0 offset:0\n"
        "ds_read_b128 v[12:15], v0 offset:1024\n"
        "ds_read_b128 v[16:19], v0 offset:2048\n"
        "ds_read_b128 v[20:23], v0 offset:3072\n"
        "s_waitcnt lgkmcnt(2)\n"
        "s_branch .Lf2_i0_%=\n"
            :: "s"(p.ws), "s"(p.w2), "s"(p.w2), "s"(p.w2), "s"(pair(w, delay)), "s"(pair(x4, pr)),
               "s"(pair(hb + w * 2048, 97 * PAGE + spread(C_P5))), "s"(pair(flag(F_F2 + fl), (w * 2048) | (8192 - w * 1024) << 16)), "s"(pair(hb + 6 * 16384 + w * 1024, 0)),
               "v"(pair(lane * 16, lane * 16)), "v"(pair(lane < 6 ? flag(F_F1 + 128 * nu + 24 * kap + 8 * (lane >> 1) + 2 * w + (lane & 1)) : OOB, fo4)), K_ARGS
            : "v0", "v1", "v2", "v3", "v4", "v5", "v6", "v7", "v8", "v9", "v10", "v11", "v12", "v13", "v14", "v15", "v16", "v17", "v18", "v19", "v20", "v21", "v22", "v23", "v24", "v25", "v26", "v27", "v28", "v29", "v30", "v31", "v32", "v33", "v34", "v35", "v36", "v37", "v38", "v39", "v40", "v41", "v42", "v43", "v44", "v45", "v46", "v47", "v48", "v49", "v50", "v51", "v52", "v53", "v54", "v55", "v56", "v57", "v58", "v59", "v60", "v61", "v62", "v63", "v64", "v65", "v66", "v67", "v68", "v69", "v70", "v71", "v72", "v73", "v74", "v75", "v76", "v77", "v78", "v79", "v255", "a255", "s30", "s31", "s32", "s33", "s34", "s35", "s36", "s37", "s38", "s39", "s40", "s41", "s42", "s43", "s44", "s45", "s46", "s47", "s48", "s49", "s50", "s51", "s52", "s53", "s54", "s55", "s56", "s57", "s58", "s59", "s60", "s61", "s62", "s63", "s64", "s65", "s66", "s67", "s68", "s69", "s70", "s71", "s72", "s73", "s74", "s75", "s76", "s77", "s78", "s79", "s80", "s81", "s82", "s83", "s84", "s85", "s86", "s87", "s88", "s89", "s90", "s91", "s92", "s93", "s94", "s95", "s96", "s97", "s98", "s99", "vcc", "scc", "memory");
    }
    __builtin_unreachable();
}

__device__ __forceinline__ void role_attn(const Params& p, int h0, int x4, int pr) {
    const unsigned lane = threadIdx.x & 63, i = lane & 15, g = lane >> 4;
    const int w = rfl(threadIdx.x >> 6), h = h0 + w;
    asm volatile(
        "s_nop 4\n"
        "s_mov_b64 s[40:41], %0\n"
        "s_and_b32 s41, s41, 0xffff\n"
        "s_mov_b32 s42, 236978176\n"
        "s_mov_b32 s43, 0x20000\n"
        "s_mov_b64 s[56:57], %1\n"
        "s_mov_b64 s[58:59], %2\n"
        "s_mov_b64 s[60:61], %3\n"
        "v_mov_b64 v[0:1], %4\n"
        "v_mov_b64 v[2:3], %5\n"
        "v_mov_b64 v[4:5], %6\n"
        "v_cmp_ge_i32 s[76:77], 0, v5\n"
        "v_cmp_ge_i32 s[78:79], -1, v5\n"
        "v_cmp_ge_i32 s[80:81], -2, v5\n"
        "v_cmp_ge_i32 s[82:83], -3, v5\n"
        "s_mov_b32 s84, 0\n"
        "s_mov_b32 s85, -1\n"
        "s_mov_b32 s75, 0x3e0293ee\n"
        "v_lshrrev_b32 v12, 4, v0\n"
        "s_mov_b32 s50, s56\n"
        "v_lshlrev_b32 v14, 7, v12\n"
        "s_sub_u32 s66, 0, s50\n"
        "s_mov_b32 s52, 32768\n"
        "s_mov_b32 s64, 0\n"
        ".Lat_pg0_top_%=:\n"
        "buffer_load_dword v13, v14, s[40:43], s52 offen sc1\n"
        "s_waitcnt vmcnt(0)\n"
        "v_cmp_gt_i32 vcc, 0, v13\n"
        "s_cbranch_vccz .Lat_pg0_ok_%=\n"
        "s_add_u32 s64, s64, 1\n"
        "s_and_b32 s65, s64, 255\n"
        "s_cbranch_scc1 .Lat_pg0_go_%=\n"
        "s_mov_b32 s65, 49152\n"
        "s_cmp_lt_u32 s64, 1048576\n"
        "s_cbranch_scc1 .Lat_pg0_chk_%=\n"
        "v_mov_b32 v13, 1\n"
        "buffer_store_dword v13, off, s[40:43], s65 sc1\n"
        "s_waitcnt vmcnt(0)\n"
        ".Lat_pg0_chk_%=:\n"
        "buffer_load_dword v13, off, s[40:43], s65 sc1\n"
        "s_waitcnt vmcnt(0)\n"
        "v_readfirstlane_b32 s65, v13\n"
        "s_nop 0\n"
        "s_cmp_lg_u32 s65, 1\n"
        "s_cbranch_scc1 .Lat_pg0_go_%=\n"
        "s_endpgm\n"
        ".Lat_pg0_go_%=:\n"
        "s_sleep 2\n"
        "s_branch .Lat_pg0_top_%=\n"
        ".Lat_pg0_ok_%=:\n"
        "v_cmp_eq_u32 s[52:53], 1, v13\n"
        "s_nop 1\n"
        "s_mov_b32 s54, 40960\n"
        "s_mov_b32 s64, 0\n"
        ".Lat_pg1_top_%=:\n"
        "buffer_load_dword v13, v14, s[40:43], s54 offen sc1\n"
        "s_waitcnt vmcnt(0)\n"
        "v_cmp_gt_i32 vcc, 0, v13\n"
        "s_cbranch_vccz .Lat_pg1_ok_%=\n"
        "s_add_u32 s64, s64, 1\n"
        "s_and_b32 s65, s64, 255\n"
        "s_cbranch_scc1 .Lat_pg1_go_%=\n"
        "s_mov_b32 s65, 49152\n"
        "s_cmp_lt_u32 s64, 1048576\n"
        "s_cbranch_scc1 .Lat_pg1_chk_%=\n"
        "v_mov_b32 v13, 1\n"
        "buffer_store_dword v13, off, s[40:43], s65 sc1\n"
        "s_waitcnt vmcnt(0)\n"
        ".Lat_pg1_chk_%=:\n"
        "buffer_load_dword v13, off, s[40:43], s65 sc1\n"
        "s_waitcnt vmcnt(0)\n"
        "v_readfirstlane_b32 s65, v13\n"
        "s_nop 0\n"
        "s_cmp_lg_u32 s65, 1\n"
        "s_cbranch_scc1 .Lat_pg1_go_%=\n"
        "s_endpgm\n"
        ".Lat_pg1_go_%=:\n"
        "s_sleep 2\n"
        "s_branch .Lat_pg1_top_%=\n"
        ".Lat_pg1_ok_%=:\n"
        "v_cmp_eq_u32 s[54:55], 1, v13\n"
        "s_nop 1\n"
        "s_mov_b32 vcc_lo, 0x55555555\n"
        "s_mov_b32 vcc_hi, 0x55555555\n"
        "s_xor_b64 vcc, vcc, s[52:53]\n"
        "s_bcnt1_i32_b64 s65, vcc\n"
        "s_sub_u32 s64, s65, 23\n"
        "s_cmp_lt_u32 s64, 19\n"
        "s_cbranch_scc1 .Lat_rawmap_%=\n"
        "s_cmp_lt_u32 s65, 32\n"
        "s_cselect_b32 s65, 0, -1\n"
        "s_xor_b32 s52, s65, 0x55555555\n"
        "s_xor_b32 s53, s65, 0x55555555\n"
        "s_xor_b32 s54, s65, 0x55555555\n"
        "s_xor_b32 s55, s65, 0xaaaaaaaa\n"
        ".Lat_rawmap_%=:\n"
        "s_xor_b32 s52, s52, s66\n"
        "s_xor_b32 s53, s53, s66\n"
        "s_xor_b32 s54, s54, s66\n"
        "s_xor_b32 s55, s55, s66\n"
        "s_and_b32 s71, s55, 1\n"
        "s_lshl_b32 s71, s71, 13\n"
        "s_add_u32 s60, s60, s71\n"
        "v_mov_b64 v[166:167], 0\n"
        "v_mov_b64 v[172:173], 0\n"
        "v_mov_b64 v[176:177], 0\n"
        "v_mov_b64 v[178:179], 0\n"
        "v_mov_b64 v[182:183], 0\n"
        "v_mov_b32 v10, 0xff61b1e6\n"
        "s_mov_b32 s68, 0\n"
        "s_mov_b32 s72, 0\n"
        "s_bitcmp1_b64 s[52:53], s68\n"
        "s_cselect_b32 s65, 8192, 0\n"
        "s_lshl_b32 s70, s68, 21\n"
        "s_add_u32 s70, s70, s65\n"
        ".Lat_top_%=:\n"
        "s_mov_b32 s69, s70\n"
        "s_lshl_b32 s67, s68, 1\n"
        "s_mov_b32 s64, 0\n"
        ".Lat_more_%=:\n"
        "buffer_load_dword v5, v1, s[40:43], s71 offen sc1\n"
        "s_mov_b32 s86, s72\n"
        "s_waitcnt vmcnt(0)\n"
        "v_min_i32_dpp v5, v5, v5 quad_perm:[1,0,3,2] row_mask:0xf bank_mask:0xf\n"
        "s_nop 1\n"
        "v_min_i32_dpp v5, v5, v5 quad_perm:[2,3,0,1] row_mask:0xf bank_mask:0xf\n"
        "s_nop 0\n"
        "v_readlane_b32 s73, v5, 0\n"
        "v_readlane_b32 s74, v5, 4\n"
        "s_nop 0\n"
        "s_bitcmp1_b32 s72, 0\n"
        "s_cbranch_scc1 .Lat_hasK_%=\n"
        "s_add_u32 s65, s67, 1\n"
        "s_cmp_ge_i32 s73, s65\n"
        "s_cbranch_scc0 .Lat_hasK_%=\n"
        "s_add_u32 s65, s69, s58\n"
        "s_add_u32 s65, s65, 524288\n"
        "s_add_u32 s66, s65, 4096\n"
        "buffer_load_dwordx4 v[48:51], v0, s[40:43], s65 offen offset:0 sc1\n"
        "buffer_load_dwordx4 v[52:55], v0, s[40:43], s65 offen offset:1024 sc1\n"
        "buffer_load_dwordx4 v[56:59], v0, s[40:43], s65 offen offset:2048 sc1\n"
        "buffer_load_dwordx4 v[60:63], v0, s[40:43], s65 offen offset:3072 sc1\n"
        "buffer_load_dwordx4 v[64:67], v0, s[40:43], s66 offen offset:0 sc1\n"
        "buffer_load_dwordx4 v[68:71], v0, s[40:43], s66 offen offset:1024 sc1\n"
        "buffer_load_dwordx4 v[72:75], v0, s[40:43], s66 offen offset:2048 sc1\n"
        "buffer_load_dwordx4 v[76:79], v0, s[40:43], s66 offen offset:3072 sc1\n"
        "s_or_b32 s72, s72, 1\n"
        ".Lat_hasK_%=:\n"
        "s_bitcmp1_b32 s72, 1\n"
        "s_cbranch_scc1 .Lat_hasV_%=\n"
        "s_add_u32 s65, s67, 1\n"
        "s_cmp_ge_i32 s73, s65\n"
        "s_cbranch_scc0 .Lat_hasV_%=\n"
        "s_add_u32 s65, s69, s58\n"
        "s_add_u32 s65, s65, 1048576\n"
        "s_add_u32 s66, s65, 4096\n"
        "buffer_load_dwordx4 v[80:83], v0, s[40:43], s65 offen offset:0 sc1\n"
        "buffer_load_dwordx4 v[84:87], v0, s[40:43], s65 offen offset:1024 sc1\n"
        "buffer_load_dwordx4 v[88:91], v0, s[40:43], s65 offen offset:2048 sc1\n"
        "buffer_load_dwordx4 v[92:95], v0, s[40:43], s65 offen offset:3072 sc1\n"
        "buffer_load_dwordx4 v[96:99], v0, s[40:43], s66 offen offset:0 sc1\n"
        "buffer_load_dwordx4 v[100:103], v0, s[40:43], s66 offen offset:1024 sc1\n"
        "buffer_load_dwordx4 v[104:107], v0, s[40:43], s66 offen offset:2048 sc1\n"
        "buffer_load_dwordx4 v[108:111], v0, s[40:43], s66 offen offset:3072 sc1\n"
        "s_or_b32 s72, s72, 2\n"
        ".Lat_hasV_%=:\n"
        "s_bitcmp1_b32 s72, 3\n"
        "s_cbranch_scc1 .Lat_hasM_%=\n"
        "s_cmp_ge_i32 s74, s68\n"
        "s_cbranch_scc0 .Lat_hasM_%=\n"
        "s_add_u32 s65, s69, s59\n"
        "buffer_load_dwordx4 v[112:115], v3, s[40:43], s65 offen offset:0 sc1\n"
        "buffer_load_dwordx4 v[116:119], v3, s[40:43], s65 offen offset:512 sc1\n"
        "buffer_load_dwordx4 v[120:123], v3, s[40:43], s65 offen offset:1024 sc1\n"
        "buffer_load_dwordx4 v[124:127], v3, s[40:43], s65 offen offset:1536 sc1\n"
        "buffer_load_dwordx4 v[128:131], v4, s[40:43], s65 offen offset:2048 sc1\n"
        "buffer_load_dwordx4 v[132:135], v4, s[40:43], s65 offen offset:2560 sc1\n"
        "buffer_load_dwordx4 v[136:139], v4, s[40:43], s65 offen offset:3072 sc1\n"
        "buffer_load_dwordx4 v[140:143], v4, s[40:43], s65 offen offset:3584 sc1\n"
        "s_or_b32 s72, s72, 8\n"
        ".Lat_hasM_%=:\n"
        "s_bitcmp1_b32 s72, 2\n"
        "s_cbranch_scc1 .Lat_hasQ_%=\n"
        "s_add_u32 s65, s67, 1\n"
        "s_cmp_ge_i32 s73, s65\n"
        "s_cbranch_scc0 .Lat_hasQ_%=\n"
        "s_add_u32 s65, s69, s58\n"
        "s_add_u32 s65, s65, 0\n"
        "s_add_u32 s66, s65, 4096\n"
        "buffer_load_dwordx4 v[16:19], v0, s[40:43], s65 offen offset:0 sc1\n"
        "buffer_load_dwordx4 v[20:23], v0, s[40:43], s65 offen offset:1024 sc1\n"
        "buffer_load_dwordx4 v[24:27], v0, s[40:43], s65 offen offset:2048 sc1\n"
        "buffer_load_dwordx4 v[28:31], v0, s[40:43], s65 offen offset:3072 sc1\n"
        "buffer_load_dwordx4 v[32:35], v0, s[40:43], s66 offen offset:0 sc1\n"
        "buffer_load_dwordx4 v[36:39], v0, s[40:43], s66 offen offset:1024 sc1\n"
        "buffer_load_dwordx4 v[40:43], v0, s[40:43], s66 offen offset:2048 sc1\n"
        "buffer_load_dwordx4 v[44:47], v0, s[40:43], s66 offen offset:3072 sc1\n"
        "s_or_b32 s72, s72, 4\n"
        ".Lat_hasQ_%=:\n"
        "s_and_b32 s65, s72, 7\n"
        "s_cmp_eq_u32 s65, 7\n"
        "s_cbranch_scc1 .Lat_T_%=\n"
        "s_add_u32 s64, s64, 1\n"
        "s_and_b32 s65, s64, 255\n"
        "s_cbranch_scc1 .Lat_sp_go_%=\n"
        "s_mov_b32 s65, 49152\n"
        "s_cmp_lt_u32 s64, 1048576\n"
        "s_cbranch_scc1 .Lat_sp_chk_%=\n"
        "v_mov_b32 v6, 1\n"
        "buffer_store_dword v6, off, s[40:43], s65 sc1\n"
        "s_waitcnt vmcnt(0)\n"
        ".Lat_sp_chk_%=:\n"
        "buffer_load_dword v6, off, s[40:43], s65 sc1\n"
        "s_waitcnt vmcnt(0)\n"
        "v_readfirstlane_b32 s65, v6\n"
        "s_nop 0\n"
        "s_cmp_lg_u32 s65, 1\n"
        "s_cbranch_scc1 .Lat_sp_go_%=\n"
        "s_endpgm\n"
        ".Lat_sp_go_%=:\n"
        "s_branch .Lat_more_%=\n"
        ".Lat_T_%=:\n"
        "s_add_u32 s66, s68, 1\n"
        "s_bitcmp1_b64 s[52:53], s66\n"
        "s_cselect_b32 s65, 8192, 0\n"
        "s_lshl_b32 s70, s66, 21\n"
        "s_add_u32 s70, s70, s65\n"
        "s_add_u32 s67, s67, 2\n"
        "s_cmp_eq_u32 s72, 15\n"
        "s_cbranch_scc1 .Lat_T2_%=\n"
        "s_waitcnt vmcnt(0)\n"
        "buffer_load_dword v5, v1, s[40:43], s71 offen sc1\n"
        "s_branch .Lat_T3_%=\n"
        ".Lat_T2_%=:\n"
        "s_bitcmp1_b32 s86, 2\n"
        "s_cbranch_scc1 .Lat_T3_%=\n"
        "s_waitcnt vmcnt(0)\n"
        ".Lat_T3_%=:\n"
        "v_mfma_f32_16x16x32_bf16 v[144:147], v[48:51], v[16:19], 0\n"
        "v_mfma_f32_16x16x32_bf16 v[152:155], v[52:55], v[20:23], 0\n"
        "v_mfma_f32_16x16x32_bf16 v[148:151], v[48:51], v[20:23], 0\n"
        "v_mfma_f32_16x16x32_bf16 v[144:147], v[56:59], v[24:27], v[144:147]\n"
        "v_mfma_f32_16x16x32_bf16 v[152:155], v[60:63], v[28:31], v[152:155]\n"
        "v_mfma_f32_16x16x32_bf16 v[148:151], v[56:59], v[28:31], v[148:151]\n"
        "v_mfma_f32_16x16x32_bf16 v[144:147], v[64:67], v[32:35], v[144:147]\n"
        "v_mfma_f32_16x16x32_bf16 v[152:155], v[68:71], v[36:39], v[152:155]\n"
        "v_mfma_f32_16x16x32_bf16 v[148:151], v[64:67], v[36:39], v[148:151]\n"
        "v_mfma_f32_16x16x32_bf16 v[144:147], v[72:75], v[40:43], v[144:147]\n"
        "v_mfma_f32_16x16x32_bf16 v[152:155], v[76:79], v[44:47], v[152:155]\n"
        "v_mfma_f32_16x16x32_bf16 v[148:151], v[72:75], v[44:47], v[148:151]\n"
        "v_cndmask_b32 v144, v10, v144, s[76:77]\n"
        "v_cndmask_b32 v145, v10, v145, s[78:79]\n"
        "v_cndmask_b32 v146, v10, v146, s[80:81]\n"
        "v_cndmask_b32 v147, v10, v147, s[82:83]\n"
        "v_cndmask_b32 v152, v10, v152, s[76:77]\n"
        "v_cndmask_b32 v153, v10, v153, s[78:79]\n"
        "v_cndmask_b32 v154, v10, v154, s[80:81]\n"
        "v_cndmask_b32 v155, v10, v155, s[82:83]\n"
        "v_max3_f32 v8, v144, v145, v146\n"
        "v_max3_f32 v9, v152, v153, v154\n"
        "v_max_f32 v8, v8, v147\n"
        "v_max3_f32 v9, v9, v155, v148\n"
        "v_max3_f32 v9, v9, v149, v150\n"
        "v_max_f32 v9, v9, v151\n"
        "v_mul_f32 v144, s75, v144\n"
        "v_mul_f32 v145, s75, v145\n"
        "v_mul_f32 v146, s75, v146\n"
        "v_mul_f32 v147, s75, v147\n"
        "v_mul_f32 v148, s75, v148\n"
        "v_mul_f32 v149, s75, v149\n"
        "v_mul_f32 v150, s75, v150\n"
        "v_mul_f32 v151, s75, v151\n"
        "v_mul_f32 v152, s75, v152\n"
        "v_mul_f32 v153, s75, v153\n"
        "v_mul_f32 v154, s75, v154\n"
        "v_mul_f32 v155, s75, v155\n"
        "s_cmp_eq_u32 s72, 15\n"
        "s_cbranch_scc1 .Lat_hasm_%=\n"
        "s_mov_b32 s64, 0\n"
        ".Lat_wm_%=:\n"
        "s_waitcnt vmcnt(0)\n"
        "v_min_i32_dpp v5, v5, v5 quad_perm:[1,0,3,2] row_mask:0xf bank_mask:0xf\n"
        "s_nop 1\n"
        "v_min_i32_dpp v5, v5, v5 quad_perm:[2,3,0,1] row_mask:0xf bank_mask:0xf\n"
        "s_nop 0\n"
        "v_readlane_b32 s73, v5, 0\n"
        "v_readlane_b32 s74, v5, 4\n"
        "s_nop 0\n"
        "s_cmp_ge_i32 s74, s68\n"
        "s_cbranch_scc1 .Lat_gm_%=\n"
        "s_add_u32 s64, s64, 1\n"
        "s_and_b32 s65, s64, 255\n"
        "s_cbranch_scc1 .Lat_sq_go_%=\n"
        "s_mov_b32 s65, 49152\n"
        "s_cmp_lt_u32 s64, 1048576\n"
        "s_cbranch_scc1 .Lat_sq_chk_%=\n"
        "v_mov_b32 v6, 1\n"
        "buffer_store_dword v6, off, s[40:43], s65 sc1\n"
        "s_waitcnt vmcnt(0)\n"
        ".Lat_sq_chk_%=:\n"
        "buffer_load_dword v6, off, s[40:43], s65 sc1\n"
        "s_waitcnt vmcnt(0)\n"
        "v_readfirstlane_b32 s65, v6\n"
        "s_nop 0\n"
        "s_cmp_lg_u32 s65, 1\n"
        "s_cbranch_scc1 .Lat_sq_go_%=\n"
        "s_endpgm\n"
        ".Lat_sq_go_%=:\n"
        "buffer_load_dword v5, v1, s[40:43], s71 offen sc1\n"
        "s_branch .Lat_wm_%=\n"
        ".Lat_gm_%=:\n"
        "s_add_u32 s65, s69, s59\n"
        "buffer_load_dwordx4 v[112:115], v3, s[40:43], s65 offen offset:0 sc1\n"
        "buffer_load_dwordx4 v[116:119], v3, s[40:43], s65 offen offset:512 sc1\n"
        "buffer_load_dwordx4 v[120:123], v3, s[40:43], s65 offen offset:1024 sc1\n"
        "buffer_load_dwordx4 v[124:127], v3, s[40:43], s65 offen offset:1536 sc1\n"
        "buffer_load_dwordx4 v[128:131], v4, s[40:43], s65 offen offset:2048 sc1\n"
        "buffer_load_dwordx4 v[132:135], v4, s[40:43], s65 offen offset:2560 sc1\n"
        "buffer_load_dwordx4 v[136:139], v4, s[40:43], s65 offen offset:3072 sc1\n"
        "buffer_load_dwordx4 v[140:143], v4, s[40:43], s65 offen offset:3584 sc1\n"
        ".Lat_hasm_%=:\n"
        "s_mov_b32 s72, 0\n"
        "s_add_u32 s65, s70, s58\n"
        "s_add_u32 s65, s65, 524288\n"
        "s_add_u32 s66, s67, 1\n"
        "s_cmp_ge_i32 s73, s66\n"
        "s_cselect_b32 s65, s65, 241172480\n"
        "s_cselect_b32 s66, 1, 0\n"
        "s_or_b32 s72, s72, s66\n"
        "s_add_u32 s66, s65, 4096\n"
        "buffer_load_dwordx4 v[48:51], v0, s[40:43], s65 offen offset:0 sc1\n"
        "buffer_load_dwordx4 v[52:55], v0, s[40:43], s65 offen offset:1024 sc1\n"
        "buffer_load_dwordx4 v[56:59], v0, s[40:43], s65 offen offset:2048 sc1\n"
        "buffer_load_dwordx4 v[60:63], v0, s[40:43], s65 offen offset:3072 sc1\n"
        "buffer_load_dwordx4 v[64:67], v0, s[40:43], s66 offen offset:0 sc1\n"
        "buffer_load_dwordx4 v[68:71], v0, s[40:43], s66 offen offset:1024 sc1\n"
        "buffer_load_dwordx4 v[72:75], v0, s[40:43], s66 offen offset:2048 sc1\n"
        "buffer_load_dwordx4 v[76:79], v0, s[40:43], s66 offen offset:3072 sc1\n"
        "s_add_u32 s65, s70, s58\n"
        "s_add_u32 s65, s65, 0\n"
        "s_add_u32 s66, s67, 1\n"
        "s_cmp_ge_i32 s73, s66\n"
        "s_cselect_b32 s65, s65, 241172480\n"
        "s_cselect_b32 s66, 4, 0\n"
        "s_or_b32 s72, s72, s66\n"
        "s_add_u32 s66, s65, 4096\n"
        "s_waitcnt vmcnt(15)\n"
        "v_mfma_f32_16x16x32_bf16 v[156:159], v[112:115], v[16:19], 0\n"
        "s_waitcnt vmcnt(14)\n"
        "v_mfma_f32_16x16x32_bf16 v[156:159], v[116:119], v[24:27], v[156:159]\n"
        "s_waitcnt vmcnt(13)\n"
        "v_mfma_f32_16x16x32_bf16 v[156:159], v[120:123], v[32:35], v[156:159]\n"
        "s_waitcnt vmcnt(12)\n"
        "v_mfma_f32_16x16x32_bf16 v[156:159], v[124:127], v[40:43], v[156:159]\n"
        "v_mfma_f32_16x16x32_bf16 v[160:163], v[112:115], v[20:23], 0\n"
        "v_mfma_f32_16x16x32_bf16 v[160:163], v[116:119], v[28:31], v[160:163]\n"
        "v_mfma_f32_16x16x32_bf16 v[160:163], v[120:123], v[36:39], v[160:163]\n"
        "v_mfma_f32_16x16x32_bf16 v[160:163], v[124:127], v[44:47], v[160:163]\n"
        "v_max3_f32 v8, v8, v156, v157\n"
        "buffer_load_dwordx4 v[16:19], v0, s[40:43], s65 offen offset:0 sc1\n"
        "v_max3_f32 v216, v8, v158, v159\n"
        "v_max3_f32 v217, v8, v158, v159\n"
        "v_mul_f32 v156, s75, v156\n"
        "v_mul_f32 v157, s75, v157\n"
        "v_permlane16_swap_b32 v216, v217\n"
        "v_max_f32 v8, v216, v217\n"
        "buffer_load_dwordx4 v[20:23], v0, s[40:43], s65 offen offset:1024 sc1\n"
        "v_max_f32 v216, v216, v217\n"
        "v_mul_f32 v158, s75, v158\n"
        "v_mul_f32 v159, s75, v159\n"
        "v_permlane32_swap_b32 v8, v216\n"
        "v_max_f32 v8, v8, v216\n"
        "v_mul_f32 v8, s75, v8\n"
        "buffer_load_dwordx4 v[24:27], v0, s[40:43], s65 offen offset:2048 sc1\n"
        "v_cndmask_b32 v216, v8, -v10, s[84:85]\n"
        "v_sub_f32 v144, v144, v8\n"
        "v_exp_f32 v144, v144\n"
        "v_sub_f32 v145, v145, v8\n"
        "v_exp_f32 v145, v145\n"
        "v_sub_f32 v146, v146, v8\n"
        "buffer_load_dwordx4 v[28:31], v0, s[40:43], s65 offen offset:3072 sc1\n"
        "v_exp_f32 v146, v146\n"
        "v_sub_f32 v147, v147, v8\n"
        "v_exp_f32 v147, v147\n"
        "v_sub_f32 v156, v156, v216\n"
        "v_exp_f32 v156, v156\n"
        "v_sub_f32 v157, v157, v216\n"
        "buffer_load_dwordx4 v[32:35], v0, s[40:43], s66 offen offset:0 sc1\n"
        "v_exp_f32 v157, v157\n"
        "v_sub_f32 v158, v158, v216\n"
        "v_exp_f32 v158, v158\n"
        "v_sub_f32 v159, v159, v216\n"
        "v_exp_f32 v159, v159\n"
        "v_add_f32 v216, v144, v145\n"
        "buffer_load_dwordx4 v[36:39], v0, s[40:43], s66 offen offset:1024 sc1\n"
        "v_add_f32 v217, v146, v147\n"
        "v_add_f32 v216, v216, v156\n"
        "v_add_f32 v217, v217, v157\n"
        "v_add_f32 v216, v216, v158\n"
        "v_add_f32 v217, v217, v159\n"
        "v_add_f32 v8, v216, v217\n"
        "buffer_load_dwordx4 v[40:43], v0, s[40:43], s66 offen offset:2048 sc1\n"
        "v_add_f32 v216, v216, v217\n"
        "s_nop 1\n"
        "v_permlane16_swap_b32 v8, v216\n"
        "v_add_f32 v217, v8, v216\n"
        "v_add_f32 v216, v8, v216\n"
        "s_nop 1\n"
        "buffer_load_dwordx4 v[44:47], v0, s[40:43], s66 offen offset:3072 sc1\n"
        "v_permlane32_swap_b32 v216, v217\n"
        "v_add_f32 v8, v216, v217\n"
        "v_rcp_f32 v8, v8\n"
        "s_nop 0\n"
        "v_mul_f32 v156, v156, v8\n"
        "v_mul_f32 v157, v157, v8\n"
        "v_mul_f32 v158, v158, v8\n"
        "v_mul_f32 v159, v159, v8\n"
        "v_cvt_pk_bf16_f32 v174, v156, v157\n"
        "v_cvt_pk_bf16_f32 v175, v158, v159\n"
        "v_mul_f32 v144, v144, v8\n"
        "v_mul_f32 v145, v145, v8\n"
        "v_cvt_pk_bf16_f32 v164, v144, v145\n"
        "v_mul_f32 v146, v146, v8\n"
        "v_mul_f32 v147, v147, v8\n"
        "v_cvt_pk_bf16_f32 v165, v146, v147\n"
        "buffer_load_dword v5, v1, s[40:43], s71 offen sc1\n"
        "s_add_u32 s65, s69, s58\n"
        "s_add_u32 s66, s65, 4096\n"
        "s_waitcnt vmcnt(17)\n"
        "v_mfma_f32_16x16x32_bf16 v[184:187], v[128:131], v[174:177], 0\n"
        "v_max3_f32 v9, v9, v160, v161\n"
        "v_max3_f32 v220, v9, v162, v163\n"
        "v_max3_f32 v221, v9, v162, v163\n"
        "v_mul_f32 v160, s75, v160\n"
        "v_mul_f32 v161, s75, v161\n"
        "v_mfma_f32_16x16x32_bf16 v[188:191], v[128:131], v[172:175], 0\n"
        "v_permlane16_swap_b32 v220, v221\n"
        "v_max_f32 v9, v220, v221\n"
        "v_max_f32 v220, v220, v221\n"
        "v_mul_f32 v162, s75, v162\n"
        "v_mul_f32 v163, s75, v163\n"
        "v_mfma_f32_16x16x32_bf16 v[184:187], v[80:83], v[164:167], v[184:187]\n"
        "v_permlane32_swap_b32 v9, v220\n"
        "v_max_f32 v9, v9, v220\n"
        "v_mul_f32 v9, s75, v9\n"
        "v_cndmask_b32 v220, v9, -v10, s[84:85]\n"
        "v_sub_f32 v148, v148, v9\n"
        "v_mfma_f32_16x16x32_bf16 v[188:191], v[84:87], v[164:167], v[188:191]\n"
        "v_exp_f32 v148, v148\n"
        "v_sub_f32 v149, v149, v9\n"
        "v_exp_f32 v149, v149\n"
        "v_sub_f32 v150, v150, v9\n"
        "v_mfma_f32_16x16x32_bf16 v[192:195], v[132:135], v[174:177], 0\n"
        "v_exp_f32 v150, v150\n"
        "v_sub_f32 v151, v151, v9\n"
        "v_exp_f32 v151, v151\n"
        "v_sub_f32 v152, v152, v9\n"
        "v_mfma_f32_16x16x32_bf16 v[196:199], v[132:135], v[172:175], 0\n"
        "v_cvt_pk_bf16_f32 v184, v184, v185\n"
        "v_cvt_pk_bf16_f32 v185, v186, v187\n"
        "v_cvt_pk_bf16_f32 v186, v188, v189\n"
        "v_cvt_pk_bf16_f32 v187, v190, v191\n"
        "buffer_store_dwordx4 v[184:187], v0, s[40:43], s65 offen offset:0 sc1\n"
        "v_mfma_f32_16x16x32_bf16 v[192:195], v[88:91], v[164:167], v[192:195]\n"
        "v_exp_f32 v152, v152\n"
        "v_sub_f32 v153, v153, v9\n"
        "v_exp_f32 v153, v153\n"
        "v_sub_f32 v154, v154, v9\n"
        "v_mfma_f32_16x16x32_bf16 v[196:199], v[92:95], v[164:167], v[196:199]\n"
        "v_exp_f32 v154, v154\n"
        "v_sub_f32 v155, v155, v9\n"
        "v_exp_f32 v155, v155\n"
        "v_sub_f32 v160, v160, v220\n"
        "v_mfma_f32_16x16x32_bf16 v[200:203], v[136:139], v[174:177], 0\n"
        "v_exp_f32 v160, v160\n"
        "v_sub_f32 v161, v161, v220\n"
        "v_exp_f32 v161, v161\n"
        "v_sub_f32 v162, v162, v220\n"
        "v_mfma_f32_16x16x32_bf16 v[204:207], v[136:139], v[172:175], 0\n"
        "v_cvt_pk_bf16_f32 v192, v192, v193\n"
        "v_cvt_pk_bf16_f32 v193, v194, v195\n"
        "v_cvt_pk_bf16_f32 v194, v196, v197\n"
        "v_cvt_pk_bf16_f32 v195, v198, v199\n"
        "buffer_store_dwordx4 v[192:195], v0, s[40:43], s65 offen offset:1024 sc1\n"
        "v_mfma_f32_16x16x32_bf16 v[200:203], v[96:99], v[164:167], v[200:203]\n"
        "v_exp_f32 v162, v162\n"
        "v_sub_f32 v163, v163, v220\n"
        "v_exp_f32 v163, v163\n"
        "v_add_f32 v220, v148, v149\n"
        "v_mfma_f32_16x16x32_bf16 v[204:207], v[100:103], v[164:167], v[204:207]\n"
        "v_add_f32 v221, v150, v151\n"
        "v_add_f32 v220, v220, v152\n"
        "v_add_f32 v221, v221, v153\n"
        "v_add_f32 v220, v220, v154\n"
        "v_add_f32 v221, v221, v155\n"
        "v_add_f32 v220, v220, v160\n"
        "v_mfma_f32_16x16x32_bf16 v[208:211], v[140:143], v[174:177], 0\n"
        "v_add_f32 v221, v221, v161\n"
        "v_add_f32 v220, v220, v162\n"
        "v_add_f32 v221, v221, v163\n"
        "v_add_f32 v9, v220, v221\n"
        "v_add_f32 v220, v220, v221\n"
        "v_mfma_f32_16x16x32_bf16 v[212:215], v[140:143], v[172:175], 0\n"
        "v_cvt_pk_bf16_f32 v200, v200, v201\n"
        "v_cvt_pk_bf16_f32 v201, v202, v203\n"
        "v_cvt_pk_bf16_f32 v202, v204, v205\n"
        "v_cvt_pk_bf16_f32 v203, v206, v207\n"
        "buffer_store_dwordx4 v[200:203], v0, s[40:43], s65 offen offset:2048 sc1\n"
        "v_mfma_f32_16x16x32_bf16 v[208:211], v[104:107], v[164:167], v[208:211]\n"
        "s_nop 1\n"
        "v_permlane16_swap_b32 v9, v220\n"
        "v_add_f32 v221, v9, v220\n"
        "v_add_f32 v220, v9, v220\n"
        "v_mfma_f32_16x16x32_bf16 v[212:215], v[108:111], v[164:167], v[212:215]\n"
        "s_nop 1\n"
        "v_permlane32_swap_b32 v220, v221\n"
        "v_add_f32 v9, v220, v221\n"
        "v_rcp_f32 v9, v9\n"
        "s_nop 0\n"
        "v_mul_f32 v160, v160, v9\n"
        "v_mul_f32 v161, v161, v9\n"
        "v_mul_f32 v162, v162, v9\n"
        "v_mul_f32 v163, v163, v9\n"
        "v_cvt_pk_bf16_f32 v180, v160, v161\n"
        "v_cvt_pk_bf16_f32 v181, v162, v163\n"
        "v_mul_f32 v148, v148, v9\n"
        "v_mul_f32 v149, v149, v9\n"
        "v_cvt_pk_bf16_f32 v168, v148, v149\n"
        "v_mul_f32 v150, v150, v9\n"
        "v_mul_f32 v151, v151, v9\n"
        "v_cvt_pk_bf16_f32 v169, v150, v151\n"
        "v_mul_f32 v152, v152, v9\n"
        "v_mul_f32 v153, v153, v9\n"
        "v_cvt_pk_bf16_f32 v170, v152, v153\n"
        "v_mul_f32 v154, v154, v9\n"
        "v_mul_f32 v155, v155, v9\n"
        "v_cvt_pk_bf16_f32 v171, v154, v155\n"
        "v_cvt_pk_bf16_f32 v208, v208, v209\n"
        "v_cvt_pk_bf16_f32 v209, v210, v211\n"
        "v_cvt_pk_bf16_f32 v210, v212, v213\n"
        "v_cvt_pk_bf16_f32 v211, v214, v215\n"
        "buffer_store_dwordx4 v[208:211], v0, s[40:43], s65 offen offset:3072 sc1\n"
        "v_mfma_f32_16x16x32_bf16 v[184:187], v[128:131], v[180:183], 0\n"
        "v_mfma_f32_16x16x32_bf16 v[188:191], v[128:131], v[178:181], 0\n"
        "v_mfma_f32_16x16x32_bf16 v[184:187], v[80:83], v[168:171], v[184:187]\n"
        "v_mfma_f32_16x16x32_bf16 v[188:191], v[84:87], v[168:171], v[188:191]\n"
        "v_mfma_f32_16x16x32_bf16 v[192:195], v[132:135], v[180:183], 0\n"
        "v_mfma_f32_16x16x32_bf16 v[196:199], v[132:135], v[178:181], 0\n"
        "v_cvt_pk_bf16_f32 v184, v184, v185\n"
        "v_cvt_pk_bf16_f32 v185, v186, v187\n"
        "v_cvt_pk_bf16_f32 v186, v188, v189\n"
        "v_mfma_f32_16x16x32_bf16 v[192:195], v[88:91], v[168:171], v[192:195]\n"
        "v_cvt_pk_bf16_f32 v187, v190, v191\n"
        "buffer_store_dwordx4 v[184:187], v0, s[40:43], s66 offen offset:0 sc1\n"
        "v_mfma_f32_16x16x32_bf16 v[196:199], v[92:95], v[168:171], v[196:199]\n"
        "v_mfma_f32_16x16x32_bf16 v[200:203], v[136:139], v[180:183], 0\n"
        "v_mfma_f32_16x16x32_bf16 v[204:207], v[136:139], v[178:181], 0\n"
        "v_cvt_pk_bf16_f32 v192, v192, v193\n"
        "v_cvt_pk_bf16_f32 v193, v194, v195\n"
        "v_cvt_pk_bf16_f32 v194, v196, v197\n"
        "v_mfma_f32_16x16x32_bf16 v[200:203], v[96:99], v[168:171], v[200:203]\n"
        "v_cvt_pk_bf16_f32 v195, v198, v199\n"
        "buffer_store_dwordx4 v[192:195], v0, s[40:43], s66 offen offset:1024 sc1\n"
        "v_mfma_f32_16x16x32_bf16 v[204:207], v[100:103], v[168:171], v[204:207]\n"
        "v_mfma_f32_16x16x32_bf16 v[208:211], v[140:143], v[180:183], 0\n"
        "v_mfma_f32_16x16x32_bf16 v[212:215], v[140:143], v[178:181], 0\n"
        "v_cvt_pk_bf16_f32 v200, v200, v201\n"
        "v_cvt_pk_bf16_f32 v201, v202, v203\n"
        "v_cvt_pk_bf16_f32 v202, v204, v205\n"
        "v_mfma_f32_16x16x32_bf16 v[208:211], v[104:107], v[168:171], v[208:211]\n"
        "v_cvt_pk_bf16_f32 v203, v206, v207\n"
        "buffer_store_dwordx4 v[200:203], v0, s[40:43], s66 offen offset:2048 sc1\n"
        "v_mfma_f32_16x16x32_bf16 v[212:215], v[108:111], v[168:171], v[212:215]\n"
        "s_waitcnt vmcnt(7)\n"
        "v_min_i32_dpp v5, v5, v5 quad_perm:[1,0,3,2] row_mask:0xf bank_mask:0xf\n"
        "s_nop 1\n"
        "v_min_i32_dpp v5, v5, v5 quad_perm:[2,3,0,1] row_mask:0xf bank_mask:0xf\n"
        "s_nop 0\n"
        "v_readlane_b32 s73, v5, 0\n"
        "v_readlane_b32 s74, v5, 4\n"
        "s_nop 0\n"
        "s_nop 0\n"
        "v_cvt_pk_bf16_f32 v208, v208, v209\n"
        "v_cvt_pk_bf16_f32 v209, v210, v211\n"
        "v_cvt_pk_bf16_f32 v210, v212, v213\n"
        "v_cvt_pk_bf16_f32 v211, v214, v215\n"
        "buffer_store_dwordx4 v[208:211], v0, s[40:43], s66 offen offset:3072 sc1\n"
        "s_lshl_b32 s65, s68, 1\n"
        "v_mov_b32 v6, s65\n"
        "s_waitcnt vmcnt(4)\n"
        "buffer_store_dword v6, v2, s[40:43], s60 offen sc1\n"
        "s_add_u32 s65, s70, s58\n"
        "s_add_u32 s65, s65, 1048576\n"
        "s_add_u32 s66, s67, 1\n"
        "s_cmp_ge_i32 s73, s66\n"
        "s_cselect_b32 s65, s65, 241172480\n"
        "s_cselect_b32 s66, 2, 0\n"
        "s_or_b32 s72, s72, s66\n"
        "s_add_u32 s66, s65, 4096\n"
        "buffer_load_dwordx4 v[80:83], v0, s[40:43], s65 offen offset:0 sc1\n"
        "buffer_load_dwordx4 v[84:87], v0, s[40:43], s65 offen offset:1024 sc1\n"
        "buffer_load_dwordx4 v[88:91], v0, s[40:43], s65 offen offset:2048 sc1\n"
        "buffer_load_dwordx4 v[92:95], v0, s[40:43], s65 offen offset:3072 sc1\n"
        "buffer_load_dwordx4 v[96:99], v0, s[40:43], s66 offen offset:0 sc1\n"
        "buffer_load_dwordx4 v[100:103], v0, s[40:43], s66 offen offset:1024 sc1\n"
        "buffer_load_dwordx4 v[104:107], v0, s[40:43], s66 offen offset:2048 sc1\n"
        "buffer_load_dwordx4 v[108:111], v0, s[40:43], s66 offen offset:3072 sc1\n"
        "v_add_u32 v6, 1, v6\n"
        "s_waitcnt vmcnt(9)\n"
        "buffer_store_dword v6, v2, s[40:43], s60 offen sc1\n"
        "s_add_u32 s68, s68, 1\n"
        "s_cmp_lt_u32 s68, 64\n"
        "s_cbranch_scc1 .Lat_top_%=\n"
        "s_endpgm\n"
        :: "s"(p.ws), "s"(pair(x4, pr)), "s"(pair(PAGE + h * 16384, PAGE + spread(A_KM) + (h >> 1) * 16384 + (h & 1) * 4096)), "s"(pair(0, w)),
           "v"(pair(lane * 16, lane < 8 ? flag(((lane & 4) ? F_CH : F_S1 + 128) + 4 * h + (lane & 3)) : OOB)), "v"(pair(lane < 3 ? flag(F_AT + 3 * h + lane) : OOB, (g * 8 + (i & 7)) * 16)),
           "v"(pair((g & 1) * 256 + i * 16, 4 * g - i))
        : "v0", "v1", "v2", "v3", "v4", "v5", "v6", "v7", "v8", "v9", "v10", "v11", "v12", "v13", "v14", "v15", "v16", "v17", "v18", "v19", "v20", "v21", "v22", "v23", "v24", "v25", "v26", "v27", "v28", "v29", "v30", "v31", "v32", "v33", "v34", "v35", "v36", "v37", "v38", "v39", "v40", "v41", "v42", "v43", "v44", "v45", "v46", "v47", "v48", "v49", "v50", "v51", "v52", "v53", "v54", "v55", "v56", "v57", "v58", "v59", "v60", "v61", "v62", "v63", "v64", "v65", "v66", "v67", "v68", "v69", "v70", "v71", "v72", "v73", "v74", "v75", "v76", "v77", "v78", "v79", "v255", "a255", "s30", "s31", "s32", "s33", "s34", "s35", "s36", "s37", "s38", "s39", "s40", "s41", "s42", "s43", "s44", "s45", "s46", "s47", "s48", "s49", "s50", "s51", "s52", "s53", "s54", "s55", "s56", "s57", "s58", "s59", "s60", "s61", "s62", "s63", "s64", "s65", "s66", "s67", "s68", "s69", "s70", "s71", "s72", "s73", "s74", "s75", "s76", "s77", "s78", "s79", "s80", "s81", "s82", "s83", "s84", "s85", "s86", "s87", "s88", "s89", "s90", "s91", "s92", "s93", "s94", "s95", "s96", "s97", "s98", "s99", "vcc", "scc", "memory");
    __builtin_unreachable();
}

template <bool R5>
__device__ __forceinline__ void role_red(const Params& p, int a, int x4, int pr, u64 epoch) {
    const unsigned lane = threadIdx.x & 63, i = lane & 15, g = lane >> 4;
    const int w = rfl(threadIdx.x >> 6);
    if (w == 3) return;
    unsigned to[3], po[3], one[4];
#pragma unroll
    for (int n = 0; n < 3; ++n) { const unsigned b = 3 * w + n; to[n] = b < 8 ? a * 16384 + b * 1024 : 65536 + a * 1024; po[n] = part_off(a, 0, 8 * b); }
#pragma unroll
    for (int e = 0; e < 4; ++e) one[e] = ((i & 7) == 2 * e) ? 0x3f80u : (((i & 7) == 2 * e + 1) ? 0x3f800000u : 0u);
    const u64 o0 = pair(one[0], one[1]), o1 = pair(one[2], one[3]);
    const bool lo = i < 8, r0 = g == 2 * (i >> 3), r1 = g == 1 + 2 * (i >> 3);
    const unsigned fin = lane < 16 ? flag((R5 ? F_F2 : F_OP) + a * 64 + w * 16 + lane) : OOB;
    const unsigned fout = (w == 0 && lane < (R5 ? 64 : 48)) ? flag((R5 ? F_XN : F_R3) + 8 * (lane >> 1) + 2 * a + (lane & 1)) : OOB;
#define RED_ARGS :: "s"(p.ws), "s"(p.x), "s"(p.out), "s"(pair(x4 | (a >> 1) << 8, pr)), "s"(pair(to[0], to[1])), "s"(pair(to[2], po[0])), "s"(pair(po[1], po[2])), \
        "v"(pair(lane * 16, fin)), "v"(pair(fout, g * 16384 + i * 16)), "v"(pair(g * (w == 2 ? 1024 : 16384) + i * 16, (8 * a + (lane & 7)) * 1152 + w * 384 + (lane >> 3) * 16)), \
        "v"(pair(w * 64 + (lane & 7) * 8, (lane & 7) * 8)), "v"(lo ? o0 : 0), "v"(lo ? o1 : 0), "v"(lo ? 0 : o0), "v"(lo ? 0 : o1), \
        "v"(r0 ? o0 : 0), "v"(r0 ? o1 : 0), "v"(r1 ? o0 : 0), "v"(r1 ? o1 : 0), "v"(epoch + 8), "v"(pair(a == 0 && threadIdx.x == 0 ? 0u : OOB, 0))
    if constexpr (R5) {
        asm volatile(
        "s_nop 4\n"
        "s_mov_b64 s[40:41], %0\n"
        "s_and_b32 s41, s41, 0xffff\n"
        "s_mov_b32 s42, 236978176\n"
        "s_mov_b32 s43, 0x20000\n"
        "s_mov_b64 s[44:45], %1\n"
        "s_and_b32 s45, s45, 0xffff\n"
        "s_mov_b32 s46, 2359296\n"
        "s_mov_b32 s47, 0x20000\n"
        "s_mov_b64 s[56:57], %3\n"
        "s_mov_b64 s[58:59], %4\n"
        "s_mov_b64 s[60:61], %5\n"
        "s_mov_b64 s[62:63], %6\n"
        "v_mov_b64 v[0:1], %7\n"
        "v_mov_b64 v[2:3], %8\n"
        "v_mov_b64 v[4:5], %9\n"
        "v_mov_b64 v[6:7], %10\n"
        "v_mov_b64 v[8:9], %11\n"
        "v_mov_b64 v[10:11], %12\n"
        "v_mov_b64 v[12:13], %13\n"
        "v_mov_b64 v[14:15], %14\n"
        "v_mov_b64 v[16:17], %15\n"
        "v_mov_b64 v[18:19], %16\n"
        "v_mov_b64 v[20:21], %17\n"
        "v_mov_b64 v[22:23], %18\n"
        "v_mov_b64 v[24:25], %19\n"
        "v_mov_b64 v[26:27], %20\n"
        "v_mov_b64 v[200:201], v[24:25]\n"
        "v_mov_b32 v202, v26\n"
        "s_lshr_b32 s74, s56, 8\n"
        "s_and_b32 s56, s56, 1\n"
        "s_mov_b32 s68, 0\n"
        "s_mul_i32 s65, s68, 36864\n"
        "buffer_load_dwordx4 v[80:83], v5, s[44:47], s65 offen offset:0\n"
        "buffer_load_dwordx4 v[84:87], v5, s[44:47], s65 offen offset:128\n"
        "buffer_load_dwordx4 v[88:91], v5, s[44:47], s65 offen offset:256\n"
        "v_lshrrev_b32 v26, 4, v0\n"
        "s_mov_b32 s50, s56\n"
        "v_lshlrev_b32 v28, 7, v26\n"
        "s_sub_u32 s66, 0, s50\n"
        "s_mov_b32 s52, 32768\n"
        "s_mov_b32 s64, 0\n"
        ".Lr5_pg0_top_%=:\n"
        "buffer_load_dword v27, v28, s[40:43], s52 offen sc1\n"
        "s_waitcnt vmcnt(0)\n"
        "v_cmp_gt_i32 vcc, 0, v27\n"
        "s_cbranch_vccz .Lr5_pg0_ok_%=\n"
        "s_add_u32 s64, s64, 1\n"
        "s_and_b32 s65, s64, 255\n"
        "s_cbranch_scc1 .Lr5_pg0_go_%=\n"
        "s_mov_b32 s65, 49152\n"
        "s_cmp_lt_u32 s64, 1048576\n"
        "s_cbranch_scc1 .Lr5_pg0_chk_%=\n"
        "v_mov_b32 v27, 1\n"
        "buffer_store_dword v27, off, s[40:43], s65 sc1\n"
        "s_waitcnt vmcnt(0)\n"
        ".Lr5_pg0_chk_%=:\n"
        "buffer_load_dword v27, off, s[40:43], s65 sc1\n"
        "s_waitcnt vmcnt(0)\n"
        "v_readfirstlane_b32 s65, v27\n"
        "s_nop 0\n"
        "s_cmp_lg_u32 s65, 1\n"
        "s_cbranch_scc1 .Lr5_pg0_go_%=\n"
        "s_endpgm\n"
        ".Lr5_pg0_go_%=:\n"
        "s_sleep 2\n"
        "s_branch .Lr5_pg0_top_%=\n"
        ".Lr5_pg0_ok_%=:\n"
        "v_cmp_eq_u32 s[52:53], 1, v27\n"
        "s_nop 1\n"
        "s_mov_b32 s54, 40960\n"
        "s_mov_b32 s64, 0\n"
        ".Lr5_pg1_top_%=:\n"
        "buffer_load_dword v27, v28, s[40:43], s54 offen sc1\n"
        "s_waitcnt vmcnt(0)\n"
        "v_cmp_gt_i32 vcc, 0, v27\n"
        "s_cbranch_vccz .Lr5_pg1_ok_%=\n"
        "s_add_u32 s64, s64, 1\n"
        "s_and_b32 s65, s64, 255\n"
        "s_cbranch_scc1 .Lr5_pg1_go_%=\n"
        "s_mov_b32 s65, 49152\n"
        "s_cmp_lt_u32 s64, 1048576\n"
        "s_cbranch_scc1 .Lr5_pg1_chk_%=\n"
        "v_mov_b32 v27, 1\n"
        "buffer_store_dword v27, off, s[40:43], s65 sc1\n"
        "s_waitcnt vmcnt(0)\n"
        ".Lr5_pg1_chk_%=:\n"
        "buffer_load_dword v27, off, s[40:43], s65 sc1\n"
        "s_waitcnt vmcnt(0)\n"
        "v_readfirstlane_b32 s65, v27\n"
        "s_nop 0\n"
        "s_cmp_lg_u32 s65, 1\n"
        "s_cbranch_scc1 .Lr5_pg1_go_%=\n"
        "s_endpgm\n"
        ".Lr5_pg1_go_%=:\n"
        "s_sleep 2\n"
        "s_branch .Lr5_pg1_top_%=\n"
        ".Lr5_pg1_ok_%=:\n"
        "v_cmp_eq_u32 s[54:55], 1, v27\n"
        "s_nop 1\n"
        "s_mov_b32 vcc_lo, 0x55555555\n"
        "s_mov_b32 vcc_hi, 0x55555555\n"
        "s_xor_b64 vcc, vcc, s[52:53]\n"
        "s_bcnt1_i32_b64 s65, vcc\n"
        "s_sub_u32 s64, s65, 23\n"
        "s_cmp_lt_u32 s64, 19\n"
        "s_cbranch_scc1 .Lr5_rawmap_%=\n"
        "s_cmp_lt_u32 s65, 32\n"
        "s_cselect_b32 s65, 0, -1\n"
        "s_xor_b32 s52, s65, 0x55555555\n"
        "s_xor_b32 s53, s65, 0x55555555\n"
        "s_xor_b32 s54, s65, 0x55555555\n"
        "s_xor_b32 s55, s65, 0xaaaaaaaa\n"
        ".Lr5_rawmap_%=:\n"
        "s_xor_b32 s52, s52, s66\n"
        "s_xor_b32 s53, s53, s66\n"
        "s_xor_b32 s54, s54, s66\n"
        "s_xor_b32 s55, s55, s66\n"
        "s_and_b32 s69, s55, 1\n"
        "s_lshl_b32 s69, s69, 13\n"
        "s_xor_b32 s70, s69, 8192\n"
        "s_cmp_eq_u32 s50, 0\n"
        "s_cbranch_scc0 .Lr5_l1_%=\n"
        ".Lr5_l0_%=:\n"
        "s_bitcmp1_b64 s[52:53], s68\n"
        "s_cselect_b32 s65, 8192, 0\n"
        "s_lshl_b32 s71, s68, 21\n"
        "s_add_u32 s71, s71, s65\n"
        "s_add_u32 s71, s71, 2097152\n"
        "s_lshr_b32 s65, s68, 2\n"
        "s_add_u32 s65, s65, 1\n"
        "s_bitcmp1_b32 s55, s65\n"
        "s_cselect_b32 s65, 8192, 0\n"
        "s_lshl_b32 s72, s68, 19\n"
        "s_add_u32 s72, s72, s65\n"
        "s_add_u32 s72, s72, 203423744\n"
        "s_add_u32 s76, s72, s61\n"
        "s_add_u32 s79, s71, s58\n"
        "s_add_u32 s79, s79, 1998848\n"
        "s_add_u32 s77, s72, s62\n"
        "s_add_u32 s80, s71, s59\n"
        "s_add_u32 s80, s80, 1998848\n"
        "s_add_u32 s78, s72, s63\n"
        "s_add_u32 s81, s71, s60\n"
        "s_add_u32 s81, s81, 1998848\n"
        "s_lshl_b32 s73, s68, 1\n"
        "s_add_u32 s73, s73, s74\n"
        "s_mov_b32 s64, 0\n"
        ".Lr5_l0w_top_%=:\n"
        "buffer_load_dword v24, v1, s[40:43], s69 offen sc1\n"
        "s_waitcnt vmcnt(0)\n"
        "v_cmp_gt_i32 vcc, s73, v24\n"
        "s_nop 0\n"
        "s_and_b64 vcc, vcc, 0xffff\n"
        "s_cbranch_vccz .Lr5_l0w_ok_%=\n"
        "s_add_u32 s64, s64, 1\n"
        "s_and_b32 s65, s64, 255\n"
        "s_cbranch_scc1 .Lr5_l0w_go_%=\n"
        "s_mov_b32 s65, 49152\n"
        "s_cmp_lt_u32 s64, 1048576\n"
        "s_cbranch_scc1 .Lr5_l0w_chk_%=\n"
        "v_mov_b32 v24, 1\n"
        "buffer_store_dword v24, off, s[40:43], s65 sc1\n"
        "s_waitcnt vmcnt(0)\n"
        ".Lr5_l0w_chk_%=:\n"
        "buffer_load_dword v24, off, s[40:43], s65 sc1\n"
        "s_waitcnt vmcnt(0)\n"
        "v_readfirstlane_b32 s65, v24\n"
        "s_nop 0\n"
        "s_cmp_lg_u32 s65, 1\n"
        "s_cbranch_scc1 .Lr5_l0w_go_%=\n"
        "s_endpgm\n"
        ".Lr5_l0w_go_%=:\n"
        "s_branch .Lr5_l0w_top_%=\n"
        ".Lr5_l0w_ok_%=:\n"
        "buffer_load_dwordx4 v[32:35], v3, s[40:43], s76 offen offset:0 sc1\n"
        "buffer_load_dwordx4 v[40:43], v3, s[40:43], s76 offen offset:512 sc1\n"
        "buffer_load_dwordx4 v[36:39], v3, s[40:43], s76 offen offset:256 sc1\n"
        "buffer_load_dwordx4 v[44:47], v3, s[40:43], s76 offen offset:768 sc1\n"
        "buffer_load_dwordx4 v[80:83], v0, s[40:43], s79 offen sc1\n"
        "buffer_load_dwordx4 v[48:51], v3, s[40:43], s77 offen offset:0 sc1\n"
        "buffer_load_dwordx4 v[56:59], v3, s[40:43], s77 offen offset:512 sc1\n"
        "buffer_load_dwordx4 v[52:55], v3, s[40:43], s77 offen offset:256 sc1\n"
        "buffer_load_dwordx4 v[60:63], v3, s[40:43], s77 offen offset:768 sc1\n"
        "buffer_load_dwordx4 v[84:87], v0, s[40:43], s80 offen sc1\n"
        "buffer_load_dwordx4 v[64:67], v4, s[40:43], s78 offen offset:0 sc1\n"
        "buffer_load_dwordx4 v[72:75], v4, s[40:43], s78 offen offset:512 sc1\n"
        "buffer_load_dwordx4 v[68:71], v4, s[40:43], s78 offen offset:256 sc1\n"
        "buffer_load_dwordx4 v[76:79], v4, s[40:43], s78 offen offset:768 sc1\n"
        "buffer_load_dwordx4 v[88:91], v0, s[40:43], s81 offen sc1\n"
        "s_xor_b32 s71, s71, 8192\n"
        "s_add_u32 s72, s71, 1835008\n"
        "s_add_u32 s71, s71, 1916928\n"
        "s_add_u32 s82, s72, s58\n"
        "s_add_u32 s85, s71, s58\n"
        "s_add_u32 s83, s72, s59\n"
        "s_add_u32 s86, s71, s59\n"
        "s_add_u32 s84, s72, s60\n"
        "s_add_u32 s87, s71, s60\n"
        "s_waitcnt vmcnt(14)\n"
        "v_mfma_f32_16x16x32_bf16 v[92:95], v[8:11], v[32:35], 0\n"
        "s_waitcnt vmcnt(13)\n"
        "v_mfma_f32_16x16x32_bf16 v[92:95], v[12:15], v[40:43], v[92:95]\n"
        "s_waitcnt vmcnt(12)\n"
        "v_mfma_f32_16x16x32_bf16 v[96:99], v[8:11], v[36:39], 0\n"
        "s_waitcnt vmcnt(11)\n"
        "v_mfma_f32_16x16x32_bf16 v[96:99], v[12:15], v[44:47], v[96:99]\n"
        "s_waitcnt vmcnt(10)\n"
        "v_mfma_f32_16x16x32_bf16 v[92:95], v[16:19], v[80:83], v[92:95]\n"
        "v_mfma_f32_16x16x32_bf16 v[96:99], v[20:23], v[80:83], v[96:99]\n"
        "s_waitcnt vmcnt(9)\n"
        "v_mfma_f32_16x16x32_bf16 v[100:103], v[8:11], v[48:51], 0\n"
        "s_waitcnt vmcnt(8)\n"
        "v_mfma_f32_16x16x32_bf16 v[100:103], v[12:15], v[56:59], v[100:103]\n"
        "s_waitcnt vmcnt(7)\n"
        "v_mfma_f32_16x16x32_bf16 v[104:107], v[8:11], v[52:55], 0\n"
        "s_waitcnt vmcnt(6)\n"
        "v_mfma_f32_16x16x32_bf16 v[104:107], v[12:15], v[60:63], v[104:107]\n"
        "s_waitcnt vmcnt(5)\n"
        "v_mfma_f32_16x16x32_bf16 v[100:103], v[16:19], v[84:87], v[100:103]\n"
        "v_mfma_f32_16x16x32_bf16 v[104:107], v[20:23], v[84:87], v[104:107]\n"
        "s_waitcnt vmcnt(4)\n"
        "v_mfma_f32_16x16x32_bf16 v[108:111], v[8:11], v[64:67], 0\n"
        "s_waitcnt vmcnt(3)\n"
        "v_mfma_f32_16x16x32_bf16 v[108:111], v[12:15], v[72:75], v[108:111]\n"
        "s_waitcnt vmcnt(2)\n"
        "v_mfma_f32_16x16x32_bf16 v[112:115], v[8:11], v[68:71], 0\n"
        "s_waitcnt vmcnt(1)\n"
        "v_mfma_f32_16x16x32_bf16 v[112:115], v[12:15], v[76:79], v[112:115]\n"
        "s_waitcnt vmcnt(0)\n"
        "v_mfma_f32_16x16x32_bf16 v[108:111], v[16:19], v[88:91], v[108:111]\n"
        "v_mfma_f32_16x16x32_bf16 v[112:115], v[20:23], v[88:91], v[112:115]\n"
        "s_add_u32 s68, s68, 1\n"
        "s_sub_u32 s68, s68, 1\n"
        "v_pk_add_f32 v[164:165], v[92:93], v[94:95]\n"
        "v_pk_add_f32 v[166:167], v[96:97], v[98:99]\n"
        "v_pk_add_f32 v[168:169], v[100:101], v[102:103]\n"
        "v_pk_add_f32 v[170:171], v[104:105], v[106:107]\n"
        "v_pk_add_f32 v[164:165], v[164:165], v[166:167]\n"
        "v_pk_add_f32 v[168:169], v[168:169], v[170:171]\n"
        "v_pk_add_f32 v[164:165], v[164:165], v[168:169]\n"
        "s_nop 0\n"
        "v_pk_add_f32 v[166:167], v[108:109], v[110:111]\n"
        "v_pk_add_f32 v[168:169], v[112:113], v[114:115]\n"
        "v_pk_add_f32 v[164:165], v[164:165], v[166:167]\n"
        "v_pk_add_f32 v[164:165], v[164:165], v[168:169]\n"
        "v_add_f32 v164, v164, v165\n"
        "v_cvt_pk_bf16_f32 v140, v92, v93\n"
        "v_cvt_pk_bf16_f32 v141, v94, v95\n"
        "v_add_f32_dpp v166, v164, v164 row_ror:8 row_mask:0xf bank_mask:0xf\n"
        "v_add_f32_dpp v167, v164, v164 row_ror:8 row_mask:0xf bank_mask:0xf\n"
        "v_cvt_pk_bf16_f32 v142, v96, v97\n"
        "v_cvt_pk_bf16_f32 v143, v98, v99\n"
        "v_permlane16_swap_b32 v166, v167\n"
        "v_add_f32 v164, v166, v167\n"
        "v_add_f32 v166, v166, v167\n"
        "v_cvt_pk_bf16_f32 v144, v100, v101\n"
        "v_cvt_pk_bf16_f32 v145, v102, v103\n"
        "v_permlane32_swap_b32 v164, v166\n"
        "v_add_f32 v164, v164, v166\n"
        "v_mul_f32 v164, 0x3baaaaab, v164\n"
        "v_pk_add_f32 v[116:117], v[92:93], v[164:165] op_sel_hi:[1,0] neg_lo:[0,1] neg_hi:[0,1]\n"
        "v_pk_add_f32 v[118:119], v[94:95], v[164:165] op_sel_hi:[1,0] neg_lo:[0,1] neg_hi:[0,1]\n"
        "v_pk_add_f32 v[120:121], v[96:97], v[164:165] op_sel_hi:[1,0] neg_lo:[0,1] neg_hi:[0,1]\n"
        "v_pk_add_f32 v[122:123], v[98:99], v[164:165] op_sel_hi:[1,0] neg_lo:[0,1] neg_hi:[0,1]\n"
        "v_pk_add_f32 v[124:125], v[100:101], v[164:165] op_sel_hi:[1,0] neg_lo:[0,1] neg_hi:[0,1]\n"
        "v_pk_add_f32 v[126:127], v[102:103], v[164:165] op_sel_hi:[1,0] neg_lo:[0,1] neg_hi:[0,1]\n"
        "v_pk_add_f32 v[128:129], v[104:105], v[164:165] op_sel_hi:[1,0] neg_lo:[0,1] neg_hi:[0,1]\n"
        "v_pk_add_f32 v[130:131], v[106:107], v[164:165] op_sel_hi:[1,0] neg_lo:[0,1] neg_hi:[0,1]\n"
        "v_pk_add_f32 v[132:133], v[108:109], v[164:165] op_sel_hi:[1,0] neg_lo:[0,1] neg_hi:[0,1]\n"
        "v_pk_add_f32 v[134:135], v[110:111], v[164:165] op_sel_hi:[1,0] neg_lo:[0,1] neg_hi:[0,1]\n"
        "v_pk_add_f32 v[136:137], v[112:113], v[164:165] op_sel_hi:[1,0] neg_lo:[0,1] neg_hi:[0,1]\n"
        "v_pk_add_f32 v[138:139], v[114:115], v[164:165] op_sel_hi:[1,0] neg_lo:[0,1] neg_hi:[0,1]\n"
        "v_pk_mul_f32 v[166:167], v[116:117], v[116:117]\n"
        "v_pk_mul_f32 v[168:169], v[118:119], v[118:119]\n"
        "v_pk_fma_f32 v[166:167], v[120:121], v[120:121], v[166:167]\n"
        "v_pk_fma_f32 v[168:169], v[122:123], v[122:123], v[168:169]\n"
        "v_pk_fma_f32 v[166:167], v[124:125], v[124:125], v[166:167]\n"
        "v_pk_fma_f32 v[168:169], v[126:127], v[126:127], v[168:169]\n"
        "v_pk_fma_f32 v[166:167], v[128:129], v[128:129], v[166:167]\n"
        "v_pk_fma_f32 v[168:169], v[130:131], v[130:131], v[168:169]\n"
        "v_pk_fma_f32 v[166:167], v[132:133], v[132:133], v[166:167]\n"
        "v_pk_fma_f32 v[168:169], v[134:135], v[134:135], v[168:169]\n"
        "v_pk_fma_f32 v[166:167], v[136:137], v[136:137], v[166:167]\n"
        "v_pk_fma_f32 v[168:169], v[138:139], v[138:139], v[168:169]\n"
        "v_pk_add_f32 v[166:167], v[166:167], v[168:169]\n"
        "v_add_f32 v165, v166, v167\n"
        "v_cvt_pk_bf16_f32 v146, v104, v105\n"
        "v_cvt_pk_bf16_f32 v147, v106, v107\n"
        "v_add_f32_dpp v166, v165, v165 row_ror:8 row_mask:0xf bank_mask:0xf\n"
        "v_add_f32_dpp v167, v165, v165 row_ror:8 row_mask:0xf bank_mask:0xf\n"
        "v_cvt_pk_bf16_f32 v148, v108, v109\n"
        "v_cvt_pk_bf16_f32 v149, v110, v111\n"
        "v_permlane16_swap_b32 v166, v167\n"
        "v_add_f32 v165, v166, v167\n"
        "v_add_f32 v166, v166, v167\n"
        "v_cvt_pk_bf16_f32 v150, v112, v113\n"
        "v_cvt_pk_bf16_f32 v151, v114, v115\n"
        "v_permlane32_swap_b32 v165, v166\n"
        "v_add_f32 v165, v165, v166\n"
        "ds_write_b64 v6, v[164:165]\n"
        "v_permlane16_swap_b32 v140, v142\n"
        "v_permlane16_swap_b32 v141, v143\n"
        "v_permlane16_swap_b32 v144, v146\n"
        "v_permlane16_swap_b32 v145, v147\n"
        "s_waitcnt lgkmcnt(0)\n"
        "s_barrier\n"
        "ds_read_b64 v[166:167], v7 offset:0\n"
        "ds_read_b64 v[168:169], v7 offset:64\n"
        "ds_read_b64 v[170:171], v7 offset:128\n"
        "v_permlane16_swap_b32 v148, v150\n"
        "v_permlane16_swap_b32 v149, v151\n"
        "buffer_store_dwordx4 v[140:143], v0, s[40:43], s85 offen sc1\n"
        "buffer_store_dwordx4 v[144:147], v0, s[40:43], s86 offen sc1\n"
        "buffer_store_dwordx4 v[148:151], v0, s[40:43], s87 offen sc1\n"
        "s_waitcnt lgkmcnt(0)\n"
        "v_add_f32 v172, v166, v168\n"
        "v_add_f32 v173, v167, v169\n"
        "v_add_f32 v172, v172, v170\n"
        "v_add_f32 v173, v173, v171\n"
        "v_mul_f32 v172, 0x3eaaaaab, v172\n"
        "v_sub_f32 v166, v166, v172\n"
        "v_sub_f32 v168, v168, v172\n"
        "v_sub_f32 v170, v170, v172\n"
        "v_mul_f32 v166, v166, v166\n"
        "v_fmac_f32 v166, v168, v168\n"
        "v_fmac_f32 v166, v170, v170\n"
        "v_fmamk_f32 v173, v166, 0x43400000, v173\n"
        "v_mul_f32 v173, 0x3ae38e39, v173\n"
        "v_cvt_pk_bf16_f32 v172, v172, v173\n"
        "v_and_b32 v173, 0xffff0000, v172\n"
        "v_lshlrev_b32 v172, 16, v172\n"
        "v_add_f32 v173, 0x3727c5ac, v173\n"
        "v_cvt_pk_bf16_f32 v173, v173, v173\n"
        "v_lshlrev_b32 v173, 16, v173\n"
        "v_sqrt_f32 v173, v173\n"
        "v_sub_f32 v172, v164, v172\n"
        "v_cvt_pk_bf16_f32 v173, v173, v173\n"
        "v_lshlrev_b32 v173, 16, v173\n"
        "v_rcp_f32 v173, v173\n"
        "v_mov_b32 v25, s68\n"
        "v_mul_f32 v172, v172, v173\n"
        "v_pk_fma_f32 v[116:117], v[116:117], v[172:173], v[172:173] op_sel:[0,1,0] op_sel_hi:[1,1,0]\n"
        "v_pk_fma_f32 v[118:119], v[118:119], v[172:173], v[172:173] op_sel:[0,1,0] op_sel_hi:[1,1,0]\n"
        "v_pk_fma_f32 v[120:121], v[120:121], v[172:173], v[172:173] op_sel:[0,1,0] op_sel_hi:[1,1,0]\n"
        "v_pk_fma_f32 v[122:123], v[122:123], v[172:173], v[172:173] op_sel:[0,1,0] op_sel_hi:[1,1,0]\n"
        "v_cvt_pk_bf16_f32 v152, v116, v117\n"
        "v_cvt_pk_bf16_f32 v153, v118, v119\n"
        "v_cvt_pk_bf16_f32 v154, v120, v121\n"
        "v_cvt_pk_bf16_f32 v155, v122, v123\n"
        "s_nop 0\n"
        "v_permlane16_swap_b32 v152, v154\n"
        "v_permlane16_swap_b32 v153, v155\n"
        "buffer_store_dwordx4 v[152:155], v0, s[40:43], s82 offen sc1\n"
        "v_pk_fma_f32 v[124:125], v[124:125], v[172:173], v[172:173] op_sel:[0,1,0] op_sel_hi:[1,1,0]\n"
        "v_pk_fma_f32 v[126:127], v[126:127], v[172:173], v[172:173] op_sel:[0,1,0] op_sel_hi:[1,1,0]\n"
        "v_pk_fma_f32 v[128:129], v[128:129], v[172:173], v[172:173] op_sel:[0,1,0] op_sel_hi:[1,1,0]\n"
        "v_pk_fma_f32 v[130:131], v[130:131], v[172:173], v[172:173] op_sel:[0,1,0] op_sel_hi:[1,1,0]\n"
        "v_cvt_pk_bf16_f32 v156, v124, v125\n"
        "v_cvt_pk_bf16_f32 v157, v126, v127\n"
        "v_cvt_pk_bf16_f32 v158, v128, v129\n"
        "v_cvt_pk_bf16_f32 v159, v130, v131\n"
        "s_nop 0\n"
        "v_permlane16_swap_b32 v156, v158\n"
        "v_permlane16_swap_b32 v157, v159\n"
        "buffer_store_dwordx4 v[156:159], v0, s[40:43], s83 offen sc1\n"
        "v_pk_fma_f32 v[132:133], v[132:133], v[172:173], v[172:173] op_sel:[0,1,0] op_sel_hi:[1,1,0]\n"
        "v_pk_fma_f32 v[134:135], v[134:135], v[172:173], v[172:173] op_sel:[0,1,0] op_sel_hi:[1,1,0]\n"
        "v_pk_fma_f32 v[136:137], v[136:137], v[172:173], v[172:173] op_sel:[0,1,0] op_sel_hi:[1,1,0]\n"
        "v_pk_fma_f32 v[138:139], v[138:139], v[172:173], v[172:173] op_sel:[0,1,0] op_sel_hi:[1,1,0]\n"
        "v_cvt_pk_bf16_f32 v160, v132, v133\n"
        "v_cvt_pk_bf16_f32 v161, v134, v135\n"
        "v_cvt_pk_bf16_f32 v162, v136, v137\n"
        "v_cvt_pk_bf16_f32 v163, v138, v139\n"
        "s_nop 0\n"
        "v_permlane16_swap_b32 v160, v162\n"
        "v_permlane16_swap_b32 v161, v163\n"
        "buffer_store_dwordx4 v[160:163], v0, s[40:43], s84 offen sc1\n"
        "s_waitcnt vmcnt(0)\n"
        "s_barrier\n"
        "buffer_store_dword v25, v2, s[40:43], s70 offen sc1\n"
        "s_add_u32 s68, s68, 1\n"
        "s_cmp_lt_u32 s68, 64\n"
        "s_cbranch_scc1 .Lr5_l0_%=\n"
        "s_endpgm\n"
        ".Lr5_l1_%=:\n"
        "s_bitcmp1_b64 s[52:53], s68\n"
        "s_cselect_b32 s65, 8192, 0\n"
        "s_lshl_b32 s71, s68, 21\n"
        "s_add_u32 s71, s71, s65\n"
        "s_add_u32 s71, s71, 2097152\n"
        "s_xor_b32 s71, s71, 8192\n"
        "s_add_u32 s72, s71, 1835008\n"
        "s_add_u32 s71, s71, 1916928\n"
        "s_add_u32 s82, s72, s58\n"
        "s_add_u32 s85, s71, s58\n"
        "s_add_u32 s83, s72, s59\n"
        "s_add_u32 s86, s71, s59\n"
        "s_add_u32 s84, s72, s60\n"
        "s_add_u32 s87, s71, s60\n"
        "s_waitcnt vmcnt(0)\n"
        "v_mfma_f32_16x16x32_bf16 v[92:95], v[16:19], v[80:83], 0\n"
        "v_mfma_f32_16x16x32_bf16 v[96:99], v[20:23], v[80:83], 0\n"
        "v_mfma_f32_16x16x32_bf16 v[100:103], v[16:19], v[84:87], 0\n"
        "v_mfma_f32_16x16x32_bf16 v[104:107], v[20:23], v[84:87], 0\n"
        "v_mfma_f32_16x16x32_bf16 v[108:111], v[16:19], v[88:91], 0\n"
        "v_mfma_f32_16x16x32_bf16 v[112:115], v[20:23], v[88:91], 0\n"
        "s_add_u32 s68, s68, 1\n"
        "s_mul_i32 s65, s68, 36864\n"
        "buffer_load_dwordx4 v[80:83], v5, s[44:47], s65 offen offset:0\n"
        "buffer_load_dwordx4 v[84:87], v5, s[44:47], s65 offen offset:128\n"
        "buffer_load_dwordx4 v[88:91], v5, s[44:47], s65 offen offset:256\n"
        "s_sub_u32 s68, s68, 1\n"
        "v_pk_add_f32 v[164:165], v[92:93], v[94:95]\n"
        "v_pk_add_f32 v[166:167], v[96:97], v[98:99]\n"
        "v_pk_add_f32 v[168:169], v[100:101], v[102:103]\n"
        "v_pk_add_f32 v[170:171], v[104:105], v[106:107]\n"
        "v_pk_add_f32 v[164:165], v[164:165], v[166:167]\n"
        "v_pk_add_f32 v[168:169], v[168:169], v[170:171]\n"
        "v_pk_add_f32 v[164:165], v[164:165], v[168:169]\n"
        "v_pk_add_f32 v[166:167], v[108:109], v[110:111]\n"
        "v_pk_add_f32 v[168:169], v[112:113], v[114:115]\n"
        "v_pk_add_f32 v[164:165], v[164:165], v[166:167]\n"
        "v_pk_add_f32 v[164:165], v[164:165], v[168:169]\n"
        "v_add_f32 v164, v164, v165\n"
        "s_nop 1\n"
        "v_add_f32_dpp v166, v164, v164 row_ror:8 row_mask:0xf bank_mask:0xf\n"
        "v_add_f32_dpp v167, v164, v164 row_ror:8 row_mask:0xf bank_mask:0xf\n"
        "s_nop 1\n"
        "v_permlane16_swap_b32 v166, v167\n"
        "v_add_f32 v164, v166, v167\n"
        "v_add_f32 v166, v166, v167\n"
        "s_nop 1\n"
        "v_permlane32_swap_b32 v164, v166\n"
        "v_add_f32 v164, v164, v166\n"
        "v_mul_f32 v164, 0x3baaaaab, v164\n"
        "v_pk_add_f32 v[116:117], v[92:93], v[164:165] op_sel_hi:[1,0] neg_lo:[0,1] neg_hi:[0,1]\n"
        "v_pk_add_f32 v[118:119], v[94:95], v[164:165] op_sel_hi:[1,0] neg_lo:[0,1] neg_hi:[0,1]\n"
        "v_pk_add_f32 v[120:121], v[96:97], v[164:165] op_sel_hi:[1,0] neg_lo:[0,1] neg_hi:[0,1]\n"
        "v_pk_add_f32 v[122:123], v[98:99], v[164:165] op_sel_hi:[1,0] neg_lo:[0,1] neg_hi:[0,1]\n"
        "v_pk_add_f32 v[124:125], v[100:101], v[164:165] op_sel_hi:[1,0] neg_lo:[0,1] neg_hi:[0,1]\n"
        "v_pk_add_f32 v[126:127], v[102:103], v[164:165] op_sel_hi:[1,0] neg_lo:[0,1] neg_hi:[0,1]\n"
        "v_pk_add_f32 v[128:129], v[104:105], v[164:165] op_sel_hi:[1,0] neg_lo:[0,1] neg_hi:[0,1]\n"
        "v_pk_add_f32 v[130:131], v[106:107], v[164:165] op_sel_hi:[1,0] neg_lo:[0,1] neg_hi:[0,1]\n"
        "v_pk_add_f32 v[132:133], v[108:109], v[164:165] op_sel_hi:[1,0] neg_lo:[0,1] neg_hi:[0,1]\n"
        "v_pk_add_f32 v[134:135], v[110:111], v[164:165] op_sel_hi:[1,0] neg_lo:[0,1] neg_hi:[0,1]\n"
        "v_pk_add_f32 v[136:137], v[112:113], v[164:165] op_sel_hi:[1,0] neg_lo:[0,1] neg_hi:[0,1]\n"
        "v_pk_add_f32 v[138:139], v[114:115], v[164:165] op_sel_hi:[1,0] neg_lo:[0,1] neg_hi:[0,1]\n"
        "v_pk_mul_f32 v[166:167], v[116:117], v[116:117]\n"
        "v_pk_mul_f32 v[168:169], v[118:119], v[118:119]\n"
        "v_pk_fma_f32 v[166:167], v[120:121], v[120:121], v[166:167]\n"
        "v_pk_fma_f32 v[168:169], v[122:123], v[122:123], v[168:169]\n"
        "v_pk_fma_f32 v[166:167], v[124:125], v[124:125], v[166:167]\n"
        "v_pk_fma_f32 v[168:169], v[126:127], v[126:127], v[168:169]\n"
        "v_pk_fma_f32 v[166:167], v[128:129], v[128:129], v[166:167]\n"
        "v_pk_fma_f32 v[168:169], v[130:131], v[130:131], v[168:169]\n"
        "v_pk_fma_f32 v[166:167], v[132:133], v[132:133], v[166:167]\n"
        "v_pk_fma_f32 v[168:169], v[134:135], v[134:135], v[168:169]\n"
        "v_pk_fma_f32 v[166:167], v[136:137], v[136:137], v[166:167]\n"
        "v_pk_fma_f32 v[168:169], v[138:139], v[138:139], v[168:169]\n"
        "v_pk_add_f32 v[166:167], v[166:167], v[168:169]\n"
        "v_add_f32 v165, v166, v167\n"
        "s_nop 1\n"
        "v_add_f32_dpp v166, v165, v165 row_ror:8 row_mask:0xf bank_mask:0xf\n"
        "v_add_f32_dpp v167, v165, v165 row_ror:8 row_mask:0xf bank_mask:0xf\n"
        "s_nop 1\n"
        "v_permlane16_swap_b32 v166, v167\n"
        "v_add_f32 v165, v166, v167\n"
        "v_add_f32 v166, v166, v167\n"
        "s_nop 1\n"
        "v_permlane32_swap_b32 v165, v166\n"
        "v_add_f32 v165, v165, v166\n"
        "ds_write_b64 v6, v[164:165]\n"
        "s_waitcnt lgkmcnt(0)\n"
        "s_barrier\n"
        "ds_read_b64 v[166:167], v7 offset:0\n"
        "ds_read_b64 v[168:169], v7 offset:64\n"
        "ds_read_b64 v[170:171], v7 offset:128\n"
        "s_waitcnt lgkmcnt(0)\n"
        "v_add_f32 v172, v166, v168\n"
        "v_add_f32 v173, v167, v169\n"
        "v_add_f32 v172, v172, v170\n"
        "v_add_f32 v173, v173, v171\n"
        "v_mul_f32 v172, 0x3eaaaaab, v172\n"
        "v_sub_f32 v166, v166, v172\n"
        "v_sub_f32 v168, v168, v172\n"
        "v_sub_f32 v170, v170, v172\n"
        "v_mul_f32 v166, v166, v166\n"
        "v_fmac_f32 v166, v168, v168\n"
        "v_fmac_f32 v166, v170, v170\n"
        "v_fmamk_f32 v173, v166, 0x43400000, v173\n"
        "v_mul_f32 v173, 0x3ae38e39, v173\n"
        "v_cvt_pk_bf16_f32 v172, v172, v173\n"
        "v_and_b32 v173, 0xffff0000, v172\n"
        "v_lshlrev_b32 v172, 16, v172\n"
        "v_add_f32 v173, 0x3727c5ac, v173\n"
        "v_cvt_pk_bf16_f32 v173, v173, v173\n"
        "v_lshlrev_b32 v173, 16, v173\n"
        "v_sqrt_f32 v173, v173\n"
        "v_sub_f32 v172, v164, v172\n"
        "v_cvt_pk_bf16_f32 v173, v173, v173\n"
        "v_lshlrev_b32 v173, 16, v173\n"
        "v_rcp_f32 v173, v173\n"
        "v_mov_b32 v25, s68\n"
        "v_mul_f32 v172, v172, v173\n"
        "v_pk_fma_f32 v[116:117], v[116:117], v[172:173], v[172:173] op_sel:[0,1,0] op_sel_hi:[1,1,0]\n"
        "v_pk_fma_f32 v[118:119], v[118:119], v[172:173], v[172:173] op_sel:[0,1,0] op_sel_hi:[1,1,0]\n"
        "v_pk_fma_f32 v[120:121], v[120:121], v[172:173], v[172:173] op_sel:[0,1,0] op_sel_hi:[1,1,0]\n"
        "v_pk_fma_f32 v[122:123], v[122:123], v[172:173], v[172:173] op_sel:[0,1,0] op_sel_hi:[1,1,0]\n"
        "v_cvt_pk_bf16_f32 v152, v116, v117\n"
        "v_cvt_pk_bf16_f32 v153, v118, v119\n"
        "v_cvt_pk_bf16_f32 v154, v120, v121\n"
        "v_cvt_pk_bf16_f32 v155, v122, v123\n"
        "s_nop 0\n"
        "v_permlane16_swap_b32 v152, v154\n"
        "v_permlane16_swap_b32 v153, v155\n"
        "buffer_store_dwordx4 v[152:155], v0, s[40:43], s82 offen sc1\n"
        "v_pk_fma_f32 v[124:125], v[124:125], v[172:173], v[172:173] op_sel:[0,1,0] op_sel_hi:[1,1,0]\n"
        "v_pk_fma_f32 v[126:127], v[126:127], v[172:173], v[172:173] op_sel:[0,1,0] op_sel_hi:[1,1,0]\n"
        "v_pk_fma_f32 v[128:129], v[128:129], v[172:173], v[172:173] op_sel:[0,1,0] op_sel_hi:[1,1,0]\n"
        "v_pk_fma_f32 v[130:131], v[130:131], v[172:173], v[172:173] op_sel:[0,1,0] op_sel_hi:[1,1,0]\n"
        "v_cvt_pk_bf16_f32 v156, v124, v125\n"
        "v_cvt_pk_bf16_f32 v157, v126, v127\n"
        "v_cvt_pk_bf16_f32 v158, v128, v129\n"
        "v_cvt_pk_bf16_f32 v159, v130, v131\n"
        "s_nop 0\n"
        "v_permlane16_swap_b32 v156, v158\n"
        "v_permlane16_swap_b32 v157, v159\n"
        "buffer_store_dwordx4 v[156:159], v0, s[40:43], s83 offen sc1\n"
        "v_pk_fma_f32 v[132:133], v[132:133], v[172:173], v[172:173] op_sel:[0,1,0] op_sel_hi:[1,1,0]\n"
        "v_pk_fma_f32 v[134:135], v[134:135], v[172:173], v[172:173] op_sel:[0,1,0] op_sel_hi:[1,1,0]\n"
        "v_pk_fma_f32 v[136:137], v[136:137], v[172:173], v[172:173] op_sel:[0,1,0] op_sel_hi:[1,1,0]\n"
        "v_pk_fma_f32 v[138:139], v[138:139], v[172:173], v[172:173] op_sel:[0,1,0] op_sel_hi:[1,1,0]\n"
        "v_cvt_pk_bf16_f32 v160, v132, v133\n"
        "v_cvt_pk_bf16_f32 v161, v134, v135\n"
        "v_cvt_pk_bf16_f32 v162, v136, v137\n"
        "v_cvt_pk_bf16_f32 v163, v138, v139\n"
        "s_nop 0\n"
        "v_permlane16_swap_b32 v160, v162\n"
        "v_permlane16_swap_b32 v161, v163\n"
        "buffer_store_dwordx4 v[160:163], v0, s[40:43], s84 offen sc1\n"
        "s_waitcnt vmcnt(0)\n"
        "s_barrier\n"
        "buffer_store_dword v25, v2, s[40:43], s70 offen sc1\n"
        "s_add_u32 s68, s68, 1\n"
        "s_cmp_lt_u32 s68, 64\n"
        "s_cbranch_scc1 .Lr5_l1_%=\n"
        "s_mov_b32 s68, 0\n"
        "s_mov_b64 s[44:45], %2\n"
        "s_and_b32 s45, s45, 0xffff\n"
        ".Lr5_fin_%=:\n"
        "s_bitcmp1_b64 s[52:53], s68\n"
        "s_cselect_b32 s65, 8192, 0\n"
        "s_lshl_b32 s71, s68, 21\n"
        "s_add_u32 s71, s71, s65\n"
        "s_add_u32 s71, s71, 2097152\n"
        "s_lshr_b32 s65, s68, 2\n"
        "s_add_u32 s65, s65, 1\n"
        "s_bitcmp1_b32 s55, s65\n"
        "s_cselect_b32 s65, 8192, 0\n"
        "s_lshl_b32 s72, s68, 19\n"
        "s_add_u32 s72, s72, s65\n"
        "s_add_u32 s72, s72, 203423744\n"
        "s_add_u32 s76, s72, s61\n"
        "s_add_u32 s79, s71, s58\n"
        "s_add_u32 s79, s79, 1998848\n"
        "s_add_u32 s77, s72, s62\n"
        "s_add_u32 s80, s71, s59\n"
        "s_add_u32 s80, s80, 1998848\n"
        "s_add_u32 s78, s72, s63\n"
        "s_add_u32 s81, s71, s60\n"
        "s_add_u32 s81, s81, 1998848\n"
        "s_lshl_b32 s73, s68, 1\n"
        "s_add_u32 s73, s73, s74\n"
        "s_mov_b32 s64, 0\n"
        ".Lr5_finw_top_%=:\n"
        "buffer_load_dword v24, v1, s[40:43], s69 offen sc1\n"
        "s_waitcnt vmcnt(0)\n"
        "v_cmp_gt_i32 vcc, s73, v24\n"
        "s_nop 0\n"
        "s_and_b64 vcc, vcc, 0xffff\n"
        "s_cbranch_vccz .Lr5_finw_ok_%=\n"
        "s_add_u32 s64, s64, 1\n"
        "s_and_b32 s65, s64, 255\n"
        "s_cbranch_scc1 .Lr5_finw_go_%=\n"
        "s_mov_b32 s65, 49152\n"
        "s_cmp_lt_u32 s64, 1048576\n"
        "s_cbranch_scc1 .Lr5_finw_chk_%=\n"
        "v_mov_b32 v24, 1\n"
        "buffer_store_dword v24, off, s[40:43], s65 sc1\n"
        "s_waitcnt vmcnt(0)\n"
        ".Lr5_finw_chk_%=:\n"
        "buffer_load_dword v24, off, s[40:43], s65 sc1\n"
        "s_waitcnt vmcnt(0)\n"
        "v_readfirstlane_b32 s65, v24\n"
        "s_nop 0\n"
        "s_cmp_lg_u32 s65, 1\n"
        "s_cbranch_scc1 .Lr5_finw_go_%=\n"
        "s_endpgm\n"
        ".Lr5_finw_go_%=:\n"
        "s_branch .Lr5_finw_top_%=\n"
        ".Lr5_finw_ok_%=:\n"
        "buffer_load_dwordx4 v[32:35], v3, s[40:43], s76 offen offset:0 sc1\n"
        "buffer_load_dwordx4 v[40:43], v3, s[40:43], s76 offen offset:512 sc1\n"
        "buffer_load_dwordx4 v[36:39], v3, s[40:43], s76 offen offset:256 sc1\n"
        "buffer_load_dwordx4 v[44:47], v3, s[40:43], s76 offen offset:768 sc1\n"
        "buffer_load_dwordx4 v[80:83], v0, s[40:43], s79 offen sc1\n"
        "buffer_load_dwordx4 v[48:51], v3, s[40:43], s77 offen offset:0 sc1\n"
        "buffer_load_dwordx4 v[56:59], v3, s[40:43], s77 offen offset:512 sc1\n"
        "buffer_load_dwordx4 v[52:55], v3, s[40:43], s77 offen offset:256 sc1\n"
        "buffer_load_dwordx4 v[60:63], v3, s[40:43], s77 offen offset:768 sc1\n"
        "buffer_load_dwordx4 v[84:87], v0, s[40:43], s80 offen sc1\n"
        "buffer_load_dwordx4 v[64:67], v4, s[40:43], s78 offen offset:0 sc1\n"
        "buffer_load_dwordx4 v[72:75], v4, s[40:43], s78 offen offset:512 sc1\n"
        "buffer_load_dwordx4 v[68:71], v4, s[40:43], s78 offen offset:256 sc1\n"
        "buffer_load_dwordx4 v[76:79], v4, s[40:43], s78 offen offset:768 sc1\n"
        "buffer_load_dwordx4 v[88:91], v0, s[40:43], s81 offen sc1\n"
        "s_waitcnt vmcnt(14)\n"
        "v_mfma_f32_16x16x32_bf16 v[92:95], v[8:11], v[32:35], 0\n"
        "s_waitcnt vmcnt(13)\n"
        "v_mfma_f32_16x16x32_bf16 v[92:95], v[12:15], v[40:43], v[92:95]\n"
        "s_waitcnt vmcnt(12)\n"
        "v_mfma_f32_16x16x32_bf16 v[96:99], v[8:11], v[36:39], 0\n"
        "s_waitcnt vmcnt(11)\n"
        "v_mfma_f32_16x16x32_bf16 v[96:99], v[12:15], v[44:47], v[96:99]\n"
        "s_waitcnt vmcnt(10)\n"
        "v_mfma_f32_16x16x32_bf16 v[92:95], v[16:19], v[80:83], v[92:95]\n"
        "v_mfma_f32_16x16x32_bf16 v[96:99], v[20:23], v[80:83], v[96:99]\n"
        "s_waitcnt vmcnt(9)\n"
        "v_mfma_f32_16x16x32_bf16 v[100:103], v[8:11], v[48:51], 0\n"
        "s_waitcnt vmcnt(8)\n"
        "v_mfma_f32_16x16x32_bf16 v[100:103], v[12:15], v[56:59], v[100:103]\n"
        "s_waitcnt vmcnt(7)\n"
        "v_mfma_f32_16x16x32_bf16 v[104:107], v[8:11], v[52:55], 0\n"
        "s_waitcnt vmcnt(6)\n"
        "v_mfma_f32_16x16x32_bf16 v[104:107], v[12:15], v[60:63], v[104:107]\n"
        "s_waitcnt vmcnt(5)\n"
        "v_mfma_f32_16x16x32_bf16 v[100:103], v[16:19], v[84:87], v[100:103]\n"
        "v_mfma_f32_16x16x32_bf16 v[104:107], v[20:23], v[84:87], v[104:107]\n"
        "s_waitcnt vmcnt(4)\n"
        "v_mfma_f32_16x16x32_bf16 v[108:111], v[8:11], v[64:67], 0\n"
        "s_waitcnt vmcnt(3)\n"
        "v_mfma_f32_16x16x32_bf16 v[108:111], v[12:15], v[72:75], v[108:111]\n"
        "s_waitcnt vmcnt(2)\n"
        "v_mfma_f32_16x16x32_bf16 v[112:115], v[8:11], v[68:71], 0\n"
        "s_waitcnt vmcnt(1)\n"
        "v_mfma_f32_16x16x32_bf16 v[112:115], v[12:15], v[76:79], v[112:115]\n"
        "s_waitcnt vmcnt(0)\n"
        "v_mfma_f32_16x16x32_bf16 v[108:111], v[16:19], v[88:91], v[108:111]\n"
        "v_mfma_f32_16x16x32_bf16 v[112:115], v[20:23], v[88:91], v[112:115]\n"
        "s_mul_i32 s65, s68, 36864\n"
        "v_cvt_pk_bf16_f32 v140, v92, v93\n"
        "v_cvt_pk_bf16_f32 v141, v94, v95\n"
        "v_cvt_pk_bf16_f32 v142, v96, v97\n"
        "v_cvt_pk_bf16_f32 v143, v98, v99\n"
        "s_nop 0\n"
        "v_permlane16_swap_b32 v140, v142\n"
        "v_permlane16_swap_b32 v141, v143\n"
        "buffer_store_dwordx4 v[140:143], v5, s[44:47], s65 offen offset:0\n"
        "v_cvt_pk_bf16_f32 v144, v100, v101\n"
        "v_cvt_pk_bf16_f32 v145, v102, v103\n"
        "v_cvt_pk_bf16_f32 v146, v104, v105\n"
        "v_cvt_pk_bf16_f32 v147, v106, v107\n"
        "s_nop 0\n"
        "v_permlane16_swap_b32 v144, v146\n"
        "v_permlane16_swap_b32 v145, v147\n"
        "buffer_store_dwordx4 v[144:147], v5, s[44:47], s65 offen offset:128\n"
        "s_nop 0\n"
        "v_cvt_pk_bf16_f32 v148, v108, v109\n"
        "v_cvt_pk_bf16_f32 v149, v110, v111\n"
        "v_cvt_pk_bf16_f32 v150, v112, v113\n"
        "v_cvt_pk_bf16_f32 v151, v114, v115\n"
        "s_nop 0\n"
        "v_permlane16_swap_b32 v148, v150\n"
        "v_permlane16_swap_b32 v149, v151\n"
        "buffer_store_dwordx4 v[148:151], v5, s[44:47], s65 offen offset:256\n"
        "s_add_u32 s68, s68, 1\n"
        "s_cmp_lt_u32 s68, 64\n"
        "s_cbranch_scc1 .Lr5_fin_%=\n"
        "s_barrier\n"
        "s_mov_b32 s65, 49280\n"
        "buffer_store_dwordx2 v[200:201], v202, s[40:43], s65 offen sc1\n"
        "s_endpgm\n"
            RED_ARGS : "v0", "v1", "v2", "v3", "v4", "v5", "v6", "v7", "v8", "v9", "v10", "v11", "v12", "v13", "v14", "v15", "v16", "v17", "v18", "v19", "v20", "v21", "v22", "v23", "v24", "v25", "v26", "v27", "v28", "v29", "v30", "v31", "v32", "v33", "v34", "v35", "v36", "v37", "v38", "v39", "v40", "v41", "v42", "v43", "v44", "v45", "v46", "v47", "v48", "v49", "v50", "v51", "v52", "v53", "v54", "v55", "v56", "v57", "v58", "v59", "v60", "v61", "v62", "v63", "v64", "v65", "v66", "v67", "v68", "v69", "v70", "v71", "v72", "v73", "v74", "v75", "v76", "v77", "v78", "v79", "v255", "a255", "s30", "s31", "s32", "s33", "s34", "s35", "s36", "s37", "s38", "s39", "s40", "s41", "s42", "s43", "s44", "s45", "s46", "s47", "s48", "s49", "s50", "s51", "s52", "s53", "s54", "s55", "s56", "s57", "s58", "s59", "s60", "s61", "s62", "s63", "s64", "s65", "s66", "s67", "s68", "s69", "s70", "s71", "s72", "s73", "s74", "s75", "s76", "s77", "s78", "s79", "s80", "s81", "s82", "s83", "s84", "s85", "s86", "s87", "s88", "s89", "s90", "s91", "s92", "s93", "s94", "s95", "s96", "s97", "s98", "s99", "vcc", "scc", "memory");
    } else {
        asm volatile(
        "s_nop 4\n"
        "s_mov_b64 s[40:41], %0\n"
        "s_and_b32 s41, s41, 0xffff\n"
        "s_mov_b32 s42, 236978176\n"
        "s_mov_b32 s43, 0x20000\n"
        "s_mov_b64 s[44:45], %1\n"
        "s_and_b32 s45, s45, 0xffff\n"
        "s_mov_b32 s46, 2359296\n"
        "s_mov_b32 s47, 0x20000\n"
        "s_mov_b64 s[56:57], %3\n"
        "s_mov_b64 s[58:59], %4\n"
        "s_mov_b64 s[60:61], %5\n"
        "s_mov_b64 s[62:63], %6\n"
        "v_mov_b64 v[0:1], %7\n"
        "v_mov_b64 v[2:3], %8\n"
        "v_mov_b64 v[4:5], %9\n"
        "v_mov_b64 v[6:7], %10\n"
        "v_mov_b64 v[8:9], %11\n"
        "v_mov_b64 v[10:11], %12\n"
        "v_mov_b64 v[12:13], %13\n"
        "v_mov_b64 v[14:15], %14\n"
        "v_mov_b64 v[16:17], %15\n"
        "v_mov_b64 v[18:19], %16\n"
        "v_mov_b64 v[20:21], %17\n"
        "v_mov_b64 v[22:23], %18\n"
        "v_mov_b64 v[24:25], %19\n"
        "v_mov_b64 v[26:27], %20\n"
        "v_mov_b64 v[200:201], v[24:25]\n"
        "v_mov_b32 v202, v26\n"
        "s_lshr_b32 s74, s56, 8\n"
        "s_and_b32 s56, s56, 1\n"
        "s_mov_b32 s68, 0\n"
        "s_mul_i32 s65, s68, 36864\n"
        "buffer_load_dwordx4 v[80:83], v5, s[44:47], s65 offen offset:0\n"
        "buffer_load_dwordx4 v[84:87], v5, s[44:47], s65 offen offset:128\n"
        "buffer_load_dwordx4 v[88:91], v5, s[44:47], s65 offen offset:256\n"
        "v_lshrrev_b32 v26, 4, v0\n"
        "s_mov_b32 s50, s56\n"
        "v_lshlrev_b32 v28, 7, v26\n"
        "s_sub_u32 s66, 0, s50\n"
        "s_mov_b32 s52, 32768\n"
        "s_mov_b32 s64, 0\n"
        ".Lr3_pg0_top_%=:\n"
        "buffer_load_dword v27, v28, s[40:43], s52 offen sc1\n"
        "s_waitcnt vmcnt(0)\n"
        "v_cmp_gt_i32 vcc, 0, v27\n"
        "s_cbranch_vccz .Lr3_pg0_ok_%=\n"
        "s_add_u32 s64, s64, 1\n"
        "s_and_b32 s65, s64, 255\n"
        "s_cbranch_scc1 .Lr3_pg0_go_%=\n"
        "s_mov_b32 s65, 49152\n"
        "s_cmp_lt_u32 s64, 1048576\n"
        "s_cbranch_scc1 .Lr3_pg0_chk_%=\n"
        "v_mov_b32 v27, 1\n"
        "buffer_store_dword v27, off, s[40:43], s65 sc1\n"
        "s_waitcnt vmcnt(0)\n"
        ".Lr3_pg0_chk_%=:\n"
        "buffer_load_dword v27, off, s[40:43], s65 sc1\n"
        "s_waitcnt vmcnt(0)\n"
        "v_readfirstlane_b32 s65, v27\n"
        "s_nop 0\n"
        "s_cmp_lg_u32 s65, 1\n"
        "s_cbranch_scc1 .Lr3_pg0_go_%=\n"
        "s_endpgm\n"
        ".Lr3_pg0_go_%=:\n"
        "s_sleep 2\n"
        "s_branch .Lr3_pg0_top_%=\n"
        ".Lr3_pg0_ok_%=:\n"
        "v_cmp_eq_u32 s[52:53], 1, v27\n"
        "s_nop 1\n"
        "s_mov_b32 s54, 40960\n"
        "s_mov_b32 s64, 0\n"
        ".Lr3_pg1_top_%=:\n"
        "buffer_load_dword v27, v28, s[40:43], s54 offen sc1\n"
        "s_waitcnt vmcnt(0)\n"
        "v_cmp_gt_i32 vcc, 0, v27\n"
        "s_cbranch_vccz .Lr3_pg1_ok_%=\n"
        "s_add_u32 s64, s64, 1\n"
        "s_and_b32 s65, s64, 255\n"
        "s_cbranch_scc1 .Lr3_pg1_go_%=\n"
        "s_mov_b32 s65, 49152\n"
        "s_cmp_lt_u32 s64, 1048576\n"
        "s_cbranch_scc1 .Lr3_pg1_chk_%=\n"
        "v_mov_b32 v27, 1\n"
        "buffer_store_dword v27, off, s[40:43], s65 sc1\n"
        "s_waitcnt vmcnt(0)\n"
        ".Lr3_pg1_chk_%=:\n"
        "buffer_load_dword v27, off, s[40:43], s65 sc1\n"
        "s_waitcnt vmcnt(0)\n"
        "v_readfirstlane_b32 s65, v27\n"
        "s_nop 0\n"
        "s_cmp_lg_u32 s65, 1\n"
        "s_cbranch_scc1 .Lr3_pg1_go_%=\n"
        "s_endpgm\n"
        ".Lr3_pg1_go_%=:\n"
        "s_sleep 2\n"
        "s_branch .Lr3_pg1_top_%=\n"
        ".Lr3_pg1_ok_%=:\n"
        "v_cmp_eq_u32 s[54:55], 1, v27\n"
        "s_nop 1\n"
        "s_mov_b32 vcc_lo, 0x55555555\n"
        "s_mov_b32 vcc_hi, 0x55555555\n"
        "s_xor_b64 vcc, vcc, s[52:53]\n"
        "s_bcnt1_i32_b64 s65, vcc\n"
        "s_sub_u32 s64, s65, 23\n"
        "s_cmp_lt_u32 s64, 19\n"
        "s_cbranch_scc1 .Lr3_rawmap_%=\n"
        "s_cmp_lt_u32 s65, 32\n"
        "s_cselect_b32 s65, 0, -1\n"
        "s_xor_b32 s52, s65, 0x55555555\n"
        "s_xor_b32 s53, s65, 0x55555555\n"
        "s_xor_b32 s54, s65, 0x55555555\n"
        "s_xor_b32 s55, s65, 0xaaaaaaaa\n"
        ".Lr3_rawmap_%=:\n"
        "s_xor_b32 s52, s52, s66\n"
        "s_xor_b32 s53, s53, s66\n"
        "s_xor_b32 s54, s54, s66\n"
        "s_xor_b32 s55, s55, s66\n"
        "s_and_b32 s69, s55, 1\n"
        "s_lshl_b32 s69, s69, 13\n"
        "s_xor_b32 s70, s69, 0\n"
        "s_cmp_eq_u32 s50, 0\n"
        "s_cbranch_scc0 .Lr3_l1_%=\n"
        ".Lr3_l0_%=:\n"
        "s_bitcmp1_b64 s[52:53], s68\n"
        "s_cselect_b32 s65, 8192, 0\n"
        "s_lshl_b32 s71, s68, 21\n"
        "s_add_u32 s71, s71, s65\n"
        "s_add_u32 s71, s71, 2097152\n"
        "s_lshr_b32 s65, s68, 1\n"
        "s_bitcmp1_b32 s54, s65\n"
        "s_cselect_b32 s65, 8192, 0\n"
        "s_lshl_b32 s72, s68, 20\n"
        "s_add_u32 s72, s72, s65\n"
        "s_add_u32 s72, s72, 136904704\n"
        "s_add_u32 s76, s72, s61\n"
        "s_add_u32 s79, s71, s58\n"
        "s_add_u32 s79, s79, 1916928\n"
        "s_add_u32 s77, s72, s62\n"
        "s_add_u32 s80, s71, s59\n"
        "s_add_u32 s80, s80, 1916928\n"
        "s_add_u32 s78, s72, s63\n"
        "s_add_u32 s81, s71, s60\n"
        "s_add_u32 s81, s81, 1916928\n"
        "s_lshl_b32 s73, s68, 1\n"
        "s_add_u32 s73, s73, s74\n"
        "s_mov_b32 s64, 0\n"
        ".Lr3_l0w_top_%=:\n"
        "buffer_load_dword v24, v1, s[40:43], s69 offen sc1\n"
        "s_waitcnt vmcnt(0)\n"
        "v_cmp_gt_i32 vcc, s73, v24\n"
        "s_nop 0\n"
        "s_and_b64 vcc, vcc, 0xffff\n"
        "s_cbranch_vccz .Lr3_l0w_ok_%=\n"
        "s_add_u32 s64, s64, 1\n"
        "s_and_b32 s65, s64, 255\n"
        "s_cbranch_scc1 .Lr3_l0w_go_%=\n"
        "s_mov_b32 s65, 49152\n"
        "s_cmp_lt_u32 s64, 1048576\n"
        "s_cbranch_scc1 .Lr3_l0w_chk_%=\n"
        "v_mov_b32 v24, 1\n"
        "buffer_store_dword v24, off, s[40:43], s65 sc1\n"
        "s_waitcnt vmcnt(0)\n"
        ".Lr3_l0w_chk_%=:\n"
        "buffer_load_dword v24, off, s[40:43], s65 sc1\n"
        "s_waitcnt vmcnt(0)\n"
        "v_readfirstlane_b32 s65, v24\n"
        "s_nop 0\n"
        "s_cmp_lg_u32 s65, 1\n"
        "s_cbranch_scc1 .Lr3_l0w_go_%=\n"
        "s_endpgm\n"
        ".Lr3_l0w_go_%=:\n"
        "s_branch .Lr3_l0w_top_%=\n"
        ".Lr3_l0w_ok_%=:\n"
        "buffer_load_dwordx4 v[32:35], v3, s[40:43], s76 offen offset:0 sc1\n"
        "buffer_load_dwordx4 v[40:43], v3, s[40:43], s76 offen offset:512 sc1\n"
        "buffer_load_dwordx4 v[36:39], v3, s[40:43], s76 offen offset:256 sc1\n"
        "buffer_load_dwordx4 v[44:47], v3, s[40:43], s76 offen offset:768 sc1\n"
        "buffer_load_dwordx4 v[48:51], v3, s[40:43], s77 offen offset:0 sc1\n"
        "buffer_load_dwordx4 v[56:59], v3, s[40:43], s77 offen offset:512 sc1\n"
        "buffer_load_dwordx4 v[52:55], v3, s[40:43], s77 offen offset:256 sc1\n"
        "buffer_load_dwordx4 v[60:63], v3, s[40:43], s77 offen offset:768 sc1\n"
        "buffer_load_dwordx4 v[64:67], v4, s[40:43], s78 offen offset:0 sc1\n"
        "buffer_load_dwordx4 v[72:75], v4, s[40:43], s78 offen offset:512 sc1\n"
        "buffer_load_dwordx4 v[68:71], v4, s[40:43], s78 offen offset:256 sc1\n"
        "buffer_load_dwordx4 v[76:79], v4, s[40:43], s78 offen offset:768 sc1\n"
        "s_add_u32 s72, s72, 294912\n"
        "s_add_u32 s71, s71, 1998848\n"
        "s_add_u32 s82, s72, s58\n"
        "s_add_u32 s85, s71, s58\n"
        "s_add_u32 s83, s72, s59\n"
        "s_add_u32 s86, s71, s59\n"
        "s_add_u32 s84, s72, s60\n"
        "s_add_u32 s87, s71, s60\n"
        "s_waitcnt vmcnt(11)\n"
        "v_mfma_f32_16x16x32_bf16 v[92:95], v[8:11], v[32:35], 0\n"
        "s_waitcnt vmcnt(10)\n"
        "v_mfma_f32_16x16x32_bf16 v[92:95], v[12:15], v[40:43], v[92:95]\n"
        "s_waitcnt vmcnt(9)\n"
        "v_mfma_f32_16x16x32_bf16 v[96:99], v[8:11], v[36:39], 0\n"
        "s_waitcnt vmcnt(8)\n"
        "v_mfma_f32_16x16x32_bf16 v[96:99], v[12:15], v[44:47], v[96:99]\n"
        "v_mfma_f32_16x16x32_bf16 v[92:95], v[16:19], v[80:83], v[92:95]\n"
        "v_mfma_f32_16x16x32_bf16 v[96:99], v[20:23], v[80:83], v[96:99]\n"
        "s_waitcnt vmcnt(7)\n"
        "v_mfma_f32_16x16x32_bf16 v[100:103], v[8:11], v[48:51], 0\n"
        "s_waitcnt vmcnt(6)\n"
        "v_mfma_f32_16x16x32_bf16 v[100:103], v[12:15], v[56:59], v[100:103]\n"
        "s_waitcnt vmcnt(5)\n"
        "v_mfma_f32_16x16x32_bf16 v[104:107], v[8:11], v[52:55], 0\n"
        "s_waitcnt vmcnt(4)\n"
        "v_mfma_f32_16x16x32_bf16 v[104:107], v[12:15], v[60:63], v[104:107]\n"
        "v_mfma_f32_16x16x32_bf16 v[100:103], v[16:19], v[84:87], v[100:103]\n"
        "v_mfma_f32_16x16x32_bf16 v[104:107], v[20:23], v[84:87], v[104:107]\n"
        "s_waitcnt vmcnt(3)\n"
        "v_mfma_f32_16x16x32_bf16 v[108:111], v[8:11], v[64:67], 0\n"
        "s_waitcnt vmcnt(2)\n"
        "v_mfma_f32_16x16x32_bf16 v[108:111], v[12:15], v[72:75], v[108:111]\n"
        "s_waitcnt vmcnt(1)\n"
        "v_mfma_f32_16x16x32_bf16 v[112:115], v[8:11], v[68:71], 0\n"
        "s_waitcnt vmcnt(0)\n"
        "v_mfma_f32_16x16x32_bf16 v[112:115], v[12:15], v[76:79], v[112:115]\n"
        "v_mfma_f32_16x16x32_bf16 v[108:111], v[16:19], v[88:91], v[108:111]\n"
        "v_mfma_f32_16x16x32_bf16 v[112:115], v[20:23], v[88:91], v[112:115]\n"
        "s_add_u32 s68, s68, 1\n"
        "s_mul_i32 s65, s68, 36864\n"
        "buffer_load_dwordx4 v[80:83], v5, s[44:47], s65 offen offset:0\n"
        "buffer_load_dwordx4 v[84:87], v5, s[44:47], s65 offen offset:128\n"
        "buffer_load_dwordx4 v[88:91], v5, s[44:47], s65 offen offset:256\n"
        "s_sub_u32 s68, s68, 1\n"
        "v_pk_add_f32 v[164:165], v[92:93], v[94:95]\n"
        "v_pk_add_f32 v[166:167], v[96:97], v[98:99]\n"
        "v_pk_add_f32 v[168:169], v[100:101], v[102:103]\n"
        "v_pk_add_f32 v[170:171], v[104:105], v[106:107]\n"
        "v_pk_add_f32 v[164:165], v[164:165], v[166:167]\n"
        "v_pk_add_f32 v[168:169], v[168:169], v[170:171]\n"
        "v_pk_add_f32 v[164:165], v[164:165], v[168:169]\n"
        "v_pk_add_f32 v[166:167], v[108:109], v[110:111]\n"
        "v_pk_add_f32 v[168:169], v[112:113], v[114:115]\n"
        "v_pk_add_f32 v[164:165], v[164:165], v[166:167]\n"
        "v_pk_add_f32 v[164:165], v[164:165], v[168:169]\n"
        "v_add_f32 v164, v164, v165\n"
        "v_cvt_pk_bf16_f32 v140, v92, v93\n"
        "v_cvt_pk_bf16_f32 v141, v94, v95\n"
        "v_add_f32_dpp v166, v164, v164 row_ror:8 row_mask:0xf bank_mask:0xf\n"
        "v_add_f32_dpp v167, v164, v164 row_ror:8 row_mask:0xf bank_mask:0xf\n"
        "v_cvt_pk_bf16_f32 v142, v96, v97\n"
        "v_cvt_pk_bf16_f32 v143, v98, v99\n"
        "v_permlane16_swap_b32 v166, v167\n"
        "v_add_f32 v164, v166, v167\n"
        "v_add_f32 v166, v166, v167\n"
        "v_cvt_pk_bf16_f32 v144, v100, v101\n"
        "v_cvt_pk_bf16_f32 v145, v102, v103\n"
        "v_permlane32_swap_b32 v164, v166\n"
        "v_add_f32 v164, v164, v166\n"
        "v_mul_f32 v164, 0x3baaaaab, v164\n"
        "v_pk_add_f32 v[116:117], v[92:93], v[164:165] op_sel_hi:[1,0] neg_lo:[0,1] neg_hi:[0,1]\n"
        "v_pk_add_f32 v[118:119], v[94:95], v[164:165] op_sel_hi:[1,0] neg_lo:[0,1] neg_hi:[0,1]\n"
        "v_pk_add_f32 v[120:121], v[96:97], v[164:165] op_sel_hi:[1,0] neg_lo:[0,1] neg_hi:[0,1]\n"
        "v_pk_add_f32 v[122:123], v[98:99], v[164:165] op_sel_hi:[1,0] neg_lo:[0,1] neg_hi:[0,1]\n"
        "v_pk_add_f32 v[124:125], v[100:101], v[164:165] op_sel_hi:[1,0] neg_lo:[0,1] neg_hi:[0,1]\n"
        "v_pk_add_f32 v[126:127], v[102:103], v[164:165] op_sel_hi:[1,0] neg_lo:[0,1] neg_hi:[0,1]\n"
        "v_pk_add_f32 v[128:129], v[104:105], v[164:165] op_sel_hi:[1,0] neg_lo:[0,1] neg_hi:[0,1]\n"
        "v_pk_add_f32 v[130:131], v[106:107], v[164:165] op_sel_hi:[1,0] neg_lo:[0,1] neg_hi:[0,1]\n"
        "v_pk_add_f32 v[132:133], v[108:109], v[164:165] op_sel_hi:[1,0] neg_lo:[0,1] neg_hi:[0,1]\n"
        "v_pk_add_f32 v[134:135], v[110:111], v[164:165] op_sel_hi:[1,0] neg_lo:[0,1] neg_hi:[0,1]\n"
        "v_pk_add_f32 v[136:137], v[112:113], v[164:165] op_sel_hi:[1,0] neg_lo:[0,1] neg_hi:[0,1]\n"
        "v_pk_add_f32 v[138:139], v[114:115], v[164:165] op_sel_hi:[1,0] neg_lo:[0,1] neg_hi:[0,1]\n"
        "v_pk_mul_f32 v[166:167], v[116:117], v[116:117]\n"
        "v_pk_mul_f32 v[168:169], v[118:119], v[118:119]\n"
        "v_pk_fma_f32 v[166:167], v[120:121], v[120:121], v[166:167]\n"
        "v_pk_fma_f32 v[168:169], v[122:123], v[122:123], v[168:169]\n"
        "v_pk_fma_f32 v[166:167], v[124:125], v[124:125], v[166:167]\n"
        "v_pk_fma_f32 v[168:169], v[126:127], v[126:127], v[168:169]\n"
        "v_pk_fma_f32 v[166:167], v[128:129], v[128:129], v[166:167]\n"
        "v_pk_fma_f32 v[168:169], v[130:131], v[130:131], v[168:169]\n"
        "v_pk_fma_f32 v[166:167], v[132:133], v[132:133], v[166:167]\n"
        "v_pk_fma_f32 v[168:169], v[134:135], v[134:135], v[168:169]\n"
        "v_pk_fma_f32 v[166:167], v[136:137], v[136:137], v[166:167]\n"
        "v_pk_fma_f32 v[168:169], v[138:139], v[138:139], v[168:169]\n"
        "v_pk_add_f32 v[166:167], v[166:167], v[168:169]\n"
        "v_add_f32 v165, v166, v167\n"
        "v_cvt_pk_bf16_f32 v146, v104, v105\n"
        "v_cvt_pk_bf16_f32 v147, v106, v107\n"
        "v_add_f32_dpp v166, v165, v165 row_ror:8 row_mask:0xf bank_mask:0xf\n"
        "v_add_f32_dpp v167, v165, v165 row_ror:8 row_mask:0xf bank_mask:0xf\n"
        "v_cvt_pk_bf16_f32 v148, v108, v109\n"
        "v_cvt_pk_bf16_f32 v149, v110, v111\n"
        "v_permlane16_swap_b32 v166, v167\n"
        "v_add_f32 v165, v166, v167\n"
        "v_add_f32 v166, v166, v167\n"
        "v_cvt_pk_bf16_f32 v150, v112, v113\n"
        "v_cvt_pk_bf16_f32 v151, v114, v115\n"
        "v_permlane32_swap_b32 v165, v166\n"
        "v_add_f32 v165, v165, v166\n"
        "ds_write_b64 v6, v[164:165]\n"
        "v_permlane16_swap_b32 v140, v142\n"
        "v_permlane16_swap_b32 v141, v143\n"
        "v_permlane16_swap_b32 v144, v146\n"
        "v_permlane16_swap_b32 v145, v147\n"
        "s_waitcnt lgkmcnt(0)\n"
        "s_barrier\n"
        "ds_read_b64 v[166:167], v7 offset:0\n"
        "ds_read_b64 v[168:169], v7 offset:64\n"
        "ds_read_b64 v[170:171], v7 offset:128\n"
        "v_permlane16_swap_b32 v148, v150\n"
        "v_permlane16_swap_b32 v149, v151\n"
        "buffer_store_dwordx4 v[140:143], v0, s[40:43], s85 offen sc1\n"
        "buffer_store_dwordx4 v[144:147], v0, s[40:43], s86 offen sc1\n"
        "buffer_store_dwordx4 v[148:151], v0, s[40:43], s87 offen sc1\n"
        "s_waitcnt lgkmcnt(0)\n"
        "v_add_f32 v172, v166, v168\n"
        "v_add_f32 v173, v167, v169\n"
        "v_add_f32 v172, v172, v170\n"
        "v_add_f32 v173, v173, v171\n"
        "v_mul_f32 v172, 0x3eaaaaab, v172\n"
        "v_sub_f32 v166, v166, v172\n"
        "v_sub_f32 v168, v168, v172\n"
        "v_sub_f32 v170, v170, v172\n"
        "v_mul_f32 v166, v166, v166\n"
        "v_fmac_f32 v166, v168, v168\n"
        "v_fmac_f32 v166, v170, v170\n"
        "v_fmamk_f32 v173, v166, 0x43400000, v173\n"
        "v_mul_f32 v173, 0x3ae38e39, v173\n"
        "v_cvt_pk_bf16_f32 v172, v172, v173\n"
        "v_and_b32 v173, 0xffff0000, v172\n"
        "v_lshlrev_b32 v172, 16, v172\n"
        "v_add_f32 v173, 0x3727c5ac, v173\n"
        "v_cvt_pk_bf16_f32 v173, v173, v173\n"
        "v_lshlrev_b32 v173, 16, v173\n"
        "v_sqrt_f32 v173, v173\n"
        "v_sub_f32 v172, v164, v172\n"
        "v_cvt_pk_bf16_f32 v173, v173, v173\n"
        "v_lshlrev_b32 v173, 16, v173\n"
        "v_rcp_f32 v173, v173\n"
        "v_mov_b32 v25, s68\n"
        "v_mul_f32 v172, v172, v173\n"
        "v_pk_fma_f32 v[116:117], v[116:117], v[172:173], v[172:173] op_sel:[0,1,0] op_sel_hi:[1,1,0]\n"
        "v_pk_fma_f32 v[118:119], v[118:119], v[172:173], v[172:173] op_sel:[0,1,0] op_sel_hi:[1,1,0]\n"
        "v_pk_fma_f32 v[120:121], v[120:121], v[172:173], v[172:173] op_sel:[0,1,0] op_sel_hi:[1,1,0]\n"
        "v_pk_fma_f32 v[122:123], v[122:123], v[172:173], v[172:173] op_sel:[0,1,0] op_sel_hi:[1,1,0]\n"
        "v_cvt_pk_bf16_f32 v152, v116, v117\n"
        "v_cvt_pk_bf16_f32 v153, v118, v119\n"
        "v_cvt_pk_bf16_f32 v154, v120, v121\n"
        "v_cvt_pk_bf16_f32 v155, v122, v123\n"
        "s_nop 0\n"
        "v_permlane16_swap_b32 v152, v154\n"
        "v_permlane16_swap_b32 v153, v155\n"
        "buffer_store_dwordx4 v[152:155], v0, s[40:43], s82 offen sc1\n"
        "v_pk_fma_f32 v[124:125], v[124:125], v[172:173], v[172:173] op_sel:[0,1,0] op_sel_hi:[1,1,0]\n"
        "v_pk_fma_f32 v[126:127], v[126:127], v[172:173], v[172:173] op_sel:[0,1,0] op_sel_hi:[1,1,0]\n"
        "v_pk_fma_f32 v[128:129], v[128:129], v[172:173], v[172:173] op_sel:[0,1,0] op_sel_hi:[1,1,0]\n"
        "v_pk_fma_f32 v[130:131], v[130:131], v[172:173], v[172:173] op_sel:[0,1,0] op_sel_hi:[1,1,0]\n"
        "v_cvt_pk_bf16_f32 v156, v124, v125\n"
        "v_cvt_pk_bf16_f32 v157, v126, v127\n"
        "v_cvt_pk_bf16_f32 v158, v128, v129\n"
        "v_cvt_pk_bf16_f32 v159, v130, v131\n"
        "s_nop 0\n"
        "v_permlane16_swap_b32 v156, v158\n"
        "v_permlane16_swap_b32 v157, v159\n"
        "buffer_store_dwordx4 v[156:159], v0, s[40:43], s83 offen sc1\n"
        "v_pk_fma_f32 v[132:133], v[132:133], v[172:173], v[172:173] op_sel:[0,1,0] op_sel_hi:[1,1,0]\n"
        "v_pk_fma_f32 v[134:135], v[134:135], v[172:173], v[172:173] op_sel:[0,1,0] op_sel_hi:[1,1,0]\n"
        "v_pk_fma_f32 v[136:137], v[136:137], v[172:173], v[172:173] op_sel:[0,1,0] op_sel_hi:[1,1,0]\n"
        "v_pk_fma_f32 v[138:139], v[138:139], v[172:173], v[172:173] op_sel:[0,1,0] op_sel_hi:[1,1,0]\n"
        "v_cvt_pk_bf16_f32 v160, v132, v133\n"
        "v_cvt_pk_bf16_f32 v161, v134, v135\n"
        "v_cvt_pk_bf16_f32 v162, v136, v137\n"
        "v_cvt_pk_bf16_f32 v163, v138, v139\n"
        "s_nop 0\n"
        "v_permlane16_swap_b32 v160, v162\n"
        "v_permlane16_swap_b32 v161, v163\n"
        "buffer_store_dwordx4 v[160:163], v0, s[40:43], s84 offen sc1\n"
        "s_waitcnt vmcnt(0)\n"
        "s_barrier\n"
        "buffer_store_dword v25, v2, s[40:43], s70 offen sc1\n"
        "s_add_u32 s68, s68, 1\n"
        "s_cmp_lt_u32 s68, 64\n"
        "s_cbranch_scc1 .Lr3_l0_%=\n"
        "s_endpgm\n"
        ".Lr3_l1_%=:\n"
        "s_bitcmp1_b64 s[52:53], s68\n"
        "s_cselect_b32 s65, 8192, 0\n"
        "s_lshl_b32 s71, s68, 21\n"
        "s_add_u32 s71, s71, s65\n"
        "s_add_u32 s71, s71, 2097152\n"
        "s_lshr_b32 s65, s68, 1\n"
        "s_bitcmp1_b32 s54, s65\n"
        "s_cselect_b32 s65, 8192, 0\n"
        "s_lshl_b32 s72, s68, 20\n"
        "s_add_u32 s72, s72, s65\n"
        "s_add_u32 s72, s72, 136904704\n"
        "s_add_u32 s76, s72, s61\n"
        "s_add_u32 s79, s71, s58\n"
        "s_add_u32 s79, s79, 1916928\n"
        "s_add_u32 s77, s72, s62\n"
        "s_add_u32 s80, s71, s59\n"
        "s_add_u32 s80, s80, 1916928\n"
        "s_add_u32 s78, s72, s63\n"
        "s_add_u32 s81, s71, s60\n"
        "s_add_u32 s81, s81, 1916928\n"
        "s_lshl_b32 s73, s68, 1\n"
        "s_add_u32 s73, s73, s74\n"
        "s_mov_b32 s64, 0\n"
        ".Lr3_l1w_top_%=:\n"
        "buffer_load_dword v24, v1, s[40:43], s69 offen sc1\n"
        "s_waitcnt vmcnt(0)\n"
        "v_cmp_gt_i32 vcc, s73, v24\n"
        "s_nop 0\n"
        "s_and_b64 vcc, vcc, 0xffff\n"
        "s_cbranch_vccz .Lr3_l1w_ok_%=\n"
        "s_add_u32 s64, s64, 1\n"
        "s_and_b32 s65, s64, 255\n"
        "s_cbranch_scc1 .Lr3_l1w_go_%=\n"
        "s_mov_b32 s65, 49152\n"
        "s_cmp_lt_u32 s64, 1048576\n"
        "s_cbranch_scc1 .Lr3_l1w_chk_%=\n"
        "v_mov_b32 v24, 1\n"
        "buffer_store_dword v24, off, s[40:43], s65 sc1\n"
        "s_waitcnt vmcnt(0)\n"
        ".Lr3_l1w_chk_%=:\n"
        "buffer_load_dword v24, off, s[40:43], s65 sc1\n"
        "s_waitcnt vmcnt(0)\n"
        "v_readfirstlane_b32 s65, v24\n"
        "s_nop 0\n"
        "s_cmp_lg_u32 s65, 1\n"
        "s_cbranch_scc1 .Lr3_l1w_go_%=\n"
        "s_endpgm\n"
        ".Lr3_l1w_go_%=:\n"
        "s_branch .Lr3_l1w_top_%=\n"
        ".Lr3_l1w_ok_%=:\n"
        "buffer_load_dwordx4 v[32:35], v3, s[40:43], s76 offen offset:0 sc1\n"
        "buffer_load_dwordx4 v[40:43], v3, s[40:43], s76 offen offset:512 sc1\n"
        "buffer_load_dwordx4 v[36:39], v3, s[40:43], s76 offen offset:256 sc1\n"
        "buffer_load_dwordx4 v[44:47], v3, s[40:43], s76 offen offset:768 sc1\n"
        "buffer_load_dwordx4 v[80:83], v0, s[40:43], s79 offen sc1\n"
        "buffer_load_dwordx4 v[48:51], v3, s[40:43], s77 offen offset:0 sc1\n"
        "buffer_load_dwordx4 v[56:59], v3, s[40:43], s77 offen offset:512 sc1\n"
        "buffer_load_dwordx4 v[52:55], v3, s[40:43], s77 offen offset:256 sc1\n"
        "buffer_load_dwordx4 v[60:63], v3, s[40:43], s77 offen offset:768 sc1\n"
        "buffer_load_dwordx4 v[84:87], v0, s[40:43], s80 offen sc1\n"
        "buffer_load_dwordx4 v[64:67], v4, s[40:43], s78 offen offset:0 sc1\n"
        "buffer_load_dwordx4 v[72:75], v4, s[40:43], s78 offen offset:512 sc1\n"
        "buffer_load_dwordx4 v[68:71], v4, s[40:43], s78 offen offset:256 sc1\n"
        "buffer_load_dwordx4 v[76:79], v4, s[40:43], s78 offen offset:768 sc1\n"
        "buffer_load_dwordx4 v[88:91], v0, s[40:43], s81 offen sc1\n"
        "s_add_u32 s72, s72, 294912\n"
        "s_add_u32 s71, s71, 1998848\n"
        "s_add_u32 s82, s72, s58\n"
        "s_add_u32 s85, s71, s58\n"
        "s_add_u32 s83, s72, s59\n"
        "s_add_u32 s86, s71, s59\n"
        "s_add_u32 s84, s72, s60\n"
        "s_add_u32 s87, s71, s60\n"
        "s_waitcnt vmcnt(14)\n"
        "v_mfma_f32_16x16x32_bf16 v[92:95], v[8:11], v[32:35], 0\n"
        "s_waitcnt vmcnt(13)\n"
        "v_mfma_f32_16x16x32_bf16 v[92:95], v[12:15], v[40:43], v[92:95]\n"
        "s_waitcnt vmcnt(12)\n"
        "v_mfma_f32_16x16x32_bf16 v[96:99], v[8:11], v[36:39], 0\n"
        "s_waitcnt vmcnt(11)\n"
        "v_mfma_f32_16x16x32_bf16 v[96:99], v[12:15], v[44:47], v[96:99]\n"
        "s_waitcnt vmcnt(10)\n"
        "v_mfma_f32_16x16x32_bf16 v[92:95], v[16:19], v[80:83], v[92:95]\n"
        "v_mfma_f32_16x16x32_bf16 v[96:99], v[20:23], v[80:83], v[96:99]\n"
        "s_waitcnt vmcnt(9)\n"
        "v_mfma_f32_16x16x32_bf16 v[100:103], v[8:11], v[48:51], 0\n"
        "s_waitcnt vmcnt(8)\n"
        "v_mfma_f32_16x16x32_bf16 v[100:103], v[12:15], v[56:59], v[100:103]\n"
        "s_waitcnt vmcnt(7)\n"
        "v_mfma_f32_16x16x32_bf16 v[104:107], v[8:11], v[52:55], 0\n"
        "s_waitcnt vmcnt(6)\n"
        "v_mfma_f32_16x16x32_bf16 v[104:107], v[12:15], v[60:63], v[104:107]\n"
        "s_waitcnt vmcnt(5)\n"
        "v_mfma_f32_16x16x32_bf16 v[100:103], v[16:19], v[84:87], v[100:103]\n"
        "v_mfma_f32_16x16x32_bf16 v[104:107], v[20:23], v[84:87], v[104:107]\n"
        "s_waitcnt vmcnt(4)\n"
        "v_mfma_f32_16x16x32_bf16 v[108:111], v[8:11], v[64:67], 0\n"
        "s_waitcnt vmcnt(3)\n"
        "v_mfma_f32_16x16x32_bf16 v[108:111], v[12:15], v[72:75], v[108:111]\n"
        "s_waitcnt vmcnt(2)\n"
        "v_mfma_f32_16x16x32_bf16 v[112:115], v[8:11], v[68:71], 0\n"
        "s_waitcnt vmcnt(1)\n"
        "v_mfma_f32_16x16x32_bf16 v[112:115], v[12:15], v[76:79], v[112:115]\n"
        "s_waitcnt vmcnt(0)\n"
        "v_mfma_f32_16x16x32_bf16 v[108:111], v[16:19], v[88:91], v[108:111]\n"
        "v_mfma_f32_16x16x32_bf16 v[112:115], v[20:23], v[88:91], v[112:115]\n"
        "s_add_u32 s68, s68, 1\n"
        "s_sub_u32 s68, s68, 1\n"
        "v_pk_add_f32 v[164:165], v[92:93], v[94:95]\n"
        "v_pk_add_f32 v[166:167], v[96:97], v[98:99]\n"
        "v_pk_add_f32 v[168:169], v[100:101], v[102:103]\n"
        "v_pk_add_f32 v[170:171], v[104:105], v[106:107]\n"
        "v_pk_add_f32 v[164:165], v[164:165], v[166:167]\n"
        "v_pk_add_f32 v[168:169], v[168:169], v[170:171]\n"
        "v_pk_add_f32 v[164:165], v[164:165], v[168:169]\n"
        "s_nop 0\n"
        "v_pk_add_f32 v[166:167], v[108:109], v[110:111]\n"
        "v_pk_add_f32 v[168:169], v[112:113], v[114:115]\n"
        "v_pk_add_f32 v[164:165], v[164:165], v[166:167]\n"
        "v_pk_add_f32 v[164:165], v[164:165], v[168:169]\n"
        "v_add_f32 v164, v164, v165\n"
        "v_cvt_pk_bf16_f32 v140, v92, v93\n"
        "v_cvt_pk_bf16_f32 v141, v94, v95\n"
        "v_add_f32_dpp v166, v164, v164 row_ror:8 row_mask:0xf bank_mask:0xf\n"
        "v_add_f32_dpp v167, v164, v164 row_ror:8 row_mask:0xf bank_mask:0xf\n"
        "v_cvt_pk_bf16_f32 v142, v96, v97\n"
        "v_cvt_pk_bf16_f32 v143, v98, v99\n"
        "v_permlane16_swap_b32 v166, v167\n"
        "v_add_f32 v164, v166, v167\n"
        "v_add_f32 v166, v166, v167\n"
        "v_cvt_pk_bf16_f32 v144, v100, v101\n"
        "v_cvt_pk_bf16_f32 v145, v102, v103\n"
        "v_permlane32_swap_b32 v164, v166\n"
        "v_add_f32 v164, v164, v166\n"
        "v_mul_f32 v164, 0x3baaaaab, v164\n"
        "v_pk_add_f32 v[116:117], v[92:93], v[164:165] op_sel_hi:[1,0] neg_lo:[0,1] neg_hi:[0,1]\n"
        "v_pk_add_f32 v[118:119], v[94:95], v[164:165] op_sel_hi:[1,0] neg_lo:[0,1] neg_hi:[0,1]\n"
        "v_pk_add_f32 v[120:121], v[96:97], v[164:165] op_sel_hi:[1,0] neg_lo:[0,1] neg_hi:[0,1]\n"
        "v_pk_add_f32 v[122:123], v[98:99], v[164:165] op_sel_hi:[1,0] neg_lo:[0,1] neg_hi:[0,1]\n"
        "v_pk_add_f32 v[124:125], v[100:101], v[164:165] op_sel_hi:[1,0] neg_lo:[0,1] neg_hi:[0,1]\n"
        "v_pk_add_f32 v[126:127], v[102:103], v[164:165] op_sel_hi:[1,0] neg_lo:[0,1] neg_hi:[0,1]\n"
        "v_pk_add_f32 v[128:129], v[104:105], v[164:165] op_sel_hi:[1,0] neg_lo:[0,1] neg_hi:[0,1]\n"
        "v_pk_add_f32 v[130:131], v[106:107], v[164:165] op_sel_hi:[1,0] neg_lo:[0,1] neg_hi:[0,1]\n"
        "v_pk_add_f32 v[132:133], v[108:109], v[164:165] op_sel_hi:[1,0] neg_lo:[0,1] neg_hi:[0,1]\n"
        "v_pk_add_f32 v[134:135], v[110:111], v[164:165] op_sel_hi:[1,0] neg_lo:[0,1] neg_hi:[0,1]\n"
        "v_pk_add_f32 v[136:137], v[112:113], v[164:165] op_sel_hi:[1,0] neg_lo:[0,1] neg_hi:[0,1]\n"
        "v_pk_add_f32 v[138:139], v[114:115], v[164:165] op_sel_hi:[1,0] neg_lo:[0,1] neg_hi:[0,1]\n"
        "v_pk_mul_f32 v[166:167], v[116:117], v[116:117]\n"
        "v_pk_mul_f32 v[168:169], v[118:119], v[118:119]\n"
        "v_pk_fma_f32 v[166:167], v[120:121], v[120:121], v[166:167]\n"
        "v_pk_fma_f32 v[168:169], v[122:123], v[122:123], v[168:169]\n"
        "v_pk_fma_f32 v[166:167], v[124:125], v[124:125], v[166:167]\n"
        "v_pk_fma_f32 v[168:169], v[126:127], v[126:127], v[168:169]\n"
        "v_pk_fma_f32 v[166:167], v[128:129], v[128:129], v[166:167]\n"
        "v_pk_fma_f32 v[168:169], v[130:131], v[130:131], v[168:169]\n"
        "v_pk_fma_f32 v[166:167], v[132:133], v[132:133], v[166:167]\n"
        "v_pk_fma_f32 v[168:169], v[134:135], v[134:135], v[168:169]\n"
        "v_pk_fma_f32 v[166:167], v[136:137], v[136:137], v[166:167]\n"
        "v_pk_fma_f32 v[168:169], v[138:139], v[138:139], v[168:169]\n"
        "v_pk_add_f32 v[166:167], v[166:167], v[168:169]\n"
        "v_add_f32 v165, v166, v167\n"
        "v_cvt_pk_bf16_f32 v146, v104, v105\n"
        "v_cvt_pk_bf16_f32 v147, v106, v107\n"
        "v_add_f32_dpp v166, v165, v165 row_ror:8 row_mask:0xf bank_mask:0xf\n"
        "v_add_f32_dpp v167, v165, v165 row_ror:8 row_mask:0xf bank_mask:0xf\n"
        "v_cvt_pk_bf16_f32 v148, v108, v109\n"
        "v_cvt_pk_bf16_f32 v149, v110, v111\n"
        "v_permlane16_swap_b32 v166, v167\n"
        "v_add_f32 v165, v166, v167\n"
        "v_add_f32 v166, v166, v167\n"
        "v_cvt_pk_bf16_f32 v150, v112, v113\n"
        "v_cvt_pk_bf16_f32 v151, v114, v115\n"
        "v_permlane32_swap_b32 v165, v166\n"
        "v_add_f32 v165, v165, v166\n"
        "ds_write_b64 v6, v[164:165]\n"
        "v_permlane16_swap_b32 v140, v142\n"
        "v_permlane16_swap_b32 v141, v143\n"
        "v_permlane16_swap_b32 v144, v146\n"
        "v_permlane16_swap_b32 v145, v147\n"
        "s_waitcnt lgkmcnt(0)\n"
        "s_barrier\n"
        "ds_read_b64 v[166:167], v7 offset:0\n"
        "ds_read_b64 v[168:169], v7 offset:64\n"
        "ds_read_b64 v[170:171], v7 offset:128\n"
        "v_permlane16_swap_b32 v148, v150\n"
        "v_permlane16_swap_b32 v149, v151\n"
        "buffer_store_dwordx4 v[140:143], v0, s[40:43], s85 offen sc1\n"
        "buffer_store_dwordx4 v[144:147], v0, s[40:43], s86 offen sc1\n"
        "buffer_store_dwordx4 v[148:151], v0, s[40:43], s87 offen sc1\n"
        "s_waitcnt lgkmcnt(0)\n"
        "v_add_f32 v172, v166, v168\n"
        "v_add_f32 v173, v167, v169\n"
        "v_add_f32 v172, v172, v170\n"
        "v_add_f32 v173, v173, v171\n"
        "v_mul_f32 v172, 0x3eaaaaab, v172\n"
        "v_sub_f32 v166, v166, v172\n"
        "v_sub_f32 v168, v168, v172\n"
        "v_sub_f32 v170, v170, v172\n"
        "v_mul_f32 v166, v166, v166\n"
        "v_fmac_f32 v166, v168, v168\n"
        "v_fmac_f32 v166, v170, v170\n"
        "v_fmamk_f32 v173, v166, 0x43400000, v173\n"
        "v_mul_f32 v173, 0x3ae38e39, v173\n"
        "v_cvt_pk_bf16_f32 v172, v172, v173\n"
        "v_and_b32 v173, 0xffff0000, v172\n"
        "v_lshlrev_b32 v172, 16, v172\n"
        "v_add_f32 v173, 0x3727c5ac, v173\n"
        "v_cvt_pk_bf16_f32 v173, v173, v173\n"
        "v_lshlrev_b32 v173, 16, v173\n"
        "v_sqrt_f32 v173, v173\n"
        "v_sub_f32 v172, v164, v172\n"
        "v_cvt_pk_bf16_f32 v173, v173, v173\n"
        "v_lshlrev_b32 v173, 16, v173\n"
        "v_rcp_f32 v173, v173\n"
        "v_mov_b32 v25, s68\n"
        "v_mul_f32 v172, v172, v173\n"
        "v_pk_fma_f32 v[116:117], v[116:117], v[172:173], v[172:173] op_sel:[0,1,0] op_sel_hi:[1,1,0]\n"
        "v_pk_fma_f32 v[118:119], v[118:119], v[172:173], v[172:173] op_sel:[0,1,0] op_sel_hi:[1,1,0]\n"
        "v_pk_fma_f32 v[120:121], v[120:121], v[172:173], v[172:173] op_sel:[0,1,0] op_sel_hi:[1,1,0]\n"
        "v_pk_fma_f32 v[122:123], v[122:123], v[172:173], v[172:173] op_sel:[0,1,0] op_sel_hi:[1,1,0]\n"
        "v_cvt_pk_bf16_f32 v152, v116, v117\n"
        "v_cvt_pk_bf16_f32 v153, v118, v119\n"
        "v_cvt_pk_bf16_f32 v154, v120, v121\n"
        "v_cvt_pk_bf16_f32 v155, v122, v123\n"
        "s_nop 0\n"
        "v_permlane16_swap_b32 v152, v154\n"
        "v_permlane16_swap_b32 v153, v155\n"
        "buffer_store_dwordx4 v[152:155], v0, s[40:43], s82 offen sc1\n"
        "v_pk_fma_f32 v[124:125], v[124:125], v[172:173], v[172:173] op_sel:[0,1,0] op_sel_hi:[1,1,0]\n"
        "v_pk_fma_f32 v[126:127], v[126:127], v[172:173], v[172:173] op_sel:[0,1,0] op_sel_hi:[1,1,0]\n"
        "v_pk_fma_f32 v[128:129], v[128:129], v[172:173], v[172:173] op_sel:[0,1,0] op_sel_hi:[1,1,0]\n"
        "v_pk_fma_f32 v[130:131], v[130:131], v[172:173], v[172:173] op_sel:[0,1,0] op_sel_hi:[1,1,0]\n"
        "v_cvt_pk_bf16_f32 v156, v124, v125\n"
        "v_cvt_pk_bf16_f32 v157, v126, v127\n"
        "v_cvt_pk_bf16_f32 v158, v128, v129\n"
        "v_cvt_pk_bf16_f32 v159, v130, v131\n"
        "s_nop 0\n"
        "v_permlane16_swap_b32 v156, v158\n"
        "v_permlane16_swap_b32 v157, v159\n"
        "buffer_store_dwordx4 v[156:159], v0, s[40:43], s83 offen sc1\n"
        "v_pk_fma_f32 v[132:133], v[132:133], v[172:173], v[172:173] op_sel:[0,1,0] op_sel_hi:[1,1,0]\n"
        "v_pk_fma_f32 v[134:135], v[134:135], v[172:173], v[172:173] op_sel:[0,1,0] op_sel_hi:[1,1,0]\n"
        "v_pk_fma_f32 v[136:137], v[136:137], v[172:173], v[172:173] op_sel:[0,1,0] op_sel_hi:[1,1,0]\n"
        "v_pk_fma_f32 v[138:139], v[138:139], v[172:173], v[172:173] op_sel:[0,1,0] op_sel_hi:[1,1,0]\n"
        "v_cvt_pk_bf16_f32 v160, v132, v133\n"
        "v_cvt_pk_bf16_f32 v161, v134, v135\n"
        "v_cvt_pk_bf16_f32 v162, v136, v137\n"
        "v_cvt_pk_bf16_f32 v163, v138, v139\n"
        "s_nop 0\n"
        "v_permlane16_swap_b32 v160, v162\n"
        "v_permlane16_swap_b32 v161, v163\n"
        "buffer_store_dwordx4 v[160:163], v0, s[40:43], s84 offen sc1\n"
        "s_waitcnt vmcnt(0)\n"
        "s_barrier\n"
        "buffer_store_dword v25, v2, s[40:43], s70 offen sc1\n"
        "s_add_u32 s68, s68, 1\n"
        "s_cmp_lt_u32 s68, 64\n"
        "s_cbranch_scc1 .Lr3_l1_%=\n"
        "s_endpgm\n"
            RED_ARGS : "v0", "v1", "v2", "v3", "v4", "v5", "v6", "v7", "v8", "v9", "v10", "v11", "v12", "v13", "v14", "v15", "v16", "v17", "v18", "v19", "v20", "v21", "v22", "v23", "v24", "v25", "v26", "v27", "v28", "v29", "v30", "v31", "v32", "v33", "v34", "v35", "v36", "v37", "v38", "v39", "v40", "v41", "v42", "v43", "v44", "v45", "v46", "v47", "v48", "v49", "v50", "v51", "v52", "v53", "v54", "v55", "v56", "v57", "v58", "v59", "v60", "v61", "v62", "v63", "v64", "v65", "v66", "v67", "v68", "v69", "v70", "v71", "v72", "v73", "v74", "v75", "v76", "v77", "v78", "v79", "v255", "a255", "s30", "s31", "s32", "s33", "s34", "s35", "s36", "s37", "s38", "s39", "s40", "s41", "s42", "s43", "s44", "s45", "s46", "s47", "s48", "s49", "s50", "s51", "s52", "s53", "s54", "s55", "s56", "s57", "s58", "s59", "s60", "s61", "s62", "s63", "s64", "s65", "s66", "s67", "s68", "s69", "s70", "s71", "s72", "s73", "s74", "s75", "s76", "s77", "s78", "s79", "s80", "s81", "s82", "s83", "s84", "s85", "s86", "s87", "s88", "s89", "s90", "s91", "s92", "s93", "s94", "s95", "s96", "s97", "s98", "s99", "vcc", "scc", "memory");
    }
    __builtin_unreachable();
}

__global__ __launch_bounds__(256, 1) void mega_kernel(Params p) {
    extern __shared__ __attribute__((aligned(16))) char smem[];
    const rsrc_t rs = __builtin_amdgcn_make_buffer_rsrc(p.ws, 0, WS_USED, 0x00020000);
    const v2u ev = __builtin_amdgcn_raw_buffer_load_b64(rs, G_EPOCH * 128, 0, 16);
    if (blockIdx.x == 0) st4(rs, threadIdx.x <= 128 ? (G_PG + threadIdx.x) * 128 : OOB, -1);
    const u64 epoch = pair(rfl(ev[0]), rfl(ev[1])), tag = (epoch + 0x9e3779b97f4a7c15ull) & ~7ull;
    if (blockIdx.x == 0) {
        DRAIN();
        BAR();
        __builtin_amdgcn_raw_buffer_store_b64(v2u{(unsigned)tag | my_xcc(), (unsigned)(tag >> 32)}, rs, (G_ROT + threadIdx.x) * 128, 0, 16);
    }
    unsigned* const srot = reinterpret_cast<unsigned*>(smem + CH_IMG);
    if (threadIdx.x == 0) {
        unsigned rot = 8;
#pragma unroll 1
        for (unsigned spins = 0; spins < SPIN_CAP; ++spins) {
            const v2u v = __builtin_amdgcn_raw_buffer_load_b64(rs, (G_ROT + blockIdx.x) * 128, 0, 16);
            if ((pair(v[0], v[1]) & ~7ull) == tag) { rot = v[0] & 7; break; }
            asm volatile("" ::: "memory");
        }
        *srot = rot;
    }
    BAR();
    const unsigned rot = rfl(*srot);
    if (rot == 8) return;
    BAR();
    const unsigned xb = (blockIdx.x + rot) & 7;
    const int x4 = xb >> 2, half = x4, pr = role_tab[(xb & 3) * 32 + (blockIdx.x >> 3)], k = pr >> 2, grp = pr & 3, x = x4 * 4 + grp;
    if (half == 1 && k < 28 && !(p.tune2 >> 60)) {
        const unsigned first = (pr * 4 + rfl(threadIdx.x >> 6)) * 1024;
        warm(p.x, 2359296, first); warm(p.Wq, 4718592, first); warm(p.Wk, 4718592, first); warm(p.Wv, 4718592, first); warm(p.Wo, 4718592, first);
        warm(p.w1, 5308416, first); warm(p.w3, 5308416, first); warm(p.w2, 5308416, first);
        DRAIN();
        BAR();
    }
    auto dl = [&](int r) -> int { return (int)(p.tune2 >> (6 * (r + 5 * half))) & 63; };
    if (k < 8) role_chain(p, grp * 8 + k, x4 | dl(0) << 8, pr, smem);
    else if (k < 16) role_s1(p, grp * 8 + k - 8, x4, pr, dl(1));
    else if (k < 22) role_f1(p, grp, k - 16, x4, pr, dl(3));
    else if (k < 25) role_k<true>(p, grp, k - 22, x4, pr, dl(2));
    else if (k < 28) role_k<false>(p, grp, k - 25, x4, pr, dl(4));
    else if (k >= 28) {
        startup(rs, ((k - 28) * 8 + x) * 4 + rfl(threadIdx.x >> 6));
        if (k < 30) role_attn(p, grp * 8 + (k - 28) * 4, x4, pr);
        else if (k == 30) role_red<false>(p, grp, x4, pr, epoch);
        else role_red<true>(p, grp, x4, pr, epoch);
    }
}

}

extern "C" void kernel_launch(void* const* d_in, const int* in_sizes, int n_in, void* d_out, int out_size,
                              void* d_ws, size_t ws_size, hipStream_t stream) {
    if (ws_size < (size_t)WS_USED + 2 * PAGE) return;
    Params p;
    p.x = (const u16*)d_in[0]; p.mem0 = (const u16*)d_in[1];
    p.Wq = (const u16*)d_in[2]; p.Wk = (const u16*)d_in[3]; p.Wv = (const u16*)d_in[4]; p.Wo = (const u16*)d_in[5];
    p.w1 = (const u16*)d_in[8]; p.w3 = (const u16*)d_in[9]; p.w2 = (const u16*)d_in[10];
    p.out = (u16*)d_out;
    p.ws = (char*)d_ws + ((0 - (size_t)d_ws) & (PAGE - 1));
    p.tune0 = (u64)D1 | (u64)D2 << 32; p.tune1 = (u64)D3 | (u64)D4 << 32;
    p.tune2 = (u64)DL0 | (u64)DL1 << 30;
    (void)hipFuncSetAttribute((const void*)mega_kernel, hipFuncAttributeMaxDynamicSharedMemorySize, LDS_BYTES);
    mega_kernel<<<256, 256, LDS_BYTES, stream>>>(p);
}
```

```cpp
#include <hip/hip_runtime.h>
#include <stdint.h>

typedef unsigned short u16;
typedef unsigned long long u64;
typedef unsigned v4u __attribute__((ext_vector_type(4)));
typedef unsigned v2u __attribute__((ext_vector_type(2)));
typedef short bf16x8 __attribute__((ext_vector_type(8)));
typedef float f32x4 __attribute__((ext_vector_type(4)));
typedef __bf16 bf2_t __attribute__((ext_vector_type(2)));
typedef __amdgpu_buffer_rsrc_t rsrc_t;

#define XF2L 0
#define XF2S 0
#define XF2M 24
#define XFN 1
#define WARMD 4
#define RSLEEP 1
namespace {

constexpr unsigned A_K = 262144u;
constexpr unsigned A_KM = 786432u;
constexpr unsigned A_PROBE = 1044480u;
constexpr unsigned A_Q = 0u;
constexpr unsigned A_ST = 1036288u;
constexpr unsigned A_V = 524288u;
constexpr unsigned A_X1 = 999424u;
constexpr unsigned A_XN = 917504u;
constexpr unsigned A_XR = 958464u;
constexpr unsigned B_H = 0u;
constexpr unsigned B_P3 = 294912u;
constexpr unsigned B_XN1 = 442368u;
constexpr unsigned C_P5 = 0u;
constexpr unsigned DBG = 236978176u;
constexpr unsigned DBG2 = 238026752u;
constexpr unsigned EXB = 294912u;
constexpr unsigned EXL = 270336u;
constexpr unsigned F_AT = 832u;
constexpr unsigned F_CH = 704u;
constexpr unsigned F_F1 = 960u;
constexpr unsigned F_F2 = 1600u;
constexpr unsigned F_LINES = 1856u;
constexpr unsigned F_OP = 1344u;
constexpr unsigned F_R3 = 256u;
constexpr unsigned F_S1 = 448u;
constexpr unsigned F_XN = 0u;
constexpr unsigned G_ABORT = 384u;
constexpr unsigned G_DONE = 400u;
constexpr unsigned G_EPOCH = 385u;
constexpr unsigned G_LINES = 512u;
constexpr unsigned G_PG = 256u;
constexpr unsigned G_ROT = 0u;
constexpr unsigned K1 = 1024u;
constexpr unsigned LF0 = 32768u;
constexpr unsigned MEMB = 40960u;
constexpr unsigned MEML = 565248u;
constexpr unsigned NPAGES = 113u;
constexpr unsigned OOB = 2147483648u;
constexpr unsigned PAGE = 2097152u;
constexpr unsigned SPIN_CAP = 1048576u;
constexpr unsigned WS_USED = 236978176u;
constexpr unsigned CH_WB0 = 0u;
constexpr unsigned CH_WB1 = 40960u;
constexpr unsigned CH_WB2 = 81920u;
constexpr unsigned CH_WB3 = 116736u;
constexpr unsigned CH_IMG = 151552u;
constexpr unsigned CH_SO = 156672u;
constexpr unsigned CH_SELT = 160768u;
constexpr unsigned NPROBE = 6u;
constexpr unsigned NCOPY = 4u;
constexpr unsigned DL0 = 169877504u;
constexpr unsigned DL1 = 10u;
constexpr unsigned D1 = 672923692u;
constexpr unsigned D2 = 1144258560u;
constexpr unsigned D3 = 0u;
constexpr unsigned D4 = 269352u;
constexpr int LDS_BYTES = 160 * 1024;
constexpr unsigned spread(unsigned o) { return (o >> 13) * 16384u + (o & 8191u); }

__device__ const unsigned char role_tab[128] = {0, 4, 8, 12, 16, 20, 24, 28, 1, 5, 9, 13, 17, 21, 25, 29, 2, 6, 10, 14, 18, 22, 26, 30, 3, 7, 11, 15, 19, 23, 27, 31, 32, 36, 40, 44, 48, 52, 56, 60, 33, 37, 64, 68, 72, 76, 80, 84, 65, 69, 88, 92, 96, 89, 93, 97, 100, 104, 108, 112, 116, 113, 120, 124, 41, 45, 49, 53, 57, 61, 34, 38, 42, 46, 73, 77, 81, 85, 66, 70, 74, 78, 90, 94, 98, 101, 105, 109, 102, 106, 110, 117, 114, 121, 125, 122, 50, 54, 58, 62, 35, 39, 43, 47, 51, 55, 59, 63, 82, 86, 67, 71, 75, 79, 83, 87, 91, 95, 99, 103, 107, 111, 118, 115, 119, 126, 123, 127};

struct Params {
    const u16* x; const u16* mem0; const u16* Wq; const u16* Wk; const u16* Wv; const u16* Wo;
    const u16* w1; const u16* w3; const u16* w2; u16* out; char* ws;
    u64 tune0, tune1, tune2;
};

__device__ __forceinline__ v4u ld16(rsrc_t rs, unsigned voff, unsigned soff) { return __builtin_amdgcn_raw_buffer_load_b128(rs, voff, soff, 16); }
__device__ __forceinline__ int ld4(rsrc_t rs, unsigned off) { return (int)__builtin_amdgcn_raw_buffer_load_b32(rs, off, 0, 16); }
__device__ __forceinline__ void st16(rsrc_t rs, unsigned voff, unsigned soff, v4u v) { __builtin_amdgcn_raw_buffer_store_b128(v, rs, voff, soff, 16); }
__device__ __forceinline__ void st4(rsrc_t rs, unsigned off, int v) { __builtin_amdgcn_raw_buffer_store_b32((unsigned)v, rs, off, 0, 16); }
#define DRAIN() asm volatile("s_waitcnt vmcnt(0)" ::: "memory")
#define BAR() do { asm volatile("s_waitcnt lgkmcnt(0)" ::: "memory"); __builtin_amdgcn_s_barrier(); asm volatile("" ::: "memory"); } while (0)
__device__ __forceinline__ int rfl(int v) { return __builtin_amdgcn_readfirstlane(v); }
__device__ __forceinline__ u64 pair(unsigned lo, unsigned hi) { return (u64)lo | (u64)hi << 32; }
__device__ __forceinline__ unsigned my_xcc() {
    unsigned id;
    asm volatile("s_getreg_b32 %0, hwreg(HW_REG_XCC_ID)" : "=s"(id));
    return id & 7;
}

__device__ __forceinline__ void startup(rsrc_t rs, int hw) {
    const int lane = threadIdx.x & 63;
    const v4u ones = {~0u, ~0u, ~0u, ~0u};
    const unsigned o = (unsigned)(hw < 96 ? hw + 1 : (hw == 96 ? 0 : hw)) * PAGE + spread(A_PROBE);
    unsigned best[2] = {~0u, ~0u};
    if (hw < NPAGES) {
#pragma unroll 1
        for (int r = 0; r < 2 * NPROBE; ++r) {
            const unsigned t0 = (unsigned)__builtin_amdgcn_s_memtime();
            st16(rs, lane < 8 ? lane * 16 : OOB, o + (r & 1) * 8192, ones);
            DRAIN();
            const unsigned t = (unsigned)__builtin_amdgcn_s_memtime() - t0;
            if (r & 1) best[1] = min(best[1], t); else best[0] = min(best[0], t);
        }
    }
#pragma unroll
    for (int q = 0; q < 11; ++q) st16(rs, lane * 16, 2 * LF0 + (hw * 11 + q) * 1024, ones);
    static_assert(2 * LF0 + 128 * 11 * 1024 >= 2 * (MEML + 4 * MEMB) && 2 * LF0 + 128 * 11 * 1024 <= 2 * A_PROBE, "flags and exchange area");
#pragma unroll
    for (int q = 0; q < 4; ++q) st16(rs, lane * 16, (unsigned)(1 + (hw >> 1)) * PAGE + spread(A_ST + q * 1024) + (hw & 1) * 8192, ones);
    DRAIN();
    if (lane == 0) st4(rs, (G_PG + hw) * 128, (best[1] < best[0] ? 1 : 0) ^ (my_xcc() >> 2));
}

__device__ __forceinline__ void warm(const u16* W, unsigned bytes, unsigned first, unsigned step) {
    const rsrc_t r = __builtin_amdgcn_make_buffer_rsrc(const_cast<u16*>(W), 0, bytes, 0x00020000);
    const unsigned vo = (threadIdx.x & 63) * 16;
#pragma unroll 1
    for (unsigned o = first; o < bytes; o += step * 1024)
        asm volatile("s_mov_b32 m0, 0\n\ts_nop 0\n\tbuffer_load_dwordx4 %0, %1, %2 offen lds\n\ts_waitcnt vmcnt(%3)" :: "v"(vo), "s"(r), "s"(o), "n"(WARMD) : "memory");
}

__device__ __forceinline__ void role_chain(const Params& p, int h, int x4, int pr, char* smem) {
    const unsigned tid = threadIdx.x, lane = tid & 63, c = lane & 15, g = lane >> 4;
    const int w = rfl(tid >> 6);
    const unsigned wb = w == 0 ? CH_WB0 : (w == 1 ? CH_WB1 : (w == 2 ? CH_WB2 : CH_WB3));
    const unsigned slot = (g * 8 + (c & 7)) * 16;
    const unsigned spr = CH_IMG + (c >> 3) * 512 + slot;
    const unsigned gam = lane >> 4, nh = (lane >> 3) & 1, ksl = (lane >> 2) & 1, tau = ((lane ^ gam) >> 1) & 1, rho = lane & 1;
    auto img = [&](unsigned r) __attribute__((always_inline)) -> unsigned { return (2 * (r & 3) + (g & 1)) * 128 + (((g ^ r) & 2) + (r >> 2)) * 16; };
    if (w >= 2) {
        v4u sel;
#pragma unroll
        for (int e = 0; e < 4; ++e) sel[e] = ((c & 7) == 2 * e) ? 0x3f80u : (((c & 7) == 2 * e + 1) ? 0x3f800000u : 0u);
        const unsigned g3 = ksl * 512 + (2 * (gam & 1) + nh) * 128 + (4 * (gam >> 1) + 2 * tau + rho) * 16;
        *reinterpret_cast<v4u*>(smem + wb + 32768 + lane * 16) = sel;
        *reinterpret_cast<v4u*>(smem + wb + 32768 + 1024 + lane * 16) =
            v4u{lane < 8 * NCOPY ? (lane >> 3) * 1024 + (lane & 7) * 16 : OOB, g3, lane < 8 * NCOPY ? (lane >> 3) * 16384 + ((lane & 7) ^ ((h >> 1) & 2)) * 16 : OOB, 0u};
    } else if (w == 0) {
        const bool onq = g == (c & 3), upq = (c & 4) != 0, onk = g == (c >> 2) && (c & 3) < 2, upk = (c & 1) != 0;
        *reinterpret_cast<v4u*>(smem + spr + CH_SELT - CH_IMG) = v4u{onq ? (upq ? 0x3f800000u : 0x3f80u) : 0u, onq ? (upq ? 0x3f80u : 0x3f800000u) : 0u,
                                                                     onk ? (upk ? 0x3f800000u : 0x3f80u) : 0u, onk ? (upk ? 0x3f80u : 0x3f800000u) : 0u};
    } else {
        const unsigned pub = (lane >> 4) * 16384 + ((lane >> 3) & 1) * 4096 + (lane & 7) * 16;
        const unsigned kmo = (c & 3) < 2 ? (g * 8 + 2 * (c >> 2) + (c & 3)) * 16 : OOB;
        const unsigned vmo = (g & 1) ? OOB : (g >> 1) * 256 + c * 16;
        *reinterpret_cast<v4u*>(smem + spr + CH_SELT - CH_IMG + 1024) = v4u{kmo, vmo, pub, CH_SO + img(c & 7) + (c >> 3) * 8};
    }
    if (w == 0) *reinterpret_cast<unsigned*>(smem + spr + CH_SELT - CH_IMG + 2048) = ~0u;
    BAR();
    const unsigned idx = (lane & 2) ? 0xeeeeu : 0x4444u;
    const unsigned va = CH_IMG + img((c >> 2) + 4 * ((((c & 3) + 1) >> 1) & 1));
    const unsigned pp = lane >> 2, row = (pp & 9) | ((pp & 2) << 1) | ((pp & 4) >> 1);
    auto qo = [&](unsigned hb) __attribute__((always_inline)) -> unsigned { return (16 * w + g) * 8192 + 256 * h + (c ^ (2 * hb + 4 * (g & 1) + 8 * (w >> 1))) * 16; };
    const unsigned o2 = (128 * h + 16 * (lane >> 5) + ((lane >> 3) & 3)) * 1152 + w * 128 + ((lane & 7) ^ (4 * (lane >> 5))) * 16;
    const unsigned oo = (128 * h + 16 * ((row >> 2) & 1) + 4 * (row >> 3) + (row & 3)) * 1152 + w * 128 + (lane & 3) * 16;
    const unsigned q = c >> 2;
    const unsigned tr = wb + ((q & 1) + 2 * (g & 1) + 8 * (g >> 1)) * 64 + (lane & 3) * 8;
    auto to = [&](unsigned t) __attribute__((always_inline)) -> unsigned { return wb + (4 * g + (q & 1)) * 128 + ((t ^ (g & 1)) * 64) + (q >> 1) * 32 + (lane & 3) * 8; };
    auto tq = [&](unsigned t, unsigned a) __attribute__((always_inline)) -> unsigned {
        const unsigned part = 2 * (q >> 1) + a;
        return (part == 3 ? CH_WB3 : part * CH_WB1) + (4 * g + (q & 1)) * 256 + (((4 * w + 2 * t) ^ (2 * (g & 1) + 4 * (q & 1) + 8 * (q >> 1))) * 16) + (lane & 3) * 8;
    };
    const unsigned m0 = CH_IMG + (gam + 4 * rho) * 1152 + (4 * ksl + 2 * tau + nh) * 16 + (w == 3 ? 2 : w) * 384;
    asm volatile(
        "s_nop 4\n"
        "s_mov_b64 s[40:41], %0\n"
        "s_and_b32 s41, s41, 0xffff\n"
        "s_mov_b32 s42, 236978176\n"
        "s_mov_b32 s43, 0x20000\n"
        "s_mov_b64 s[48:49], %6\n"
        "s_mov_b64 s[56:57], %7\n"
        "s_mov_b64 s[60:61], %8\n"
        "s_mov_b64 s[62:63], %9\n"
        "v_mov_b64 v[0:1], %10\n"
        "v_mov_b64 v[2:3], %11\n"
        "v_mov_b64 v[4:5], %12\n"
        "v_mov_b64 v[6:7], %13\n"
        "v_mov_b64 v[8:9], %14\n"
        "v_mov_b64 v[10:11], %15\n"
        "v_mov_b64 v[12:13], %16\n"
        "v_mov_b64 v[14:15], %17\n"
        "v_mov_b64 v[16:17], %18\n"
        "s_mov_b32 s47, 0x20000\n"
        "v_readfirstlane_b32 s82, v2\n"
        "s_lshr_b32 s65, s56, 8\n"
        "s_and_b32 s56, s56, 1\n"
        ".Lch_start_%=:\n"
        "s_sub_u32 s65, s65, 1\n"
        "s_cbranch_scc1 .Lch_start2_%=\n"
        "s_sleep 16\n"
        "s_branch .Lch_start_%=\n"
        ".Lch_start2_%=:\n"
        "s_mov_b64 s[44:45], %1\n"
        "s_and_b32 s45, s45, 0xffff\n"
        "s_mov_b32 s46, 4718592\n"
        "s_add_u32 m0, s82, 0\n"
        "s_mov_b32 s65, 0\n"
        "buffer_load_dwordx4 v6, s[44:47], s65 offen offset:0 lds\n"
        "s_mov_b32 s65, 31744\n"
        "buffer_load_dwordx4 v7, s[44:47], s65 offen offset:1024 lds\n"
        "s_mov_b32 s65, 63488\n"
        "buffer_load_dwordx4 v6, s[44:47], s65 offen offset:2048 lds\n"
        "s_mov_b32 s65, 95232\n"
        "buffer_load_dwordx4 v7, s[44:47], s65 offen offset:3072 lds\n"
        "s_add_u32 m0, s82, 4096\n"
        "s_mov_b32 s65, 524288\n"
        "buffer_load_dwordx4 v6, s[44:47], s65 offen offset:0 lds\n"
        "s_mov_b32 s65, 556032\n"
        "buffer_load_dwordx4 v7, s[44:47], s65 offen offset:1024 lds\n"
        "s_mov_b32 s65, 587776\n"
        "buffer_load_dwordx4 v6, s[44:47], s65 offen offset:2048 lds\n"
        "s_mov_b32 s65, 619520\n"
        "buffer_load_dwordx4 v7, s[44:47], s65 offen offset:3072 lds\n"
        "s_add_u32 m0, s82, 8192\n"
        "s_mov_b32 s65, 1048576\n"
        "buffer_load_dwordx4 v6, s[44:47], s65 offen offset:0 lds\n"
        "s_mov_b32 s65, 1080320\n"
        "buffer_load_dwordx4 v7, s[44:47], s65 offen offset:1024 lds\n"
        "s_mov_b32 s65, 1112064\n"
        "buffer_load_dwordx4 v6, s[44:47], s65 offen offset:2048 lds\n"
        "s_mov_b32 s65, 1143808\n"
        "buffer_load_dwordx4 v7, s[44:47], s65 offen offset:3072 lds\n"
        "s_waitcnt vmcnt(8)\n"
        "s_waitcnt lgkmcnt(0)\n"
        "s_barrier\n"
        "ds_read_b64_tr_b16 a[0:1], v14 offset:0\n"
        "ds_read_b64_tr_b16 a[2:3], v14 offset:512\n"
        "ds_read_b64_tr_b16 a[4:5], v15 offset:0\n"
        "ds_read_b64_tr_b16 a[6:7], v15 offset:512\n"
        "ds_read_b64_tr_b16 a[8:9], v16 offset:0\n"
        "ds_read_b64_tr_b16 a[10:11], v16 offset:512\n"
        "ds_read_b64_tr_b16 a[12:13], v17 offset:0\n"
        "ds_read_b64_tr_b16 a[14:15], v17 offset:512\n"
        "s_waitcnt vmcnt(4)\n"
        "s_waitcnt lgkmcnt(0)\n"
        "s_barrier\n"
        "s_add_u32 m0, s82, 12288\n"
        "s_mov_b32 s65, 1572864\n"
        "buffer_load_dwordx4 v6, s[44:47], s65 offen offset:0 lds\n"
        "s_mov_b32 s65, 1604608\n"
        "buffer_load_dwordx4 v7, s[44:47], s65 offen offset:1024 lds\n"
        "s_mov_b32 s65, 1636352\n"
        "buffer_load_dwordx4 v6, s[44:47], s65 offen offset:2048 lds\n"
        "s_mov_b32 s65, 1668096\n"
        "buffer_load_dwordx4 v7, s[44:47], s65 offen offset:3072 lds\n"
        "ds_read_b64_tr_b16 a[16:17], v14 offset:4096\n"
        "ds_read_b64_tr_b16 a[18:19], v14 offset:4608\n"
        "ds_read_b64_tr_b16 a[20:21], v15 offset:4096\n"
        "ds_read_b64_tr_b16 a[22:23], v15 offset:4608\n"
        "ds_read_b64_tr_b16 a[24:25], v16 offset:4096\n"
        "ds_read_b64_tr_b16 a[26:27], v16 offset:4608\n"
        "ds_read_b64_tr_b16 a[28:29], v17 offset:4096\n"
        "ds_read_b64_tr_b16 a[30:31], v17 offset:4608\n"
        "s_waitcnt vmcnt(4)\n"
        "s_waitcnt lgkmcnt(0)\n"
        "s_barrier\n"
        "s_add_u32 m0, s82, 16384\n"
        "s_mov_b32 s65, 2097152\n"
        "buffer_load_dwordx4 v6, s[44:47], s65 offen offset:0 lds\n"
        "s_mov_b32 s65, 2128896\n"
        "buffer_load_dwordx4 v7, s[44:47], s65 offen offset:1024 lds\n"
        "s_mov_b32 s65, 2160640\n"
        "buffer_load_dwordx4 v6, s[44:47], s65 offen offset:2048 lds\n"
        "s_mov_b32 s65, 2192384\n"
        "buffer_load_dwordx4 v7, s[44:47], s65 offen offset:3072 lds\n"
        "s_mov_b64 s[44:45], %5\n"
        "s_and_b32 s45, s45, 0xffff\n"
        "s_mov_b32 s46, 9216\n"
        "s_mul_i32 s65, s48, 3072\n"
        "s_add_u32 m0, s65, 151552\n"
        "s_cmp_eq_u32 s48, 3\n"
        "s_cbranch_scc1 .Lch_nom_%=\n"
        "buffer_load_dwordx4 v3, s[44:47], s65 offen offset:0 lds\n"
        "buffer_load_dwordx4 v3, s[44:47], s65 offen offset:1024 lds\n"
        "buffer_load_dwordx4 v3, s[44:47], s65 offen offset:2048 lds\n"
        ".Lch_nom_%=:\n"
        "ds_read_b64_tr_b16 a[32:33], v14 offset:8192\n"
        "ds_read_b64_tr_b16 a[34:35], v14 offset:8704\n"
        "ds_read_b64_tr_b16 a[36:37], v15 offset:8192\n"
        "ds_read_b64_tr_b16 a[38:39], v15 offset:8704\n"
        "ds_read_b64_tr_b16 a[40:41], v16 offset:8192\n"
        "ds_read_b64_tr_b16 a[42:43], v16 offset:8704\n"
        "ds_read_b64_tr_b16 a[44:45], v17 offset:8192\n"
        "ds_read_b64_tr_b16 a[46:47], v17 offset:8704\n"
        "s_waitcnt vmcnt(4)\n"
        "s_waitcnt lgkmcnt(0)\n"
        "s_barrier\n"
        "s_mov_b64 s[44:45], %1\n"
        "s_and_b32 s45, s45, 0xffff\n"
        "s_mov_b32 s46, 4718592\n"
        "s_add_u32 m0, s82, 20480\n"
        "s_mov_b32 s65, 2621440\n"
        "buffer_load_dwordx4 v6, s[44:47], s65 offen offset:0 lds\n"
        "s_mov_b32 s65, 2653184\n"
        "buffer_load_dwordx4 v7, s[44:47], s65 offen offset:1024 lds\n"
        "s_mov_b32 s65, 2684928\n"
        "buffer_load_dwordx4 v6, s[44:47], s65 offen offset:2048 lds\n"
        "s_mov_b32 s65, 2716672\n"
        "buffer_load_dwordx4 v7, s[44:47], s65 offen offset:3072 lds\n"
        "ds_read_b64_tr_b16 a[48:49], v14 offset:12288\n"
        "ds_read_b64_tr_b16 a[50:51], v14 offset:12800\n"
        "ds_read_b64_tr_b16 a[52:53], v15 offset:12288\n"
        "ds_read_b64_tr_b16 a[54:55], v15 offset:12800\n"
        "ds_read_b64_tr_b16 a[56:57], v16 offset:12288\n"
        "ds_read_b64_tr_b16 a[58:59], v16 offset:12800\n"
        "ds_read_b64_tr_b16 a[60:61], v17 offset:12288\n"
        "ds_read_b64_tr_b16 a[62:63], v17 offset:12800\n"
        "s_waitcnt vmcnt(4)\n"
        "s_waitcnt lgkmcnt(0)\n"
        "s_barrier\n"
        "s_add_u32 m0, s82, 24576\n"
        "s_mov_b32 s65, 3145728\n"
        "buffer_load_dwordx4 v6, s[44:47], s65 offen offset:0 lds\n"
        "s_mov_b32 s65, 3177472\n"
        "buffer_load_dwordx4 v7, s[44:47], s65 offen offset:1024 lds\n"
        "s_mov_b32 s65, 3209216\n"
        "buffer_load_dwordx4 v6, s[44:47], s65 offen offset:2048 lds\n"
        "s_mov_b32 s65, 3240960\n"
        "buffer_load_dwordx4 v7, s[44:47], s65 offen offset:3072 lds\n"
        "s_getpc_b64 s[44:45]\n"
        ".Lch_pc_%=:\n"
        "s_add_u32 s44, s44, .Lch_code_%=-.Lch_pc_%=\n"
        "s_addc_u32 s45, s45, 0\n"
        "s_and_b32 s45, s45, 0xffff\n"
        "s_and_b32 s44, s44, -4\n"
        "s_mov_b32 s46, .Lch_end_%=-.Lch_code_%=\n"
        "s_mov_b32 s47, 0x20000\n"
        "s_lshl_b32 s65, s48, 10\n"
        "buffer_load_dwordx4 v[64:67], v3, s[44:47], s65 offen\n"
        "s_add_u32 s65, s65, 4096\n"
        "buffer_load_dwordx4 v[68:71], v3, s[44:47], s65 offen\n"
        "s_add_u32 s65, s65, 4096\n"
        "ds_read_b64_tr_b16 a[64:65], v14 offset:16384\n"
        "ds_read_b64_tr_b16 a[66:67], v14 offset:16896\n"
        "ds_read_b64_tr_b16 a[68:69], v15 offset:16384\n"
        "ds_read_b64_tr_b16 a[70:71], v15 offset:16896\n"
        "ds_read_b64_tr_b16 a[72:73], v16 offset:16384\n"
        "ds_read_b64_tr_b16 a[74:75], v16 offset:16896\n"
        "ds_read_b64_tr_b16 a[76:77], v17 offset:16384\n"
        "ds_read_b64_tr_b16 a[78:79], v17 offset:16896\n"
        "s_waitcnt vmcnt(6)\n"
        "s_waitcnt lgkmcnt(0)\n"
        "s_barrier\n"
        "s_mov_b64 s[44:45], %1\n"
        "s_and_b32 s45, s45, 0xffff\n"
        "s_mov_b32 s46, 4718592\n"
        "s_add_u32 m0, s82, 28672\n"
        "s_mov_b32 s65, 3670016\n"
        "buffer_load_dwordx4 v6, s[44:47], s65 offen offset:0 lds\n"
        "s_mov_b32 s65, 3701760\n"
        "buffer_load_dwordx4 v7, s[44:47], s65 offen offset:1024 lds\n"
        "s_mov_b32 s65, 3733504\n"
        "buffer_load_dwordx4 v6, s[44:47], s65 offen offset:2048 lds\n"
        "s_mov_b32 s65, 3765248\n"
        "buffer_load_dwordx4 v7, s[44:47], s65 offen offset:3072 lds\n"
        "ds_read_b64_tr_b16 a[80:81], v14 offset:20480\n"
        "ds_read_b64_tr_b16 a[82:83], v14 offset:20992\n"
        "ds_read_b64_tr_b16 a[84:85], v15 offset:20480\n"
        "ds_read_b64_tr_b16 a[86:87], v15 offset:20992\n"
        "ds_read_b64_tr_b16 a[88:89], v16 offset:20480\n"
        "ds_read_b64_tr_b16 a[90:91], v16 offset:20992\n"
        "ds_read_b64_tr_b16 a[92:93], v17 offset:20480\n"
        "ds_read_b64_tr_b16 a[94:95], v17 offset:20992\n"
        "s_waitcnt vmcnt(6)\n"
        "s_waitcnt lgkmcnt(0)\n"
        "s_barrier\n"
        "s_add_u32 m0, s82, 0\n"
        "s_mov_b32 s65, 4194304\n"
        "buffer_load_dwordx4 v6, s[44:47], s65 offen offset:0 lds\n"
        "s_mov_b32 s65, 4226048\n"
        "buffer_load_dwordx4 v7, s[44:47], s65 offen offset:1024 lds\n"
        "s_mov_b32 s65, 4257792\n"
        "buffer_load_dwordx4 v6, s[44:47], s65 offen offset:2048 lds\n"
        "s_mov_b32 s65, 4289536\n"
        "buffer_load_dwordx4 v7, s[44:47], s65 offen offset:3072 lds\n"
        "ds_read_b64_tr_b16 a[96:97], v14 offset:24576\n"
        "ds_read_b64_tr_b16 a[98:99], v14 offset:25088\n"
        "ds_read_b64_tr_b16 a[100:101], v15 offset:24576\n"
        "ds_read_b64_tr_b16 a[102:103], v15 offset:25088\n"
        "ds_read_b64_tr_b16 a[104:105], v16 offset:24576\n"
        "ds_read_b64_tr_b16 a[106:107], v16 offset:25088\n"
        "ds_read_b64_tr_b16 a[108:109], v17 offset:24576\n"
        "ds_read_b64_tr_b16 a[110:111], v17 offset:25088\n"
        "s_waitcnt vmcnt(4)\n"
        "s_waitcnt lgkmcnt(0)\n"
        "s_barrier\n"
        "s_mov_b64 s[44:45], %2\n"
        "s_and_b32 s45, s45, 0xffff\n"
        "s_mov_b32 s46, 4718592\n"
        "s_add_u32 m0, s82, 4096\n"
        "s_mov_b32 s65, 0\n"
        "buffer_load_dwordx4 v6, s[44:47], s65 offen offset:0 lds\n"
        "s_mov_b32 s65, 31744\n"
        "buffer_load_dwordx4 v7, s[44:47], s65 offen offset:1024 lds\n"
        "s_mov_b32 s65, 63488\n"
        "buffer_load_dwordx4 v6, s[44:47], s65 offen offset:2048 lds\n"
        "s_mov_b32 s65, 95232\n"
        "buffer_load_dwordx4 v7, s[44:47], s65 offen offset:3072 lds\n"
        "ds_read_b64_tr_b16 a[112:113], v14 offset:28672\n"
        "ds_read_b64_tr_b16 a[114:115], v14 offset:29184\n"
        "ds_read_b64_tr_b16 a[116:117], v15 offset:28672\n"
        "ds_read_b64_tr_b16 a[118:119], v15 offset:29184\n"
        "ds_read_b64_tr_b16 a[120:121], v16 offset:28672\n"
        "ds_read_b64_tr_b16 a[122:123], v16 offset:29184\n"
        "ds_read_b64_tr_b16 a[124:125], v17 offset:28672\n"
        "ds_read_b64_tr_b16 a[126:127], v17 offset:29184\n"
        "s_waitcnt vmcnt(4)\n"
        "s_waitcnt lgkmcnt(0)\n"
        "s_barrier\n"
        "s_add_u32 m0, s82, 8192\n"
        "s_mov_b32 s65, 524288\n"
        "buffer_load_dwordx4 v6, s[44:47], s65 offen offset:0 lds\n"
        "s_mov_b32 s65, 556032\n"
        "buffer_load_dwordx4 v7, s[44:47], s65 offen offset:1024 lds\n"
        "s_mov_b32 s65, 587776\n"
        "buffer_load_dwordx4 v6, s[44:47], s65 offen offset:2048 lds\n"
        "s_mov_b32 s65, 619520\n"
        "buffer_load_dwordx4 v7, s[44:47], s65 offen offset:3072 lds\n"
        "ds_read_b64_tr_b16 a[128:129], v14 offset:0\n"
        "ds_read_b64_tr_b16 a[130:131], v14 offset:512\n"
        "ds_read_b64_tr_b16 a[132:133], v15 offset:0\n"
        "ds_read_b64_tr_b16 a[134:135], v15 offset:512\n"
        "ds_read_b64_tr_b16 a[136:137], v16 offset:0\n"
        "ds_read_b64_tr_b16 a[138:139], v16 offset:512\n"
        "ds_read_b64_tr_b16 a[140:141], v17 offset:0\n"
        "ds_read_b64_tr_b16 a[142:143], v17 offset:512\n"
        "s_waitcnt vmcnt(4)\n"
        "s_waitcnt lgkmcnt(0)\n"
        "s_barrier\n"
        "s_add_u32 m0, s82, 12288\n"
        "s_mov_b32 s65, 1048576\n"
        "buffer_load_dwordx4 v6, s[44:47], s65 offen offset:0 lds\n"
        "s_mov_b32 s65, 1080320\n"
        "buffer_load_dwordx4 v7, s[44:47], s65 offen offset:1024 lds\n"
        "s_mov_b32 s65, 1112064\n"
        "buffer_load_dwordx4 v6, s[44:47], s65 offen offset:2048 lds\n"
        "s_mov_b32 s65, 1143808\n"
        "buffer_load_dwordx4 v7, s[44:47], s65 offen offset:3072 lds\n"
        "ds_read_b64_tr_b16 a[144:145], v14 offset:4096\n"
        "ds_read_b64_tr_b16 a[146:147], v14 offset:4608\n"
        "ds_read_b64_tr_b16 a[148:149], v15 offset:4096\n"
        "ds_read_b64_tr_b16 a[150:151], v15 offset:4608\n"
        "ds_read_b64_tr_b16 a[152:153], v16 offset:4096\n"
        "ds_read_b64_tr_b16 a[154:155], v16 offset:4608\n"
        "ds_read_b64_tr_b16 a[156:157], v17 offset:4096\n"
        "ds_read_b64_tr_b16 a[158:159], v17 offset:4608\n"
        "s_waitcnt vmcnt(4)\n"
        "s_waitcnt lgkmcnt(0)\n"
        "s_barrier\n"
        "s_add_u32 m0, s82, 16384\n"
        "s_mov_b32 s65, 1572864\n"
        "buffer_load_dwordx4 v6, s[44:47], s65 offen offset:0 lds\n"
        "s_mov_b32 s65, 1604608\n"
        "buffer_load_dwordx4 v7, s[44:47], s65 offen offset:1024 lds\n"
        "s_mov_b32 s65, 1636352\n"
        "buffer_load_dwordx4 v6, s[44:47], s65 offen offset:2048 lds\n"
        "s_mov_b32 s65, 1668096\n"
        "buffer_load_dwordx4 v7, s[44:47], s65 offen offset:3072 lds\n"
        "ds_read_b64_tr_b16 a[160:161], v14 offset:8192\n"
        "ds_read_b64_tr_b16 a[162:163], v14 offset:8704\n"
        "ds_read_b64_tr_b16 a[164:165], v15 offset:8192\n"
        "ds_read_b64_tr_b16 a[166:167], v15 offset:8704\n"
        "ds_read_b64_tr_b16 a[168:169], v16 offset:8192\n"
        "ds_read_b64_tr_b16 a[170:171], v16 offset:8704\n"
        "ds_read_b64_tr_b16 a[172:173], v17 offset:8192\n"
        "ds_read_b64_tr_b16 a[174:175], v17 offset:8704\n"
        "s_waitcnt vmcnt(4)\n"
        "s_waitcnt lgkmcnt(0)\n"
        "s_barrier\n"
        "s_add_u32 m0, s82, 20480\n"
        "s_mov_b32 s65, 2097152\n"
        "buffer_load_dwordx4 v6, s[44:47], s65 offen offset:0 lds\n"
        "s_mov_b32 s65, 2128896\n"
        "buffer_load_dwordx4 v7, s[44:47], s65 offen offset:1024 lds\n"
        "s_mov_b32 s65, 2160640\n"
        "buffer_load_dwordx4 v6, s[44:47], s65 offen offset:2048 lds\n"
        "s_mov_b32 s65, 2192384\n"
        "buffer_load_dwordx4 v7, s[44:47], s65 offen offset:3072 lds\n"
        "ds_read_b64_tr_b16 a[176:177], v14 offset:12288\n"
        "ds_read_b64_tr_b16 a[178:179], v14 offset:12800\n"
        "ds_read_b64_tr_b16 a[180:181], v15 offset:12288\n"
        "ds_read_b64_tr_b16 a[182:183], v15 offset:12800\n"
        "ds_read_b64_tr_b16 a[184:185], v16 offset:12288\n"
        "ds_read_b64_tr_b16 a[186:187], v16 offset:12800\n"
        "ds_read_b64_tr_b16 a[188:189], v17 offset:12288\n"
        "ds_read_b64_tr_b16 a[190:191], v17 offset:12800\n"
        "s_waitcnt vmcnt(4)\n"
        "s_waitcnt lgkmcnt(0)\n"
        "s_barrier\n"
        "s_add_u32 m0, s82, 24576\n"
        "s_mov_b32 s65, 2621440\n"
        "buffer_load_dwordx4 v6, s[44:47], s65 offen offset:0 lds\n"
        "s_mov_b32 s65, 2653184\n"
        "buffer_load_dwordx4 v7, s[44:47], s65 offen offset:1024 lds\n"
        "s_mov_b32 s65, 2684928\n"
        "buffer_load_dwordx4 v6, s[44:47], s65 offen offset:2048 lds\n"
        "s_mov_b32 s65, 2716672\n"
        "buffer_load_dwordx4 v7, s[44:47], s65 offen offset:3072 lds\n"
        "ds_read_b128 v[24:27], v11 offset:0\n"
        "ds_read_b128 v[28:31], v11 offset:128\n"
        "ds_read_b128 v[32:35], v11 offset:256\n"
        "s_min_u32 s65, s48, 2\n"
        "s_mul_i32 s65, s65, 3072\n"
        "s_add_u32 s65, s65, 151552\n"
        "v_add_u32 v36, s65, v3\n"
        "ds_read_b64_tr_b16 a[192:193], v14 offset:16384\n"
        "ds_read_b64_tr_b16 a[194:195], v14 offset:16896\n"
        "ds_read_b64_tr_b16 a[196:197], v15 offset:16384\n"
        "ds_read_b64_tr_b16 a[198:199], v15 offset:16896\n"
        "ds_read_b64_tr_b16 a[200:201], v16 offset:16384\n"
        "ds_read_b64_tr_b16 a[202:203], v16 offset:16896\n"
        "ds_read_b64_tr_b16 a[204:205], v17 offset:16384\n"
        "ds_read_b64_tr_b16 a[206:207], v17 offset:16896\n"
        "s_waitcnt vmcnt(4)\n"
        "s_waitcnt lgkmcnt(0)\n"
        "s_barrier\n"
        "s_add_u32 m0, s82, 28672\n"
        "s_mov_b32 s65, 3145728\n"
        "buffer_load_dwordx4 v6, s[44:47], s65 offen offset:0 lds\n"
        "s_mov_b32 s65, 3177472\n"
        "buffer_load_dwordx4 v7, s[44:47], s65 offen offset:1024 lds\n"
        "s_mov_b32 s65, 3209216\n"
        "buffer_load_dwordx4 v6, s[44:47], s65 offen offset:2048 lds\n"
        "s_mov_b32 s65, 3240960\n"
        "buffer_load_dwordx4 v7, s[44:47], s65 offen offset:3072 lds\n"
        "ds_write_b128 v36, v[24:27] offset:0\n"
        "ds_write_b128 v36, v[28:31] offset:1024\n"
        "ds_write_b128 v36, v[32:35] offset:2048\n"
        "ds_read_b64_tr_b16 a[208:209], v14 offset:20480\n"
        "ds_read_b64_tr_b16 a[210:211], v14 offset:20992\n"
        "ds_read_b64_tr_b16 a[212:213], v15 offset:20480\n"
        "ds_read_b64_tr_b16 a[214:215], v15 offset:20992\n"
        "ds_read_b64_tr_b16 a[216:217], v16 offset:20480\n"
        "ds_read_b64_tr_b16 a[218:219], v16 offset:20992\n"
        "ds_read_b64_tr_b16 a[220:221], v17 offset:20480\n"
        "ds_read_b64_tr_b16 a[222:223], v17 offset:20992\n"
        "s_waitcnt vmcnt(4)\n"
        "s_waitcnt lgkmcnt(0)\n"
        "s_barrier\n"
        "s_add_u32 m0, s82, 0\n"
        "s_mov_b32 s65, 3670016\n"
        "buffer_load_dwordx4 v6, s[44:47], s65 offen offset:0 lds\n"
        "s_mov_b32 s65, 3701760\n"
        "buffer_load_dwordx4 v7, s[44:47], s65 offen offset:1024 lds\n"
        "s_mov_b32 s65, 3733504\n"
        "buffer_load_dwordx4 v6, s[44:47], s65 offen offset:2048 lds\n"
        "s_mov_b32 s65, 3765248\n"
        "buffer_load_dwordx4 v7, s[44:47], s65 offen offset:3072 lds\n"
        "ds_read_b64_tr_b16 a[224:225], v14 offset:24576\n"
        "ds_read_b64_tr_b16 a[226:227], v14 offset:25088\n"
        "ds_read_b64_tr_b16 a[228:229], v15 offset:24576\n"
        "ds_read_b64_tr_b16 a[230:231], v15 offset:25088\n"
        "ds_read_b64_tr_b16 a[232:233], v16 offset:24576\n"
        "ds_read_b64_tr_b16 a[234:235], v16 offset:25088\n"
        "ds_read_b64_tr_b16 a[236:237], v17 offset:24576\n"
        "ds_read_b64_tr_b16 a[238:239], v17 offset:25088\n"
        "s_waitcnt vmcnt(4)\n"
        "s_waitcnt lgkmcnt(0)\n"
        "s_barrier\n"
        "s_add_u32 m0, s82, 4096\n"
        "s_mov_b32 s65, 4194304\n"
        "buffer_load_dwordx4 v6, s[44:47], s65 offen offset:0 lds\n"
        "s_mov_b32 s65, 4226048\n"
        "buffer_load_dwordx4 v7, s[44:47], s65 offen offset:1024 lds\n"
        "s_mov_b32 s65, 4257792\n"
        "buffer_load_dwordx4 v6, s[44:47], s65 offen offset:2048 lds\n"
        "s_mov_b32 s65, 4289536\n"
        "buffer_load_dwordx4 v7, s[44:47], s65 offen offset:3072 lds\n"
        "ds_read_b64_tr_b16 a[240:241], v14 offset:28672\n"
        "ds_read_b64_tr_b16 a[242:243], v14 offset:29184\n"
        "ds_read_b64_tr_b16 a[244:245], v15 offset:28672\n"
        "ds_read_b64_tr_b16 a[246:247], v15 offset:29184\n"
        "ds_read_b64_tr_b16 a[248:249], v16 offset:28672\n"
        "ds_read_b64_tr_b16 a[250:251], v16 offset:29184\n"
        "ds_read_b64_tr_b16 a[252:253], v17 offset:28672\n"
        "ds_read_b64_tr_b16 a[254:255], v17 offset:29184\n"
        "s_waitcnt vmcnt(4)\n"
        "s_waitcnt lgkmcnt(0)\n"
        "s_barrier\n"
        "s_mov_b64 s[44:45], %4\n"
        "s_and_b32 s45, s45, 0xffff\n"
        "s_mov_b32 s46, 4718592\n"
        "s_add_u32 m0, s82, 8192\n"
        "s_mov_b32 s65, 0\n"
        "buffer_load_dwordx4 v9, s[44:47], s65 offen offset:0 lds\n"
        "s_mov_b32 s65, 3584\n"
        "buffer_load_dwordx4 v9, s[44:47], s65 offen offset:1024 lds\n"
        "s_mov_b32 s65, 7168\n"
        "buffer_load_dwordx4 v9, s[44:47], s65 offen offset:2048 lds\n"
        "s_mov_b32 s65, 10752\n"
        "buffer_load_dwordx4 v9, s[44:47], s65 offen offset:3072 lds\n"
        "ds_read_b64_tr_b16 v[80:81], v14 offset:0\n"
        "ds_read_b64_tr_b16 v[82:83], v14 offset:512\n"
        "ds_read_b64_tr_b16 v[84:85], v15 offset:0\n"
        "ds_read_b64_tr_b16 v[86:87], v15 offset:512\n"
        "ds_read_b64_tr_b16 v[88:89], v16 offset:0\n"
        "ds_read_b64_tr_b16 v[90:91], v16 offset:512\n"
        "ds_read_b64_tr_b16 v[92:93], v17 offset:0\n"
        "ds_read_b64_tr_b16 v[94:95], v17 offset:512\n"
        "s_waitcnt vmcnt(4)\n"
        "s_waitcnt lgkmcnt(0)\n"
        "s_barrier\n"
        "s_add_u32 m0, s82, 12288\n"
        "s_mov_b32 s65, 36864\n"
        "buffer_load_dwordx4 v9, s[44:47], s65 offen offset:0 lds\n"
        "s_mov_b32 s65, 40448\n"
        "buffer_load_dwordx4 v9, s[44:47], s65 offen offset:1024 lds\n"
        "s_mov_b32 s65, 44032\n"
        "buffer_load_dwordx4 v9, s[44:47], s65 offen offset:2048 lds\n"
        "s_mov_b32 s65, 47616\n"
        "buffer_load_dwordx4 v9, s[44:47], s65 offen offset:3072 lds\n"
        "ds_read_b64_tr_b16 v[96:97], v14 offset:4096\n"
        "ds_read_b64_tr_b16 v[98:99], v14 offset:4608\n"
        "ds_read_b64_tr_b16 v[100:101], v15 offset:4096\n"
        "ds_read_b64_tr_b16 v[102:103], v15 offset:4608\n"
        "ds_read_b64_tr_b16 v[104:105], v16 offset:4096\n"
        "ds_read_b64_tr_b16 v[106:107], v16 offset:4608\n"
        "ds_read_b64_tr_b16 v[108:109], v17 offset:4096\n"
        "ds_read_b64_tr_b16 v[110:111], v17 offset:4608\n"
        "s_cmp_lt_u32 s48, 2\n"
        "s_cbranch_scc0 .Lch_noex_%=\n"
        "s_lshl_b32 s66, s48, 6\n"
        "s_add_u32 m0, s82, 32768\n"
        "s_sub_u32 s65, 1024, s66\n"
        "buffer_load_dwordx4 v8, s[44:47], s65 offen lds\n"
        "s_sub_u32 s65, 9216, s66\n"
        "buffer_load_dwordx4 v8, s[44:47], s65 offen offset:1024 lds\n"
        "s_add_u32 m0, s82, 34816\n"
        "s_sub_u32 s65, 37888, s66\n"
        "buffer_load_dwordx4 v8, s[44:47], s65 offen lds\n"
        "s_sub_u32 s65, 46080, s66\n"
        "buffer_load_dwordx4 v8, s[44:47], s65 offen offset:1024 lds\n"
        "s_add_u32 m0, s82, 36864\n"
        "s_sub_u32 s65, 74752, s66\n"
        "buffer_load_dwordx4 v8, s[44:47], s65 offen lds\n"
        "s_sub_u32 s65, 82944, s66\n"
        "buffer_load_dwordx4 v8, s[44:47], s65 offen offset:1024 lds\n"
        "s_add_u32 m0, s82, 38912\n"
        "s_sub_u32 s65, 111616, s66\n"
        "buffer_load_dwordx4 v8, s[44:47], s65 offen lds\n"
        "s_sub_u32 s65, 119808, s66\n"
        "buffer_load_dwordx4 v8, s[44:47], s65 offen offset:1024 lds\n"
        ".Lch_noex_%=:\n"
        "s_waitcnt vmcnt(4)\n"
        "s_waitcnt lgkmcnt(0)\n"
        "s_barrier\n"
        "s_add_u32 m0, s82, 16384\n"
        "s_mov_b32 s65, 73728\n"
        "buffer_load_dwordx4 v9, s[44:47], s65 offen offset:0 lds\n"
        "s_mov_b32 s65, 77312\n"
        "buffer_load_dwordx4 v9, s[44:47], s65 offen offset:1024 lds\n"
        "s_mov_b32 s65, 80896\n"
        "buffer_load_dwordx4 v9, s[44:47], s65 offen offset:2048 lds\n"
        "s_mov_b32 s65, 84480\n"
        "buffer_load_dwordx4 v9, s[44:47], s65 offen offset:3072 lds\n"
        "ds_read_b64_tr_b16 v[112:113], v12 offset:8192\n"
        "ds_read_b64_tr_b16 v[114:115], v12 offset:8448\n"
        "ds_read_b64_tr_b16 v[116:117], v12 offset:10240\n"
        "ds_read_b64_tr_b16 v[118:119], v12 offset:10496\n"
        "ds_read_b64_tr_b16 v[144:145], v13 offset:8192\n"
        "ds_read_b64_tr_b16 v[146:147], v13 offset:8448\n"
        "ds_read_b64_tr_b16 v[148:149], v13 offset:10240\n"
        "ds_read_b64_tr_b16 v[150:151], v13 offset:10496\n"
        "s_waitcnt vmcnt(4)\n"
        "s_waitcnt lgkmcnt(0)\n"
        "s_barrier\n"
        "s_add_u32 m0, s82, 20480\n"
        "s_mov_b32 s65, 110592\n"
        "buffer_load_dwordx4 v9, s[44:47], s65 offen offset:0 lds\n"
        "s_mov_b32 s65, 114176\n"
        "buffer_load_dwordx4 v9, s[44:47], s65 offen offset:1024 lds\n"
        "s_mov_b32 s65, 117760\n"
        "buffer_load_dwordx4 v9, s[44:47], s65 offen offset:2048 lds\n"
        "s_mov_b32 s65, 121344\n"
        "buffer_load_dwordx4 v9, s[44:47], s65 offen offset:3072 lds\n"
        "ds_read_b64_tr_b16 v[120:121], v12 offset:12288\n"
        "ds_read_b64_tr_b16 v[122:123], v12 offset:12544\n"
        "ds_read_b64_tr_b16 v[124:125], v12 offset:14336\n"
        "ds_read_b64_tr_b16 v[126:127], v12 offset:14592\n"
        "ds_read_b64_tr_b16 v[152:153], v13 offset:12288\n"
        "ds_read_b64_tr_b16 v[154:155], v13 offset:12544\n"
        "ds_read_b64_tr_b16 v[156:157], v13 offset:14336\n"
        "ds_read_b64_tr_b16 v[158:159], v13 offset:14592\n"
        "s_waitcnt vmcnt(4)\n"
        "s_waitcnt lgkmcnt(0)\n"
        "s_barrier\n"
        "s_add_u32 m0, s82, 24576\n"
        "s_mov_b32 s65, 512\n"
        "buffer_load_dwordx4 v9, s[44:47], s65 offen offset:0 lds\n"
        "s_mov_b32 s65, 4096\n"
        "buffer_load_dwordx4 v9, s[44:47], s65 offen offset:1024 lds\n"
        "s_mov_b32 s65, 7680\n"
        "buffer_load_dwordx4 v9, s[44:47], s65 offen offset:2048 lds\n"
        "s_mov_b32 s65, 11264\n"
        "buffer_load_dwordx4 v9, s[44:47], s65 offen offset:3072 lds\n"
        "ds_read_b64_tr_b16 v[128:129], v12 offset:16384\n"
        "ds_read_b64_tr_b16 v[130:131], v12 offset:16640\n"
        "ds_read_b64_tr_b16 v[132:133], v12 offset:18432\n"
        "ds_read_b64_tr_b16 v[134:135], v12 offset:18688\n"
        "ds_read_b64_tr_b16 v[160:161], v13 offset:16384\n"
        "ds_read_b64_tr_b16 v[162:163], v13 offset:16640\n"
        "ds_read_b64_tr_b16 v[164:165], v13 offset:18432\n"
        "ds_read_b64_tr_b16 v[166:167], v13 offset:18688\n"
        "s_waitcnt vmcnt(4)\n"
        "s_waitcnt lgkmcnt(0)\n"
        "s_barrier\n"
        "s_add_u32 m0, s82, 28672\n"
        "s_mov_b32 s65, 37376\n"
        "buffer_load_dwordx4 v9, s[44:47], s65 offen offset:0 lds\n"
        "s_mov_b32 s65, 40960\n"
        "buffer_load_dwordx4 v9, s[44:47], s65 offen offset:1024 lds\n"
        "s_mov_b32 s65, 44544\n"
        "buffer_load_dwordx4 v9, s[44:47], s65 offen offset:2048 lds\n"
        "s_mov_b32 s65, 48128\n"
        "buffer_load_dwordx4 v9, s[44:47], s65 offen offset:3072 lds\n"
        "ds_read_b64_tr_b16 v[136:137], v12 offset:20480\n"
        "ds_read_b64_tr_b16 v[138:139], v12 offset:20736\n"
        "ds_read_b64_tr_b16 v[140:141], v12 offset:22528\n"
        "ds_read_b64_tr_b16 v[142:143], v12 offset:22784\n"
        "ds_read_b64_tr_b16 v[168:169], v13 offset:20480\n"
        "ds_read_b64_tr_b16 v[170:171], v13 offset:20736\n"
        "ds_read_b64_tr_b16 v[172:173], v13 offset:22528\n"
        "ds_read_b64_tr_b16 v[174:175], v13 offset:22784\n"
        "s_waitcnt vmcnt(4)\n"
        "s_waitcnt lgkmcnt(0)\n"
        "s_barrier\n"
        "s_add_u32 m0, s82, 0\n"
        "s_mov_b32 s65, 74240\n"
        "buffer_load_dwordx4 v9, s[44:47], s65 offen offset:0 lds\n"
        "s_mov_b32 s65, 77824\n"
        "buffer_load_dwordx4 v9, s[44:47], s65 offen offset:1024 lds\n"
        "s_mov_b32 s65, 81408\n"
        "buffer_load_dwordx4 v9, s[44:47], s65 offen offset:2048 lds\n"
        "s_mov_b32 s65, 84992\n"
        "buffer_load_dwordx4 v9, s[44:47], s65 offen offset:3072 lds\n"
        "ds_read_b64_tr_b16 v[176:177], v12 offset:24576\n"
        "ds_read_b64_tr_b16 v[178:179], v12 offset:24832\n"
        "ds_read_b64_tr_b16 v[180:181], v12 offset:26624\n"
        "ds_read_b64_tr_b16 v[182:183], v12 offset:26880\n"
        "ds_read_b64_tr_b16 v[208:209], v13 offset:24576\n"
        "ds_read_b64_tr_b16 v[210:211], v13 offset:24832\n"
        "ds_read_b64_tr_b16 v[212:213], v13 offset:26624\n"
        "ds_read_b64_tr_b16 v[214:215], v13 offset:26880\n"
        "s_waitcnt vmcnt(4)\n"
        "s_waitcnt lgkmcnt(0)\n"
        "s_barrier\n"
        "s_add_u32 m0, s82, 4096\n"
        "s_mov_b32 s65, 111104\n"
        "buffer_load_dwordx4 v9, s[44:47], s65 offen offset:0 lds\n"
        "s_mov_b32 s65, 114688\n"
        "buffer_load_dwordx4 v9, s[44:47], s65 offen offset:1024 lds\n"
        "s_mov_b32 s65, 118272\n"
        "buffer_load_dwordx4 v9, s[44:47], s65 offen offset:2048 lds\n"
        "s_mov_b32 s65, 121856\n"
        "buffer_load_dwordx4 v9, s[44:47], s65 offen offset:3072 lds\n"
        "ds_read_b64_tr_b16 v[184:185], v12 offset:28672\n"
        "ds_read_b64_tr_b16 v[186:187], v12 offset:28928\n"
        "ds_read_b64_tr_b16 v[188:189], v12 offset:30720\n"
        "ds_read_b64_tr_b16 v[190:191], v12 offset:30976\n"
        "ds_read_b64_tr_b16 v[216:217], v13 offset:28672\n"
        "ds_read_b64_tr_b16 v[218:219], v13 offset:28928\n"
        "ds_read_b64_tr_b16 v[220:221], v13 offset:30720\n"
        "ds_read_b64_tr_b16 v[222:223], v13 offset:30976\n"
        "s_waitcnt vmcnt(4)\n"
        "s_waitcnt lgkmcnt(0)\n"
        "s_barrier\n"
        "s_mov_b64 s[44:45], %3\n"
        "s_and_b32 s45, s45, 0xffff\n"
        "s_mov_b32 s46, 4718592\n"
        "s_add_u32 m0, s82, 8192\n"
        "s_mov_b32 s65, 4194304\n"
        "buffer_load_dwordx4 v6, s[44:47], s65 offen offset:0 lds\n"
        "s_mov_b32 s65, 4226048\n"
        "buffer_load_dwordx4 v7, s[44:47], s65 offen offset:1024 lds\n"
        "s_mov_b32 s65, 4257792\n"
        "buffer_load_dwordx4 v6, s[44:47], s65 offen offset:2048 lds\n"
        "s_mov_b32 s65, 4289536\n"
        "buffer_load_dwordx4 v7, s[44:47], s65 offen offset:3072 lds\n"
        "ds_read_b64_tr_b16 v[192:193], v12 offset:0\n"
        "ds_read_b64_tr_b16 v[194:195], v12 offset:256\n"
        "ds_read_b64_tr_b16 v[196:197], v12 offset:2048\n"
        "ds_read_b64_tr_b16 v[198:199], v12 offset:2304\n"
        "ds_read_b64_tr_b16 v[224:225], v13 offset:0\n"
        "ds_read_b64_tr_b16 v[226:227], v13 offset:256\n"
        "ds_read_b64_tr_b16 v[228:229], v13 offset:2048\n"
        "ds_read_b64_tr_b16 v[230:231], v13 offset:2304\n"
        "s_cmp_lt_u32 s48, 2\n"
        "s_cbranch_scc0 .Lch_noex2_%=\n"
        "ds_read_b64_tr_b16 v[24:25], v10 offset:32768\n"
        "ds_read_b64_tr_b16 v[26:27], v10 offset:33024\n"
        "ds_read_b64_tr_b16 v[28:29], v10 offset:33792\n"
        "ds_read_b64_tr_b16 v[30:31], v10 offset:34048\n"
        "ds_read_b64_tr_b16 v[32:33], v10 offset:34816\n"
        "ds_read_b64_tr_b16 v[34:35], v10 offset:35072\n"
        "ds_read_b64_tr_b16 v[36:37], v10 offset:35840\n"
        "ds_read_b64_tr_b16 v[38:39], v10 offset:36096\n"
        "ds_read_b64_tr_b16 v[40:41], v10 offset:36864\n"
        "ds_read_b64_tr_b16 v[42:43], v10 offset:37120\n"
        "ds_read_b64_tr_b16 v[44:45], v10 offset:37888\n"
        "ds_read_b64_tr_b16 v[46:47], v10 offset:38144\n"
        "ds_read_b64_tr_b16 v[48:49], v10 offset:38912\n"
        "ds_read_b64_tr_b16 v[50:51], v10 offset:39168\n"
        "ds_read_b64_tr_b16 v[52:53], v10 offset:39936\n"
        "ds_read_b64_tr_b16 v[54:55], v10 offset:40192\n"
        "s_waitcnt lgkmcnt(0)\n"
        "ds_write_b128 v2, v[24:27] offset:32768\n"
        "ds_write_b128 v2, v[28:31] offset:33792\n"
        "ds_write_b128 v2, v[32:35] offset:34816\n"
        "ds_write_b128 v2, v[36:39] offset:35840\n"
        "ds_write_b128 v2, v[40:43] offset:36864\n"
        "ds_write_b128 v2, v[44:47] offset:37888\n"
        "ds_write_b128 v2, v[48:51] offset:38912\n"
        "ds_write_b128 v2, v[52:55] offset:39936\n"
        ".Lch_noex2_%=:\n"
        "s_waitcnt vmcnt(4)\n"
        "s_waitcnt lgkmcnt(0)\n"
        "s_barrier\n"
        "s_add_u32 m0, s82, 12288\n"
        "s_mov_b32 s65, 1572864\n"
        "buffer_load_dwordx4 v6, s[44:47], s65 offen offset:0 lds\n"
        "s_mov_b32 s65, 1604608\n"
        "buffer_load_dwordx4 v7, s[44:47], s65 offen offset:1024 lds\n"
        "s_mov_b32 s65, 1636352\n"
        "buffer_load_dwordx4 v6, s[44:47], s65 offen offset:2048 lds\n"
        "s_mov_b32 s65, 1668096\n"
        "buffer_load_dwordx4 v7, s[44:47], s65 offen offset:3072 lds\n"
        "ds_read_b64_tr_b16 v[200:201], v12 offset:4096\n"
        "ds_read_b64_tr_b16 v[202:203], v12 offset:4352\n"
        "ds_read_b64_tr_b16 v[204:205], v12 offset:6144\n"
        "ds_read_b64_tr_b16 v[206:207], v12 offset:6400\n"
        "ds_read_b64_tr_b16 v[232:233], v13 offset:4096\n"
        "ds_read_b64_tr_b16 v[234:235], v13 offset:4352\n"
        "ds_read_b64_tr_b16 v[236:237], v13 offset:6144\n"
        "ds_read_b64_tr_b16 v[238:239], v13 offset:6400\n"
        "s_waitcnt vmcnt(4)\n"
        "s_waitcnt lgkmcnt(0)\n"
        "s_barrier\n"
        "s_add_u32 m0, s82, 16384\n"
        "s_mov_b32 s65, 2097152\n"
        "buffer_load_dwordx4 v6, s[44:47], s65 offen offset:0 lds\n"
        "s_mov_b32 s65, 2128896\n"
        "buffer_load_dwordx4 v7, s[44:47], s65 offen offset:1024 lds\n"
        "s_mov_b32 s65, 2160640\n"
        "buffer_load_dwordx4 v6, s[44:47], s65 offen offset:2048 lds\n"
        "s_mov_b32 s65, 2192384\n"
        "buffer_load_dwordx4 v7, s[44:47], s65 offen offset:3072 lds\n"
        "ds_read_b64_tr_b16 v[240:241], v14 offset:8192\n"
        "ds_read_b64_tr_b16 v[242:243], v14 offset:8704\n"
        "ds_read_b64_tr_b16 v[244:245], v15 offset:8192\n"
        "ds_read_b64_tr_b16 v[246:247], v15 offset:8704\n"
        "ds_read_b64_tr_b16 v[248:249], v16 offset:8192\n"
        "ds_read_b64_tr_b16 v[250:251], v16 offset:8704\n"
        "ds_read_b64_tr_b16 v[252:253], v17 offset:8192\n"
        "ds_read_b64_tr_b16 v[254:255], v17 offset:8704\n"
        "s_waitcnt vmcnt(4)\n"
        "s_waitcnt lgkmcnt(0)\n"
        "s_barrier\n"
        "s_add_u32 m0, s82, 20480\n"
        "s_mov_b32 s65, 2621440\n"
        "buffer_load_dwordx4 v6, s[44:47], s65 offen offset:0 lds\n"
        "s_mov_b32 s65, 2653184\n"
        "buffer_load_dwordx4 v7, s[44:47], s65 offen offset:1024 lds\n"
        "s_mov_b32 s65, 2684928\n"
        "buffer_load_dwordx4 v6, s[44:47], s65 offen offset:2048 lds\n"
        "s_mov_b32 s65, 2716672\n"
        "buffer_load_dwordx4 v7, s[44:47], s65 offen offset:3072 lds\n"
        "ds_read_b64_tr_b16 v[40:41], v14 offset:12288\n"
        "ds_read_b64_tr_b16 v[42:43], v14 offset:12800\n"
        "ds_read_b64_tr_b16 v[44:45], v15 offset:12288\n"
        "ds_read_b64_tr_b16 v[46:47], v15 offset:12800\n"
        "ds_read_b64_tr_b16 v[48:49], v16 offset:12288\n"
        "ds_read_b64_tr_b16 v[50:51], v16 offset:12800\n"
        "ds_read_b64_tr_b16 v[52:53], v17 offset:12288\n"
        "ds_read_b64_tr_b16 v[54:55], v17 offset:12800\n"
        "s_waitcnt vmcnt(4)\n"
        "s_waitcnt lgkmcnt(0)\n"
        "s_barrier\n"
        "ds_write_b128 v2, v[40:43] offset:12288\n"
        "ds_write_b128 v2, v[44:47] offset:13312\n"
        "ds_write_b128 v2, v[48:51] offset:14336\n"
        "ds_write_b128 v2, v[52:55] offset:15360\n"
        "s_add_u32 m0, s82, 24576\n"
        "s_mov_b32 s65, 3145728\n"
        "buffer_load_dwordx4 v6, s[44:47], s65 offen offset:0 lds\n"
        "s_mov_b32 s65, 3177472\n"
        "buffer_load_dwordx4 v7, s[44:47], s65 offen offset:1024 lds\n"
        "s_mov_b32 s65, 3209216\n"
        "buffer_load_dwordx4 v6, s[44:47], s65 offen offset:2048 lds\n"
        "s_mov_b32 s65, 3240960\n"
        "buffer_load_dwordx4 v7, s[44:47], s65 offen offset:3072 lds\n"
        "ds_read_b64_tr_b16 v[24:25], v14 offset:16384\n"
        "ds_read_b64_tr_b16 v[26:27], v14 offset:16896\n"
        "ds_read_b64_tr_b16 v[28:29], v15 offset:16384\n"
        "ds_read_b64_tr_b16 v[30:31], v15 offset:16896\n"
        "ds_read_b64_tr_b16 v[32:33], v16 offset:16384\n"
        "ds_read_b64_tr_b16 v[34:35], v16 offset:16896\n"
        "ds_read_b64_tr_b16 v[36:37], v17 offset:16384\n"
        "ds_read_b64_tr_b16 v[38:39], v17 offset:16896\n"
        "s_waitcnt vmcnt(4)\n"
        "s_waitcnt lgkmcnt(0)\n"
        "s_barrier\n"
        "ds_write_b128 v2, v[24:27] offset:16384\n"
        "ds_write_b128 v2, v[28:31] offset:17408\n"
        "ds_write_b128 v2, v[32:35] offset:18432\n"
        "ds_write_b128 v2, v[36:39] offset:19456\n"
        "s_add_u32 m0, s82, 28672\n"
        "s_mov_b32 s65, 3670016\n"
        "buffer_load_dwordx4 v6, s[44:47], s65 offen offset:0 lds\n"
        "s_mov_b32 s65, 3701760\n"
        "buffer_load_dwordx4 v7, s[44:47], s65 offen offset:1024 lds\n"
        "s_mov_b32 s65, 3733504\n"
        "buffer_load_dwordx4 v6, s[44:47], s65 offen offset:2048 lds\n"
        "s_mov_b32 s65, 3765248\n"
        "buffer_load_dwordx4 v7, s[44:47], s65 offen offset:3072 lds\n"
        "ds_read_b64_tr_b16 v[40:41], v14 offset:20480\n"
        "ds_read_b64_tr_b16 v[42:43], v14 offset:20992\n"
        "ds_read_b64_tr_b16 v[44:45], v15 offset:20480\n"
        "ds_read_b64_tr_b16 v[46:47], v15 offset:20992\n"
        "ds_read_b64_tr_b16 v[48:49], v16 offset:20480\n"
        "ds_read_b64_tr_b16 v[50:51], v16 offset:20992\n"
        "ds_read_b64_tr_b16 v[52:53], v17 offset:20480\n"
        "ds_read_b64_tr_b16 v[54:55], v17 offset:20992\n"
        "s_waitcnt vmcnt(4)\n"
        "s_waitcnt lgkmcnt(0)\n"
        "s_barrier\n"
        "ds_write_b128 v2, v[40:43] offset:20480\n"
        "ds_write_b128 v2, v[44:47] offset:21504\n"
        "ds_write_b128 v2, v[48:51] offset:22528\n"
        "ds_write_b128 v2, v[52:55] offset:23552\n"
        "s_add_u32 m0, s82, 0\n"
        "s_mov_b32 s65, 0\n"
        "buffer_load_dwordx4 v6, s[44:47], s65 offen offset:0 lds\n"
        "s_mov_b32 s65, 31744\n"
        "buffer_load_dwordx4 v7, s[44:47], s65 offen offset:1024 lds\n"
        "s_mov_b32 s65, 63488\n"
        "buffer_load_dwordx4 v6, s[44:47], s65 offen offset:2048 lds\n"
        "s_mov_b32 s65, 95232\n"
        "buffer_load_dwordx4 v7, s[44:47], s65 offen offset:3072 lds\n"
        "ds_read_b64_tr_b16 v[24:25], v14 offset:24576\n"
        "ds_read_b64_tr_b16 v[26:27], v14 offset:25088\n"
        "ds_read_b64_tr_b16 v[28:29], v15 offset:24576\n"
        "ds_read_b64_tr_b16 v[30:31], v15 offset:25088\n"
        "ds_read_b64_tr_b16 v[32:33], v16 offset:24576\n"
        "ds_read_b64_tr_b16 v[34:35], v16 offset:25088\n"
        "ds_read_b64_tr_b16 v[36:37], v17 offset:24576\n"
        "ds_read_b64_tr_b16 v[38:39], v17 offset:25088\n"
        "s_waitcnt vmcnt(4)\n"
        "s_waitcnt lgkmcnt(0)\n"
        "s_barrier\n"
        "ds_write_b128 v2, v[24:27] offset:24576\n"
        "ds_write_b128 v2, v[28:31] offset:25600\n"
        "ds_write_b128 v2, v[32:35] offset:26624\n"
        "ds_write_b128 v2, v[36:39] offset:27648\n"
        "s_add_u32 m0, s82, 4096\n"
        "s_mov_b32 s65, 524288\n"
        "buffer_load_dwordx4 v6, s[44:47], s65 offen offset:0 lds\n"
        "s_mov_b32 s65, 556032\n"
        "buffer_load_dwordx4 v7, s[44:47], s65 offen offset:1024 lds\n"
        "s_mov_b32 s65, 587776\n"
        "buffer_load_dwordx4 v6, s[44:47], s65 offen offset:2048 lds\n"
        "s_mov_b32 s65, 619520\n"
        "buffer_load_dwordx4 v7, s[44:47], s65 offen offset:3072 lds\n"
        "ds_read_b64_tr_b16 v[40:41], v14 offset:28672\n"
        "ds_read_b64_tr_b16 v[42:43], v14 offset:29184\n"
        "ds_read_b64_tr_b16 v[44:45], v15 offset:28672\n"
        "ds_read_b64_tr_b16 v[46:47], v15 offset:29184\n"
        "ds_read_b64_tr_b16 v[48:49], v16 offset:28672\n"
        "ds_read_b64_tr_b16 v[50:51], v16 offset:29184\n"
        "ds_read_b64_tr_b16 v[52:53], v17 offset:28672\n"
        "ds_read_b64_tr_b16 v[54:55], v17 offset:29184\n"
        "s_waitcnt vmcnt(4)\n"
        "s_waitcnt lgkmcnt(0)\n"
        "s_barrier\n"
        "ds_write_b128 v2, v[40:43] offset:28672\n"
        "ds_write_b128 v2, v[44:47] offset:29696\n"
        "ds_write_b128 v2, v[48:51] offset:30720\n"
        "ds_write_b128 v2, v[52:55] offset:31744\n"
        "s_add_u32 m0, s82, 8192\n"
        "s_mov_b32 s65, 1048576\n"
        "buffer_load_dwordx4 v6, s[44:47], s65 offen offset:0 lds\n"
        "s_mov_b32 s65, 1080320\n"
        "buffer_load_dwordx4 v7, s[44:47], s65 offen offset:1024 lds\n"
        "s_mov_b32 s65, 1112064\n"
        "buffer_load_dwordx4 v6, s[44:47], s65 offen offset:2048 lds\n"
        "s_mov_b32 s65, 1143808\n"
        "buffer_load_dwordx4 v7, s[44:47], s65 offen offset:3072 lds\n"
        "ds_read_b64_tr_b16 v[24:25], v14 offset:0\n"
        "ds_read_b64_tr_b16 v[26:27], v14 offset:512\n"
        "ds_read_b64_tr_b16 v[28:29], v15 offset:0\n"
        "ds_read_b64_tr_b16 v[30:31], v15 offset:512\n"
        "ds_read_b64_tr_b16 v[32:33], v16 offset:0\n"
        "ds_read_b64_tr_b16 v[34:35], v16 offset:512\n"
        "ds_read_b64_tr_b16 v[36:37], v17 offset:0\n"
        "ds_read_b64_tr_b16 v[38:39], v17 offset:512\n"
        "s_waitcnt vmcnt(4)\n"
        "s_waitcnt lgkmcnt(0)\n"
        "s_barrier\n"
        "ds_write_b128 v2, v[24:27] offset:0\n"
        "ds_write_b128 v2, v[28:31] offset:1024\n"
        "ds_write_b128 v2, v[32:35] offset:2048\n"
        "ds_write_b128 v2, v[36:39] offset:3072\n"
        "v_lshlrev_b32 v60, 3, v3\n"
        "s_mov_b32 s65, 32768\n"
        "buffer_load_dword v61, v60, s[40:43], s65 offen sc1\n"
        "s_mov_b32 s65, 40960\n"
        "buffer_load_dword v62, v60, s[40:43], s65 offen sc1\n"
        "ds_read_b64_tr_b16 v[40:41], v14 offset:4096\n"
        "ds_read_b64_tr_b16 v[42:43], v14 offset:4608\n"
        "ds_read_b64_tr_b16 v[44:45], v15 offset:4096\n"
        "ds_read_b64_tr_b16 v[46:47], v15 offset:4608\n"
        "ds_read_b64_tr_b16 v[48:49], v16 offset:4096\n"
        "ds_read_b64_tr_b16 v[50:51], v16 offset:4608\n"
        "ds_read_b64_tr_b16 v[52:53], v17 offset:4096\n"
        "ds_read_b64_tr_b16 v[54:55], v17 offset:4608\n"
        "s_waitcnt vmcnt(2)\n"
        "s_waitcnt lgkmcnt(0)\n"
        "s_barrier\n"
        "ds_write_b128 v2, v[40:43] offset:4096\n"
        "ds_write_b128 v2, v[44:47] offset:5120\n"
        "ds_write_b128 v2, v[48:51] offset:6144\n"
        "ds_write_b128 v2, v[52:55] offset:7168\n"
        "ds_read_b64_tr_b16 v[24:25], v14 offset:8192\n"
        "ds_read_b64_tr_b16 v[26:27], v14 offset:8704\n"
        "ds_read_b64_tr_b16 v[28:29], v15 offset:8192\n"
        "ds_read_b64_tr_b16 v[30:31], v15 offset:8704\n"
        "ds_read_b64_tr_b16 v[32:33], v16 offset:8192\n"
        "ds_read_b64_tr_b16 v[34:35], v16 offset:8704\n"
        "ds_read_b64_tr_b16 v[36:37], v17 offset:8192\n"
        "ds_read_b64_tr_b16 v[38:39], v17 offset:8704\n"
        "s_waitcnt lgkmcnt(0)\n"
        "s_barrier\n"
        "ds_write_b128 v2, v[24:27] offset:8192\n"
        "ds_write_b128 v2, v[28:31] offset:9216\n"
        "ds_write_b128 v2, v[32:35] offset:10240\n"
        "ds_write_b128 v2, v[36:39] offset:11264\n"
        "s_waitcnt vmcnt(0) lgkmcnt(0)\n"
        "v_lshrrev_b32 v12, 4, v3\n"
        "s_mov_b32 s50, s56\n"
        "v_lshlrev_b32 v14, 7, v12\n"
        "s_sub_u32 s66, 0, s50\n"
        "s_mov_b32 s52, 32768\n"
        "v_cmp_gt_i32 vcc, 0, v61\n"
        "s_cbranch_vccz .Lch_pg0_ok_%=\n"
        "s_mov_b32 s64, 0\n"
        ".Lch_pg0_top_%=:\n"
        "buffer_load_dword v61, v14, s[40:43], s52 offen sc1\n"
        "s_waitcnt vmcnt(0)\n"
        "v_cmp_gt_i32 vcc, 0, v61\n"
        "s_cbranch_vccz .Lch_pg0_ok_%=\n"
        "s_add_u32 s64, s64, 1\n"
        "s_and_b32 s65, s64, 255\n"
        "s_cbranch_scc1 .Lch_pg0_go_%=\n"
        "s_mov_b32 s65, 49152\n"
        "s_cmp_lt_u32 s64, 1048576\n"
        "s_cbranch_scc1 .Lch_pg0_chk_%=\n"
        "v_mov_b32 v61, 1\n"
        "buffer_store_dword v61, off, s[40:43], s65 sc1\n"
        "s_waitcnt vmcnt(0)\n"
        ".Lch_pg0_chk_%=:\n"
        "buffer_load_dword v61, off, s[40:43], s65 sc1\n"
        "s_waitcnt vmcnt(0)\n"
        "v_readfirstlane_b32 s65, v61\n"
        "s_nop 0\n"
        "s_cmp_lg_u32 s65, 1\n"
        "s_cbranch_scc1 .Lch_pg0_go_%=\n"
        "s_endpgm\n"
        ".Lch_pg0_go_%=:\n"
        "s_sleep 2\n"
        "s_branch .Lch_pg0_top_%=\n"
        ".Lch_pg0_ok_%=:\n"
        "v_cmp_eq_u32 s[52:53], 1, v61\n"
        "s_nop 1\n"
        "s_mov_b32 s54, 40960\n"
        "v_cmp_gt_i32 vcc, 0, v62\n"
        "s_cbranch_vccz .Lch_pg1_ok_%=\n"
        "s_mov_b32 s64, 0\n"
        ".Lch_pg1_top_%=:\n"
        "buffer_load_dword v62, v14, s[40:43], s54 offen sc1\n"
        "s_waitcnt vmcnt(0)\n"
        "v_cmp_gt_i32 vcc, 0, v62\n"
        "s_cbranch_vccz .Lch_pg1_ok_%=\n"
        "s_add_u32 s64, s64, 1\n"
        "s_and_b32 s65, s64, 255\n"
        "s_cbranch_scc1 .Lch_pg1_go_%=\n"
        "s_mov_b32 s65, 49152\n"
        "s_cmp_lt_u32 s64, 1048576\n"
        "s_cbranch_scc1 .Lch_pg1_chk_%=\n"
        "v_mov_b32 v62, 1\n"
        "buffer_store_dword v62, off, s[40:43], s65 sc1\n"
        "s_waitcnt vmcnt(0)\n"
        ".Lch_pg1_chk_%=:\n"
        "buffer_load_dword v62, off, s[40:43], s65 sc1\n"
        "s_waitcnt vmcnt(0)\n"
        "v_readfirstlane_b32 s65, v62\n"
        "s_nop 0\n"
        "s_cmp_lg_u32 s65, 1\n"
        "s_cbranch_scc1 .Lch_pg1_go_%=\n"
        "s_endpgm\n"
        ".Lch_pg1_go_%=:\n"
        "s_sleep 2\n"
        "s_branch .Lch_pg1_top_%=\n"
        ".Lch_pg1_ok_%=:\n"
        "v_cmp_eq_u32 s[54:55], 1, v62\n"
        "s_nop 1\n"
        "s_mov_b32 vcc_lo, 0x55555555\n"
        "s_mov_b32 vcc_hi, 0x55555555\n"
        "s_xor_b64 vcc, vcc, s[52:53]\n"
        "s_bcnt1_i32_b64 s65, vcc\n"
        "s_sub_u32 s64, s65, 23\n"
        "s_cmp_lt_u32 s64, 19\n"
        "s_cbranch_scc1 .Lch_rawmap_%=\n"
        "s_cmp_lt_u32 s65, 32\n"
        "s_cselect_b32 s65, 0, -1\n"
        "s_xor_b32 s52, s65, 0x55555555\n"
        "s_xor_b32 s53, s65, 0x55555555\n"
        "s_xor_b32 s54, s65, 0x55555555\n"
        "s_xor_b32 s55, s65, 0xaaaaaaaa\n"
        ".Lch_rawmap_%=:\n"
        "s_xor_b32 s52, s52, s66\n"
        "s_xor_b32 s53, s53, s66\n"
        "s_xor_b32 s54, s54, s66\n"
        "s_xor_b32 s55, s55, s66\n"
        "s_mov_b32 s58, 0xff00ff00\n"
        "s_mov_b32 s59, 0xff00ff00\n"
        "s_and_b32 s67, s55, 1\n"
        "s_lshl_b32 s67, s67, 13\n"
        "s_lshl_b32 s65, s49, 2\n"
        "s_add_u32 s65, s65, s48\n"
        "s_lshl_b32 s65, s65, 7\n"
        "s_lshr_b32 s66, s65, 13\n"
        "s_lshl_b32 s66, s66, 13\n"
        "s_add_u32 s66, s66, s65\n"
        "s_add_u32 s66, s66, s67\n"
        "s_add_u32 s78, s66, 180224\n"
        "s_add_u32 s79, s66, 245760\n"
        "s_lshl_b32 s83, s49, 14\n"
        "s_lshl_b32 s65, s48, 11\n"
        "s_add_u32 s83, s83, s65\n"
        "s_add_u32 s83, s83, 524288\n"
        "s_lshl_b32 s65, s49, 12\n"
        "s_lshr_b32 s84, s65, 13\n"
        "s_lshl_b32 s84, s84, 13\n"
        "s_add_u32 s84, s84, s65\n"
        "s_lshl_b32 s65, s48, 9\n"
        "s_add_u32 s84, s84, s65\n"
        "s_add_u32 s84, s84, 1572864\n"
        "s_lshr_b32 s65, s49, 2\n"
        "s_add_u32 s65, s65, 64\n"
        "s_cmp_eq_u32 s48, 2\n"
        "s_cselect_b32 s65, s49, s65\n"
        "s_add_u32 s66, s49, 32\n"
        "s_cmp_eq_u32 s48, 3\n"
        "s_cselect_b32 s65, s66, s65\n"
        "s_lshl_b32 s85, s65, 7\n"
        "s_cmp_eq_u32 s48, 0\n"
        "s_cselect_b32 s66, 57344, 0\n"
        "s_add_u32 s85, s85, s66\n"
        "s_add_u32 s85, s85, s67\n"
        "s_add_u32 s85, s85, 1130496\n"
        "s_lshr_b32 s66, s49, 3\n"
        "s_lshl_b32 s54, s66, 14\n"
        "s_lshl_b32 s55, s66, 10\n"
        "s_and_b32 s66, s48, 1\n"
        "s_lshl_b32 s66, s66, 12\n"
        "s_add_u32 s54, s54, s66\n"
        "s_add_u32 s66, s67, 1130496\n"
        "s_add_u32 s54, s54, s66\n"
        "s_add_u32 s55, s55, s66\n"
        "s_add_u32 s55, s55, 65536\n"
        "s_add_u32 s72, s85, 245760\n"
        "s_lshl_b32 s66, s65, 12\n"
        "s_lshr_b32 s87, s66, 13\n"
        "s_lshl_b32 s87, s87, 13\n"
        "s_add_u32 s87, s87, s66\n"
        "s_and_b32 s65, s49, 3\n"
        "s_or_b32 s88, s65, s48\n"
        "s_lshl_b32 s86, s48, 16\n"
        "s_lshl_b32 s65, s49, 7\n"
        "s_add_u32 s86, s86, s65\n"
        "s_add_u32 s67, s67, 540672\n"
        "s_add_u32 s86, s86, s67\n"
        "s_add_u32 s87, s87, s67\n"
        "s_mov_b32 s73, s86\n"
        "s_add_u32 s74, s73, 262144\n"
        "s_mov_b32 s76, s87\n"
        "s_lshl_b32 s65, s48, 15\n"
        "s_sub_u32 s75, s73, s65\n"
        "s_add_u32 s75, s75, 524288\n"
        "s_lshl_b32 s65, s48, 3\n"
        "s_lshr_b32 s60, s60, s65\n"
        "s_and_b32 s60, s60, 255\n"
        "s_sub_u32 s60, s60, 32\n"
        "s_lshr_b32 s34, s61, s65\n"
        "s_and_b32 s34, s34, 255\n"
        "s_lshr_b32 s61, s62, s65\n"
        "s_and_b32 s61, s61, 255\n"
        "s_mov_b32 s35, 90\n"
        "s_lshl_b32 s33, s48, 10\n"
        "s_lshl_b32 s32, s48, 2\n"
        "s_lshr_b32 s65, s48, 1\n"
        "s_lshl_b32 s31, s65, 10\n"
        "s_and_b32 s65, s48, 1\n"
        "s_lshl_b32 s65, s65, 6\n"
        "s_add_u32 s31, s31, s65\n"
        "s_mov_b32 s51, -1\n"
        "s_mov_b32 s80, -1\n"
        "s_lshl_b32 s62, s49, 7\n"
        "s_cmp_eq_u32 s48, 1\n"
        "s_cselect_b32 s62, s62, 2147483648\n"
        "s_bfe_u32 s56, s63, 0x80008\n"
        "s_lshr_b32 s57, s63, 16\n"
        "s_and_b32 s63, s63, 255\n"
        "s_mov_b32 s44, 0x3f803f80\n"
        "s_mov_b32 s45, 0x3f803f80\n"
        "s_branch .Lch_idle2_%=\n"
        ".Lch_code_%=:\n"
        ".Lch_Q_%=:\n"
        "ds_read_b128 v[32:35], v1 offset:0\n"
        "ds_read_b32 v7, v5 offset:0\n"
        "ds_read_b128 v[36:39], v1 offset:1024\n"
        "s_waitcnt lgkmcnt(2)\n"
        "v_smfmac_f32_16x16x64_bf16 v[8:11], v[32:35], a[0:7], v0\n"
        "v_max3_u32 v6, v6, v32, v33\n"
        "v_smfmac_f32_16x16x64_bf16 v[16:19], v[32:35], a[144:151], v0\n"
        "s_waitcnt lgkmcnt(1)\n"
        "v_cmp_eq_u32 vcc, -1, v7\n"
        "v_smfmac_f32_16x16x64_bf16 v[24:27], v[32:35], v[40:47], v0\n"
        "ds_read_b128 v[40:43], v2 offset:6144\n"
        "v_max3_u32 v6, v6, v34, v35\n"
        "v_smfmac_f32_16x16x64_bf16 v[12:15], v[32:35], a[8:15], v0\n"
        "s_cbranch_vccnz .Lch_rt0_%=\n"
        "v_smfmac_f32_16x16x64_bf16 v[20:23], v[32:35], a[152:159], v0\n"
        "s_waitcnt lgkmcnt(1)\n"
        "ds_read_b128 v[44:47], v2 offset:7168\n"
        "v_smfmac_f32_16x16x64_bf16 v[28:31], v[32:35], v[48:55], v0\n"
        "ds_read_b128 v[48:51], v2 offset:8192\n"
        "v_smfmac_f32_16x16x64_bf16 v[8:11], v[36:39], a[16:23], v0\n"
        "ds_read_b128 v[32:35], v1 offset:2048\n"
        "v_max3_u32 v6, v6, v36, v37\n"
        "v_smfmac_f32_16x16x64_bf16 v[16:19], v[36:39], a[160:167], v0\n"
        "ds_read_b128 v[52:55], v2 offset:9216\n"
        "v_smfmac_f32_16x16x64_bf16 v[24:27], v[36:39], v[56:63], v0\n"
        "ds_read_b128 v[56:59], v2 offset:10240\n"
        "v_max3_u32 v6, v6, v38, v39\n"
        "v_smfmac_f32_16x16x64_bf16 v[12:15], v[36:39], a[24:31], v0\n"
        "ds_read_b128 v[60:63], v2 offset:11264\n"
        "v_smfmac_f32_16x16x64_bf16 v[20:23], v[36:39], a[168:175], v0\n"
        "s_waitcnt lgkmcnt(3)\n"
        "v_smfmac_f32_16x16x64_bf16 v[28:31], v[36:39], v[40:47], v0\n"
        "ds_read_b128 v[40:43], v2 offset:12288\n"
        "v_smfmac_f32_16x16x64_bf16 v[8:11], v[32:35], a[32:39], v0\n"
        "ds_read_b128 v[36:39], v1 offset:3072\n"
        "v_max3_u32 v6, v6, v32, v33\n"
        "v_smfmac_f32_16x16x64_bf16 v[16:19], v[32:35], a[176:183], v0\n"
        "s_waitcnt lgkmcnt(4)\n"
        "ds_read_b128 v[44:47], v2 offset:13312\n"
        "buffer_load_dwordx4 v[64:67], v3, s[40:43], s30 offen offset:0 sc1\n"
        "v_smfmac_f32_16x16x64_bf16 v[24:27], v[32:35], v[48:55], v0\n"
        "ds_read_b128 v[48:51], v2 offset:14336\n"
        "v_max3_u32 v6, v6, v34, v35\n"
        "v_smfmac_f32_16x16x64_bf16 v[12:15], v[32:35], a[40:47], v0\n"
        "ds_read_b128 v[52:55], v2 offset:15360\n"
        "buffer_load_dwordx4 v[68:71], v3, s[40:43], s30 offen offset:1024 sc1\n"
        "v_smfmac_f32_16x16x64_bf16 v[20:23], v[32:35], a[184:191], v0\n"
        "s_waitcnt lgkmcnt(3)\n"
        "s_waitcnt vmcnt(3)\n"
        "s_barrier\n"
        "v_smfmac_f32_16x16x64_bf16 v[28:31], v[32:35], v[56:63], v0\n"
        "ds_read_b128 v[56:59], v2 offset:16384\n"
        "ds_read_b32 v7, v5 offset:4096\n"
        "v_smfmac_f32_16x16x64_bf16 v[8:11], v[36:39], a[48:55], v0\n"
        "ds_read_b128 v[32:35], v1 offset:4096\n"
        "v_max3_u32 v6, v6, v36, v37\n"
        "v_smfmac_f32_16x16x64_bf16 v[16:19], v[36:39], a[192:199], v0\n"
        "s_waitcnt lgkmcnt(5)\n"
        "ds_read_b128 v[60:63], v2 offset:17408\n"
        "v_smfmac_f32_16x16x64_bf16 v[24:27], v[36:39], v[40:47], v0\n"
        "ds_read_b128 v[40:43], v2 offset:18432\n"
        "v_max3_u32 v6, v6, v38, v39\n"
        "v_smfmac_f32_16x16x64_bf16 v[12:15], v[36:39], a[56:63], v0\n"
        "ds_read_b128 v[44:47], v2 offset:19456\n"
        "v_smfmac_f32_16x16x64_bf16 v[20:23], v[36:39], a[200:207], v0\n"
        "s_waitcnt lgkmcnt(3)\n"
        "v_cmp_eq_u32 vcc, -1, v7\n"
        "v_smfmac_f32_16x16x64_bf16 v[28:31], v[36:39], v[48:55], v0\n"
        "s_cbranch_vccnz .Lch_rt1_%=\n"
        ".Lch_ok1_%=:\n"
        "v_smfmac_f32_16x16x64_bf16 v[8:11], v[32:35], a[64:71], v0\n"
        "ds_read_b128 v[36:39], v1 offset:5120\n"
        "v_max3_u32 v6, v6, v32, v33\n"
        "v_smfmac_f32_16x16x64_bf16 v[16:19], v[32:35], a[208:215], v0\n"
        "s_waitcnt lgkmcnt(3)\n"
        "ds_read_b128 v[52:55], v2 offset:21504\n"
        "ds_read_b128 v[48:51], v2 offset:20480\n"
        "v_smfmac_f32_16x16x64_bf16 v[24:27], v[32:35], v[56:63], v0\n"
        "ds_read_b128 v[56:59], v2 offset:22528\n"
        "v_max3_u32 v6, v6, v34, v35\n"
        "v_smfmac_f32_16x16x64_bf16 v[12:15], v[32:35], a[72:79], v0\n"
        "ds_read_b128 v[60:63], v2 offset:23552\n"
        "v_smfmac_f32_16x16x64_bf16 v[20:23], v[32:35], a[216:223], v0\n"
        "s_waitcnt lgkmcnt(4)\n"
        "buffer_load_dwordx4 v[72:75], v3, s[40:43], s89 offen offset:0 sc1\n"
        "v_smfmac_f32_16x16x64_bf16 v[28:31], v[32:35], v[40:47], v0\n"
        "ds_read_b128 v[40:43], v2 offset:24576\n"
        "v_smfmac_f32_16x16x64_bf16 v[8:11], v[36:39], a[80:87], v0\n"
        "ds_read_b128 v[32:35], v1 offset:6144\n"
        "v_max3_u32 v6, v6, v36, v37\n"
        "v_smfmac_f32_16x16x64_bf16 v[16:19], v[36:39], a[224:231], v0\n"
        "s_waitcnt lgkmcnt(4)\n"
        "ds_read_b128 v[44:47], v2 offset:25600\n"
        "v_smfmac_f32_16x16x64_bf16 v[24:27], v[36:39], v[48:55], v0\n"
        "ds_read_b128 v[48:51], v2 offset:26624\n"
        "v_max3_u32 v6, v6, v38, v39\n"
        "v_smfmac_f32_16x16x64_bf16 v[12:15], v[36:39], a[88:95], v0\n"
        "ds_read_b128 v[52:55], v2 offset:27648\n"
        "buffer_load_dwordx4 v[76:79], v3, s[40:43], s77 offen offset:1024 sc1\n"
        "v_smfmac_f32_16x16x64_bf16 v[20:23], v[36:39], a[232:239], v0\n"
        "s_waitcnt lgkmcnt(3)\n"
        "v_smfmac_f32_16x16x64_bf16 v[28:31], v[36:39], v[56:63], v0\n"
        "s_waitcnt lgkmcnt(2)\n"
        "ds_read_b128 v[56:59], v2 offset:28672\n"
        "v_smfmac_f32_16x16x64_bf16 v[24:27], v[32:35], v[40:47], v0\n"
        "s_waitcnt lgkmcnt(1)\n"
        "ds_read_b128 v[36:39], v1 offset:7168\n"
        "ds_read_b32 v40, v4 offset:11264\n"
        "v_smfmac_f32_16x16x64_bf16 v[28:31], v[32:35], v[48:55], v0\n"
        "s_waitcnt vmcnt(4)\n"
        "s_barrier\n"
        "ds_read_b32 v7, v5 offset:8192\n"
        "v_smfmac_f32_16x16x64_bf16 v[8:11], v[32:35], a[96:103], v0\n"
        "s_memrealtime s[36:37]\n"
        "ds_read_b128 v[48:51], v1 offset:8192\n"
        "v_max3_u32 v6, v6, v32, v33\n"
        "v_smfmac_f32_16x16x64_bf16 v[16:19], v[32:35], a[240:247], v0\n"
        "ds_read_b128 v[52:55], v4 offset:9216\n"
        "v_max3_u32 v6, v6, v34, v35\n"
        "v_smfmac_f32_16x16x64_bf16 v[12:15], v[32:35], a[104:111], v0\n"
        "v_smfmac_f32_16x16x64_bf16 v[20:23], v[32:35], a[248:255], v0\n"
        "s_waitcnt lgkmcnt(2)\n"
        "s_nop 0\n"
        "v_cmp_eq_u32 vcc, -1, v7\n"
        "v_smfmac_f32_16x16x64_bf16 v[8:11], v[36:39], a[112:119], v0\n"
        "s_waitcnt lgkmcnt(0)\n"
        "s_and_b64 vcc, vcc, 0xffff\n"
        "v_max3_u32 v6, v6, v36, v37\n"
        "v_smfmac_f32_16x16x64_bf16 v[12:15], v[36:39], a[120:127], v0\n"
        "s_cbranch_vccnz .Lch_rt2_%=\n"
        ".Lch_ok2_%=:\n"
        "v_smfmac_f32_16x16x64_bf16 v[8:11], v[48:51], a[128:135], v0\n"
        "s_add_u32 s37, s36, s35\n"
        "ds_read_b128 v[44:47], v2 offset:31744\n"
        "v_readfirstlane_b32 s81, v40\n"
        "v_smfmac_f32_16x16x64_bf16 v[12:15], v[48:51], a[136:143], v0\n"
        "s_cmp_eq_u32 s81, s51\n"
        "v_and_b32 v7, -2, s37\n"
        "v_smfmac_f32_16x16x64_bf16 v[16:19], v[36:39], v[80:87], v0\n"
        "s_cselect_b32 s65, 2147483648, s46\n"
        "v_cvt_pk_bf16_f32 v8, v8, v9\n"
        "v_smfmac_f32_16x16x64_bf16 v[16:19], v[48:51], v[96:103], v0\n"
        "s_mov_b64 exec, 0xff\n"
        "buffer_store_dwordx4 v[4:7], v3, s[40:43], s65 offen sc1\n"
        "s_mov_b64 exec, -1\n"
        "v_cvt_pk_bf16_f32 v9, v10, v11\n"
        "v_smfmac_f32_16x16x64_bf16 v[20:23], v[36:39], v[88:95], v0\n"
        "ds_read_b128 v[60:63], v2 offset:29696\n"
        "v_cvt_pk_bf16_f32 v12, v12, v13\n"
        "v_smfmac_f32_16x16x64_bf16 v[20:23], v[48:51], v[104:111], v0\n"
        "ds_read_b128 v[40:43], v2 offset:30720\n"
        "v_cvt_pk_bf16_f32 v13, v14, v15\n"
        "v_mfma_f32_16x16x16_bf16 v[32:35], v[8:9], v[52:53], 0\n"
        "v_cvt_pk_bf16_f32 v16, v16, v17\n"
        "v_cvt_pk_bf16_f32 v17, v18, v19\n"
        "v_mfma_f32_16x16x16_bf16 v[8:11], v[12:13], v[52:53], 0\n"
        "v_cvt_pk_bf16_f32 v20, v20, v21\n"
        "v_cvt_pk_bf16_f32 v21, v22, v23\n"
        "v_mfma_f32_16x16x16_bf16 v[12:15], v[16:17], v[54:55], 0\n"
        "v_cvt_pk_bf16_f32 v32, v32, v33\n"
        "v_cvt_pk_bf16_f32 v33, v34, v35\n"
        "v_mfma_f32_16x16x16_bf16 v[16:19], v[20:21], v[54:55], 0\n"
        "v_cvt_pk_bf16_f32 v34, v8, v9\n"
        "v_cvt_pk_bf16_f32 v35, v10, v11\n"
        "s_cbranch_scc1 .Lch_slow_%=\n"
        ".Lch_back_%=:\n"
        "s_waitcnt vmcnt(3)\n"
        "v_mfma_f32_16x16x32_bf16 v[8:11], v[64:67], v[32:35], 0\n"
        "v_cvt_pk_bf16_f32 v12, v12, v13\n"
        "v_cvt_pk_bf16_f32 v13, v14, v15\n"
        "v_mfma_f32_16x16x32_bf16 v[20:23], v[68:71], v[32:35], 0\n"
        "v_cvt_pk_bf16_f32 v14, v16, v17\n"
        "v_cvt_pk_bf16_f32 v15, v18, v19\n"
        "v_add_u32 v52, s33, v4\n"
        "v_add_u32 v53, s32, v4\n"
        "v_mfma_f32_16x16x32_bf16 v[16:19], v[12:15], v[32:35], 0\n"
        "v_max3_u32 v6, v6, v38, v39\n"
        "v_max3_u32 v6, v6, v48, v49\n"
        "v_max3_u32 v6, v6, v50, v51\n"
        "v_mov_b32_dpp v8, v20 quad_perm:[0,1,2,3] row_mask:0xf bank_mask:0xc\n"
        "v_mov_b32_dpp v9, v21 quad_perm:[0,1,2,3] row_mask:0xf bank_mask:0xc\n"
        "v_mov_b32_dpp v10, v22 quad_perm:[0,1,2,3] row_mask:0xf bank_mask:0xc\n"
        "v_mov_b32_dpp v11, v23 quad_perm:[0,1,2,3] row_mask:0xf bank_mask:0xc\n"
        "ds_write_b128 v52, v[8:11]\n"
        "v_mov_b32_dpp v16, v17 quad_perm:[0,1,2,3] row_mask:0xf bank_mask:0xc\n"
        "ds_write_b32 v53, v16 offset:4096\n"
        "s_waitcnt lgkmcnt(2)\n"
        "v_smfmac_f32_16x16x64_bf16 v[24:27], v[36:39], v[56:63], v0\n"
        "ds_read_b128 v[68:71], v4 offset:10240\n"
        "v_cmp_eq_u32 vcc, -1, v6\n"
        "v_smfmac_f32_16x16x64_bf16 v[28:31], v[36:39], v[40:47], v0\n"
        "v_mov_b64 v[52:53], s[44:45]\n"
        "v_smfmac_f32_16x16x64_bf16 v[24:27], v[48:51], v[240:247], v0\n"
        "s_cbranch_vccnz .Lch_redo_%=\n"
        "v_smfmac_f32_16x16x64_bf16 v[28:31], v[48:51], v[248:255], v0\n"
        "s_waitcnt lgkmcnt(0)\n"
        "s_barrier\n"
        "ds_read_b128 v[32:35], v4 offset:0\n"
        "ds_read_b128 v[36:39], v4 offset:1024\n"
        "ds_read_b128 v[40:43], v4 offset:2048\n"
        "ds_read_b128 v[44:47], v4 offset:3072\n"
        "ds_read_b128 v[48:51], v4 offset:4096\n"
        "buffer_store_dwordx4 v[12:15], v68, s[40:43], s70 offen sc1\n"
        "v_add_u32 v71, s31, v71\n"
        "v_mov_b64 v[54:55], s[44:45]\n"
        "v_add_f32 v24, v24, v27\n"
        "v_add_f32 v25, v25, v26\n"
        "v_add_f32 v28, v28, v31\n"
        "v_add_f32 v29, v29, v30\n"
        "v_cvt_pk_bf16_f32 v30, v24, v25\n"
        "v_cvt_pk_bf16_f32 v31, v29, v28\n"
        "s_waitcnt lgkmcnt(3)\n"
        "v_pk_add_f32 v[32:33], v[32:33], v[36:37]\n"
        "v_pk_add_f32 v[34:35], v[34:35], v[38:39]\n"
        "s_waitcnt lgkmcnt(1)\n"
        "v_pk_add_f32 v[40:41], v[40:41], v[44:45]\n"
        "v_pk_add_f32 v[42:43], v[42:43], v[46:47]\n"
        "v_pk_add_f32 v[32:33], v[32:33], v[40:41]\n"
        "v_pk_add_f32 v[34:35], v[34:35], v[42:43]\n"
        "s_waitcnt lgkmcnt(0)\n"
        "v_pk_add_f32 v[48:49], v[48:49], v[50:51]\n"
        "v_max3_f32 v36, v32, v33, v34\n"
        "v_add_f32 v48, v48, v49\n"
        "v_max3_f32 v36, v36, v35, v48\n"
        "v_mul_f32 v48, 0x3e0293ee, v48\n"
        "v_mul_f32 v32, 0x3e0293ee, v32\n"
        "v_max_f32_dpp v37, v36, v36 row_ror:8 row_mask:0xf bank_mask:0xf\n"
        "v_max_f32_dpp v38, v36, v36 row_ror:8 row_mask:0xf bank_mask:0xf\n"
        "v_mul_f32 v33, 0x3e0293ee, v33\n"
        "v_mul_f32 v34, 0x3e0293ee, v34\n"
        "v_permlane16_swap_b32 v37, v38\n"
        "v_max_f32 v36, v37, v38\n"
        "v_max_f32 v39, v37, v38\n"
        "v_mul_f32 v35, 0x3e0293ee, v35\n"
        "v_lshlrev_b32 v6, 3, v3\n"
        "v_permlane32_swap_b32 v36, v39\n"
        "v_max_f32 v36, v36, v39\n"
        "v_fmamk_f32 v48, v36, 0xbe0293ee, v48\n"
        "v_exp_f32 v48, v48\n"
        "v_fmamk_f32 v32, v36, 0xbe0293ee, v32\n"
        "v_fmamk_f32 v33, v36, 0xbe0293ee, v33\n"
        "v_mov_b32_dpp v49, v48 row_ror:8 row_mask:0xf bank_mask:0xf\n"
        "v_exp_f32 v32, v32\n"
        "v_cvt_pk_bf16_f32 v49, v48, v49\n"
        "v_exp_f32 v33, v33\n"
        "v_cndmask_b32 v44, v49, 0, s[58:59]\n"
        "v_cndmask_b32 v45, 0, v49, s[58:59]\n"
        "v_fmamk_f32 v34, v36, 0xbe0293ee, v34\n"
        "v_fmamk_f32 v35, v36, 0xbe0293ee, v35\n"
        "v_mfma_f32_16x16x16_bf16 v[56:59], v[52:53], v[44:45], 0\n"
        "v_exp_f32 v34, v34\n"
        "v_exp_f32 v35, v35\n"
        "v_cvt_pk_bf16_f32 v40, v32, v33\n"
        "v_mfma_f32_16x16x16_bf16 v[60:63], v[30:31], v[44:45], 0\n"
        "v_cvt_pk_bf16_f32 v41, v34, v35\n"
        "v_mov_b32_dpp v42, v40 row_ror:8 row_mask:0xf bank_mask:0xf\n"
        "s_waitcnt vmcnt(2)\n"
        "v_mov_b32_dpp v43, v41 row_ror:8 row_mask:0xf bank_mask:0xf\n"
        "v_swap_b32 v76, v78\n"
        "v_swap_b32 v77, v79\n"
        "v_mfma_f32_16x16x32_bf16 v[56:59], v[52:55], v[40:43], v[56:59]\n"
        "v_mov_b64 v[10:11], 0\n"
        "v_mov_b64 v[12:13], 0\n"
        "v_mfma_f32_16x16x32_bf16 v[32:35], v[72:75], v[40:43], v[60:63]\n"
        "s_mov_b32 exec_hi, 0\n"
        "buffer_load_dword v6, v6, s[40:43], s47 offen offset:12 sc1\n"
        "s_mov_b32 exec_hi, -1\n"
        "v_mfma_f32_16x16x32_bf16 v[48:51], v[76:79], v[40:43], v[60:63]\n"
        "v_rcp_f32 v36, v56\n"
        "v_mov_b64 v[14:15], 0\n"
        "v_mov_b64 v[16:17], 0\n"
        "v_mov_b64 v[18:19], 0\n"
        "v_mul_f32 v32, v32, v36\n"
        "v_mul_f32 v33, v33, v36\n"
        "v_mul_f32 v34, v34, v36\n"
        "v_mul_f32 v35, v35, v36\n"
        "v_mul_f32_dpp v32, v48, v36 quad_perm:[0,1,2,3] row_mask:0xf bank_mask:0xc\n"
        "v_mul_f32_dpp v33, v49, v36 quad_perm:[0,1,2,3] row_mask:0xf bank_mask:0xc\n"
        "v_mul_f32_dpp v34, v50, v36 quad_perm:[0,1,2,3] row_mask:0xf bank_mask:0xc\n"
        "v_mul_f32_dpp v35, v51, v36 quad_perm:[0,1,2,3] row_mask:0xf bank_mask:0xc\n"
        "v_cvt_pk_bf16_f32 v44, v32, v33\n"
        "v_cvt_pk_bf16_f32 v45, v34, v35\n"
        "ds_write_b64 v71, v[44:45]\n"
        "v_and_b32 v7, 0xffffffbf, v1\n"
        "v_cvt_pk_bf16_f32 v24, v24, v25\n"
        "v_cvt_pk_bf16_f32 v26, v28, v29\n"
        "v_mov_b64 v[8:9], 0\n"
        "v_mov_b64 v[20:21], 0\n"
        "v_mov_b64 v[22:23], 0\n"
        "s_waitcnt lgkmcnt(0)\n"
        "s_barrier\n"
        "ds_read_b128 v[28:31], v7 offset:5120\n"
        "ds_read_b128 v[32:35], v7 offset:5184\n"
        "ds_read_b128 v[36:39], v7 offset:6144\n"
        "ds_read_b128 v[40:43], v7 offset:6208\n"
        "v_permlane16_swap_b32 v24, v25\n"
        "v_permlane16_swap_b32 v26, v27\n"
        "buffer_store_dwordx4 v[24:27], v69, s[40:43], s70 offen offset:2048 sc1\n"
        "s_waitcnt lgkmcnt(3)\n"
        "v_smfmac_f32_16x16x64_bf16 v[8:11], v[28:31], v[112:119], v0\n"
        "s_waitcnt lgkmcnt(2)\n"
        "v_smfmac_f32_16x16x64_bf16 v[8:11], v[32:35], v[120:127], v0\n"
        "v_mov_b64 v[24:25], 0\n"
        "s_waitcnt lgkmcnt(1)\n"
        "v_smfmac_f32_16x16x64_bf16 v[8:11], v[36:39], v[128:135], v0\n"
        "v_mov_b64 v[26:27], 0\n"
        "s_waitcnt lgkmcnt(0)\n"
        "v_smfmac_f32_16x16x64_bf16 v[8:11], v[40:43], v[136:143], v0\n"
        "s_barrier\n"
        "v_smfmac_f32_16x16x64_bf16 v[12:15], v[28:31], v[144:151], v0\n"
        "ds_read_b128 v[44:47], v2 offset:32768\n"
        "v_smfmac_f32_16x16x64_bf16 v[12:15], v[32:35], v[152:159], v0\n"
        "ds_read_b128 v[48:51], v2 offset:33792\n"
        "v_cvt_pk_bf16_f32 v8, v8, v9\n"
        "v_smfmac_f32_16x16x64_bf16 v[12:15], v[36:39], v[160:167], v0\n"
        "ds_read_b128 v[52:55], v2 offset:34816\n"
        "v_cvt_pk_bf16_f32 v9, v11, v10\n"
        "v_smfmac_f32_16x16x64_bf16 v[12:15], v[40:43], v[168:175], v0\n"
        "ds_read_b128 v[56:59], v2 offset:35840\n"
        "v_bfi_b32 v8, -2, v8, s51\n"
        "v_smfmac_f32_16x16x64_bf16 v[16:19], v[28:31], v[176:183], v0\n"
        "ds_read_b128 v[60:63], v2 offset:36864\n"
        "v_bfi_b32 v9, -2, v9, s51\n"
        "v_smfmac_f32_16x16x64_bf16 v[16:19], v[32:35], v[184:191], v0\n"
        "v_cvt_pk_bf16_f32 v10, v12, v13\n"
        "v_cvt_pk_bf16_f32 v11, v15, v14\n"
        "v_bfi_b32 v10, -2, v10, s51\n"
        "v_bfi_b32 v11, -2, v11, s51\n"
        "buffer_store_dwordx4 v[8:11], v70, s[40:43], s73 offen sc1\n"
        "v_smfmac_f32_16x16x64_bf16 v[16:19], v[36:39], v[192:199], v0\n"
        "ds_read_b128 v[64:67], v2 offset:37888\n"
        "v_smfmac_f32_16x16x64_bf16 v[16:19], v[40:43], v[200:207], v0\n"
        "ds_read_b128 v[72:75], v2 offset:38912\n"
        "v_smfmac_f32_16x16x64_bf16 v[20:23], v[28:31], v[208:215], v0\n"
        "ds_read_b128 v[76:79], v2 offset:39936\n"
        "v_smfmac_f32_16x16x64_bf16 v[20:23], v[32:35], v[216:223], v0\n"
        "v_cvt_pk_bf16_f32 v16, v16, v17\n"
        "v_smfmac_f32_16x16x64_bf16 v[20:23], v[36:39], v[224:231], v0\n"
        "s_cmp_lt_u32 s48, 2\n"
        "v_cvt_pk_bf16_f32 v17, v19, v18\n"
        "v_smfmac_f32_16x16x64_bf16 v[20:23], v[40:43], v[232:239], v0\n"
        "v_bfi_b32 v16, -2, v16, s51\n"
        "s_cbranch_scc0 .Lch_R23_%=\n"
        "s_waitcnt lgkmcnt(0)\n"
        "v_smfmac_f32_16x16x64_bf16 v[24:27], v[28:31], v[44:51], v0\n"
        "v_bfi_b32 v17, -2, v17, s51\n"
        "v_smfmac_f32_16x16x64_bf16 v[24:27], v[32:35], v[52:59], v0\n"
        "v_cvt_pk_bf16_f32 v18, v20, v21\n"
        "v_cvt_pk_bf16_f32 v19, v23, v22\n"
        "v_bfi_b32 v18, -2, v18, s51\n"
        "v_bfi_b32 v19, -2, v19, s51\n"
        "buffer_store_dwordx4 v[16:19], v70, s[40:43], s74 offen sc1\n"
        "v_smfmac_f32_16x16x64_bf16 v[24:27], v[36:39], v[60:67], v0\n"
        "v_smfmac_f32_16x16x64_bf16 v[24:27], v[40:43], v[72:79], v0\n"
        "v_add_u32 v12, 114688, v3\n"
        "ds_read_b128 v[44:47], v12\n"
        "ds_read_b32 v50, v12 offset:1024\n"
        "s_waitcnt vmcnt(3)\n"
        "s_nop 3\n"
        "v_cvt_pk_bf16_f32 v24, v24, v25\n"
        "v_cvt_pk_bf16_f32 v25, v27, v26\n"
        "v_bfi_b32 v24, -2, v24, s51\n"
        "v_bfi_b32 v25, -2, v25, s51\n"
        "s_nop 0\n"
        "v_permlane32_swap_b32 v24, v26\n"
        "v_permlane32_swap_b32 v25, v27\n"
        "s_mov_b32 exec_hi, 0\n"
        "buffer_store_dwordx4 v[24:27], v70, s[40:43], s75 offen sc1\n"
        "s_memrealtime s[90:91]\n"
        "s_cmp_eq_u32 s88, 0\n"
        "s_cbranch_scc1 .Lch_R_%=\n"
        "s_mov_b32 exec_hi, -1\n"
        "s_waitcnt lgkmcnt(0)\n"
        "s_sub_u32 s65, s90, s36\n"
        "s_min_u32 s65, s65, 120\n"
        "s_lshl_b32 s65, s65, 3\n"
        "s_cmp_eq_u32 s51, 0\n"
        "s_cselect_b32 s38, s65, s38\n"
        "s_sub_u32 s65, s65, s38\n"
        "s_ashr_i32 s65, s65, 2\n"
        "s_add_u32 s38, s38, s65\n"
        "s_lshr_b32 s35, s38, 3\n"
        "s_waitcnt vmcnt(3)\n"
        "v_mov_b32 v12, s51\n"
        "s_mov_b64 exec, 1\n"
        "buffer_store_dword v12, off, s[40:43], s79 sc1\n"
        "s_mov_b64 exec, -1\n"
        "s_mov_b32 s65, s63\n"
        ".Lch_d4_%=:\n"
        "s_sub_u32 s65, s65, 1\n"
        "s_cbranch_scc0 .Lch_d4_%=\n"
        ".Lch_idle2_%=:\n"
        "s_mov_b64 exec, 1\n"
        "buffer_load_dword v7, off, s[40:43], s78 sc1\n"
        "s_mov_b64 exec, -1\n"
        "s_branch .Lch_nog_%=\n"
        ".Lch_R23_%=:\n"
        "s_waitcnt vmcnt(2)\n"
        "v_bfi_b32 v17, -2, v17, s51\n"
        "s_nop 3\n"
        "v_cvt_pk_bf16_f32 v18, v20, v21\n"
        "v_cvt_pk_bf16_f32 v19, v23, v22\n"
        "v_bfi_b32 v18, -2, v18, s51\n"
        "v_bfi_b32 v19, -2, v19, s51\n"
        "buffer_store_dwordx4 v[16:19], v70, s[40:43], s74 offen sc1\n"
        "s_memrealtime s[90:91]\n"
        "s_mov_b32 exec_hi, 0\n"
        ".Lch_R_%=:\n"
        ".Lch_st1_%=:\n"
        "v_cmp_eq_u32 vcc, -1, v6\n"
        "s_cbranch_vccnz .Lch_stwait_%=\n"
        ".Lch_st2_%=:\n"
        "s_waitcnt lgkmcnt(0)\n"
        "s_sub_u32 s65, s90, s60\n"
        "s_memrealtime s[90:91]\n"
        "v_sub_u32 v12, s65, v6\n"
        "v_cmp_lt_u32 vcc, 0xfffffc18, v12\n"
        "s_cbranch_vccnz .Lch_st2_%=\n"
        "s_mov_b32 exec_hi, -1\n"
        ".Lch_rl_%=:\n"
        "buffer_load_dwordx4 v[28:31], v3, s[40:43], s76 offen offset:0 sc1\n"
        "buffer_load_dwordx4 v[32:35], v3, s[40:43], s76 offen offset:1024 sc1\n"
        "buffer_load_dwordx4 v[36:39], v3, s[40:43], s76 offen offset:2048 sc1\n"
        "buffer_load_dwordx4 v[40:43], v3, s[40:43], s76 offen offset:3072 sc1\n"
        "v_mov_b32 v10, s51\n"
        "s_waitcnt vmcnt(6)\n"
        "s_mov_b64 exec, 1\n"
        "buffer_store_dword v10, off, s[40:43], s79 sc1\n"
        "s_mov_b64 exec, -1\n"
        "v_subrev_u32 v8, s36, v6\n"
        "v_subrev_u32 v9, s36, v6\n"
        "s_nop 0\n"
        "v_max_i32_dpp v8, v8, v8 row_ror:8 row_mask:0xf bank_mask:0xf\n"
        "v_min_i32_dpp v9, v9, v9 row_ror:8 row_mask:0xf bank_mask:0xf\n"
        "s_nop 0\n"
        "v_max_i32_dpp v8, v8, v8 row_ror:4 row_mask:0xf bank_mask:0xf\n"
        "v_min_i32_dpp v9, v9, v9 row_ror:4 row_mask:0xf bank_mask:0xf\n"
        "s_nop 0\n"
        "v_max_i32_dpp v8, v8, v8 row_ror:2 row_mask:0xf bank_mask:0xf\n"
        "v_min_i32_dpp v9, v9, v9 row_ror:2 row_mask:0xf bank_mask:0xf\n"
        "s_nop 0\n"
        "v_max_i32_dpp v8, v8, v8 row_ror:1 row_mask:0xf bank_mask:0xf\n"
        "v_min_i32_dpp v9, v9, v9 row_ror:1 row_mask:0xf bank_mask:0xf\n"
        "s_waitcnt lgkmcnt(0)\n"
        "v_readlane_b32 s65, v8, 0\n"
        "v_readlane_b32 s66, v8, 16\n"
        "v_readlane_b32 s90, v9, 0\n"
        "v_readlane_b32 s91, v9, 16\n"
        "s_max_i32 s65, s65, s66\n"
        "s_min_i32 s90, s90, s91\n"
        "s_sub_u32 s90, s65, s90\n"
        "s_add_u32 s65, s65, s36\n"
        "s_add_u32 s65, s65, s34\n"
        "s_cmp_gt_u32 s90, s56\n"
        "s_cselect_b32 s90, s57, 0\n"
        "s_add_u32 s82, s65, s90\n"
        "s_and_b32 s71, s51, 3\n"
        "s_mul_i32 s71, s71, 81920\n"
        "s_add_u32 s39, s71, s85\n"
        "v_mov_b64 v[64:65], -1\n"
        "v_mov_b64 v[66:67], -1\n"
        "s_waitcnt vmcnt(4)\n"
        "v_mfma_f32_16x16x32_bf16 v[12:15], v[28:31], v[44:47], 0\n"
        "v_bitop3_b32 v10, v28, v29, s51 bitop3:0x7e\n"
        "v_bitop3_b32 v11, v30, v31, s51 bitop3:0x7e\n"
        "v_or_b32 v8, v10, v11\n"
        "s_waitcnt vmcnt(3)\n"
        "v_mfma_f32_16x16x32_bf16 v[12:15], v[32:35], v[44:47], v[12:15]\n"
        "v_bitop3_b32 v10, v32, v33, s51 bitop3:0x7e\n"
        "v_bitop3_b32 v11, v34, v35, s51 bitop3:0x7e\n"
        "v_or3_b32 v8, v8, v10, v11\n"
        "s_waitcnt vmcnt(2)\n"
        "v_mfma_f32_16x16x32_bf16 v[12:15], v[36:39], v[44:47], v[12:15]\n"
        "v_bitop3_b32 v10, v36, v37, s51 bitop3:0x7e\n"
        "v_bitop3_b32 v11, v38, v39, s51 bitop3:0x7e\n"
        "v_or3_b32 v8, v8, v10, v11\n"
        "s_waitcnt vmcnt(1)\n"
        "v_mfma_f32_16x16x32_bf16 v[12:15], v[40:43], v[44:47], v[12:15]\n"
        "v_bitop3_b32 v10, v40, v41, s51 bitop3:0x7e\n"
        "v_bitop3_b32 v11, v42, v43, s51 bitop3:0x7e\n"
        "v_or3_b32 v8, v8, v10, v11\n"
        "v_and_b32 v8, 1, v8\n"
        "v_cmp_ne_u32 vcc, 0, v8\n"
        "s_cbranch_vccnz .Lch_rretry_%=\n"
        "s_nop 1\n"
        "v_permlane32_swap_b32 v12, v14\n"
        "v_permlane32_swap_b32 v13, v15\n"
        "v_add_f32 v12, v12, v14\n"
        "v_add_f32 v13, v13, v15\n"
        "v_cvt_pk_bf16_f32 v16, v12, v13\n"
        "v_cvt_pk_bf16_f32 v18, v12, v13\n"
        "s_nop 1\n"
        "v_permlane16_swap_b32 v16, v18\n"
        "s_nop 1\n"
        "v_permlane32_swap_b32 v16, v17\n"
        "v_permlane32_swap_b32 v18, v19\n"
        "buffer_store_dwordx4 v[16:19], v50, s[40:43], s39 offen sc1\n"
        "buffer_store_dwordx4 v[64:67], v50, s[40:43], s72 offen sc1\n"
        "s_mov_b64 exec, 1\n"
        "buffer_load_dword v7, off, s[40:43], s78 sc1\n"
        "s_mov_b64 exec, -1\n"
        "s_mov_b32 s72, s39\n"
        "v_mov_b32 v32, v49\n"
        "s_cmp_eq_u32 s48, 2\n"
        "s_cbranch_scc0 .Lch_nog_%=\n"
        "s_and_b32 s66, s33, 1024\n"
        "s_lshl_b32 s66, s66, 2\n"
        "s_add_u32 m0, s66, 151552\n"
        "s_add_u32 s66, s54, s71\n"
        ".Lch_gwa_%=:\n"
        "s_memrealtime s[90:91]\n"
        "s_waitcnt lgkmcnt(0)\n"
        "s_sub_u32 s65, s90, s82\n"
        "s_cmp_gt_u32 s65, -1000\n"
        "s_cbranch_scc1 .Lch_gwa_%=\n"
        "s_mov_b32 s65, s61\n"
        ".Lch_d2a_%=:\n"
        "s_sub_u32 s65, s65, 1\n"
        "s_cbranch_scc0 .Lch_d2a_%=\n"
        "buffer_load_dwordx4 v3, s[40:43], s66 offen offset:0 sc1 lds\n"
        "buffer_load_dwordx4 v3, s[40:43], s66 offen offset:1024 sc1 lds\n"
        "buffer_load_dwordx4 v3, s[40:43], s66 offen offset:2048 sc1 lds\n"
        "buffer_load_dwordx4 v3, s[40:43], s66 offen offset:3072 sc1 lds\n"
        "s_add_u32 s51, s51, 1\n"
        "s_add_u32 s80, s80, 2\n"
        "s_bitcmp1_b64 s[52:53], s51\n"
        "s_cselect_b32 s65, 8192, 0\n"
        "s_lshl_b32 s68, s51, 21\n"
        "s_add_u32 s68, s68, s65\n"
        "s_add_u32 s68, s68, 2097152\n"
        "s_add_u32 s70, s68, s84\n"
        "s_add_u32 s69, s68, s83\n"
        "s_add_u32 s47, s68, 2068480\n"
        "s_add_u32 s46, s47, s62\n"
        "ds_read_b128 v[40:43], v2 offset:0\n"
        "ds_read_b128 v[44:47], v2 offset:1024\n"
        "ds_read_b128 v[48:51], v2 offset:2048\n"
        "ds_read_b128 v[52:55], v2 offset:3072\n"
        "ds_read_b128 v[56:59], v2 offset:4096\n"
        "ds_read_b128 v[60:63], v2 offset:5120\n"
        "v_mov_b64 v[8:9], 0\n"
        "v_mov_b64 v[10:11], 0\n"
        "v_mov_b64 v[12:13], 0\n"
        "v_mov_b64 v[14:15], 0\n"
        "v_mov_b64 v[16:17], 0\n"
        "v_mov_b64 v[18:19], 0\n"
        "v_mov_b64 v[20:21], 0\n"
        "v_mov_b64 v[22:23], 0\n"
        "v_mov_b64 v[24:25], 0\n"
        "v_mov_b64 v[26:27], 0\n"
        "v_mov_b64 v[28:29], 0\n"
        "v_mov_b64 v[30:31], 0\n"
        "v_mov_b32 v6, 0\n"
        "s_waitcnt vmcnt(4)\n"
        "v_readfirstlane_b32 s65, v7\n"
        "s_cmp_eq_u32 s51, 63\n"
        "s_cselect_b32 s65, -2, s65\n"
        "s_add_u32 s89, s69, 524288\n"
        "s_mov_b32 s30, s69\n"
        "s_mov_b32 s77, s89\n"
        "s_cmp_ge_i32 s65, s80\n"
        "s_cbranch_scc1 .Lch_fasta_%=\n"
        "s_mov_b32 s30, 2147483648\n"
        "s_mov_b32 s89, 2147483648\n"
        "s_sub_u32 s77, s78, 1024\n"
        "v_mov_b32 v7, s51\n"
        "ds_write_b32 v4, v7 offset:11264\n"
        ".Lch_fasta_%=:\n"
        "s_waitcnt vmcnt(0)\n"
        "s_branch .Lch_A_%=\n"
        ".Lch_nog_%=:\n"
        "s_add_u32 s51, s51, 1\n"
        "s_add_u32 s80, s80, 2\n"
        "s_bitcmp1_b64 s[52:53], s51\n"
        "s_cselect_b32 s65, 8192, 0\n"
        "s_lshl_b32 s68, s51, 21\n"
        "s_add_u32 s68, s68, s65\n"
        "s_add_u32 s68, s68, 2097152\n"
        "s_add_u32 s70, s68, s84\n"
        "s_add_u32 s69, s68, s83\n"
        "s_add_u32 s47, s68, 2068480\n"
        "s_add_u32 s46, s47, s62\n"
        "ds_read_b128 v[40:43], v2 offset:0\n"
        "ds_read_b128 v[44:47], v2 offset:1024\n"
        "ds_read_b128 v[48:51], v2 offset:2048\n"
        "ds_read_b128 v[52:55], v2 offset:3072\n"
        "ds_read_b128 v[56:59], v2 offset:4096\n"
        "ds_read_b128 v[60:63], v2 offset:5120\n"
        "v_mov_b64 v[8:9], 0\n"
        "v_mov_b64 v[10:11], 0\n"
        "v_mov_b64 v[12:13], 0\n"
        "v_mov_b64 v[14:15], 0\n"
        "v_mov_b64 v[16:17], 0\n"
        "v_mov_b64 v[18:19], 0\n"
        "v_mov_b64 v[20:21], 0\n"
        "v_mov_b64 v[22:23], 0\n"
        "v_mov_b64 v[24:25], 0\n"
        "v_mov_b64 v[26:27], 0\n"
        "v_mov_b64 v[28:29], 0\n"
        "v_mov_b64 v[30:31], 0\n"
        "v_mov_b32 v6, 0\n"
        "s_waitcnt vmcnt(0)\n"
        "v_readfirstlane_b32 s65, v7\n"
        "s_cmp_eq_u32 s51, 63\n"
        "s_cselect_b32 s65, -2, s65\n"
        "s_add_u32 s89, s69, 524288\n"
        "s_mov_b32 s30, s69\n"
        "s_mov_b32 s77, s89\n"
        "s_cmp_ge_i32 s65, s80\n"
        "s_cbranch_scc1 .Lch_fastb_%=\n"
        "s_mov_b32 s30, 2147483648\n"
        "s_mov_b32 s89, 2147483648\n"
        "s_sub_u32 s77, s78, 1024\n"
        "v_mov_b32 v7, s51\n"
        "ds_write_b32 v4, v7 offset:11264\n"
        ".Lch_fastb_%=:\n"
        "s_cmp_eq_u32 s48, 3\n"
        "s_cselect_b32 s65, s51, 0\n"
        "s_cmp_eq_u32 s65, 0\n"
        "s_cbranch_scc1 .Lch_A_%=\n"
        "s_and_b32 s66, s33, 1024\n"
        "s_lshl_b32 s66, s66, 2\n"
        "s_add_u32 m0, s66, 151552\n"
        "s_add_u32 s66, s54, s71\n"
        ".Lch_gwb_%=:\n"
        "s_memrealtime s[90:91]\n"
        "s_waitcnt lgkmcnt(0)\n"
        "s_sub_u32 s65, s90, s82\n"
        "s_cmp_gt_u32 s65, -1000\n"
        "s_cbranch_scc1 .Lch_gwb_%=\n"
        "s_mov_b32 s65, s61\n"
        ".Lch_d2b_%=:\n"
        "s_sub_u32 s65, s65, 1\n"
        "s_cbranch_scc0 .Lch_d2b_%=\n"
        "buffer_load_dwordx4 v3, s[40:43], s66 offen offset:0 sc1 lds\n"
        "buffer_load_dwordx4 v3, s[40:43], s66 offen offset:1024 sc1 lds\n"
        "buffer_load_dwordx4 v3, s[40:43], s66 offen offset:2048 sc1 lds\n"
        "buffer_load_dwordx4 v3, s[40:43], s66 offen offset:3072 sc1 lds\n"
        "s_mov_b32 m0, 159744\n"
        "s_add_u32 s66, s71, s55\n"
        "buffer_load_dwordx4 v32, s[40:43], s66 offen sc1 lds\n"
        ".Lch_A_%=:\n"
        "s_waitcnt lgkmcnt(0)\n"
        "s_barrier\n"
        "s_branch .Lch_Q_%=\n"
        ".Lch_rretry_%=:\n"
        "s_waitcnt vmcnt(0)\n"
        "s_add_u32 s64, s64, 1\n"
        "s_and_b32 s65, s64, 255\n"
        "s_cbranch_scc1 .Lch_rr_go_%=\n"
        "s_mov_b32 s65, 49152\n"
        "s_cmp_lt_u32 s64, 1048576\n"
        "s_cbranch_scc1 .Lch_rr_chk_%=\n"
        "v_mov_b32 v8, 1\n"
        "buffer_store_dword v8, off, s[40:43], s65 sc1\n"
        "s_waitcnt vmcnt(0)\n"
        ".Lch_rr_chk_%=:\n"
        "buffer_load_dword v8, off, s[40:43], s65 sc1\n"
        "s_waitcnt vmcnt(0)\n"
        "v_readfirstlane_b32 s65, v8\n"
        "s_nop 0\n"
        "s_cmp_lg_u32 s65, 1\n"
        "s_cbranch_scc1 .Lch_rr_go_%=\n"
        "s_endpgm\n"
        ".Lch_rr_go_%=:\n"
        "s_branch .Lch_rl_%=\n"
        ".Lch_stwait_%=:\n"
        "s_add_u32 s64, s64, 1\n"
        "s_and_b32 s65, s64, 255\n"
        "s_cbranch_scc1 .Lch_sw_go_%=\n"
        "s_mov_b32 s65, 49152\n"
        "s_cmp_lt_u32 s64, 1048576\n"
        "s_cbranch_scc1 .Lch_sw_chk_%=\n"
        "v_mov_b32 v12, 1\n"
        "buffer_store_dword v12, off, s[40:43], s65 sc1\n"
        "s_waitcnt vmcnt(0)\n"
        ".Lch_sw_chk_%=:\n"
        "buffer_load_dword v12, off, s[40:43], s65 sc1\n"
        "s_waitcnt vmcnt(0)\n"
        "v_readfirstlane_b32 s65, v12\n"
        "s_nop 0\n"
        "s_cmp_lg_u32 s65, 1\n"
        "s_cbranch_scc1 .Lch_sw_go_%=\n"
        "s_endpgm\n"
        ".Lch_sw_go_%=:\n"
        "s_sleep 1\n"
        "v_lshlrev_b32 v6, 3, v3\n"
        "buffer_load_dword v6, v6, s[40:43], s47 offen offset:12 sc1\n"
        "s_waitcnt vmcnt(0)\n"
        "s_branch .Lch_st1_%=\n"
        ".Lch_rt0_%=:\n"
        "s_waitcnt lgkmcnt(0)\n"
        "s_barrier\n"
        "s_add_u32 s64, s64, 1\n"
        "s_and_b32 s65, s64, 255\n"
        "s_cbranch_scc1 .Lch_rt0_go_%=\n"
        "s_mov_b32 s65, 49152\n"
        "s_cmp_lt_u32 s64, 1048576\n"
        "s_cbranch_scc1 .Lch_rt0_chk_%=\n"
        "v_mov_b32 v7, 1\n"
        "buffer_store_dword v7, off, s[40:43], s65 sc1\n"
        "s_waitcnt vmcnt(0)\n"
        ".Lch_rt0_chk_%=:\n"
        "buffer_load_dword v7, off, s[40:43], s65 sc1\n"
        "s_waitcnt vmcnt(0)\n"
        "v_readfirstlane_b32 s65, v7\n"
        "s_nop 0\n"
        "s_cmp_lg_u32 s65, 1\n"
        "s_cbranch_scc1 .Lch_rt0_go_%=\n"
        "s_endpgm\n"
        ".Lch_rt0_go_%=:\n"
        "s_cmp_eq_u32 s48, 2\n"
        "s_cbranch_scc0 .Lch_rw0_%=\n"
        "s_and_b32 s66, s33, 1024\n"
        "s_lshl_b32 s66, s66, 2\n"
        "s_add_u32 m0, s66, 151552\n"
        "s_add_u32 s66, s54, s71\n"
        "buffer_load_dwordx4 v3, s[40:43], s66 offen offset:0 sc1 lds\n"
        "buffer_load_dwordx4 v3, s[40:43], s66 offen offset:1024 sc1 lds\n"
        "buffer_load_dwordx4 v3, s[40:43], s66 offen offset:2048 sc1 lds\n"
        "buffer_load_dwordx4 v3, s[40:43], s66 offen offset:3072 sc1 lds\n"
        "s_waitcnt vmcnt(0)\n"
        ".Lch_rw0_%=:\n"
        "s_waitcnt vmcnt(0)\n"
        "ds_read_b128 v[40:43], v2 offset:0\n"
        "ds_read_b128 v[44:47], v2 offset:1024\n"
        "ds_read_b128 v[48:51], v2 offset:2048\n"
        "ds_read_b128 v[52:55], v2 offset:3072\n"
        "ds_read_b128 v[56:59], v2 offset:4096\n"
        "ds_read_b128 v[60:63], v2 offset:5120\n"
        "v_mov_b64 v[8:9], 0\n"
        "v_mov_b64 v[10:11], 0\n"
        "v_mov_b64 v[12:13], 0\n"
        "v_mov_b64 v[14:15], 0\n"
        "v_mov_b64 v[16:17], 0\n"
        "v_mov_b64 v[18:19], 0\n"
        "v_mov_b64 v[20:21], 0\n"
        "v_mov_b64 v[22:23], 0\n"
        "v_mov_b64 v[24:25], 0\n"
        "v_mov_b64 v[26:27], 0\n"
        "v_mov_b64 v[28:29], 0\n"
        "v_mov_b64 v[30:31], 0\n"
        "v_mov_b32 v6, 0\n"
        "s_branch .Lch_A_%=\n"
        ".Lch_rt1_%=:\n"
        "s_waitcnt lgkmcnt(0)\n"
        "s_barrier\n"
        "s_add_u32 s64, s64, 1\n"
        "s_and_b32 s65, s64, 255\n"
        "s_cbranch_scc1 .Lch_rt1_go_%=\n"
        "s_mov_b32 s65, 49152\n"
        "s_cmp_lt_u32 s64, 1048576\n"
        "s_cbranch_scc1 .Lch_rt1_chk_%=\n"
        "v_mov_b32 v7, 1\n"
        "buffer_store_dword v7, off, s[40:43], s65 sc1\n"
        "s_waitcnt vmcnt(0)\n"
        ".Lch_rt1_chk_%=:\n"
        "buffer_load_dword v7, off, s[40:43], s65 sc1\n"
        "s_waitcnt vmcnt(0)\n"
        "v_readfirstlane_b32 s65, v7\n"
        "s_nop 0\n"
        "s_cmp_lg_u32 s65, 1\n"
        "s_cbranch_scc1 .Lch_rt1_go_%=\n"
        "s_endpgm\n"
        ".Lch_rt1_go_%=:\n"
        "s_cmp_eq_u32 s48, 3\n"
        "s_cbranch_scc0 .Lch_rw1_%=\n"
        "s_and_b32 s66, s33, 1024\n"
        "s_lshl_b32 s66, s66, 2\n"
        "s_add_u32 m0, s66, 151552\n"
        "s_add_u32 s66, s54, s71\n"
        "buffer_load_dwordx4 v3, s[40:43], s66 offen offset:0 sc1 lds\n"
        "buffer_load_dwordx4 v3, s[40:43], s66 offen offset:1024 sc1 lds\n"
        "buffer_load_dwordx4 v3, s[40:43], s66 offen offset:2048 sc1 lds\n"
        "buffer_load_dwordx4 v3, s[40:43], s66 offen offset:3072 sc1 lds\n"
        "s_waitcnt vmcnt(0)\n"
        ".Lch_rw1_%=:\n"
        "s_barrier\n"
        "ds_read_b32 v7, v5 offset:4096\n"
        "ds_read_b128 v[32:35], v1 offset:4096\n"
        "s_waitcnt lgkmcnt(0)\n"
        "v_cmp_eq_u32 vcc, -1, v7\n"
        "s_cbranch_vccnz .Lch_rt1_%=\n"
        "s_branch .Lch_ok1_%=\n"
        ".Lch_rt2_%=:\n"
        "s_waitcnt lgkmcnt(0)\n"
        "s_barrier\n"
        "s_add_u32 s64, s64, 1\n"
        "s_and_b32 s65, s64, 255\n"
        "s_cbranch_scc1 .Lch_rt2_go_%=\n"
        "s_mov_b32 s65, 49152\n"
        "s_cmp_lt_u32 s64, 1048576\n"
        "s_cbranch_scc1 .Lch_rt2_chk_%=\n"
        "v_mov_b32 v7, 1\n"
        "buffer_store_dword v7, off, s[40:43], s65 sc1\n"
        "s_waitcnt vmcnt(0)\n"
        ".Lch_rt2_chk_%=:\n"
        "buffer_load_dword v7, off, s[40:43], s65 sc1\n"
        "s_waitcnt vmcnt(0)\n"
        "v_readfirstlane_b32 s65, v7\n"
        "s_nop 0\n"
        "s_cmp_lg_u32 s65, 1\n"
        "s_cbranch_scc1 .Lch_rt2_go_%=\n"
        "s_endpgm\n"
        ".Lch_rt2_go_%=:\n"
        "s_cmp_eq_u32 s48, 3\n"
        "s_cbranch_scc0 .Lch_rw2_%=\n"
        "ds_read_b32 v7, v2 offset:33796\n"
        "s_waitcnt lgkmcnt(0)\n"
        "s_mov_b32 m0, 159744\n"
        "s_add_u32 s66, s71, s55\n"
        "buffer_load_dwordx4 v7, s[40:43], s66 offen sc1 lds\n"
        "s_waitcnt vmcnt(0)\n"
        ".Lch_rw2_%=:\n"
        "s_barrier\n"
        "ds_read_b32 v7, v5 offset:8192\n"
        "ds_read_b128 v[48:51], v1 offset:8192\n"
        "s_memrealtime s[36:37]\n"
        "s_waitcnt lgkmcnt(0)\n"
        "v_cmp_eq_u32 vcc, -1, v7\n"
        "s_nop 0\n"
        "s_and_b64 vcc, vcc, 0xffff\n"
        "s_cbranch_vccnz .Lch_rt2_%=\n"
        "s_branch .Lch_ok2_%=\n"
        ".Lch_redo_%=:\n"
        "s_waitcnt vmcnt(0) lgkmcnt(0)\n"
        "s_barrier\n"
        "s_add_u32 s64, s64, 1\n"
        "s_and_b32 s65, s64, 255\n"
        "s_cbranch_scc1 .Lch_rd_go_%=\n"
        "s_mov_b32 s65, 49152\n"
        "s_cmp_lt_u32 s64, 1048576\n"
        "s_cbranch_scc1 .Lch_rd_chk_%=\n"
        "v_mov_b32 v7, 1\n"
        "buffer_store_dword v7, off, s[40:43], s65 sc1\n"
        "s_waitcnt vmcnt(0)\n"
        ".Lch_rd_chk_%=:\n"
        "buffer_load_dword v7, off, s[40:43], s65 sc1\n"
        "s_waitcnt vmcnt(0)\n"
        "v_readfirstlane_b32 s65, v7\n"
        "s_nop 0\n"
        "s_cmp_lg_u32 s65, 1\n"
        "s_cbranch_scc1 .Lch_rd_go_%=\n"
        "s_endpgm\n"
        ".Lch_rd_go_%=:\n"
        "s_cmp_lt_u32 s48, 2\n"
        "s_cbranch_scc1 .Lch_rd2_%=\n"
        "s_and_b32 s66, s33, 1024\n"
        "s_lshl_b32 s66, s66, 2\n"
        "s_add_u32 m0, s66, 151552\n"
        "s_add_u32 s66, s54, s71\n"
        "buffer_load_dwordx4 v3, s[40:43], s66 offen offset:0 sc1 lds\n"
        "buffer_load_dwordx4 v3, s[40:43], s66 offen offset:1024 sc1 lds\n"
        "buffer_load_dwordx4 v3, s[40:43], s66 offen offset:2048 sc1 lds\n"
        "buffer_load_dwordx4 v3, s[40:43], s66 offen offset:3072 sc1 lds\n"
        "ds_read_b32 v7, v2 offset:33796\n"
        "s_waitcnt lgkmcnt(0)\n"
        "s_mov_b32 m0, 159744\n"
        "s_add_u32 s66, s71, s55\n"
        "buffer_load_dwordx4 v7, s[40:43], s66 offen sc1 lds\n"
        ".Lch_rd2_%=:\n"
        "s_nop 7\n"
        "ds_read_b128 v[40:43], v2 offset:0\n"
        "ds_read_b128 v[44:47], v2 offset:1024\n"
        "ds_read_b128 v[48:51], v2 offset:2048\n"
        "ds_read_b128 v[52:55], v2 offset:3072\n"
        "ds_read_b128 v[56:59], v2 offset:4096\n"
        "ds_read_b128 v[60:63], v2 offset:5120\n"
        "v_mov_b64 v[8:9], 0\n"
        "v_mov_b64 v[10:11], 0\n"
        "v_mov_b64 v[12:13], 0\n"
        "v_mov_b64 v[14:15], 0\n"
        "v_mov_b64 v[16:17], 0\n"
        "v_mov_b64 v[18:19], 0\n"
        "v_mov_b64 v[20:21], 0\n"
        "v_mov_b64 v[22:23], 0\n"
        "v_mov_b64 v[24:25], 0\n"
        "v_mov_b64 v[26:27], 0\n"
        "v_mov_b64 v[28:29], 0\n"
        "v_mov_b64 v[30:31], 0\n"
        "v_mov_b32 v6, 0\n"
        "s_waitcnt vmcnt(0)\n"
        "s_branch .Lch_A_%=\n"
        ".Lch_slow_%=:\n"
        "v_max3_u32 v6, v6, v38, v39\n"
        "v_max3_u32 v6, v6, v48, v49\n"
        "v_max3_u32 v6, v6, v50, v51\n"
        "v_cmp_eq_u32 vcc, -1, v6\n"
        "s_cbranch_vccnz .Lch_redo_%=\n"
        "s_waitcnt vmcnt(0) lgkmcnt(0)\n"
        "s_cmp_eq_u32 s30, s69\n"
        "s_cbranch_scc1 .Lch_slow3_%=\n"
        "v_readfirstlane_b32 s65, v76\n"
        "s_cmp_eq_u32 s51, 63\n"
        "s_cselect_b32 s65, -2, s65\n"
        "s_cmp_ge_i32 s65, s80\n"
        "s_cbranch_scc1 .Lch_kvw_ok_%=\n"
        "ds_read_b128 v[68:71], v4 offset:10240\n"
        "v_mov_b64 v[72:73], v[24:25]\n"
        "v_mov_b64 v[74:75], v[26:27]\n"
        "v_mov_b64 v[76:77], v[28:29]\n"
        "v_mov_b64 v[78:79], v[30:31]\n"
        "v_cvt_pk_bf16_f32 v64, v12, v13\n"
        "v_cvt_pk_bf16_f32 v65, v14, v15\n"
        "v_cvt_pk_bf16_f32 v66, v16, v17\n"
        "v_cvt_pk_bf16_f32 v67, v18, v19\n"
        "v_smfmac_f32_16x16x64_bf16 v[72:75], v[36:39], v[56:63], v0\n"
        "v_smfmac_f32_16x16x64_bf16 v[76:79], v[36:39], v[40:47], v0\n"
        "v_smfmac_f32_16x16x64_bf16 v[72:75], v[48:51], v[240:247], v0\n"
        "v_smfmac_f32_16x16x64_bf16 v[76:79], v[48:51], v[248:255], v0\n"
        "s_waitcnt lgkmcnt(0)\n"
        "buffer_store_dwordx4 v[64:67], v68, s[40:43], s70 offen sc1\n"
        "s_nop 5\n"
        "v_add_f32 v72, v72, v75\n"
        "v_add_f32 v73, v73, v74\n"
        "v_add_f32 v76, v76, v79\n"
        "v_add_f32 v77, v77, v78\n"
        "v_cvt_pk_bf16_f32 v72, v72, v73\n"
        "v_cvt_pk_bf16_f32 v74, v76, v77\n"
        "s_nop 1\n"
        "v_permlane16_swap_b32 v72, v73\n"
        "v_permlane16_swap_b32 v74, v75\n"
        "buffer_store_dwordx4 v[72:75], v69, s[40:43], s70 offen offset:2048 sc1\n"
        "s_mov_b64 exec, 1\n"
        "buffer_load_dword v64, off, s[40:43], s78 sc1\n"
        "s_mov_b64 exec, -1\n"
        "s_waitcnt vmcnt(0)\n"
        "v_mov_b32 v7, s51\n"
        "s_mov_b64 exec, 1\n"
        "buffer_store_dword v7, off, s[40:43], s79 sc1\n"
        "s_mov_b64 exec, -1\n"
        "s_cmp_eq_u32 s51, 63\n"
        "s_cbranch_scc0 .Lch_slow2_%=\n"
        "s_or_b32 s65, s49, s48\n"
        "s_cbranch_scc1 .Lch_bye_%=\n"
        "s_lshl_b32 s66, s50, 7\n"
        "s_add_u32 s66, s66, 51200\n"
        ".Lch_bye_%=:\n"
        "s_endpgm\n"
        ".Lch_slow2_%=:\n"
        "s_mov_b32 s70, 241172480\n"
        "v_readfirstlane_b32 s65, v64\n"
        "s_nop 0\n"
        "s_cmp_ge_i32 s65, s80\n"
        "s_cbranch_scc1 .Lch_kvw_ok_%=\n"
        "s_mov_b32 s64, 0\n"
        ".Lch_kvw_top_%=:\n"
        "s_mov_b64 exec, 1\n"
        "buffer_load_dword v7, off, s[40:43], s78 sc1\n"
        "s_mov_b64 exec, -1\n"
        "s_waitcnt vmcnt(0)\n"
        "v_cmp_gt_i32 vcc, s80, v7\n"
        "s_nop 0\n"
        "s_and_b64 vcc, vcc, 1\n"
        "s_cbranch_vccz .Lch_kvw_ok_%=\n"
        "s_add_u32 s64, s64, 1\n"
        "s_and_b32 s65, s64, 255\n"
        "s_cbranch_scc1 .Lch_kvw_go_%=\n"
        "s_mov_b32 s65, 49152\n"
        "s_cmp_lt_u32 s64, 1048576\n"
        "s_cbranch_scc1 .Lch_kvw_chk_%=\n"
        "v_mov_b32 v7, 1\n"
        "buffer_store_dword v7, off, s[40:43], s65 sc1\n"
        "s_waitcnt vmcnt(0)\n"
        ".Lch_kvw_chk_%=:\n"
        "buffer_load_dword v7, off, s[40:43], s65 sc1\n"
        "s_waitcnt vmcnt(0)\n"
        "v_readfirstlane_b32 s65, v7\n"
        "s_nop 0\n"
        "s_cmp_lg_u32 s65, 1\n"
        "s_cbranch_scc1 .Lch_kvw_go_%=\n"
        "s_endpgm\n"
        ".Lch_kvw_go_%=:\n"
        "s_branch .Lch_kvw_top_%=\n"
        ".Lch_kvw_ok_%=:\n"
        "s_add_u32 s89, s69, 524288\n"
        "buffer_load_dwordx4 v[68:71], v3, s[40:43], s69 offen offset:1024 sc1\n"
        "buffer_load_dwordx4 v[64:67], v3, s[40:43], s69 offen sc1\n"
        "buffer_load_dwordx4 v[72:75], v3, s[40:43], s89 offen sc1\n"
        "buffer_load_dwordx4 v[76:79], v3, s[40:43], s89 offen offset:1024 sc1\n"
        "s_mov_b32 s77, s89\n"
        ".Lch_slow3_%=:\n"
        "s_barrier\n"
        "s_memrealtime s[36:37]\n"
        "s_waitcnt lgkmcnt(0)\n"
        "s_add_u32 s36, s36, 12\n"
        "s_add_u32 s37, s36, s35\n"
        "v_and_b32 v7, -2, s37\n"
        "s_mov_b64 exec, 0xff\n"
        "buffer_store_dwordx4 v[4:7], v3, s[40:43], s46 offen sc1\n"
        "s_mov_b64 exec, -1\n"
        "s_mov_b32 s30, s69\n"
        "s_branch .Lch_back_%=\n"
        ".Lch_end_%=:\n"
        :: "s"(p.ws), "s"(p.Wq), "s"(p.Wk), "s"(p.Wv), "s"(p.Wo), "s"(p.mem0), "s"(pair(w, h)), "s"(pair(x4, pr)), "s"(p.tune0), "s"(p.tune1),
           "v"(pair(idx, va + (lane & 2) * 32)), "v"(pair(wb + lane * 16, lane * 16)), "v"(pair(spr, CH_IMG + lane * 64 + c * 4)),
           "v"(pair(qo(0), qo(1))), "v"(pair(oo, o2)), "v"(pair(tr + (q >> 1) * 32, m0)), "v"(pair(to(0), to(1))), "v"(pair(tq(0, 0), tq(0, 1))), "v"(pair(tq(1, 0), tq(1, 1)))
        : "v0", "v1", "v2", "v3", "v4", "v5", "v6", "v7", "v8", "v9", "v10", "v11", "v12", "v13", "v14", "v15", "v16", "v17", "v18", "v19", "v20", "v21", "v22", "v23", "v24", "v25", "v26", "v27", "v28", "v29", "v30", "v31", "v32", "v33", "v34", "v35", "v36", "v37", "v38", "v39", "v40", "v41", "v42", "v43", "v44", "v45", "v46", "v47", "v48", "v49", "v50", "v51", "v52", "v53", "v54", "v55", "v56", "v57", "v58", "v59", "v60", "v61", "v62", "v63", "v64", "v65", "v66", "v67", "v68", "v69", "v70", "v71", "v72", "v73", "v74", "v75", "v76", "v77", "v78", "v79", "v255", "a255", "s30", "s31", "s32", "s33", "s34", "s35", "s36", "s37", "s38", "s39", "s40", "s41", "s42", "s43", "s44", "s45", "s46", "s47", "s48", "s49", "s50", "s51", "s52", "s53", "s54", "s55", "s56", "s57", "s58", "s59", "s60", "s61", "s62", "s63", "s64", "s65", "s66", "s67", "s68", "s69", "s70", "s71", "s72", "s73", "s74", "s75", "s76", "s77", "s78", "s79", "s80", "s81", "s82", "s83", "s84", "s85", "s86", "s87", "s88", "s89", "s90", "s91", "s92", "s93", "s94", "s95", "s96", "s97", "s98", "s99", "vcc", "scc", "memory");
    __builtin_unreachable();
}

constexpr unsigned flag(unsigned i) { return spread(LF0 + i * 128); }
#define GEMM_HEAD const unsigned lane = threadIdx.x & 63, i = lane & 15, g = lane >> 4, q = i >> 2, pp = lane & 3; const int w = rfl(threadIdx.x >> 6)
#define TILE_ARGS(base) "s"(pair(base + (w >> 1) * 16384 + (w & 1) * 4096, out1)), "s"(pair(fout, ((w >> 1) * 9216 + (w & 1) * 4096) | ((w >> 1) * 9216 + 8192) << 16)), \
        "s"(pair(base + 65536 + (w >> 1) * 1024, out2)), "v"(pair((i >> 3) * 9216 + g * 128 + (i & 7) * 16, lane * 16))
__device__ __forceinline__ void role_s1(const Params& p, int h, int x4, int pr, int delay) {
    GEMM_HEAD;
    const unsigned vo = (8 * g + q) * 8192 + (128 * h + 32 * w) * 2 + (pp ^ (2 * (g & 1))) * 16;
    const unsigned tr = w * 40960 + (4 * g + q) * 64 + pp * 8;
    const unsigned out1 = PAGE + h * 16384 + w * 2048, out2 = 0, fout = flag(F_S1 + 4 * h + w);
    asm volatile(
        "s_nop 4\n"
        "s_mov_b64 s[40:41], %0\n"
        "s_and_b32 s41, s41, 0xffff\n"
        "s_mov_b32 s42, 236978176\n"
        "s_mov_b32 s43, 0x20000\n"
        "s_mov_b64 s[48:49], %4\n"
        "s_mov_b64 s[56:57], %5\n"
        "s_mov_b64 s[58:59], %6\n"
        "s_mov_b64 s[60:61], %7\n"
        "s_mov_b64 s[62:63], %8\n"
        "v_mov_b64 v[0:1], %9\n"
        "v_mov_b64 v[2:3], %10\n"
        "v_mov_b64 v[4:5], %11\n"
        "v_mov_b64 v[6:7], %12\n"
        "v_mov_b64 v[8:9], %13\n"
        "v_mov_b64 v[10:11], %14\n"
        "s_mov_b32 s47, 0x20000\n"
        "s_mul_i32 s79, s48, 40960\n"
        ".Ls1_start_%=:\n"
        "s_sub_u32 s49, s49, 1\n"
        "s_cbranch_scc1 .Ls1_start2_%=\n"
        "s_sleep 16\n"
        "s_branch .Ls1_start_%=\n"
        ".Ls1_start2_%=:\n"
        "s_mov_b64 s[44:45], %3\n"
        "s_and_b32 s45, s45, 0xffff\n"
        "s_mov_b32 s46, 4718592\n"
        "s_add_u32 m0, s79, 0\n"
        "s_mov_b32 s65, 0\n"
        "buffer_load_dwordx4 v8, s[44:47], s65 offen lds\n"
        "s_mov_b32 s65, 31744\n"
        "buffer_load_dwordx4 v8, s[44:47], s65 offen offset:1024 lds\n"
        "s_add_u32 m0, s79, 2048\n"
        "s_mov_b32 s65, 262144\n"
        "buffer_load_dwordx4 v8, s[44:47], s65 offen lds\n"
        "s_mov_b32 s65, 293888\n"
        "buffer_load_dwordx4 v8, s[44:47], s65 offen offset:1024 lds\n"
        "s_add_u32 m0, s79, 4096\n"
        "s_mov_b32 s65, 524288\n"
        "buffer_load_dwordx4 v8, s[44:47], s65 offen lds\n"
        "s_mov_b32 s65, 556032\n"
        "buffer_load_dwordx4 v8, s[44:47], s65 offen offset:1024 lds\n"
        "s_waitcnt vmcnt(4)\n"
        "ds_read_b64_tr_b16 a[16:17], v10 offset:0\n"
        "ds_read_b64_tr_b16 a[18:19], v10 offset:1024\n"
        "ds_read_b64_tr_b16 a[20:21], v11 offset:0\n"
        "ds_read_b64_tr_b16 a[22:23], v11 offset:1024\n"
        "s_waitcnt vmcnt(2)\n"
        "ds_read_b64_tr_b16 a[40:41], v10 offset:2048\n"
        "ds_read_b64_tr_b16 a[42:43], v10 offset:3072\n"
        "ds_read_b64_tr_b16 a[44:45], v11 offset:2048\n"
        "ds_read_b64_tr_b16 a[46:47], v11 offset:3072\n"
        "s_waitcnt lgkmcnt(4)\n"
        "s_add_u32 m0, s79, 0\n"
        "s_mov_b32 s65, 786432\n"
        "buffer_load_dwordx4 v8, s[44:47], s65 offen lds\n"
        "s_mov_b32 s65, 818176\n"
        "buffer_load_dwordx4 v8, s[44:47], s65 offen offset:1024 lds\n"
        "s_waitcnt vmcnt(2)\n"
        "ds_read_b64_tr_b16 a[64:65], v10 offset:4096\n"
        "ds_read_b64_tr_b16 a[66:67], v10 offset:5120\n"
        "ds_read_b64_tr_b16 a[68:69], v11 offset:4096\n"
        "ds_read_b64_tr_b16 a[70:71], v11 offset:5120\n"
        "s_waitcnt lgkmcnt(4)\n"
        "s_add_u32 m0, s79, 2048\n"
        "s_mov_b32 s65, 1048576\n"
        "buffer_load_dwordx4 v8, s[44:47], s65 offen lds\n"
        "s_mov_b32 s65, 1080320\n"
        "buffer_load_dwordx4 v8, s[44:47], s65 offen offset:1024 lds\n"
        "s_waitcnt vmcnt(2)\n"
        "ds_read_b64_tr_b16 a[88:89], v10 offset:0\n"
        "ds_read_b64_tr_b16 a[90:91], v10 offset:1024\n"
        "ds_read_b64_tr_b16 a[92:93], v11 offset:0\n"
        "ds_read_b64_tr_b16 a[94:95], v11 offset:1024\n"
        "s_waitcnt lgkmcnt(4)\n"
        "s_add_u32 m0, s79, 4096\n"
        "s_mov_b32 s65, 1310720\n"
        "buffer_load_dwordx4 v8, s[44:47], s65 offen lds\n"
        "s_mov_b32 s65, 1342464\n"
        "buffer_load_dwordx4 v8, s[44:47], s65 offen offset:1024 lds\n"
        "s_waitcnt vmcnt(2)\n"
        "ds_read_b64_tr_b16 a[112:113], v10 offset:2048\n"
        "ds_read_b64_tr_b16 a[114:115], v10 offset:3072\n"
        "ds_read_b64_tr_b16 a[116:117], v11 offset:2048\n"
        "ds_read_b64_tr_b16 a[118:119], v11 offset:3072\n"
        "s_waitcnt lgkmcnt(4)\n"
        "s_add_u32 m0, s79, 0\n"
        "s_mov_b32 s65, 1572864\n"
        "buffer_load_dwordx4 v8, s[44:47], s65 offen lds\n"
        "s_mov_b32 s65, 1604608\n"
        "buffer_load_dwordx4 v8, s[44:47], s65 offen offset:1024 lds\n"
        "s_waitcnt vmcnt(2)\n"
        "ds_read_b64_tr_b16 a[136:137], v10 offset:4096\n"
        "ds_read_b64_tr_b16 a[138:139], v10 offset:5120\n"
        "ds_read_b64_tr_b16 a[140:141], v11 offset:4096\n"
        "ds_read_b64_tr_b16 a[142:143], v11 offset:5120\n"
        "s_waitcnt lgkmcnt(4)\n"
        "s_add_u32 m0, s79, 2048\n"
        "s_mov_b32 s65, 1835008\n"
        "buffer_load_dwordx4 v8, s[44:47], s65 offen lds\n"
        "s_mov_b32 s65, 1866752\n"
        "buffer_load_dwordx4 v8, s[44:47], s65 offen offset:1024 lds\n"
        "s_waitcnt vmcnt(2)\n"
        "ds_read_b64_tr_b16 a[160:161], v10 offset:0\n"
        "ds_read_b64_tr_b16 a[162:163], v10 offset:1024\n"
        "ds_read_b64_tr_b16 a[164:165], v11 offset:0\n"
        "ds_read_b64_tr_b16 a[166:167], v11 offset:1024\n"
        "s_waitcnt lgkmcnt(4)\n"
        "s_add_u32 m0, s79, 4096\n"
        "s_mov_b32 s65, 2097152\n"
        "buffer_load_dwordx4 v8, s[44:47], s65 offen lds\n"
        "s_mov_b32 s65, 2128896\n"
        "buffer_load_dwordx4 v8, s[44:47], s65 offen offset:1024 lds\n"
        "s_waitcnt vmcnt(2)\n"
        "ds_read_b64_tr_b16 a[184:185], v10 offset:2048\n"
        "ds_read_b64_tr_b16 a[186:187], v10 offset:3072\n"
        "ds_read_b64_tr_b16 a[188:189], v11 offset:2048\n"
        "ds_read_b64_tr_b16 a[190:191], v11 offset:3072\n"
        "s_waitcnt lgkmcnt(4)\n"
        "s_add_u32 m0, s79, 0\n"
        "s_mov_b32 s65, 2359296\n"
        "buffer_load_dwordx4 v8, s[44:47], s65 offen lds\n"
        "s_mov_b32 s65, 2391040\n"
        "buffer_load_dwordx4 v8, s[44:47], s65 offen offset:1024 lds\n"
        "s_waitcnt vmcnt(2)\n"
        "ds_read_b64_tr_b16 a[208:209], v10 offset:4096\n"
        "ds_read_b64_tr_b16 a[210:211], v10 offset:5120\n"
        "ds_read_b64_tr_b16 a[212:213], v11 offset:4096\n"
        "ds_read_b64_tr_b16 a[214:215], v11 offset:5120\n"
        "s_waitcnt lgkmcnt(4)\n"
        "s_add_u32 m0, s79, 2048\n"
        "s_mov_b32 s65, 2621440\n"
        "buffer_load_dwordx4 v8, s[44:47], s65 offen lds\n"
        "s_mov_b32 s65, 2653184\n"
        "buffer_load_dwordx4 v8, s[44:47], s65 offen offset:1024 lds\n"
        "s_waitcnt vmcnt(2)\n"
        "ds_read_b64_tr_b16 a[232:233], v10 offset:0\n"
        "ds_read_b64_tr_b16 a[234:235], v10 offset:1024\n"
        "ds_read_b64_tr_b16 a[236:237], v11 offset:0\n"
        "ds_read_b64_tr_b16 a[238:239], v11 offset:1024\n"
        "s_waitcnt lgkmcnt(4)\n"
        "s_add_u32 m0, s79, 4096\n"
        "s_mov_b32 s65, 2883584\n"
        "buffer_load_dwordx4 v8, s[44:47], s65 offen lds\n"
        "s_mov_b32 s65, 2915328\n"
        "buffer_load_dwordx4 v8, s[44:47], s65 offen offset:1024 lds\n"
        "s_waitcnt vmcnt(2)\n"
        "ds_read_b64_tr_b16 v[80:81], v10 offset:2048\n"
        "ds_read_b64_tr_b16 v[82:83], v10 offset:3072\n"
        "ds_read_b64_tr_b16 v[84:85], v11 offset:2048\n"
        "ds_read_b64_tr_b16 v[86:87], v11 offset:3072\n"
        "s_waitcnt lgkmcnt(4)\n"
        "s_add_u32 m0, s79, 0\n"
        "s_mov_b32 s65, 3145728\n"
        "buffer_load_dwordx4 v8, s[44:47], s65 offen lds\n"
        "s_mov_b32 s65, 3177472\n"
        "buffer_load_dwordx4 v8, s[44:47], s65 offen offset:1024 lds\n"
        "s_waitcnt vmcnt(2)\n"
        "ds_read_b64_tr_b16 v[104:105], v10 offset:4096\n"
        "ds_read_b64_tr_b16 v[106:107], v10 offset:5120\n"
        "ds_read_b64_tr_b16 v[108:109], v11 offset:4096\n"
        "ds_read_b64_tr_b16 v[110:111], v11 offset:5120\n"
        "s_waitcnt lgkmcnt(4)\n"
        "s_add_u32 m0, s79, 2048\n"
        "s_mov_b32 s65, 3407872\n"
        "buffer_load_dwordx4 v8, s[44:47], s65 offen lds\n"
        "s_mov_b32 s65, 3439616\n"
        "buffer_load_dwordx4 v8, s[44:47], s65 offen offset:1024 lds\n"
        "s_waitcnt vmcnt(2)\n"
        "ds_read_b64_tr_b16 v[128:129], v10 offset:0\n"
        "ds_read_b64_tr_b16 v[130:131], v10 offset:1024\n"
        "ds_read_b64_tr_b16 v[132:133], v11 offset:0\n"
        "ds_read_b64_tr_b16 v[134:135], v11 offset:1024\n"
        "s_waitcnt lgkmcnt(4)\n"
        "s_add_u32 m0, s79, 4096\n"
        "s_mov_b32 s65, 3670016\n"
        "buffer_load_dwordx4 v8, s[44:47], s65 offen lds\n"
        "s_mov_b32 s65, 3701760\n"
        "buffer_load_dwordx4 v8, s[44:47], s65 offen offset:1024 lds\n"
        "s_waitcnt vmcnt(2)\n"
        "ds_read_b64_tr_b16 v[152:153], v10 offset:2048\n"
        "ds_read_b64_tr_b16 v[154:155], v10 offset:3072\n"
        "ds_read_b64_tr_b16 v[156:157], v11 offset:2048\n"
        "ds_read_b64_tr_b16 v[158:159], v11 offset:3072\n"
        "s_waitcnt lgkmcnt(4)\n"
        "s_add_u32 m0, s79, 0\n"
        "s_mov_b32 s65, 3932160\n"
        "buffer_load_dwordx4 v8, s[44:47], s65 offen lds\n"
        "s_mov_b32 s65, 3963904\n"
        "buffer_load_dwordx4 v8, s[44:47], s65 offen offset:1024 lds\n"
        "s_waitcnt vmcnt(2)\n"
        "ds_read_b64_tr_b16 v[176:177], v10 offset:4096\n"
        "ds_read_b64_tr_b16 v[178:179], v10 offset:5120\n"
        "ds_read_b64_tr_b16 v[180:181], v11 offset:4096\n"
        "ds_read_b64_tr_b16 v[182:183], v11 offset:5120\n"
        "s_waitcnt lgkmcnt(4)\n"
        "s_add_u32 m0, s79, 2048\n"
        "s_mov_b32 s65, 4194304\n"
        "buffer_load_dwordx4 v8, s[44:47], s65 offen lds\n"
        "s_mov_b32 s65, 4226048\n"
        "buffer_load_dwordx4 v8, s[44:47], s65 offen offset:1024 lds\n"
        "s_waitcnt vmcnt(2)\n"
        "ds_read_b64_tr_b16 v[200:201], v10 offset:0\n"
        "ds_read_b64_tr_b16 v[202:203], v10 offset:1024\n"
        "ds_read_b64_tr_b16 v[204:205], v11 offset:0\n"
        "ds_read_b64_tr_b16 v[206:207], v11 offset:1024\n"
        "s_waitcnt lgkmcnt(4)\n"
        "s_add_u32 m0, s79, 4096\n"
        "s_mov_b32 s65, 4456448\n"
        "buffer_load_dwordx4 v8, s[44:47], s65 offen lds\n"
        "s_mov_b32 s65, 4488192\n"
        "buffer_load_dwordx4 v8, s[44:47], s65 offen offset:1024 lds\n"
        "s_waitcnt vmcnt(2)\n"
        "ds_read_b64_tr_b16 v[224:225], v10 offset:2048\n"
        "ds_read_b64_tr_b16 v[226:227], v10 offset:3072\n"
        "ds_read_b64_tr_b16 v[228:229], v11 offset:2048\n"
        "ds_read_b64_tr_b16 v[230:231], v11 offset:3072\n"
        "s_waitcnt lgkmcnt(4)\n"
        "s_mov_b64 s[44:45], %1\n"
        "s_and_b32 s45, s45, 0xffff\n"
        "s_mov_b32 s46, 4718592\n"
        "s_add_u32 m0, s79, 0\n"
        "s_mov_b32 s65, 0\n"
        "buffer_load_dwordx4 v8, s[44:47], s65 offen lds\n"
        "s_mov_b32 s65, 31744\n"
        "buffer_load_dwordx4 v8, s[44:47], s65 offen offset:1024 lds\n"
        "s_waitcnt vmcnt(2)\n"
        "ds_read_b64_tr_b16 v[248:249], v10 offset:4096\n"
        "ds_read_b64_tr_b16 v[250:251], v10 offset:5120\n"
        "ds_read_b64_tr_b16 v[252:253], v11 offset:4096\n"
        "ds_read_b64_tr_b16 v[254:255], v11 offset:5120\n"
        "s_waitcnt lgkmcnt(4)\n"
        "s_add_u32 m0, s79, 2048\n"
        "s_mov_b32 s65, 262144\n"
        "buffer_load_dwordx4 v8, s[44:47], s65 offen lds\n"
        "s_mov_b32 s65, 293888\n"
        "buffer_load_dwordx4 v8, s[44:47], s65 offen offset:1024 lds\n"
        "s_waitcnt vmcnt(2)\n"
        "ds_read_b64_tr_b16 a[0:1], v10 offset:0\n"
        "ds_read_b64_tr_b16 a[2:3], v10 offset:1024\n"
        "ds_read_b64_tr_b16 a[4:5], v11 offset:0\n"
        "ds_read_b64_tr_b16 a[6:7], v11 offset:1024\n"
        "s_waitcnt lgkmcnt(4)\n"
        "s_add_u32 m0, s79, 4096\n"
        "s_mov_b32 s65, 524288\n"
        "buffer_load_dwordx4 v8, s[44:47], s65 offen lds\n"
        "s_mov_b32 s65, 556032\n"
        "buffer_load_dwordx4 v8, s[44:47], s65 offen offset:1024 lds\n"
        "s_waitcnt vmcnt(2)\n"
        "ds_read_b64_tr_b16 a[24:25], v10 offset:2048\n"
        "ds_read_b64_tr_b16 a[26:27], v10 offset:3072\n"
        "ds_read_b64_tr_b16 a[28:29], v11 offset:2048\n"
        "ds_read_b64_tr_b16 a[30:31], v11 offset:3072\n"
        "s_waitcnt lgkmcnt(4)\n"
        "s_add_u32 m0, s79, 0\n"
        "s_mov_b32 s65, 786432\n"
        "buffer_load_dwordx4 v8, s[44:47], s65 offen lds\n"
        "s_mov_b32 s65, 818176\n"
        "buffer_load_dwordx4 v8, s[44:47], s65 offen offset:1024 lds\n"
        "s_waitcnt vmcnt(2)\n"
        "ds_read_b64_tr_b16 a[48:49], v10 offset:4096\n"
        "ds_read_b64_tr_b16 a[50:51], v10 offset:5120\n"
        "ds_read_b64_tr_b16 a[52:53], v11 offset:4096\n"
        "ds_read_b64_tr_b16 a[54:55], v11 offset:5120\n"
        "s_waitcnt lgkmcnt(4)\n"
        "s_add_u32 m0, s79, 2048\n"
        "s_mov_b32 s65, 1048576\n"
        "buffer_load_dwordx4 v8, s[44:47], s65 offen lds\n"
        "s_mov_b32 s65, 1080320\n"
        "buffer_load_dwordx4 v8, s[44:47], s65 offen offset:1024 lds\n"
        "s_waitcnt vmcnt(2)\n"
        "ds_read_b64_tr_b16 a[72:73], v10 offset:0\n"
        "ds_read_b64_tr_b16 a[74:75], v10 offset:1024\n"
        "ds_read_b64_tr_b16 a[76:77], v11 offset:0\n"
        "ds_read_b64_tr_b16 a[78:79], v11 offset:1024\n"
        "s_waitcnt lgkmcnt(4)\n"
        "s_add_u32 m0, s79, 4096\n"
        "s_mov_b32 s65, 1310720\n"
        "buffer_load_dwordx4 v8, s[44:47], s65 offen lds\n"
        "s_mov_b32 s65, 1342464\n"
        "buffer_load_dwordx4 v8, s[44:47], s65 offen offset:1024 lds\n"
        "s_waitcnt vmcnt(2)\n"
        "ds_read_b64_tr_b16 a[96:97], v10 offset:2048\n"
        "ds_read_b64_tr_b16 a[98:99], v10 offset:3072\n"
        "ds_read_b64_tr_b16 a[100:101], v11 offset:2048\n"
        "ds_read_b64_tr_b16 a[102:103], v11 offset:3072\n"
        "s_waitcnt lgkmcnt(4)\n"
        "s_add_u32 m0, s79, 0\n"
        "s_mov_b32 s65, 1572864\n"
        "buffer_load_dwordx4 v8, s[44:47], s65 offen lds\n"
        "s_mov_b32 s65, 1604608\n"
        "buffer_load_dwordx4 v8, s[44:47], s65 offen offset:1024 lds\n"
        "s_waitcnt vmcnt(2)\n"
        "ds_read_b64_tr_b16 a[120:121], v10 offset:4096\n"
        "ds_read_b64_tr_b16 a[122:123], v10 offset:5120\n"
        "ds_read_b64_tr_b16 a[124:125], v11 offset:4096\n"
        "ds_read_b64_tr_b16 a[126:127], v11 offset:5120\n"
        "s_waitcnt lgkmcnt(4)\n"
        "s_add_u32 m0, s79, 2048\n"
        "s_mov_b32 s65, 1835008\n"
        "buffer_load_dwordx4 v8, s[44:47], s65 offen lds\n"
        "s_mov_b32 s65, 1866752\n"
        "buffer_load_dwordx4 v8, s[44:47], s65 offen offset:1024 lds\n"
        "s_waitcnt vmcnt(2)\n"
        "ds_read_b64_tr_b16 a[144:145], v10 offset:0\n"
        "ds_read_b64_tr_b16 a[146:147], v10 offset:1024\n"
        "ds_read_b64_tr_b16 a[148:149], v11 offset:0\n"
        "ds_read_b64_tr_b16 a[150:151], v11 offset:1024\n"
        "s_waitcnt lgkmcnt(4)\n"
        "s_add_u32 m0, s79, 4096\n"
        "s_mov_b32 s65, 2097152\n"
        "buffer_load_dwordx4 v8, s[44:47], s65 offen lds\n"
        "s_mov_b32 s65, 2128896\n"
        "buffer_load_dwordx4 v8, s[44:47], s65 offen offset:1024 lds\n"
        "s_waitcnt vmcnt(2)\n"
        "ds_read_b64_tr_b16 a[168:169], v10 offset:2048\n"
        "ds_read_b64_tr_b16 a[170:171], v10 offset:3072\n"
        "ds_read_b64_tr_b16 a[172:173], v11 offset:2048\n"
        "ds_read_b64_tr_b16 a[174:175], v11 offset:3072\n"
        "s_waitcnt lgkmcnt(4)\n"
        "s_add_u32 m0, s79, 0\n"
        "s_mov_b32 s65, 2359296\n"
        "buffer_load_dwordx4 v8, s[44:47], s65 offen lds\n"
        "s_mov_b32 s65, 2391040\n"
        "buffer_load_dwordx4 v8, s[44:47], s65 offen offset:1024 lds\n"
        "s_waitcnt vmcnt(2)\n"
        "ds_read_b64_tr_b16 a[192:193], v10 offset:4096\n"
        "ds_read_b64_tr_b16 a[194:195], v10 offset:5120\n"
        "ds_read_b64_tr_b16 a[196:197], v11 offset:4096\n"
        "ds_read_b64_tr_b16 a[198:199], v11 offset:5120\n"
        "s_waitcnt lgkmcnt(4)\n"
        "s_add_u32 m0, s79, 2048\n"
        "s_mov_b32 s65, 2621440\n"
        "buffer_load_dwordx4 v8, s[44:47], s65 offen lds\n"
        "s_mov_b32 s65, 2653184\n"
        "buffer_load_dwordx4 v8, s[44:47], s65 offen offset:1024 lds\n"
        "s_waitcnt vmcnt(2)\n"
        "ds_read_b64_tr_b16 a[216:217], v10 offset:0\n"
        "ds_read_b64_tr_b16 a[218:219], v10 offset:1024\n"
        "ds_read_b64_tr_b16 a[220:221], v11 offset:0\n"
        "ds_read_b64_tr_b16 a[222:223], v11 offset:1024\n"
        "s_waitcnt lgkmcnt(4)\n"
        "s_add_u32 m0, s79, 4096\n"
        "s_mov_b32 s65, 2883584\n"
        "buffer_load_dwordx4 v8, s[44:47], s65 offen lds\n"
        "s_mov_b32 s65, 2915328\n"
        "buffer_load_dwordx4 v8, s[44:47], s65 offen offset:1024 lds\n"
        "s_waitcnt vmcnt(2)\n"
        "ds_read_b64_tr_b16 a[240:241], v10 offset:2048\n"
        "ds_read_b64_tr_b16 a[242:243], v10 offset:3072\n"
        "ds_read_b64_tr_b16 a[244:245], v11 offset:2048\n"
        "ds_read_b64_tr_b16 a[246:247], v11 offset:3072\n"
        "s_waitcnt lgkmcnt(4)\n"
        "s_add_u32 m0, s79, 0\n"
        "s_mov_b32 s65, 3145728\n"
        "buffer_load_dwordx4 v8, s[44:47], s65 offen lds\n"
        "s_mov_b32 s65, 3177472\n"
        "buffer_load_dwordx4 v8, s[44:47], s65 offen offset:1024 lds\n"
        "s_waitcnt vmcnt(2)\n"
        "ds_read_b64_tr_b16 v[88:89], v10 offset:4096\n"
        "ds_read_b64_tr_b16 v[90:91], v10 offset:5120\n"
        "ds_read_b64_tr_b16 v[92:93], v11 offset:4096\n"
        "ds_read_b64_tr_b16 v[94:95], v11 offset:5120\n"
        "s_waitcnt lgkmcnt(4)\n"
        "s_add_u32 m0, s79, 2048\n"
        "s_mov_b32 s65, 3407872\n"
        "buffer_load_dwordx4 v8, s[44:47], s65 offen lds\n"
        "s_mov_b32 s65, 3439616\n"
        "buffer_load_dwordx4 v8, s[44:47], s65 offen offset:1024 lds\n"
        "s_waitcnt vmcnt(2)\n"
        "ds_read_b64_tr_b16 v[112:113], v10 offset:0\n"
        "ds_read_b64_tr_b16 v[114:115], v10 offset:1024\n"
        "ds_read_b64_tr_b16 v[116:117], v11 offset:0\n"
        "ds_read_b64_tr_b16 v[118:119], v11 offset:1024\n"
        "s_waitcnt lgkmcnt(4)\n"
        "s_add_u32 m0, s79, 4096\n"
        "s_mov_b32 s65, 3670016\n"
        "buffer_load_dwordx4 v8, s[44:47], s65 offen lds\n"
        "s_mov_b32 s65, 3701760\n"
        "buffer_load_dwordx4 v8, s[44:47], s65 offen offset:1024 lds\n"
        "s_waitcnt vmcnt(2)\n"
        "ds_read_b64_tr_b16 v[136:137], v10 offset:2048\n"
        "ds_read_b64_tr_b16 v[138:139], v10 offset:3072\n"
        "ds_read_b64_tr_b16 v[140:141], v11 offset:2048\n"
        "ds_read_b64_tr_b16 v[142:143], v11 offset:3072\n"
        "s_waitcnt lgkmcnt(4)\n"
        "s_add_u32 m0, s79, 0\n"
        "s_mov_b32 s65, 3932160\n"
        "buffer_load_dwordx4 v8, s[44:47], s65 offen lds\n"
        "s_mov_b32 s65, 3963904\n"
        "buffer_load_dwordx4 v8, s[44:47], s65 offen offset:1024 lds\n"
        "s_waitcnt vmcnt(2)\n"
        "ds_read_b64_tr_b16 v[160:161], v10 offset:4096\n"
        "ds_read_b64_tr_b16 v[162:163], v10 offset:5120\n"
        "ds_read_b64_tr_b16 v[164:165], v11 offset:4096\n"
        "ds_read_b64_tr_b16 v[166:167], v11 offset:5120\n"
        "s_waitcnt lgkmcnt(4)\n"
        "s_add_u32 m0, s79, 2048\n"
        "s_mov_b32 s65, 4194304\n"
        "buffer_load_dwordx4 v8, s[44:47], s65 offen lds\n"
        "s_mov_b32 s65, 4226048\n"
        "buffer_load_dwordx4 v8, s[44:47], s65 offen offset:1024 lds\n"
        "s_waitcnt vmcnt(2)\n"
        "ds_read_b64_tr_b16 v[184:185], v10 offset:0\n"
        "ds_read_b64_tr_b16 v[186:187], v10 offset:1024\n"
        "ds_read_b64_tr_b16 v[188:189], v11 offset:0\n"
        "ds_read_b64_tr_b16 v[190:191], v11 offset:1024\n"
        "s_waitcnt lgkmcnt(4)\n"
        "s_add_u32 m0, s79, 4096\n"
        "s_mov_b32 s65, 4456448\n"
        "buffer_load_dwordx4 v8, s[44:47], s65 offen lds\n"
        "s_mov_b32 s65, 4488192\n"
        "buffer_load_dwordx4 v8, s[44:47], s65 offen offset:1024 lds\n"
        "s_waitcnt vmcnt(2)\n"
        "ds_read_b64_tr_b16 v[208:209], v10 offset:2048\n"
        "ds_read_b64_tr_b16 v[210:211], v10 offset:3072\n"
        "ds_read_b64_tr_b16 v[212:213], v11 offset:2048\n"
        "ds_read_b64_tr_b16 v[214:215], v11 offset:3072\n"
        "s_waitcnt lgkmcnt(4)\n"
        "s_mov_b64 s[44:45], %2\n"
        "s_and_b32 s45, s45, 0xffff\n"
        "s_mov_b32 s46, 4718592\n"
        "s_add_u32 m0, s79, 0\n"
        "s_mov_b32 s65, 0\n"
        "buffer_load_dwordx4 v8, s[44:47], s65 offen lds\n"
        "s_mov_b32 s65, 31744\n"
        "buffer_load_dwordx4 v8, s[44:47], s65 offen offset:1024 lds\n"
        "s_waitcnt vmcnt(2)\n"
        "ds_read_b64_tr_b16 v[232:233], v10 offset:4096\n"
        "ds_read_b64_tr_b16 v[234:235], v10 offset:5120\n"
        "ds_read_b64_tr_b16 v[236:237], v11 offset:4096\n"
        "ds_read_b64_tr_b16 v[238:239], v11 offset:5120\n"
        "s_waitcnt lgkmcnt(4)\n"
        "s_add_u32 m0, s79, 2048\n"
        "s_mov_b32 s65, 262144\n"
        "buffer_load_dwordx4 v8, s[44:47], s65 offen lds\n"
        "s_mov_b32 s65, 293888\n"
        "buffer_load_dwordx4 v8, s[44:47], s65 offen offset:1024 lds\n"
        "s_waitcnt vmcnt(2)\n"
        "ds_read_b64_tr_b16 a[8:9], v10 offset:0\n"
        "ds_read_b64_tr_b16 a[10:11], v10 offset:1024\n"
        "ds_read_b64_tr_b16 a[12:13], v11 offset:0\n"
        "ds_read_b64_tr_b16 a[14:15], v11 offset:1024\n"
        "s_waitcnt lgkmcnt(4)\n"
        "s_add_u32 m0, s79, 4096\n"
        "s_mov_b32 s65, 524288\n"
        "buffer_load_dwordx4 v8, s[44:47], s65 offen lds\n"
        "s_mov_b32 s65, 556032\n"
        "buffer_load_dwordx4 v8, s[44:47], s65 offen offset:1024 lds\n"
        "s_waitcnt vmcnt(2)\n"
        "ds_read_b64_tr_b16 a[32:33], v10 offset:2048\n"
        "ds_read_b64_tr_b16 a[34:35], v10 offset:3072\n"
        "ds_read_b64_tr_b16 a[36:37], v11 offset:2048\n"
        "ds_read_b64_tr_b16 a[38:39], v11 offset:3072\n"
        "s_waitcnt lgkmcnt(4)\n"
        "s_add_u32 m0, s79, 0\n"
        "s_mov_b32 s65, 786432\n"
        "buffer_load_dwordx4 v8, s[44:47], s65 offen lds\n"
        "s_mov_b32 s65, 818176\n"
        "buffer_load_dwordx4 v8, s[44:47], s65 offen offset:1024 lds\n"
        "s_waitcnt vmcnt(2)\n"
        "ds_read_b64_tr_b16 a[56:57], v10 offset:4096\n"
        "ds_read_b64_tr_b16 a[58:59], v10 offset:5120\n"
        "ds_read_b64_tr_b16 a[60:61], v11 offset:4096\n"
        "ds_read_b64_tr_b16 a[62:63], v11 offset:5120\n"
        "s_waitcnt lgkmcnt(4)\n"
        "s_add_u32 m0, s79, 2048\n"
        "s_mov_b32 s65, 1048576\n"
        "buffer_load_dwordx4 v8, s[44:47], s65 offen lds\n"
        "s_mov_b32 s65, 1080320\n"
        "buffer_load_dwordx4 v8, s[44:47], s65 offen offset:1024 lds\n"
        "s_waitcnt vmcnt(2)\n"
        "ds_read_b64_tr_b16 a[80:81], v10 offset:0\n"
        "ds_read_b64_tr_b16 a[82:83], v10 offset:1024\n"
        "ds_read_b64_tr_b16 a[84:85], v11 offset:0\n"
        "ds_read_b64_tr_b16 a[86:87], v11 offset:1024\n"
        "s_waitcnt lgkmcnt(4)\n"
        "s_add_u32 m0, s79, 4096\n"
        "s_mov_b32 s65, 1310720\n"
        "buffer_load_dwordx4 v8, s[44:47], s65 offen lds\n"
        "s_mov_b32 s65, 1342464\n"
        "buffer_load_dwordx4 v8, s[44:47], s65 offen offset:1024 lds\n"
        "s_waitcnt vmcnt(2)\n"
        "ds_read_b64_tr_b16 a[104:105], v10 offset:2048\n"
        "ds_read_b64_tr_b16 a[106:107], v10 offset:3072\n"
        "ds_read_b64_tr_b16 a[108:109], v11 offset:2048\n"
        "ds_read_b64_tr_b16 a[110:111], v11 offset:3072\n"
        "s_waitcnt lgkmcnt(4)\n"
        "s_add_u32 m0, s79, 0\n"
        "s_mov_b32 s65, 1572864\n"
        "buffer_load_dwordx4 v8, s[44:47], s65 offen lds\n"
        "s_mov_b32 s65, 1604608\n"
        "buffer_load_dwordx4 v8, s[44:47], s65 offen offset:1024 lds\n"
        "s_waitcnt vmcnt(2)\n"
        "ds_read_b64_tr_b16 a[128:129], v10 offset:4096\n"
        "ds_read_b64_tr_b16 a[130:131], v10 offset:5120\n"
        "ds_read_b64_tr_b16 a[132:133], v11 offset:4096\n"
        "ds_read_b64_tr_b16 a[134:135], v11 offset:5120\n"
        "s_waitcnt lgkmcnt(4)\n"
        "s_add_u32 m0, s79, 2048\n"
        "s_mov_b32 s65, 1835008\n"
        "buffer_load_dwordx4 v8, s[44:47], s65 offen lds\n"
        "s_mov_b32 s65, 1866752\n"
        "buffer_load_dwordx4 v8, s[44:47], s65 offen offset:1024 lds\n"
        "s_waitcnt vmcnt(2)\n"
        "ds_read_b64_tr_b16 a[152:153], v10 offset:0\n"
        "ds_read_b64_tr_b16 a[154:155], v10 offset:1024\n"
        "ds_read_b64_tr_b16 a[156:157], v11 offset:0\n"
        "ds_read_b64_tr_b16 a[158:159], v11 offset:1024\n"
        "s_waitcnt lgkmcnt(4)\n"
        "s_add_u32 m0, s79, 4096\n"
        "s_mov_b32 s65, 2097152\n"
        "buffer_load_dwordx4 v8, s[44:47], s65 offen lds\n"
        "s_mov_b32 s65, 2128896\n"
        "buffer_load_dwordx4 v8, s[44:47], s65 offen offset:1024 lds\n"
        "s_waitcnt vmcnt(2)\n"
        "ds_read_b64_tr_b16 a[176:177], v10 offset:2048\n"
        "ds_read_b64_tr_b16 a[178:179], v10 offset:3072\n"
        "ds_read_b64_tr_b16 a[180:181], v11 offset:2048\n"
        "ds_read_b64_tr_b16 a[182:183], v11 offset:3072\n"
        "s_waitcnt lgkmcnt(4)\n"
        "s_add_u32 m0, s79, 0\n"
        "s_mov_b32 s65, 2359296\n"
        "buffer_load_dwordx4 v8, s[44:47], s65 offen lds\n"
        "s_mov_b32 s65, 2391040\n"
        "buffer_load_dwordx4 v8, s[44:47], s65 offen offset:1024 lds\n"
        "s_waitcnt vmcnt(2)\n"
        "ds_read_b64_tr_b16 a[200:201], v10 offset:4096\n"
        "ds_read_b64_tr_b16 a[202:203], v10 offset:5120\n"
        "ds_read_b64_tr_b16 a[204:205], v11 offset:4096\n"
        "ds_read_b64_tr_b16 a[206:207], v11 offset:5120\n"
        "s_waitcnt lgkmcnt(4)\n"
        "s_add_u32 m0, s79, 2048\n"
        "s_mov_b32 s65, 2621440\n"
        "buffer_load_dwordx4 v8, s[44:47], s65 offen lds\n"
        "s_mov_b32 s65, 2653184\n"
        "buffer_load_dwordx4 v8, s[44:47], s65 offen offset:1024 lds\n"
        "s_waitcnt vmcnt(2)\n"
        "ds_read_b64_tr_b16 a[224:225], v10 offset:0\n"
        "ds_read_b64_tr_b16 a[226:227], v10 offset:1024\n"
        "ds_read_b64_tr_b16 a[228:229], v11 offset:0\n"
        "ds_read_b64_tr_b16 a[230:231], v11 offset:1024\n"
        "s_waitcnt lgkmcnt(4)\n"
        "s_add_u32 m0, s79, 4096\n"
        "s_mov_b32 s65, 2883584\n"
        "buffer_load_dwordx4 v8, s[44:47], s65 offen lds\n"
        "s_mov_b32 s65, 2915328\n"
        "buffer_load_dwordx4 v8, s[44:47], s65 offen offset:1024 lds\n"
        "s_waitcnt vmcnt(2)\n"
        "ds_read_b64_tr_b16 a[248:249], v10 offset:2048\n"
        "ds_read_b64_tr_b16 a[250:251], v10 offset:3072\n"
        "ds_read_b64_tr_b16 a[252:253], v11 offset:2048\n"
        "ds_read_b64_tr_b16 a[254:255], v11 offset:3072\n"
        "s_waitcnt lgkmcnt(4)\n"
        "s_add_u32 m0, s79, 0\n"
        "s_mov_b32 s65, 3145728\n"
        "buffer_load_dwordx4 v8, s[44:47], s65 offen lds\n"
        "s_mov_b32 s65, 3177472\n"
        "buffer_load_dwordx4 v8, s[44:47], s65 offen offset:1024 lds\n"
        "s_waitcnt vmcnt(2)\n"
        "ds_read_b64_tr_b16 v[96:97], v10 offset:4096\n"
        "ds_read_b64_tr_b16 v[98:99], v10 offset:5120\n"
        "ds_read_b64_tr_b16 v[100:101], v11 offset:4096\n"
        "ds_read_b64_tr_b16 v[102:103], v11 offset:5120\n"
        "s_waitcnt lgkmcnt(4)\n"
        "s_add_u32 m0, s79, 2048\n"
        "s_mov_b32 s65, 3407872\n"
        "buffer_load_dwordx4 v8, s[44:47], s65 offen lds\n"
        "s_mov_b32 s65, 3439616\n"
        "buffer_load_dwordx4 v8, s[44:47], s65 offen offset:1024 lds\n"
        "s_waitcnt vmcnt(2)\n"
        "ds_read_b64_tr_b16 v[120:121], v10 offset:0\n"
        "ds_read_b64_tr_b16 v[122:123], v10 offset:1024\n"
        "ds_read_b64_tr_b16 v[124:125], v11 offset:0\n"
        "ds_read_b64_tr_b16 v[126:127], v11 offset:1024\n"
        "s_waitcnt lgkmcnt(4)\n"
        "s_add_u32 m0, s79, 4096\n"
        "s_mov_b32 s65, 3670016\n"
        "buffer_load_dwordx4 v8, s[44:47], s65 offen lds\n"
        "s_mov_b32 s65, 3701760\n"
        "buffer_load_dwordx4 v8, s[44:47], s65 offen offset:1024 lds\n"
        "s_waitcnt vmcnt(2)\n"
        "ds_read_b64_tr_b16 v[144:145], v10 offset:2048\n"
        "ds_read_b64_tr_b16 v[146:147], v10 offset:3072\n"
        "ds_read_b64_tr_b16 v[148:149], v11 offset:2048\n"
        "ds_read_b64_tr_b16 v[150:151], v11 offset:3072\n"
        "s_waitcnt lgkmcnt(4)\n"
        "s_add_u32 m0, s79, 0\n"
        "s_mov_b32 s65, 3932160\n"
        "buffer_load_dwordx4 v8, s[44:47], s65 offen lds\n"
        "s_mov_b32 s65, 3963904\n"
        "buffer_load_dwordx4 v8, s[44:47], s65 offen offset:1024 lds\n"
        "s_waitcnt vmcnt(2)\n"
        "ds_read_b64_tr_b16 v[168:169], v10 offset:4096\n"
        "ds_read_b64_tr_b16 v[170:171], v10 offset:5120\n"
        "ds_read_b64_tr_b16 v[172:173], v11 offset:4096\n"
        "ds_read_b64_tr_b16 v[174:175], v11 offset:5120\n"
        "s_waitcnt lgkmcnt(4)\n"
        "s_add_u32 m0, s79, 2048\n"
        "s_mov_b32 s65, 4194304\n"
        "buffer_load_dwordx4 v8, s[44:47], s65 offen lds\n"
        "s_mov_b32 s65, 4226048\n"
        "buffer_load_dwordx4 v8, s[44:47], s65 offen offset:1024 lds\n"
        "s_waitcnt vmcnt(2)\n"
        "ds_read_b64_tr_b16 v[192:193], v10 offset:0\n"
        "ds_read_b64_tr_b16 v[194:195], v10 offset:1024\n"
        "ds_read_b64_tr_b16 v[196:197], v11 offset:0\n"
        "ds_read_b64_tr_b16 v[198:199], v11 offset:1024\n"
        "s_waitcnt lgkmcnt(4)\n"
        "s_add_u32 m0, s79, 4096\n"
        "s_mov_b32 s65, 4456448\n"
        "buffer_load_dwordx4 v8, s[44:47], s65 offen lds\n"
        "s_mov_b32 s65, 4488192\n"
        "buffer_load_dwordx4 v8, s[44:47], s65 offen offset:1024 lds\n"
        "s_waitcnt vmcnt(2)\n"
        "ds_read_b64_tr_b16 v[216:217], v10 offset:2048\n"
        "ds_read_b64_tr_b16 v[218:219], v10 offset:3072\n"
        "ds_read_b64_tr_b16 v[220:221], v11 offset:2048\n"
        "ds_read_b64_tr_b16 v[222:223], v11 offset:3072\n"
        "s_waitcnt vmcnt(0)\n"
        "ds_read_b64_tr_b16 v[240:241], v10 offset:4096\n"
        "ds_read_b64_tr_b16 v[242:243], v10 offset:5120\n"
        "ds_read_b64_tr_b16 v[244:245], v11 offset:4096\n"
        "ds_read_b64_tr_b16 v[246:247], v11 offset:5120\n"
        "s_waitcnt lgkmcnt(0)\n"
        "s_getpc_b64 s[44:45]\n"
        ".Ls1_pc_%=:\n"
        "s_add_u32 s44, s44, .Ls1_code_%=-.Ls1_pc_%=\n"
        "s_addc_u32 s45, s45, 0\n"
        "s_and_b32 s45, s45, 0xffff\n"
        "s_and_b32 s44, s44, -4\n"
        "s_mov_b32 s46, .Ls1_end_%=-.Ls1_code_%=\n"
        "s_mov_b32 s47, 0x20000\n"
        "s_lshl_b32 s65, s48, 10\n"
        "buffer_load_dwordx4 v[24:27], v1, s[44:47], s65 offen\n"
        "s_add_u32 s65, s65, 4096\n"
        "buffer_load_dwordx4 v[28:31], v1, s[44:47], s65 offen\n"
        "s_add_u32 s65, s65, 4096\n"
        "buffer_load_dwordx4 v[32:35], v1, s[44:47], s65 offen\n"
        "s_add_u32 s65, s65, 4096\n"
        "buffer_load_dwordx4 v[36:39], v1, s[44:47], s65 offen\n"
        "s_add_u32 s65, s65, 4096\n"
        "buffer_load_dwordx4 v[40:43], v1, s[44:47], s65 offen\n"
        "s_add_u32 s65, s65, 4096\n"
        "v_lshrrev_b32 v20, 4, v1\n"
        "s_mov_b32 s50, s56\n"
        "v_lshlrev_b32 v22, 7, v20\n"
        "s_sub_u32 s66, 0, s50\n"
        "s_mov_b32 s52, 32768\n"
        "s_mov_b32 s64, 0\n"
        ".Ls1_pg0_top_%=:\n"
        "buffer_load_dword v21, v22, s[40:43], s52 offen sc1\n"
        "s_waitcnt vmcnt(0)\n"
        "v_cmp_gt_i32 vcc, 0, v21\n"
        "s_cbranch_vccz .Ls1_pg0_ok_%=\n"
        "s_add_u32 s64, s64, 1\n"
        "s_and_b32 s65, s64, 255\n"
        "s_cbranch_scc1 .Ls1_pg0_go_%=\n"
        "s_mov_b32 s65, 49152\n"
        "s_cmp_lt_u32 s64, 1048576\n"
        "s_cbranch_scc1 .Ls1_pg0_chk_%=\n"
        "v_mov_b32 v21, 1\n"
        "buffer_store_dword v21, off, s[40:43], s65 sc1\n"
        "s_waitcnt vmcnt(0)\n"
        ".Ls1_pg0_chk_%=:\n"
        "buffer_load_dword v21, off, s[40:43], s65 sc1\n"
        "s_waitcnt vmcnt(0)\n"
        "v_readfirstlane_b32 s65, v21\n"
        "s_nop 0\n"
        "s_cmp_lg_u32 s65, 1\n"
        "s_cbranch_scc1 .Ls1_pg0_go_%=\n"
        "s_endpgm\n"
        ".Ls1_pg0_go_%=:\n"
        "s_sleep 2\n"
        "s_branch .Ls1_pg0_top_%=\n"
        ".Ls1_pg0_ok_%=:\n"
        "v_cmp_eq_u32 s[52:53], 1, v21\n"
        "s_nop 1\n"
        "s_mov_b32 s54, 40960\n"
        "s_mov_b32 s64, 0\n"
        ".Ls1_pg1_top_%=:\n"
        "buffer_load_dword v21, v22, s[40:43], s54 offen sc1\n"
        "s_waitcnt vmcnt(0)\n"
        "v_cmp_gt_i32 vcc, 0, v21\n"
        "s_cbranch_vccz .Ls1_pg1_ok_%=\n"
        "s_add_u32 s64, s64, 1\n"
        "s_and_b32 s65, s64, 255\n"
        "s_cbranch_scc1 .Ls1_pg1_go_%=\n"
        "s_mov_b32 s65, 49152\n"
        "s_cmp_lt_u32 s64, 1048576\n"
        "s_cbranch_scc1 .Ls1_pg1_chk_%=\n"
        "v_mov_b32 v21, 1\n"
        "buffer_store_dword v21, off, s[40:43], s65 sc1\n"
        "s_waitcnt vmcnt(0)\n"
        ".Ls1_pg1_chk_%=:\n"
        "buffer_load_dword v21, off, s[40:43], s65 sc1\n"
        "s_waitcnt vmcnt(0)\n"
        "v_readfirstlane_b32 s65, v21\n"
        "s_nop 0\n"
        "s_cmp_lg_u32 s65, 1\n"
        "s_cbranch_scc1 .Ls1_pg1_go_%=\n"
        "s_endpgm\n"
        ".Ls1_pg1_go_%=:\n"
        "s_sleep 2\n"
        "s_branch .Ls1_pg1_top_%=\n"
        ".Ls1_pg1_ok_%=:\n"
        "v_cmp_eq_u32 s[54:55], 1, v21\n"
        "s_nop 1\n"
        "s_mov_b32 vcc_lo, 0x55555555\n"
        "s_mov_b32 vcc_hi, 0x55555555\n"
        "s_xor_b64 vcc, vcc, s[52:53]\n"
        "s_bcnt1_i32_b64 s65, vcc\n"
        "s_sub_u32 s64, s65, 23\n"
        "s_cmp_lt_u32 s64, 19\n"
        "s_cbranch_scc1 .Ls1_rawmap_%=\n"
        "s_cmp_lt_u32 s65, 32\n"
        "s_cselect_b32 s65, 0, -1\n"
        "s_xor_b32 s52, s65, 0x55555555\n"
        "s_xor_b32 s53, s65, 0x55555555\n"
        "s_xor_b32 s54, s65, 0x55555555\n"
        "s_xor_b32 s55, s65, 0xaaaaaaaa\n"
        ".Ls1_rawmap_%=:\n"
        "s_xor_b32 s52, s52, s66\n"
        "s_xor_b32 s53, s53, s66\n"
        "s_xor_b32 s54, s54, s66\n"
        "s_xor_b32 s55, s55, s66\n"
        "s_and_b32 s76, s55, 1\n"
        "s_lshl_b32 s76, s76, 13\n"
        "s_add_u32 s60, s60, s76\n"
        "s_barrier\n"
        "s_mov_b32 s68, -1\n"
        "s_mov_b32 s69, 0\n"
        "s_mov_b32 s78, 0\n"
        "s_mov_b32 s74, -1\n"
        "s_mov_b32 s86, s76\n"
        "s_lshr_b32 s85, s61, 16\n"
        "s_and_b32 s61, s61, 0xffff\n"
        "s_mov_b32 s89, 241172480\n"
        "s_mov_b32 s84, 241172480\n"
        "v_mov_b32 v6, v0\n"
        "s_mov_b32 s80, 0xbfb8aa3b\n"
        "s_lshr_b32 s67, s69, 1\n"
        "s_bitcmp1_b64 s[52:53], s67\n"
        "s_cselect_b32 s65, 8192, 0\n"
        "s_lshl_b32 s77, s67, 21\n"
        "s_add_u32 s77, s77, s65\n"
        "s_add_u32 s71, s77, s58\n"
        "s_add_u32 s81, s77, s62\n"
        "s_add_u32 s78, s78, 18432\n"
        "s_cmp_eq_u32 s78, 55296\n"
        "s_cselect_b32 s78, 0, s78\n"
        "s_add_u32 m0, s78, s61\n"
        "s_lshr_b32 s70, s69, 1\n"
        "s_branch .Ls1_wait1_%=\n"
        ".Ls1_code_%=:\n"
        ".Ls1_i0_%=:\n"
        "v_mfma_f32_16x16x32_bf16 v[24:27], a[0:3], v[8:11], 0\n"
        "s_add_u32 s86, s76, 512\n"
        "buffer_load_dword v4, v2, s[40:43], s86 offen sc1\n"
        "v_mfma_f32_16x16x32_bf16 v[28:31], a[4:7], v[8:11], 0\n"
        "s_add_u32 s71, s77, s58\n"
        "v_cvt_pk_bf16_f32 v48, v48, v49\n"
        "v_cvt_pk_bf16_f32 v49, v50, v51\n"
        "v_mfma_f32_16x16x32_bf16 v[32:35], v[8:11], a[8:11], 0\n"
        "s_add_u32 s71, s71, 32768\n"
        "v_cvt_pk_bf16_f32 v50, v52, v53\n"
        "v_cvt_pk_bf16_f32 v51, v54, v55\n"
        "v_mfma_f32_16x16x32_bf16 v[36:39], v[8:11], a[12:15], 0\n"
        "s_add_u32 s81, s77, s62\n"
        "s_add_u32 s66, s72, 524288\n"
        "v_mfma_f32_16x16x32_bf16 v[40:43], a[16:19], v[8:11], 0\n"
        "s_add_u32 s81, s81, 2048\n"
        "buffer_store_dwordx4 v[48:51], v1, s[40:43], s66 offen offset:1024 sc1\n"
        "v_mfma_f32_16x16x32_bf16 v[44:47], a[20:23], v[8:11], 0\n"
        "s_waitcnt lgkmcnt(1)\n"
        "ds_read_b128 v[8:11], v0 offset:1536\n"
        "v_cvt_pk_bf16_f32 v74, v56, v57\n"
        "v_mfma_f32_16x16x32_bf16 v[24:27], a[24:27], v[12:15], v[24:27]\n"
        "s_add_u32 s78, s78, 18432\n"
        "v_cvt_pk_bf16_f32 v75, v58, v59\n"
        "s_add_u32 s66, s72, 1048576\n"
        "v_mfma_f32_16x16x32_bf16 v[28:31], a[28:31], v[12:15], v[28:31]\n"
        "s_cmp_eq_u32 s78, 55296\n"
        "s_cselect_b32 s78, 0, s78\n"
        "v_mfma_f32_16x16x32_bf16 v[32:35], v[12:15], a[32:35], v[32:35]\n"
        "s_add_u32 m0, s78, s61\n"
        "v_mfma_f32_16x16x32_bf16 v[36:39], v[12:15], a[36:39], v[36:39]\n"
        "s_lshr_b32 s70, s69, 1\n"
        "v_mfma_f32_16x16x32_bf16 v[40:43], a[40:43], v[12:15], v[40:43]\n"
        "v_mfma_f32_16x16x32_bf16 v[44:47], a[44:47], v[12:15], v[44:47]\n"
        "s_waitcnt lgkmcnt(1)\n"
        "ds_read_b128 v[12:15], v0 offset:2048\n"
        "v_mfma_f32_16x16x32_bf16 v[24:27], a[48:51], v[16:19], v[24:27]\n"
        "buffer_store_dwordx4 v[72:75], v1, s[40:43], s66 offen offset:0 sc1\n"
        "v_mfma_f32_16x16x32_bf16 v[28:31], a[52:55], v[16:19], v[28:31]\n"
        "v_cvt_pk_bf16_f32 v78, v60, v61\n"
        "v_cvt_pk_bf16_f32 v79, v62, v63\n"
        "v_mfma_f32_16x16x32_bf16 v[32:35], v[16:19], a[56:59], v[32:35]\n"
        "s_waitcnt vmcnt(3)\n"
        "v_mfma_f32_16x16x32_bf16 v[36:39], v[16:19], a[60:63], v[36:39]\n"
        "buffer_store_dword v5, v3, s[40:43], s84 offen sc1\n"
        "v_mfma_f32_16x16x32_bf16 v[40:43], a[64:67], v[16:19], v[40:43]\n"
        "v_mfma_f32_16x16x32_bf16 v[44:47], a[68:71], v[16:19], v[44:47]\n"
        "s_waitcnt lgkmcnt(1)\n"
        "ds_read_b128 v[16:19], v0 offset:2560\n"
        "v_mfma_f32_16x16x32_bf16 v[24:27], a[72:75], v[8:11], v[24:27]\n"
        "v_mfma_f32_16x16x32_bf16 v[28:31], a[76:79], v[8:11], v[28:31]\n"
        "buffer_store_dwordx4 v[76:79], v1, s[40:43], s66 offen offset:1024 sc1\n"
        "v_mfma_f32_16x16x32_bf16 v[32:35], v[8:11], a[80:83], v[32:35]\n"
        "v_cvt_pk_bf16_f32 v64, v64, v65\n"
        "v_cvt_pk_bf16_f32 v65, v66, v67\n"
        "v_mfma_f32_16x16x32_bf16 v[36:39], v[8:11], a[84:87], v[36:39]\n"
        "v_cvt_pk_bf16_f32 v66, v68, v69\n"
        "v_cvt_pk_bf16_f32 v67, v70, v71\n"
        "v_mfma_f32_16x16x32_bf16 v[40:43], a[88:91], v[8:11], v[40:43]\n"
        "v_mfma_f32_16x16x32_bf16 v[44:47], a[92:95], v[8:11], v[44:47]\n"
        "s_waitcnt lgkmcnt(1)\n"
        "ds_read_b128 v[8:11], v0 offset:3072\n"
        "v_mfma_f32_16x16x32_bf16 v[24:27], a[96:99], v[12:15], v[24:27]\n"
        "v_mfma_f32_16x16x32_bf16 v[28:31], a[100:103], v[12:15], v[28:31]\n"
        "v_mfma_f32_16x16x32_bf16 v[32:35], v[12:15], a[104:107], v[32:35]\n"
        "buffer_store_dwordx4 v[64:67], v1, s[40:43], s72 offen offset:1024 sc1\n"
        "v_mfma_f32_16x16x32_bf16 v[36:39], v[12:15], a[108:111], v[36:39]\n"
        "v_mov_b32 v5, s74\n"
        "v_mfma_f32_16x16x32_bf16 v[40:43], a[112:115], v[12:15], v[40:43]\n"
        "s_waitcnt vmcnt(5)\n"
        "v_cmp_gt_i32 vcc, s70, v4\n"
        "v_mfma_f32_16x16x32_bf16 v[44:47], a[116:119], v[12:15], v[44:47]\n"
        "s_waitcnt lgkmcnt(1)\n"
        "ds_read_b128 v[12:15], v0 offset:3584\n"
        "v_mfma_f32_16x16x32_bf16 v[24:27], a[120:123], v[16:19], v[24:27]\n"
        "s_and_b64 vcc, vcc, 1\n"
        "v_mfma_f32_16x16x32_bf16 v[28:31], a[124:127], v[16:19], v[28:31]\n"
        "s_cbranch_vccnz .Ls1_i00_%=\n"
        "v_mfma_f32_16x16x32_bf16 v[32:35], v[16:19], a[128:131], v[32:35]\n"
        "buffer_load_dwordx4 v1, s[40:43], s71 offen offset:0 sc1 lds\n"
        "v_mfma_f32_16x16x32_bf16 v[36:39], v[16:19], a[132:135], v[36:39]\n"
        "v_mfma_f32_16x16x32_bf16 v[40:43], a[136:139], v[16:19], v[40:43]\n"
        "v_mfma_f32_16x16x32_bf16 v[44:47], a[140:143], v[16:19], v[44:47]\n"
        "s_waitcnt lgkmcnt(1)\n"
        "ds_read_b128 v[16:19], v0 offset:4096\n"
        "v_mfma_f32_16x16x32_bf16 v[24:27], a[144:147], v[8:11], v[24:27]\n"
        "buffer_load_dwordx4 v1, s[40:43], s71 offen offset:1024 sc1 lds\n"
        "v_mfma_f32_16x16x32_bf16 v[28:31], a[148:151], v[8:11], v[28:31]\n"
        "v_mfma_f32_16x16x32_bf16 v[32:35], v[8:11], a[152:155], v[32:35]\n"
        "v_mfma_f32_16x16x32_bf16 v[36:39], v[8:11], a[156:159], v[36:39]\n"
        "v_mfma_f32_16x16x32_bf16 v[40:43], a[160:163], v[8:11], v[40:43]\n"
        "buffer_load_dwordx4 v1, s[40:43], s71 offen offset:2048 sc1 lds\n"
        "v_mfma_f32_16x16x32_bf16 v[44:47], a[164:167], v[8:11], v[44:47]\n"
        "s_waitcnt lgkmcnt(1)\n"
        "ds_read_b128 v[8:11], v0 offset:4608\n"
        "v_mfma_f32_16x16x32_bf16 v[24:27], a[168:171], v[12:15], v[24:27]\n"
        "v_mfma_f32_16x16x32_bf16 v[28:31], a[172:175], v[12:15], v[28:31]\n"
        "v_mfma_f32_16x16x32_bf16 v[32:35], v[12:15], a[176:179], v[32:35]\n"
        "buffer_load_dwordx4 v1, s[40:43], s71 offen offset:3072 sc1 lds\n"
        "s_add_u32 m0, s78, s85\n"
        "v_mfma_f32_16x16x32_bf16 v[36:39], v[12:15], a[180:183], v[36:39]\n"
        "s_add_u32 s69, s69, 1\n"
        "v_mfma_f32_16x16x32_bf16 v[40:43], a[184:187], v[12:15], v[40:43]\n"
        "v_mfma_f32_16x16x32_bf16 v[44:47], a[188:191], v[12:15], v[44:47]\n"
        "s_waitcnt lgkmcnt(1)\n"
        "ds_read_b128 v[12:15], v0 offset:5120\n"
        "v_mfma_f32_16x16x32_bf16 v[24:27], a[192:195], v[16:19], v[24:27]\n"
        "buffer_load_dwordx4 v1, s[40:43], s81 offen offset:0 sc1 lds\n"
        "v_mfma_f32_16x16x32_bf16 v[28:31], a[196:199], v[16:19], v[28:31]\n"
        "v_mfma_f32_16x16x32_bf16 v[32:35], v[16:19], a[200:203], v[32:35]\n"
        "s_getreg_b32 s88, hwreg(HW_REG_IB_STS, 0, 4)\n"
        "v_mfma_f32_16x16x32_bf16 v[36:39], v[16:19], a[204:207], v[36:39]\n"
        "s_cmp_le_u32 s88, 5\n"
        "s_cselect_b32 s88, s73, s89\n"
        "s_cselect_b32 s73, s89, s73\n"
        "v_mfma_f32_16x16x32_bf16 v[40:43], a[208:211], v[16:19], v[40:43]\n"
        "buffer_store_dword v5, v3, s[40:43], s88 offen sc1\n"
        "v_mfma_f32_16x16x32_bf16 v[44:47], a[212:215], v[16:19], v[44:47]\n"
        "s_waitcnt lgkmcnt(1)\n"
        "ds_read_b128 v[16:19], v0 offset:5632\n"
        "v_mfma_f32_16x16x32_bf16 v[24:27], a[216:219], v[8:11], v[24:27]\n"
        "v_mfma_f32_16x16x32_bf16 v[28:31], a[220:223], v[8:11], v[28:31]\n"
        "v_mfma_f32_16x16x32_bf16 v[32:35], v[8:11], a[224:227], v[32:35]\n"
        "v_mfma_f32_16x16x32_bf16 v[36:39], v[8:11], a[228:231], v[36:39]\n"
        "v_mfma_f32_16x16x32_bf16 v[40:43], a[232:235], v[8:11], v[40:43]\n"
        "s_getreg_b32 s88, hwreg(HW_REG_IB_STS, 0, 4)\n"
        "v_mfma_f32_16x16x32_bf16 v[44:47], a[236:239], v[8:11], v[44:47]\n"
        "s_waitcnt lgkmcnt(1)\n"
        "ds_read_b128 v[8:11], v0 offset:6144\n"
        "v_mfma_f32_16x16x32_bf16 v[24:27], a[240:243], v[12:15], v[24:27]\n"
        "s_cmp_le_u32 s88, 6\n"
        "s_cselect_b32 s88, s73, s89\n"
        "s_cselect_b32 s73, s89, s73\n"
        "v_mfma_f32_16x16x32_bf16 v[28:31], a[244:247], v[12:15], v[28:31]\n"
        "buffer_store_dword v5, v3, s[40:43], s88 offen sc1\n"
        "v_mfma_f32_16x16x32_bf16 v[32:35], v[12:15], a[248:251], v[32:35]\n"
        "v_mfma_f32_16x16x32_bf16 v[36:39], v[12:15], a[252:255], v[36:39]\n"
        "v_mfma_f32_16x16x32_bf16 v[40:43], v[80:83], v[12:15], v[40:43]\n"
        "v_mfma_f32_16x16x32_bf16 v[44:47], v[84:87], v[12:15], v[44:47]\n"
        "s_waitcnt lgkmcnt(1)\n"
        "ds_read_b128 v[12:15], v0 offset:6656\n"
        "v_mfma_f32_16x16x32_bf16 v[24:27], v[88:91], v[16:19], v[24:27]\n"
        "s_getreg_b32 s88, hwreg(HW_REG_IB_STS, 0, 4)\n"
        "v_mfma_f32_16x16x32_bf16 v[28:31], v[92:95], v[16:19], v[28:31]\n"
        "s_cmp_le_u32 s88, 7\n"
        "s_cselect_b32 s88, s73, s89\n"
        "s_cselect_b32 s73, s89, s73\n"
        "v_mfma_f32_16x16x32_bf16 v[32:35], v[16:19], v[96:99], v[32:35]\n"
        "buffer_store_dword v5, v3, s[40:43], s88 offen sc1\n"
        "v_mfma_f32_16x16x32_bf16 v[36:39], v[16:19], v[100:103], v[36:39]\n"
        "v_mfma_f32_16x16x32_bf16 v[40:43], v[104:107], v[16:19], v[40:43]\n"
        "v_mfma_f32_16x16x32_bf16 v[44:47], v[108:111], v[16:19], v[44:47]\n"
        "s_waitcnt lgkmcnt(1)\n"
        "ds_read_b128 v[16:19], v0 offset:7168\n"
        "v_mfma_f32_16x16x32_bf16 v[24:27], v[112:115], v[8:11], v[24:27]\n"
        "v_mfma_f32_16x16x32_bf16 v[28:31], v[116:119], v[8:11], v[28:31]\n"
        "v_mfma_f32_16x16x32_bf16 v[32:35], v[8:11], v[120:123], v[32:35]\n"
        "v_mfma_f32_16x16x32_bf16 v[36:39], v[8:11], v[124:127], v[36:39]\n"
        "v_mfma_f32_16x16x32_bf16 v[40:43], v[128:131], v[8:11], v[40:43]\n"
        "v_mfma_f32_16x16x32_bf16 v[44:47], v[132:135], v[8:11], v[44:47]\n"
        "s_waitcnt lgkmcnt(1)\n"
        "ds_read_b128 v[8:11], v0 offset:7680\n"
        "v_mfma_f32_16x16x32_bf16 v[24:27], v[136:139], v[12:15], v[24:27]\n"
        "v_mfma_f32_16x16x32_bf16 v[28:31], v[140:143], v[12:15], v[28:31]\n"
        "v_mfma_f32_16x16x32_bf16 v[32:35], v[12:15], v[144:147], v[32:35]\n"
        "s_waitcnt vmcnt(8)\n"
        "buffer_store_dword v5, v3, s[40:43], s73 offen sc1\n"
        "s_add_u32 s74, s74, 1\n"
        "v_mfma_f32_16x16x32_bf16 v[36:39], v[12:15], v[148:151], v[36:39]\n"
        "s_mov_b32 s84, s89\n"
        "s_mov_b32 s73, s60\n"
        "s_add_u32 s68, s68, 1\n"
        "v_mfma_f32_16x16x32_bf16 v[40:43], v[152:155], v[12:15], v[40:43]\n"
        "s_bitcmp1_b64 s[52:53], s68\n"
        "s_cselect_b32 s66, 8192, 0\n"
        "s_lshl_b32 s75, s68, 21\n"
        "v_mfma_f32_16x16x32_bf16 v[44:47], v[156:159], v[12:15], v[44:47]\n"
        "s_waitcnt lgkmcnt(1)\n"
        "ds_read_b128 v[12:15], v0 offset:8192\n"
        "s_add_u32 s75, s75, s66\n"
        "v_mfma_f32_16x16x32_bf16 v[24:27], v[160:163], v[16:19], v[24:27]\n"
        "s_add_u32 s72, s75, s59\n"
        "v_mfma_f32_16x16x32_bf16 v[28:31], v[164:167], v[16:19], v[28:31]\n"
        "v_mfma_f32_16x16x32_bf16 v[32:35], v[16:19], v[168:171], v[32:35]\n"
        "v_mfma_f32_16x16x32_bf16 v[36:39], v[16:19], v[172:175], v[36:39]\n"
        "v_mfma_f32_16x16x32_bf16 v[40:43], v[176:179], v[16:19], v[40:43]\n"
        "v_mfma_f32_16x16x32_bf16 v[44:47], v[180:183], v[16:19], v[44:47]\n"
        "s_waitcnt lgkmcnt(1)\n"
        "ds_read_b128 v[16:19], v0 offset:8704\n"
        "v_mfma_f32_16x16x32_bf16 v[24:27], v[184:187], v[8:11], v[24:27]\n"
        "v_mfma_f32_16x16x32_bf16 v[28:31], v[188:191], v[8:11], v[28:31]\n"
        "v_mfma_f32_16x16x32_bf16 v[32:35], v[8:11], v[192:195], v[32:35]\n"
        "v_mfma_f32_16x16x32_bf16 v[36:39], v[8:11], v[196:199], v[36:39]\n"
        "v_mfma_f32_16x16x32_bf16 v[40:43], v[200:203], v[8:11], v[40:43]\n"
        "s_waitcnt vmcnt(4)\n"
        "s_barrier\n"
        "v_add_u32 v0, s78, v6\n"
        "v_mfma_f32_16x16x32_bf16 v[44:47], v[204:207], v[8:11], v[44:47]\n"
        "s_waitcnt lgkmcnt(1)\n"
        "ds_read_b128 v[8:11], v0 offset:0\n"
        "v_mfma_f32_16x16x32_bf16 v[24:27], v[208:211], v[12:15], v[24:27]\n"
        "v_mfma_f32_16x16x32_bf16 v[28:31], v[212:215], v[12:15], v[28:31]\n"
        "v_mfma_f32_16x16x32_bf16 v[32:35], v[12:15], v[216:219], v[32:35]\n"
        "v_mfma_f32_16x16x32_bf16 v[36:39], v[12:15], v[220:223], v[36:39]\n"
        "v_mfma_f32_16x16x32_bf16 v[40:43], v[224:227], v[12:15], v[40:43]\n"
        "v_mfma_f32_16x16x32_bf16 v[44:47], v[228:231], v[12:15], v[44:47]\n"
        "s_waitcnt lgkmcnt(1)\n"
        "ds_read_b128 v[12:15], v0 offset:512\n"
        "v_mfma_f32_16x16x32_bf16 v[24:27], v[232:235], v[16:19], v[24:27]\n"
        "v_mfma_f32_16x16x32_bf16 v[28:31], v[236:239], v[16:19], v[28:31]\n"
        "v_mfma_f32_16x16x32_bf16 v[32:35], v[16:19], v[240:243], v[32:35]\n"
        "v_mfma_f32_16x16x32_bf16 v[36:39], v[16:19], v[244:247], v[36:39]\n"
        "v_mfma_f32_16x16x32_bf16 v[40:43], v[248:251], v[16:19], v[40:43]\n"
        "v_mfma_f32_16x16x32_bf16 v[44:47], v[252:255], v[16:19], v[44:47]\n"
        "s_waitcnt lgkmcnt(1)\n"
        "ds_read_b128 v[16:19], v0 offset:1024\n"
        "s_branch .Ls1_i1_%=\n"
        ".Ls1_i00_%=:\n"
        "v_mfma_f32_16x16x32_bf16 v[32:35], v[16:19], a[128:131], v[32:35]\n"
        "buffer_load_dword v4, v2, s[40:43], s86 offen sc1\n"
        "v_mfma_f32_16x16x32_bf16 v[36:39], v[16:19], a[132:135], v[36:39]\n"
        "v_mfma_f32_16x16x32_bf16 v[40:43], a[136:139], v[16:19], v[40:43]\n"
        "v_mfma_f32_16x16x32_bf16 v[44:47], a[140:143], v[16:19], v[44:47]\n"
        "s_waitcnt lgkmcnt(1)\n"
        "ds_read_b128 v[16:19], v0 offset:4096\n"
        "v_mfma_f32_16x16x32_bf16 v[24:27], a[144:147], v[8:11], v[24:27]\n"
        "v_mfma_f32_16x16x32_bf16 v[28:31], a[148:151], v[8:11], v[28:31]\n"
        "v_mfma_f32_16x16x32_bf16 v[32:35], v[8:11], a[152:155], v[32:35]\n"
        "v_mfma_f32_16x16x32_bf16 v[36:39], v[8:11], a[156:159], v[36:39]\n"
        "v_mfma_f32_16x16x32_bf16 v[40:43], a[160:163], v[8:11], v[40:43]\n"
        "v_mfma_f32_16x16x32_bf16 v[44:47], a[164:167], v[8:11], v[44:47]\n"
        "s_waitcnt lgkmcnt(1)\n"
        "ds_read_b128 v[8:11], v0 offset:4608\n"
        "v_mfma_f32_16x16x32_bf16 v[24:27], a[168:171], v[12:15], v[24:27]\n"
        "v_mfma_f32_16x16x32_bf16 v[28:31], a[172:175], v[12:15], v[28:31]\n"
        "v_mfma_f32_16x16x32_bf16 v[32:35], v[12:15], a[176:179], v[32:35]\n"
        "v_mfma_f32_16x16x32_bf16 v[36:39], v[12:15], a[180:183], v[36:39]\n"
        "v_mfma_f32_16x16x32_bf16 v[40:43], a[184:187], v[12:15], v[40:43]\n"
        "v_mfma_f32_16x16x32_bf16 v[44:47], a[188:191], v[12:15], v[44:47]\n"
        "s_waitcnt lgkmcnt(1)\n"
        "ds_read_b128 v[12:15], v0 offset:5120\n"
        "v_mfma_f32_16x16x32_bf16 v[24:27], a[192:195], v[16:19], v[24:27]\n"
        "v_mfma_f32_16x16x32_bf16 v[28:31], a[196:199], v[16:19], v[28:31]\n"
        "v_mfma_f32_16x16x32_bf16 v[32:35], v[16:19], a[200:203], v[32:35]\n"
        "s_getreg_b32 s88, hwreg(HW_REG_IB_STS, 0, 4)\n"
        "v_mfma_f32_16x16x32_bf16 v[36:39], v[16:19], a[204:207], v[36:39]\n"
        "s_cmp_le_u32 s88, 1\n"
        "s_cselect_b32 s88, s73, s89\n"
        "s_cselect_b32 s73, s89, s73\n"
        "v_mfma_f32_16x16x32_bf16 v[40:43], a[208:211], v[16:19], v[40:43]\n"
        "buffer_store_dword v5, v3, s[40:43], s88 offen sc1\n"
        "v_mfma_f32_16x16x32_bf16 v[44:47], a[212:215], v[16:19], v[44:47]\n"
        "s_waitcnt lgkmcnt(1)\n"
        "ds_read_b128 v[16:19], v0 offset:5632\n"
        "v_mfma_f32_16x16x32_bf16 v[24:27], a[216:219], v[8:11], v[24:27]\n"
        "v_mfma_f32_16x16x32_bf16 v[28:31], a[220:223], v[8:11], v[28:31]\n"
        "v_mfma_f32_16x16x32_bf16 v[32:35], v[8:11], a[224:227], v[32:35]\n"
        "v_mfma_f32_16x16x32_bf16 v[36:39], v[8:11], a[228:231], v[36:39]\n"
        "v_mfma_f32_16x16x32_bf16 v[40:43], a[232:235], v[8:11], v[40:43]\n"
        "s_getreg_b32 s88, hwreg(HW_REG_IB_STS, 0, 4)\n"
        "v_mfma_f32_16x16x32_bf16 v[44:47], a[236:239], v[8:11], v[44:47]\n"
        "s_waitcnt vmcnt(1)\n"
        "v_cmp_gt_i32 vcc, s70, v4\n"
        "s_waitcnt lgkmcnt(1)\n"
        "ds_read_b128 v[8:11], v0 offset:6144\n"
        "v_mfma_f32_16x16x32_bf16 v[24:27], a[240:243], v[12:15], v[24:27]\n"
        "s_cmp_le_u32 s88, 2\n"
        "s_cselect_b32 s88, s73, s89\n"
        "s_cselect_b32 s73, s89, s73\n"
        "v_mfma_f32_16x16x32_bf16 v[28:31], a[244:247], v[12:15], v[28:31]\n"
        "buffer_store_dword v5, v3, s[40:43], s88 offen sc1\n"
        "s_and_b64 vcc, vcc, 1\n"
        "v_mfma_f32_16x16x32_bf16 v[32:35], v[12:15], a[248:251], v[32:35]\n"
        "s_cbranch_vccnz .Ls1_i01_%=\n"
        "v_mfma_f32_16x16x32_bf16 v[36:39], v[12:15], a[252:255], v[36:39]\n"
        "buffer_load_dwordx4 v1, s[40:43], s71 offen offset:0 sc1 lds\n"
        "v_mfma_f32_16x16x32_bf16 v[40:43], v[80:83], v[12:15], v[40:43]\n"
        "v_mfma_f32_16x16x32_bf16 v[44:47], v[84:87], v[12:15], v[44:47]\n"
        "s_waitcnt lgkmcnt(1)\n"
        "ds_read_b128 v[12:15], v0 offset:6656\n"
        "v_mfma_f32_16x16x32_bf16 v[24:27], v[88:91], v[16:19], v[24:27]\n"
        "s_getreg_b32 s88, hwreg(HW_REG_IB_STS, 0, 4)\n"
        "buffer_load_dwordx4 v1, s[40:43], s71 offen offset:1024 sc1 lds\n"
        "v_mfma_f32_16x16x32_bf16 v[28:31], v[92:95], v[16:19], v[28:31]\n"
        "s_cmp_le_u32 s88, 15\n"
        "s_cselect_b32 s88, s73, s89\n"
        "s_cselect_b32 s73, s89, s73\n"
        "v_mfma_f32_16x16x32_bf16 v[32:35], v[16:19], v[96:99], v[32:35]\n"
        "buffer_store_dword v5, v3, s[40:43], s88 offen sc1\n"
        "v_mfma_f32_16x16x32_bf16 v[36:39], v[16:19], v[100:103], v[36:39]\n"
        "buffer_load_dwordx4 v1, s[40:43], s71 offen offset:2048 sc1 lds\n"
        "v_mfma_f32_16x16x32_bf16 v[40:43], v[104:107], v[16:19], v[40:43]\n"
        "v_mfma_f32_16x16x32_bf16 v[44:47], v[108:111], v[16:19], v[44:47]\n"
        "s_waitcnt lgkmcnt(1)\n"
        "ds_read_b128 v[16:19], v0 offset:7168\n"
        "v_mfma_f32_16x16x32_bf16 v[24:27], v[112:115], v[8:11], v[24:27]\n"
        "buffer_load_dwordx4 v1, s[40:43], s71 offen offset:3072 sc1 lds\n"
        "s_add_u32 m0, s78, s85\n"
        "v_mfma_f32_16x16x32_bf16 v[28:31], v[116:119], v[8:11], v[28:31]\n"
        "s_add_u32 s69, s69, 1\n"
        "v_mfma_f32_16x16x32_bf16 v[32:35], v[8:11], v[120:123], v[32:35]\n"
        "v_mfma_f32_16x16x32_bf16 v[36:39], v[8:11], v[124:127], v[36:39]\n"
        "buffer_load_dwordx4 v1, s[40:43], s81 offen offset:0 sc1 lds\n"
        "v_mfma_f32_16x16x32_bf16 v[40:43], v[128:131], v[8:11], v[40:43]\n"
        "v_mfma_f32_16x16x32_bf16 v[44:47], v[132:135], v[8:11], v[44:47]\n"
        "s_waitcnt lgkmcnt(1)\n"
        "ds_read_b128 v[8:11], v0 offset:7680\n"
        "v_mfma_f32_16x16x32_bf16 v[24:27], v[136:139], v[12:15], v[24:27]\n"
        "v_mfma_f32_16x16x32_bf16 v[28:31], v[140:143], v[12:15], v[28:31]\n"
        "v_mfma_f32_16x16x32_bf16 v[32:35], v[12:15], v[144:147], v[32:35]\n"
        "buffer_store_dword v5, v3, s[40:43], s73 offen sc1\n"
        "s_add_u32 s74, s74, 1\n"
        "v_mfma_f32_16x16x32_bf16 v[36:39], v[12:15], v[148:151], v[36:39]\n"
        "s_mov_b32 s84, s89\n"
        "s_mov_b32 s73, s60\n"
        "s_add_u32 s68, s68, 1\n"
        "v_mfma_f32_16x16x32_bf16 v[40:43], v[152:155], v[12:15], v[40:43]\n"
        "s_bitcmp1_b64 s[52:53], s68\n"
        "s_cselect_b32 s66, 8192, 0\n"
        "s_lshl_b32 s75, s68, 21\n"
        "v_mfma_f32_16x16x32_bf16 v[44:47], v[156:159], v[12:15], v[44:47]\n"
        "s_waitcnt lgkmcnt(1)\n"
        "ds_read_b128 v[12:15], v0 offset:8192\n"
        "s_add_u32 s75, s75, s66\n"
        "v_mfma_f32_16x16x32_bf16 v[24:27], v[160:163], v[16:19], v[24:27]\n"
        "s_add_u32 s72, s75, s59\n"
        "v_mfma_f32_16x16x32_bf16 v[28:31], v[164:167], v[16:19], v[28:31]\n"
        "v_mfma_f32_16x16x32_bf16 v[32:35], v[16:19], v[168:171], v[32:35]\n"
        "v_mfma_f32_16x16x32_bf16 v[36:39], v[16:19], v[172:175], v[36:39]\n"
        "v_mfma_f32_16x16x32_bf16 v[40:43], v[176:179], v[16:19], v[40:43]\n"
        "v_mfma_f32_16x16x32_bf16 v[44:47], v[180:183], v[16:19], v[44:47]\n"
        "s_waitcnt lgkmcnt(1)\n"
        "ds_read_b128 v[16:19], v0 offset:8704\n"
        "v_mfma_f32_16x16x32_bf16 v[24:27], v[184:187], v[8:11], v[24:27]\n"
        "v_mfma_f32_16x16x32_bf16 v[28:31], v[188:191], v[8:11], v[28:31]\n"
        "v_mfma_f32_16x16x32_bf16 v[32:35], v[8:11], v[192:195], v[32:35]\n"
        "v_mfma_f32_16x16x32_bf16 v[36:39], v[8:11], v[196:199], v[36:39]\n"
        "v_mfma_f32_16x16x32_bf16 v[40:43], v[200:203], v[8:11], v[40:43]\n"
        "s_waitcnt vmcnt(1)\n"
        "s_barrier\n"
        "v_add_u32 v0, s78, v6\n"
        "v_mfma_f32_16x16x32_bf16 v[44:47], v[204:207], v[8:11], v[44:47]\n"
        "s_waitcnt lgkmcnt(1)\n"
        "ds_read_b128 v[8:11], v0 offset:0\n"
        "v_mfma_f32_16x16x32_bf16 v[24:27], v[208:211], v[12:15], v[24:27]\n"
        "v_mfma_f32_16x16x32_bf16 v[28:31], v[212:215], v[12:15], v[28:31]\n"
        "v_mfma_f32_16x16x32_bf16 v[32:35], v[12:15], v[216:219], v[32:35]\n"
        "v_mfma_f32_16x16x32_bf16 v[36:39], v[12:15], v[220:223], v[36:39]\n"
        "v_mfma_f32_16x16x32_bf16 v[40:43], v[224:227], v[12:15], v[40:43]\n"
        "v_mfma_f32_16x16x32_bf16 v[44:47], v[228:231], v[12:15], v[44:47]\n"
        "s_waitcnt lgkmcnt(1)\n"
        "ds_read_b128 v[12:15], v0 offset:512\n"
        "v_mfma_f32_16x16x32_bf16 v[24:27], v[232:235], v[16:19], v[24:27]\n"
        "v_mfma_f32_16x16x32_bf16 v[28:31], v[236:239], v[16:19], v[28:31]\n"
        "v_mfma_f32_16x16x32_bf16 v[32:35], v[16:19], v[240:243], v[32:35]\n"
        "v_mfma_f32_16x16x32_bf16 v[36:39], v[16:19], v[244:247], v[36:39]\n"
        "v_mfma_f32_16x16x32_bf16 v[40:43], v[248:251], v[16:19], v[40:43]\n"
        "v_mfma_f32_16x16x32_bf16 v[44:47], v[252:255], v[16:19], v[44:47]\n"
        "s_waitcnt lgkmcnt(1)\n"
        "ds_read_b128 v[16:19], v0 offset:1024\n"
        "s_branch .Ls1_i1_%=\n"
        ".Ls1_i01_%=:\n"
        "v_mfma_f32_16x16x32_bf16 v[36:39], v[12:15], a[252:255], v[36:39]\n"
        "buffer_load_dword v4, v2, s[40:43], s86 offen sc1\n"
        "v_mfma_f32_16x16x32_bf16 v[40:43], v[80:83], v[12:15], v[40:43]\n"
        "v_mfma_f32_16x16x32_bf16 v[44:47], v[84:87], v[12:15], v[44:47]\n"
        "s_waitcnt lgkmcnt(1)\n"
        "ds_read_b128 v[12:15], v0 offset:6656\n"
        "v_mfma_f32_16x16x32_bf16 v[24:27], v[88:91], v[16:19], v[24:27]\n"
        "s_getreg_b32 s88, hwreg(HW_REG_IB_STS, 0, 4)\n"
        "v_mfma_f32_16x16x32_bf16 v[28:31], v[92:95], v[16:19], v[28:31]\n"
        "s_cmp_le_u32 s88, 15\n"
        "s_cselect_b32 s88, s73, s89\n"
        "s_cselect_b32 s73, s89, s73\n"
        "v_mfma_f32_16x16x32_bf16 v[32:35], v[16:19], v[96:99], v[32:35]\n"
        "buffer_store_dword v5, v3, s[40:43], s88 offen sc1\n"
        "v_mfma_f32_16x16x32_bf16 v[36:39], v[16:19], v[100:103], v[36:39]\n"
        "v_mfma_f32_16x16x32_bf16 v[40:43], v[104:107], v[16:19], v[40:43]\n"
        "v_mfma_f32_16x16x32_bf16 v[44:47], v[108:111], v[16:19], v[44:47]\n"
        "s_waitcnt lgkmcnt(1)\n"
        "ds_read_b128 v[16:19], v0 offset:7168\n"
        "v_mfma_f32_16x16x32_bf16 v[24:27], v[112:115], v[8:11], v[24:27]\n"
        "v_mfma_f32_16x16x32_bf16 v[28:31], v[116:119], v[8:11], v[28:31]\n"
        "v_mfma_f32_16x16x32_bf16 v[32:35], v[8:11], v[120:123], v[32:35]\n"
        "v_mfma_f32_16x16x32_bf16 v[36:39], v[8:11], v[124:127], v[36:39]\n"
        "v_mfma_f32_16x16x32_bf16 v[40:43], v[128:131], v[8:11], v[40:43]\n"
        "v_mfma_f32_16x16x32_bf16 v[44:47], v[132:135], v[8:11], v[44:47]\n"
        "s_waitcnt lgkmcnt(1)\n"
        "ds_read_b128 v[8:11], v0 offset:7680\n"
        "v_mfma_f32_16x16x32_bf16 v[24:27], v[136:139], v[12:15], v[24:27]\n"
        "v_mfma_f32_16x16x32_bf16 v[28:31], v[140:143], v[12:15], v[28:31]\n"
        "v_mfma_f32_16x16x32_bf16 v[32:35], v[12:15], v[144:147], v[32:35]\n"
        "buffer_store_dword v5, v3, s[40:43], s73 offen sc1\n"
        "s_add_u32 s74, s74, 1\n"
        "v_mfma_f32_16x16x32_bf16 v[36:39], v[12:15], v[148:151], v[36:39]\n"
        "s_mov_b32 s84, s89\n"
        "s_mov_b32 s73, s60\n"
        "s_add_u32 s68, s68, 1\n"
        "v_mfma_f32_16x16x32_bf16 v[40:43], v[152:155], v[12:15], v[40:43]\n"
        "s_bitcmp1_b64 s[52:53], s68\n"
        "s_cselect_b32 s66, 8192, 0\n"
        "s_lshl_b32 s75, s68, 21\n"
        "v_mfma_f32_16x16x32_bf16 v[44:47], v[156:159], v[12:15], v[44:47]\n"
        "s_waitcnt lgkmcnt(1)\n"
        "ds_read_b128 v[12:15], v0 offset:8192\n"
        "s_add_u32 s75, s75, s66\n"
        "v_mfma_f32_16x16x32_bf16 v[24:27], v[160:163], v[16:19], v[24:27]\n"
        "s_add_u32 s72, s75, s59\n"
        "v_mfma_f32_16x16x32_bf16 v[28:31], v[164:167], v[16:19], v[28:31]\n"
        "v_mfma_f32_16x16x32_bf16 v[32:35], v[16:19], v[168:171], v[32:35]\n"
        "v_mfma_f32_16x16x32_bf16 v[36:39], v[16:19], v[172:175], v[36:39]\n"
        "v_mfma_f32_16x16x32_bf16 v[40:43], v[176:179], v[16:19], v[40:43]\n"
        "v_mfma_f32_16x16x32_bf16 v[44:47], v[180:183], v[16:19], v[44:47]\n"
        "s_waitcnt lgkmcnt(1)\n"
        "ds_read_b128 v[16:19], v0 offset:8704\n"
        "v_mfma_f32_16x16x32_bf16 v[24:27], v[184:187], v[8:11], v[24:27]\n"
        "s_waitcnt vmcnt(2)\n"
        "v_cmp_gt_i32 vcc, s70, v4\n"
        "v_mfma_f32_16x16x32_bf16 v[28:31], v[188:191], v[8:11], v[28:31]\n"
        "v_mfma_f32_16x16x32_bf16 v[32:35], v[8:11], v[192:195], v[32:35]\n"
        "s_and_b64 vcc, vcc, 1\n"
        "v_mfma_f32_16x16x32_bf16 v[36:39], v[8:11], v[196:199], v[36:39]\n"
        "s_cbranch_vccnz .Ls1_i02_%=\n"
        "v_mfma_f32_16x16x32_bf16 v[40:43], v[200:203], v[8:11], v[40:43]\n"
        "buffer_load_dwordx4 v1, s[40:43], s71 offen offset:0 sc1 lds\n"
        "v_mfma_f32_16x16x32_bf16 v[44:47], v[204:207], v[8:11], v[44:47]\n"
        "s_waitcnt lgkmcnt(1)\n"
        "v_mfma_f32_16x16x32_bf16 v[24:27], v[208:211], v[12:15], v[24:27]\n"
        "buffer_load_dwordx4 v1, s[40:43], s71 offen offset:1024 sc1 lds\n"
        "v_mfma_f32_16x16x32_bf16 v[28:31], v[212:215], v[12:15], v[28:31]\n"
        "v_mfma_f32_16x16x32_bf16 v[32:35], v[12:15], v[216:219], v[32:35]\n"
        "buffer_load_dwordx4 v1, s[40:43], s71 offen offset:2048 sc1 lds\n"
        "v_mfma_f32_16x16x32_bf16 v[36:39], v[12:15], v[220:223], v[36:39]\n"
        "v_mfma_f32_16x16x32_bf16 v[40:43], v[224:227], v[12:15], v[40:43]\n"
        "buffer_load_dwordx4 v1, s[40:43], s71 offen offset:3072 sc1 lds\n"
        "s_add_u32 m0, s78, s85\n"
        "s_add_u32 s69, s69, 1\n"
        "v_mfma_f32_16x16x32_bf16 v[44:47], v[228:231], v[12:15], v[44:47]\n"
        "s_waitcnt lgkmcnt(0)\n"
        "v_mfma_f32_16x16x32_bf16 v[24:27], v[232:235], v[16:19], v[24:27]\n"
        "buffer_load_dwordx4 v1, s[40:43], s81 offen offset:0 sc1 lds\n"
        "v_mfma_f32_16x16x32_bf16 v[28:31], v[236:239], v[16:19], v[28:31]\n"
        "v_mfma_f32_16x16x32_bf16 v[32:35], v[16:19], v[240:243], v[32:35]\n"
        "v_mfma_f32_16x16x32_bf16 v[36:39], v[16:19], v[244:247], v[36:39]\n"
        "v_mfma_f32_16x16x32_bf16 v[40:43], v[248:251], v[16:19], v[40:43]\n"
        "v_mfma_f32_16x16x32_bf16 v[44:47], v[252:255], v[16:19], v[44:47]\n"
        "v_cvt_pk_bf16_f32 v24, v24, v25\n"
        "v_cvt_pk_bf16_f32 v25, v26, v27\n"
        "v_cvt_pk_bf16_f32 v26, v28, v29\n"
        "v_cvt_pk_bf16_f32 v27, v30, v31\n"
        "s_add_u32 s66, s72, 524288\n"
        "buffer_store_dwordx4 v[24:27], v1, s[40:43], s66 offen offset:0 sc1\n"
        "v_cvt_pk_bf16_f32 v72, v32, v33\n"
        "v_cvt_pk_bf16_f32 v73, v34, v35\n"
        "v_cvt_pk_bf16_f32 v76, v36, v37\n"
        "v_cvt_pk_bf16_f32 v77, v38, v39\n"
        "v_cvt_pk_bf16_f32 v40, v40, v41\n"
        "v_cvt_pk_bf16_f32 v41, v42, v43\n"
        "v_cvt_pk_bf16_f32 v42, v44, v45\n"
        "v_cvt_pk_bf16_f32 v43, v46, v47\n"
        "buffer_store_dwordx4 v[40:43], v1, s[40:43], s72 offen offset:0 sc1\n"
        "v_mov_b32 v5, s74\n"
        "s_mov_b32 s84, s60\n"
        "s_waitcnt vmcnt(2)\n"
        "s_branch .Ls1_go0_%=\n"
        ".Ls1_i02_%=:\n"
        "v_mfma_f32_16x16x32_bf16 v[40:43], v[200:203], v[8:11], v[40:43]\n"
        "buffer_load_dword v4, v2, s[40:43], s86 offen sc1\n"
        "v_mfma_f32_16x16x32_bf16 v[44:47], v[204:207], v[8:11], v[44:47]\n"
        "s_waitcnt lgkmcnt(1)\n"
        "v_mfma_f32_16x16x32_bf16 v[24:27], v[208:211], v[12:15], v[24:27]\n"
        "v_mfma_f32_16x16x32_bf16 v[28:31], v[212:215], v[12:15], v[28:31]\n"
        "v_mfma_f32_16x16x32_bf16 v[32:35], v[12:15], v[216:219], v[32:35]\n"
        "v_mfma_f32_16x16x32_bf16 v[36:39], v[12:15], v[220:223], v[36:39]\n"
        "v_mfma_f32_16x16x32_bf16 v[40:43], v[224:227], v[12:15], v[40:43]\n"
        "v_mfma_f32_16x16x32_bf16 v[44:47], v[228:231], v[12:15], v[44:47]\n"
        "s_waitcnt lgkmcnt(0)\n"
        "v_mfma_f32_16x16x32_bf16 v[24:27], v[232:235], v[16:19], v[24:27]\n"
        "v_mfma_f32_16x16x32_bf16 v[28:31], v[236:239], v[16:19], v[28:31]\n"
        "v_mfma_f32_16x16x32_bf16 v[32:35], v[16:19], v[240:243], v[32:35]\n"
        "v_mfma_f32_16x16x32_bf16 v[36:39], v[16:19], v[244:247], v[36:39]\n"
        "v_mfma_f32_16x16x32_bf16 v[40:43], v[248:251], v[16:19], v[40:43]\n"
        "v_mfma_f32_16x16x32_bf16 v[44:47], v[252:255], v[16:19], v[44:47]\n"
        "v_cvt_pk_bf16_f32 v24, v24, v25\n"
        "v_cvt_pk_bf16_f32 v25, v26, v27\n"
        "v_cvt_pk_bf16_f32 v26, v28, v29\n"
        "v_cvt_pk_bf16_f32 v27, v30, v31\n"
        "s_add_u32 s66, s72, 524288\n"
        "buffer_store_dwordx4 v[24:27], v1, s[40:43], s66 offen offset:0 sc1\n"
        "v_cvt_pk_bf16_f32 v72, v32, v33\n"
        "v_cvt_pk_bf16_f32 v73, v34, v35\n"
        "v_cvt_pk_bf16_f32 v76, v36, v37\n"
        "v_cvt_pk_bf16_f32 v77, v38, v39\n"
        "v_cvt_pk_bf16_f32 v40, v40, v41\n"
        "v_cvt_pk_bf16_f32 v41, v42, v43\n"
        "v_cvt_pk_bf16_f32 v42, v44, v45\n"
        "v_cvt_pk_bf16_f32 v43, v46, v47\n"
        "buffer_store_dwordx4 v[40:43], v1, s[40:43], s72 offen offset:0 sc1\n"
        "s_waitcnt vmcnt(2)\n"
        "v_cmp_gt_i32 vcc, s70, v4\n"
        "s_nop 0\n"
        "s_and_b64 vcc, vcc, 1\n"
        "s_cbranch_vccnz .Ls1_late0_%=\n"
        "buffer_load_dwordx4 v1, s[40:43], s71 offen offset:0 sc1 lds\n"
        "buffer_load_dwordx4 v1, s[40:43], s71 offen offset:1024 sc1 lds\n"
        "buffer_load_dwordx4 v1, s[40:43], s71 offen offset:2048 sc1 lds\n"
        "buffer_load_dwordx4 v1, s[40:43], s71 offen offset:3072 sc1 lds\n"
        "s_add_u32 m0, s78, s85\n"
        "s_add_u32 s69, s69, 1\n"
        "buffer_load_dwordx4 v1, s[40:43], s81 offen offset:0 sc1 lds\n"
        "s_waitcnt vmcnt(5)\n"
        "v_mov_b32 v5, s74\n"
        "buffer_store_dword v5, v3, s[40:43], s73 offen sc1\n"
        "s_waitcnt vmcnt(1)\n"
        "s_branch .Ls1_go0_%=\n"
        ".Ls1_late0_%=:\n"
        "s_waitcnt vmcnt(0)\n"
        "v_mov_b32 v5, s74\n"
        "buffer_store_dword v5, v3, s[40:43], s73 offen sc1\n"
        "s_mov_b32 s64, 0\n"
        ".Ls1_in0_top_%=:\n"
        "buffer_load_dword v4, v2, s[40:43], s86 offen sc1\n"
        "s_waitcnt vmcnt(0)\n"
        "v_cmp_gt_i32 vcc, s70, v4\n"
        "s_nop 0\n"
        "s_and_b64 vcc, vcc, 1\n"
        "s_cbranch_vccz .Ls1_in0_ok_%=\n"
        "s_add_u32 s64, s64, 1\n"
        "s_and_b32 s65, s64, 255\n"
        "s_cbranch_scc1 .Ls1_in0_go_%=\n"
        "s_mov_b32 s65, 49152\n"
        "s_cmp_lt_u32 s64, 1048576\n"
        "s_cbranch_scc1 .Ls1_in0_chk_%=\n"
        "v_mov_b32 v4, 1\n"
        "buffer_store_dword v4, off, s[40:43], s65 sc1\n"
        "s_waitcnt vmcnt(0)\n"
        ".Ls1_in0_chk_%=:\n"
        "buffer_load_dword v4, off, s[40:43], s65 sc1\n"
        "s_waitcnt vmcnt(0)\n"
        "v_readfirstlane_b32 s65, v4\n"
        "s_nop 0\n"
        "s_cmp_lg_u32 s65, 1\n"
        "s_cbranch_scc1 .Ls1_in0_go_%=\n"
        "s_endpgm\n"
        ".Ls1_in0_go_%=:\n"
        "s_branch .Ls1_in0_top_%=\n"
        ".Ls1_in0_ok_%=:\n"
        "buffer_load_dwordx4 v1, s[40:43], s71 offen offset:0 sc1 lds\n"
        "buffer_load_dwordx4 v1, s[40:43], s71 offen offset:1024 sc1 lds\n"
        "buffer_load_dwordx4 v1, s[40:43], s71 offen offset:2048 sc1 lds\n"
        "buffer_load_dwordx4 v1, s[40:43], s71 offen offset:3072 sc1 lds\n"
        "s_add_u32 m0, s78, s85\n"
        "s_add_u32 s69, s69, 1\n"
        "buffer_load_dwordx4 v1, s[40:43], s81 offen offset:0 sc1 lds\n"
        "s_waitcnt vmcnt(0)\n"
        ".Ls1_go0_%=:\n"
        "s_mov_b32 s72, 241172480\n"
        "s_mov_b32 s83, 241172480\n"
        "s_mov_b32 s73, 241172480\n"
        "v_add_u32 v0, s78, v6\n"
        "s_barrier\n"
        "ds_read_b128 v[8:11], v0 offset:0\n"
        "ds_read_b128 v[12:15], v0 offset:512\n"
        "ds_read_b128 v[16:19], v0 offset:1024\n"
        "s_waitcnt lgkmcnt(2)\n"
        "s_branch .Ls1_i1_%=\n"
        ".Ls1_i1_%=:\n"
        "v_mfma_f32_16x16x32_bf16 v[48:51], a[0:3], v[8:11], 0\n"
        "s_add_u32 s86, s76, 0\n"
        "buffer_load_dword v4, v2, s[40:43], s86 offen sc1\n"
        "v_mfma_f32_16x16x32_bf16 v[52:55], a[4:7], v[8:11], 0\n"
        "s_lshr_b32 s67, s69, 1\n"
        "v_cvt_pk_bf16_f32 v24, v24, v25\n"
        "v_cvt_pk_bf16_f32 v25, v26, v27\n"
        "v_mfma_f32_16x16x32_bf16 v[56:59], v[8:11], a[8:11], 0\n"
        "s_bitcmp1_b64 s[52:53], s67\n"
        "s_cselect_b32 s65, 8192, 0\n"
        "v_cvt_pk_bf16_f32 v26, v28, v29\n"
        "v_mfma_f32_16x16x32_bf16 v[60:63], v[8:11], a[12:15], 0\n"
        "s_lshl_b32 s77, s67, 21\n"
        "v_cvt_pk_bf16_f32 v27, v30, v31\n"
        "s_add_u32 s66, s72, 524288\n"
        "v_mfma_f32_16x16x32_bf16 v[64:67], a[16:19], v[8:11], 0\n"
        "s_add_u32 s77, s77, s65\n"
        "buffer_store_dwordx4 v[24:27], v1, s[40:43], s66 offen offset:0 sc1\n"
        "v_mfma_f32_16x16x32_bf16 v[68:71], a[20:23], v[8:11], 0\n"
        "s_waitcnt lgkmcnt(1)\n"
        "ds_read_b128 v[8:11], v0 offset:1536\n"
        "v_cvt_pk_bf16_f32 v72, v32, v33\n"
        "v_mfma_f32_16x16x32_bf16 v[48:51], a[24:27], v[12:15], v[48:51]\n"
        "s_add_u32 s71, s77, s58\n"
        "v_cvt_pk_bf16_f32 v73, v34, v35\n"
        "v_cvt_pk_bf16_f32 v76, v36, v37\n"
        "v_mfma_f32_16x16x32_bf16 v[52:55], a[28:31], v[12:15], v[52:55]\n"
        "s_add_u32 s81, s77, s62\n"
        "v_cvt_pk_bf16_f32 v77, v38, v39\n"
        "v_cvt_pk_bf16_f32 v40, v40, v41\n"
        "v_mfma_f32_16x16x32_bf16 v[56:59], v[12:15], a[32:35], v[56:59]\n"
        "s_add_u32 s78, s78, 18432\n"
        "v_cvt_pk_bf16_f32 v41, v42, v43\n"
        "v_cvt_pk_bf16_f32 v42, v44, v45\n"
        "v_mfma_f32_16x16x32_bf16 v[60:63], v[12:15], a[36:39], v[60:63]\n"
        "s_cmp_eq_u32 s78, 55296\n"
        "s_cselect_b32 s78, 0, s78\n"
        "v_cvt_pk_bf16_f32 v43, v46, v47\n"
        "v_mfma_f32_16x16x32_bf16 v[64:67], a[40:43], v[12:15], v[64:67]\n"
        "s_add_u32 m0, s78, s61\n"
        "v_mfma_f32_16x16x32_bf16 v[68:71], a[44:47], v[12:15], v[68:71]\n"
        "s_waitcnt lgkmcnt(1)\n"
        "ds_read_b128 v[12:15], v0 offset:2048\n"
        "v_mfma_f32_16x16x32_bf16 v[48:51], a[48:51], v[16:19], v[48:51]\n"
        "s_lshr_b32 s70, s69, 1\n"
        "buffer_store_dwordx4 v[40:43], v1, s[40:43], s72 offen offset:0 sc1\n"
        "v_mfma_f32_16x16x32_bf16 v[52:55], a[52:55], v[16:19], v[52:55]\n"
        "v_mfma_f32_16x16x32_bf16 v[56:59], v[16:19], a[56:59], v[56:59]\n"
        "s_waitcnt vmcnt(3)\n"
        "v_mfma_f32_16x16x32_bf16 v[60:63], v[16:19], a[60:63], v[60:63]\n"
        "buffer_store_dword v5, v3, s[40:43], s84 offen sc1\n"
        "v_mfma_f32_16x16x32_bf16 v[64:67], a[64:67], v[16:19], v[64:67]\n"
        "v_mov_b32 v5, s74\n"
        "v_mfma_f32_16x16x32_bf16 v[68:71], a[68:71], v[16:19], v[68:71]\n"
        "s_waitcnt lgkmcnt(1)\n"
        "ds_read_b128 v[16:19], v0 offset:2560\n"
        "v_mfma_f32_16x16x32_bf16 v[48:51], a[72:75], v[8:11], v[48:51]\n"
        "v_mfma_f32_16x16x32_bf16 v[52:55], a[76:79], v[8:11], v[52:55]\n"
        "v_mfma_f32_16x16x32_bf16 v[56:59], v[8:11], a[80:83], v[56:59]\n"
        "v_mfma_f32_16x16x32_bf16 v[60:63], v[8:11], a[84:87], v[60:63]\n"
        "v_mfma_f32_16x16x32_bf16 v[64:67], a[88:91], v[8:11], v[64:67]\n"
        "v_mfma_f32_16x16x32_bf16 v[68:71], a[92:95], v[8:11], v[68:71]\n"
        "s_waitcnt lgkmcnt(1)\n"
        "ds_read_b128 v[8:11], v0 offset:3072\n"
        "v_mfma_f32_16x16x32_bf16 v[48:51], a[96:99], v[12:15], v[48:51]\n"
        "v_mfma_f32_16x16x32_bf16 v[52:55], a[100:103], v[12:15], v[52:55]\n"
        "v_mfma_f32_16x16x32_bf16 v[56:59], v[12:15], a[104:107], v[56:59]\n"
        "v_mfma_f32_16x16x32_bf16 v[60:63], v[12:15], a[108:111], v[60:63]\n"
        "v_mfma_f32_16x16x32_bf16 v[64:67], a[112:115], v[12:15], v[64:67]\n"
        "s_waitcnt vmcnt(3)\n"
        "v_cmp_gt_i32 vcc, s70, v4\n"
        "v_mfma_f32_16x16x32_bf16 v[68:71], a[116:119], v[12:15], v[68:71]\n"
        "s_waitcnt lgkmcnt(1)\n"
        "ds_read_b128 v[12:15], v0 offset:3584\n"
        "v_mfma_f32_16x16x32_bf16 v[48:51], a[120:123], v[16:19], v[48:51]\n"
        "s_and_b64 vcc, vcc, 1\n"
        "v_mfma_f32_16x16x32_bf16 v[52:55], a[124:127], v[16:19], v[52:55]\n"
        "s_cbranch_vccnz .Ls1_i10_%=\n"
        "v_mfma_f32_16x16x32_bf16 v[56:59], v[16:19], a[128:131], v[56:59]\n"
        "buffer_load_dwordx4 v1, s[40:43], s71 offen offset:0 sc1 lds\n"
        "v_mfma_f32_16x16x32_bf16 v[60:63], v[16:19], a[132:135], v[60:63]\n"
        "v_mfma_f32_16x16x32_bf16 v[64:67], a[136:139], v[16:19], v[64:67]\n"
        "v_mfma_f32_16x16x32_bf16 v[68:71], a[140:143], v[16:19], v[68:71]\n"
        "s_waitcnt lgkmcnt(1)\n"
        "ds_read_b128 v[16:19], v0 offset:4096\n"
        "v_mfma_f32_16x16x32_bf16 v[48:51], a[144:147], v[8:11], v[48:51]\n"
        "s_getreg_b32 s88, hwreg(HW_REG_IB_STS, 0, 4)\n"
        "buffer_load_dwordx4 v1, s[40:43], s71 offen offset:1024 sc1 lds\n"
        "v_mfma_f32_16x16x32_bf16 v[52:55], a[148:151], v[8:11], v[52:55]\n"
        "s_cmp_le_u32 s88, 2\n"
        "s_cselect_b32 s88, s73, s89\n"
        "s_cselect_b32 s73, s89, s73\n"
        "v_mfma_f32_16x16x32_bf16 v[56:59], v[8:11], a[152:155], v[56:59]\n"
        "buffer_store_dword v5, v3, s[40:43], s88 offen sc1\n"
        "v_mfma_f32_16x16x32_bf16 v[60:63], v[8:11], a[156:159], v[60:63]\n"
        "v_mfma_f32_16x16x32_bf16 v[64:67], a[160:163], v[8:11], v[64:67]\n"
        "buffer_load_dwordx4 v1, s[40:43], s71 offen offset:2048 sc1 lds\n"
        "v_mfma_f32_16x16x32_bf16 v[68:71], a[164:167], v[8:11], v[68:71]\n"
        "s_waitcnt lgkmcnt(1)\n"
        "ds_read_b128 v[8:11], v0 offset:4608\n"
        "v_mfma_f32_16x16x32_bf16 v[48:51], a[168:171], v[12:15], v[48:51]\n"
        "v_mfma_f32_16x16x32_bf16 v[52:55], a[172:175], v[12:15], v[52:55]\n"
        "v_mfma_f32_16x16x32_bf16 v[56:59], v[12:15], a[176:179], v[56:59]\n"
        "s_getreg_b32 s88, hwreg(HW_REG_IB_STS, 0, 4)\n"
        "buffer_load_dwordx4 v1, s[40:43], s71 offen offset:3072 sc1 lds\n"
        "s_add_u32 m0, s78, s85\n"
        "v_mfma_f32_16x16x32_bf16 v[60:63], v[12:15], a[180:183], v[60:63]\n"
        "s_cmp_le_u32 s88, 5\n"
        "s_cselect_b32 s88, s73, s89\n"
        "s_cselect_b32 s73, s89, s73\n"
        "v_mfma_f32_16x16x32_bf16 v[64:67], a[184:187], v[12:15], v[64:67]\n"
        "s_add_u32 s69, s69, 1\n"
        "buffer_store_dword v5, v3, s[40:43], s88 offen sc1\n"
        "v_mfma_f32_16x16x32_bf16 v[68:71], a[188:191], v[12:15], v[68:71]\n"
        "s_waitcnt lgkmcnt(1)\n"
        "ds_read_b128 v[12:15], v0 offset:5120\n"
        "v_mfma_f32_16x16x32_bf16 v[48:51], a[192:195], v[16:19], v[48:51]\n"
        "buffer_load_dwordx4 v1, s[40:43], s81 offen offset:0 sc1 lds\n"
        "v_mfma_f32_16x16x32_bf16 v[52:55], a[196:199], v[16:19], v[52:55]\n"
        "v_mfma_f32_16x16x32_bf16 v[56:59], v[16:19], a[200:203], v[56:59]\n"
        "v_mfma_f32_16x16x32_bf16 v[60:63], v[16:19], a[204:207], v[60:63]\n"
        "v_mfma_f32_16x16x32_bf16 v[64:67], a[208:211], v[16:19], v[64:67]\n"
        "s_getreg_b32 s88, hwreg(HW_REG_IB_STS, 0, 4)\n"
        "v_mfma_f32_16x16x32_bf16 v[68:71], a[212:215], v[16:19], v[68:71]\n"
        "s_waitcnt lgkmcnt(1)\n"
        "ds_read_b128 v[16:19], v0 offset:5632\n"
        "v_mfma_f32_16x16x32_bf16 v[48:51], a[216:219], v[8:11], v[48:51]\n"
        "s_cmp_le_u32 s88, 8\n"
        "s_cselect_b32 s88, s73, s89\n"
        "s_cselect_b32 s73, s89, s73\n"
        "v_mfma_f32_16x16x32_bf16 v[52:55], a[220:223], v[8:11], v[52:55]\n"
        "buffer_store_dword v5, v3, s[40:43], s88 offen sc1\n"
        "v_mfma_f32_16x16x32_bf16 v[56:59], v[8:11], a[224:227], v[56:59]\n"
        "v_mfma_f32_16x16x32_bf16 v[60:63], v[8:11], a[228:231], v[60:63]\n"
        "v_mfma_f32_16x16x32_bf16 v[64:67], a[232:235], v[8:11], v[64:67]\n"
        "v_mfma_f32_16x16x32_bf16 v[68:71], a[236:239], v[8:11], v[68:71]\n"
        "s_waitcnt lgkmcnt(1)\n"
        "ds_read_b128 v[8:11], v0 offset:6144\n"
        "v_mfma_f32_16x16x32_bf16 v[48:51], a[240:243], v[12:15], v[48:51]\n"
        "v_mfma_f32_16x16x32_bf16 v[52:55], a[244:247], v[12:15], v[52:55]\n"
        "v_mfma_f32_16x16x32_bf16 v[56:59], v[12:15], a[248:251], v[56:59]\n"
        "v_mfma_f32_16x16x32_bf16 v[60:63], v[12:15], a[252:255], v[60:63]\n"
        "v_mfma_f32_16x16x32_bf16 v[64:67], v[80:83], v[12:15], v[64:67]\n"
        "v_mfma_f32_16x16x32_bf16 v[68:71], v[84:87], v[12:15], v[68:71]\n"
        "s_waitcnt lgkmcnt(1)\n"
        "ds_read_b128 v[12:15], v0 offset:6656\n"
        "v_mfma_f32_16x16x32_bf16 v[48:51], v[88:91], v[16:19], v[48:51]\n"
        "s_waitcnt vmcnt(9)\n"
        "buffer_store_dword v5, v3, s[40:43], s73 offen sc1\n"
        "s_add_u32 s74, s74, 1\n"
        "v_mfma_f32_16x16x32_bf16 v[52:55], v[92:95], v[16:19], v[52:55]\n"
        "s_mov_b32 s84, s89\n"
        "s_mov_b32 s73, s60\n"
        "s_add_u32 s72, s75, s59\n"
        "v_mfma_f32_16x16x32_bf16 v[56:59], v[16:19], v[96:99], v[56:59]\n"
        "v_mfma_f32_16x16x32_bf16 v[60:63], v[16:19], v[100:103], v[60:63]\n"
        "v_mfma_f32_16x16x32_bf16 v[64:67], v[104:107], v[16:19], v[64:67]\n"
        "v_mfma_f32_16x16x32_bf16 v[68:71], v[108:111], v[16:19], v[68:71]\n"
        "s_waitcnt lgkmcnt(1)\n"
        "ds_read_b128 v[16:19], v0 offset:7168\n"
        "v_mfma_f32_16x16x32_bf16 v[48:51], v[112:115], v[8:11], v[48:51]\n"
        "v_mfma_f32_16x16x32_bf16 v[52:55], v[116:119], v[8:11], v[52:55]\n"
        "v_mfma_f32_16x16x32_bf16 v[56:59], v[8:11], v[120:123], v[56:59]\n"
        "v_mfma_f32_16x16x32_bf16 v[60:63], v[8:11], v[124:127], v[60:63]\n"
        "v_mfma_f32_16x16x32_bf16 v[64:67], v[128:131], v[8:11], v[64:67]\n"
        "v_mfma_f32_16x16x32_bf16 v[68:71], v[132:135], v[8:11], v[68:71]\n"
        "s_waitcnt lgkmcnt(1)\n"
        "ds_read_b128 v[8:11], v0 offset:7680\n"
        "v_mfma_f32_16x16x32_bf16 v[48:51], v[136:139], v[12:15], v[48:51]\n"
        "v_mfma_f32_16x16x32_bf16 v[52:55], v[140:143], v[12:15], v[52:55]\n"
        "v_mfma_f32_16x16x32_bf16 v[56:59], v[12:15], v[144:147], v[56:59]\n"
        "v_mfma_f32_16x16x32_bf16 v[60:63], v[12:15], v[148:151], v[60:63]\n"
        "v_mfma_f32_16x16x32_bf16 v[64:67], v[152:155], v[12:15], v[64:67]\n"
        "v_mfma_f32_16x16x32_bf16 v[68:71], v[156:159], v[12:15], v[68:71]\n"
        "s_waitcnt lgkmcnt(1)\n"
        "ds_read_b128 v[12:15], v0 offset:8192\n"
        "v_mfma_f32_16x16x32_bf16 v[48:51], v[160:163], v[16:19], v[48:51]\n"
        "v_mfma_f32_16x16x32_bf16 v[52:55], v[164:167], v[16:19], v[52:55]\n"
        "v_mfma_f32_16x16x32_bf16 v[56:59], v[16:19], v[168:171], v[56:59]\n"
        "v_mfma_f32_16x16x32_bf16 v[60:63], v[16:19], v[172:175], v[60:63]\n"
        "v_mfma_f32_16x16x32_bf16 v[64:67], v[176:179], v[16:19], v[64:67]\n"
        "v_mfma_f32_16x16x32_bf16 v[68:71], v[180:183], v[16:19], v[68:71]\n"
        "s_waitcnt lgkmcnt(1)\n"
        "ds_read_b128 v[16:19], v0 offset:8704\n"
        "v_mfma_f32_16x16x32_bf16 v[48:51], v[184:187], v[8:11], v[48:51]\n"
        "v_mfma_f32_16x16x32_bf16 v[52:55], v[188:191], v[8:11], v[52:55]\n"
        "v_mfma_f32_16x16x32_bf16 v[56:59], v[8:11], v[192:195], v[56:59]\n"
        "v_mfma_f32_16x16x32_bf16 v[60:63], v[8:11], v[196:199], v[60:63]\n"
        "v_mfma_f32_16x16x32_bf16 v[64:67], v[200:203], v[8:11], v[64:67]\n"
        "s_waitcnt vmcnt(2)\n"
        "s_barrier\n"
        "v_add_u32 v0, s78, v6\n"
        "v_mfma_f32_16x16x32_bf16 v[68:71], v[204:207], v[8:11], v[68:71]\n"
        "s_waitcnt lgkmcnt(1)\n"
        "ds_read_b128 v[8:11], v0 offset:0\n"
        "v_mfma_f32_16x16x32_bf16 v[48:51], v[208:211], v[12:15], v[48:51]\n"
        "v_mfma_f32_16x16x32_bf16 v[52:55], v[212:215], v[12:15], v[52:55]\n"
        "v_mfma_f32_16x16x32_bf16 v[56:59], v[12:15], v[216:219], v[56:59]\n"
        "v_mfma_f32_16x16x32_bf16 v[60:63], v[12:15], v[220:223], v[60:63]\n"
        "v_mfma_f32_16x16x32_bf16 v[64:67], v[224:227], v[12:15], v[64:67]\n"
        "v_mfma_f32_16x16x32_bf16 v[68:71], v[228:231], v[12:15], v[68:71]\n"
        "s_waitcnt lgkmcnt(1)\n"
        "ds_read_b128 v[12:15], v0 offset:512\n"
        "v_mfma_f32_16x16x32_bf16 v[48:51], v[232:235], v[16:19], v[48:51]\n"
        "v_mfma_f32_16x16x32_bf16 v[52:55], v[236:239], v[16:19], v[52:55]\n"
        "v_mfma_f32_16x16x32_bf16 v[56:59], v[16:19], v[240:243], v[56:59]\n"
        "v_mfma_f32_16x16x32_bf16 v[60:63], v[16:19], v[244:247], v[60:63]\n"
        "v_mfma_f32_16x16x32_bf16 v[64:67], v[248:251], v[16:19], v[64:67]\n"
        "v_mfma_f32_16x16x32_bf16 v[68:71], v[252:255], v[16:19], v[68:71]\n"
        "s_waitcnt lgkmcnt(1)\n"
        "ds_read_b128 v[16:19], v0 offset:1024\n"
        "s_branch .Ls1_i0_%=\n"
        ".Ls1_i10_%=:\n"
        "v_mfma_f32_16x16x32_bf16 v[56:59], v[16:19], a[128:131], v[56:59]\n"
        "buffer_load_dword v4, v2, s[40:43], s86 offen sc1\n"
        "v_mfma_f32_16x16x32_bf16 v[60:63], v[16:19], a[132:135], v[60:63]\n"
        "v_mfma_f32_16x16x32_bf16 v[64:67], a[136:139], v[16:19], v[64:67]\n"
        "v_mfma_f32_16x16x32_bf16 v[68:71], a[140:143], v[16:19], v[68:71]\n"
        "s_waitcnt lgkmcnt(1)\n"
        "ds_read_b128 v[16:19], v0 offset:4096\n"
        "v_mfma_f32_16x16x32_bf16 v[48:51], a[144:147], v[8:11], v[48:51]\n"
        "s_getreg_b32 s88, hwreg(HW_REG_IB_STS, 0, 4)\n"
        "v_mfma_f32_16x16x32_bf16 v[52:55], a[148:151], v[8:11], v[52:55]\n"
        "s_cmp_le_u32 s88, 2\n"
        "s_cselect_b32 s88, s73, s89\n"
        "s_cselect_b32 s73, s89, s73\n"
        "v_mfma_f32_16x16x32_bf16 v[56:59], v[8:11], a[152:155], v[56:59]\n"
        "buffer_store_dword v5, v3, s[40:43], s88 offen sc1\n"
        "v_mfma_f32_16x16x32_bf16 v[60:63], v[8:11], a[156:159], v[60:63]\n"
        "v_mfma_f32_16x16x32_bf16 v[64:67], a[160:163], v[8:11], v[64:67]\n"
        "v_mfma_f32_16x16x32_bf16 v[68:71], a[164:167], v[8:11], v[68:71]\n"
        "s_waitcnt lgkmcnt(1)\n"
        "ds_read_b128 v[8:11], v0 offset:4608\n"
        "v_mfma_f32_16x16x32_bf16 v[48:51], a[168:171], v[12:15], v[48:51]\n"
        "v_mfma_f32_16x16x32_bf16 v[52:55], a[172:175], v[12:15], v[52:55]\n"
        "v_mfma_f32_16x16x32_bf16 v[56:59], v[12:15], a[176:179], v[56:59]\n"
        "s_getreg_b32 s88, hwreg(HW_REG_IB_STS, 0, 4)\n"
        "v_mfma_f32_16x16x32_bf16 v[60:63], v[12:15], a[180:183], v[60:63]\n"
        "s_cmp_le_u32 s88, 3\n"
        "s_cselect_b32 s88, s73, s89\n"
        "s_cselect_b32 s73, s89, s73\n"
        "v_mfma_f32_16x16x32_bf16 v[64:67], a[184:187], v[12:15], v[64:67]\n"
        "buffer_store_dword v5, v3, s[40:43], s88 offen sc1\n"
        "v_mfma_f32_16x16x32_bf16 v[68:71], a[188:191], v[12:15], v[68:71]\n"
        "s_waitcnt lgkmcnt(1)\n"
        "ds_read_b128 v[12:15], v0 offset:5120\n"
        "v_mfma_f32_16x16x32_bf16 v[48:51], a[192:195], v[16:19], v[48:51]\n"
        "v_mfma_f32_16x16x32_bf16 v[52:55], a[196:199], v[16:19], v[52:55]\n"
        "v_mfma_f32_16x16x32_bf16 v[56:59], v[16:19], a[200:203], v[56:59]\n"
        "v_mfma_f32_16x16x32_bf16 v[60:63], v[16:19], a[204:207], v[60:63]\n"
        "v_mfma_f32_16x16x32_bf16 v[64:67], a[208:211], v[16:19], v[64:67]\n"
        "s_getreg_b32 s88, hwreg(HW_REG_IB_STS, 0, 4)\n"
        "v_mfma_f32_16x16x32_bf16 v[68:71], a[212:215], v[16:19], v[68:71]\n"
        "s_waitcnt lgkmcnt(1)\n"
        "ds_read_b128 v[16:19], v0 offset:5632\n"
        "v_mfma_f32_16x16x32_bf16 v[48:51], a[216:219], v[8:11], v[48:51]\n"
        "s_cmp_le_u32 s88, 4\n"
        "s_cselect_b32 s88, s73, s89\n"
        "s_cselect_b32 s73, s89, s73\n"
        "v_mfma_f32_16x16x32_bf16 v[52:55], a[220:223], v[8:11], v[52:55]\n"
        "buffer_store_dword v5, v3, s[40:43], s88 offen sc1\n"
        "v_mfma_f32_16x16x32_bf16 v[56:59], v[8:11], a[224:227], v[56:59]\n"
        "v_mfma_f32_16x16x32_bf16 v[60:63], v[8:11], a[228:231], v[60:63]\n"
        "v_mfma_f32_16x16x32_bf16 v[64:67], a[232:235], v[8:11], v[64:67]\n"
        "v_mfma_f32_16x16x32_bf16 v[68:71], a[236:239], v[8:11], v[68:71]\n"
        "s_waitcnt vmcnt(3)\n"
        "v_cmp_gt_i32 vcc, s70, v4\n"
        "s_waitcnt lgkmcnt(1)\n"
        "ds_read_b128 v[8:11], v0 offset:6144\n"
        "v_mfma_f32_16x16x32_bf16 v[48:51], a[240:243], v[12:15], v[48:51]\n"
        "v_mfma_f32_16x16x32_bf16 v[52:55], a[244:247], v[12:15], v[52:55]\n"
        "s_and_b64 vcc, vcc, 1\n"
        "v_mfma_f32_16x16x32_bf16 v[56:59], v[12:15], a[248:251], v[56:59]\n"
        "s_cbranch_vccnz .Ls1_i11_%=\n"
        "v_mfma_f32_16x16x32_bf16 v[60:63], v[12:15], a[252:255], v[60:63]\n"
        "buffer_load_dwordx4 v1, s[40:43], s71 offen offset:0 sc1 lds\n"
        "v_mfma_f32_16x16x32_bf16 v[64:67], v[80:83], v[12:15], v[64:67]\n"
        "v_mfma_f32_16x16x32_bf16 v[68:71], v[84:87], v[12:15], v[68:71]\n"
        "s_waitcnt lgkmcnt(1)\n"
        "ds_read_b128 v[12:15], v0 offset:6656\n"
        "v_mfma_f32_16x16x32_bf16 v[48:51], v[88:91], v[16:19], v[48:51]\n"
        "buffer_store_dword v5, v3, s[40:43], s73 offen sc1\n"
        "s_add_u32 s74, s74, 1\n"
        "buffer_load_dwordx4 v1, s[40:43], s71 offen offset:1024 sc1 lds\n"
        "v_mfma_f32_16x16x32_bf16 v[52:55], v[92:95], v[16:19], v[52:55]\n"
        "s_mov_b32 s84, s89\n"
        "s_mov_b32 s73, s60\n"
        "s_add_u32 s72, s75, s59\n"
        "v_mfma_f32_16x16x32_bf16 v[56:59], v[16:19], v[96:99], v[56:59]\n"
        "v_mfma_f32_16x16x32_bf16 v[60:63], v[16:19], v[100:103], v[60:63]\n"
        "buffer_load_dwordx4 v1, s[40:43], s71 offen offset:2048 sc1 lds\n"
        "v_mfma_f32_16x16x32_bf16 v[64:67], v[104:107], v[16:19], v[64:67]\n"
        "v_mfma_f32_16x16x32_bf16 v[68:71], v[108:111], v[16:19], v[68:71]\n"
        "s_waitcnt lgkmcnt(1)\n"
        "ds_read_b128 v[16:19], v0 offset:7168\n"
        "v_mfma_f32_16x16x32_bf16 v[48:51], v[112:115], v[8:11], v[48:51]\n"
        "buffer_load_dwordx4 v1, s[40:43], s71 offen offset:3072 sc1 lds\n"
        "s_add_u32 m0, s78, s85\n"
        "v_mfma_f32_16x16x32_bf16 v[52:55], v[116:119], v[8:11], v[52:55]\n"
        "s_add_u32 s69, s69, 1\n"
        "v_mfma_f32_16x16x32_bf16 v[56:59], v[8:11], v[120:123], v[56:59]\n"
        "v_mfma_f32_16x16x32_bf16 v[60:63], v[8:11], v[124:127], v[60:63]\n"
        "buffer_load_dwordx4 v1, s[40:43], s81 offen offset:0 sc1 lds\n"
        "v_mfma_f32_16x16x32_bf16 v[64:67], v[128:131], v[8:11], v[64:67]\n"
        "v_mfma_f32_16x16x32_bf16 v[68:71], v[132:135], v[8:11], v[68:71]\n"
        "s_waitcnt lgkmcnt(1)\n"
        "ds_read_b128 v[8:11], v0 offset:7680\n"
        "v_mfma_f32_16x16x32_bf16 v[48:51], v[136:139], v[12:15], v[48:51]\n"
        "v_mfma_f32_16x16x32_bf16 v[52:55], v[140:143], v[12:15], v[52:55]\n"
        "v_mfma_f32_16x16x32_bf16 v[56:59], v[12:15], v[144:147], v[56:59]\n"
        "v_mfma_f32_16x16x32_bf16 v[60:63], v[12:15], v[148:151], v[60:63]\n"
        "v_mfma_f32_16x16x32_bf16 v[64:67], v[152:155], v[12:15], v[64:67]\n"
        "v_mfma_f32_16x16x32_bf16 v[68:71], v[156:159], v[12:15], v[68:71]\n"
        "s_waitcnt lgkmcnt(1)\n"
        "ds_read_b128 v[12:15], v0 offset:8192\n"
        "v_mfma_f32_16x16x32_bf16 v[48:51], v[160:163], v[16:19], v[48:51]\n"
        "v_mfma_f32_16x16x32_bf16 v[52:55], v[164:167], v[16:19], v[52:55]\n"
        "v_mfma_f32_16x16x32_bf16 v[56:59], v[16:19], v[168:171], v[56:59]\n"
        "v_mfma_f32_16x16x32_bf16 v[60:63], v[16:19], v[172:175], v[60:63]\n"
        "v_mfma_f32_16x16x32_bf16 v[64:67], v[176:179], v[16:19], v[64:67]\n"
        "v_mfma_f32_16x16x32_bf16 v[68:71], v[180:183], v[16:19], v[68:71]\n"
        "s_waitcnt lgkmcnt(1)\n"
        "ds_read_b128 v[16:19], v0 offset:8704\n"
        "v_mfma_f32_16x16x32_bf16 v[48:51], v[184:187], v[8:11], v[48:51]\n"
        "v_mfma_f32_16x16x32_bf16 v[52:55], v[188:191], v[8:11], v[52:55]\n"
        "v_mfma_f32_16x16x32_bf16 v[56:59], v[8:11], v[192:195], v[56:59]\n"
        "v_mfma_f32_16x16x32_bf16 v[60:63], v[8:11], v[196:199], v[60:63]\n"
        "v_mfma_f32_16x16x32_bf16 v[64:67], v[200:203], v[8:11], v[64:67]\n"
        "s_waitcnt vmcnt(0)\n"
        "s_barrier\n"
        "v_add_u32 v0, s78, v6\n"
        "v_mfma_f32_16x16x32_bf16 v[68:71], v[204:207], v[8:11], v[68:71]\n"
        "s_waitcnt lgkmcnt(1)\n"
        "ds_read_b128 v[8:11], v0 offset:0\n"
        "v_mfma_f32_16x16x32_bf16 v[48:51], v[208:211], v[12:15], v[48:51]\n"
        "v_mfma_f32_16x16x32_bf16 v[52:55], v[212:215], v[12:15], v[52:55]\n"
        "v_mfma_f32_16x16x32_bf16 v[56:59], v[12:15], v[216:219], v[56:59]\n"
        "v_mfma_f32_16x16x32_bf16 v[60:63], v[12:15], v[220:223], v[60:63]\n"
        "v_mfma_f32_16x16x32_bf16 v[64:67], v[224:227], v[12:15], v[64:67]\n"
        "v_mfma_f32_16x16x32_bf16 v[68:71], v[228:231], v[12:15], v[68:71]\n"
        "s_waitcnt lgkmcnt(1)\n"
        "ds_read_b128 v[12:15], v0 offset:512\n"
        "v_mfma_f32_16x16x32_bf16 v[48:51], v[232:235], v[16:19], v[48:51]\n"
        "v_mfma_f32_16x16x32_bf16 v[52:55], v[236:239], v[16:19], v[52:55]\n"
        "v_mfma_f32_16x16x32_bf16 v[56:59], v[16:19], v[240:243], v[56:59]\n"
        "v_mfma_f32_16x16x32_bf16 v[60:63], v[16:19], v[244:247], v[60:63]\n"
        "v_mfma_f32_16x16x32_bf16 v[64:67], v[248:251], v[16:19], v[64:67]\n"
        "v_mfma_f32_16x16x32_bf16 v[68:71], v[252:255], v[16:19], v[68:71]\n"
        "s_waitcnt lgkmcnt(1)\n"
        "ds_read_b128 v[16:19], v0 offset:1024\n"
        "s_branch .Ls1_i0_%=\n"
        ".Ls1_i11_%=:\n"
        "v_mfma_f32_16x16x32_bf16 v[60:63], v[12:15], a[252:255], v[60:63]\n"
        "buffer_load_dword v4, v2, s[40:43], s86 offen sc1\n"
        "v_mfma_f32_16x16x32_bf16 v[64:67], v[80:83], v[12:15], v[64:67]\n"
        "v_mfma_f32_16x16x32_bf16 v[68:71], v[84:87], v[12:15], v[68:71]\n"
        "s_waitcnt lgkmcnt(1)\n"
        "ds_read_b128 v[12:15], v0 offset:6656\n"
        "v_mfma_f32_16x16x32_bf16 v[48:51], v[88:91], v[16:19], v[48:51]\n"
        "buffer_store_dword v5, v3, s[40:43], s73 offen sc1\n"
        "s_add_u32 s74, s74, 1\n"
        "v_mfma_f32_16x16x32_bf16 v[52:55], v[92:95], v[16:19], v[52:55]\n"
        "s_mov_b32 s84, s89\n"
        "s_mov_b32 s73, s60\n"
        "s_add_u32 s72, s75, s59\n"
        "v_mfma_f32_16x16x32_bf16 v[56:59], v[16:19], v[96:99], v[56:59]\n"
        "v_mfma_f32_16x16x32_bf16 v[60:63], v[16:19], v[100:103], v[60:63]\n"
        "v_mfma_f32_16x16x32_bf16 v[64:67], v[104:107], v[16:19], v[64:67]\n"
        "v_mfma_f32_16x16x32_bf16 v[68:71], v[108:111], v[16:19], v[68:71]\n"
        "s_waitcnt lgkmcnt(1)\n"
        "ds_read_b128 v[16:19], v0 offset:7168\n"
        "v_mfma_f32_16x16x32_bf16 v[48:51], v[112:115], v[8:11], v[48:51]\n"
        "v_mfma_f32_16x16x32_bf16 v[52:55], v[116:119], v[8:11], v[52:55]\n"
        "v_mfma_f32_16x16x32_bf16 v[56:59], v[8:11], v[120:123], v[56:59]\n"
        "v_mfma_f32_16x16x32_bf16 v[60:63], v[8:11], v[124:127], v[60:63]\n"
        "v_mfma_f32_16x16x32_bf16 v[64:67], v[128:131], v[8:11], v[64:67]\n"
        "v_mfma_f32_16x16x32_bf16 v[68:71], v[132:135], v[8:11], v[68:71]\n"
        "s_waitcnt lgkmcnt(1)\n"
        "ds_read_b128 v[8:11], v0 offset:7680\n"
        "v_mfma_f32_16x16x32_bf16 v[48:51], v[136:139], v[12:15], v[48:51]\n"
        "v_mfma_f32_16x16x32_bf16 v[52:55], v[140:143], v[12:15], v[52:55]\n"
        "v_mfma_f32_16x16x32_bf16 v[56:59], v[12:15], v[144:147], v[56:59]\n"
        "v_mfma_f32_16x16x32_bf16 v[60:63], v[12:15], v[148:151], v[60:63]\n"
        "v_mfma_f32_16x16x32_bf16 v[64:67], v[152:155], v[12:15], v[64:67]\n"
        "v_mfma_f32_16x16x32_bf16 v[68:71], v[156:159], v[12:15], v[68:71]\n"
        "s_waitcnt lgkmcnt(1)\n"
        "ds_read_b128 v[12:15], v0 offset:8192\n"
        "v_mfma_f32_16x16x32_bf16 v[48:51], v[160:163], v[16:19], v[48:51]\n"
        "v_mfma_f32_16x16x32_bf16 v[52:55], v[164:167], v[16:19], v[52:55]\n"
        "v_mfma_f32_16x16x32_bf16 v[56:59], v[16:19], v[168:171], v[56:59]\n"
        "v_mfma_f32_16x16x32_bf16 v[60:63], v[16:19], v[172:175], v[60:63]\n"
        "v_mfma_f32_16x16x32_bf16 v[64:67], v[176:179], v[16:19], v[64:67]\n"
        "v_mfma_f32_16x16x32_bf16 v[68:71], v[180:183], v[16:19], v[68:71]\n"
        "s_waitcnt lgkmcnt(1)\n"
        "ds_read_b128 v[16:19], v0 offset:8704\n"
        "v_mfma_f32_16x16x32_bf16 v[48:51], v[184:187], v[8:11], v[48:51]\n"
        "s_waitcnt vmcnt(1)\n"
        "v_cmp_gt_i32 vcc, s70, v4\n"
        "v_mfma_f32_16x16x32_bf16 v[52:55], v[188:191], v[8:11], v[52:55]\n"
        "v_mfma_f32_16x16x32_bf16 v[56:59], v[8:11], v[192:195], v[56:59]\n"
        "s_and_b64 vcc, vcc, 1\n"
        "v_mfma_f32_16x16x32_bf16 v[60:63], v[8:11], v[196:199], v[60:63]\n"
        "s_cbranch_vccnz .Ls1_i12_%=\n"
        "v_mfma_f32_16x16x32_bf16 v[64:67], v[200:203], v[8:11], v[64:67]\n"
        "buffer_load_dwordx4 v1, s[40:43], s71 offen offset:0 sc1 lds\n"
        "v_mfma_f32_16x16x32_bf16 v[68:71], v[204:207], v[8:11], v[68:71]\n"
        "s_waitcnt lgkmcnt(1)\n"
        "v_mfma_f32_16x16x32_bf16 v[48:51], v[208:211], v[12:15], v[48:51]\n"
        "buffer_load_dwordx4 v1, s[40:43], s71 offen offset:1024 sc1 lds\n"
        "v_mfma_f32_16x16x32_bf16 v[52:55], v[212:215], v[12:15], v[52:55]\n"
        "v_mfma_f32_16x16x32_bf16 v[56:59], v[12:15], v[216:219], v[56:59]\n"
        "buffer_load_dwordx4 v1, s[40:43], s71 offen offset:2048 sc1 lds\n"
        "v_mfma_f32_16x16x32_bf16 v[60:63], v[12:15], v[220:223], v[60:63]\n"
        "v_mfma_f32_16x16x32_bf16 v[64:67], v[224:227], v[12:15], v[64:67]\n"
        "buffer_load_dwordx4 v1, s[40:43], s71 offen offset:3072 sc1 lds\n"
        "s_add_u32 m0, s78, s85\n"
        "s_add_u32 s69, s69, 1\n"
        "v_mfma_f32_16x16x32_bf16 v[68:71], v[228:231], v[12:15], v[68:71]\n"
        "s_waitcnt lgkmcnt(0)\n"
        "v_mfma_f32_16x16x32_bf16 v[48:51], v[232:235], v[16:19], v[48:51]\n"
        "buffer_load_dwordx4 v1, s[40:43], s81 offen offset:0 sc1 lds\n"
        "v_mfma_f32_16x16x32_bf16 v[52:55], v[236:239], v[16:19], v[52:55]\n"
        "v_mfma_f32_16x16x32_bf16 v[56:59], v[16:19], v[240:243], v[56:59]\n"
        "v_mfma_f32_16x16x32_bf16 v[60:63], v[16:19], v[244:247], v[60:63]\n"
        "v_mfma_f32_16x16x32_bf16 v[64:67], v[248:251], v[16:19], v[64:67]\n"
        "v_mfma_f32_16x16x32_bf16 v[68:71], v[252:255], v[16:19], v[68:71]\n"
        "v_cvt_pk_bf16_f32 v48, v48, v49\n"
        "v_cvt_pk_bf16_f32 v49, v50, v51\n"
        "v_cvt_pk_bf16_f32 v50, v52, v53\n"
        "v_cvt_pk_bf16_f32 v51, v54, v55\n"
        "s_add_u32 s66, s72, 524288\n"
        "buffer_store_dwordx4 v[48:51], v1, s[40:43], s66 offen offset:1024 sc1\n"
        "v_cvt_pk_bf16_f32 v74, v56, v57\n"
        "v_cvt_pk_bf16_f32 v75, v58, v59\n"
        "s_add_u32 s66, s72, 1048576\n"
        "buffer_store_dwordx4 v[72:75], v1, s[40:43], s66 offen offset:0 sc1\n"
        "v_cvt_pk_bf16_f32 v78, v60, v61\n"
        "v_cvt_pk_bf16_f32 v79, v62, v63\n"
        "buffer_store_dwordx4 v[76:79], v1, s[40:43], s66 offen offset:1024 sc1\n"
        "v_cvt_pk_bf16_f32 v64, v64, v65\n"
        "v_cvt_pk_bf16_f32 v65, v66, v67\n"
        "v_cvt_pk_bf16_f32 v66, v68, v69\n"
        "v_cvt_pk_bf16_f32 v67, v70, v71\n"
        "buffer_store_dwordx4 v[64:67], v1, s[40:43], s72 offen offset:1024 sc1\n"
        "v_mov_b32 v5, s74\n"
        "s_mov_b32 s84, s60\n"
        "s_waitcnt vmcnt(4)\n"
        "s_branch .Ls1_go1_%=\n"
        ".Ls1_i12_%=:\n"
        "v_mfma_f32_16x16x32_bf16 v[64:67], v[200:203], v[8:11], v[64:67]\n"
        "buffer_load_dword v4, v2, s[40:43], s86 offen sc1\n"
        "v_mfma_f32_16x16x32_bf16 v[68:71], v[204:207], v[8:11], v[68:71]\n"
        "s_waitcnt lgkmcnt(1)\n"
        "v_mfma_f32_16x16x32_bf16 v[48:51], v[208:211], v[12:15], v[48:51]\n"
        "v_mfma_f32_16x16x32_bf16 v[52:55], v[212:215], v[12:15], v[52:55]\n"
        "v_mfma_f32_16x16x32_bf16 v[56:59], v[12:15], v[216:219], v[56:59]\n"
        "v_mfma_f32_16x16x32_bf16 v[60:63], v[12:15], v[220:223], v[60:63]\n"
        "v_mfma_f32_16x16x32_bf16 v[64:67], v[224:227], v[12:15], v[64:67]\n"
        "v_mfma_f32_16x16x32_bf16 v[68:71], v[228:231], v[12:15], v[68:71]\n"
        "s_waitcnt lgkmcnt(0)\n"
        "v_mfma_f32_16x16x32_bf16 v[48:51], v[232:235], v[16:19], v[48:51]\n"
        "v_mfma_f32_16x16x32_bf16 v[52:55], v[236:239], v[16:19], v[52:55]\n"
        "v_mfma_f32_16x16x32_bf16 v[56:59], v[16:19], v[240:243], v[56:59]\n"
        "v_mfma_f32_16x16x32_bf16 v[60:63], v[16:19], v[244:247], v[60:63]\n"
        "v_mfma_f32_16x16x32_bf16 v[64:67], v[248:251], v[16:19], v[64:67]\n"
        "v_mfma_f32_16x16x32_bf16 v[68:71], v[252:255], v[16:19], v[68:71]\n"
        "v_cvt_pk_bf16_f32 v48, v48, v49\n"
        "v_cvt_pk_bf16_f32 v49, v50, v51\n"
        "v_cvt_pk_bf16_f32 v50, v52, v53\n"
        "v_cvt_pk_bf16_f32 v51, v54, v55\n"
        "s_add_u32 s66, s72, 524288\n"
        "buffer_store_dwordx4 v[48:51], v1, s[40:43], s66 offen offset:1024 sc1\n"
        "v_cvt_pk_bf16_f32 v74, v56, v57\n"
        "v_cvt_pk_bf16_f32 v75, v58, v59\n"
        "s_add_u32 s66, s72, 1048576\n"
        "buffer_store_dwordx4 v[72:75], v1, s[40:43], s66 offen offset:0 sc1\n"
        "v_cvt_pk_bf16_f32 v78, v60, v61\n"
        "v_cvt_pk_bf16_f32 v79, v62, v63\n"
        "buffer_store_dwordx4 v[76:79], v1, s[40:43], s66 offen offset:1024 sc1\n"
        "v_cvt_pk_bf16_f32 v64, v64, v65\n"
        "v_cvt_pk_bf16_f32 v65, v66, v67\n"
        "v_cvt_pk_bf16_f32 v66, v68, v69\n"
        "v_cvt_pk_bf16_f32 v67, v70, v71\n"
        "buffer_store_dwordx4 v[64:67], v1, s[40:43], s72 offen offset:1024 sc1\n"
        "s_waitcnt vmcnt(4)\n"
        "v_cmp_gt_i32 vcc, s70, v4\n"
        "s_nop 0\n"
        "s_and_b64 vcc, vcc, 1\n"
        "s_cbranch_vccnz .Ls1_late1_%=\n"
        "buffer_load_dwordx4 v1, s[40:43], s71 offen offset:0 sc1 lds\n"
        "buffer_load_dwordx4 v1, s[40:43], s71 offen offset:1024 sc1 lds\n"
        "buffer_load_dwordx4 v1, s[40:43], s71 offen offset:2048 sc1 lds\n"
        "buffer_load_dwordx4 v1, s[40:43], s71 offen offset:3072 sc1 lds\n"
        "s_add_u32 m0, s78, s85\n"
        "s_add_u32 s69, s69, 1\n"
        "buffer_load_dwordx4 v1, s[40:43], s81 offen offset:0 sc1 lds\n"
        "s_waitcnt vmcnt(5)\n"
        "v_mov_b32 v5, s74\n"
        "buffer_store_dword v5, v3, s[40:43], s73 offen sc1\n"
        "s_waitcnt vmcnt(1)\n"
        "s_branch .Ls1_go1_%=\n"
        ".Ls1_late1_%=:\n"
        "s_waitcnt vmcnt(0)\n"
        "v_mov_b32 v5, s74\n"
        "buffer_store_dword v5, v3, s[40:43], s73 offen sc1\n"
        "s_cmp_eq_u32 s69, 128\n"
        "s_cbranch_scc0 .Ls1_wait1_%=\n"
        "s_endpgm\n"
        ".Ls1_wait1_%=:\n"
        "s_mov_b32 s64, 0\n"
        ".Ls1_in1_top_%=:\n"
        "buffer_load_dword v4, v2, s[40:43], s86 offen sc1\n"
        "s_waitcnt vmcnt(0)\n"
        "v_cmp_gt_i32 vcc, s70, v4\n"
        "s_nop 0\n"
        "s_and_b64 vcc, vcc, 1\n"
        "s_cbranch_vccz .Ls1_in1_ok_%=\n"
        "s_add_u32 s64, s64, 1\n"
        "s_and_b32 s65, s64, 255\n"
        "s_cbranch_scc1 .Ls1_in1_go_%=\n"
        "s_mov_b32 s65, 49152\n"
        "s_cmp_lt_u32 s64, 1048576\n"
        "s_cbranch_scc1 .Ls1_in1_chk_%=\n"
        "v_mov_b32 v4, 1\n"
        "buffer_store_dword v4, off, s[40:43], s65 sc1\n"
        "s_waitcnt vmcnt(0)\n"
        ".Ls1_in1_chk_%=:\n"
        "buffer_load_dword v4, off, s[40:43], s65 sc1\n"
        "s_waitcnt vmcnt(0)\n"
        "v_readfirstlane_b32 s65, v4\n"
        "s_nop 0\n"
        "s_cmp_lg_u32 s65, 1\n"
        "s_cbranch_scc1 .Ls1_in1_go_%=\n"
        "s_endpgm\n"
        ".Ls1_in1_go_%=:\n"
        "s_branch .Ls1_in1_top_%=\n"
        ".Ls1_in1_ok_%=:\n"
        "buffer_load_dwordx4 v1, s[40:43], s71 offen offset:0 sc1 lds\n"
        "buffer_load_dwordx4 v1, s[40:43], s71 offen offset:1024 sc1 lds\n"
        "buffer_load_dwordx4 v1, s[40:43], s71 offen offset:2048 sc1 lds\n"
        "buffer_load_dwordx4 v1, s[40:43], s71 offen offset:3072 sc1 lds\n"
        "s_add_u32 m0, s78, s85\n"
        "s_add_u32 s69, s69, 1\n"
        "buffer_load_dwordx4 v1, s[40:43], s81 offen offset:0 sc1 lds\n"
        "s_waitcnt vmcnt(0)\n"
        ".Ls1_go1_%=:\n"
        "s_mov_b32 s72, 241172480\n"
        "s_mov_b32 s83, 241172480\n"
        "s_mov_b32 s73, 241172480\n"
        "v_add_u32 v0, s78, v6\n"
        "s_barrier\n"
        "ds_read_b128 v[8:11], v0 offset:0\n"
        "ds_read_b128 v[12:15], v0 offset:512\n"
        "ds_read_b128 v[16:19], v0 offset:1024\n"
        "s_waitcnt lgkmcnt(2)\n"
        "s_branch .Ls1_i0_%=\n"
        ".Ls1_end_%=:\n"
        :: "s"(p.ws), "s"(p.Wk), "s"(p.Wv), "s"(p.Wq), "s"(pair(w, delay)), "s"(pair(x4, pr)), TILE_ARGS(PAGE + spread(A_XN)),
           "v"(pair(lane == 0 ? flag(F_XN + 8 * h + w) : OOB, lane < 2 ? lane * 32768 : OOB)), "v"(pair(0, 0)), "v"(pair(0, 0)),
           "v"(pair(vo, 0)), "v"(pair(tr + (g & 1) * 32, tr + (1 - (g & 1)) * 32))
        : "v0", "v1", "v2", "v3", "v4", "v5", "v6", "v7", "v8", "v9", "v10", "v11", "v12", "v13", "v14", "v15", "v16", "v17", "v18", "v19", "v20", "v21", "v22", "v23", "v24", "v25", "v26", "v27", "v28", "v29", "v30", "v31", "v32", "v33", "v34", "v35", "v36", "v37", "v38", "v39", "v40", "v41", "v42", "v43", "v44", "v45", "v46", "v47", "v48", "v49", "v50", "v51", "v52", "v53", "v54", "v55", "v56", "v57", "v58", "v59", "v60", "v61", "v62", "v63", "v64", "v65", "v66", "v67", "v68", "v69", "v70", "v71", "v72", "v73", "v74", "v75", "v76", "v77", "v78", "v79", "v255", "a255", "s30", "s31", "s32", "s33", "s34", "s35", "s36", "s37", "s38", "s39", "s40", "s41", "s42", "s43", "s44", "s45", "s46", "s47", "s48", "s49", "s50", "s51", "s52", "s53", "s54", "s55", "s56", "s57", "s58", "s59", "s60", "s61", "s62", "s63", "s64", "s65", "s66", "s67", "s68", "s69", "s70", "s71", "s72", "s73", "s74", "s75", "s76", "s77", "s78", "s79", "s80", "s81", "s82", "s83", "s84", "s85", "s86", "s87", "s88", "s89", "s90", "s91", "s92", "s93", "s94", "s95", "s96", "s97", "s98", "s99", "vcc", "scc", "memory");
    __builtin_unreachable();
}
__device__ __forceinline__ void role_f1(const Params& p, int kap, int jj, int x4, int pr, int delay) {
    GEMM_HEAD;
    const unsigned u = jj * 4 + w;
    unsigned vo[3];
#pragma unroll
    for (int n = 0; n < 3; ++n) {
        const unsigned ps = (64 * n + lane) / 6;
        vo[n] = (8 * ((ps >> 2) & 3) + 4 * (ps >> 4) + (ps & 3)) * 9216 + (1152 * kap + 48 * u) * 2 + ((64 * n + lane) % 6) * 16 - 1024 * n;
    }
    const unsigned hb = 65 * PAGE + spread(B_H + kap * 73728);
    const unsigned out1 = hb + spread(u * 1024), out2 = hb + (6 + (u >> 3)) * 16384 + (u & 7) * 512, fout = flag(F_F1 + 24 * kap + u);
    asm volatile(
        "s_nop 4\n"
        "s_mov_b64 s[40:41], %0\n"
        "s_and_b32 s41, s41, 0xffff\n"
        "s_mov_b32 s42, 236978176\n"
        "s_mov_b32 s43, 0x20000\n"
        "s_mov_b64 s[48:49], %4\n"
        "s_mov_b64 s[56:57], %5\n"
        "s_mov_b64 s[58:59], %6\n"
        "s_mov_b64 s[60:61], %7\n"
        "s_mov_b64 s[62:63], %8\n"
        "v_mov_b64 v[0:1], %9\n"
        "v_mov_b64 v[2:3], %10\n"
        "v_mov_b64 v[4:5], %11\n"
        "v_mov_b64 v[6:7], %12\n"
        "v_mov_b64 v[8:9], %13\n"
        "v_mov_b64 v[10:11], %14\n"
        "s_mov_b32 s47, 0x20000\n"
        "s_mul_i32 s79, s48, 40960\n"
        ".Lf1_start_%=:\n"
        "s_sub_u32 s49, s49, 1\n"
        "s_cbranch_scc1 .Lf1_start2_%=\n"
        "s_sleep 16\n"
        "s_branch .Lf1_start_%=\n"
        ".Lf1_start2_%=:\n"
        "s_mov_b64 s[44:45], %1\n"
        "s_and_b32 s45, s45, 0xffff\n"
        "s_mov_b32 s46, 5308416\n"
        "s_add_u32 m0, s79, 0\n"
        "s_mov_b32 s65, 0\n"
        "buffer_load_dwordx4 v8, s[44:47], s65 offen lds\n"
        "buffer_load_dwordx4 v9, s[44:47], s65 offen offset:1024 lds\n"
        "buffer_load_dwordx4 v10, s[44:47], s65 offen offset:2048 lds\n"
        "s_add_u32 m0, s79, 3072\n"
        "s_mov_b32 s65, 294912\n"
        "buffer_load_dwordx4 v8, s[44:47], s65 offen lds\n"
        "buffer_load_dwordx4 v9, s[44:47], s65 offen offset:1024 lds\n"
        "buffer_load_dwordx4 v10, s[44:47], s65 offen offset:2048 lds\n"
        "s_waitcnt vmcnt(3)\n"
        "ds_read_b64_tr_b16 a[0:1], v11 offset:0\n"
        "ds_read_b64_tr_b16 a[2:3], v11 offset:1536\n"
        "ds_read_b64_tr_b16 a[8:9], v11 offset:32\n"
        "ds_read_b64_tr_b16 a[10:11], v11 offset:1568\n"
        "ds_read_b64_tr_b16 a[16:17], v11 offset:64\n"
        "ds_read_b64_tr_b16 a[18:19], v11 offset:1600\n"
        "s_waitcnt vmcnt(0)\n"
        "ds_read_b64_tr_b16 a[24:25], v11 offset:3072\n"
        "ds_read_b64_tr_b16 a[26:27], v11 offset:4608\n"
        "ds_read_b64_tr_b16 a[32:33], v11 offset:3104\n"
        "ds_read_b64_tr_b16 a[34:35], v11 offset:4640\n"
        "ds_read_b64_tr_b16 a[40:41], v11 offset:3136\n"
        "ds_read_b64_tr_b16 a[42:43], v11 offset:4672\n"
        "s_waitcnt lgkmcnt(6)\n"
        "s_add_u32 m0, s79, 0\n"
        "s_mov_b32 s65, 589824\n"
        "buffer_load_dwordx4 v8, s[44:47], s65 offen lds\n"
        "buffer_load_dwordx4 v9, s[44:47], s65 offen offset:1024 lds\n"
        "buffer_load_dwordx4 v10, s[44:47], s65 offen offset:2048 lds\n"
        "s_waitcnt vmcnt(0)\n"
        "ds_read_b64_tr_b16 a[48:49], v11 offset:0\n"
        "ds_read_b64_tr_b16 a[50:51], v11 offset:1536\n"
        "ds_read_b64_tr_b16 a[56:57], v11 offset:32\n"
        "ds_read_b64_tr_b16 a[58:59], v11 offset:1568\n"
        "ds_read_b64_tr_b16 a[64:65], v11 offset:64\n"
        "ds_read_b64_tr_b16 a[66:67], v11 offset:1600\n"
        "s_waitcnt lgkmcnt(6)\n"
        "s_add_u32 m0, s79, 3072\n"
        "s_mov_b32 s65, 884736\n"
        "buffer_load_dwordx4 v8, s[44:47], s65 offen lds\n"
        "buffer_load_dwordx4 v9, s[44:47], s65 offen offset:1024 lds\n"
        "buffer_load_dwordx4 v10, s[44:47], s65 offen offset:2048 lds\n"
        "s_waitcnt vmcnt(0)\n"
        "ds_read_b64_tr_b16 a[72:73], v11 offset:3072\n"
        "ds_read_b64_tr_b16 a[74:75], v11 offset:4608\n"
        "ds_read_b64_tr_b16 a[80:81], v11 offset:3104\n"
        "ds_read_b64_tr_b16 a[82:83], v11 offset:4640\n"
        "ds_read_b64_tr_b16 a[88:89], v11 offset:3136\n"
        "ds_read_b64_tr_b16 a[90:91], v11 offset:4672\n"
        "s_waitcnt lgkmcnt(6)\n"
        "s_add_u32 m0, s79, 0\n"
        "s_mov_b32 s65, 1179648\n"
        "buffer_load_dwordx4 v8, s[44:47], s65 offen lds\n"
        "buffer_load_dwordx4 v9, s[44:47], s65 offen offset:1024 lds\n"
        "buffer_load_dwordx4 v10, s[44:47], s65 offen offset:2048 lds\n"
        "s_waitcnt vmcnt(0)\n"
        "ds_read_b64_tr_b16 a[96:97], v11 offset:0\n"
        "ds_read_b64_tr_b16 a[98:99], v11 offset:1536\n"
        "ds_read_b64_tr_b16 a[104:105], v11 offset:32\n"
        "ds_read_b64_tr_b16 a[106:107], v11 offset:1568\n"
        "ds_read_b64_tr_b16 a[112:113], v11 offset:64\n"
        "ds_read_b64_tr_b16 a[114:115], v11 offset:1600\n"
        "s_waitcnt lgkmcnt(6)\n"
        "s_add_u32 m0, s79, 3072\n"
        "s_mov_b32 s65, 1474560\n"
        "buffer_load_dwordx4 v8, s[44:47], s65 offen lds\n"
        "buffer_load_dwordx4 v9, s[44:47], s65 offen offset:1024 lds\n"
        "buffer_load_dwordx4 v10, s[44:47], s65 offen offset:2048 lds\n"
        "s_waitcnt vmcnt(0)\n"
        "ds_read_b64_tr_b16 a[120:121], v11 offset:3072\n"
        "ds_read_b64_tr_b16 a[122:123], v11 offset:4608\n"
        "ds_read_b64_tr_b16 a[128:129], v11 offset:3104\n"
        "ds_read_b64_tr_b16 a[130:131], v11 offset:4640\n"
        "ds_read_b64_tr_b16 a[136:137], v11 offset:3136\n"
        "ds_read_b64_tr_b16 a[138:139], v11 offset:4672\n"
        "s_waitcnt lgkmcnt(6)\n"
        "s_add_u32 m0, s79, 0\n"
        "s_mov_b32 s65, 1769472\n"
        "buffer_load_dwordx4 v8, s[44:47], s65 offen lds\n"
        "buffer_load_dwordx4 v9, s[44:47], s65 offen offset:1024 lds\n"
        "buffer_load_dwordx4 v10, s[44:47], s65 offen offset:2048 lds\n"
        "s_waitcnt vmcnt(0)\n"
        "ds_read_b64_tr_b16 a[144:145], v11 offset:0\n"
        "ds_read_b64_tr_b16 a[146:147], v11 offset:1536\n"
        "ds_read_b64_tr_b16 a[152:153], v11 offset:32\n"
        "ds_read_b64_tr_b16 a[154:155], v11 offset:1568\n"
        "ds_read_b64_tr_b16 a[160:161], v11 offset:64\n"
        "ds_read_b64_tr_b16 a[162:163], v11 offset:1600\n"
        "s_waitcnt lgkmcnt(6)\n"
        "s_add_u32 m0, s79, 3072\n"
        "s_mov_b32 s65, 2064384\n"
        "buffer_load_dwordx4 v8, s[44:47], s65 offen lds\n"
        "buffer_load_dwordx4 v9, s[44:47], s65 offen offset:1024 lds\n"
        "buffer_load_dwordx4 v10, s[44:47], s65 offen offset:2048 lds\n"
        "s_waitcnt vmcnt(0)\n"
        "ds_read_b64_tr_b16 a[168:169], v11 offset:3072\n"
        "ds_read_b64_tr_b16 a[170:171], v11 offset:4608\n"
        "ds_read_b64_tr_b16 a[176:177], v11 offset:3104\n"
        "ds_read_b64_tr_b16 a[178:179], v11 offset:4640\n"
        "ds_read_b64_tr_b16 a[184:185], v11 offset:3136\n"
        "ds_read_b64_tr_b16 a[186:187], v11 offset:4672\n"
        "s_waitcnt lgkmcnt(6)\n"
        "s_add_u32 m0, s79, 0\n"
        "s_mov_b32 s65, 2359296\n"
        "buffer_load_dwordx4 v8, s[44:47], s65 offen lds\n"
        "buffer_load_dwordx4 v9, s[44:47], s65 offen offset:1024 lds\n"
        "buffer_load_dwordx4 v10, s[44:47], s65 offen offset:2048 lds\n"
        "s_waitcnt vmcnt(0)\n"
        "ds_read_b64_tr_b16 a[192:193], v11 offset:0\n"
        "ds_read_b64_tr_b16 a[194:195], v11 offset:1536\n"
        "ds_read_b64_tr_b16 a[200:201], v11 offset:32\n"
        "ds_read_b64_tr_b16 a[202:203], v11 offset:1568\n"
        "ds_read_b64_tr_b16 a[208:209], v11 offset:64\n"
        "ds_read_b64_tr_b16 a[210:211], v11 offset:1600\n"
        "s_waitcnt lgkmcnt(6)\n"
        "s_add_u32 m0, s79, 3072\n"
        "s_mov_b32 s65, 2654208\n"
        "buffer_load_dwordx4 v8, s[44:47], s65 offen lds\n"
        "buffer_load_dwordx4 v9, s[44:47], s65 offen offset:1024 lds\n"
        "buffer_load_dwordx4 v10, s[44:47], s65 offen offset:2048 lds\n"
        "s_waitcnt vmcnt(0)\n"
        "ds_read_b64_tr_b16 a[216:217], v11 offset:3072\n"
        "ds_read_b64_tr_b16 a[218:219], v11 offset:4608\n"
        "ds_read_b64_tr_b16 a[224:225], v11 offset:3104\n"
        "ds_read_b64_tr_b16 a[226:227], v11 offset:4640\n"
        "ds_read_b64_tr_b16 a[232:233], v11 offset:3136\n"
        "ds_read_b64_tr_b16 a[234:235], v11 offset:4672\n"
        "s_waitcnt lgkmcnt(6)\n"
        "s_add_u32 m0, s79, 0\n"
        "s_mov_b32 s65, 2949120\n"
        "buffer_load_dwordx4 v8, s[44:47], s65 offen lds\n"
        "buffer_load_dwordx4 v9, s[44:47], s65 offen offset:1024 lds\n"
        "buffer_load_dwordx4 v10, s[44:47], s65 offen offset:2048 lds\n"
        "s_waitcnt vmcnt(0)\n"
        "ds_read_b64_tr_b16 a[240:241], v11 offset:0\n"
        "ds_read_b64_tr_b16 a[242:243], v11 offset:1536\n"
        "ds_read_b64_tr_b16 a[248:249], v11 offset:32\n"
        "ds_read_b64_tr_b16 a[250:251], v11 offset:1568\n"
        "ds_read_b64_tr_b16 v[80:81], v11 offset:64\n"
        "ds_read_b64_tr_b16 v[82:83], v11 offset:1600\n"
        "s_waitcnt lgkmcnt(6)\n"
        "s_add_u32 m0, s79, 3072\n"
        "s_mov_b32 s65, 3244032\n"
        "buffer_load_dwordx4 v8, s[44:47], s65 offen lds\n"
        "buffer_load_dwordx4 v9, s[44:47], s65 offen offset:1024 lds\n"
        "buffer_load_dwordx4 v10, s[44:47], s65 offen offset:2048 lds\n"
        "s_waitcnt vmcnt(0)\n"
        "ds_read_b64_tr_b16 v[88:89], v11 offset:3072\n"
        "ds_read_b64_tr_b16 v[90:91], v11 offset:4608\n"
        "ds_read_b64_tr_b16 v[96:97], v11 offset:3104\n"
        "ds_read_b64_tr_b16 v[98:99], v11 offset:4640\n"
        "ds_read_b64_tr_b16 v[104:105], v11 offset:3136\n"
        "ds_read_b64_tr_b16 v[106:107], v11 offset:4672\n"
        "s_waitcnt lgkmcnt(6)\n"
        "s_add_u32 m0, s79, 0\n"
        "s_mov_b32 s65, 3538944\n"
        "buffer_load_dwordx4 v8, s[44:47], s65 offen lds\n"
        "buffer_load_dwordx4 v9, s[44:47], s65 offen offset:1024 lds\n"
        "buffer_load_dwordx4 v10, s[44:47], s65 offen offset:2048 lds\n"
        "s_waitcnt vmcnt(0)\n"
        "ds_read_b64_tr_b16 v[112:113], v11 offset:0\n"
        "ds_read_b64_tr_b16 v[114:115], v11 offset:1536\n"
        "ds_read_b64_tr_b16 v[120:121], v11 offset:32\n"
        "ds_read_b64_tr_b16 v[122:123], v11 offset:1568\n"
        "ds_read_b64_tr_b16 v[128:129], v11 offset:64\n"
        "ds_read_b64_tr_b16 v[130:131], v11 offset:1600\n"
        "s_waitcnt lgkmcnt(6)\n"
        "s_add_u32 m0, s79, 3072\n"
        "s_mov_b32 s65, 3833856\n"
        "buffer_load_dwordx4 v8, s[44:47], s65 offen lds\n"
        "buffer_load_dwordx4 v9, s[44:47], s65 offen offset:1024 lds\n"
        "buffer_load_dwordx4 v10, s[44:47], s65 offen offset:2048 lds\n"
        "s_waitcnt vmcnt(0)\n"
        "ds_read_b64_tr_b16 v[136:137], v11 offset:3072\n"
        "ds_read_b64_tr_b16 v[138:139], v11 offset:4608\n"
        "ds_read_b64_tr_b16 v[144:145], v11 offset:3104\n"
        "ds_read_b64_tr_b16 v[146:147], v11 offset:4640\n"
        "ds_read_b64_tr_b16 v[152:153], v11 offset:3136\n"
        "ds_read_b64_tr_b16 v[154:155], v11 offset:4672\n"
        "s_waitcnt lgkmcnt(6)\n"
        "s_add_u32 m0, s79, 0\n"
        "s_mov_b32 s65, 4128768\n"
        "buffer_load_dwordx4 v8, s[44:47], s65 offen lds\n"
        "buffer_load_dwordx4 v9, s[44:47], s65 offen offset:1024 lds\n"
        "buffer_load_dwordx4 v10, s[44:47], s65 offen offset:2048 lds\n"
        "s_waitcnt vmcnt(0)\n"
        "ds_read_b64_tr_b16 v[160:161], v11 offset:0\n"
        "ds_read_b64_tr_b16 v[162:163], v11 offset:1536\n"
        "ds_read_b64_tr_b16 v[168:169], v11 offset:32\n"
        "ds_read_b64_tr_b16 v[170:171], v11 offset:1568\n"
        "ds_read_b64_tr_b16 v[176:177], v11 offset:64\n"
        "ds_read_b64_tr_b16 v[178:179], v11 offset:1600\n"
        "s_waitcnt lgkmcnt(6)\n"
        "s_add_u32 m0, s79, 3072\n"
        "s_mov_b32 s65, 4423680\n"
        "buffer_load_dwordx4 v8, s[44:47], s65 offen lds\n"
        "buffer_load_dwordx4 v9, s[44:47], s65 offen offset:1024 lds\n"
        "buffer_load_dwordx4 v10, s[44:47], s65 offen offset:2048 lds\n"
        "s_waitcnt vmcnt(0)\n"
        "ds_read_b64_tr_b16 v[184:185], v11 offset:3072\n"
        "ds_read_b64_tr_b16 v[186:187], v11 offset:4608\n"
        "ds_read_b64_tr_b16 v[192:193], v11 offset:3104\n"
        "ds_read_b64_tr_b16 v[194:195], v11 offset:4640\n"
        "ds_read_b64_tr_b16 v[200:201], v11 offset:3136\n"
        "ds_read_b64_tr_b16 v[202:203], v11 offset:4672\n"
        "s_waitcnt lgkmcnt(6)\n"
        "s_add_u32 m0, s79, 0\n"
        "s_mov_b32 s65, 4718592\n"
        "buffer_load_dwordx4 v8, s[44:47], s65 offen lds\n"
        "buffer_load_dwordx4 v9, s[44:47], s65 offen offset:1024 lds\n"
        "buffer_load_dwordx4 v10, s[44:47], s65 offen offset:2048 lds\n"
        "s_waitcnt vmcnt(0)\n"
        "ds_read_b64_tr_b16 v[208:209], v11 offset:0\n"
        "ds_read_b64_tr_b16 v[210:211], v11 offset:1536\n"
        "ds_read_b64_tr_b16 v[216:217], v11 offset:32\n"
        "ds_read_b64_tr_b16 v[218:219], v11 offset:1568\n"
        "ds_read_b64_tr_b16 v[224:225], v11 offset:64\n"
        "ds_read_b64_tr_b16 v[226:227], v11 offset:1600\n"
        "s_waitcnt lgkmcnt(6)\n"
        "s_add_u32 m0, s79, 3072\n"
        "s_mov_b32 s65, 5013504\n"
        "buffer_load_dwordx4 v8, s[44:47], s65 offen lds\n"
        "buffer_load_dwordx4 v9, s[44:47], s65 offen offset:1024 lds\n"
        "buffer_load_dwordx4 v10, s[44:47], s65 offen offset:2048 lds\n"
        "s_waitcnt vmcnt(0)\n"
        "ds_read_b64_tr_b16 v[232:233], v11 offset:3072\n"
        "ds_read_b64_tr_b16 v[234:235], v11 offset:4608\n"
        "ds_read_b64_tr_b16 v[240:241], v11 offset:3104\n"
        "ds_read_b64_tr_b16 v[242:243], v11 offset:4640\n"
        "ds_read_b64_tr_b16 v[248:249], v11 offset:3136\n"
        "ds_read_b64_tr_b16 v[250:251], v11 offset:4672\n"
        "s_waitcnt lgkmcnt(6)\n"
        "s_mov_b64 s[44:45], %2\n"
        "s_and_b32 s45, s45, 0xffff\n"
        "s_mov_b32 s46, 5308416\n"
        "s_add_u32 m0, s79, 0\n"
        "s_mov_b32 s65, 0\n"
        "buffer_load_dwordx4 v8, s[44:47], s65 offen lds\n"
        "buffer_load_dwordx4 v9, s[44:47], s65 offen offset:1024 lds\n"
        "buffer_load_dwordx4 v10, s[44:47], s65 offen offset:2048 lds\n"
        "s_waitcnt vmcnt(0)\n"
        "ds_read_b64_tr_b16 a[4:5], v11 offset:0\n"
        "ds_read_b64_tr_b16 a[6:7], v11 offset:1536\n"
        "ds_read_b64_tr_b16 a[12:13], v11 offset:32\n"
        "ds_read_b64_tr_b16 a[14:15], v11 offset:1568\n"
        "ds_read_b64_tr_b16 a[20:21], v11 offset:64\n"
        "ds_read_b64_tr_b16 a[22:23], v11 offset:1600\n"
        "s_waitcnt lgkmcnt(6)\n"
        "s_add_u32 m0, s79, 3072\n"
        "s_mov_b32 s65, 294912\n"
        "buffer_load_dwordx4 v8, s[44:47], s65 offen lds\n"
        "buffer_load_dwordx4 v9, s[44:47], s65 offen offset:1024 lds\n"
        "buffer_load_dwordx4 v10, s[44:47], s65 offen offset:2048 lds\n"
        "s_waitcnt vmcnt(0)\n"
        "ds_read_b64_tr_b16 a[28:29], v11 offset:3072\n"
        "ds_read_b64_tr_b16 a[30:31], v11 offset:4608\n"
        "ds_read_b64_tr_b16 a[36:37], v11 offset:3104\n"
        "ds_read_b64_tr_b16 a[38:39], v11 offset:4640\n"
        "ds_read_b64_tr_b16 a[44:45], v11 offset:3136\n"
        "ds_read_b64_tr_b16 a[46:47], v11 offset:4672\n"
        "s_waitcnt lgkmcnt(6)\n"
        "s_add_u32 m0, s79, 0\n"
        "s_mov_b32 s65, 589824\n"
        "buffer_load_dwordx4 v8, s[44:47], s65 offen lds\n"
        "buffer_load_dwordx4 v9, s[44:47], s65 offen offset:1024 lds\n"
        "buffer_load_dwordx4 v10, s[44:47], s65 offen offset:2048 lds\n"
        "s_waitcnt vmcnt(0)\n"
        "ds_read_b64_tr_b16 a[52:53], v11 offset:0\n"
        "ds_read_b64_tr_b16 a[54:55], v11 offset:1536\n"
        "ds_read_b64_tr_b16 a[60:61], v11 offset:32\n"
        "ds_read_b64_tr_b16 a[62:63], v11 offset:1568\n"
        "ds_read_b64_tr_b16 a[68:69], v11 offset:64\n"
        "ds_read_b64_tr_b16 a[70:71], v11 offset:1600\n"
        "s_waitcnt lgkmcnt(6)\n"
        "s_add_u32 m0, s79, 3072\n"
        "s_mov_b32 s65, 884736\n"
        "buffer_load_dwordx4 v8, s[44:47], s65 offen lds\n"
        "buffer_load_dwordx4 v9, s[44:47], s65 offen offset:1024 lds\n"
        "buffer_load_dwordx4 v10, s[44:47], s65 offen offset:2048 lds\n"
        "s_waitcnt vmcnt(0)\n"
        "ds_read_b64_tr_b16 a[76:77], v11 offset:3072\n"
        "ds_read_b64_tr_b16 a[78:79], v11 offset:4608\n"
        "ds_read_b64_tr_b16 a[84:85], v11 offset:3104\n"
        "ds_read_b64_tr_b16 a[86:87], v11 offset:4640\n"
        "ds_read_b64_tr_b16 a[92:93], v11 offset:3136\n"
        "ds_read_b64_tr_b16 a[94:95], v11 offset:4672\n"
        "s_waitcnt lgkmcnt(6)\n"
        "s_add_u32 m0, s79, 0\n"
        "s_mov_b32 s65, 1179648\n"
        "buffer_load_dwordx4 v8, s[44:47], s65 offen lds\n"
        "buffer_load_dwordx4 v9, s[44:47], s65 offen offset:1024 lds\n"
        "buffer_load_dwordx4 v10, s[44:47], s65 offen offset:2048 lds\n"
        "s_waitcnt vmcnt(0)\n"
        "ds_read_b64_tr_b16 a[100:101], v11 offset:0\n"
        "ds_read_b64_tr_b16 a[102:103], v11 offset:1536\n"
        "ds_read_b64_tr_b16 a[108:109], v11 offset:32\n"
        "ds_read_b64_tr_b16 a[110:111], v11 offset:1568\n"
        "ds_read_b64_tr_b16 a[116:117], v11 offset:64\n"
        "ds_read_b64_tr_b16 a[118:119], v11 offset:1600\n"
        "s_waitcnt lgkmcnt(6)\n"
        "s_add_u32 m0, s79, 3072\n"
        "s_mov_b32 s65, 1474560\n"
        "buffer_load_dwordx4 v8, s[44:47], s65 offen lds\n"
        "buffer_load_dwordx4 v9, s[44:47], s65 offen offset:1024 lds\n"
        "buffer_load_dwordx4 v10, s[44:47], s65 offen offset:2048 lds\n"
        "s_waitcnt vmcnt(0)\n"
        "ds_read_b64_tr_b16 a[124:125], v11 offset:3072\n"
        "ds_read_b64_tr_b16 a[126:127], v11 offset:4608\n"
        "ds_read_b64_tr_b16 a[132:133], v11 offset:3104\n"
        "ds_read_b64_tr_b16 a[134:135], v11 offset:4640\n"
        "ds_read_b64_tr_b16 a[140:141], v11 offset:3136\n"
        "ds_read_b64_tr_b16 a[142:143], v11 offset:4672\n"
        "s_waitcnt lgkmcnt(6)\n"
        "s_add_u32 m0, s79, 0\n"
        "s_mov_b32 s65, 1769472\n"
        "buffer_load_dwordx4 v8, s[44:47], s65 offen lds\n"
        "buffer_load_dwordx4 v9, s[44:47], s65 offen offset:1024 lds\n"
        "buffer_load_dwordx4 v10, s[44:47], s65 offen offset:2048 lds\n"
        "s_waitcnt vmcnt(0)\n"
        "ds_read_b64_tr_b16 a[148:149], v11 offset:0\n"
        "ds_read_b64_tr_b16 a[150:151], v11 offset:1536\n"
        "ds_read_b64_tr_b16 a[156:157], v11 offset:32\n"
        "ds_read_b64_tr_b16 a[158:159], v11 offset:1568\n"
        "ds_read_b64_tr_b16 a[164:165], v11 offset:64\n"
        "ds_read_b64_tr_b16 a[166:167], v11 offset:1600\n"
        "s_waitcnt lgkmcnt(6)\n"
        "s_add_u32 m0, s79, 3072\n"
        "s_mov_b32 s65, 2064384\n"
        "buffer_load_dwordx4 v8, s[44:47], s65 offen lds\n"
        "buffer_load_dwordx4 v9, s[44:47], s65 offen offset:1024 lds\n"
        "buffer_load_dwordx4 v10, s[44:47], s65 offen offset:2048 lds\n"
        "s_waitcnt vmcnt(0)\n"
        "ds_read_b64_tr_b16 a[172:173], v11 offset:3072\n"
        "ds_read_b64_tr_b16 a[174:175], v11 offset:4608\n"
        "ds_read_b64_tr_b16 a[180:181], v11 offset:3104\n"
        "ds_read_b64_tr_b16 a[182:183], v11 offset:4640\n"
        "ds_read_b64_tr_b16 a[188:189], v11 offset:3136\n"
        "ds_read_b64_tr_b16 a[190:191], v11 offset:4672\n"
        "s_waitcnt lgkmcnt(6)\n"
        "s_add_u32 m0, s79, 0\n"
        "s_mov_b32 s65, 2359296\n"
        "buffer_load_dwordx4 v8, s[44:47], s65 offen lds\n"
        "buffer_load_dwordx4 v9, s[44:47], s65 offen offset:1024 lds\n"
        "buffer_load_dwordx4 v10, s[44:47], s65 offen offset:2048 lds\n"
        "s_waitcnt vmcnt(0)\n"
        "ds_read_b64_tr_b16 a[196:197], v11 offset:0\n"
        "ds_read_b64_tr_b16 a[198:199], v11 offset:1536\n"
        "ds_read_b64_tr_b16 a[204:205], v11 offset:32\n"
        "ds_read_b64_tr_b16 a[206:207], v11 offset:1568\n"
        "ds_read_b64_tr_b16 a[212:213], v11 offset:64\n"
        "ds_read_b64_tr_b16 a[214:215], v11 offset:1600\n"
        "s_waitcnt lgkmcnt(6)\n"
        "s_add_u32 m0, s79, 3072\n"
        "s_mov_b32 s65, 2654208\n"
        "buffer_load_dwordx4 v8, s[44:47], s65 offen lds\n"
        "buffer_load_dwordx4 v9, s[44:47], s65 offen offset:1024 lds\n"
        "buffer_load_dwordx4 v10, s[44:47], s65 offen offset:2048 lds\n"
        "s_waitcnt vmcnt(0)\n"
        "ds_read_b64_tr_b16 a[220:221], v11 offset:3072\n"
        "ds_read_b64_tr_b16 a[222:223], v11 offset:4608\n"
        "ds_read_b64_tr_b16 a[228:229], v11 offset:3104\n"
        "ds_read_b64_tr_b16 a[230:231], v11 offset:4640\n"
        "ds_read_b64_tr_b16 a[236:237], v11 offset:3136\n"
        "ds_read_b64_tr_b16 a[238:239], v11 offset:4672\n"
        "s_waitcnt lgkmcnt(6)\n"
        "s_add_u32 m0, s79, 0\n"
        "s_mov_b32 s65, 2949120\n"
        "buffer_load_dwordx4 v8, s[44:47], s65 offen lds\n"
        "buffer_load_dwordx4 v9, s[44:47], s65 offen offset:1024 lds\n"
        "buffer_load_dwordx4 v10, s[44:47], s65 offen offset:2048 lds\n"
        "s_waitcnt vmcnt(0)\n"
        "ds_read_b64_tr_b16 a[244:245], v11 offset:0\n"
        "ds_read_b64_tr_b16 a[246:247], v11 offset:1536\n"
        "ds_read_b64_tr_b16 a[252:253], v11 offset:32\n"
        "ds_read_b64_tr_b16 a[254:255], v11 offset:1568\n"
        "ds_read_b64_tr_b16 v[84:85], v11 offset:64\n"
        "ds_read_b64_tr_b16 v[86:87], v11 offset:1600\n"
        "s_waitcnt lgkmcnt(6)\n"
        "s_add_u32 m0, s79, 3072\n"
        "s_mov_b32 s65, 3244032\n"
        "buffer_load_dwordx4 v8, s[44:47], s65 offen lds\n"
        "buffer_load_dwordx4 v9, s[44:47], s65 offen offset:1024 lds\n"
        "buffer_load_dwordx4 v10, s[44:47], s65 offen offset:2048 lds\n"
        "s_waitcnt vmcnt(0)\n"
        "ds_read_b64_tr_b16 v[92:93], v11 offset:3072\n"
        "ds_read_b64_tr_b16 v[94:95], v11 offset:4608\n"
        "ds_read_b64_tr_b16 v[100:101], v11 offset:3104\n"
        "ds_read_b64_tr_b16 v[102:103], v11 offset:4640\n"
        "ds_read_b64_tr_b16 v[108:109], v11 offset:3136\n"
        "ds_read_b64_tr_b16 v[110:111], v11 offset:4672\n"
        "s_waitcnt lgkmcnt(6)\n"
        "s_add_u32 m0, s79, 0\n"
        "s_mov_b32 s65, 3538944\n"
        "buffer_load_dwordx4 v8, s[44:47], s65 offen lds\n"
        "buffer_load_dwordx4 v9, s[44:47], s65 offen offset:1024 lds\n"
        "buffer_load_dwordx4 v10, s[44:47], s65 offen offset:2048 lds\n"
        "s_waitcnt vmcnt(0)\n"
        "ds_read_b64_tr_b16 v[116:117], v11 offset:0\n"
        "ds_read_b64_tr_b16 v[118:119], v11 offset:1536\n"
        "ds_read_b64_tr_b16 v[124:125], v11 offset:32\n"
        "ds_read_b64_tr_b16 v[126:127], v11 offset:1568\n"
        "ds_read_b64_tr_b16 v[132:133], v11 offset:64\n"
        "ds_read_b64_tr_b16 v[134:135], v11 offset:1600\n"
        "s_waitcnt lgkmcnt(6)\n"
        "s_add_u32 m0, s79, 3072\n"
        "s_mov_b32 s65, 3833856\n"
        "buffer_load_dwordx4 v8, s[44:47], s65 offen lds\n"
        "buffer_load_dwordx4 v9, s[44:47], s65 offen offset:1024 lds\n"
        "buffer_load_dwordx4 v10, s[44:47], s65 offen offset:2048 lds\n"
        "s_waitcnt vmcnt(0)\n"
        "ds_read_b64_tr_b16 v[140:141], v11 offset:3072\n"
        "ds_read_b64_tr_b16 v[142:143], v11 offset:4608\n"
        "ds_read_b64_tr_b16 v[148:149], v11 offset:3104\n"
        "ds_read_b64_tr_b16 v[150:151], v11 offset:4640\n"
        "ds_read_b64_tr_b16 v[156:157], v11 offset:3136\n"
        "ds_read_b64_tr_b16 v[158:159], v11 offset:4672\n"
        "s_waitcnt lgkmcnt(6)\n"
        "s_add_u32 m0, s79, 0\n"
        "s_mov_b32 s65, 4128768\n"
        "buffer_load_dwordx4 v8, s[44:47], s65 offen lds\n"
        "buffer_load_dwordx4 v9, s[44:47], s65 offen offset:1024 lds\n"
        "buffer_load_dwordx4 v10, s[44:47], s65 offen offset:2048 lds\n"
        "s_waitcnt vmcnt(0)\n"
        "ds_read_b64_tr_b16 v[164:165], v11 offset:0\n"
        "ds_read_b64_tr_b16 v[166:167], v11 offset:1536\n"
        "ds_read_b64_tr_b16 v[172:173], v11 offset:32\n"
        "ds_read_b64_tr_b16 v[174:175], v11 offset:1568\n"
        "ds_read_b64_tr_b16 v[180:181], v11 offset:64\n"
        "ds_read_b64_tr_b16 v[182:183], v11 offset:1600\n"
        "s_waitcnt lgkmcnt(6)\n"
        "s_add_u32 m0, s79, 3072\n"
        "s_mov_b32 s65, 4423680\n"
        "buffer_load_dwordx4 v8, s[44:47], s65 offen lds\n"
        "buffer_load_dwordx4 v9, s[44:47], s65 offen offset:1024 lds\n"
        "buffer_load_dwordx4 v10, s[44:47], s65 offen offset:2048 lds\n"
        "s_waitcnt vmcnt(0)\n"
        "ds_read_b64_tr_b16 v[188:189], v11 offset:3072\n"
        "ds_read_b64_tr_b16 v[190:191], v11 offset:4608\n"
        "ds_read_b64_tr_b16 v[196:197], v11 offset:3104\n"
        "ds_read_b64_tr_b16 v[198:199], v11 offset:4640\n"
        "ds_read_b64_tr_b16 v[204:205], v11 offset:3136\n"
        "ds_read_b64_tr_b16 v[206:207], v11 offset:4672\n"
        "s_waitcnt lgkmcnt(6)\n"
        "s_add_u32 m0, s79, 0\n"
        "s_mov_b32 s65, 4718592\n"
        "buffer_load_dwordx4 v8, s[44:47], s65 offen lds\n"
        "buffer_load_dwordx4 v9, s[44:47], s65 offen offset:1024 lds\n"
        "buffer_load_dwordx4 v10, s[44:47], s65 offen offset:2048 lds\n"
        "s_waitcnt vmcnt(0)\n"
        "ds_read_b64_tr_b16 v[212:213], v11 offset:0\n"
        "ds_read_b64_tr_b16 v[214:215], v11 offset:1536\n"
        "ds_read_b64_tr_b16 v[220:221], v11 offset:32\n"
        "ds_read_b64_tr_b16 v[222:223], v11 offset:1568\n"
        "ds_read_b64_tr_b16 v[228:229], v11 offset:64\n"
        "ds_read_b64_tr_b16 v[230:231], v11 offset:1600\n"
        "s_waitcnt lgkmcnt(6)\n"
        "s_add_u32 m0, s79, 3072\n"
        "s_mov_b32 s65, 5013504\n"
        "buffer_load_dwordx4 v8, s[44:47], s65 offen lds\n"
        "buffer_load_dwordx4 v9, s[44:47], s65 offen offset:1024 lds\n"
        "buffer_load_dwordx4 v10, s[44:47], s65 offen offset:2048 lds\n"
        "s_waitcnt vmcnt(0)\n"
        "ds_read_b64_tr_b16 v[236:237], v11 offset:3072\n"
        "ds_read_b64_tr_b16 v[238:239], v11 offset:4608\n"
        "ds_read_b64_tr_b16 v[244:245], v11 offset:3104\n"
        "ds_read_b64_tr_b16 v[246:247], v11 offset:4640\n"
        "ds_read_b64_tr_b16 v[252:253], v11 offset:3136\n"
        "ds_read_b64_tr_b16 v[254:255], v11 offset:4672\n"
        "s_waitcnt lgkmcnt(0)\n"
        "s_getpc_b64 s[44:45]\n"
        ".Lf1_pc_%=:\n"
        "s_add_u32 s44, s44, .Lf1_code_%=-.Lf1_pc_%=\n"
        "s_addc_u32 s45, s45, 0\n"
        "s_and_b32 s45, s45, 0xffff\n"
        "s_and_b32 s44, s44, -4\n"
        "s_mov_b32 s46, .Lf1_end_%=-.Lf1_code_%=\n"
        "s_mov_b32 s47, 0x20000\n"
        "s_lshl_b32 s65, s48, 10\n"
        "buffer_load_dwordx4 v[24:27], v1, s[44:47], s65 offen\n"
        "s_add_u32 s65, s65, 4096\n"
        "buffer_load_dwordx4 v[28:31], v1, s[44:47], s65 offen\n"
        "s_add_u32 s65, s65, 4096\n"
        "buffer_load_dwordx4 v[32:35], v1, s[44:47], s65 offen\n"
        "s_add_u32 s65, s65, 4096\n"
        "buffer_load_dwordx4 v[36:39], v1, s[44:47], s65 offen\n"
        "s_add_u32 s65, s65, 4096\n"
        "buffer_load_dwordx4 v[40:43], v1, s[44:47], s65 offen\n"
        "s_add_u32 s65, s65, 4096\n"
        "v_lshrrev_b32 v20, 4, v1\n"
        "s_mov_b32 s50, s56\n"
        "v_lshlrev_b32 v22, 7, v20\n"
        "s_sub_u32 s66, 0, s50\n"
        "s_mov_b32 s52, 32768\n"
        "s_mov_b32 s64, 0\n"
        ".Lf1_pg0_top_%=:\n"
        "buffer_load_dword v21, v22, s[40:43], s52 offen sc1\n"
        "s_waitcnt vmcnt(0)\n"
        "v_cmp_gt_i32 vcc, 0, v21\n"
        "s_cbranch_vccz .Lf1_pg0_ok_%=\n"
        "s_add_u32 s64, s64, 1\n"
        "s_and_b32 s65, s64, 255\n"
        "s_cbranch_scc1 .Lf1_pg0_go_%=\n"
        "s_mov_b32 s65, 49152\n"
        "s_cmp_lt_u32 s64, 1048576\n"
        "s_cbranch_scc1 .Lf1_pg0_chk_%=\n"
        "v_mov_b32 v21, 1\n"
        "buffer_store_dword v21, off, s[40:43], s65 sc1\n"
        "s_waitcnt vmcnt(0)\n"
        ".Lf1_pg0_chk_%=:\n"
        "buffer_load_dword v21, off, s[40:43], s65 sc1\n"
        "s_waitcnt vmcnt(0)\n"
        "v_readfirstlane_b32 s65, v21\n"
        "s_nop 0\n"
        "s_cmp_lg_u32 s65, 1\n"
        "s_cbranch_scc1 .Lf1_pg0_go_%=\n"
        "s_endpgm\n"
        ".Lf1_pg0_go_%=:\n"
        "s_sleep 2\n"
        "s_branch .Lf1_pg0_top_%=\n"
        ".Lf1_pg0_ok_%=:\n"
        "v_cmp_eq_u32 s[52:53], 1, v21\n"
        "s_nop 1\n"
        "s_mov_b32 s54, 40960\n"
        "s_mov_b32 s64, 0\n"
        ".Lf1_pg1_top_%=:\n"
        "buffer_load_dword v21, v22, s[40:43], s54 offen sc1\n"
        "s_waitcnt vmcnt(0)\n"
        "v_cmp_gt_i32 vcc, 0, v21\n"
        "s_cbranch_vccz .Lf1_pg1_ok_%=\n"
        "s_add_u32 s64, s64, 1\n"
        "s_and_b32 s65, s64, 255\n"
        "s_cbranch_scc1 .Lf1_pg1_go_%=\n"
        "s_mov_b32 s65, 49152\n"
        "s_cmp_lt_u32 s64, 1048576\n"
        "s_cbranch_scc1 .Lf1_pg1_chk_%=\n"
        "v_mov_b32 v21, 1\n"
        "buffer_store_dword v21, off, s[40:43], s65 sc1\n"
        "s_waitcnt vmcnt(0)\n"
        ".Lf1_pg1_chk_%=:\n"
        "buffer_load_dword v21, off, s[40:43], s65 sc1\n"
        "s_waitcnt vmcnt(0)\n"
        "v_readfirstlane_b32 s65, v21\n"
        "s_nop 0\n"
        "s_cmp_lg_u32 s65, 1\n"
        "s_cbranch_scc1 .Lf1_pg1_go_%=\n"
        "s_endpgm\n"
        ".Lf1_pg1_go_%=:\n"
        "s_sleep 2\n"
        "s_branch .Lf1_pg1_top_%=\n"
        ".Lf1_pg1_ok_%=:\n"
        "v_cmp_eq_u32 s[54:55], 1, v21\n"
        "s_nop 1\n"
        "s_mov_b32 vcc_lo, 0x55555555\n"
        "s_mov_b32 vcc_hi, 0x55555555\n"
        "s_xor_b64 vcc, vcc, s[52:53]\n"
        "s_bcnt1_i32_b64 s65, vcc\n"
        "s_sub_u32 s64, s65, 23\n"
        "s_cmp_lt_u32 s64, 19\n"
        "s_cbranch_scc1 .Lf1_rawmap_%=\n"
        "s_cmp_lt_u32 s65, 32\n"
        "s_cselect_b32 s65, 0, -1\n"
        "s_xor_b32 s52, s65, 0x55555555\n"
        "s_xor_b32 s53, s65, 0x55555555\n"
        "s_xor_b32 s54, s65, 0x55555555\n"
        "s_xor_b32 s55, s65, 0xaaaaaaaa\n"
        ".Lf1_rawmap_%=:\n"
        "s_xor_b32 s52, s52, s66\n"
        "s_xor_b32 s53, s53, s66\n"
        "s_xor_b32 s54, s54, s66\n"
        "s_xor_b32 s55, s55, s66\n"
        "s_and_b32 s76, s55, 1\n"
        "s_lshl_b32 s76, s76, 13\n"
        "s_add_u32 s60, s60, s76\n"
        "s_barrier\n"
        "s_mov_b32 s68, -1\n"
        "s_mov_b32 s69, 0\n"
        "s_mov_b32 s78, 0\n"
        "s_mov_b32 s74, -1\n"
        "s_mov_b32 s86, s76\n"
        "s_lshr_b32 s85, s61, 16\n"
        "s_and_b32 s61, s61, 0xffff\n"
        "s_mov_b32 s89, 241172480\n"
        "s_mov_b32 s84, 241172480\n"
        "v_mov_b32 v6, v0\n"
        "s_mov_b32 s80, 0xbfb8aa3b\n"
        "s_lshr_b32 s67, s69, 1\n"
        "s_lshr_b32 s65, s67, 1\n"
        "s_bitcmp1_b32 s54, s65\n"
        "s_cselect_b32 s65, 8192, 0\n"
        "s_lshl_b32 s77, s67, 20\n"
        "s_add_u32 s77, s77, s65\n"
        "s_add_u32 s71, s77, s58\n"
        "s_add_u32 s81, s77, s62\n"
        "s_add_u32 s78, s78, 18432\n"
        "s_cmp_eq_u32 s78, 55296\n"
        "s_cselect_b32 s78, 0, s78\n"
        "s_add_u32 m0, s78, s61\n"
        "s_lshr_b32 s70, s69, 1\n"
        "s_branch .Lf1_wait1_%=\n"
        ".Lf1_code_%=:\n"
        ".Lf1_i0_%=:\n"
        "v_mfma_f32_16x16x32_bf16 v[24:27], a[0:3], v[8:11], 0\n"
        "s_add_u32 s86, s76, 512\n"
        "buffer_load_dword v4, v2, s[40:43], s86 offen sc1\n"
        "v_mfma_f32_16x16x32_bf16 v[28:31], a[4:7], v[8:11], 0\n"
        "s_add_u32 s71, s77, s58\n"
        "v_mul_f32 v72, s80, v48\n"
        "v_mul_f32 v73, s80, v49\n"
        "v_mfma_f32_16x16x32_bf16 v[32:35], a[8:11], v[8:11], 0\n"
        "s_add_u32 s71, s71, 32768\n"
        "v_mul_f32 v74, s80, v50\n"
        "v_mul_f32 v75, s80, v51\n"
        "v_mfma_f32_16x16x32_bf16 v[36:39], a[12:15], v[8:11], 0\n"
        "s_add_u32 s81, s77, s62\n"
        "v_exp_f32 v72, v72\n"
        "v_mfma_f32_16x16x32_bf16 v[40:43], a[16:19], v[8:11], 0\n"
        "s_add_u32 s81, s81, 2048\n"
        "v_exp_f32 v73, v73\n"
        "v_mfma_f32_16x16x32_bf16 v[44:47], a[20:23], v[8:11], 0\n"
        "s_waitcnt lgkmcnt(1)\n"
        "ds_read_b128 v[8:11], v0 offset:1536\n"
        "v_mul_f32 v76, s80, v56\n"
        "v_mfma_f32_16x16x32_bf16 v[24:27], a[24:27], v[12:15], v[24:27]\n"
        "s_add_u32 s78, s78, 18432\n"
        "v_exp_f32 v74, v74\n"
        "v_mfma_f32_16x16x32_bf16 v[28:31], a[28:31], v[12:15], v[28:31]\n"
        "s_cmp_eq_u32 s78, 55296\n"
        "s_cselect_b32 s78, 0, s78\n"
        "v_mul_f32 v77, s80, v57\n"
        "v_mfma_f32_16x16x32_bf16 v[32:35], a[32:35], v[12:15], v[32:35]\n"
        "s_add_u32 m0, s78, s61\n"
        "v_exp_f32 v75, v75\n"
        "v_mfma_f32_16x16x32_bf16 v[36:39], a[36:39], v[12:15], v[36:39]\n"
        "s_lshr_b32 s70, s69, 1\n"
        "v_mul_f32 v48, v48, v52\n"
        "v_mul_f32 v49, v49, v53\n"
        "v_mfma_f32_16x16x32_bf16 v[40:43], a[40:43], v[12:15], v[40:43]\n"
        "v_mul_f32 v50, v50, v54\n"
        "v_mul_f32 v51, v51, v55\n"
        "v_mfma_f32_16x16x32_bf16 v[44:47], a[44:47], v[12:15], v[44:47]\n"
        "s_waitcnt lgkmcnt(1)\n"
        "ds_read_b128 v[12:15], v0 offset:2048\n"
        "v_add_f32 v72, 1.0, v72\n"
        "v_mfma_f32_16x16x32_bf16 v[24:27], a[48:51], v[16:19], v[24:27]\n"
        "v_add_f32 v73, 1.0, v73\n"
        "v_add_f32 v74, 1.0, v74\n"
        "v_mfma_f32_16x16x32_bf16 v[28:31], a[52:55], v[16:19], v[28:31]\n"
        "v_add_f32 v75, 1.0, v75\n"
        "v_mul_f32 v78, s80, v58\n"
        "v_mfma_f32_16x16x32_bf16 v[32:35], a[56:59], v[16:19], v[32:35]\n"
        "s_waitcnt vmcnt(3)\n"
        "v_rcp_f32 v72, v72\n"
        "v_mfma_f32_16x16x32_bf16 v[36:39], a[60:63], v[16:19], v[36:39]\n"
        "buffer_store_dword v5, v3, s[40:43], s84 offen sc1\n"
        "v_mul_f32 v79, s80, v59\n"
        "v_mfma_f32_16x16x32_bf16 v[40:43], a[64:67], v[16:19], v[40:43]\n"
        "v_rcp_f32 v73, v73\n"
        "v_mfma_f32_16x16x32_bf16 v[44:47], a[68:71], v[16:19], v[44:47]\n"
        "s_waitcnt lgkmcnt(1)\n"
        "ds_read_b128 v[16:19], v0 offset:2560\n"
        "v_mfma_f32_16x16x32_bf16 v[24:27], a[72:75], v[8:11], v[24:27]\n"
        "v_rcp_f32 v74, v74\n"
        "v_mfma_f32_16x16x32_bf16 v[28:31], a[76:79], v[8:11], v[28:31]\n"
        "v_rcp_f32 v75, v75\n"
        "v_mfma_f32_16x16x32_bf16 v[32:35], a[80:83], v[8:11], v[32:35]\n"
        "v_mul_f32 v48, v48, v72\n"
        "v_mul_f32 v49, v49, v73\n"
        "v_mfma_f32_16x16x32_bf16 v[36:39], a[84:87], v[8:11], v[36:39]\n"
        "v_mul_f32 v50, v50, v74\n"
        "v_mul_f32 v51, v51, v75\n"
        "v_mfma_f32_16x16x32_bf16 v[40:43], a[88:91], v[8:11], v[40:43]\n"
        "v_cvt_pk_bf16_f32 v52, v48, v49\n"
        "v_cvt_pk_bf16_f32 v53, v50, v51\n"
        "v_mfma_f32_16x16x32_bf16 v[44:47], a[92:95], v[8:11], v[44:47]\n"
        "s_waitcnt lgkmcnt(1)\n"
        "ds_read_b128 v[8:11], v0 offset:3072\n"
        "v_mul_f32 v72, s80, v64\n"
        "v_mfma_f32_16x16x32_bf16 v[24:27], a[96:99], v[12:15], v[24:27]\n"
        "v_exp_f32 v76, v76\n"
        "v_mfma_f32_16x16x32_bf16 v[28:31], a[100:103], v[12:15], v[28:31]\n"
        "v_exp_f32 v77, v77\n"
        "v_mfma_f32_16x16x32_bf16 v[32:35], a[104:107], v[12:15], v[32:35]\n"
        "v_exp_f32 v78, v78\n"
        "v_mfma_f32_16x16x32_bf16 v[36:39], a[108:111], v[12:15], v[36:39]\n"
        "v_exp_f32 v79, v79\n"
        "v_mfma_f32_16x16x32_bf16 v[40:43], a[112:115], v[12:15], v[40:43]\n"
        "s_waitcnt vmcnt(1)\n"
        "v_cmp_gt_i32 vcc, s70, v4\n"
        "v_mul_f32 v56, v56, v60\n"
        "v_mfma_f32_16x16x32_bf16 v[44:47], a[116:119], v[12:15], v[44:47]\n"
        "s_waitcnt lgkmcnt(1)\n"
        "ds_read_b128 v[12:15], v0 offset:3584\n"
        "v_mul_f32 v57, v57, v61\n"
        "v_mfma_f32_16x16x32_bf16 v[24:27], a[120:123], v[16:19], v[24:27]\n"
        "v_mul_f32 v58, v58, v62\n"
        "s_and_b64 vcc, vcc, 1\n"
        "v_mfma_f32_16x16x32_bf16 v[28:31], a[124:127], v[16:19], v[28:31]\n"
        "s_cbranch_vccnz .Lf1_i00_%=\n"
        "v_mfma_f32_16x16x32_bf16 v[32:35], a[128:131], v[16:19], v[32:35]\n"
        "v_mul_f32 v59, v59, v63\n"
        "buffer_load_dwordx4 v1, s[40:43], s71 offen offset:0 sc1 lds\n"
        "v_mfma_f32_16x16x32_bf16 v[36:39], a[132:135], v[16:19], v[36:39]\n"
        "v_add_f32 v76, 1.0, v76\n"
        "v_add_f32 v77, 1.0, v77\n"
        "v_mfma_f32_16x16x32_bf16 v[40:43], a[136:139], v[16:19], v[40:43]\n"
        "v_add_f32 v78, 1.0, v78\n"
        "v_add_f32 v79, 1.0, v79\n"
        "v_mfma_f32_16x16x32_bf16 v[44:47], a[140:143], v[16:19], v[44:47]\n"
        "s_waitcnt lgkmcnt(1)\n"
        "ds_read_b128 v[16:19], v0 offset:4096\n"
        "v_mul_f32 v73, s80, v65\n"
        "v_mfma_f32_16x16x32_bf16 v[24:27], a[144:147], v[8:11], v[24:27]\n"
        "v_rcp_f32 v76, v76\n"
        "buffer_load_dwordx4 v1, s[40:43], s71 offen offset:1024 sc1 lds\n"
        "v_mfma_f32_16x16x32_bf16 v[28:31], a[148:151], v[8:11], v[28:31]\n"
        "v_rcp_f32 v77, v77\n"
        "v_mfma_f32_16x16x32_bf16 v[32:35], a[152:155], v[8:11], v[32:35]\n"
        "v_rcp_f32 v78, v78\n"
        "v_mfma_f32_16x16x32_bf16 v[36:39], a[156:159], v[8:11], v[36:39]\n"
        "v_rcp_f32 v79, v79\n"
        "v_mfma_f32_16x16x32_bf16 v[40:43], a[160:163], v[8:11], v[40:43]\n"
        "v_mul_f32 v56, v56, v76\n"
        "v_mul_f32 v57, v57, v77\n"
        "buffer_load_dwordx4 v1, s[40:43], s71 offen offset:2048 sc1 lds\n"
        "v_mfma_f32_16x16x32_bf16 v[44:47], a[164:167], v[8:11], v[44:47]\n"
        "s_waitcnt lgkmcnt(1)\n"
        "ds_read_b128 v[8:11], v0 offset:4608\n"
        "v_mul_f32 v58, v58, v78\n"
        "v_mfma_f32_16x16x32_bf16 v[24:27], a[168:171], v[12:15], v[24:27]\n"
        "v_mul_f32 v59, v59, v79\n"
        "v_cvt_pk_bf16_f32 v54, v56, v57\n"
        "v_mfma_f32_16x16x32_bf16 v[28:31], a[172:175], v[12:15], v[28:31]\n"
        "v_cvt_pk_bf16_f32 v55, v58, v59\n"
        "v_mul_f32 v74, s80, v66\n"
        "v_mfma_f32_16x16x32_bf16 v[32:35], a[176:179], v[12:15], v[32:35]\n"
        "buffer_store_dwordx4 v[52:55], v1, s[40:43], s72 offen offset:0 sc1\n"
        "v_mfma_f32_16x16x32_bf16 v[36:39], a[180:183], v[12:15], v[36:39]\n"
        "v_mul_f32 v75, s80, v67\n"
        "buffer_load_dwordx4 v1, s[40:43], s71 offen offset:3072 sc1 lds\n"
        "v_mfma_f32_16x16x32_bf16 v[40:43], a[184:187], v[12:15], v[40:43]\n"
        "s_add_u32 m0, s78, s85\n"
        "v_exp_f32 v72, v72\n"
        "v_mfma_f32_16x16x32_bf16 v[44:47], a[188:191], v[12:15], v[44:47]\n"
        "s_waitcnt lgkmcnt(1)\n"
        "ds_read_b128 v[12:15], v0 offset:5120\n"
        "s_add_u32 s69, s69, 1\n"
        "v_mfma_f32_16x16x32_bf16 v[24:27], a[192:195], v[16:19], v[24:27]\n"
        "v_exp_f32 v73, v73\n"
        "buffer_load_dwordx4 v1, s[40:43], s81 offen offset:0 sc1 lds\n"
        "v_mfma_f32_16x16x32_bf16 v[28:31], a[196:199], v[16:19], v[28:31]\n"
        "v_exp_f32 v74, v74\n"
        "v_mfma_f32_16x16x32_bf16 v[32:35], a[200:203], v[16:19], v[32:35]\n"
        "v_exp_f32 v75, v75\n"
        "v_mfma_f32_16x16x32_bf16 v[36:39], a[204:207], v[16:19], v[36:39]\n"
        "v_mul_f32 v64, v64, v68\n"
        "v_mul_f32 v65, v65, v69\n"
        "v_mfma_f32_16x16x32_bf16 v[40:43], a[208:211], v[16:19], v[40:43]\n"
        "v_mul_f32 v66, v66, v70\n"
        "v_mul_f32 v67, v67, v71\n"
        "v_mfma_f32_16x16x32_bf16 v[44:47], a[212:215], v[16:19], v[44:47]\n"
        "s_waitcnt lgkmcnt(1)\n"
        "ds_read_b128 v[16:19], v0 offset:5632\n"
        "v_add_f32 v72, 1.0, v72\n"
        "v_mfma_f32_16x16x32_bf16 v[24:27], a[216:219], v[8:11], v[24:27]\n"
        "v_add_f32 v73, 1.0, v73\n"
        "v_add_f32 v74, 1.0, v74\n"
        "v_mfma_f32_16x16x32_bf16 v[28:31], a[220:223], v[8:11], v[28:31]\n"
        "v_add_f32 v75, 1.0, v75\n"
        "v_mfma_f32_16x16x32_bf16 v[32:35], a[224:227], v[8:11], v[32:35]\n"
        "v_rcp_f32 v72, v72\n"
        "v_mfma_f32_16x16x32_bf16 v[36:39], a[228:231], v[8:11], v[36:39]\n"
        "v_rcp_f32 v73, v73\n"
        "v_mfma_f32_16x16x32_bf16 v[40:43], a[232:235], v[8:11], v[40:43]\n"
        "v_rcp_f32 v74, v74\n"
        "v_mfma_f32_16x16x32_bf16 v[44:47], a[236:239], v[8:11], v[44:47]\n"
        "s_waitcnt lgkmcnt(1)\n"
        "ds_read_b128 v[8:11], v0 offset:6144\n"
        "v_mfma_f32_16x16x32_bf16 v[24:27], a[240:243], v[12:15], v[24:27]\n"
        "v_rcp_f32 v75, v75\n"
        "v_mfma_f32_16x16x32_bf16 v[28:31], a[244:247], v[12:15], v[28:31]\n"
        "v_mul_f32 v64, v64, v72\n"
        "v_mfma_f32_16x16x32_bf16 v[32:35], a[248:251], v[12:15], v[32:35]\n"
        "v_mfma_f32_16x16x32_bf16 v[36:39], a[252:255], v[12:15], v[36:39]\n"
        "v_mul_f32 v65, v65, v73\n"
        "v_mfma_f32_16x16x32_bf16 v[40:43], v[80:83], v[12:15], v[40:43]\n"
        "v_mul_f32 v66, v66, v74\n"
        "v_mul_f32 v67, v67, v75\n"
        "v_mfma_f32_16x16x32_bf16 v[44:47], v[84:87], v[12:15], v[44:47]\n"
        "s_waitcnt lgkmcnt(1)\n"
        "ds_read_b128 v[12:15], v0 offset:6656\n"
        "v_cvt_pk_bf16_f32 v68, v64, v65\n"
        "v_mfma_f32_16x16x32_bf16 v[24:27], v[88:91], v[16:19], v[24:27]\n"
        "v_cvt_pk_bf16_f32 v69, v66, v67\n"
        "s_nop 0\n"
        "v_mfma_f32_16x16x32_bf16 v[28:31], v[92:95], v[16:19], v[28:31]\n"
        "v_permlane16_swap_b32 v68, v70\n"
        "v_mfma_f32_16x16x32_bf16 v[32:35], v[96:99], v[16:19], v[32:35]\n"
        "v_permlane16_swap_b32 v69, v71\n"
        "v_mfma_f32_16x16x32_bf16 v[36:39], v[100:103], v[16:19], v[36:39]\n"
        "buffer_store_dwordx4 v[68:71], v7, s[40:43], s83 offen offset:0 sc1\n"
        "v_mfma_f32_16x16x32_bf16 v[40:43], v[104:107], v[16:19], v[40:43]\n"
        "v_mov_b32 v5, s74\n"
        "v_mfma_f32_16x16x32_bf16 v[44:47], v[108:111], v[16:19], v[44:47]\n"
        "s_waitcnt lgkmcnt(1)\n"
        "ds_read_b128 v[16:19], v0 offset:7168\n"
        "v_mfma_f32_16x16x32_bf16 v[24:27], v[112:115], v[8:11], v[24:27]\n"
        "v_mfma_f32_16x16x32_bf16 v[28:31], v[116:119], v[8:11], v[28:31]\n"
        "v_mfma_f32_16x16x32_bf16 v[32:35], v[120:123], v[8:11], v[32:35]\n"
        "v_mfma_f32_16x16x32_bf16 v[36:39], v[124:127], v[8:11], v[36:39]\n"
        "v_mfma_f32_16x16x32_bf16 v[40:43], v[128:131], v[8:11], v[40:43]\n"
        "v_mfma_f32_16x16x32_bf16 v[44:47], v[132:135], v[8:11], v[44:47]\n"
        "s_waitcnt lgkmcnt(1)\n"
        "ds_read_b128 v[8:11], v0 offset:7680\n"
        "v_mfma_f32_16x16x32_bf16 v[24:27], v[136:139], v[12:15], v[24:27]\n"
        "v_mfma_f32_16x16x32_bf16 v[28:31], v[140:143], v[12:15], v[28:31]\n"
        "v_mfma_f32_16x16x32_bf16 v[32:35], v[144:147], v[12:15], v[32:35]\n"
        "v_mfma_f32_16x16x32_bf16 v[36:39], v[148:151], v[12:15], v[36:39]\n"
        "v_mfma_f32_16x16x32_bf16 v[40:43], v[152:155], v[12:15], v[40:43]\n"
        "v_mfma_f32_16x16x32_bf16 v[44:47], v[156:159], v[12:15], v[44:47]\n"
        "s_waitcnt lgkmcnt(1)\n"
        "ds_read_b128 v[12:15], v0 offset:8192\n"
        "v_mfma_f32_16x16x32_bf16 v[24:27], v[160:163], v[16:19], v[24:27]\n"
        "v_mfma_f32_16x16x32_bf16 v[28:31], v[164:167], v[16:19], v[28:31]\n"
        "v_mfma_f32_16x16x32_bf16 v[32:35], v[168:171], v[16:19], v[32:35]\n"
        "v_mfma_f32_16x16x32_bf16 v[36:39], v[172:175], v[16:19], v[36:39]\n"
        "v_mfma_f32_16x16x32_bf16 v[40:43], v[176:179], v[16:19], v[40:43]\n"
        "v_mfma_f32_16x16x32_bf16 v[44:47], v[180:183], v[16:19], v[44:47]\n"
        "s_waitcnt lgkmcnt(1)\n"
        "ds_read_b128 v[16:19], v0 offset:8704\n"
        "v_mfma_f32_16x16x32_bf16 v[24:27], v[184:187], v[8:11], v[24:27]\n"
        "v_mfma_f32_16x16x32_bf16 v[28:31], v[188:191], v[8:11], v[28:31]\n"
        "s_getreg_b32 s88, hwreg(HW_REG_IB_STS, 0, 4)\n"
        "v_mfma_f32_16x16x32_bf16 v[32:35], v[192:195], v[8:11], v[32:35]\n"
        "v_mfma_f32_16x16x32_bf16 v[36:39], v[196:199], v[8:11], v[36:39]\n"
        "v_mfma_f32_16x16x32_bf16 v[40:43], v[200:203], v[8:11], v[40:43]\n"
        "s_waitcnt vmcnt(1)\n"
        "s_barrier\n"
        "v_add_u32 v0, s78, v6\n"
        "v_mfma_f32_16x16x32_bf16 v[44:47], v[204:207], v[8:11], v[44:47]\n"
        "s_waitcnt lgkmcnt(1)\n"
        "ds_read_b128 v[8:11], v0 offset:0\n"
        "v_mfma_f32_16x16x32_bf16 v[24:27], v[208:211], v[12:15], v[24:27]\n"
        "s_cmp_le_u32 s88, 0\n"
        "s_cselect_b32 s88, s73, s89\n"
        "s_cselect_b32 s73, s89, s73\n"
        "v_mfma_f32_16x16x32_bf16 v[28:31], v[212:215], v[12:15], v[28:31]\n"
        "buffer_store_dword v5, v3, s[40:43], s88 offen sc1\n"
        "v_mfma_f32_16x16x32_bf16 v[32:35], v[216:219], v[12:15], v[32:35]\n"
        "s_getreg_b32 s88, hwreg(HW_REG_IB_STS, 0, 4)\n"
        "v_mfma_f32_16x16x32_bf16 v[36:39], v[220:223], v[12:15], v[36:39]\n"
        "s_cmp_le_u32 s88, 1\n"
        "s_cselect_b32 s88, s73, s89\n"
        "s_cselect_b32 s73, s89, s73\n"
        "v_mfma_f32_16x16x32_bf16 v[40:43], v[224:227], v[12:15], v[40:43]\n"
        "buffer_store_dword v5, v3, s[40:43], s88 offen sc1\n"
        "s_add_u32 s74, s74, 1\n"
        "v_mfma_f32_16x16x32_bf16 v[44:47], v[228:231], v[12:15], v[44:47]\n"
        "s_waitcnt lgkmcnt(1)\n"
        "ds_read_b128 v[12:15], v0 offset:512\n"
        "s_mov_b32 s84, s73\n"
        "v_mfma_f32_16x16x32_bf16 v[24:27], v[232:235], v[16:19], v[24:27]\n"
        "s_mov_b32 s73, s60\n"
        "s_add_u32 s68, s68, 1\n"
        "s_lshr_b32 s66, s68, 1\n"
        "v_mfma_f32_16x16x32_bf16 v[28:31], v[236:239], v[16:19], v[28:31]\n"
        "s_bitcmp1_b32 s54, s66\n"
        "s_cselect_b32 s66, 8192, 0\n"
        "s_lshl_b32 s75, s68, 20\n"
        "v_mfma_f32_16x16x32_bf16 v[32:35], v[240:243], v[16:19], v[32:35]\n"
        "s_add_u32 s75, s75, s66\n"
        "s_add_u32 s72, s75, s59\n"
        "s_add_u32 s83, s75, s63\n"
        "v_mfma_f32_16x16x32_bf16 v[36:39], v[244:247], v[16:19], v[36:39]\n"
        "v_mfma_f32_16x16x32_bf16 v[40:43], v[248:251], v[16:19], v[40:43]\n"
        "v_mfma_f32_16x16x32_bf16 v[44:47], v[252:255], v[16:19], v[44:47]\n"
        "s_waitcnt lgkmcnt(1)\n"
        "ds_read_b128 v[16:19], v0 offset:1024\n"
        "s_branch .Lf1_i1_%=\n"
        ".Lf1_i00_%=:\n"
        "v_mfma_f32_16x16x32_bf16 v[32:35], a[128:131], v[16:19], v[32:35]\n"
        "v_mul_f32 v59, v59, v63\n"
        "buffer_load_dword v4, v2, s[40:43], s86 offen sc1\n"
        "v_mfma_f32_16x16x32_bf16 v[36:39], a[132:135], v[16:19], v[36:39]\n"
        "v_add_f32 v76, 1.0, v76\n"
        "v_add_f32 v77, 1.0, v77\n"
        "v_mfma_f32_16x16x32_bf16 v[40:43], a[136:139], v[16:19], v[40:43]\n"
        "v_add_f32 v78, 1.0, v78\n"
        "v_add_f32 v79, 1.0, v79\n"
        "v_mfma_f32_16x16x32_bf16 v[44:47], a[140:143], v[16:19], v[44:47]\n"
        "s_waitcnt lgkmcnt(1)\n"
        "ds_read_b128 v[16:19], v0 offset:4096\n"
        "v_mul_f32 v73, s80, v65\n"
        "v_mfma_f32_16x16x32_bf16 v[24:27], a[144:147], v[8:11], v[24:27]\n"
        "v_rcp_f32 v76, v76\n"
        "v_mfma_f32_16x16x32_bf16 v[28:31], a[148:151], v[8:11], v[28:31]\n"
        "v_rcp_f32 v77, v77\n"
        "v_mfma_f32_16x16x32_bf16 v[32:35], a[152:155], v[8:11], v[32:35]\n"
        "v_rcp_f32 v78, v78\n"
        "v_mfma_f32_16x16x32_bf16 v[36:39], a[156:159], v[8:11], v[36:39]\n"
        "v_rcp_f32 v79, v79\n"
        "v_mfma_f32_16x16x32_bf16 v[40:43], a[160:163], v[8:11], v[40:43]\n"
        "v_mul_f32 v56, v56, v76\n"
        "v_mul_f32 v57, v57, v77\n"
        "v_mfma_f32_16x16x32_bf16 v[44:47], a[164:167], v[8:11], v[44:47]\n"
        "s_waitcnt lgkmcnt(1)\n"
        "ds_read_b128 v[8:11], v0 offset:4608\n"
        "v_mul_f32 v58, v58, v78\n"
        "v_mfma_f32_16x16x32_bf16 v[24:27], a[168:171], v[12:15], v[24:27]\n"
        "v_mul_f32 v59, v59, v79\n"
        "v_cvt_pk_bf16_f32 v54, v56, v57\n"
        "v_mfma_f32_16x16x32_bf16 v[28:31], a[172:175], v[12:15], v[28:31]\n"
        "v_cvt_pk_bf16_f32 v55, v58, v59\n"
        "v_mul_f32 v74, s80, v66\n"
        "v_mfma_f32_16x16x32_bf16 v[32:35], a[176:179], v[12:15], v[32:35]\n"
        "buffer_store_dwordx4 v[52:55], v1, s[40:43], s72 offen offset:0 sc1\n"
        "v_mfma_f32_16x16x32_bf16 v[36:39], a[180:183], v[12:15], v[36:39]\n"
        "v_mul_f32 v75, s80, v67\n"
        "v_mfma_f32_16x16x32_bf16 v[40:43], a[184:187], v[12:15], v[40:43]\n"
        "v_exp_f32 v72, v72\n"
        "v_mfma_f32_16x16x32_bf16 v[44:47], a[188:191], v[12:15], v[44:47]\n"
        "s_waitcnt lgkmcnt(1)\n"
        "ds_read_b128 v[12:15], v0 offset:5120\n"
        "v_mfma_f32_16x16x32_bf16 v[24:27], a[192:195], v[16:19], v[24:27]\n"
        "v_exp_f32 v73, v73\n"
        "v_mfma_f32_16x16x32_bf16 v[28:31], a[196:199], v[16:19], v[28:31]\n"
        "v_exp_f32 v74, v74\n"
        "v_mfma_f32_16x16x32_bf16 v[32:35], a[200:203], v[16:19], v[32:35]\n"
        "v_exp_f32 v75, v75\n"
        "v_mfma_f32_16x16x32_bf16 v[36:39], a[204:207], v[16:19], v[36:39]\n"
        "v_mul_f32 v64, v64, v68\n"
        "v_mul_f32 v65, v65, v69\n"
        "v_mfma_f32_16x16x32_bf16 v[40:43], a[208:211], v[16:19], v[40:43]\n"
        "v_mul_f32 v66, v66, v70\n"
        "v_mul_f32 v67, v67, v71\n"
        "v_mfma_f32_16x16x32_bf16 v[44:47], a[212:215], v[16:19], v[44:47]\n"
        "s_waitcnt lgkmcnt(1)\n"
        "ds_read_b128 v[16:19], v0 offset:5632\n"
        "v_add_f32 v72, 1.0, v72\n"
        "v_mfma_f32_16x16x32_bf16 v[24:27], a[216:219], v[8:11], v[24:27]\n"
        "v_add_f32 v73, 1.0, v73\n"
        "v_add_f32 v74, 1.0, v74\n"
        "v_mfma_f32_16x16x32_bf16 v[28:31], a[220:223], v[8:11], v[28:31]\n"
        "v_add_f32 v75, 1.0, v75\n"
        "v_mfma_f32_16x16x32_bf16 v[32:35], a[224:227], v[8:11], v[32:35]\n"
        "v_rcp_f32 v72, v72\n"
        "v_mfma_f32_16x16x32_bf16 v[36:39], a[228:231], v[8:11], v[36:39]\n"
        "v_rcp_f32 v73, v73\n"
        "v_mfma_f32_16x16x32_bf16 v[40:43], a[232:235], v[8:11], v[40:43]\n"
        "v_rcp_f32 v74, v74\n"
        "v_mfma_f32_16x16x32_bf16 v[44:47], a[236:239], v[8:11], v[44:47]\n"
        "s_waitcnt vmcnt(1)\n"
        "v_cmp_gt_i32 vcc, s70, v4\n"
        "s_waitcnt lgkmcnt(1)\n"
        "ds_read_b128 v[8:11], v0 offset:6144\n"
        "v_mfma_f32_16x16x32_bf16 v[24:27], a[240:243], v[12:15], v[24:27]\n"
        "v_rcp_f32 v75, v75\n"
        "v_mfma_f32_16x16x32_bf16 v[28:31], a[244:247], v[12:15], v[28:31]\n"
        "v_mul_f32 v64, v64, v72\n"
        "s_and_b64 vcc, vcc, 1\n"
        "v_mfma_f32_16x16x32_bf16 v[32:35], a[248:251], v[12:15], v[32:35]\n"
        "s_cbranch_vccnz .Lf1_i01_%=\n"
        "v_mfma_f32_16x16x32_bf16 v[36:39], a[252:255], v[12:15], v[36:39]\n"
        "v_mul_f32 v65, v65, v73\n"
        "buffer_load_dwordx4 v1, s[40:43], s71 offen offset:0 sc1 lds\n"
        "v_mfma_f32_16x16x32_bf16 v[40:43], v[80:83], v[12:15], v[40:43]\n"
        "v_mul_f32 v66, v66, v74\n"
        "v_mul_f32 v67, v67, v75\n"
        "v_mfma_f32_16x16x32_bf16 v[44:47], v[84:87], v[12:15], v[44:47]\n"
        "s_waitcnt lgkmcnt(1)\n"
        "ds_read_b128 v[12:15], v0 offset:6656\n"
        "v_cvt_pk_bf16_f32 v68, v64, v65\n"
        "v_mfma_f32_16x16x32_bf16 v[24:27], v[88:91], v[16:19], v[24:27]\n"
        "v_cvt_pk_bf16_f32 v69, v66, v67\n"
        "s_nop 0\n"
        "buffer_load_dwordx4 v1, s[40:43], s71 offen offset:1024 sc1 lds\n"
        "v_mfma_f32_16x16x32_bf16 v[28:31], v[92:95], v[16:19], v[28:31]\n"
        "v_permlane16_swap_b32 v68, v70\n"
        "v_mfma_f32_16x16x32_bf16 v[32:35], v[96:99], v[16:19], v[32:35]\n"
        "v_permlane16_swap_b32 v69, v71\n"
        "v_mfma_f32_16x16x32_bf16 v[36:39], v[100:103], v[16:19], v[36:39]\n"
        "buffer_store_dwordx4 v[68:71], v7, s[40:43], s83 offen offset:0 sc1\n"
        "v_mfma_f32_16x16x32_bf16 v[40:43], v[104:107], v[16:19], v[40:43]\n"
        "v_mov_b32 v5, s74\n"
        "buffer_load_dwordx4 v1, s[40:43], s71 offen offset:2048 sc1 lds\n"
        "v_mfma_f32_16x16x32_bf16 v[44:47], v[108:111], v[16:19], v[44:47]\n"
        "s_waitcnt lgkmcnt(1)\n"
        "ds_read_b128 v[16:19], v0 offset:7168\n"
        "v_mfma_f32_16x16x32_bf16 v[24:27], v[112:115], v[8:11], v[24:27]\n"
        "buffer_load_dwordx4 v1, s[40:43], s71 offen offset:3072 sc1 lds\n"
        "s_add_u32 m0, s78, s85\n"
        "v_mfma_f32_16x16x32_bf16 v[28:31], v[116:119], v[8:11], v[28:31]\n"
        "s_add_u32 s69, s69, 1\n"
        "v_mfma_f32_16x16x32_bf16 v[32:35], v[120:123], v[8:11], v[32:35]\n"
        "v_mfma_f32_16x16x32_bf16 v[36:39], v[124:127], v[8:11], v[36:39]\n"
        "buffer_load_dwordx4 v1, s[40:43], s81 offen offset:0 sc1 lds\n"
        "v_mfma_f32_16x16x32_bf16 v[40:43], v[128:131], v[8:11], v[40:43]\n"
        "v_mfma_f32_16x16x32_bf16 v[44:47], v[132:135], v[8:11], v[44:47]\n"
        "s_waitcnt lgkmcnt(1)\n"
        "ds_read_b128 v[8:11], v0 offset:7680\n"
        "v_mfma_f32_16x16x32_bf16 v[24:27], v[136:139], v[12:15], v[24:27]\n"
        "v_mfma_f32_16x16x32_bf16 v[28:31], v[140:143], v[12:15], v[28:31]\n"
        "v_mfma_f32_16x16x32_bf16 v[32:35], v[144:147], v[12:15], v[32:35]\n"
        "v_mfma_f32_16x16x32_bf16 v[36:39], v[148:151], v[12:15], v[36:39]\n"
        "v_mfma_f32_16x16x32_bf16 v[40:43], v[152:155], v[12:15], v[40:43]\n"
        "v_mfma_f32_16x16x32_bf16 v[44:47], v[156:159], v[12:15], v[44:47]\n"
        "s_waitcnt lgkmcnt(1)\n"
        "ds_read_b128 v[12:15], v0 offset:8192\n"
        "v_mfma_f32_16x16x32_bf16 v[24:27], v[160:163], v[16:19], v[24:27]\n"
        "v_mfma_f32_16x16x32_bf16 v[28:31], v[164:167], v[16:19], v[28:31]\n"
        "v_mfma_f32_16x16x32_bf16 v[32:35], v[168:171], v[16:19], v[32:35]\n"
        "v_mfma_f32_16x16x32_bf16 v[36:39], v[172:175], v[16:19], v[36:39]\n"
        "v_mfma_f32_16x16x32_bf16 v[40:43], v[176:179], v[16:19], v[40:43]\n"
        "v_mfma_f32_16x16x32_bf16 v[44:47], v[180:183], v[16:19], v[44:47]\n"
        "s_waitcnt lgkmcnt(1)\n"
        "ds_read_b128 v[16:19], v0 offset:8704\n"
        "v_mfma_f32_16x16x32_bf16 v[24:27], v[184:187], v[8:11], v[24:27]\n"
        "v_mfma_f32_16x16x32_bf16 v[28:31], v[188:191], v[8:11], v[28:31]\n"
        "s_getreg_b32 s88, hwreg(HW_REG_IB_STS, 0, 4)\n"
        "v_mfma_f32_16x16x32_bf16 v[32:35], v[192:195], v[8:11], v[32:35]\n"
        "v_mfma_f32_16x16x32_bf16 v[36:39], v[196:199], v[8:11], v[36:39]\n"
        "v_mfma_f32_16x16x32_bf16 v[40:43], v[200:203], v[8:11], v[40:43]\n"
        "s_waitcnt vmcnt(0)\n"
        "s_barrier\n"
        "v_add_u32 v0, s78, v6\n"
        "v_mfma_f32_16x16x32_bf16 v[44:47], v[204:207], v[8:11], v[44:47]\n"
        "s_waitcnt lgkmcnt(1)\n"
        "ds_read_b128 v[8:11], v0 offset:0\n"
        "v_mfma_f32_16x16x32_bf16 v[24:27], v[208:211], v[12:15], v[24:27]\n"
        "s_cmp_le_u32 s88, 3\n"
        "s_cselect_b32 s88, s73, s89\n"
        "s_cselect_b32 s73, s89, s73\n"
        "v_mfma_f32_16x16x32_bf16 v[28:31], v[212:215], v[12:15], v[28:31]\n"
        "buffer_store_dword v5, v3, s[40:43], s88 offen sc1\n"
        "v_mfma_f32_16x16x32_bf16 v[32:35], v[216:219], v[12:15], v[32:35]\n"
        "s_getreg_b32 s88, hwreg(HW_REG_IB_STS, 0, 4)\n"
        "v_mfma_f32_16x16x32_bf16 v[36:39], v[220:223], v[12:15], v[36:39]\n"
        "s_cmp_le_u32 s88, 15\n"
        "s_cselect_b32 s88, s73, s89\n"
        "s_cselect_b32 s73, s89, s73\n"
        "v_mfma_f32_16x16x32_bf16 v[40:43], v[224:227], v[12:15], v[40:43]\n"
        "buffer_store_dword v5, v3, s[40:43], s88 offen sc1\n"
        "s_add_u32 s74, s74, 1\n"
        "v_mfma_f32_16x16x32_bf16 v[44:47], v[228:231], v[12:15], v[44:47]\n"
        "s_waitcnt lgkmcnt(1)\n"
        "ds_read_b128 v[12:15], v0 offset:512\n"
        "s_mov_b32 s84, s73\n"
        "v_mfma_f32_16x16x32_bf16 v[24:27], v[232:235], v[16:19], v[24:27]\n"
        "s_mov_b32 s73, s60\n"
        "s_add_u32 s68, s68, 1\n"
        "s_lshr_b32 s66, s68, 1\n"
        "v_mfma_f32_16x16x32_bf16 v[28:31], v[236:239], v[16:19], v[28:31]\n"
        "s_bitcmp1_b32 s54, s66\n"
        "s_cselect_b32 s66, 8192, 0\n"
        "s_lshl_b32 s75, s68, 20\n"
        "v_mfma_f32_16x16x32_bf16 v[32:35], v[240:243], v[16:19], v[32:35]\n"
        "s_add_u32 s75, s75, s66\n"
        "s_add_u32 s72, s75, s59\n"
        "s_add_u32 s83, s75, s63\n"
        "v_mfma_f32_16x16x32_bf16 v[36:39], v[244:247], v[16:19], v[36:39]\n"
        "v_mfma_f32_16x16x32_bf16 v[40:43], v[248:251], v[16:19], v[40:43]\n"
        "v_mfma_f32_16x16x32_bf16 v[44:47], v[252:255], v[16:19], v[44:47]\n"
        "s_waitcnt lgkmcnt(1)\n"
        "ds_read_b128 v[16:19], v0 offset:1024\n"
        "s_branch .Lf1_i1_%=\n"
        ".Lf1_i01_%=:\n"
        "v_mfma_f32_16x16x32_bf16 v[36:39], a[252:255], v[12:15], v[36:39]\n"
        "v_mul_f32 v65, v65, v73\n"
        "buffer_load_dword v4, v2, s[40:43], s86 offen sc1\n"
        "v_mfma_f32_16x16x32_bf16 v[40:43], v[80:83], v[12:15], v[40:43]\n"
        "v_mul_f32 v66, v66, v74\n"
        "v_mul_f32 v67, v67, v75\n"
        "v_mfma_f32_16x16x32_bf16 v[44:47], v[84:87], v[12:15], v[44:47]\n"
        "s_waitcnt lgkmcnt(1)\n"
        "ds_read_b128 v[12:15], v0 offset:6656\n"
        "v_cvt_pk_bf16_f32 v68, v64, v65\n"
        "v_mfma_f32_16x16x32_bf16 v[24:27], v[88:91], v[16:19], v[24:27]\n"
        "v_cvt_pk_bf16_f32 v69, v66, v67\n"
        "s_nop 0\n"
        "v_mfma_f32_16x16x32_bf16 v[28:31], v[92:95], v[16:19], v[28:31]\n"
        "v_permlane16_swap_b32 v68, v70\n"
        "v_mfma_f32_16x16x32_bf16 v[32:35], v[96:99], v[16:19], v[32:35]\n"
        "v_permlane16_swap_b32 v69, v71\n"
        "v_mfma_f32_16x16x32_bf16 v[36:39], v[100:103], v[16:19], v[36:39]\n"
        "buffer_store_dwordx4 v[68:71], v7, s[40:43], s83 offen offset:0 sc1\n"
        "v_mfma_f32_16x16x32_bf16 v[40:43], v[104:107], v[16:19], v[40:43]\n"
        "v_mov_b32 v5, s74\n"
        "v_mfma_f32_16x16x32_bf16 v[44:47], v[108:111], v[16:19], v[44:47]\n"
        "s_waitcnt lgkmcnt(1)\n"
        "ds_read_b128 v[16:19], v0 offset:7168\n"
        "v_mfma_f32_16x16x32_bf16 v[24:27], v[112:115], v[8:11], v[24:27]\n"
        "v_mfma_f32_16x16x32_bf16 v[28:31], v[116:119], v[8:11], v[28:31]\n"
        "v_mfma_f32_16x16x32_bf16 v[32:35], v[120:123], v[8:11], v[32:35]\n"
        "v_mfma_f32_16x16x32_bf16 v[36:39], v[124:127], v[8:11], v[36:39]\n"
        "v_mfma_f32_16x16x32_bf16 v[40:43], v[128:131], v[8:11], v[40:43]\n"
        "v_mfma_f32_16x16x32_bf16 v[44:47], v[132:135], v[8:11], v[44:47]\n"
        "s_waitcnt lgkmcnt(1)\n"
        "ds_read_b128 v[8:11], v0 offset:7680\n"
        "v_mfma_f32_16x16x32_bf16 v[24:27], v[136:139], v[12:15], v[24:27]\n"
        "v_mfma_f32_16x16x32_bf16 v[28:31], v[140:143], v[12:15], v[28:31]\n"
        "v_mfma_f32_16x16x32_bf16 v[32:35], v[144:147], v[12:15], v[32:35]\n"
        "v_mfma_f32_16x16x32_bf16 v[36:39], v[148:151], v[12:15], v[36:39]\n"
        "v_mfma_f32_16x16x32_bf16 v[40:43], v[152:155], v[12:15], v[40:43]\n"
        "v_mfma_f32_16x16x32_bf16 v[44:47], v[156:159], v[12:15], v[44:47]\n"
        "s_waitcnt lgkmcnt(1)\n"
        "ds_read_b128 v[12:15], v0 offset:8192\n"
        "v_mfma_f32_16x16x32_bf16 v[24:27], v[160:163], v[16:19], v[24:27]\n"
        "v_mfma_f32_16x16x32_bf16 v[28:31], v[164:167], v[16:19], v[28:31]\n"
        "v_mfma_f32_16x16x32_bf16 v[32:35], v[168:171], v[16:19], v[32:35]\n"
        "v_mfma_f32_16x16x32_bf16 v[36:39], v[172:175], v[16:19], v[36:39]\n"
        "v_mfma_f32_16x16x32_bf16 v[40:43], v[176:179], v[16:19], v[40:43]\n"
        "v_mfma_f32_16x16x32_bf16 v[44:47], v[180:183], v[16:19], v[44:47]\n"
        "s_waitcnt lgkmcnt(1)\n"
        "ds_read_b128 v[16:19], v0 offset:8704\n"
        "v_mfma_f32_16x16x32_bf16 v[24:27], v[184:187], v[8:11], v[24:27]\n"
        "s_waitcnt vmcnt(1)\n"
        "v_cmp_gt_i32 vcc, s70, v4\n"
        "v_mfma_f32_16x16x32_bf16 v[28:31], v[188:191], v[8:11], v[28:31]\n"
        "s_getreg_b32 s88, hwreg(HW_REG_IB_STS, 0, 4)\n"
        "v_mfma_f32_16x16x32_bf16 v[32:35], v[192:195], v[8:11], v[32:35]\n"
        "s_and_b64 vcc, vcc, 1\n"
        "v_mfma_f32_16x16x32_bf16 v[36:39], v[196:199], v[8:11], v[36:39]\n"
        "s_cbranch_vccnz .Lf1_i02_%=\n"
        "v_mfma_f32_16x16x32_bf16 v[40:43], v[200:203], v[8:11], v[40:43]\n"
        "buffer_load_dwordx4 v1, s[40:43], s71 offen offset:0 sc1 lds\n"
        "v_mfma_f32_16x16x32_bf16 v[44:47], v[204:207], v[8:11], v[44:47]\n"
        "s_waitcnt lgkmcnt(1)\n"
        "v_mfma_f32_16x16x32_bf16 v[24:27], v[208:211], v[12:15], v[24:27]\n"
        "s_cmp_le_u32 s88, 0\n"
        "s_cselect_b32 s88, s73, s89\n"
        "s_cselect_b32 s73, s89, s73\n"
        "buffer_load_dwordx4 v1, s[40:43], s71 offen offset:1024 sc1 lds\n"
        "v_mfma_f32_16x16x32_bf16 v[28:31], v[212:215], v[12:15], v[28:31]\n"
        "buffer_store_dword v5, v3, s[40:43], s88 offen sc1\n"
        "v_mfma_f32_16x16x32_bf16 v[32:35], v[216:219], v[12:15], v[32:35]\n"
        "s_getreg_b32 s88, hwreg(HW_REG_IB_STS, 0, 4)\n"
        "buffer_load_dwordx4 v1, s[40:43], s71 offen offset:2048 sc1 lds\n"
        "v_mfma_f32_16x16x32_bf16 v[36:39], v[220:223], v[12:15], v[36:39]\n"
        "s_cmp_le_u32 s88, 3\n"
        "s_cselect_b32 s88, s73, s89\n"
        "s_cselect_b32 s73, s89, s73\n"
        "v_mfma_f32_16x16x32_bf16 v[40:43], v[224:227], v[12:15], v[40:43]\n"
        "buffer_store_dword v5, v3, s[40:43], s88 offen sc1\n"
        "s_add_u32 s74, s74, 1\n"
        "buffer_load_dwordx4 v1, s[40:43], s71 offen offset:3072 sc1 lds\n"
        "s_add_u32 m0, s78, s85\n"
        "s_add_u32 s69, s69, 1\n"
        "v_mfma_f32_16x16x32_bf16 v[44:47], v[228:231], v[12:15], v[44:47]\n"
        "s_waitcnt lgkmcnt(0)\n"
        "s_mov_b32 s84, s73\n"
        "v_mfma_f32_16x16x32_bf16 v[24:27], v[232:235], v[16:19], v[24:27]\n"
        "s_mov_b32 s73, s60\n"
        "s_add_u32 s68, s68, 1\n"
        "s_lshr_b32 s66, s68, 1\n"
        "buffer_load_dwordx4 v1, s[40:43], s81 offen offset:0 sc1 lds\n"
        "v_mfma_f32_16x16x32_bf16 v[28:31], v[236:239], v[16:19], v[28:31]\n"
        "s_bitcmp1_b32 s54, s66\n"
        "s_cselect_b32 s66, 8192, 0\n"
        "s_lshl_b32 s75, s68, 20\n"
        "v_mfma_f32_16x16x32_bf16 v[32:35], v[240:243], v[16:19], v[32:35]\n"
        "s_add_u32 s75, s75, s66\n"
        "s_add_u32 s72, s75, s59\n"
        "s_add_u32 s83, s75, s63\n"
        "v_mfma_f32_16x16x32_bf16 v[36:39], v[244:247], v[16:19], v[36:39]\n"
        "v_mfma_f32_16x16x32_bf16 v[40:43], v[248:251], v[16:19], v[40:43]\n"
        "v_mfma_f32_16x16x32_bf16 v[44:47], v[252:255], v[16:19], v[44:47]\n"
        "v_mul_f32 v72, s80, v24\n"
        "v_mul_f32 v73, s80, v25\n"
        "v_mul_f32 v74, s80, v26\n"
        "v_mul_f32 v75, s80, v27\n"
        "v_exp_f32 v72, v72\n"
        "v_exp_f32 v73, v73\n"
        "v_exp_f32 v74, v74\n"
        "v_exp_f32 v75, v75\n"
        "v_mul_f32 v24, v24, v28\n"
        "v_mul_f32 v25, v25, v29\n"
        "v_mul_f32 v26, v26, v30\n"
        "v_mul_f32 v27, v27, v31\n"
        "v_add_f32 v72, 1.0, v72\n"
        "v_add_f32 v73, 1.0, v73\n"
        "v_add_f32 v74, 1.0, v74\n"
        "v_add_f32 v75, 1.0, v75\n"
        "v_rcp_f32 v72, v72\n"
        "v_rcp_f32 v73, v73\n"
        "v_rcp_f32 v74, v74\n"
        "v_rcp_f32 v75, v75\n"
        "v_mul_f32 v24, v24, v72\n"
        "v_mul_f32 v25, v25, v73\n"
        "v_mul_f32 v26, v26, v74\n"
        "v_mul_f32 v27, v27, v75\n"
        "v_cvt_pk_bf16_f32 v28, v24, v25\n"
        "v_cvt_pk_bf16_f32 v29, v26, v27\n"
        "s_waitcnt vmcnt(7)\n"
        "buffer_store_dword v5, v3, s[40:43], s84 offen sc1\n"
        "s_mov_b32 s84, s89\n"
        "v_mul_f32 v76, s80, v32\n"
        "v_mul_f32 v77, s80, v33\n"
        "v_mul_f32 v78, s80, v34\n"
        "v_mul_f32 v79, s80, v35\n"
        "v_exp_f32 v76, v76\n"
        "v_exp_f32 v77, v77\n"
        "v_exp_f32 v78, v78\n"
        "v_exp_f32 v79, v79\n"
        "v_mul_f32 v32, v32, v36\n"
        "v_mul_f32 v33, v33, v37\n"
        "v_mul_f32 v34, v34, v38\n"
        "v_mul_f32 v35, v35, v39\n"
        "v_add_f32 v76, 1.0, v76\n"
        "v_add_f32 v77, 1.0, v77\n"
        "v_add_f32 v78, 1.0, v78\n"
        "v_add_f32 v79, 1.0, v79\n"
        "v_rcp_f32 v76, v76\n"
        "v_rcp_f32 v77, v77\n"
        "v_rcp_f32 v78, v78\n"
        "v_rcp_f32 v79, v79\n"
        "v_mul_f32 v32, v32, v76\n"
        "v_mul_f32 v33, v33, v77\n"
        "v_mul_f32 v34, v34, v78\n"
        "v_mul_f32 v35, v35, v79\n"
        "v_cvt_pk_bf16_f32 v30, v32, v33\n"
        "v_cvt_pk_bf16_f32 v31, v34, v35\n"
        "buffer_store_dwordx4 v[28:31], v1, s[40:43], s72 offen offset:0 sc1\n"
        "v_mul_f32 v72, s80, v40\n"
        "v_mul_f32 v73, s80, v41\n"
        "v_mul_f32 v74, s80, v42\n"
        "v_mul_f32 v75, s80, v43\n"
        "v_exp_f32 v72, v72\n"
        "v_exp_f32 v73, v73\n"
        "v_exp_f32 v74, v74\n"
        "v_exp_f32 v75, v75\n"
        "v_mul_f32 v40, v40, v44\n"
        "v_mul_f32 v41, v41, v45\n"
        "v_mul_f32 v42, v42, v46\n"
        "v_mul_f32 v43, v43, v47\n"
        "v_add_f32 v72, 1.0, v72\n"
        "v_add_f32 v73, 1.0, v73\n"
        "v_add_f32 v74, 1.0, v74\n"
        "v_add_f32 v75, 1.0, v75\n"
        "v_rcp_f32 v72, v72\n"
        "v_rcp_f32 v73, v73\n"
        "v_rcp_f32 v74, v74\n"
        "v_rcp_f32 v75, v75\n"
        "v_mul_f32 v40, v40, v72\n"
        "v_mul_f32 v41, v41, v73\n"
        "v_mul_f32 v42, v42, v74\n"
        "v_mul_f32 v43, v43, v75\n"
        "v_cvt_pk_bf16_f32 v44, v40, v41\n"
        "v_cvt_pk_bf16_f32 v45, v42, v43\n"
        "s_nop 0\n"
        "v_permlane16_swap_b32 v44, v46\n"
        "v_permlane16_swap_b32 v45, v47\n"
        "buffer_store_dwordx4 v[44:47], v7, s[40:43], s83 offen offset:0 sc1\n"
        "v_mov_b32 v5, s74\n"
        "s_mov_b32 s84, s60\n"
        "buffer_store_dword v5, v3, s[40:43], s89 offen sc1\n"
        "buffer_store_dword v5, v3, s[40:43], s89 offen sc1\n"
        "s_waitcnt vmcnt(5)\n"
        "s_branch .Lf1_go0_%=\n"
        ".Lf1_i02_%=:\n"
        "v_mfma_f32_16x16x32_bf16 v[40:43], v[200:203], v[8:11], v[40:43]\n"
        "buffer_load_dword v4, v2, s[40:43], s86 offen sc1\n"
        "v_mfma_f32_16x16x32_bf16 v[44:47], v[204:207], v[8:11], v[44:47]\n"
        "s_waitcnt lgkmcnt(1)\n"
        "v_mfma_f32_16x16x32_bf16 v[24:27], v[208:211], v[12:15], v[24:27]\n"
        "s_cmp_le_u32 s88, 0\n"
        "s_cselect_b32 s88, s73, s89\n"
        "s_cselect_b32 s73, s89, s73\n"
        "v_mfma_f32_16x16x32_bf16 v[28:31], v[212:215], v[12:15], v[28:31]\n"
        "buffer_store_dword v5, v3, s[40:43], s88 offen sc1\n"
        "v_mfma_f32_16x16x32_bf16 v[32:35], v[216:219], v[12:15], v[32:35]\n"
        "s_getreg_b32 s88, hwreg(HW_REG_IB_STS, 0, 4)\n"
        "v_mfma_f32_16x16x32_bf16 v[36:39], v[220:223], v[12:15], v[36:39]\n"
        "s_cmp_le_u32 s88, 2\n"
        "s_cselect_b32 s88, s73, s89\n"
        "s_cselect_b32 s73, s89, s73\n"
        "v_mfma_f32_16x16x32_bf16 v[40:43], v[224:227], v[12:15], v[40:43]\n"
        "buffer_store_dword v5, v3, s[40:43], s88 offen sc1\n"
        "s_add_u32 s74, s74, 1\n"
        "v_mfma_f32_16x16x32_bf16 v[44:47], v[228:231], v[12:15], v[44:47]\n"
        "s_waitcnt lgkmcnt(0)\n"
        "s_mov_b32 s84, s73\n"
        "v_mfma_f32_16x16x32_bf16 v[24:27], v[232:235], v[16:19], v[24:27]\n"
        "s_mov_b32 s73, s60\n"
        "s_add_u32 s68, s68, 1\n"
        "s_lshr_b32 s66, s68, 1\n"
        "v_mfma_f32_16x16x32_bf16 v[28:31], v[236:239], v[16:19], v[28:31]\n"
        "s_bitcmp1_b32 s54, s66\n"
        "s_cselect_b32 s66, 8192, 0\n"
        "s_lshl_b32 s75, s68, 20\n"
        "v_mfma_f32_16x16x32_bf16 v[32:35], v[240:243], v[16:19], v[32:35]\n"
        "s_add_u32 s75, s75, s66\n"
        "s_add_u32 s72, s75, s59\n"
        "s_add_u32 s83, s75, s63\n"
        "v_mfma_f32_16x16x32_bf16 v[36:39], v[244:247], v[16:19], v[36:39]\n"
        "v_mfma_f32_16x16x32_bf16 v[40:43], v[248:251], v[16:19], v[40:43]\n"
        "v_mfma_f32_16x16x32_bf16 v[44:47], v[252:255], v[16:19], v[44:47]\n"
        "v_mul_f32 v72, s80, v24\n"
        "v_mul_f32 v73, s80, v25\n"
        "v_mul_f32 v74, s80, v26\n"
        "v_mul_f32 v75, s80, v27\n"
        "v_exp_f32 v72, v72\n"
        "v_exp_f32 v73, v73\n"
        "v_exp_f32 v74, v74\n"
        "v_exp_f32 v75, v75\n"
        "v_mul_f32 v24, v24, v28\n"
        "v_mul_f32 v25, v25, v29\n"
        "v_mul_f32 v26, v26, v30\n"
        "v_mul_f32 v27, v27, v31\n"
        "v_add_f32 v72, 1.0, v72\n"
        "v_add_f32 v73, 1.0, v73\n"
        "v_add_f32 v74, 1.0, v74\n"
        "v_add_f32 v75, 1.0, v75\n"
        "v_rcp_f32 v72, v72\n"
        "v_rcp_f32 v73, v73\n"
        "v_rcp_f32 v74, v74\n"
        "v_rcp_f32 v75, v75\n"
        "v_mul_f32 v24, v24, v72\n"
        "v_mul_f32 v25, v25, v73\n"
        "v_mul_f32 v26, v26, v74\n"
        "v_mul_f32 v27, v27, v75\n"
        "v_cvt_pk_bf16_f32 v28, v24, v25\n"
        "v_cvt_pk_bf16_f32 v29, v26, v27\n"
        "s_waitcnt vmcnt(3)\n"
        "buffer_store_dword v5, v3, s[40:43], s84 offen sc1\n"
        "s_mov_b32 s84, s89\n"
        "v_mul_f32 v76, s80, v32\n"
        "v_mul_f32 v77, s80, v33\n"
        "v_mul_f32 v78, s80, v34\n"
        "v_mul_f32 v79, s80, v35\n"
        "v_exp_f32 v76, v76\n"
        "v_exp_f32 v77, v77\n"
        "v_exp_f32 v78, v78\n"
        "v_exp_f32 v79, v79\n"
        "v_mul_f32 v32, v32, v36\n"
        "v_mul_f32 v33, v33, v37\n"
        "v_mul_f32 v34, v34, v38\n"
        "v_mul_f32 v35, v35, v39\n"
        "v_add_f32 v76, 1.0, v76\n"
        "v_add_f32 v77, 1.0, v77\n"
        "v_add_f32 v78, 1.0, v78\n"
        "v_add_f32 v79, 1.0, v79\n"
        "v_rcp_f32 v76, v76\n"
        "v_rcp_f32 v77, v77\n"
        "v_rcp_f32 v78, v78\n"
        "v_rcp_f32 v79, v79\n"
        "v_mul_f32 v32, v32, v76\n"
        "v_mul_f32 v33, v33, v77\n"
        "v_mul_f32 v34, v34, v78\n"
        "v_mul_f32 v35, v35, v79\n"
        "v_cvt_pk_bf16_f32 v30, v32, v33\n"
        "v_cvt_pk_bf16_f32 v31, v34, v35\n"
        "buffer_store_dwordx4 v[28:31], v1, s[40:43], s72 offen offset:0 sc1\n"
        "v_mul_f32 v72, s80, v40\n"
        "v_mul_f32 v73, s80, v41\n"
        "v_mul_f32 v74, s80, v42\n"
        "v_mul_f32 v75, s80, v43\n"
        "v_exp_f32 v72, v72\n"
        "v_exp_f32 v73, v73\n"
        "v_exp_f32 v74, v74\n"
        "v_exp_f32 v75, v75\n"
        "v_mul_f32 v40, v40, v44\n"
        "v_mul_f32 v41, v41, v45\n"
        "v_mul_f32 v42, v42, v46\n"
        "v_mul_f32 v43, v43, v47\n"
        "v_add_f32 v72, 1.0, v72\n"
        "v_add_f32 v73, 1.0, v73\n"
        "v_add_f32 v74, 1.0, v74\n"
        "v_add_f32 v75, 1.0, v75\n"
        "v_rcp_f32 v72, v72\n"
        "v_rcp_f32 v73, v73\n"
        "v_rcp_f32 v74, v74\n"
        "v_rcp_f32 v75, v75\n"
        "v_mul_f32 v40, v40, v72\n"
        "v_mul_f32 v41, v41, v73\n"
        "v_mul_f32 v42, v42, v74\n"
        "v_mul_f32 v43, v43, v75\n"
        "v_cvt_pk_bf16_f32 v44, v40, v41\n"
        "v_cvt_pk_bf16_f32 v45, v42, v43\n"
        "s_nop 0\n"
        "v_permlane16_swap_b32 v44, v46\n"
        "v_permlane16_swap_b32 v45, v47\n"
        "buffer_store_dwordx4 v[44:47], v7, s[40:43], s83 offen offset:0 sc1\n"
        "s_waitcnt vmcnt(5)\n"
        "v_cmp_gt_i32 vcc, s70, v4\n"
        "s_nop 0\n"
        "s_and_b64 vcc, vcc, 1\n"
        "s_cbranch_vccnz .Lf1_late0_%=\n"
        "buffer_load_dwordx4 v1, s[40:43], s71 offen offset:0 sc1 lds\n"
        "buffer_load_dwordx4 v1, s[40:43], s71 offen offset:1024 sc1 lds\n"
        "buffer_load_dwordx4 v1, s[40:43], s71 offen offset:2048 sc1 lds\n"
        "buffer_load_dwordx4 v1, s[40:43], s71 offen offset:3072 sc1 lds\n"
        "s_add_u32 m0, s78, s85\n"
        "s_add_u32 s69, s69, 1\n"
        "buffer_load_dwordx4 v1, s[40:43], s81 offen offset:0 sc1 lds\n"
        "s_waitcnt vmcnt(5)\n"
        "v_mov_b32 v5, s74\n"
        "buffer_store_dword v5, v3, s[40:43], s73 offen sc1\n"
        "s_waitcnt vmcnt(1)\n"
        "s_branch .Lf1_go0_%=\n"
        ".Lf1_late0_%=:\n"
        "s_waitcnt vmcnt(0)\n"
        "v_mov_b32 v5, s74\n"
        "buffer_store_dword v5, v3, s[40:43], s73 offen sc1\n"
        "s_mov_b32 s64, 0\n"
        ".Lf1_in0_top_%=:\n"
        "buffer_load_dword v4, v2, s[40:43], s86 offen sc1\n"
        "s_waitcnt vmcnt(0)\n"
        "v_cmp_gt_i32 vcc, s70, v4\n"
        "s_nop 0\n"
        "s_and_b64 vcc, vcc, 1\n"
        "s_cbranch_vccz .Lf1_in0_ok_%=\n"
        "s_add_u32 s64, s64, 1\n"
        "s_and_b32 s65, s64, 255\n"
        "s_cbranch_scc1 .Lf1_in0_go_%=\n"
        "s_mov_b32 s65, 49152\n"
        "s_cmp_lt_u32 s64, 1048576\n"
        "s_cbranch_scc1 .Lf1_in0_chk_%=\n"
        "v_mov_b32 v4, 1\n"
        "buffer_store_dword v4, off, s[40:43], s65 sc1\n"
        "s_waitcnt vmcnt(0)\n"
        ".Lf1_in0_chk_%=:\n"
        "buffer_load_dword v4, off, s[40:43], s65 sc1\n"
        "s_waitcnt vmcnt(0)\n"
        "v_readfirstlane_b32 s65, v4\n"
        "s_nop 0\n"
        "s_cmp_lg_u32 s65, 1\n"
        "s_cbranch_scc1 .Lf1_in0_go_%=\n"
        "s_endpgm\n"
        ".Lf1_in0_go_%=:\n"
        "s_branch .Lf1_in0_top_%=\n"
        ".Lf1_in0_ok_%=:\n"
        "buffer_load_dwordx4 v1, s[40:43], s71 offen offset:0 sc1 lds\n"
        "buffer_load_dwordx4 v1, s[40:43], s71 offen offset:1024 sc1 lds\n"
        "buffer_load_dwordx4 v1, s[40:43], s71 offen offset:2048 sc1 lds\n"
        "buffer_load_dwordx4 v1, s[40:43], s71 offen offset:3072 sc1 lds\n"
        "s_add_u32 m0, s78, s85\n"
        "s_add_u32 s69, s69, 1\n"
        "buffer_load_dwordx4 v1, s[40:43], s81 offen offset:0 sc1 lds\n"
        "s_waitcnt vmcnt(0)\n"
        ".Lf1_go0_%=:\n"
        "s_mov_b32 s72, 241172480\n"
        "s_mov_b32 s83, 241172480\n"
        "s_mov_b32 s73, 241172480\n"
        "v_add_u32 v0, s78, v6\n"
        "s_barrier\n"
        "ds_read_b128 v[8:11], v0 offset:0\n"
        "ds_read_b128 v[12:15], v0 offset:512\n"
        "ds_read_b128 v[16:19], v0 offset:1024\n"
        "s_waitcnt lgkmcnt(2)\n"
        "s_branch .Lf1_i1_%=\n"
        ".Lf1_i1_%=:\n"
        "v_mfma_f32_16x16x32_bf16 v[48:51], a[0:3], v[8:11], 0\n"
        "s_add_u32 s86, s76, 0\n"
        "buffer_load_dword v4, v2, s[40:43], s86 offen sc1\n"
        "v_mfma_f32_16x16x32_bf16 v[52:55], a[4:7], v[8:11], 0\n"
        "s_lshr_b32 s67, s69, 1\n"
        "v_mul_f32 v72, s80, v24\n"
        "v_mul_f32 v73, s80, v25\n"
        "v_mfma_f32_16x16x32_bf16 v[56:59], a[8:11], v[8:11], 0\n"
        "s_lshr_b32 s65, s67, 1\n"
        "v_mul_f32 v74, s80, v26\n"
        "v_mul_f32 v75, s80, v27\n"
        "v_mfma_f32_16x16x32_bf16 v[60:63], a[12:15], v[8:11], 0\n"
        "s_bitcmp1_b32 s54, s65\n"
        "s_cselect_b32 s65, 8192, 0\n"
        "v_mul_f32 v76, s80, v32\n"
        "v_mfma_f32_16x16x32_bf16 v[64:67], a[16:19], v[8:11], 0\n"
        "s_lshl_b32 s77, s67, 20\n"
        "v_exp_f32 v72, v72\n"
        "v_mfma_f32_16x16x32_bf16 v[68:71], a[20:23], v[8:11], 0\n"
        "s_waitcnt lgkmcnt(1)\n"
        "ds_read_b128 v[8:11], v0 offset:1536\n"
        "v_mul_f32 v77, s80, v33\n"
        "v_mfma_f32_16x16x32_bf16 v[48:51], a[24:27], v[12:15], v[48:51]\n"
        "s_add_u32 s77, s77, s65\n"
        "v_exp_f32 v73, v73\n"
        "v_mfma_f32_16x16x32_bf16 v[52:55], a[28:31], v[12:15], v[52:55]\n"
        "s_add_u32 s71, s77, s58\n"
        "v_exp_f32 v74, v74\n"
        "v_mfma_f32_16x16x32_bf16 v[56:59], a[32:35], v[12:15], v[56:59]\n"
        "s_add_u32 s81, s77, s62\n"
        "v_exp_f32 v75, v75\n"
        "v_mfma_f32_16x16x32_bf16 v[60:63], a[36:39], v[12:15], v[60:63]\n"
        "s_add_u32 s78, s78, 18432\n"
        "v_mul_f32 v24, v24, v28\n"
        "v_mul_f32 v25, v25, v29\n"
        "v_mfma_f32_16x16x32_bf16 v[64:67], a[40:43], v[12:15], v[64:67]\n"
        "s_cmp_eq_u32 s78, 55296\n"
        "s_cselect_b32 s78, 0, s78\n"
        "v_mul_f32 v26, v26, v30\n"
        "v_mfma_f32_16x16x32_bf16 v[68:71], a[44:47], v[12:15], v[68:71]\n"
        "s_waitcnt lgkmcnt(1)\n"
        "ds_read_b128 v[12:15], v0 offset:2048\n"
        "v_mul_f32 v27, v27, v31\n"
        "v_mfma_f32_16x16x32_bf16 v[48:51], a[48:51], v[16:19], v[48:51]\n"
        "s_add_u32 m0, s78, s61\n"
        "v_add_f32 v72, 1.0, v72\n"
        "v_add_f32 v73, 1.0, v73\n"
        "v_mfma_f32_16x16x32_bf16 v[52:55], a[52:55], v[16:19], v[52:55]\n"
        "s_lshr_b32 s70, s69, 1\n"
        "v_add_f32 v74, 1.0, v74\n"
        "v_add_f32 v75, 1.0, v75\n"
        "v_mfma_f32_16x16x32_bf16 v[56:59], a[56:59], v[16:19], v[56:59]\n"
        "s_waitcnt vmcnt(3)\n"
        "v_rcp_f32 v72, v72\n"
        "v_mfma_f32_16x16x32_bf16 v[60:63], a[60:63], v[16:19], v[60:63]\n"
        "buffer_store_dword v5, v3, s[40:43], s84 offen sc1\n"
        "v_mul_f32 v78, s80, v34\n"
        "v_mfma_f32_16x16x32_bf16 v[64:67], a[64:67], v[16:19], v[64:67]\n"
        "v_rcp_f32 v73, v73\n"
        "v_mfma_f32_16x16x32_bf16 v[68:71], a[68:71], v[16:19], v[68:71]\n"
        "s_waitcnt lgkmcnt(1)\n"
        "ds_read_b128 v[16:19], v0 offset:2560\n"
        "v_mul_f32 v79, s80, v35\n"
        "v_mfma_f32_16x16x32_bf16 v[48:51], a[72:75], v[8:11], v[48:51]\n"
        "v_rcp_f32 v74, v74\n"
        "v_mfma_f32_16x16x32_bf16 v[52:55], a[76:79], v[8:11], v[52:55]\n"
        "v_rcp_f32 v75, v75\n"
        "v_mfma_f32_16x16x32_bf16 v[56:59], a[80:83], v[8:11], v[56:59]\n"
        "v_mul_f32 v24, v24, v72\n"
        "v_mul_f32 v25, v25, v73\n"
        "v_mfma_f32_16x16x32_bf16 v[60:63], a[84:87], v[8:11], v[60:63]\n"
        "v_mul_f32 v26, v26, v74\n"
        "v_mul_f32 v27, v27, v75\n"
        "v_mfma_f32_16x16x32_bf16 v[64:67], a[88:91], v[8:11], v[64:67]\n"
        "v_cvt_pk_bf16_f32 v28, v24, v25\n"
        "v_cvt_pk_bf16_f32 v29, v26, v27\n"
        "v_mfma_f32_16x16x32_bf16 v[68:71], a[92:95], v[8:11], v[68:71]\n"
        "s_waitcnt lgkmcnt(1)\n"
        "ds_read_b128 v[8:11], v0 offset:3072\n"
        "v_mul_f32 v72, s80, v40\n"
        "v_mfma_f32_16x16x32_bf16 v[48:51], a[96:99], v[12:15], v[48:51]\n"
        "v_exp_f32 v76, v76\n"
        "v_mfma_f32_16x16x32_bf16 v[52:55], a[100:103], v[12:15], v[52:55]\n"
        "v_exp_f32 v77, v77\n"
        "v_mfma_f32_16x16x32_bf16 v[56:59], a[104:107], v[12:15], v[56:59]\n"
        "v_exp_f32 v78, v78\n"
        "v_mfma_f32_16x16x32_bf16 v[60:63], a[108:111], v[12:15], v[60:63]\n"
        "v_exp_f32 v79, v79\n"
        "v_mfma_f32_16x16x32_bf16 v[64:67], a[112:115], v[12:15], v[64:67]\n"
        "s_waitcnt vmcnt(1)\n"
        "v_cmp_gt_i32 vcc, s70, v4\n"
        "v_mul_f32 v32, v32, v36\n"
        "v_mfma_f32_16x16x32_bf16 v[68:71], a[116:119], v[12:15], v[68:71]\n"
        "s_waitcnt lgkmcnt(1)\n"
        "ds_read_b128 v[12:15], v0 offset:3584\n"
        "v_mul_f32 v33, v33, v37\n"
        "v_mfma_f32_16x16x32_bf16 v[48:51], a[120:123], v[16:19], v[48:51]\n"
        "v_mul_f32 v34, v34, v38\n"
        "s_and_b64 vcc, vcc, 1\n"
        "v_mfma_f32_16x16x32_bf16 v[52:55], a[124:127], v[16:19], v[52:55]\n"
        "s_cbranch_vccnz .Lf1_i10_%=\n"
        "v_mfma_f32_16x16x32_bf16 v[56:59], a[128:131], v[16:19], v[56:59]\n"
        "v_mul_f32 v35, v35, v39\n"
        "buffer_load_dwordx4 v1, s[40:43], s71 offen offset:0 sc1 lds\n"
        "v_mfma_f32_16x16x32_bf16 v[60:63], a[132:135], v[16:19], v[60:63]\n"
        "v_add_f32 v76, 1.0, v76\n"
        "v_add_f32 v77, 1.0, v77\n"
        "v_mfma_f32_16x16x32_bf16 v[64:67], a[136:139], v[16:19], v[64:67]\n"
        "v_add_f32 v78, 1.0, v78\n"
        "v_add_f32 v79, 1.0, v79\n"
        "v_mfma_f32_16x16x32_bf16 v[68:71], a[140:143], v[16:19], v[68:71]\n"
        "s_waitcnt lgkmcnt(1)\n"
        "ds_read_b128 v[16:19], v0 offset:4096\n"
        "v_mul_f32 v73, s80, v41\n"
        "v_mfma_f32_16x16x32_bf16 v[48:51], a[144:147], v[8:11], v[48:51]\n"
        "v_rcp_f32 v76, v76\n"
        "buffer_load_dwordx4 v1, s[40:43], s71 offen offset:1024 sc1 lds\n"
        "v_mfma_f32_16x16x32_bf16 v[52:55], a[148:151], v[8:11], v[52:55]\n"
        "v_rcp_f32 v77, v77\n"
        "v_mfma_f32_16x16x32_bf16 v[56:59], a[152:155], v[8:11], v[56:59]\n"
        "v_rcp_f32 v78, v78\n"
        "v_mfma_f32_16x16x32_bf16 v[60:63], a[156:159], v[8:11], v[60:63]\n"
        "v_rcp_f32 v79, v79\n"
        "v_mfma_f32_16x16x32_bf16 v[64:67], a[160:163], v[8:11], v[64:67]\n"
        "v_mul_f32 v32, v32, v76\n"
        "v_mul_f32 v33, v33, v77\n"
        "buffer_load_dwordx4 v1, s[40:43], s71 offen offset:2048 sc1 lds\n"
        "v_mfma_f32_16x16x32_bf16 v[68:71], a[164:167], v[8:11], v[68:71]\n"
        "s_waitcnt lgkmcnt(1)\n"
        "ds_read_b128 v[8:11], v0 offset:4608\n"
        "v_mul_f32 v34, v34, v78\n"
        "v_mfma_f32_16x16x32_bf16 v[48:51], a[168:171], v[12:15], v[48:51]\n"
        "v_mul_f32 v35, v35, v79\n"
        "v_cvt_pk_bf16_f32 v30, v32, v33\n"
        "v_mfma_f32_16x16x32_bf16 v[52:55], a[172:175], v[12:15], v[52:55]\n"
        "v_cvt_pk_bf16_f32 v31, v34, v35\n"
        "v_mul_f32 v74, s80, v42\n"
        "v_mfma_f32_16x16x32_bf16 v[56:59], a[176:179], v[12:15], v[56:59]\n"
        "buffer_store_dwordx4 v[28:31], v1, s[40:43], s72 offen offset:0 sc1\n"
        "v_mfma_f32_16x16x32_bf16 v[60:63], a[180:183], v[12:15], v[60:63]\n"
        "v_mul_f32 v75, s80, v43\n"
        "buffer_load_dwordx4 v1, s[40:43], s71 offen offset:3072 sc1 lds\n"
        "v_mfma_f32_16x16x32_bf16 v[64:67], a[184:187], v[12:15], v[64:67]\n"
        "s_add_u32 m0, s78, s85\n"
        "v_exp_f32 v72, v72\n"
        "v_mfma_f32_16x16x32_bf16 v[68:71], a[188:191], v[12:15], v[68:71]\n"
        "s_waitcnt lgkmcnt(1)\n"
        "ds_read_b128 v[12:15], v0 offset:5120\n"
        "s_add_u32 s69, s69, 1\n"
        "v_mfma_f32_16x16x32_bf16 v[48:51], a[192:195], v[16:19], v[48:51]\n"
        "v_exp_f32 v73, v73\n"
        "buffer_load_dwordx4 v1, s[40:43], s81 offen offset:0 sc1 lds\n"
        "v_mfma_f32_16x16x32_bf16 v[52:55], a[196:199], v[16:19], v[52:55]\n"
        "v_exp_f32 v74, v74\n"
        "v_mfma_f32_16x16x32_bf16 v[56:59], a[200:203], v[16:19], v[56:59]\n"
        "v_exp_f32 v75, v75\n"
        "v_mfma_f32_16x16x32_bf16 v[60:63], a[204:207], v[16:19], v[60:63]\n"
        "v_mul_f32 v40, v40, v44\n"
        "v_mul_f32 v41, v41, v45\n"
        "v_mfma_f32_16x16x32_bf16 v[64:67], a[208:211], v[16:19], v[64:67]\n"
        "v_mul_f32 v42, v42, v46\n"
        "v_mul_f32 v43, v43, v47\n"
        "v_mfma_f32_16x16x32_bf16 v[68:71], a[212:215], v[16:19], v[68:71]\n"
        "s_waitcnt lgkmcnt(1)\n"
        "ds_read_b128 v[16:19], v0 offset:5632\n"
        "v_add_f32 v72, 1.0, v72\n"
        "v_mfma_f32_16x16x32_bf16 v[48:51], a[216:219], v[8:11], v[48:51]\n"
        "v_add_f32 v73, 1.0, v73\n"
        "v_add_f32 v74, 1.0, v74\n"
        "v_mfma_f32_16x16x32_bf16 v[52:55], a[220:223], v[8:11], v[52:55]\n"
        "v_add_f32 v75, 1.0, v75\n"
        "v_mfma_f32_16x16x32_bf16 v[56:59], a[224:227], v[8:11], v[56:59]\n"
        "v_rcp_f32 v72, v72\n"
        "v_mfma_f32_16x16x32_bf16 v[60:63], a[228:231], v[8:11], v[60:63]\n"
        "v_rcp_f32 v73, v73\n"
        "v_mfma_f32_16x16x32_bf16 v[64:67], a[232:235], v[8:11], v[64:67]\n"
        "v_rcp_f32 v74, v74\n"
        "v_mfma_f32_16x16x32_bf16 v[68:71], a[236:239], v[8:11], v[68:71]\n"
        "s_waitcnt lgkmcnt(1)\n"
        "ds_read_b128 v[8:11], v0 offset:6144\n"
        "v_mfma_f32_16x16x32_bf16 v[48:51], a[240:243], v[12:15], v[48:51]\n"
        "v_rcp_f32 v75, v75\n"
        "v_mfma_f32_16x16x32_bf16 v[52:55], a[244:247], v[12:15], v[52:55]\n"
        "v_mul_f32 v40, v40, v72\n"
        "v_mfma_f32_16x16x32_bf16 v[56:59], a[248:251], v[12:15], v[56:59]\n"
        "v_mfma_f32_16x16x32_bf16 v[60:63], a[252:255], v[12:15], v[60:63]\n"
        "v_mul_f32 v41, v41, v73\n"
        "v_mfma_f32_16x16x32_bf16 v[64:67], v[80:83], v[12:15], v[64:67]\n"
        "v_mul_f32 v42, v42, v74\n"
        "v_mul_f32 v43, v43, v75\n"
        "v_mfma_f32_16x16x32_bf16 v[68:71], v[84:87], v[12:15], v[68:71]\n"
        "s_waitcnt lgkmcnt(1)\n"
        "ds_read_b128 v[12:15], v0 offset:6656\n"
        "v_cvt_pk_bf16_f32 v44, v40, v41\n"
        "v_mfma_f32_16x16x32_bf16 v[48:51], v[88:91], v[16:19], v[48:51]\n"
        "v_cvt_pk_bf16_f32 v45, v42, v43\n"
        "s_nop 0\n"
        "v_mfma_f32_16x16x32_bf16 v[52:55], v[92:95], v[16:19], v[52:55]\n"
        "v_permlane16_swap_b32 v44, v46\n"
        "v_mfma_f32_16x16x32_bf16 v[56:59], v[96:99], v[16:19], v[56:59]\n"
        "v_permlane16_swap_b32 v45, v47\n"
        "v_mfma_f32_16x16x32_bf16 v[60:63], v[100:103], v[16:19], v[60:63]\n"
        "buffer_store_dwordx4 v[44:47], v7, s[40:43], s83 offen offset:0 sc1\n"
        "v_mfma_f32_16x16x32_bf16 v[64:67], v[104:107], v[16:19], v[64:67]\n"
        "v_mov_b32 v5, s74\n"
        "v_mfma_f32_16x16x32_bf16 v[68:71], v[108:111], v[16:19], v[68:71]\n"
        "s_waitcnt lgkmcnt(1)\n"
        "ds_read_b128 v[16:19], v0 offset:7168\n"
        "v_mfma_f32_16x16x32_bf16 v[48:51], v[112:115], v[8:11], v[48:51]\n"
        "v_mfma_f32_16x16x32_bf16 v[52:55], v[116:119], v[8:11], v[52:55]\n"
        "v_mfma_f32_16x16x32_bf16 v[56:59], v[120:123], v[8:11], v[56:59]\n"
        "v_mfma_f32_16x16x32_bf16 v[60:63], v[124:127], v[8:11], v[60:63]\n"
        "v_mfma_f32_16x16x32_bf16 v[64:67], v[128:131], v[8:11], v[64:67]\n"
        "v_mfma_f32_16x16x32_bf16 v[68:71], v[132:135], v[8:11], v[68:71]\n"
        "s_waitcnt lgkmcnt(1)\n"
        "ds_read_b128 v[8:11], v0 offset:7680\n"
        "v_mfma_f32_16x16x32_bf16 v[48:51], v[136:139], v[12:15], v[48:51]\n"
        "v_mfma_f32_16x16x32_bf16 v[52:55], v[140:143], v[12:15], v[52:55]\n"
        "v_mfma_f32_16x16x32_bf16 v[56:59], v[144:147], v[12:15], v[56:59]\n"
        "v_mfma_f32_16x16x32_bf16 v[60:63], v[148:151], v[12:15], v[60:63]\n"
        "v_mfma_f32_16x16x32_bf16 v[64:67], v[152:155], v[12:15], v[64:67]\n"
        "v_mfma_f32_16x16x32_bf16 v[68:71], v[156:159], v[12:15], v[68:71]\n"
        "s_waitcnt lgkmcnt(1)\n"
        "ds_read_b128 v[12:15], v0 offset:8192\n"
        "v_mfma_f32_16x16x32_bf16 v[48:51], v[160:163], v[16:19], v[48:51]\n"
        "v_mfma_f32_16x16x32_bf16 v[52:55], v[164:167], v[16:19], v[52:55]\n"
        "v_mfma_f32_16x16x32_bf16 v[56:59], v[168:171], v[16:19], v[56:59]\n"
        "v_mfma_f32_16x16x32_bf16 v[60:63], v[172:175], v[16:19], v[60:63]\n"
        "v_mfma_f32_16x16x32_bf16 v[64:67], v[176:179], v[16:19], v[64:67]\n"
        "v_mfma_f32_16x16x32_bf16 v[68:71], v[180:183], v[16:19], v[68:71]\n"
        "s_waitcnt lgkmcnt(1)\n"
        "ds_read_b128 v[16:19], v0 offset:8704\n"
        "v_mfma_f32_16x16x32_bf16 v[48:51], v[184:187], v[8:11], v[48:51]\n"
        "v_mfma_f32_16x16x32_bf16 v[52:55], v[188:191], v[8:11], v[52:55]\n"
        "s_getreg_b32 s88, hwreg(HW_REG_IB_STS, 0, 4)\n"
        "v_mfma_f32_16x16x32_bf16 v[56:59], v[192:195], v[8:11], v[56:59]\n"
        "v_mfma_f32_16x16x32_bf16 v[60:63], v[196:199], v[8:11], v[60:63]\n"
        "v_mfma_f32_16x16x32_bf16 v[64:67], v[200:203], v[8:11], v[64:67]\n"
        "s_waitcnt vmcnt(1)\n"
        "s_barrier\n"
        "v_add_u32 v0, s78, v6\n"
        "v_mfma_f32_16x16x32_bf16 v[68:71], v[204:207], v[8:11], v[68:71]\n"
        "s_waitcnt lgkmcnt(1)\n"
        "ds_read_b128 v[8:11], v0 offset:0\n"
        "v_mfma_f32_16x16x32_bf16 v[48:51], v[208:211], v[12:15], v[48:51]\n"
        "s_cmp_le_u32 s88, 0\n"
        "s_cselect_b32 s88, s73, s89\n"
        "s_cselect_b32 s73, s89, s73\n"
        "v_mfma_f32_16x16x32_bf16 v[52:55], v[212:215], v[12:15], v[52:55]\n"
        "buffer_store_dword v5, v3, s[40:43], s88 offen sc1\n"
        "v_mfma_f32_16x16x32_bf16 v[56:59], v[216:219], v[12:15], v[56:59]\n"
        "s_getreg_b32 s88, hwreg(HW_REG_IB_STS, 0, 4)\n"
        "v_mfma_f32_16x16x32_bf16 v[60:63], v[220:223], v[12:15], v[60:63]\n"
        "s_cmp_le_u32 s88, 1\n"
        "s_cselect_b32 s88, s73, s89\n"
        "s_cselect_b32 s73, s89, s73\n"
        "v_mfma_f32_16x16x32_bf16 v[64:67], v[224:227], v[12:15], v[64:67]\n"
        "buffer_store_dword v5, v3, s[40:43], s88 offen sc1\n"
        "s_add_u32 s74, s74, 1\n"
        "v_mfma_f32_16x16x32_bf16 v[68:71], v[228:231], v[12:15], v[68:71]\n"
        "s_waitcnt lgkmcnt(1)\n"
        "ds_read_b128 v[12:15], v0 offset:512\n"
        "s_mov_b32 s84, s73\n"
        "v_mfma_f32_16x16x32_bf16 v[48:51], v[232:235], v[16:19], v[48:51]\n"
        "s_mov_b32 s73, s60\n"
        "s_add_u32 s72, s75, s59\n"
        "s_add_u32 s83, s75, s63\n"
        "v_mfma_f32_16x16x32_bf16 v[52:55], v[236:239], v[16:19], v[52:55]\n"
        "s_add_u32 s72, s72, 49152\n"
        "s_add_u32 s83, s83, 4096\n"
        "v_mfma_f32_16x16x32_bf16 v[56:59], v[240:243], v[16:19], v[56:59]\n"
        "v_mfma_f32_16x16x32_bf16 v[60:63], v[244:247], v[16:19], v[60:63]\n"
        "v_mfma_f32_16x16x32_bf16 v[64:67], v[248:251], v[16:19], v[64:67]\n"
        "v_mfma_f32_16x16x32_bf16 v[68:71], v[252:255], v[16:19], v[68:71]\n"
        "s_waitcnt lgkmcnt(1)\n"
        "ds_read_b128 v[16:19], v0 offset:1024\n"
        "s_branch .Lf1_i0_%=\n"
        ".Lf1_i10_%=:\n"
        "v_mfma_f32_16x16x32_bf16 v[56:59], a[128:131], v[16:19], v[56:59]\n"
        "v_mul_f32 v35, v35, v39\n"
        "buffer_load_dword v4, v2, s[40:43], s86 offen sc1\n"
        "v_mfma_f32_16x16x32_bf16 v[60:63], a[132:135], v[16:19], v[60:63]\n"
        "v_add_f32 v76, 1.0, v76\n"
        "v_add_f32 v77, 1.0, v77\n"
        "v_mfma_f32_16x16x32_bf16 v[64:67], a[136:139], v[16:19], v[64:67]\n"
        "v_add_f32 v78, 1.0, v78\n"
        "v_add_f32 v79, 1.0, v79\n"
        "v_mfma_f32_16x16x32_bf16 v[68:71], a[140:143], v[16:19], v[68:71]\n"
        "s_waitcnt lgkmcnt(1)\n"
        "ds_read_b128 v[16:19], v0 offset:4096\n"
        "v_mul_f32 v73, s80, v41\n"
        "v_mfma_f32_16x16x32_bf16 v[48:51], a[144:147], v[8:11], v[48:51]\n"
        "v_rcp_f32 v76, v76\n"
        "v_mfma_f32_16x16x32_bf16 v[52:55], a[148:151], v[8:11], v[52:55]\n"
        "v_rcp_f32 v77, v77\n"
        "v_mfma_f32_16x16x32_bf16 v[56:59], a[152:155], v[8:11], v[56:59]\n"
        "v_rcp_f32 v78, v78\n"
        "v_mfma_f32_16x16x32_bf16 v[60:63], a[156:159], v[8:11], v[60:63]\n"
        "v_rcp_f32 v79, v79\n"
        "v_mfma_f32_16x16x32_bf16 v[64:67], a[160:163], v[8:11], v[64:67]\n"
        "v_mul_f32 v32, v32, v76\n"
        "v_mul_f32 v33, v33, v77\n"
        "v_mfma_f32_16x16x32_bf16 v[68:71], a[164:167], v[8:11], v[68:71]\n"
        "s_waitcnt lgkmcnt(1)\n"
        "ds_read_b128 v[8:11], v0 offset:4608\n"
        "v_mul_f32 v34, v34, v78\n"
        "v_mfma_f32_16x16x32_bf16 v[48:51], a[168:171], v[12:15], v[48:51]\n"
        "v_mul_f32 v35, v35, v79\n"
        "v_cvt_pk_bf16_f32 v30, v32, v33\n"
        "v_mfma_f32_16x16x32_bf16 v[52:55], a[172:175], v[12:15], v[52:55]\n"
        "v_cvt_pk_bf16_f32 v31, v34, v35\n"
        "v_mul_f32 v74, s80, v42\n"
        "v_mfma_f32_16x16x32_bf16 v[56:59], a[176:179], v[12:15], v[56:59]\n"
        "buffer_store_dwordx4 v[28:31], v1, s[40:43], s72 offen offset:0 sc1\n"
        "v_mfma_f32_16x16x32_bf16 v[60:63], a[180:183], v[12:15], v[60:63]\n"
        "v_mul_f32 v75, s80, v43\n"
        "v_mfma_f32_16x16x32_bf16 v[64:67], a[184:187], v[12:15], v[64:67]\n"
        "v_exp_f32 v72, v72\n"
        "v_mfma_f32_16x16x32_bf16 v[68:71], a[188:191], v[12:15], v[68:71]\n"
        "s_waitcnt lgkmcnt(1)\n"
        "ds_read_b128 v[12:15], v0 offset:5120\n"
        "v_mfma_f32_16x16x32_bf16 v[48:51], a[192:195], v[16:19], v[48:51]\n"
        "v_exp_f32 v73, v73\n"
        "v_mfma_f32_16x16x32_bf16 v[52:55], a[196:199], v[16:19], v[52:55]\n"
        "v_exp_f32 v74, v74\n"
        "v_mfma_f32_16x16x32_bf16 v[56:59], a[200:203], v[16:19], v[56:59]\n"
        "v_exp_f32 v75, v75\n"
        "v_mfma_f32_16x16x32_bf16 v[60:63], a[204:207], v[16:19], v[60:63]\n"
        "v_mul_f32 v40, v40, v44\n"
        "v_mul_f32 v41, v41, v45\n"
        "v_mfma_f32_16x16x32_bf16 v[64:67], a[208:211], v[16:19], v[64:67]\n"
        "v_mul_f32 v42, v42, v46\n"
        "v_mul_f32 v43, v43, v47\n"
        "v_mfma_f32_16x16x32_bf16 v[68:71], a[212:215], v[16:19], v[68:71]\n"
        "s_waitcnt lgkmcnt(1)\n"
        "ds_read_b128 v[16:19], v0 offset:5632\n"
        "v_add_f32 v72, 1.0, v72\n"
        "v_mfma_f32_16x16x32_bf16 v[48:51], a[216:219], v[8:11], v[48:51]\n"
        "v_add_f32 v73, 1.0, v73\n"
        "v_add_f32 v74, 1.0, v74\n"
        "v_mfma_f32_16x16x32_bf16 v[52:55], a[220:223], v[8:11], v[52:55]\n"
        "v_add_f32 v75, 1.0, v75\n"
        "v_mfma_f32_16x16x32_bf16 v[56:59], a[224:227], v[8:11], v[56:59]\n"
        "v_rcp_f32 v72, v72\n"
        "v_mfma_f32_16x16x32_bf16 v[60:63], a[228:231], v[8:11], v[60:63]\n"
        "v_rcp_f32 v73, v73\n"
        "v_mfma_f32_16x16x32_bf16 v[64:67], a[232:235], v[8:11], v[64:67]\n"
        "v_rcp_f32 v74, v74\n"
        "v_mfma_f32_16x16x32_bf16 v[68:71], a[236:239], v[8:11], v[68:71]\n"
        "s_waitcnt vmcnt(1)\n"
        "v_cmp_gt_i32 vcc, s70, v4\n"
        "s_waitcnt lgkmcnt(1)\n"
        "ds_read_b128 v[8:11], v0 offset:6144\n"
        "v_mfma_f32_16x16x32_bf16 v[48:51], a[240:243], v[12:15], v[48:51]\n"
        "v_rcp_f32 v75, v75\n"
        "v_mfma_f32_16x16x32_bf16 v[52:55], a[244:247], v[12:15], v[52:55]\n"
        "v_mul_f32 v40, v40, v72\n"
        "s_and_b64 vcc, vcc, 1\n"
        "v_mfma_f32_16x16x32_bf16 v[56:59], a[248:251], v[12:15], v[56:59]\n"
        "s_cbranch_vccnz .Lf1_i11_%=\n"
        "v_mfma_f32_16x16x32_bf16 v[60:63], a[252:255], v[12:15], v[60:63]\n"
        "v_mul_f32 v41, v41, v73\n"
        "buffer_load_dwordx4 v1, s[40:43], s71 offen offset:0 sc1 lds\n"
        "v_mfma_f32_16x16x32_bf16 v[64:67], v[80:83], v[12:15], v[64:67]\n"
        "v_mul_f32 v42, v42, v74\n"
        "v_mul_f32 v43, v43, v75\n"
        "v_mfma_f32_16x16x32_bf16 v[68:71], v[84:87], v[12:15], v[68:71]\n"
        "s_waitcnt lgkmcnt(1)\n"
        "ds_read_b128 v[12:15], v0 offset:6656\n"
        "v_cvt_pk_bf16_f32 v44, v40, v41\n"
        "v_mfma_f32_16x16x32_bf16 v[48:51], v[88:91], v[16:19], v[48:51]\n"
        "v_cvt_pk_bf16_f32 v45, v42, v43\n"
        "s_nop 0\n"
        "buffer_load_dwordx4 v1, s[40:43], s71 offen offset:1024 sc1 lds\n"
        "v_mfma_f32_16x16x32_bf16 v[52:55], v[92:95], v[16:19], v[52:55]\n"
        "v_permlane16_swap_b32 v44, v46\n"
        "v_mfma_f32_16x16x32_bf16 v[56:59], v[96:99], v[16:19], v[56:59]\n"
        "v_permlane16_swap_b32 v45, v47\n"
        "v_mfma_f32_16x16x32_bf16 v[60:63], v[100:103], v[16:19], v[60:63]\n"
        "buffer_store_dwordx4 v[44:47], v7, s[40:43], s83 offen offset:0 sc1\n"
        "v_mfma_f32_16x16x32_bf16 v[64:67], v[104:107], v[16:19], v[64:67]\n"
        "v_mov_b32 v5, s74\n"
        "buffer_load_dwordx4 v1, s[40:43], s71 offen offset:2048 sc1 lds\n"
        "v_mfma_f32_16x16x32_bf16 v[68:71], v[108:111], v[16:19], v[68:71]\n"
        "s_waitcnt lgkmcnt(1)\n"
        "ds_read_b128 v[16:19], v0 offset:7168\n"
        "v_mfma_f32_16x16x32_bf16 v[48:51], v[112:115], v[8:11], v[48:51]\n"
        "buffer_load_dwordx4 v1, s[40:43], s71 offen offset:3072 sc1 lds\n"
        "s_add_u32 m0, s78, s85\n"
        "v_mfma_f32_16x16x32_bf16 v[52:55], v[116:119], v[8:11], v[52:55]\n"
        "s_add_u32 s69, s69, 1\n"
        "v_mfma_f32_16x16x32_bf16 v[56:59], v[120:123], v[8:11], v[56:59]\n"
        "v_mfma_f32_16x16x32_bf16 v[60:63], v[124:127], v[8:11], v[60:63]\n"
        "buffer_load_dwordx4 v1, s[40:43], s81 offen offset:0 sc1 lds\n"
        "v_mfma_f32_16x16x32_bf16 v[64:67], v[128:131], v[8:11], v[64:67]\n"
        "v_mfma_f32_16x16x32_bf16 v[68:71], v[132:135], v[8:11], v[68:71]\n"
        "s_waitcnt lgkmcnt(1)\n"
        "ds_read_b128 v[8:11], v0 offset:7680\n"
        "v_mfma_f32_16x16x32_bf16 v[48:51], v[136:139], v[12:15], v[48:51]\n"
        "v_mfma_f32_16x16x32_bf16 v[52:55], v[140:143], v[12:15], v[52:55]\n"
        "v_mfma_f32_16x16x32_bf16 v[56:59], v[144:147], v[12:15], v[56:59]\n"
        "v_mfma_f32_16x16x32_bf16 v[60:63], v[148:151], v[12:15], v[60:63]\n"
        "v_mfma_f32_16x16x32_bf16 v[64:67], v[152:155], v[12:15], v[64:67]\n"
        "v_mfma_f32_16x16x32_bf16 v[68:71], v[156:159], v[12:15], v[68:71]\n"
        "s_waitcnt lgkmcnt(1)\n"
        "ds_read_b128 v[12:15], v0 offset:8192\n"
        "v_mfma_f32_16x16x32_bf16 v[48:51], v[160:163], v[16:19], v[48:51]\n"
        "v_mfma_f32_16x16x32_bf16 v[52:55], v[164:167], v[16:19], v[52:55]\n"
        "v_mfma_f32_16x16x32_bf16 v[56:59], v[168:171], v[16:19], v[56:59]\n"
        "v_mfma_f32_16x16x32_bf16 v[60:63], v[172:175], v[16:19], v[60:63]\n"
        "v_mfma_f32_16x16x32_bf16 v[64:67], v[176:179], v[16:19], v[64:67]\n"
        "v_mfma_f32_16x16x32_bf16 v[68:71], v[180:183], v[16:19], v[68:71]\n"
        "s_waitcnt lgkmcnt(1)\n"
        "ds_read_b128 v[16:19], v0 offset:8704\n"
        "v_mfma_f32_16x16x32_bf16 v[48:51], v[184:187], v[8:11], v[48:51]\n"
        "v_mfma_f32_16x16x32_bf16 v[52:55], v[188:191], v[8:11], v[52:55]\n"
        "s_getreg_b32 s88, hwreg(HW_REG_IB_STS, 0, 4)\n"
        "v_mfma_f32_16x16x32_bf16 v[56:59], v[192:195], v[8:11], v[56:59]\n"
        "v_mfma_f32_16x16x32_bf16 v[60:63], v[196:199], v[8:11], v[60:63]\n"
        "v_mfma_f32_16x16x32_bf16 v[64:67], v[200:203], v[8:11], v[64:67]\n"
        "s_waitcnt vmcnt(0)\n"
        "s_barrier\n"
        "v_add_u32 v0, s78, v6\n"
        "v_mfma_f32_16x16x32_bf16 v[68:71], v[204:207], v[8:11], v[68:71]\n"
        "s_waitcnt lgkmcnt(1)\n"
        "ds_read_b128 v[8:11], v0 offset:0\n"
        "v_mfma_f32_16x16x32_bf16 v[48:51], v[208:211], v[12:15], v[48:51]\n"
        "s_cmp_le_u32 s88, 3\n"
        "s_cselect_b32 s88, s73, s89\n"
        "s_cselect_b32 s73, s89, s73\n"
        "v_mfma_f32_16x16x32_bf16 v[52:55], v[212:215], v[12:15], v[52:55]\n"
        "buffer_store_dword v5, v3, s[40:43], s88 offen sc1\n"
        "v_mfma_f32_16x16x32_bf16 v[56:59], v[216:219], v[12:15], v[56:59]\n"
        "s_getreg_b32 s88, hwreg(HW_REG_IB_STS, 0, 4)\n"
        "v_mfma_f32_16x16x32_bf16 v[60:63], v[220:223], v[12:15], v[60:63]\n"
        "s_cmp_le_u32 s88, 15\n"
        "s_cselect_b32 s88, s73, s89\n"
        "s_cselect_b32 s73, s89, s73\n"
        "v_mfma_f32_16x16x32_bf16 v[64:67], v[224:227], v[12:15], v[64:67]\n"
        "buffer_store_dword v5, v3, s[40:43], s88 offen sc1\n"
        "s_add_u32 s74, s74, 1\n"
        "v_mfma_f32_16x16x32_bf16 v[68:71], v[228:231], v[12:15], v[68:71]\n"
        "s_waitcnt lgkmcnt(1)\n"
        "ds_read_b128 v[12:15], v0 offset:512\n"
        "s_mov_b32 s84, s73\n"
        "v_mfma_f32_16x16x32_bf16 v[48:51], v[232:235], v[16:19], v[48:51]\n"
        "s_mov_b32 s73, s60\n"
        "s_add_u32 s72, s75, s59\n"
        "s_add_u32 s83, s75, s63\n"
        "v_mfma_f32_16x16x32_bf16 v[52:55], v[236:239], v[16:19], v[52:55]\n"
        "s_add_u32 s72, s72, 49152\n"
        "s_add_u32 s83, s83, 4096\n"
        "v_mfma_f32_16x16x32_bf16 v[56:59], v[240:243], v[16:19], v[56:59]\n"
        "v_mfma_f32_16x16x32_bf16 v[60:63], v[244:247], v[16:19], v[60:63]\n"
        "v_mfma_f32_16x16x32_bf16 v[64:67], v[248:251], v[16:19], v[64:67]\n"
        "v_mfma_f32_16x16x32_bf16 v[68:71], v[252:255], v[16:19], v[68:71]\n"
        "s_waitcnt lgkmcnt(1)\n"
        "ds_read_b128 v[16:19], v0 offset:1024\n"
        "s_branch .Lf1_i0_%=\n"
        ".Lf1_i11_%=:\n"
        "v_mfma_f32_16x16x32_bf16 v[60:63], a[252:255], v[12:15], v[60:63]\n"
        "v_mul_f32 v41, v41, v73\n"
        "buffer_load_dword v4, v2, s[40:43], s86 offen sc1\n"
        "v_mfma_f32_16x16x32_bf16 v[64:67], v[80:83], v[12:15], v[64:67]\n"
        "v_mul_f32 v42, v42, v74\n"
        "v_mul_f32 v43, v43, v75\n"
        "v_mfma_f32_16x16x32_bf16 v[68:71], v[84:87], v[12:15], v[68:71]\n"
        "s_waitcnt lgkmcnt(1)\n"
        "ds_read_b128 v[12:15], v0 offset:6656\n"
        "v_cvt_pk_bf16_f32 v44, v40, v41\n"
        "v_mfma_f32_16x16x32_bf16 v[48:51], v[88:91], v[16:19], v[48:51]\n"
        "v_cvt_pk_bf16_f32 v45, v42, v43\n"
        "s_nop 0\n"
        "v_mfma_f32_16x16x32_bf16 v[52:55], v[92:95], v[16:19], v[52:55]\n"
        "v_permlane16_swap_b32 v44, v46\n"
        "v_mfma_f32_16x16x32_bf16 v[56:59], v[96:99], v[16:19], v[56:59]\n"
        "v_permlane16_swap_b32 v45, v47\n"
        "v_mfma_f32_16x16x32_bf16 v[60:63], v[100:103], v[16:19], v[60:63]\n"
        "buffer_store_dwordx4 v[44:47], v7, s[40:43], s83 offen offset:0 sc1\n"
        "v_mfma_f32_16x16x32_bf16 v[64:67], v[104:107], v[16:19], v[64:67]\n"
        "v_mov_b32 v5, s74\n"
        "v_mfma_f32_16x16x32_bf16 v[68:71], v[108:111], v[16:19], v[68:71]\n"
        "s_waitcnt lgkmcnt(1)\n"
        "ds_read_b128 v[16:19], v0 offset:7168\n"
        "v_mfma_f32_16x16x32_bf16 v[48:51], v[112:115], v[8:11], v[48:51]\n"
        "v_mfma_f32_16x16x32_bf16 v[52:55], v[116:119], v[8:11], v[52:55]\n"
        "v_mfma_f32_16x16x32_bf16 v[56:59], v[120:123], v[8:11], v[56:59]\n"
        "v_mfma_f32_16x16x32_bf16 v[60:63], v[124:127], v[8:11], v[60:63]\n"
        "v_mfma_f32_16x16x32_bf16 v[64:67], v[128:131], v[8:11], v[64:67]\n"
        "v_mfma_f32_16x16x32_bf16 v[68:71], v[132:135], v[8:11], v[68:71]\n"
        "s_waitcnt lgkmcnt(1)\n"
        "ds_read_b128 v[8:11], v0 offset:7680\n"
        "v_mfma_f32_16x16x32_bf16 v[48:51], v[136:139], v[12:15], v[48:51]\n"
        "v_mfma_f32_16x16x32_bf16 v[52:55], v[140:143], v[12:15], v[52:55]\n"
        "v_mfma_f32_16x16x32_bf16 v[56:59], v[144:147], v[12:15], v[56:59]\n"
        "v_mfma_f32_16x16x32_bf16 v[60:63], v[148:151], v[12:15], v[60:63]\n"
        "v_mfma_f32_16x16x32_bf16 v[64:67], v[152:155], v[12:15], v[64:67]\n"
        "v_mfma_f32_16x16x32_bf16 v[68:71], v[156:159], v[12:15], v[68:71]\n"
        "s_waitcnt lgkmcnt(1)\n"
        "ds_read_b128 v[12:15], v0 offset:8192\n"
        "v_mfma_f32_16x16x32_bf16 v[48:51], v[160:163], v[16:19], v[48:51]\n"
        "v_mfma_f32_16x16x32_bf16 v[52:55], v[164:167], v[16:19], v[52:55]\n"
        "v_mfma_f32_16x16x32_bf16 v[56:59], v[168:171], v[16:19], v[56:59]\n"
        "v_mfma_f32_16x16x32_bf16 v[60:63], v[172:175], v[16:19], v[60:63]\n"
        "v_mfma_f32_16x16x32_bf16 v[64:67], v[176:179], v[16:19], v[64:67]\n"
        "v_mfma_f32_16x16x32_bf16 v[68:71], v[180:183], v[16:19], v[68:71]\n"
        "s_waitcnt lgkmcnt(1)\n"
        "ds_read_b128 v[16:19], v0 offset:8704\n"
        "v_mfma_f32_16x16x32_bf16 v[48:51], v[184:187], v[8:11], v[48:51]\n"
        "s_waitcnt vmcnt(1)\n"
        "v_cmp_gt_i32 vcc, s70, v4\n"
        "v_mfma_f32_16x16x32_bf16 v[52:55], v[188:191], v[8:11], v[52:55]\n"
        "s_getreg_b32 s88, hwreg(HW_REG_IB_STS, 0, 4)\n"
        "v_mfma_f32_16x16x32_bf16 v[56:59], v[192:195], v[8:11], v[56:59]\n"
        "s_and_b64 vcc, vcc, 1\n"
        "v_mfma_f32_16x16x32_bf16 v[60:63], v[196:199], v[8:11], v[60:63]\n"
        "s_cbranch_vccnz .Lf1_i12_%=\n"
        "v_mfma_f32_16x16x32_bf16 v[64:67], v[200:203], v[8:11], v[64:67]\n"
        "buffer_load_dwordx4 v1, s[40:43], s71 offen offset:0 sc1 lds\n"
        "v_mfma_f32_16x16x32_bf16 v[68:71], v[204:207], v[8:11], v[68:71]\n"
        "s_waitcnt lgkmcnt(1)\n"
        "v_mfma_f32_16x16x32_bf16 v[48:51], v[208:211], v[12:15], v[48:51]\n"
        "s_cmp_le_u32 s88, 0\n"
        "s_cselect_b32 s88, s73, s89\n"
        "s_cselect_b32 s73, s89, s73\n"
        "buffer_load_dwordx4 v1, s[40:43], s71 offen offset:1024 sc1 lds\n"
        "v_mfma_f32_16x16x32_bf16 v[52:55], v[212:215], v[12:15], v[52:55]\n"
        "buffer_store_dword v5, v3, s[40:43], s88 offen sc1\n"
        "v_mfma_f32_16x16x32_bf16 v[56:59], v[216:219], v[12:15], v[56:59]\n"
        "s_getreg_b32 s88, hwreg(HW_REG_IB_STS, 0, 4)\n"
        "buffer_load_dwordx4 v1, s[40:43], s71 offen offset:2048 sc1 lds\n"
        "v_mfma_f32_16x16x32_bf16 v[60:63], v[220:223], v[12:15], v[60:63]\n"
        "s_cmp_le_u32 s88, 3\n"
        "s_cselect_b32 s88, s73, s89\n"
        "s_cselect_b32 s73, s89, s73\n"
        "v_mfma_f32_16x16x32_bf16 v[64:67], v[224:227], v[12:15], v[64:67]\n"
        "buffer_store_dword v5, v3, s[40:43], s88 offen sc1\n"
        "s_add_u32 s74, s74, 1\n"
        "buffer_load_dwordx4 v1, s[40:43], s71 offen offset:3072 sc1 lds\n"
        "s_add_u32 m0, s78, s85\n"
        "s_add_u32 s69, s69, 1\n"
        "v_mfma_f32_16x16x32_bf16 v[68:71], v[228:231], v[12:15], v[68:71]\n"
        "s_waitcnt lgkmcnt(0)\n"
        "s_mov_b32 s84, s73\n"
        "v_mfma_f32_16x16x32_bf16 v[48:51], v[232:235], v[16:19], v[48:51]\n"
        "s_mov_b32 s73, s60\n"
        "s_add_u32 s72, s75, s59\n"
        "s_add_u32 s83, s75, s63\n"
        "buffer_load_dwordx4 v1, s[40:43], s81 offen offset:0 sc1 lds\n"
        "v_mfma_f32_16x16x32_bf16 v[52:55], v[236:239], v[16:19], v[52:55]\n"
        "s_add_u32 s72, s72, 49152\n"
        "s_add_u32 s83, s83, 4096\n"
        "v_mfma_f32_16x16x32_bf16 v[56:59], v[240:243], v[16:19], v[56:59]\n"
        "v_mfma_f32_16x16x32_bf16 v[60:63], v[244:247], v[16:19], v[60:63]\n"
        "v_mfma_f32_16x16x32_bf16 v[64:67], v[248:251], v[16:19], v[64:67]\n"
        "v_mfma_f32_16x16x32_bf16 v[68:71], v[252:255], v[16:19], v[68:71]\n"
        "v_mul_f32 v72, s80, v48\n"
        "v_mul_f32 v73, s80, v49\n"
        "v_mul_f32 v74, s80, v50\n"
        "v_mul_f32 v75, s80, v51\n"
        "v_exp_f32 v72, v72\n"
        "v_exp_f32 v73, v73\n"
        "v_exp_f32 v74, v74\n"
        "v_exp_f32 v75, v75\n"
        "v_mul_f32 v48, v48, v52\n"
        "v_mul_f32 v49, v49, v53\n"
        "v_mul_f32 v50, v50, v54\n"
        "v_mul_f32 v51, v51, v55\n"
        "v_add_f32 v72, 1.0, v72\n"
        "v_add_f32 v73, 1.0, v73\n"
        "v_add_f32 v74, 1.0, v74\n"
        "v_add_f32 v75, 1.0, v75\n"
        "v_rcp_f32 v72, v72\n"
        "v_rcp_f32 v73, v73\n"
        "v_rcp_f32 v74, v74\n"
        "v_rcp_f32 v75, v75\n"
        "v_mul_f32 v48, v48, v72\n"
        "v_mul_f32 v49, v49, v73\n"
        "v_mul_f32 v50, v50, v74\n"
        "v_mul_f32 v51, v51, v75\n"
        "v_cvt_pk_bf16_f32 v52, v48, v49\n"
        "v_cvt_pk_bf16_f32 v53, v50, v51\n"
        "s_waitcnt vmcnt(7)\n"
        "buffer_store_dword v5, v3, s[40:43], s84 offen sc1\n"
        "s_mov_b32 s84, s89\n"
        "v_mul_f32 v76, s80, v56\n"
        "v_mul_f32 v77, s80, v57\n"
        "v_mul_f32 v78, s80, v58\n"
        "v_mul_f32 v79, s80, v59\n"
        "v_exp_f32 v76, v76\n"
        "v_exp_f32 v77, v77\n"
        "v_exp_f32 v78, v78\n"
        "v_exp_f32 v79, v79\n"
        "v_mul_f32 v56, v56, v60\n"
        "v_mul_f32 v57, v57, v61\n"
        "v_mul_f32 v58, v58, v62\n"
        "v_mul_f32 v59, v59, v63\n"
        "v_add_f32 v76, 1.0, v76\n"
        "v_add_f32 v77, 1.0, v77\n"
        "v_add_f32 v78, 1.0, v78\n"
        "v_add_f32 v79, 1.0, v79\n"
        "v_rcp_f32 v76, v76\n"
        "v_rcp_f32 v77, v77\n"
        "v_rcp_f32 v78, v78\n"
        "v_rcp_f32 v79, v79\n"
        "v_mul_f32 v56, v56, v76\n"
        "v_mul_f32 v57, v57, v77\n"
        "v_mul_f32 v58, v58, v78\n"
        "v_mul_f32 v59, v59, v79\n"
        "v_cvt_pk_bf16_f32 v54, v56, v57\n"
        "v_cvt_pk_bf16_f32 v55, v58, v59\n"
        "buffer_store_dwordx4 v[52:55], v1, s[40:43], s72 offen offset:0 sc1\n"
        "v_mul_f32 v72, s80, v64\n"
        "v_mul_f32 v73, s80, v65\n"
        "v_mul_f32 v74, s80, v66\n"
        "v_mul_f32 v75, s80, v67\n"
        "v_exp_f32 v72, v72\n"
        "v_exp_f32 v73, v73\n"
        "v_exp_f32 v74, v74\n"
        "v_exp_f32 v75, v75\n"
        "v_mul_f32 v64, v64, v68\n"
        "v_mul_f32 v65, v65, v69\n"
        "v_mul_f32 v66, v66, v70\n"
        "v_mul_f32 v67, v67, v71\n"
        "v_add_f32 v72, 1.0, v72\n"
        "v_add_f32 v73, 1.0, v73\n"
        "v_add_f32 v74, 1.0, v74\n"
        "v_add_f32 v75, 1.0, v75\n"
        "v_rcp_f32 v72, v72\n"
        "v_rcp_f32 v73, v73\n"
        "v_rcp_f32 v74, v74\n"
        "v_rcp_f32 v75, v75\n"
        "v_mul_f32 v64, v64, v72\n"
        "v_mul_f32 v65, v65, v73\n"
        "v_mul_f32 v66, v66, v74\n"
        "v_mul_f32 v67, v67, v75\n"
        "v_cvt_pk_bf16_f32 v68, v64, v65\n"
        "v_cvt_pk_bf16_f32 v69, v66, v67\n"
        "s_nop 0\n"
        "v_permlane16_swap_b32 v68, v70\n"
        "v_permlane16_swap_b32 v69, v71\n"
        "buffer_store_dwordx4 v[68:71], v7, s[40:43], s83 offen offset:0 sc1\n"
        "v_mov_b32 v5, s74\n"
        "s_mov_b32 s84, s60\n"
        "buffer_store_dword v5, v3, s[40:43], s89 offen sc1\n"
        "buffer_store_dword v5, v3, s[40:43], s89 offen sc1\n"
        "s_waitcnt vmcnt(5)\n"
        "s_branch .Lf1_go1_%=\n"
        ".Lf1_i12_%=:\n"
        "v_mfma_f32_16x16x32_bf16 v[64:67], v[200:203], v[8:11], v[64:67]\n"
        "buffer_load_dword v4, v2, s[40:43], s86 offen sc1\n"
        "v_mfma_f32_16x16x32_bf16 v[68:71], v[204:207], v[8:11], v[68:71]\n"
        "s_waitcnt lgkmcnt(1)\n"
        "v_mfma_f32_16x16x32_bf16 v[48:51], v[208:211], v[12:15], v[48:51]\n"
        "s_cmp_le_u32 s88, 0\n"
        "s_cselect_b32 s88, s73, s89\n"
        "s_cselect_b32 s73, s89, s73\n"
        "v_mfma_f32_16x16x32_bf16 v[52:55], v[212:215], v[12:15], v[52:55]\n"
        "buffer_store_dword v5, v3, s[40:43], s88 offen sc1\n"
        "v_mfma_f32_16x16x32_bf16 v[56:59], v[216:219], v[12:15], v[56:59]\n"
        "s_getreg_b32 s88, hwreg(HW_REG_IB_STS, 0, 4)\n"
        "v_mfma_f32_16x16x32_bf16 v[60:63], v[220:223], v[12:15], v[60:63]\n"
        "s_cmp_le_u32 s88, 2\n"
        "s_cselect_b32 s88, s73, s89\n"
        "s_cselect_b32 s73, s89, s73\n"
        "v_mfma_f32_16x16x32_bf16 v[64:67], v[224:227], v[12:15], v[64:67]\n"
        "buffer_store_dword v5, v3, s[40:43], s88 offen sc1\n"
        "s_add_u32 s74, s74, 1\n"
        "v_mfma_f32_16x16x32_bf16 v[68:71], v[228:231], v[12:15], v[68:71]\n"
        "s_waitcnt lgkmcnt(0)\n"
        "s_mov_b32 s84, s73\n"
        "v_mfma_f32_16x16x32_bf16 v[48:51], v[232:235], v[16:19], v[48:51]\n"
        "s_mov_b32 s73, s60\n"
        "s_add_u32 s72, s75, s59\n"
        "s_add_u32 s83, s75, s63\n"
        "v_mfma_f32_16x16x32_bf16 v[52:55], v[236:239], v[16:19], v[52:55]\n"
        "s_add_u32 s72, s72, 49152\n"
        "s_add_u32 s83, s83, 4096\n"
        "v_mfma_f32_16x16x32_bf16 v[56:59], v[240:243], v[16:19], v[56:59]\n"
        "v_mfma_f32_16x16x32_bf16 v[60:63], v[244:247], v[16:19], v[60:63]\n"
        "v_mfma_f32_16x16x32_bf16 v[64:67], v[248:251], v[16:19], v[64:67]\n"
        "v_mfma_f32_16x16x32_bf16 v[68:71], v[252:255], v[16:19], v[68:71]\n"
        "v_mul_f32 v72, s80, v48\n"
        "v_mul_f32 v73, s80, v49\n"
        "v_mul_f32 v74, s80, v50\n"
        "v_mul_f32 v75, s80, v51\n"
        "v_exp_f32 v72, v72\n"
        "v_exp_f32 v73, v73\n"
        "v_exp_f32 v74, v74\n"
        "v_exp_f32 v75, v75\n"
        "v_mul_f32 v48, v48, v52\n"
        "v_mul_f32 v49, v49, v53\n"
        "v_mul_f32 v50, v50, v54\n"
        "v_mul_f32 v51, v51, v55\n"
        "v_add_f32 v72, 1.0, v72\n"
        "v_add_f32 v73, 1.0, v73\n"
        "v_add_f32 v74, 1.0, v74\n"
        "v_add_f32 v75, 1.0, v75\n"
        "v_rcp_f32 v72, v72\n"
        "v_rcp_f32 v73, v73\n"
        "v_rcp_f32 v74, v74\n"
        "v_rcp_f32 v75, v75\n"
        "v_mul_f32 v48, v48, v72\n"
        "v_mul_f32 v49, v49, v73\n"
        "v_mul_f32 v50, v50, v74\n"
        "v_mul_f32 v51, v51, v75\n"
        "v_cvt_pk_bf16_f32 v52, v48, v49\n"
        "v_cvt_pk_bf16_f32 v53, v50, v51\n"
        "s_waitcnt vmcnt(3)\n"
        "buffer_store_dword v5, v3, s[40:43], s84 offen sc1\n"
        "s_mov_b32 s84, s89\n"
        "v_mul_f32 v76, s80, v56\n"
        "v_mul_f32 v77, s80, v57\n"
        "v_mul_f32 v78, s80, v58\n"
        "v_mul_f32 v79, s80, v59\n"
        "v_exp_f32 v76, v76\n"
        "v_exp_f32 v77, v77\n"
        "v_exp_f32 v78, v78\n"
        "v_exp_f32 v79, v79\n"
        "v_mul_f32 v56, v56, v60\n"
        "v_mul_f32 v57, v57, v61\n"
        "v_mul_f32 v58, v58, v62\n"
        "v_mul_f32 v59, v59, v63\n"
        "v_add_f32 v76, 1.0, v76\n"
        "v_add_f32 v77, 1.0, v77\n"
        "v_add_f32 v78, 1.0, v78\n"
        "v_add_f32 v79, 1.0, v79\n"
        "v_rcp_f32 v76, v76\n"
        "v_rcp_f32 v77, v77\n"
        "v_rcp_f32 v78, v78\n"
        "v_rcp_f32 v79, v79\n"
        "v_mul_f32 v56, v56, v76\n"
        "v_mul_f32 v57, v57, v77\n"
        "v_mul_f32 v58, v58, v78\n"
        "v_mul_f32 v59, v59, v79\n"
        "v_cvt_pk_bf16_f32 v54, v56, v57\n"
        "v_cvt_pk_bf16_f32 v55, v58, v59\n"
        "buffer_store_dwordx4 v[52:55], v1, s[40:43], s72 offen offset:0 sc1\n"
        "v_mul_f32 v72, s80, v64\n"
        "v_mul_f32 v73, s80, v65\n"
        "v_mul_f32 v74, s80, v66\n"
        "v_mul_f32 v75, s80, v67\n"
        "v_exp_f32 v72, v72\n"
        "v_exp_f32 v73, v73\n"
        "v_exp_f32 v74, v74\n"
        "v_exp_f32 v75, v75\n"
        "v_mul_f32 v64, v64, v68\n"
        "v_mul_f32 v65, v65, v69\n"
        "v_mul_f32 v66, v66, v70\n"
        "v_mul_f32 v67, v67, v71\n"
        "v_add_f32 v72, 1.0, v72\n"
        "v_add_f32 v73, 1.0, v73\n"
        "v_add_f32 v74, 1.0, v74\n"
        "v_add_f32 v75, 1.0, v75\n"
        "v_rcp_f32 v72, v72\n"
        "v_rcp_f32 v73, v73\n"
        "v_rcp_f32 v74, v74\n"
        "v_rcp_f32 v75, v75\n"
        "v_mul_f32 v64, v64, v72\n"
        "v_mul_f32 v65, v65, v73\n"
        "v_mul_f32 v66, v66, v74\n"
        "v_mul_f32 v67, v67, v75\n"
        "v_cvt_pk_bf16_f32 v68, v64, v65\n"
        "v_cvt_pk_bf16_f32 v69, v66, v67\n"
        "s_nop 0\n"
        "v_permlane16_swap_b32 v68, v70\n"
        "v_permlane16_swap_b32 v69, v71\n"
        "buffer_store_dwordx4 v[68:71], v7, s[40:43], s83 offen offset:0 sc1\n"
        "s_waitcnt vmcnt(5)\n"
        "v_cmp_gt_i32 vcc, s70, v4\n"
        "s_nop 0\n"
        "s_and_b64 vcc, vcc, 1\n"
        "s_cbranch_vccnz .Lf1_late1_%=\n"
        "buffer_load_dwordx4 v1, s[40:43], s71 offen offset:0 sc1 lds\n"
        "buffer_load_dwordx4 v1, s[40:43], s71 offen offset:1024 sc1 lds\n"
        "buffer_load_dwordx4 v1, s[40:43], s71 offen offset:2048 sc1 lds\n"
        "buffer_load_dwordx4 v1, s[40:43], s71 offen offset:3072 sc1 lds\n"
        "s_add_u32 m0, s78, s85\n"
        "s_add_u32 s69, s69, 1\n"
        "buffer_load_dwordx4 v1, s[40:43], s81 offen offset:0 sc1 lds\n"
        "s_waitcnt vmcnt(5)\n"
        "v_mov_b32 v5, s74\n"
        "buffer_store_dword v5, v3, s[40:43], s73 offen sc1\n"
        "s_waitcnt vmcnt(1)\n"
        "s_branch .Lf1_go1_%=\n"
        ".Lf1_late1_%=:\n"
        "s_waitcnt vmcnt(0)\n"
        "v_mov_b32 v5, s74\n"
        "buffer_store_dword v5, v3, s[40:43], s73 offen sc1\n"
        "s_cmp_eq_u32 s69, 128\n"
        "s_cbranch_scc0 .Lf1_wait1_%=\n"
        "s_endpgm\n"
        ".Lf1_wait1_%=:\n"
        "s_mov_b32 s64, 0\n"
        ".Lf1_in1_top_%=:\n"
        "buffer_load_dword v4, v2, s[40:43], s86 offen sc1\n"
        "s_waitcnt vmcnt(0)\n"
        "v_cmp_gt_i32 vcc, s70, v4\n"
        "s_nop 0\n"
        "s_and_b64 vcc, vcc, 1\n"
        "s_cbranch_vccz .Lf1_in1_ok_%=\n"
        "s_add_u32 s64, s64, 1\n"
        "s_and_b32 s65, s64, 255\n"
        "s_cbranch_scc1 .Lf1_in1_go_%=\n"
        "s_mov_b32 s65, 49152\n"
        "s_cmp_lt_u32 s64, 1048576\n"
        "s_cbranch_scc1 .Lf1_in1_chk_%=\n"
        "v_mov_b32 v4, 1\n"
        "buffer_store_dword v4, off, s[40:43], s65 sc1\n"
        "s_waitcnt vmcnt(0)\n"
        ".Lf1_in1_chk_%=:\n"
        "buffer_load_dword v4, off, s[40:43], s65 sc1\n"
        "s_waitcnt vmcnt(0)\n"
        "v_readfirstlane_b32 s65, v4\n"
        "s_nop 0\n"
        "s_cmp_lg_u32 s65, 1\n"
        "s_cbranch_scc1 .Lf1_in1_go_%=\n"
        "s_endpgm\n"
        ".Lf1_in1_go_%=:\n"
        "s_branch .Lf1_in1_top_%=\n"
        ".Lf1_in1_ok_%=:\n"
        "buffer_load_dwordx4 v1, s[40:43], s71 offen offset:0 sc1 lds\n"
        "buffer_load_dwordx4 v1, s[40:43], s71 offen offset:1024 sc1 lds\n"
        "buffer_load_dwordx4 v1, s[40:43], s71 offen offset:2048 sc1 lds\n"
        "buffer_load_dwordx4 v1, s[40:43], s71 offen offset:3072 sc1 lds\n"
        "s_add_u32 m0, s78, s85\n"
        "s_add_u32 s69, s69, 1\n"
        "buffer_load_dwordx4 v1, s[40:43], s81 offen offset:0 sc1 lds\n"
        "s_waitcnt vmcnt(0)\n"
        ".Lf1_go1_%=:\n"
        "s_mov_b32 s72, 241172480\n"
        "s_mov_b32 s83, 241172480\n"
        "s_mov_b32 s73, 241172480\n"
        "v_add_u32 v0, s78, v6\n"
        "s_barrier\n"
        "ds_read_b128 v[8:11], v0 offset:0\n"
        "ds_read_b128 v[12:15], v0 offset:512\n"
        "ds_read_b128 v[16:19], v0 offset:1024\n"
        "s_waitcnt lgkmcnt(2)\n"
        "s_branch .Lf1_i0_%=\n"
        ".Lf1_end_%=:\n"
        :: "s"(p.ws), "s"(p.w1), "s"(p.w3), "s"(p.w3), "s"(pair(w, delay)), "s"(pair(x4, pr)), TILE_ARGS(65 * PAGE + spread(B_XN1)),
           "v"(pair(lane == 0 ? flag(F_R3 + 8 * (6 * kap + jj) + w) : OOB, lane < 3 ? lane * 32768 : OOB)), "v"(pair(0, 0)), "v"(pair(0, (g & 1) ? OOB : (g >> 1) * 256 + i * 16)),
           "v"(pair(vo[0], vo[1])), "v"(pair(vo[2], w * 40960 + (4 * g + q) * 96 + 8 * pp))
        : "v0", "v1", "v2", "v3", "v4", "v5", "v6", "v7", "v8", "v9", "v10", "v11", "v12", "v13", "v14", "v15", "v16", "v17", "v18", "v19", "v20", "v21", "v22", "v23", "v24", "v25", "v26", "v27", "v28", "v29", "v30", "v31", "v32", "v33", "v34", "v35", "v36", "v37", "v38", "v39", "v40", "v41", "v42", "v43", "v44", "v45", "v46", "v47", "v48", "v49", "v50", "v51", "v52", "v53", "v54", "v55", "v56", "v57", "v58", "v59", "v60", "v61", "v62", "v63", "v64", "v65", "v66", "v67", "v68", "v69", "v70", "v71", "v72", "v73", "v74", "v75", "v76", "v77", "v78", "v79", "v255", "a255", "s30", "s31", "s32", "s33", "s34", "s35", "s36", "s37", "s38", "s39", "s40", "s41", "s42", "s43", "s44", "s45", "s46", "s47", "s48", "s49", "s50", "s51", "s52", "s53", "s54", "s55", "s56", "s57", "s58", "s59", "s60", "s61", "s62", "s63", "s64", "s65", "s66", "s67", "s68", "s69", "s70", "s71", "s72", "s73", "s74", "s75", "s76", "s77", "s78", "s79", "s80", "s81", "s82", "s83", "s84", "s85", "s86", "s87", "s88", "s89", "s90", "s91", "s92", "s93", "s94", "s95", "s96", "s97", "s98", "s99", "vcc", "scc", "memory");
    __builtin_unreachable();
}
__device__ __forceinline__ unsigned part_off(unsigned a, unsigned kap, unsigned chunk) {
    const unsigned b = (a & 1) * 4 + kap;
    return (a >> 1) * (9 * 16384) + (chunk < 64 ? b * 16384 + chunk * 128 : 8 * 16384 + b * 1024 + (chunk - 64) * 128);
}
template <bool OP>
__device__ __forceinline__ void role_k(const Params& p, int kap, int nu, int x4, int pr, int delay) {
    GEMM_HEAD;
    unsigned vo[3], vt[3];
#pragma unroll
    for (int n = 0; n < 3; ++n) {
        const unsigned ps = (64 * n + lane) / 6, gg = (ps >> 2) & 3;
        const unsigned col = (192 * nu + 48 * w) * 2 + ((64 * n + lane) % 6) * 16 - 1024 * n;
        vo[n] = ((OP ? 1024 : 1152) * kap + ps) * 1152 + col;
        vt[n] = (1152 * kap + 48 * (gg >> 1) + 32 + 8 * (gg & 1) + 4 * (ps >> 4) + (ps & 3)) * 1152 + col;
    }
    const unsigned tr = w * 40960 + (4 * g + q) * 96;
    const unsigned c0 = 24 * nu + 6 * w;
    const unsigned s01 = part_off(i >> 3, kap, c0 + g) + (i & 7) * 16, s2 = (g & 1) ? OOB : part_off(i >> 3, kap, c0 + 4 + (g >> 1)) + (i & 7) * 16;
    const unsigned fl = nu * 16 + kap * 4 + w, fo4 = lane < 4 ? lane * 16384 : OOB;
#define K_ARGS "v"(pair(0, 0)), "v"(pair(s01, s2)), "v"(pair(vo[0], vo[1])), "v"(pair(vo[2], tr + 16 * pp)), "v"(pair(vt[0], vt[1])), "v"(pair(vt[2], tr + 64 + 8 * pp))
    if constexpr (OP) {
        asm volatile(
        "s_nop 4\n"
        "s_mov_b64 s[40:41], %0\n"
        "s_and_b32 s41, s41, 0xffff\n"
        "s_mov_b32 s42, 236978176\n"
        "s_mov_b32 s43, 0x20000\n"
        "s_mov_b64 s[48:49], %4\n"
        "s_mov_b64 s[56:57], %5\n"
        "s_mov_b64 s[58:59], %6\n"
        "s_mov_b64 s[60:61], %7\n"
        "s_mov_b64 s[62:63], %8\n"
        "v_mov_b64 v[0:1], %9\n"
        "v_mov_b64 v[2:3], %10\n"
        "v_mov_b64 v[4:5], %11\n"
        "v_mov_b64 v[6:7], %12\n"
        "v_mov_b64 v[8:9], %13\n"
        "v_mov_b64 v[10:11], %14\n"
        "v_mov_b64 v[12:13], %15\n"
        "v_mov_b64 v[14:15], %16\n"
        "s_mov_b32 s47, 0x20000\n"
        "s_mul_i32 s79, s48, 40960\n"
        ".Lop_start_%=:\n"
        "s_sub_u32 s49, s49, 1\n"
        "s_cbranch_scc1 .Lop_start2_%=\n"
        "s_sleep 16\n"
        "s_branch .Lop_start_%=\n"
        ".Lop_start2_%=:\n"
        "s_mov_b64 s[44:45], %1\n"
        "s_and_b32 s45, s45, 0xffff\n"
        "s_mov_b32 s46, 4718592\n"
        "s_add_u32 m0, s79, 0\n"
        "s_mov_b32 s65, 0\n"
        "buffer_load_dwordx4 v8, s[44:47], s65 offen lds\n"
        "buffer_load_dwordx4 v9, s[44:47], s65 offen offset:1024 lds\n"
        "buffer_load_dwordx4 v10, s[44:47], s65 offen offset:2048 lds\n"
        "s_add_u32 m0, s79, 3072\n"
        "s_mov_b32 s65, 36864\n"
        "buffer_load_dwordx4 v8, s[44:47], s65 offen lds\n"
        "buffer_load_dwordx4 v9, s[44:47], s65 offen offset:1024 lds\n"
        "buffer_load_dwordx4 v10, s[44:47], s65 offen offset:2048 lds\n"
        "s_waitcnt vmcnt(3)\n"
        "ds_read_b64_tr_b16 a[0:1], v11 offset:0\n"
        "ds_read_b64_tr_b16 a[2:3], v11 offset:1536\n"
        "ds_read_b64_tr_b16 a[4:5], v11 offset:8\n"
        "ds_read_b64_tr_b16 a[6:7], v11 offset:1544\n"
        "ds_read_b64_tr_b16 a[8:9], v15 offset:0\n"
        "ds_read_b64_tr_b16 a[10:11], v15 offset:1536\n"
        "s_waitcnt vmcnt(0)\n"
        "ds_read_b64_tr_b16 a[12:13], v11 offset:3072\n"
        "ds_read_b64_tr_b16 a[14:15], v11 offset:4608\n"
        "ds_read_b64_tr_b16 a[16:17], v11 offset:3080\n"
        "ds_read_b64_tr_b16 a[18:19], v11 offset:4616\n"
        "ds_read_b64_tr_b16 a[20:21], v15 offset:3072\n"
        "ds_read_b64_tr_b16 a[22:23], v15 offset:4608\n"
        "s_waitcnt lgkmcnt(6)\n"
        "s_add_u32 m0, s79, 0\n"
        "s_mov_b32 s65, 73728\n"
        "buffer_load_dwordx4 v8, s[44:47], s65 offen lds\n"
        "buffer_load_dwordx4 v9, s[44:47], s65 offen offset:1024 lds\n"
        "buffer_load_dwordx4 v10, s[44:47], s65 offen offset:2048 lds\n"
        "s_waitcnt vmcnt(0)\n"
        "ds_read_b64_tr_b16 a[24:25], v11 offset:0\n"
        "ds_read_b64_tr_b16 a[26:27], v11 offset:1536\n"
        "ds_read_b64_tr_b16 a[28:29], v11 offset:8\n"
        "ds_read_b64_tr_b16 a[30:31], v11 offset:1544\n"
        "ds_read_b64_tr_b16 a[32:33], v15 offset:0\n"
        "ds_read_b64_tr_b16 a[34:35], v15 offset:1536\n"
        "s_waitcnt lgkmcnt(6)\n"
        "s_add_u32 m0, s79, 3072\n"
        "s_mov_b32 s65, 110592\n"
        "buffer_load_dwordx4 v8, s[44:47], s65 offen lds\n"
        "buffer_load_dwordx4 v9, s[44:47], s65 offen offset:1024 lds\n"
        "buffer_load_dwordx4 v10, s[44:47], s65 offen offset:2048 lds\n"
        "s_waitcnt vmcnt(0)\n"
        "ds_read_b64_tr_b16 a[36:37], v11 offset:3072\n"
        "ds_read_b64_tr_b16 a[38:39], v11 offset:4608\n"
        "ds_read_b64_tr_b16 a[40:41], v11 offset:3080\n"
        "ds_read_b64_tr_b16 a[42:43], v11 offset:4616\n"
        "ds_read_b64_tr_b16 a[44:45], v15 offset:3072\n"
        "ds_read_b64_tr_b16 a[46:47], v15 offset:4608\n"
        "s_waitcnt lgkmcnt(6)\n"
        "s_add_u32 m0, s79, 0\n"
        "s_mov_b32 s65, 147456\n"
        "buffer_load_dwordx4 v8, s[44:47], s65 offen lds\n"
        "buffer_load_dwordx4 v9, s[44:47], s65 offen offset:1024 lds\n"
        "buffer_load_dwordx4 v10, s[44:47], s65 offen offset:2048 lds\n"
        "s_waitcnt vmcnt(0)\n"
        "ds_read_b64_tr_b16 a[48:49], v11 offset:0\n"
        "ds_read_b64_tr_b16 a[50:51], v11 offset:1536\n"
        "ds_read_b64_tr_b16 a[52:53], v11 offset:8\n"
        "ds_read_b64_tr_b16 a[54:55], v11 offset:1544\n"
        "ds_read_b64_tr_b16 a[56:57], v15 offset:0\n"
        "ds_read_b64_tr_b16 a[58:59], v15 offset:1536\n"
        "s_waitcnt lgkmcnt(6)\n"
        "s_add_u32 m0, s79, 3072\n"
        "s_mov_b32 s65, 184320\n"
        "buffer_load_dwordx4 v8, s[44:47], s65 offen lds\n"
        "buffer_load_dwordx4 v9, s[44:47], s65 offen offset:1024 lds\n"
        "buffer_load_dwordx4 v10, s[44:47], s65 offen offset:2048 lds\n"
        "s_waitcnt vmcnt(0)\n"
        "ds_read_b64_tr_b16 a[60:61], v11 offset:3072\n"
        "ds_read_b64_tr_b16 a[62:63], v11 offset:4608\n"
        "ds_read_b64_tr_b16 a[64:65], v11 offset:3080\n"
        "ds_read_b64_tr_b16 a[66:67], v11 offset:4616\n"
        "ds_read_b64_tr_b16 a[68:69], v15 offset:3072\n"
        "ds_read_b64_tr_b16 a[70:71], v15 offset:4608\n"
        "s_waitcnt lgkmcnt(6)\n"
        "s_add_u32 m0, s79, 0\n"
        "s_mov_b32 s65, 221184\n"
        "buffer_load_dwordx4 v8, s[44:47], s65 offen lds\n"
        "buffer_load_dwordx4 v9, s[44:47], s65 offen offset:1024 lds\n"
        "buffer_load_dwordx4 v10, s[44:47], s65 offen offset:2048 lds\n"
        "s_waitcnt vmcnt(0)\n"
        "ds_read_b64_tr_b16 a[72:73], v11 offset:0\n"
        "ds_read_b64_tr_b16 a[74:75], v11 offset:1536\n"
        "ds_read_b64_tr_b16 a[76:77], v11 offset:8\n"
        "ds_read_b64_tr_b16 a[78:79], v11 offset:1544\n"
        "ds_read_b64_tr_b16 a[80:81], v15 offset:0\n"
        "ds_read_b64_tr_b16 a[82:83], v15 offset:1536\n"
        "s_waitcnt lgkmcnt(6)\n"
        "s_add_u32 m0, s79, 3072\n"
        "s_mov_b32 s65, 258048\n"
        "buffer_load_dwordx4 v8, s[44:47], s65 offen lds\n"
        "buffer_load_dwordx4 v9, s[44:47], s65 offen offset:1024 lds\n"
        "buffer_load_dwordx4 v10, s[44:47], s65 offen offset:2048 lds\n"
        "s_waitcnt vmcnt(0)\n"
        "ds_read_b64_tr_b16 a[84:85], v11 offset:3072\n"
        "ds_read_b64_tr_b16 a[86:87], v11 offset:4608\n"
        "ds_read_b64_tr_b16 a[88:89], v11 offset:3080\n"
        "ds_read_b64_tr_b16 a[90:91], v11 offset:4616\n"
        "ds_read_b64_tr_b16 a[92:93], v15 offset:3072\n"
        "ds_read_b64_tr_b16 a[94:95], v15 offset:4608\n"
        "s_waitcnt lgkmcnt(6)\n"
        "s_add_u32 m0, s79, 0\n"
        "s_mov_b32 s65, 294912\n"
        "buffer_load_dwordx4 v8, s[44:47], s65 offen lds\n"
        "buffer_load_dwordx4 v9, s[44:47], s65 offen offset:1024 lds\n"
        "buffer_load_dwordx4 v10, s[44:47], s65 offen offset:2048 lds\n"
        "s_waitcnt vmcnt(0)\n"
        "ds_read_b64_tr_b16 a[96:97], v11 offset:0\n"
        "ds_read_b64_tr_b16 a[98:99], v11 offset:1536\n"
        "ds_read_b64_tr_b16 a[100:101], v11 offset:8\n"
        "ds_read_b64_tr_b16 a[102:103], v11 offset:1544\n"
        "ds_read_b64_tr_b16 a[104:105], v15 offset:0\n"
        "ds_read_b64_tr_b16 a[106:107], v15 offset:1536\n"
        "s_waitcnt lgkmcnt(6)\n"
        "s_add_u32 m0, s79, 3072\n"
        "s_mov_b32 s65, 331776\n"
        "buffer_load_dwordx4 v8, s[44:47], s65 offen lds\n"
        "buffer_load_dwordx4 v9, s[44:47], s65 offen offset:1024 lds\n"
        "buffer_load_dwordx4 v10, s[44:47], s65 offen offset:2048 lds\n"
        "s_waitcnt vmcnt(0)\n"
        "ds_read_b64_tr_b16 a[108:109], v11 offset:3072\n"
        "ds_read_b64_tr_b16 a[110:111], v11 offset:4608\n"
        "ds_read_b64_tr_b16 a[112:113], v11 offset:3080\n"
        "ds_read_b64_tr_b16 a[114:115], v11 offset:4616\n"
        "ds_read_b64_tr_b16 a[116:117], v15 offset:3072\n"
        "ds_read_b64_tr_b16 a[118:119], v15 offset:4608\n"
        "s_waitcnt lgkmcnt(6)\n"
        "s_add_u32 m0, s79, 0\n"
        "s_mov_b32 s65, 368640\n"
        "buffer_load_dwordx4 v8, s[44:47], s65 offen lds\n"
        "buffer_load_dwordx4 v9, s[44:47], s65 offen offset:1024 lds\n"
        "buffer_load_dwordx4 v10, s[44:47], s65 offen offset:2048 lds\n"
        "s_waitcnt vmcnt(0)\n"
        "ds_read_b64_tr_b16 a[120:121], v11 offset:0\n"
        "ds_read_b64_tr_b16 a[122:123], v11 offset:1536\n"
        "ds_read_b64_tr_b16 a[124:125], v11 offset:8\n"
        "ds_read_b64_tr_b16 a[126:127], v11 offset:1544\n"
        "ds_read_b64_tr_b16 a[128:129], v15 offset:0\n"
        "ds_read_b64_tr_b16 a[130:131], v15 offset:1536\n"
        "s_waitcnt lgkmcnt(6)\n"
        "s_add_u32 m0, s79, 3072\n"
        "s_mov_b32 s65, 405504\n"
        "buffer_load_dwordx4 v8, s[44:47], s65 offen lds\n"
        "buffer_load_dwordx4 v9, s[44:47], s65 offen offset:1024 lds\n"
        "buffer_load_dwordx4 v10, s[44:47], s65 offen offset:2048 lds\n"
        "s_waitcnt vmcnt(0)\n"
        "ds_read_b64_tr_b16 a[132:133], v11 offset:3072\n"
        "ds_read_b64_tr_b16 a[134:135], v11 offset:4608\n"
        "ds_read_b64_tr_b16 a[136:137], v11 offset:3080\n"
        "ds_read_b64_tr_b16 a[138:139], v11 offset:4616\n"
        "ds_read_b64_tr_b16 a[140:141], v15 offset:3072\n"
        "ds_read_b64_tr_b16 a[142:143], v15 offset:4608\n"
        "s_waitcnt lgkmcnt(6)\n"
        "s_add_u32 m0, s79, 0\n"
        "s_mov_b32 s65, 442368\n"
        "buffer_load_dwordx4 v8, s[44:47], s65 offen lds\n"
        "buffer_load_dwordx4 v9, s[44:47], s65 offen offset:1024 lds\n"
        "buffer_load_dwordx4 v10, s[44:47], s65 offen offset:2048 lds\n"
        "s_waitcnt vmcnt(0)\n"
        "ds_read_b64_tr_b16 a[144:145], v11 offset:0\n"
        "ds_read_b64_tr_b16 a[146:147], v11 offset:1536\n"
        "ds_read_b64_tr_b16 a[148:149], v11 offset:8\n"
        "ds_read_b64_tr_b16 a[150:151], v11 offset:1544\n"
        "ds_read_b64_tr_b16 a[152:153], v15 offset:0\n"
        "ds_read_b64_tr_b16 a[154:155], v15 offset:1536\n"
        "s_waitcnt lgkmcnt(6)\n"
        "s_add_u32 m0, s79, 3072\n"
        "s_mov_b32 s65, 479232\n"
        "buffer_load_dwordx4 v8, s[44:47], s65 offen lds\n"
        "buffer_load_dwordx4 v9, s[44:47], s65 offen offset:1024 lds\n"
        "buffer_load_dwordx4 v10, s[44:47], s65 offen offset:2048 lds\n"
        "s_waitcnt vmcnt(0)\n"
        "ds_read_b64_tr_b16 a[156:157], v11 offset:3072\n"
        "ds_read_b64_tr_b16 a[158:159], v11 offset:4608\n"
        "ds_read_b64_tr_b16 a[160:161], v11 offset:3080\n"
        "ds_read_b64_tr_b16 a[162:163], v11 offset:4616\n"
        "ds_read_b64_tr_b16 a[164:165], v15 offset:3072\n"
        "ds_read_b64_tr_b16 a[166:167], v15 offset:4608\n"
        "s_waitcnt lgkmcnt(6)\n"
        "s_add_u32 m0, s79, 0\n"
        "s_mov_b32 s65, 516096\n"
        "buffer_load_dwordx4 v8, s[44:47], s65 offen lds\n"
        "buffer_load_dwordx4 v9, s[44:47], s65 offen offset:1024 lds\n"
        "buffer_load_dwordx4 v10, s[44:47], s65 offen offset:2048 lds\n"
        "s_waitcnt vmcnt(0)\n"
        "ds_read_b64_tr_b16 a[168:169], v11 offset:0\n"
        "ds_read_b64_tr_b16 a[170:171], v11 offset:1536\n"
        "ds_read_b64_tr_b16 a[172:173], v11 offset:8\n"
        "ds_read_b64_tr_b16 a[174:175], v11 offset:1544\n"
        "ds_read_b64_tr_b16 a[176:177], v15 offset:0\n"
        "ds_read_b64_tr_b16 a[178:179], v15 offset:1536\n"
        "s_waitcnt lgkmcnt(6)\n"
        "s_add_u32 m0, s79, 3072\n"
        "s_mov_b32 s65, 552960\n"
        "buffer_load_dwordx4 v8, s[44:47], s65 offen lds\n"
        "buffer_load_dwordx4 v9, s[44:47], s65 offen offset:1024 lds\n"
        "buffer_load_dwordx4 v10, s[44:47], s65 offen offset:2048 lds\n"
        "s_waitcnt vmcnt(0)\n"
        "ds_read_b64_tr_b16 a[180:181], v11 offset:3072\n"
        "ds_read_b64_tr_b16 a[182:183], v11 offset:4608\n"
        "ds_read_b64_tr_b16 a[184:185], v11 offset:3080\n"
        "ds_read_b64_tr_b16 a[186:187], v11 offset:4616\n"
        "ds_read_b64_tr_b16 a[188:189], v15 offset:3072\n"
        "ds_read_b64_tr_b16 a[190:191], v15 offset:4608\n"
        "s_waitcnt lgkmcnt(6)\n"
        "s_add_u32 m0, s79, 0\n"
        "s_mov_b32 s65, 589824\n"
        "buffer_load_dwordx4 v8, s[44:47], s65 offen lds\n"
        "buffer_load_dwordx4 v9, s[44:47], s65 offen offset:1024 lds\n"
        "buffer_load_dwordx4 v10, s[44:47], s65 offen offset:2048 lds\n"
        "s_waitcnt vmcnt(0)\n"
        "ds_read_b64_tr_b16 a[192:193], v11 offset:0\n"
        "ds_read_b64_tr_b16 a[194:195], v11 offset:1536\n"
        "ds_read_b64_tr_b16 a[196:197], v11 offset:8\n"
        "ds_read_b64_tr_b16 a[198:199], v11 offset:1544\n"
        "ds_read_b64_tr_b16 a[200:201], v15 offset:0\n"
        "ds_read_b64_tr_b16 a[202:203], v15 offset:1536\n"
        "s_waitcnt lgkmcnt(6)\n"
        "s_add_u32 m0, s79, 3072\n"
        "s_mov_b32 s65, 626688\n"
        "buffer_load_dwordx4 v8, s[44:47], s65 offen lds\n"
        "buffer_load_dwordx4 v9, s[44:47], s65 offen offset:1024 lds\n"
        "buffer_load_dwordx4 v10, s[44:47], s65 offen offset:2048 lds\n"
        "s_waitcnt vmcnt(0)\n"
        "ds_read_b64_tr_b16 a[204:205], v11 offset:3072\n"
        "ds_read_b64_tr_b16 a[206:207], v11 offset:4608\n"
        "ds_read_b64_tr_b16 a[208:209], v11 offset:3080\n"
        "ds_read_b64_tr_b16 a[210:211], v11 offset:4616\n"
        "ds_read_b64_tr_b16 a[212:213], v15 offset:3072\n"
        "ds_read_b64_tr_b16 a[214:215], v15 offset:4608\n"
        "s_waitcnt lgkmcnt(6)\n"
        "s_add_u32 m0, s79, 0\n"
        "s_mov_b32 s65, 663552\n"
        "buffer_load_dwordx4 v8, s[44:47], s65 offen lds\n"
        "buffer_load_dwordx4 v9, s[44:47], s65 offen offset:1024 lds\n"
        "buffer_load_dwordx4 v10, s[44:47], s65 offen offset:2048 lds\n"
        "s_waitcnt vmcnt(0)\n"
        "ds_read_b64_tr_b16 a[216:217], v11 offset:0\n"
        "ds_read_b64_tr_b16 a[218:219], v11 offset:1536\n"
        "ds_read_b64_tr_b16 a[220:221], v11 offset:8\n"
        "ds_read_b64_tr_b16 a[222:223], v11 offset:1544\n"
        "ds_read_b64_tr_b16 a[224:225], v15 offset:0\n"
        "ds_read_b64_tr_b16 a[226:227], v15 offset:1536\n"
        "s_waitcnt lgkmcnt(6)\n"
        "s_add_u32 m0, s79, 3072\n"
        "s_mov_b32 s65, 700416\n"
        "buffer_load_dwordx4 v8, s[44:47], s65 offen lds\n"
        "buffer_load_dwordx4 v9, s[44:47], s65 offen offset:1024 lds\n"
        "buffer_load_dwordx4 v10, s[44:47], s65 offen offset:2048 lds\n"
        "s_waitcnt vmcnt(0)\n"
        "ds_read_b64_tr_b16 a[228:229], v11 offset:3072\n"
        "ds_read_b64_tr_b16 a[230:231], v11 offset:4608\n"
        "ds_read_b64_tr_b16 a[232:233], v11 offset:3080\n"
        "ds_read_b64_tr_b16 a[234:235], v11 offset:4616\n"
        "ds_read_b64_tr_b16 a[236:237], v15 offset:3072\n"
        "ds_read_b64_tr_b16 a[238:239], v15 offset:4608\n"
        "s_waitcnt lgkmcnt(6)\n"
        "s_add_u32 m0, s79, 0\n"
        "s_mov_b32 s65, 737280\n"
        "buffer_load_dwordx4 v8, s[44:47], s65 offen lds\n"
        "buffer_load_dwordx4 v9, s[44:47], s65 offen offset:1024 lds\n"
        "buffer_load_dwordx4 v10, s[44:47], s65 offen offset:2048 lds\n"
        "s_waitcnt vmcnt(0)\n"
        "ds_read_b64_tr_b16 a[240:241], v11 offset:0\n"
        "ds_read_b64_tr_b16 a[242:243], v11 offset:1536\n"
        "ds_read_b64_tr_b16 a[244:245], v11 offset:8\n"
        "ds_read_b64_tr_b16 a[246:247], v11 offset:1544\n"
        "ds_read_b64_tr_b16 a[248:249], v15 offset:0\n"
        "ds_read_b64_tr_b16 a[250:251], v15 offset:1536\n"
        "s_waitcnt lgkmcnt(6)\n"
        "s_add_u32 m0, s79, 3072\n"
        "s_mov_b32 s65, 774144\n"
        "buffer_load_dwordx4 v8, s[44:47], s65 offen lds\n"
        "buffer_load_dwordx4 v9, s[44:47], s65 offen offset:1024 lds\n"
        "buffer_load_dwordx4 v10, s[44:47], s65 offen offset:2048 lds\n"
        "s_waitcnt vmcnt(0)\n"
        "ds_read_b64_tr_b16 a[252:253], v11 offset:3072\n"
        "ds_read_b64_tr_b16 a[254:255], v11 offset:4608\n"
        "ds_read_b64_tr_b16 v[80:81], v11 offset:3080\n"
        "ds_read_b64_tr_b16 v[82:83], v11 offset:4616\n"
        "ds_read_b64_tr_b16 v[84:85], v15 offset:3072\n"
        "ds_read_b64_tr_b16 v[86:87], v15 offset:4608\n"
        "s_waitcnt lgkmcnt(6)\n"
        "s_add_u32 m0, s79, 0\n"
        "s_mov_b32 s65, 811008\n"
        "buffer_load_dwordx4 v8, s[44:47], s65 offen lds\n"
        "buffer_load_dwordx4 v9, s[44:47], s65 offen offset:1024 lds\n"
        "buffer_load_dwordx4 v10, s[44:47], s65 offen offset:2048 lds\n"
        "s_waitcnt vmcnt(0)\n"
        "ds_read_b64_tr_b16 v[88:89], v11 offset:0\n"
        "ds_read_b64_tr_b16 v[90:91], v11 offset:1536\n"
        "ds_read_b64_tr_b16 v[92:93], v11 offset:8\n"
        "ds_read_b64_tr_b16 v[94:95], v11 offset:1544\n"
        "ds_read_b64_tr_b16 v[96:97], v15 offset:0\n"
        "ds_read_b64_tr_b16 v[98:99], v15 offset:1536\n"
        "s_waitcnt lgkmcnt(6)\n"
        "s_add_u32 m0, s79, 3072\n"
        "s_mov_b32 s65, 847872\n"
        "buffer_load_dwordx4 v8, s[44:47], s65 offen lds\n"
        "buffer_load_dwordx4 v9, s[44:47], s65 offen offset:1024 lds\n"
        "buffer_load_dwordx4 v10, s[44:47], s65 offen offset:2048 lds\n"
        "s_waitcnt vmcnt(0)\n"
        "ds_read_b64_tr_b16 v[100:101], v11 offset:3072\n"
        "ds_read_b64_tr_b16 v[102:103], v11 offset:4608\n"
        "ds_read_b64_tr_b16 v[104:105], v11 offset:3080\n"
        "ds_read_b64_tr_b16 v[106:107], v11 offset:4616\n"
        "ds_read_b64_tr_b16 v[108:109], v15 offset:3072\n"
        "ds_read_b64_tr_b16 v[110:111], v15 offset:4608\n"
        "s_waitcnt lgkmcnt(6)\n"
        "s_add_u32 m0, s79, 0\n"
        "s_mov_b32 s65, 884736\n"
        "buffer_load_dwordx4 v8, s[44:47], s65 offen lds\n"
        "buffer_load_dwordx4 v9, s[44:47], s65 offen offset:1024 lds\n"
        "buffer_load_dwordx4 v10, s[44:47], s65 offen offset:2048 lds\n"
        "s_waitcnt vmcnt(0)\n"
        "ds_read_b64_tr_b16 v[112:113], v11 offset:0\n"
        "ds_read_b64_tr_b16 v[114:115], v11 offset:1536\n"
        "ds_read_b64_tr_b16 v[116:117], v11 offset:8\n"
        "ds_read_b64_tr_b16 v[118:119], v11 offset:1544\n"
        "ds_read_b64_tr_b16 v[120:121], v15 offset:0\n"
        "ds_read_b64_tr_b16 v[122:123], v15 offset:1536\n"
        "s_waitcnt lgkmcnt(6)\n"
        "s_add_u32 m0, s79, 3072\n"
        "s_mov_b32 s65, 921600\n"
        "buffer_load_dwordx4 v8, s[44:47], s65 offen lds\n"
        "buffer_load_dwordx4 v9, s[44:47], s65 offen offset:1024 lds\n"
        "buffer_load_dwordx4 v10, s[44:47], s65 offen offset:2048 lds\n"
        "s_waitcnt vmcnt(0)\n"
        "ds_read_b64_tr_b16 v[124:125], v11 offset:3072\n"
        "ds_read_b64_tr_b16 v[126:127], v11 offset:4608\n"
        "ds_read_b64_tr_b16 v[128:129], v11 offset:3080\n"
        "ds_read_b64_tr_b16 v[130:131], v11 offset:4616\n"
        "ds_read_b64_tr_b16 v[132:133], v15 offset:3072\n"
        "ds_read_b64_tr_b16 v[134:135], v15 offset:4608\n"
        "s_waitcnt lgkmcnt(6)\n"
        "s_add_u32 m0, s79, 0\n"
        "s_mov_b32 s65, 958464\n"
        "buffer_load_dwordx4 v8, s[44:47], s65 offen lds\n"
        "buffer_load_dwordx4 v9, s[44:47], s65 offen offset:1024 lds\n"
        "buffer_load_dwordx4 v10, s[44:47], s65 offen offset:2048 lds\n"
        "s_waitcnt vmcnt(0)\n"
        "ds_read_b64_tr_b16 v[136:137], v11 offset:0\n"
        "ds_read_b64_tr_b16 v[138:139], v11 offset:1536\n"
        "ds_read_b64_tr_b16 v[140:141], v11 offset:8\n"
        "ds_read_b64_tr_b16 v[142:143], v11 offset:1544\n"
        "ds_read_b64_tr_b16 v[144:145], v15 offset:0\n"
        "ds_read_b64_tr_b16 v[146:147], v15 offset:1536\n"
        "s_waitcnt lgkmcnt(6)\n"
        "s_add_u32 m0, s79, 3072\n"
        "s_mov_b32 s65, 995328\n"
        "buffer_load_dwordx4 v8, s[44:47], s65 offen lds\n"
        "buffer_load_dwordx4 v9, s[44:47], s65 offen offset:1024 lds\n"
        "buffer_load_dwordx4 v10, s[44:47], s65 offen offset:2048 lds\n"
        "s_waitcnt vmcnt(0)\n"
        "ds_read_b64_tr_b16 v[148:149], v11 offset:3072\n"
        "ds_read_b64_tr_b16 v[150:151], v11 offset:4608\n"
        "ds_read_b64_tr_b16 v[152:153], v11 offset:3080\n"
        "ds_read_b64_tr_b16 v[154:155], v11 offset:4616\n"
        "ds_read_b64_tr_b16 v[156:157], v15 offset:3072\n"
        "ds_read_b64_tr_b16 v[158:159], v15 offset:4608\n"
        "s_waitcnt lgkmcnt(6)\n"
        "s_add_u32 m0, s79, 0\n"
        "s_mov_b32 s65, 1032192\n"
        "buffer_load_dwordx4 v8, s[44:47], s65 offen lds\n"
        "buffer_load_dwordx4 v9, s[44:47], s65 offen offset:1024 lds\n"
        "buffer_load_dwordx4 v10, s[44:47], s65 offen offset:2048 lds\n"
        "s_waitcnt vmcnt(0)\n"
        "ds_read_b64_tr_b16 v[160:161], v11 offset:0\n"
        "ds_read_b64_tr_b16 v[162:163], v11 offset:1536\n"
        "ds_read_b64_tr_b16 v[164:165], v11 offset:8\n"
        "ds_read_b64_tr_b16 v[166:167], v11 offset:1544\n"
        "ds_read_b64_tr_b16 v[168:169], v15 offset:0\n"
        "ds_read_b64_tr_b16 v[170:171], v15 offset:1536\n"
        "s_waitcnt lgkmcnt(6)\n"
        "s_add_u32 m0, s79, 3072\n"
        "s_mov_b32 s65, 1069056\n"
        "buffer_load_dwordx4 v8, s[44:47], s65 offen lds\n"
        "buffer_load_dwordx4 v9, s[44:47], s65 offen offset:1024 lds\n"
        "buffer_load_dwordx4 v10, s[44:47], s65 offen offset:2048 lds\n"
        "s_waitcnt vmcnt(0)\n"
        "ds_read_b64_tr_b16 v[172:173], v11 offset:3072\n"
        "ds_read_b64_tr_b16 v[174:175], v11 offset:4608\n"
        "ds_read_b64_tr_b16 v[176:177], v11 offset:3080\n"
        "ds_read_b64_tr_b16 v[178:179], v11 offset:4616\n"
        "ds_read_b64_tr_b16 v[180:181], v15 offset:3072\n"
        "ds_read_b64_tr_b16 v[182:183], v15 offset:4608\n"
        "s_waitcnt lgkmcnt(6)\n"
        "s_add_u32 m0, s79, 0\n"
        "s_mov_b32 s65, 1105920\n"
        "buffer_load_dwordx4 v8, s[44:47], s65 offen lds\n"
        "buffer_load_dwordx4 v9, s[44:47], s65 offen offset:1024 lds\n"
        "buffer_load_dwordx4 v10, s[44:47], s65 offen offset:2048 lds\n"
        "s_waitcnt vmcnt(0)\n"
        "ds_read_b64_tr_b16 v[184:185], v11 offset:0\n"
        "ds_read_b64_tr_b16 v[186:187], v11 offset:1536\n"
        "ds_read_b64_tr_b16 v[188:189], v11 offset:8\n"
        "ds_read_b64_tr_b16 v[190:191], v11 offset:1544\n"
        "ds_read_b64_tr_b16 v[192:193], v15 offset:0\n"
        "ds_read_b64_tr_b16 v[194:195], v15 offset:1536\n"
        "s_waitcnt lgkmcnt(6)\n"
        "s_add_u32 m0, s79, 3072\n"
        "s_mov_b32 s65, 1142784\n"
        "buffer_load_dwordx4 v8, s[44:47], s65 offen lds\n"
        "buffer_load_dwordx4 v9, s[44:47], s65 offen offset:1024 lds\n"
        "buffer_load_dwordx4 v10, s[44:47], s65 offen offset:2048 lds\n"
        "s_waitcnt vmcnt(0)\n"
        "ds_read_b64_tr_b16 v[196:197], v11 offset:3072\n"
        "ds_read_b64_tr_b16 v[198:199], v11 offset:4608\n"
        "ds_read_b64_tr_b16 v[200:201], v11 offset:3080\n"
        "ds_read_b64_tr_b16 v[202:203], v11 offset:4616\n"
        "ds_read_b64_tr_b16 v[204:205], v15 offset:3072\n"
        "ds_read_b64_tr_b16 v[206:207], v15 offset:4608\n"
        "s_waitcnt lgkmcnt(0)\n"
        "s_getpc_b64 s[44:45]\n"
        ".Lop_pc_%=:\n"
        "s_add_u32 s44, s44, .Lop_code_%=-.Lop_pc_%=\n"
        "s_addc_u32 s45, s45, 0\n"
        "s_and_b32 s45, s45, 0xffff\n"
        "s_and_b32 s44, s44, -4\n"
        "s_mov_b32 s46, .Lop_end_%=-.Lop_code_%=\n"
        "s_mov_b32 s47, 0x20000\n"
        "s_lshl_b32 s65, s48, 10\n"
        "buffer_load_dwordx4 v[24:27], v1, s[44:47], s65 offen\n"
        "s_add_u32 s65, s65, 4096\n"
        "buffer_load_dwordx4 v[28:31], v1, s[44:47], s65 offen\n"
        "s_add_u32 s65, s65, 4096\n"
        "buffer_load_dwordx4 v[32:35], v1, s[44:47], s65 offen\n"
        "s_add_u32 s65, s65, 4096\n"
        "buffer_load_dwordx4 v[36:39], v1, s[44:47], s65 offen\n"
        "s_add_u32 s65, s65, 4096\n"
        "buffer_load_dwordx4 v[40:43], v1, s[44:47], s65 offen\n"
        "s_add_u32 s65, s65, 4096\n"
        "v_lshrrev_b32 v20, 4, v1\n"
        "s_mov_b32 s50, s56\n"
        "v_lshlrev_b32 v22, 7, v20\n"
        "s_sub_u32 s66, 0, s50\n"
        "s_mov_b32 s52, 32768\n"
        "s_mov_b32 s64, 0\n"
        ".Lop_pg0_top_%=:\n"
        "buffer_load_dword v21, v22, s[40:43], s52 offen sc1\n"
        "s_waitcnt vmcnt(0)\n"
        "v_cmp_gt_i32 vcc, 0, v21\n"
        "s_cbranch_vccz .Lop_pg0_ok_%=\n"
        "s_add_u32 s64, s64, 1\n"
        "s_and_b32 s65, s64, 255\n"
        "s_cbranch_scc1 .Lop_pg0_go_%=\n"
        "s_mov_b32 s65, 49152\n"
        "s_cmp_lt_u32 s64, 1048576\n"
        "s_cbranch_scc1 .Lop_pg0_chk_%=\n"
        "v_mov_b32 v21, 1\n"
        "buffer_store_dword v21, off, s[40:43], s65 sc1\n"
        "s_waitcnt vmcnt(0)\n"
        ".Lop_pg0_chk_%=:\n"
        "buffer_load_dword v21, off, s[40:43], s65 sc1\n"
        "s_waitcnt vmcnt(0)\n"
        "v_readfirstlane_b32 s65, v21\n"
        "s_nop 0\n"
        "s_cmp_lg_u32 s65, 1\n"
        "s_cbranch_scc1 .Lop_pg0_go_%=\n"
        "s_endpgm\n"
        ".Lop_pg0_go_%=:\n"
        "s_sleep 2\n"
        "s_branch .Lop_pg0_top_%=\n"
        ".Lop_pg0_ok_%=:\n"
        "v_cmp_eq_u32 s[52:53], 1, v21\n"
        "s_nop 1\n"
        "s_mov_b32 s54, 40960\n"
        "s_mov_b32 s64, 0\n"
        ".Lop_pg1_top_%=:\n"
        "buffer_load_dword v21, v22, s[40:43], s54 offen sc1\n"
        "s_waitcnt vmcnt(0)\n"
        "v_cmp_gt_i32 vcc, 0, v21\n"
        "s_cbranch_vccz .Lop_pg1_ok_%=\n"
        "s_add_u32 s64, s64, 1\n"
        "s_and_b32 s65, s64, 255\n"
        "s_cbranch_scc1 .Lop_pg1_go_%=\n"
        "s_mov_b32 s65, 49152\n"
        "s_cmp_lt_u32 s64, 1048576\n"
        "s_cbranch_scc1 .Lop_pg1_chk_%=\n"
        "v_mov_b32 v21, 1\n"
        "buffer_store_dword v21, off, s[40:43], s65 sc1\n"
        "s_waitcnt vmcnt(0)\n"
        ".Lop_pg1_chk_%=:\n"
        "buffer_load_dword v21, off, s[40:43], s65 sc1\n"
        "s_waitcnt vmcnt(0)\n"
        "v_readfirstlane_b32 s65, v21\n"
        "s_nop 0\n"
        "s_cmp_lg_u32 s65, 1\n"
        "s_cbranch_scc1 .Lop_pg1_go_%=\n"
        "s_endpgm\n"
        ".Lop_pg1_go_%=:\n"
        "s_sleep 2\n"
        "s_branch .Lop_pg1_top_%=\n"
        ".Lop_pg1_ok_%=:\n"
        "v_cmp_eq_u32 s[54:55], 1, v21\n"
        "s_nop 1\n"
        "s_mov_b32 vcc_lo, 0x55555555\n"
        "s_mov_b32 vcc_hi, 0x55555555\n"
        "s_xor_b64 vcc, vcc, s[52:53]\n"
        "s_bcnt1_i32_b64 s65, vcc\n"
        "s_sub_u32 s64, s65, 23\n"
        "s_cmp_lt_u32 s64, 19\n"
        "s_cbranch_scc1 .Lop_rawmap_%=\n"
        "s_cmp_lt_u32 s65, 32\n"
        "s_cselect_b32 s65, 0, -1\n"
        "s_xor_b32 s52, s65, 0x55555555\n"
        "s_xor_b32 s53, s65, 0x55555555\n"
        "s_xor_b32 s54, s65, 0x55555555\n"
        "s_xor_b32 s55, s65, 0xaaaaaaaa\n"
        ".Lop_rawmap_%=:\n"
        "s_xor_b32 s52, s52, s66\n"
        "s_xor_b32 s53, s53, s66\n"
        "s_xor_b32 s54, s54, s66\n"
        "s_xor_b32 s55, s55, s66\n"
        "s_and_b32 s76, s55, 1\n"
        "s_lshl_b32 s76, s76, 13\n"
        "s_add_u32 s60, s60, s76\n"
        "s_barrier\n"
        "v_mov_b32 v48, v6\n"
        "s_mov_b32 s68, -1\n"
        "s_mov_b32 s69, 0\n"
        "s_mov_b32 s78, 0\n"
        "s_mov_b32 s74, -1\n"
        "s_mov_b32 s86, s76\n"
        "s_lshr_b32 s85, s61, 16\n"
        "s_and_b32 s61, s61, 0xffff\n"
        "s_mov_b32 s89, 241172480\n"
        "s_mov_b32 s84, 241172480\n"
        "v_mov_b32 v6, v0\n"
        "s_mov_b32 s80, 0xbfb8aa3b\n"
        "s_lshr_b32 s67, s69, 1\n"
        "s_bitcmp1_b64 s[52:53], s67\n"
        "s_cselect_b32 s65, 8192, 0\n"
        "s_lshl_b32 s77, s67, 21\n"
        "s_add_u32 s77, s77, s65\n"
        "s_add_u32 s71, s77, s58\n"
        "s_add_u32 s78, s78, 32768\n"
        "s_cmp_eq_u32 s78, 98304\n"
        "s_cselect_b32 s78, 0, s78\n"
        "s_add_u32 m0, s78, s61\n"
        "s_lshr_b32 s70, s69, 0\n"
        "s_branch .Lop_wait1_%=\n"
        ".Lop_code_%=:\n"
        ".Lop_i0_%=:\n"
        "v_mfma_f32_16x16x32_bf16 v[24:27], a[0:3], v[8:11], 0\n"
        "buffer_load_dword v4, v2, s[40:43], s86 offen sc1\n"
        "v_mfma_f32_16x16x32_bf16 v[28:31], a[4:7], v[8:11], 0\n"
        "s_add_u32 s71, s77, s58\n"
        "s_add_u32 s71, s71, 4096\n"
        "s_add_u32 s78, s78, 32768\n"
        "v_mfma_f32_16x16x32_bf16 v[32:35], a[8:11], v[8:11], 0\n"
        "s_cmp_eq_u32 s78, 98304\n"
        "s_cselect_b32 s78, 0, s78\n"
        "ds_read_b128 v[8:11], v0 offset:4096\n"
        "v_mfma_f32_16x16x32_bf16 v[24:27], a[12:15], v[12:15], v[24:27]\n"
        "s_add_u32 m0, s78, s61\n"
        "s_lshr_b32 s70, s69, 0\n"
        "v_cvt_pk_bf16_f32 v36, v36, v37\n"
        "v_mfma_f32_16x16x32_bf16 v[28:31], a[16:19], v[12:15], v[28:31]\n"
        "v_cvt_pk_bf16_f32 v37, v38, v39\n"
        "v_cvt_pk_bf16_f32 v38, v40, v41\n"
        "v_mfma_f32_16x16x32_bf16 v[32:35], a[20:23], v[12:15], v[32:35]\n"
        "s_waitcnt lgkmcnt(1)\n"
        "ds_read_b128 v[12:15], v0 offset:5120\n"
        "v_cvt_pk_bf16_f32 v39, v42, v43\n"
        "v_mfma_f32_16x16x32_bf16 v[24:27], a[24:27], v[16:19], v[24:27]\n"
        "buffer_store_dwordx4 v[36:39], v48, s[40:43], s72 offen offset:0 sc1\n"
        "v_mfma_f32_16x16x32_bf16 v[28:31], a[28:31], v[16:19], v[28:31]\n"
        "v_cvt_pk_bf16_f32 v44, v44, v45\n"
        "v_cvt_pk_bf16_f32 v45, v46, v47\n"
        "v_mfma_f32_16x16x32_bf16 v[32:35], a[32:35], v[16:19], v[32:35]\n"
        "s_nop 0\n"
        "ds_read_b128 v[16:19], v0 offset:6144\n"
        "v_mfma_f32_16x16x32_bf16 v[24:27], a[36:39], v[20:23], v[24:27]\n"
        "v_permlane16_swap_b32 v44, v46\n"
        "v_mfma_f32_16x16x32_bf16 v[28:31], a[40:43], v[20:23], v[28:31]\n"
        "v_permlane16_swap_b32 v45, v47\n"
        "v_mfma_f32_16x16x32_bf16 v[32:35], a[44:47], v[20:23], v[32:35]\n"
        "s_waitcnt lgkmcnt(1)\n"
        "ds_read_b128 v[20:23], v0 offset:7168\n"
        "v_mfma_f32_16x16x32_bf16 v[24:27], a[48:51], v[8:11], v[24:27]\n"
        "v_mfma_f32_16x16x32_bf16 v[28:31], a[52:55], v[8:11], v[28:31]\n"
        "buffer_store_dwordx4 v[44:47], v7, s[40:43], s72 offen offset:0 sc1\n"
        "v_mfma_f32_16x16x32_bf16 v[32:35], a[56:59], v[8:11], v[32:35]\n"
        "ds_read_b128 v[8:11], v0 offset:8192\n"
        "v_mfma_f32_16x16x32_bf16 v[24:27], a[60:63], v[12:15], v[24:27]\n"
        "v_mfma_f32_16x16x32_bf16 v[28:31], a[64:67], v[12:15], v[28:31]\n"
        "v_mfma_f32_16x16x32_bf16 v[32:35], a[68:71], v[12:15], v[32:35]\n"
        "s_waitcnt lgkmcnt(1)\n"
        "ds_read_b128 v[12:15], v0 offset:9216\n"
        "v_mfma_f32_16x16x32_bf16 v[24:27], a[72:75], v[16:19], v[24:27]\n"
        "v_mfma_f32_16x16x32_bf16 v[28:31], a[76:79], v[16:19], v[28:31]\n"
        "s_waitcnt vmcnt(2)\n"
        "v_cmp_gt_i32 vcc, s70, v4\n"
        "v_mfma_f32_16x16x32_bf16 v[32:35], a[80:83], v[16:19], v[32:35]\n"
        "ds_read_b128 v[16:19], v0 offset:10240\n"
        "v_mfma_f32_16x16x32_bf16 v[24:27], a[84:87], v[20:23], v[24:27]\n"
        "s_and_b64 vcc, vcc, 3\n"
        "v_mfma_f32_16x16x32_bf16 v[28:31], a[88:91], v[20:23], v[28:31]\n"
        "s_cbranch_vccnz .Lop_i00_%=\n"
        "v_mfma_f32_16x16x32_bf16 v[32:35], a[92:95], v[20:23], v[32:35]\n"
        "s_waitcnt lgkmcnt(1)\n"
        "ds_read_b128 v[20:23], v0 offset:11264\n"
        "buffer_load_dwordx4 v1, s[40:43], s71 offen offset:0 sc1 lds\n"
        "v_mfma_f32_16x16x32_bf16 v[24:27], a[96:99], v[8:11], v[24:27]\n"
        "v_mfma_f32_16x16x32_bf16 v[28:31], a[100:103], v[8:11], v[28:31]\n"
        "v_mfma_f32_16x16x32_bf16 v[32:35], a[104:107], v[8:11], v[32:35]\n"
        "ds_read_b128 v[8:11], v0 offset:12288\n"
        "v_mfma_f32_16x16x32_bf16 v[24:27], a[108:111], v[12:15], v[24:27]\n"
        "buffer_load_dwordx4 v1, s[40:43], s71 offen offset:1024 sc1 lds\n"
        "v_mfma_f32_16x16x32_bf16 v[28:31], a[112:115], v[12:15], v[28:31]\n"
        "v_mfma_f32_16x16x32_bf16 v[32:35], a[116:119], v[12:15], v[32:35]\n"
        "s_waitcnt lgkmcnt(1)\n"
        "ds_read_b128 v[12:15], v0 offset:13312\n"
        "v_mfma_f32_16x16x32_bf16 v[24:27], a[120:123], v[16:19], v[24:27]\n"
        "v_mfma_f32_16x16x32_bf16 v[28:31], a[124:127], v[16:19], v[28:31]\n"
        "buffer_load_dwordx4 v1, s[40:43], s71 offen offset:2048 sc1 lds\n"
        "v_mfma_f32_16x16x32_bf16 v[32:35], a[128:131], v[16:19], v[32:35]\n"
        "ds_read_b128 v[16:19], v0 offset:14336\n"
        "v_mfma_f32_16x16x32_bf16 v[24:27], a[132:135], v[20:23], v[24:27]\n"
        "v_mfma_f32_16x16x32_bf16 v[28:31], a[136:139], v[20:23], v[28:31]\n"
        "v_mfma_f32_16x16x32_bf16 v[32:35], a[140:143], v[20:23], v[32:35]\n"
        "s_waitcnt lgkmcnt(1)\n"
        "ds_read_b128 v[20:23], v0 offset:15360\n"
        "buffer_load_dwordx4 v1, s[40:43], s71 offen offset:3072 sc1 lds\n"
        "v_mfma_f32_16x16x32_bf16 v[24:27], a[144:147], v[8:11], v[24:27]\n"
        "s_add_u32 m0, m0, 16384\n"
        "s_add_u32 s71, s71, 65536\n"
        "v_mfma_f32_16x16x32_bf16 v[28:31], a[148:151], v[8:11], v[28:31]\n"
        "s_barrier\n"
        "v_mfma_f32_16x16x32_bf16 v[32:35], a[152:155], v[8:11], v[32:35]\n"
        "ds_read_b128 v[8:11], v0 offset:16384\n"
        "buffer_store_dword v5, v3, s[40:43], s84 offen sc1\n"
        "v_mfma_f32_16x16x32_bf16 v[24:27], a[156:159], v[12:15], v[24:27]\n"
        "v_mov_b32 v5, s74\n"
        "buffer_load_dwordx4 v1, s[40:43], s71 offen offset:0 sc1 lds\n"
        "v_mfma_f32_16x16x32_bf16 v[28:31], a[160:163], v[12:15], v[28:31]\n"
        "s_getreg_b32 s88, hwreg(HW_REG_IB_STS, 0, 4)\n"
        "v_mfma_f32_16x16x32_bf16 v[32:35], a[164:167], v[12:15], v[32:35]\n"
        "s_waitcnt lgkmcnt(1)\n"
        "ds_read_b128 v[12:15], v0 offset:17408\n"
        "v_mfma_f32_16x16x32_bf16 v[24:27], a[168:171], v[16:19], v[24:27]\n"
        "s_cmp_le_u32 s88, 6\n"
        "s_cselect_b32 s88, s73, s89\n"
        "s_cselect_b32 s73, s89, s73\n"
        "v_mfma_f32_16x16x32_bf16 v[28:31], a[172:175], v[16:19], v[28:31]\n"
        "buffer_store_dword v5, v3, s[40:43], s88 offen sc1\n"
        "buffer_load_dwordx4 v1, s[40:43], s71 offen offset:1024 sc1 lds\n"
        "v_mfma_f32_16x16x32_bf16 v[32:35], a[176:179], v[16:19], v[32:35]\n"
        "ds_read_b128 v[16:19], v0 offset:18432\n"
        "v_mfma_f32_16x16x32_bf16 v[24:27], a[180:183], v[20:23], v[24:27]\n"
        "s_getreg_b32 s88, hwreg(HW_REG_IB_STS, 0, 4)\n"
        "v_mfma_f32_16x16x32_bf16 v[28:31], a[184:187], v[20:23], v[28:31]\n"
        "s_cmp_le_u32 s88, 8\n"
        "s_cselect_b32 s88, s73, s89\n"
        "s_cselect_b32 s73, s89, s73\n"
        "v_mfma_f32_16x16x32_bf16 v[32:35], a[188:191], v[20:23], v[32:35]\n"
        "s_waitcnt lgkmcnt(1)\n"
        "ds_read_b128 v[20:23], v0 offset:19456\n"
        "buffer_store_dword v5, v3, s[40:43], s88 offen sc1\n"
        "v_mfma_f32_16x16x32_bf16 v[24:27], a[192:195], v[8:11], v[24:27]\n"
        "buffer_load_dwordx4 v1, s[40:43], s71 offen offset:2048 sc1 lds\n"
        "v_mfma_f32_16x16x32_bf16 v[28:31], a[196:199], v[8:11], v[28:31]\n"
        "v_mfma_f32_16x16x32_bf16 v[32:35], a[200:203], v[8:11], v[32:35]\n"
        "ds_read_b128 v[8:11], v0 offset:20480\n"
        "v_mfma_f32_16x16x32_bf16 v[24:27], a[204:207], v[12:15], v[24:27]\n"
        "buffer_load_dwordx4 v1, s[40:43], s71 offen offset:3072 sc1 lds\n"
        "s_add_u32 s69, s69, 1\n"
        "v_mfma_f32_16x16x32_bf16 v[28:31], a[208:211], v[12:15], v[28:31]\n"
        "v_mfma_f32_16x16x32_bf16 v[32:35], a[212:215], v[12:15], v[32:35]\n"
        "s_waitcnt lgkmcnt(1)\n"
        "ds_read_b128 v[12:15], v0 offset:21504\n"
        "v_mfma_f32_16x16x32_bf16 v[24:27], a[216:219], v[16:19], v[24:27]\n"
        "s_getreg_b32 s88, hwreg(HW_REG_IB_STS, 0, 4)\n"
        "v_mfma_f32_16x16x32_bf16 v[28:31], a[220:223], v[16:19], v[28:31]\n"
        "s_cmp_le_u32 s88, 11\n"
        "s_cselect_b32 s88, s73, s89\n"
        "s_cselect_b32 s73, s89, s73\n"
        "v_mfma_f32_16x16x32_bf16 v[32:35], a[224:227], v[16:19], v[32:35]\n"
        "ds_read_b128 v[16:19], v0 offset:22528\n"
        "buffer_store_dword v5, v3, s[40:43], s88 offen sc1\n"
        "v_mfma_f32_16x16x32_bf16 v[24:27], a[228:231], v[20:23], v[24:27]\n"
        "v_mfma_f32_16x16x32_bf16 v[28:31], a[232:235], v[20:23], v[28:31]\n"
        "v_mfma_f32_16x16x32_bf16 v[32:35], a[236:239], v[20:23], v[32:35]\n"
        "s_waitcnt lgkmcnt(1)\n"
        "ds_read_b128 v[20:23], v0 offset:23552\n"
        "v_mfma_f32_16x16x32_bf16 v[24:27], a[240:243], v[8:11], v[24:27]\n"
        "v_mfma_f32_16x16x32_bf16 v[28:31], a[244:247], v[8:11], v[28:31]\n"
        "v_mfma_f32_16x16x32_bf16 v[32:35], a[248:251], v[8:11], v[32:35]\n"
        "ds_read_b128 v[8:11], v0 offset:24576\n"
        "v_mfma_f32_16x16x32_bf16 v[24:27], a[252:255], v[12:15], v[24:27]\n"
        "v_mfma_f32_16x16x32_bf16 v[28:31], v[80:83], v[12:15], v[28:31]\n"
        "v_mfma_f32_16x16x32_bf16 v[32:35], v[84:87], v[12:15], v[32:35]\n"
        "s_waitcnt lgkmcnt(1)\n"
        "ds_read_b128 v[12:15], v0 offset:25600\n"
        "v_mfma_f32_16x16x32_bf16 v[24:27], v[88:91], v[16:19], v[24:27]\n"
        "v_mfma_f32_16x16x32_bf16 v[28:31], v[92:95], v[16:19], v[28:31]\n"
        "s_waitcnt vmcnt(12)\n"
        "buffer_store_dword v5, v3, s[40:43], s73 offen sc1\n"
        "s_add_u32 s74, s74, 1\n"
        "v_mfma_f32_16x16x32_bf16 v[32:35], v[96:99], v[16:19], v[32:35]\n"
        "s_mov_b32 s84, s89\n"
        "ds_read_b128 v[16:19], v0 offset:26624\n"
        "s_mov_b32 s73, s60\n"
        "v_mfma_f32_16x16x32_bf16 v[24:27], v[100:103], v[20:23], v[24:27]\n"
        "s_add_u32 s68, s68, 1\n"
        "s_lshr_b32 s66, s68, 1\n"
        "v_mfma_f32_16x16x32_bf16 v[28:31], v[104:107], v[20:23], v[28:31]\n"
        "s_bitcmp1_b32 s54, s66\n"
        "s_cselect_b32 s66, 8192, 0\n"
        "s_lshl_b32 s75, s68, 20\n"
        "v_mfma_f32_16x16x32_bf16 v[32:35], v[108:111], v[20:23], v[32:35]\n"
        "s_waitcnt lgkmcnt(1)\n"
        "ds_read_b128 v[20:23], v0 offset:27648\n"
        "s_add_u32 s75, s75, s66\n"
        "v_mfma_f32_16x16x32_bf16 v[24:27], v[112:115], v[8:11], v[24:27]\n"
        "s_add_u32 s72, s75, s59\n"
        "v_mfma_f32_16x16x32_bf16 v[28:31], v[116:119], v[8:11], v[28:31]\n"
        "v_mfma_f32_16x16x32_bf16 v[32:35], v[120:123], v[8:11], v[32:35]\n"
        "ds_read_b128 v[8:11], v0 offset:28672\n"
        "v_mfma_f32_16x16x32_bf16 v[24:27], v[124:127], v[12:15], v[24:27]\n"
        "v_mfma_f32_16x16x32_bf16 v[28:31], v[128:131], v[12:15], v[28:31]\n"
        "v_mfma_f32_16x16x32_bf16 v[32:35], v[132:135], v[12:15], v[32:35]\n"
        "s_waitcnt lgkmcnt(1)\n"
        "ds_read_b128 v[12:15], v0 offset:29696\n"
        "v_mfma_f32_16x16x32_bf16 v[24:27], v[136:139], v[16:19], v[24:27]\n"
        "v_mfma_f32_16x16x32_bf16 v[28:31], v[140:143], v[16:19], v[28:31]\n"
        "v_mfma_f32_16x16x32_bf16 v[32:35], v[144:147], v[16:19], v[32:35]\n"
        "ds_read_b128 v[16:19], v0 offset:30720\n"
        "v_mfma_f32_16x16x32_bf16 v[24:27], v[148:151], v[20:23], v[24:27]\n"
        "v_mfma_f32_16x16x32_bf16 v[28:31], v[152:155], v[20:23], v[28:31]\n"
        "v_mfma_f32_16x16x32_bf16 v[32:35], v[156:159], v[20:23], v[32:35]\n"
        "s_waitcnt lgkmcnt(1)\n"
        "ds_read_b128 v[20:23], v0 offset:31744\n"
        "v_mfma_f32_16x16x32_bf16 v[24:27], v[160:163], v[8:11], v[24:27]\n"
        "v_mfma_f32_16x16x32_bf16 v[28:31], v[164:167], v[8:11], v[28:31]\n"
        "s_waitcnt vmcnt(9)\n"
        "s_barrier\n"
        "v_add_u32 v0, s78, v6\n"
        "v_mfma_f32_16x16x32_bf16 v[32:35], v[168:171], v[8:11], v[32:35]\n"
        "ds_read_b128 v[8:11], v0 offset:0\n"
        "v_mfma_f32_16x16x32_bf16 v[24:27], v[172:175], v[12:15], v[24:27]\n"
        "v_mfma_f32_16x16x32_bf16 v[28:31], v[176:179], v[12:15], v[28:31]\n"
        "v_mfma_f32_16x16x32_bf16 v[32:35], v[180:183], v[12:15], v[32:35]\n"
        "s_waitcnt lgkmcnt(1)\n"
        "ds_read_b128 v[12:15], v0 offset:1024\n"
        "v_mfma_f32_16x16x32_bf16 v[24:27], v[184:187], v[16:19], v[24:27]\n"
        "v_mfma_f32_16x16x32_bf16 v[28:31], v[188:191], v[16:19], v[28:31]\n"
        "v_mfma_f32_16x16x32_bf16 v[32:35], v[192:195], v[16:19], v[32:35]\n"
        "ds_read_b128 v[16:19], v0 offset:2048\n"
        "v_mfma_f32_16x16x32_bf16 v[24:27], v[196:199], v[20:23], v[24:27]\n"
        "v_mfma_f32_16x16x32_bf16 v[28:31], v[200:203], v[20:23], v[28:31]\n"
        "v_mfma_f32_16x16x32_bf16 v[32:35], v[204:207], v[20:23], v[32:35]\n"
        "s_waitcnt lgkmcnt(1)\n"
        "ds_read_b128 v[20:23], v0 offset:3072\n"
        "s_branch .Lop_i1_%=\n"
        ".Lop_i00_%=:\n"
        "v_mfma_f32_16x16x32_bf16 v[32:35], a[92:95], v[20:23], v[32:35]\n"
        "s_waitcnt lgkmcnt(1)\n"
        "ds_read_b128 v[20:23], v0 offset:11264\n"
        "buffer_load_dword v4, v2, s[40:43], s86 offen sc1\n"
        "v_mfma_f32_16x16x32_bf16 v[24:27], a[96:99], v[8:11], v[24:27]\n"
        "v_mfma_f32_16x16x32_bf16 v[28:31], a[100:103], v[8:11], v[28:31]\n"
        "v_mfma_f32_16x16x32_bf16 v[32:35], a[104:107], v[8:11], v[32:35]\n"
        "ds_read_b128 v[8:11], v0 offset:12288\n"
        "v_mfma_f32_16x16x32_bf16 v[24:27], a[108:111], v[12:15], v[24:27]\n"
        "v_mfma_f32_16x16x32_bf16 v[28:31], a[112:115], v[12:15], v[28:31]\n"
        "v_mfma_f32_16x16x32_bf16 v[32:35], a[116:119], v[12:15], v[32:35]\n"
        "s_waitcnt lgkmcnt(1)\n"
        "ds_read_b128 v[12:15], v0 offset:13312\n"
        "v_mfma_f32_16x16x32_bf16 v[24:27], a[120:123], v[16:19], v[24:27]\n"
        "v_mfma_f32_16x16x32_bf16 v[28:31], a[124:127], v[16:19], v[28:31]\n"
        "v_mfma_f32_16x16x32_bf16 v[32:35], a[128:131], v[16:19], v[32:35]\n"
        "ds_read_b128 v[16:19], v0 offset:14336\n"
        "v_mfma_f32_16x16x32_bf16 v[24:27], a[132:135], v[20:23], v[24:27]\n"
        "v_mfma_f32_16x16x32_bf16 v[28:31], a[136:139], v[20:23], v[28:31]\n"
        "v_mfma_f32_16x16x32_bf16 v[32:35], a[140:143], v[20:23], v[32:35]\n"
        "s_waitcnt lgkmcnt(1)\n"
        "ds_read_b128 v[20:23], v0 offset:15360\n"
        "v_mfma_f32_16x16x32_bf16 v[24:27], a[144:147], v[8:11], v[24:27]\n"
        "v_mfma_f32_16x16x32_bf16 v[28:31], a[148:151], v[8:11], v[28:31]\n"
        "s_barrier\n"
        "v_mfma_f32_16x16x32_bf16 v[32:35], a[152:155], v[8:11], v[32:35]\n"
        "ds_read_b128 v[8:11], v0 offset:16384\n"
        "buffer_store_dword v5, v3, s[40:43], s84 offen sc1\n"
        "v_mfma_f32_16x16x32_bf16 v[24:27], a[156:159], v[12:15], v[24:27]\n"
        "v_mov_b32 v5, s74\n"
        "v_mfma_f32_16x16x32_bf16 v[28:31], a[160:163], v[12:15], v[28:31]\n"
        "s_getreg_b32 s88, hwreg(HW_REG_IB_STS, 0, 4)\n"
        "v_mfma_f32_16x16x32_bf16 v[32:35], a[164:167], v[12:15], v[32:35]\n"
        "s_waitcnt lgkmcnt(1)\n"
        "ds_read_b128 v[12:15], v0 offset:17408\n"
        "v_mfma_f32_16x16x32_bf16 v[24:27], a[168:171], v[16:19], v[24:27]\n"
        "s_cmp_le_u32 s88, 2\n"
        "s_cselect_b32 s88, s73, s89\n"
        "s_cselect_b32 s73, s89, s73\n"
        "v_mfma_f32_16x16x32_bf16 v[28:31], a[172:175], v[16:19], v[28:31]\n"
        "buffer_store_dword v5, v3, s[40:43], s88 offen sc1\n"
        "v_mfma_f32_16x16x32_bf16 v[32:35], a[176:179], v[16:19], v[32:35]\n"
        "ds_read_b128 v[16:19], v0 offset:18432\n"
        "v_mfma_f32_16x16x32_bf16 v[24:27], a[180:183], v[20:23], v[24:27]\n"
        "s_getreg_b32 s88, hwreg(HW_REG_IB_STS, 0, 4)\n"
        "v_mfma_f32_16x16x32_bf16 v[28:31], a[184:187], v[20:23], v[28:31]\n"
        "s_cmp_le_u32 s88, 3\n"
        "s_cselect_b32 s88, s73, s89\n"
        "s_cselect_b32 s73, s89, s73\n"
        "v_mfma_f32_16x16x32_bf16 v[32:35], a[188:191], v[20:23], v[32:35]\n"
        "s_waitcnt lgkmcnt(1)\n"
        "ds_read_b128 v[20:23], v0 offset:19456\n"
        "buffer_store_dword v5, v3, s[40:43], s88 offen sc1\n"
        "v_mfma_f32_16x16x32_bf16 v[24:27], a[192:195], v[8:11], v[24:27]\n"
        "v_mfma_f32_16x16x32_bf16 v[28:31], a[196:199], v[8:11], v[28:31]\n"
        "s_waitcnt vmcnt(3)\n"
        "v_cmp_gt_i32 vcc, s70, v4\n"
        "v_mfma_f32_16x16x32_bf16 v[32:35], a[200:203], v[8:11], v[32:35]\n"
        "ds_read_b128 v[8:11], v0 offset:20480\n"
        "v_mfma_f32_16x16x32_bf16 v[24:27], a[204:207], v[12:15], v[24:27]\n"
        "s_and_b64 vcc, vcc, 3\n"
        "v_mfma_f32_16x16x32_bf16 v[28:31], a[208:211], v[12:15], v[28:31]\n"
        "s_cbranch_vccnz .Lop_i01_%=\n"
        "v_mfma_f32_16x16x32_bf16 v[32:35], a[212:215], v[12:15], v[32:35]\n"
        "s_waitcnt lgkmcnt(1)\n"
        "ds_read_b128 v[12:15], v0 offset:21504\n"
        "buffer_load_dwordx4 v1, s[40:43], s71 offen offset:0 sc1 lds\n"
        "v_mfma_f32_16x16x32_bf16 v[24:27], a[216:219], v[16:19], v[24:27]\n"
        "s_getreg_b32 s88, hwreg(HW_REG_IB_STS, 0, 4)\n"
        "v_mfma_f32_16x16x32_bf16 v[28:31], a[220:223], v[16:19], v[28:31]\n"
        "s_cmp_le_u32 s88, 15\n"
        "s_cselect_b32 s88, s73, s89\n"
        "s_cselect_b32 s73, s89, s73\n"
        "v_mfma_f32_16x16x32_bf16 v[32:35], a[224:227], v[16:19], v[32:35]\n"
        "ds_read_b128 v[16:19], v0 offset:22528\n"
        "buffer_store_dword v5, v3, s[40:43], s88 offen sc1\n"
        "v_mfma_f32_16x16x32_bf16 v[24:27], a[228:231], v[20:23], v[24:27]\n"
        "buffer_load_dwordx4 v1, s[40:43], s71 offen offset:1024 sc1 lds\n"
        "v_mfma_f32_16x16x32_bf16 v[28:31], a[232:235], v[20:23], v[28:31]\n"
        "v_mfma_f32_16x16x32_bf16 v[32:35], a[236:239], v[20:23], v[32:35]\n"
        "s_waitcnt lgkmcnt(1)\n"
        "ds_read_b128 v[20:23], v0 offset:23552\n"
        "buffer_load_dwordx4 v1, s[40:43], s71 offen offset:2048 sc1 lds\n"
        "v_mfma_f32_16x16x32_bf16 v[24:27], a[240:243], v[8:11], v[24:27]\n"
        "v_mfma_f32_16x16x32_bf16 v[28:31], a[244:247], v[8:11], v[28:31]\n"
        "v_mfma_f32_16x16x32_bf16 v[32:35], a[248:251], v[8:11], v[32:35]\n"
        "ds_read_b128 v[8:11], v0 offset:24576\n"
        "buffer_load_dwordx4 v1, s[40:43], s71 offen offset:3072 sc1 lds\n"
        "v_mfma_f32_16x16x32_bf16 v[24:27], a[252:255], v[12:15], v[24:27]\n"
        "s_add_u32 m0, m0, 16384\n"
        "s_add_u32 s71, s71, 65536\n"
        "v_mfma_f32_16x16x32_bf16 v[28:31], v[80:83], v[12:15], v[28:31]\n"
        "v_mfma_f32_16x16x32_bf16 v[32:35], v[84:87], v[12:15], v[32:35]\n"
        "s_waitcnt lgkmcnt(1)\n"
        "ds_read_b128 v[12:15], v0 offset:25600\n"
        "buffer_load_dwordx4 v1, s[40:43], s71 offen offset:0 sc1 lds\n"
        "v_mfma_f32_16x16x32_bf16 v[24:27], v[88:91], v[16:19], v[24:27]\n"
        "v_mfma_f32_16x16x32_bf16 v[28:31], v[92:95], v[16:19], v[28:31]\n"
        "buffer_store_dword v5, v3, s[40:43], s73 offen sc1\n"
        "s_add_u32 s74, s74, 1\n"
        "v_mfma_f32_16x16x32_bf16 v[32:35], v[96:99], v[16:19], v[32:35]\n"
        "s_mov_b32 s84, s89\n"
        "ds_read_b128 v[16:19], v0 offset:26624\n"
        "s_mov_b32 s73, s60\n"
        "v_mfma_f32_16x16x32_bf16 v[24:27], v[100:103], v[20:23], v[24:27]\n"
        "s_add_u32 s68, s68, 1\n"
        "s_lshr_b32 s66, s68, 1\n"
        "v_mfma_f32_16x16x32_bf16 v[28:31], v[104:107], v[20:23], v[28:31]\n"
        "s_bitcmp1_b32 s54, s66\n"
        "s_cselect_b32 s66, 8192, 0\n"
        "s_lshl_b32 s75, s68, 20\n"
        "v_mfma_f32_16x16x32_bf16 v[32:35], v[108:111], v[20:23], v[32:35]\n"
        "s_waitcnt lgkmcnt(1)\n"
        "ds_read_b128 v[20:23], v0 offset:27648\n"
        "s_add_u32 s75, s75, s66\n"
        "v_mfma_f32_16x16x32_bf16 v[24:27], v[112:115], v[8:11], v[24:27]\n"
        "s_add_u32 s72, s75, s59\n"
        "v_mfma_f32_16x16x32_bf16 v[28:31], v[116:119], v[8:11], v[28:31]\n"
        "buffer_load_dwordx4 v1, s[40:43], s71 offen offset:1024 sc1 lds\n"
        "v_mfma_f32_16x16x32_bf16 v[32:35], v[120:123], v[8:11], v[32:35]\n"
        "ds_read_b128 v[8:11], v0 offset:28672\n"
        "v_mfma_f32_16x16x32_bf16 v[24:27], v[124:127], v[12:15], v[24:27]\n"
        "v_mfma_f32_16x16x32_bf16 v[28:31], v[128:131], v[12:15], v[28:31]\n"
        "v_mfma_f32_16x16x32_bf16 v[32:35], v[132:135], v[12:15], v[32:35]\n"
        "s_waitcnt lgkmcnt(1)\n"
        "ds_read_b128 v[12:15], v0 offset:29696\n"
        "buffer_load_dwordx4 v1, s[40:43], s71 offen offset:2048 sc1 lds\n"
        "v_mfma_f32_16x16x32_bf16 v[24:27], v[136:139], v[16:19], v[24:27]\n"
        "v_mfma_f32_16x16x32_bf16 v[28:31], v[140:143], v[16:19], v[28:31]\n"
        "v_mfma_f32_16x16x32_bf16 v[32:35], v[144:147], v[16:19], v[32:35]\n"
        "ds_read_b128 v[16:19], v0 offset:30720\n"
        "v_mfma_f32_16x16x32_bf16 v[24:27], v[148:151], v[20:23], v[24:27]\n"
        "buffer_load_dwordx4 v1, s[40:43], s71 offen offset:3072 sc1 lds\n"
        "s_add_u32 s69, s69, 1\n"
        "v_mfma_f32_16x16x32_bf16 v[28:31], v[152:155], v[20:23], v[28:31]\n"
        "v_mfma_f32_16x16x32_bf16 v[32:35], v[156:159], v[20:23], v[32:35]\n"
        "s_waitcnt lgkmcnt(1)\n"
        "ds_read_b128 v[20:23], v0 offset:31744\n"
        "v_mfma_f32_16x16x32_bf16 v[24:27], v[160:163], v[8:11], v[24:27]\n"
        "v_mfma_f32_16x16x32_bf16 v[28:31], v[164:167], v[8:11], v[28:31]\n"
        "s_waitcnt vmcnt(5)\n"
        "s_barrier\n"
        "v_add_u32 v0, s78, v6\n"
        "v_mfma_f32_16x16x32_bf16 v[32:35], v[168:171], v[8:11], v[32:35]\n"
        "ds_read_b128 v[8:11], v0 offset:0\n"
        "v_mfma_f32_16x16x32_bf16 v[24:27], v[172:175], v[12:15], v[24:27]\n"
        "v_mfma_f32_16x16x32_bf16 v[28:31], v[176:179], v[12:15], v[28:31]\n"
        "v_mfma_f32_16x16x32_bf16 v[32:35], v[180:183], v[12:15], v[32:35]\n"
        "s_waitcnt lgkmcnt(1)\n"
        "ds_read_b128 v[12:15], v0 offset:1024\n"
        "v_mfma_f32_16x16x32_bf16 v[24:27], v[184:187], v[16:19], v[24:27]\n"
        "v_mfma_f32_16x16x32_bf16 v[28:31], v[188:191], v[16:19], v[28:31]\n"
        "v_mfma_f32_16x16x32_bf16 v[32:35], v[192:195], v[16:19], v[32:35]\n"
        "ds_read_b128 v[16:19], v0 offset:2048\n"
        "v_mfma_f32_16x16x32_bf16 v[24:27], v[196:199], v[20:23], v[24:27]\n"
        "v_mfma_f32_16x16x32_bf16 v[28:31], v[200:203], v[20:23], v[28:31]\n"
        "v_mfma_f32_16x16x32_bf16 v[32:35], v[204:207], v[20:23], v[32:35]\n"
        "s_waitcnt lgkmcnt(1)\n"
        "ds_read_b128 v[20:23], v0 offset:3072\n"
        "s_branch .Lop_i1_%=\n"
        ".Lop_i01_%=:\n"
        "v_mfma_f32_16x16x32_bf16 v[32:35], a[212:215], v[12:15], v[32:35]\n"
        "s_waitcnt lgkmcnt(1)\n"
        "ds_read_b128 v[12:15], v0 offset:21504\n"
        "buffer_load_dword v4, v2, s[40:43], s86 offen sc1\n"
        "v_mfma_f32_16x16x32_bf16 v[24:27], a[216:219], v[16:19], v[24:27]\n"
        "s_getreg_b32 s88, hwreg(HW_REG_IB_STS, 0, 4)\n"
        "v_mfma_f32_16x16x32_bf16 v[28:31], a[220:223], v[16:19], v[28:31]\n"
        "s_cmp_le_u32 s88, 15\n"
        "s_cselect_b32 s88, s73, s89\n"
        "s_cselect_b32 s73, s89, s73\n"
        "v_mfma_f32_16x16x32_bf16 v[32:35], a[224:227], v[16:19], v[32:35]\n"
        "ds_read_b128 v[16:19], v0 offset:22528\n"
        "buffer_store_dword v5, v3, s[40:43], s88 offen sc1\n"
        "v_mfma_f32_16x16x32_bf16 v[24:27], a[228:231], v[20:23], v[24:27]\n"
        "v_mfma_f32_16x16x32_bf16 v[28:31], a[232:235], v[20:23], v[28:31]\n"
        "v_mfma_f32_16x16x32_bf16 v[32:35], a[236:239], v[20:23], v[32:35]\n"
        "s_waitcnt lgkmcnt(1)\n"
        "ds_read_b128 v[20:23], v0 offset:23552\n"
        "v_mfma_f32_16x16x32_bf16 v[24:27], a[240:243], v[8:11], v[24:27]\n"
        "v_mfma_f32_16x16x32_bf16 v[28:31], a[244:247], v[8:11], v[28:31]\n"
        "v_mfma_f32_16x16x32_bf16 v[32:35], a[248:251], v[8:11], v[32:35]\n"
        "ds_read_b128 v[8:11], v0 offset:24576\n"
        "v_mfma_f32_16x16x32_bf16 v[24:27], a[252:255], v[12:15], v[24:27]\n"
        "v_mfma_f32_16x16x32_bf16 v[28:31], v[80:83], v[12:15], v[28:31]\n"
        "v_mfma_f32_16x16x32_bf16 v[32:35], v[84:87], v[12:15], v[32:35]\n"
        "s_waitcnt lgkmcnt(1)\n"
        "ds_read_b128 v[12:15], v0 offset:25600\n"
        "v_mfma_f32_16x16x32_bf16 v[24:27], v[88:91], v[16:19], v[24:27]\n"
        "v_mfma_f32_16x16x32_bf16 v[28:31], v[92:95], v[16:19], v[28:31]\n"
        "buffer_store_dword v5, v3, s[40:43], s73 offen sc1\n"
        "s_add_u32 s74, s74, 1\n"
        "v_mfma_f32_16x16x32_bf16 v[32:35], v[96:99], v[16:19], v[32:35]\n"
        "s_mov_b32 s84, s89\n"
        "ds_read_b128 v[16:19], v0 offset:26624\n"
        "s_mov_b32 s73, s60\n"
        "v_mfma_f32_16x16x32_bf16 v[24:27], v[100:103], v[20:23], v[24:27]\n"
        "s_add_u32 s68, s68, 1\n"
        "s_lshr_b32 s66, s68, 1\n"
        "v_mfma_f32_16x16x32_bf16 v[28:31], v[104:107], v[20:23], v[28:31]\n"
        "s_bitcmp1_b32 s54, s66\n"
        "s_cselect_b32 s66, 8192, 0\n"
        "s_lshl_b32 s75, s68, 20\n"
        "v_mfma_f32_16x16x32_bf16 v[32:35], v[108:111], v[20:23], v[32:35]\n"
        "s_waitcnt lgkmcnt(1)\n"
        "ds_read_b128 v[20:23], v0 offset:27648\n"
        "s_add_u32 s75, s75, s66\n"
        "v_mfma_f32_16x16x32_bf16 v[24:27], v[112:115], v[8:11], v[24:27]\n"
        "s_add_u32 s72, s75, s59\n"
        "v_mfma_f32_16x16x32_bf16 v[28:31], v[116:119], v[8:11], v[28:31]\n"
        "v_mfma_f32_16x16x32_bf16 v[32:35], v[120:123], v[8:11], v[32:35]\n"
        "ds_read_b128 v[8:11], v0 offset:28672\n"
        "v_mfma_f32_16x16x32_bf16 v[24:27], v[124:127], v[12:15], v[24:27]\n"
        "v_mfma_f32_16x16x32_bf16 v[28:31], v[128:131], v[12:15], v[28:31]\n"
        "v_mfma_f32_16x16x32_bf16 v[32:35], v[132:135], v[12:15], v[32:35]\n"
        "s_waitcnt lgkmcnt(1)\n"
        "ds_read_b128 v[12:15], v0 offset:29696\n"
        "v_mfma_f32_16x16x32_bf16 v[24:27], v[136:139], v[16:19], v[24:27]\n"
        "v_mfma_f32_16x16x32_bf16 v[28:31], v[140:143], v[16:19], v[28:31]\n"
        "v_mfma_f32_16x16x32_bf16 v[32:35], v[144:147], v[16:19], v[32:35]\n"
        "ds_read_b128 v[16:19], v0 offset:30720\n"
        "v_mfma_f32_16x16x32_bf16 v[24:27], v[148:151], v[20:23], v[24:27]\n"
        "s_waitcnt vmcnt(2)\n"
        "v_cmp_gt_i32 vcc, s70, v4\n"
        "v_mfma_f32_16x16x32_bf16 v[28:31], v[152:155], v[20:23], v[28:31]\n"
        "v_mfma_f32_16x16x32_bf16 v[32:35], v[156:159], v[20:23], v[32:35]\n"
        "s_waitcnt lgkmcnt(1)\n"
        "ds_read_b128 v[20:23], v0 offset:31744\n"
        "s_and_b64 vcc, vcc, 3\n"
        "v_mfma_f32_16x16x32_bf16 v[24:27], v[160:163], v[8:11], v[24:27]\n"
        "s_cbranch_vccnz .Lop_i02_%=\n"
        "v_mfma_f32_16x16x32_bf16 v[28:31], v[164:167], v[8:11], v[28:31]\n"
        "buffer_load_dwordx4 v1, s[40:43], s71 offen offset:0 sc1 lds\n"
        "v_mfma_f32_16x16x32_bf16 v[32:35], v[168:171], v[8:11], v[32:35]\n"
        "v_mfma_f32_16x16x32_bf16 v[24:27], v[172:175], v[12:15], v[24:27]\n"
        "buffer_load_dwordx4 v1, s[40:43], s71 offen offset:1024 sc1 lds\n"
        "v_mfma_f32_16x16x32_bf16 v[28:31], v[176:179], v[12:15], v[28:31]\n"
        "v_mfma_f32_16x16x32_bf16 v[32:35], v[180:183], v[12:15], v[32:35]\n"
        "s_waitcnt lgkmcnt(0)\n"
        "buffer_load_dwordx4 v1, s[40:43], s71 offen offset:2048 sc1 lds\n"
        "v_mfma_f32_16x16x32_bf16 v[24:27], v[184:187], v[16:19], v[24:27]\n"
        "v_mfma_f32_16x16x32_bf16 v[28:31], v[188:191], v[16:19], v[28:31]\n"
        "buffer_load_dwordx4 v1, s[40:43], s71 offen offset:3072 sc1 lds\n"
        "s_add_u32 m0, m0, 16384\n"
        "s_add_u32 s71, s71, 65536\n"
        "v_mfma_f32_16x16x32_bf16 v[32:35], v[192:195], v[16:19], v[32:35]\n"
        "v_mfma_f32_16x16x32_bf16 v[24:27], v[196:199], v[20:23], v[24:27]\n"
        "buffer_load_dwordx4 v1, s[40:43], s71 offen offset:0 sc1 lds\n"
        "v_mfma_f32_16x16x32_bf16 v[28:31], v[200:203], v[20:23], v[28:31]\n"
        "v_mfma_f32_16x16x32_bf16 v[32:35], v[204:207], v[20:23], v[32:35]\n"
        "buffer_load_dwordx4 v1, s[40:43], s71 offen offset:1024 sc1 lds\n"
        "buffer_load_dwordx4 v1, s[40:43], s71 offen offset:2048 sc1 lds\n"
        "buffer_load_dwordx4 v1, s[40:43], s71 offen offset:3072 sc1 lds\n"
        "s_add_u32 s69, s69, 1\n"
        "s_nop 2\n"
        "v_cvt_pk_bf16_f32 v24, v24, v25\n"
        "v_cvt_pk_bf16_f32 v25, v26, v27\n"
        "v_cvt_pk_bf16_f32 v26, v28, v29\n"
        "v_cvt_pk_bf16_f32 v27, v30, v31\n"
        "buffer_store_dwordx4 v[24:27], v48, s[40:43], s72 offen offset:0 sc1\n"
        "v_cvt_pk_bf16_f32 v32, v32, v33\n"
        "v_cvt_pk_bf16_f32 v33, v34, v35\n"
        "s_nop 0\n"
        "v_permlane16_swap_b32 v32, v34\n"
        "v_permlane16_swap_b32 v33, v35\n"
        "buffer_store_dwordx4 v[32:35], v7, s[40:43], s72 offen offset:0 sc1\n"
        "v_mov_b32 v5, s74\n"
        "s_mov_b32 s84, s60\n"
        "s_waitcnt vmcnt(6)\n"
        "s_branch .Lop_go0_%=\n"
        ".Lop_i02_%=:\n"
        "v_mfma_f32_16x16x32_bf16 v[28:31], v[164:167], v[8:11], v[28:31]\n"
        "buffer_load_dword v4, v2, s[40:43], s86 offen sc1\n"
        "v_mfma_f32_16x16x32_bf16 v[32:35], v[168:171], v[8:11], v[32:35]\n"
        "v_mfma_f32_16x16x32_bf16 v[24:27], v[172:175], v[12:15], v[24:27]\n"
        "v_mfma_f32_16x16x32_bf16 v[28:31], v[176:179], v[12:15], v[28:31]\n"
        "v_mfma_f32_16x16x32_bf16 v[32:35], v[180:183], v[12:15], v[32:35]\n"
        "s_waitcnt lgkmcnt(0)\n"
        "v_mfma_f32_16x16x32_bf16 v[24:27], v[184:187], v[16:19], v[24:27]\n"
        "v_mfma_f32_16x16x32_bf16 v[28:31], v[188:191], v[16:19], v[28:31]\n"
        "v_mfma_f32_16x16x32_bf16 v[32:35], v[192:195], v[16:19], v[32:35]\n"
        "v_mfma_f32_16x16x32_bf16 v[24:27], v[196:199], v[20:23], v[24:27]\n"
        "v_mfma_f32_16x16x32_bf16 v[28:31], v[200:203], v[20:23], v[28:31]\n"
        "v_mfma_f32_16x16x32_bf16 v[32:35], v[204:207], v[20:23], v[32:35]\n"
        "s_nop 2\n"
        "v_cvt_pk_bf16_f32 v24, v24, v25\n"
        "v_cvt_pk_bf16_f32 v25, v26, v27\n"
        "v_cvt_pk_bf16_f32 v26, v28, v29\n"
        "v_cvt_pk_bf16_f32 v27, v30, v31\n"
        "buffer_store_dwordx4 v[24:27], v48, s[40:43], s72 offen offset:0 sc1\n"
        "v_cvt_pk_bf16_f32 v32, v32, v33\n"
        "v_cvt_pk_bf16_f32 v33, v34, v35\n"
        "s_nop 0\n"
        "v_permlane16_swap_b32 v32, v34\n"
        "v_permlane16_swap_b32 v33, v35\n"
        "buffer_store_dwordx4 v[32:35], v7, s[40:43], s72 offen offset:0 sc1\n"
        "s_waitcnt vmcnt(2)\n"
        "v_cmp_gt_i32 vcc, s70, v4\n"
        "s_nop 0\n"
        "s_and_b64 vcc, vcc, 3\n"
        "s_cbranch_vccnz .Lop_late0_%=\n"
        "buffer_load_dwordx4 v1, s[40:43], s71 offen offset:0 sc1 lds\n"
        "buffer_load_dwordx4 v1, s[40:43], s71 offen offset:1024 sc1 lds\n"
        "buffer_load_dwordx4 v1, s[40:43], s71 offen offset:2048 sc1 lds\n"
        "buffer_load_dwordx4 v1, s[40:43], s71 offen offset:3072 sc1 lds\n"
        "s_add_u32 m0, m0, 16384\n"
        "s_add_u32 s71, s71, 65536\n"
        "buffer_load_dwordx4 v1, s[40:43], s71 offen offset:0 sc1 lds\n"
        "buffer_load_dwordx4 v1, s[40:43], s71 offen offset:1024 sc1 lds\n"
        "buffer_load_dwordx4 v1, s[40:43], s71 offen offset:2048 sc1 lds\n"
        "buffer_load_dwordx4 v1, s[40:43], s71 offen offset:3072 sc1 lds\n"
        "s_add_u32 s69, s69, 1\n"
        "s_waitcnt vmcnt(8)\n"
        "v_mov_b32 v5, s74\n"
        "buffer_store_dword v5, v3, s[40:43], s73 offen sc1\n"
        "s_waitcnt vmcnt(5)\n"
        "s_branch .Lop_go0_%=\n"
        ".Lop_late0_%=:\n"
        "s_waitcnt vmcnt(0)\n"
        "v_mov_b32 v5, s74\n"
        "buffer_store_dword v5, v3, s[40:43], s73 offen sc1\n"
        "s_mov_b32 s64, 0\n"
        ".Lop_in0_top_%=:\n"
        "buffer_load_dword v4, v2, s[40:43], s86 offen sc1\n"
        "s_waitcnt vmcnt(0)\n"
        "v_cmp_gt_i32 vcc, s70, v4\n"
        "s_nop 0\n"
        "s_and_b64 vcc, vcc, 3\n"
        "s_cbranch_vccz .Lop_in0_ok_%=\n"
        "s_add_u32 s64, s64, 1\n"
        "s_and_b32 s65, s64, 255\n"
        "s_cbranch_scc1 .Lop_in0_go_%=\n"
        "s_mov_b32 s65, 49152\n"
        "s_cmp_lt_u32 s64, 1048576\n"
        "s_cbranch_scc1 .Lop_in0_chk_%=\n"
        "v_mov_b32 v4, 1\n"
        "buffer_store_dword v4, off, s[40:43], s65 sc1\n"
        "s_waitcnt vmcnt(0)\n"
        ".Lop_in0_chk_%=:\n"
        "buffer_load_dword v4, off, s[40:43], s65 sc1\n"
        "s_waitcnt vmcnt(0)\n"
        "v_readfirstlane_b32 s65, v4\n"
        "s_nop 0\n"
        "s_cmp_lg_u32 s65, 1\n"
        "s_cbranch_scc1 .Lop_in0_go_%=\n"
        "s_endpgm\n"
        ".Lop_in0_go_%=:\n"
        "s_branch .Lop_in0_top_%=\n"
        ".Lop_in0_ok_%=:\n"
        "buffer_load_dwordx4 v1, s[40:43], s71 offen offset:0 sc1 lds\n"
        "buffer_load_dwordx4 v1, s[40:43], s71 offen offset:1024 sc1 lds\n"
        "buffer_load_dwordx4 v1, s[40:43], s71 offen offset:2048 sc1 lds\n"
        "buffer_load_dwordx4 v1, s[40:43], s71 offen offset:3072 sc1 lds\n"
        "s_add_u32 m0, m0, 16384\n"
        "s_add_u32 s71, s71, 65536\n"
        "buffer_load_dwordx4 v1, s[40:43], s71 offen offset:0 sc1 lds\n"
        "buffer_load_dwordx4 v1, s[40:43], s71 offen offset:1024 sc1 lds\n"
        "buffer_load_dwordx4 v1, s[40:43], s71 offen offset:2048 sc1 lds\n"
        "buffer_load_dwordx4 v1, s[40:43], s71 offen offset:3072 sc1 lds\n"
        "s_add_u32 s69, s69, 1\n"
        "s_waitcnt vmcnt(4)\n"
        ".Lop_go0_%=:\n"
        "s_mov_b32 s72, 241172480\n"
        "s_mov_b32 s83, 241172480\n"
        "s_mov_b32 s73, 241172480\n"
        "v_add_u32 v0, s78, v6\n"
        "s_barrier\n"
        "ds_read_b128 v[8:11], v0 offset:0\n"
        "ds_read_b128 v[12:15], v0 offset:1024\n"
        "ds_read_b128 v[16:19], v0 offset:2048\n"
        "ds_read_b128 v[20:23], v0 offset:3072\n"
        "s_waitcnt lgkmcnt(2)\n"
        "s_branch .Lop_i1_%=\n"
        ".Lop_i1_%=:\n"
        "v_mfma_f32_16x16x32_bf16 v[36:39], a[0:3], v[8:11], 0\n"
        "buffer_load_dword v4, v2, s[40:43], s86 offen sc1\n"
        "v_mfma_f32_16x16x32_bf16 v[40:43], a[4:7], v[8:11], 0\n"
        "s_lshr_b32 s67, s69, 1\n"
        "s_bitcmp1_b64 s[52:53], s67\n"
        "s_cselect_b32 s65, 8192, 0\n"
        "v_mfma_f32_16x16x32_bf16 v[44:47], a[8:11], v[8:11], 0\n"
        "s_lshl_b32 s77, s67, 21\n"
        "ds_read_b128 v[8:11], v0 offset:4096\n"
        "s_add_u32 s77, s77, s65\n"
        "v_mfma_f32_16x16x32_bf16 v[36:39], a[12:15], v[12:15], v[36:39]\n"
        "s_add_u32 s71, s77, s58\n"
        "s_add_u32 s78, s78, 32768\n"
        "v_cvt_pk_bf16_f32 v24, v24, v25\n"
        "v_mfma_f32_16x16x32_bf16 v[40:43], a[16:19], v[12:15], v[40:43]\n"
        "s_cmp_eq_u32 s78, 98304\n"
        "s_cselect_b32 s78, 0, s78\n"
        "s_add_u32 m0, s78, s61\n"
        "v_mfma_f32_16x16x32_bf16 v[44:47], a[20:23], v[12:15], v[44:47]\n"
        "s_waitcnt lgkmcnt(1)\n"
        "ds_read_b128 v[12:15], v0 offset:5120\n"
        "s_lshr_b32 s70, s69, 0\n"
        "v_mfma_f32_16x16x32_bf16 v[36:39], a[24:27], v[16:19], v[36:39]\n"
        "v_cvt_pk_bf16_f32 v25, v26, v27\n"
        "v_cvt_pk_bf16_f32 v26, v28, v29\n"
        "v_mfma_f32_16x16x32_bf16 v[40:43], a[28:31], v[16:19], v[40:43]\n"
        "v_cvt_pk_bf16_f32 v27, v30, v31\n"
        "v_mfma_f32_16x16x32_bf16 v[44:47], a[32:35], v[16:19], v[44:47]\n"
        "ds_read_b128 v[16:19], v0 offset:6144\n"
        "v_mfma_f32_16x16x32_bf16 v[36:39], a[36:39], v[20:23], v[36:39]\n"
        "buffer_store_dwordx4 v[24:27], v48, s[40:43], s72 offen offset:0 sc1\n"
        "v_mfma_f32_16x16x32_bf16 v[40:43], a[40:43], v[20:23], v[40:43]\n"
        "v_cvt_pk_bf16_f32 v32, v32, v33\n"
        "v_cvt_pk_bf16_f32 v33, v34, v35\n"
        "v_mfma_f32_16x16x32_bf16 v[44:47], a[44:47], v[20:23], v[44:47]\n"
        "s_waitcnt lgkmcnt(1)\n"
        "ds_read_b128 v[20:23], v0 offset:7168\n"
        "s_nop 0\n"
        "v_mfma_f32_16x16x32_bf16 v[36:39], a[48:51], v[8:11], v[36:39]\n"
        "v_permlane16_swap_b32 v32, v34\n"
        "v_mfma_f32_16x16x32_bf16 v[40:43], a[52:55], v[8:11], v[40:43]\n"
        "v_permlane16_swap_b32 v33, v35\n"
        "v_mfma_f32_16x16x32_bf16 v[44:47], a[56:59], v[8:11], v[44:47]\n"
        "ds_read_b128 v[8:11], v0 offset:8192\n"
        "v_mfma_f32_16x16x32_bf16 v[36:39], a[60:63], v[12:15], v[36:39]\n"
        "v_mfma_f32_16x16x32_bf16 v[40:43], a[64:67], v[12:15], v[40:43]\n"
        "buffer_store_dwordx4 v[32:35], v7, s[40:43], s72 offen offset:0 sc1\n"
        "v_mfma_f32_16x16x32_bf16 v[44:47], a[68:71], v[12:15], v[44:47]\n"
        "s_waitcnt lgkmcnt(1)\n"
        "ds_read_b128 v[12:15], v0 offset:9216\n"
        "v_mfma_f32_16x16x32_bf16 v[36:39], a[72:75], v[16:19], v[36:39]\n"
        "v_mfma_f32_16x16x32_bf16 v[40:43], a[76:79], v[16:19], v[40:43]\n"
        "s_waitcnt vmcnt(2)\n"
        "v_cmp_gt_i32 vcc, s70, v4\n"
        "v_mfma_f32_16x16x32_bf16 v[44:47], a[80:83], v[16:19], v[44:47]\n"
        "ds_read_b128 v[16:19], v0 offset:10240\n"
        "v_mfma_f32_16x16x32_bf16 v[36:39], a[84:87], v[20:23], v[36:39]\n"
        "s_and_b64 vcc, vcc, 3\n"
        "v_mfma_f32_16x16x32_bf16 v[40:43], a[88:91], v[20:23], v[40:43]\n"
        "s_cbranch_vccnz .Lop_i10_%=\n"
        "v_mfma_f32_16x16x32_bf16 v[44:47], a[92:95], v[20:23], v[44:47]\n"
        "s_waitcnt lgkmcnt(1)\n"
        "ds_read_b128 v[20:23], v0 offset:11264\n"
        "buffer_load_dwordx4 v1, s[40:43], s71 offen offset:0 sc1 lds\n"
        "v_mfma_f32_16x16x32_bf16 v[36:39], a[96:99], v[8:11], v[36:39]\n"
        "v_mfma_f32_16x16x32_bf16 v[40:43], a[100:103], v[8:11], v[40:43]\n"
        "v_mfma_f32_16x16x32_bf16 v[44:47], a[104:107], v[8:11], v[44:47]\n"
        "ds_read_b128 v[8:11], v0 offset:12288\n"
        "v_mfma_f32_16x16x32_bf16 v[36:39], a[108:111], v[12:15], v[36:39]\n"
        "buffer_load_dwordx4 v1, s[40:43], s71 offen offset:1024 sc1 lds\n"
        "v_mfma_f32_16x16x32_bf16 v[40:43], a[112:115], v[12:15], v[40:43]\n"
        "v_mfma_f32_16x16x32_bf16 v[44:47], a[116:119], v[12:15], v[44:47]\n"
        "s_waitcnt lgkmcnt(1)\n"
        "ds_read_b128 v[12:15], v0 offset:13312\n"
        "v_mfma_f32_16x16x32_bf16 v[36:39], a[120:123], v[16:19], v[36:39]\n"
        "v_mfma_f32_16x16x32_bf16 v[40:43], a[124:127], v[16:19], v[40:43]\n"
        "buffer_load_dwordx4 v1, s[40:43], s71 offen offset:2048 sc1 lds\n"
        "v_mfma_f32_16x16x32_bf16 v[44:47], a[128:131], v[16:19], v[44:47]\n"
        "ds_read_b128 v[16:19], v0 offset:14336\n"
        "v_mfma_f32_16x16x32_bf16 v[36:39], a[132:135], v[20:23], v[36:39]\n"
        "v_mfma_f32_16x16x32_bf16 v[40:43], a[136:139], v[20:23], v[40:43]\n"
        "v_mfma_f32_16x16x32_bf16 v[44:47], a[140:143], v[20:23], v[44:47]\n"
        "s_waitcnt lgkmcnt(1)\n"
        "ds_read_b128 v[20:23], v0 offset:15360\n"
        "buffer_load_dwordx4 v1, s[40:43], s71 offen offset:3072 sc1 lds\n"
        "v_mfma_f32_16x16x32_bf16 v[36:39], a[144:147], v[8:11], v[36:39]\n"
        "s_add_u32 m0, m0, 16384\n"
        "s_add_u32 s71, s71, 65536\n"
        "v_mfma_f32_16x16x32_bf16 v[40:43], a[148:151], v[8:11], v[40:43]\n"
        "s_barrier\n"
        "v_mfma_f32_16x16x32_bf16 v[44:47], a[152:155], v[8:11], v[44:47]\n"
        "ds_read_b128 v[8:11], v0 offset:16384\n"
        "buffer_store_dword v5, v3, s[40:43], s84 offen sc1\n"
        "v_mfma_f32_16x16x32_bf16 v[36:39], a[156:159], v[12:15], v[36:39]\n"
        "v_mov_b32 v5, s74\n"
        "buffer_load_dwordx4 v1, s[40:43], s71 offen offset:0 sc1 lds\n"
        "v_mfma_f32_16x16x32_bf16 v[40:43], a[160:163], v[12:15], v[40:43]\n"
        "s_getreg_b32 s88, hwreg(HW_REG_IB_STS, 0, 4)\n"
        "v_mfma_f32_16x16x32_bf16 v[44:47], a[164:167], v[12:15], v[44:47]\n"
        "s_waitcnt lgkmcnt(1)\n"
        "ds_read_b128 v[12:15], v0 offset:17408\n"
        "v_mfma_f32_16x16x32_bf16 v[36:39], a[168:171], v[16:19], v[36:39]\n"
        "s_cmp_le_u32 s88, 6\n"
        "s_cselect_b32 s88, s73, s89\n"
        "s_cselect_b32 s73, s89, s73\n"
        "v_mfma_f32_16x16x32_bf16 v[40:43], a[172:175], v[16:19], v[40:43]\n"
        "buffer_store_dword v5, v3, s[40:43], s88 offen sc1\n"
        "buffer_load_dwordx4 v1, s[40:43], s71 offen offset:1024 sc1 lds\n"
        "v_mfma_f32_16x16x32_bf16 v[44:47], a[176:179], v[16:19], v[44:47]\n"
        "ds_read_b128 v[16:19], v0 offset:18432\n"
        "v_mfma_f32_16x16x32_bf16 v[36:39], a[180:183], v[20:23], v[36:39]\n"
        "v_mfma_f32_16x16x32_bf16 v[40:43], a[184:187], v[20:23], v[40:43]\n"
        "v_mfma_f32_16x16x32_bf16 v[44:47], a[188:191], v[20:23], v[44:47]\n"
        "s_waitcnt lgkmcnt(1)\n"
        "ds_read_b128 v[20:23], v0 offset:19456\n"
        "buffer_load_dwordx4 v1, s[40:43], s71 offen offset:2048 sc1 lds\n"
        "v_mfma_f32_16x16x32_bf16 v[36:39], a[192:195], v[8:11], v[36:39]\n"
        "s_getreg_b32 s88, hwreg(HW_REG_IB_STS, 0, 4)\n"
        "v_mfma_f32_16x16x32_bf16 v[40:43], a[196:199], v[8:11], v[40:43]\n"
        "v_mfma_f32_16x16x32_bf16 v[44:47], a[200:203], v[8:11], v[44:47]\n"
        "ds_read_b128 v[8:11], v0 offset:20480\n"
        "v_mfma_f32_16x16x32_bf16 v[36:39], a[204:207], v[12:15], v[36:39]\n"
        "buffer_load_dwordx4 v1, s[40:43], s71 offen offset:3072 sc1 lds\n"
        "s_add_u32 s69, s69, 1\n"
        "v_mfma_f32_16x16x32_bf16 v[40:43], a[208:211], v[12:15], v[40:43]\n"
        "v_mfma_f32_16x16x32_bf16 v[44:47], a[212:215], v[12:15], v[44:47]\n"
        "s_waitcnt lgkmcnt(1)\n"
        "ds_read_b128 v[12:15], v0 offset:21504\n"
        "v_mfma_f32_16x16x32_bf16 v[36:39], a[216:219], v[16:19], v[36:39]\n"
        "s_cmp_le_u32 s88, 9\n"
        "s_cselect_b32 s88, s73, s89\n"
        "s_cselect_b32 s73, s89, s73\n"
        "v_mfma_f32_16x16x32_bf16 v[40:43], a[220:223], v[16:19], v[40:43]\n"
        "buffer_store_dword v5, v3, s[40:43], s88 offen sc1\n"
        "v_mfma_f32_16x16x32_bf16 v[44:47], a[224:227], v[16:19], v[44:47]\n"
        "s_getreg_b32 s88, hwreg(HW_REG_IB_STS, 0, 4)\n"
        "ds_read_b128 v[16:19], v0 offset:22528\n"
        "v_mfma_f32_16x16x32_bf16 v[36:39], a[228:231], v[20:23], v[36:39]\n"
        "s_cmp_le_u32 s88, 11\n"
        "s_cselect_b32 s88, s73, s89\n"
        "s_cselect_b32 s73, s89, s73\n"
        "v_mfma_f32_16x16x32_bf16 v[40:43], a[232:235], v[20:23], v[40:43]\n"
        "buffer_store_dword v5, v3, s[40:43], s88 offen sc1\n"
        "v_mfma_f32_16x16x32_bf16 v[44:47], a[236:239], v[20:23], v[44:47]\n"
        "s_waitcnt lgkmcnt(1)\n"
        "ds_read_b128 v[20:23], v0 offset:23552\n"
        "v_mfma_f32_16x16x32_bf16 v[36:39], a[240:243], v[8:11], v[36:39]\n"
        "v_mfma_f32_16x16x32_bf16 v[40:43], a[244:247], v[8:11], v[40:43]\n"
        "v_mfma_f32_16x16x32_bf16 v[44:47], a[248:251], v[8:11], v[44:47]\n"
        "ds_read_b128 v[8:11], v0 offset:24576\n"
        "v_mfma_f32_16x16x32_bf16 v[36:39], a[252:255], v[12:15], v[36:39]\n"
        "v_mfma_f32_16x16x32_bf16 v[40:43], v[80:83], v[12:15], v[40:43]\n"
        "v_mfma_f32_16x16x32_bf16 v[44:47], v[84:87], v[12:15], v[44:47]\n"
        "s_waitcnt lgkmcnt(1)\n"
        "ds_read_b128 v[12:15], v0 offset:25600\n"
        "v_mfma_f32_16x16x32_bf16 v[36:39], v[88:91], v[16:19], v[36:39]\n"
        "v_mfma_f32_16x16x32_bf16 v[40:43], v[92:95], v[16:19], v[40:43]\n"
        "v_mfma_f32_16x16x32_bf16 v[44:47], v[96:99], v[16:19], v[44:47]\n"
        "ds_read_b128 v[16:19], v0 offset:26624\n"
        "v_mfma_f32_16x16x32_bf16 v[36:39], v[100:103], v[20:23], v[36:39]\n"
        "v_mfma_f32_16x16x32_bf16 v[40:43], v[104:107], v[20:23], v[40:43]\n"
        "s_waitcnt vmcnt(12)\n"
        "buffer_store_dword v5, v3, s[40:43], s73 offen sc1\n"
        "s_add_u32 s74, s74, 1\n"
        "v_mfma_f32_16x16x32_bf16 v[44:47], v[108:111], v[20:23], v[44:47]\n"
        "s_waitcnt lgkmcnt(1)\n"
        "ds_read_b128 v[20:23], v0 offset:27648\n"
        "s_mov_b32 s84, s89\n"
        "v_mfma_f32_16x16x32_bf16 v[36:39], v[112:115], v[8:11], v[36:39]\n"
        "s_mov_b32 s73, s60\n"
        "s_add_u32 s72, s75, s59\n"
        "s_add_u32 s72, s72, 147456\n"
        "v_mfma_f32_16x16x32_bf16 v[40:43], v[116:119], v[8:11], v[40:43]\n"
        "v_mfma_f32_16x16x32_bf16 v[44:47], v[120:123], v[8:11], v[44:47]\n"
        "ds_read_b128 v[8:11], v0 offset:28672\n"
        "v_mfma_f32_16x16x32_bf16 v[36:39], v[124:127], v[12:15], v[36:39]\n"
        "v_mfma_f32_16x16x32_bf16 v[40:43], v[128:131], v[12:15], v[40:43]\n"
        "v_mfma_f32_16x16x32_bf16 v[44:47], v[132:135], v[12:15], v[44:47]\n"
        "s_waitcnt lgkmcnt(1)\n"
        "ds_read_b128 v[12:15], v0 offset:29696\n"
        "v_mfma_f32_16x16x32_bf16 v[36:39], v[136:139], v[16:19], v[36:39]\n"
        "v_mfma_f32_16x16x32_bf16 v[40:43], v[140:143], v[16:19], v[40:43]\n"
        "v_mfma_f32_16x16x32_bf16 v[44:47], v[144:147], v[16:19], v[44:47]\n"
        "ds_read_b128 v[16:19], v0 offset:30720\n"
        "v_mfma_f32_16x16x32_bf16 v[36:39], v[148:151], v[20:23], v[36:39]\n"
        "v_mfma_f32_16x16x32_bf16 v[40:43], v[152:155], v[20:23], v[40:43]\n"
        "v_mfma_f32_16x16x32_bf16 v[44:47], v[156:159], v[20:23], v[44:47]\n"
        "s_waitcnt lgkmcnt(1)\n"
        "ds_read_b128 v[20:23], v0 offset:31744\n"
        "v_mfma_f32_16x16x32_bf16 v[36:39], v[160:163], v[8:11], v[36:39]\n"
        "v_mfma_f32_16x16x32_bf16 v[40:43], v[164:167], v[8:11], v[40:43]\n"
        "s_waitcnt vmcnt(9)\n"
        "s_barrier\n"
        "v_add_u32 v0, s78, v6\n"
        "v_mfma_f32_16x16x32_bf16 v[44:47], v[168:171], v[8:11], v[44:47]\n"
        "ds_read_b128 v[8:11], v0 offset:0\n"
        "v_mfma_f32_16x16x32_bf16 v[36:39], v[172:175], v[12:15], v[36:39]\n"
        "v_mfma_f32_16x16x32_bf16 v[40:43], v[176:179], v[12:15], v[40:43]\n"
        "v_mfma_f32_16x16x32_bf16 v[44:47], v[180:183], v[12:15], v[44:47]\n"
        "s_waitcnt lgkmcnt(1)\n"
        "ds_read_b128 v[12:15], v0 offset:1024\n"
        "v_mfma_f32_16x16x32_bf16 v[36:39], v[184:187], v[16:19], v[36:39]\n"
        "v_mfma_f32_16x16x32_bf16 v[40:43], v[188:191], v[16:19], v[40:43]\n"
        "v_mfma_f32_16x16x32_bf16 v[44:47], v[192:195], v[16:19], v[44:47]\n"
        "ds_read_b128 v[16:19], v0 offset:2048\n"
        "v_mfma_f32_16x16x32_bf16 v[36:39], v[196:199], v[20:23], v[36:39]\n"
        "v_mfma_f32_16x16x32_bf16 v[40:43], v[200:203], v[20:23], v[40:43]\n"
        "v_mfma_f32_16x16x32_bf16 v[44:47], v[204:207], v[20:23], v[44:47]\n"
        "s_waitcnt lgkmcnt(1)\n"
        "ds_read_b128 v[20:23], v0 offset:3072\n"
        "s_branch .Lop_i0_%=\n"
        ".Lop_i10_%=:\n"
        "v_mfma_f32_16x16x32_bf16 v[44:47], a[92:95], v[20:23], v[44:47]\n"
        "s_waitcnt lgkmcnt(1)\n"
        "ds_read_b128 v[20:23], v0 offset:11264\n"
        "buffer_load_dword v4, v2, s[40:43], s86 offen sc1\n"
        "v_mfma_f32_16x16x32_bf16 v[36:39], a[96:99], v[8:11], v[36:39]\n"
        "v_mfma_f32_16x16x32_bf16 v[40:43], a[100:103], v[8:11], v[40:43]\n"
        "v_mfma_f32_16x16x32_bf16 v[44:47], a[104:107], v[8:11], v[44:47]\n"
        "ds_read_b128 v[8:11], v0 offset:12288\n"
        "v_mfma_f32_16x16x32_bf16 v[36:39], a[108:111], v[12:15], v[36:39]\n"
        "v_mfma_f32_16x16x32_bf16 v[40:43], a[112:115], v[12:15], v[40:43]\n"
        "v_mfma_f32_16x16x32_bf16 v[44:47], a[116:119], v[12:15], v[44:47]\n"
        "s_waitcnt lgkmcnt(1)\n"
        "ds_read_b128 v[12:15], v0 offset:13312\n"
        "v_mfma_f32_16x16x32_bf16 v[36:39], a[120:123], v[16:19], v[36:39]\n"
        "v_mfma_f32_16x16x32_bf16 v[40:43], a[124:127], v[16:19], v[40:43]\n"
        "v_mfma_f32_16x16x32_bf16 v[44:47], a[128:131], v[16:19], v[44:47]\n"
        "ds_read_b128 v[16:19], v0 offset:14336\n"
        "v_mfma_f32_16x16x32_bf16 v[36:39], a[132:135], v[20:23], v[36:39]\n"
        "v_mfma_f32_16x16x32_bf16 v[40:43], a[136:139], v[20:23], v[40:43]\n"
        "v_mfma_f32_16x16x32_bf16 v[44:47], a[140:143], v[20:23], v[44:47]\n"
        "s_waitcnt lgkmcnt(1)\n"
        "ds_read_b128 v[20:23], v0 offset:15360\n"
        "v_mfma_f32_16x16x32_bf16 v[36:39], a[144:147], v[8:11], v[36:39]\n"
        "v_mfma_f32_16x16x32_bf16 v[40:43], a[148:151], v[8:11], v[40:43]\n"
        "s_barrier\n"
        "v_mfma_f32_16x16x32_bf16 v[44:47], a[152:155], v[8:11], v[44:47]\n"
        "ds_read_b128 v[8:11], v0 offset:16384\n"
        "buffer_store_dword v5, v3, s[40:43], s84 offen sc1\n"
        "v_mfma_f32_16x16x32_bf16 v[36:39], a[156:159], v[12:15], v[36:39]\n"
        "v_mov_b32 v5, s74\n"
        "v_mfma_f32_16x16x32_bf16 v[40:43], a[160:163], v[12:15], v[40:43]\n"
        "s_getreg_b32 s88, hwreg(HW_REG_IB_STS, 0, 4)\n"
        "v_mfma_f32_16x16x32_bf16 v[44:47], a[164:167], v[12:15], v[44:47]\n"
        "s_waitcnt lgkmcnt(1)\n"
        "ds_read_b128 v[12:15], v0 offset:17408\n"
        "v_mfma_f32_16x16x32_bf16 v[36:39], a[168:171], v[16:19], v[36:39]\n"
        "s_cmp_le_u32 s88, 2\n"
        "s_cselect_b32 s88, s73, s89\n"
        "s_cselect_b32 s73, s89, s73\n"
        "v_mfma_f32_16x16x32_bf16 v[40:43], a[172:175], v[16:19], v[40:43]\n"
        "buffer_store_dword v5, v3, s[40:43], s88 offen sc1\n"
        "v_mfma_f32_16x16x32_bf16 v[44:47], a[176:179], v[16:19], v[44:47]\n"
        "ds_read_b128 v[16:19], v0 offset:18432\n"
        "v_mfma_f32_16x16x32_bf16 v[36:39], a[180:183], v[20:23], v[36:39]\n"
        "v_mfma_f32_16x16x32_bf16 v[40:43], a[184:187], v[20:23], v[40:43]\n"
        "v_mfma_f32_16x16x32_bf16 v[44:47], a[188:191], v[20:23], v[44:47]\n"
        "s_waitcnt lgkmcnt(1)\n"
        "ds_read_b128 v[20:23], v0 offset:19456\n"
        "v_mfma_f32_16x16x32_bf16 v[36:39], a[192:195], v[8:11], v[36:39]\n"
        "s_getreg_b32 s88, hwreg(HW_REG_IB_STS, 0, 4)\n"
        "v_mfma_f32_16x16x32_bf16 v[40:43], a[196:199], v[8:11], v[40:43]\n"
        "s_waitcnt vmcnt(2)\n"
        "v_cmp_gt_i32 vcc, s70, v4\n"
        "v_mfma_f32_16x16x32_bf16 v[44:47], a[200:203], v[8:11], v[44:47]\n"
        "ds_read_b128 v[8:11], v0 offset:20480\n"
        "v_mfma_f32_16x16x32_bf16 v[36:39], a[204:207], v[12:15], v[36:39]\n"
        "s_and_b64 vcc, vcc, 3\n"
        "v_mfma_f32_16x16x32_bf16 v[40:43], a[208:211], v[12:15], v[40:43]\n"
        "s_cbranch_vccnz .Lop_i11_%=\n"
        "v_mfma_f32_16x16x32_bf16 v[44:47], a[212:215], v[12:15], v[44:47]\n"
        "s_waitcnt lgkmcnt(1)\n"
        "ds_read_b128 v[12:15], v0 offset:21504\n"
        "buffer_load_dwordx4 v1, s[40:43], s71 offen offset:0 sc1 lds\n"
        "v_mfma_f32_16x16x32_bf16 v[36:39], a[216:219], v[16:19], v[36:39]\n"
        "s_cmp_le_u32 s88, 3\n"
        "s_cselect_b32 s88, s73, s89\n"
        "s_cselect_b32 s73, s89, s73\n"
        "v_mfma_f32_16x16x32_bf16 v[40:43], a[220:223], v[16:19], v[40:43]\n"
        "buffer_store_dword v5, v3, s[40:43], s88 offen sc1\n"
        "v_mfma_f32_16x16x32_bf16 v[44:47], a[224:227], v[16:19], v[44:47]\n"
        "s_getreg_b32 s88, hwreg(HW_REG_IB_STS, 0, 4)\n"
        "ds_read_b128 v[16:19], v0 offset:22528\n"
        "buffer_load_dwordx4 v1, s[40:43], s71 offen offset:1024 sc1 lds\n"
        "v_mfma_f32_16x16x32_bf16 v[36:39], a[228:231], v[20:23], v[36:39]\n"
        "s_cmp_le_u32 s88, 15\n"
        "s_cselect_b32 s88, s73, s89\n"
        "s_cselect_b32 s73, s89, s73\n"
        "v_mfma_f32_16x16x32_bf16 v[40:43], a[232:235], v[20:23], v[40:43]\n"
        "buffer_store_dword v5, v3, s[40:43], s88 offen sc1\n"
        "v_mfma_f32_16x16x32_bf16 v[44:47], a[236:239], v[20:23], v[44:47]\n"
        "s_waitcnt lgkmcnt(1)\n"
        "ds_read_b128 v[20:23], v0 offset:23552\n"
        "buffer_load_dwordx4 v1, s[40:43], s71 offen offset:2048 sc1 lds\n"
        "v_mfma_f32_16x16x32_bf16 v[36:39], a[240:243], v[8:11], v[36:39]\n"
        "v_mfma_f32_16x16x32_bf16 v[40:43], a[244:247], v[8:11], v[40:43]\n"
        "v_mfma_f32_16x16x32_bf16 v[44:47], a[248:251], v[8:11], v[44:47]\n"
        "ds_read_b128 v[8:11], v0 offset:24576\n"
        "buffer_load_dwordx4 v1, s[40:43], s71 offen offset:3072 sc1 lds\n"
        "v_mfma_f32_16x16x32_bf16 v[36:39], a[252:255], v[12:15], v[36:39]\n"
        "s_add_u32 m0, m0, 16384\n"
        "s_add_u32 s71, s71, 65536\n"
        "v_mfma_f32_16x16x32_bf16 v[40:43], v[80:83], v[12:15], v[40:43]\n"
        "v_mfma_f32_16x16x32_bf16 v[44:47], v[84:87], v[12:15], v[44:47]\n"
        "s_waitcnt lgkmcnt(1)\n"
        "ds_read_b128 v[12:15], v0 offset:25600\n"
        "buffer_load_dwordx4 v1, s[40:43], s71 offen offset:0 sc1 lds\n"
        "v_mfma_f32_16x16x32_bf16 v[36:39], v[88:91], v[16:19], v[36:39]\n"
        "v_mfma_f32_16x16x32_bf16 v[40:43], v[92:95], v[16:19], v[40:43]\n"
        "v_mfma_f32_16x16x32_bf16 v[44:47], v[96:99], v[16:19], v[44:47]\n"
        "ds_read_b128 v[16:19], v0 offset:26624\n"
        "v_mfma_f32_16x16x32_bf16 v[36:39], v[100:103], v[20:23], v[36:39]\n"
        "v_mfma_f32_16x16x32_bf16 v[40:43], v[104:107], v[20:23], v[40:43]\n"
        "buffer_store_dword v5, v3, s[40:43], s73 offen sc1\n"
        "s_add_u32 s74, s74, 1\n"
        "v_mfma_f32_16x16x32_bf16 v[44:47], v[108:111], v[20:23], v[44:47]\n"
        "s_waitcnt lgkmcnt(1)\n"
        "ds_read_b128 v[20:23], v0 offset:27648\n"
        "s_mov_b32 s84, s89\n"
        "v_mfma_f32_16x16x32_bf16 v[36:39], v[112:115], v[8:11], v[36:39]\n"
        "s_mov_b32 s73, s60\n"
        "s_add_u32 s72, s75, s59\n"
        "s_add_u32 s72, s72, 147456\n"
        "v_mfma_f32_16x16x32_bf16 v[40:43], v[116:119], v[8:11], v[40:43]\n"
        "buffer_load_dwordx4 v1, s[40:43], s71 offen offset:1024 sc1 lds\n"
        "v_mfma_f32_16x16x32_bf16 v[44:47], v[120:123], v[8:11], v[44:47]\n"
        "ds_read_b128 v[8:11], v0 offset:28672\n"
        "v_mfma_f32_16x16x32_bf16 v[36:39], v[124:127], v[12:15], v[36:39]\n"
        "v_mfma_f32_16x16x32_bf16 v[40:43], v[128:131], v[12:15], v[40:43]\n"
        "v_mfma_f32_16x16x32_bf16 v[44:47], v[132:135], v[12:15], v[44:47]\n"
        "s_waitcnt lgkmcnt(1)\n"
        "ds_read_b128 v[12:15], v0 offset:29696\n"
        "buffer_load_dwordx4 v1, s[40:43], s71 offen offset:2048 sc1 lds\n"
        "v_mfma_f32_16x16x32_bf16 v[36:39], v[136:139], v[16:19], v[36:39]\n"
        "v_mfma_f32_16x16x32_bf16 v[40:43], v[140:143], v[16:19], v[40:43]\n"
        "v_mfma_f32_16x16x32_bf16 v[44:47], v[144:147], v[16:19], v[44:47]\n"
        "ds_read_b128 v[16:19], v0 offset:30720\n"
        "v_mfma_f32_16x16x32_bf16 v[36:39], v[148:151], v[20:23], v[36:39]\n"
        "buffer_load_dwordx4 v1, s[40:43], s71 offen offset:3072 sc1 lds\n"
        "s_add_u32 s69, s69, 1\n"
        "v_mfma_f32_16x16x32_bf16 v[40:43], v[152:155], v[20:23], v[40:43]\n"
        "v_mfma_f32_16x16x32_bf16 v[44:47], v[156:159], v[20:23], v[44:47]\n"
        "s_waitcnt lgkmcnt(1)\n"
        "ds_read_b128 v[20:23], v0 offset:31744\n"
        "v_mfma_f32_16x16x32_bf16 v[36:39], v[160:163], v[8:11], v[36:39]\n"
        "v_mfma_f32_16x16x32_bf16 v[40:43], v[164:167], v[8:11], v[40:43]\n"
        "s_waitcnt vmcnt(5)\n"
        "s_barrier\n"
        "v_add_u32 v0, s78, v6\n"
        "v_mfma_f32_16x16x32_bf16 v[44:47], v[168:171], v[8:11], v[44:47]\n"
        "ds_read_b128 v[8:11], v0 offset:0\n"
        "v_mfma_f32_16x16x32_bf16 v[36:39], v[172:175], v[12:15], v[36:39]\n"
        "v_mfma_f32_16x16x32_bf16 v[40:43], v[176:179], v[12:15], v[40:43]\n"
        "v_mfma_f32_16x16x32_bf16 v[44:47], v[180:183], v[12:15], v[44:47]\n"
        "s_waitcnt lgkmcnt(1)\n"
        "ds_read_b128 v[12:15], v0 offset:1024\n"
        "v_mfma_f32_16x16x32_bf16 v[36:39], v[184:187], v[16:19], v[36:39]\n"
        "v_mfma_f32_16x16x32_bf16 v[40:43], v[188:191], v[16:19], v[40:43]\n"
        "v_mfma_f32_16x16x32_bf16 v[44:47], v[192:195], v[16:19], v[44:47]\n"
        "ds_read_b128 v[16:19], v0 offset:2048\n"
        "v_mfma_f32_16x16x32_bf16 v[36:39], v[196:199], v[20:23], v[36:39]\n"
        "v_mfma_f32_16x16x32_bf16 v[40:43], v[200:203], v[20:23], v[40:43]\n"
        "v_mfma_f32_16x16x32_bf16 v[44:47], v[204:207], v[20:23], v[44:47]\n"
        "s_waitcnt lgkmcnt(1)\n"
        "ds_read_b128 v[20:23], v0 offset:3072\n"
        "s_branch .Lop_i0_%=\n"
        ".Lop_i11_%=:\n"
        "v_mfma_f32_16x16x32_bf16 v[44:47], a[212:215], v[12:15], v[44:47]\n"
        "s_waitcnt lgkmcnt(1)\n"
        "ds_read_b128 v[12:15], v0 offset:21504\n"
        "buffer_load_dword v4, v2, s[40:43], s86 offen sc1\n"
        "v_mfma_f32_16x16x32_bf16 v[36:39], a[216:219], v[16:19], v[36:39]\n"
        "s_cmp_le_u32 s88, 3\n"
        "s_cselect_b32 s88, s73, s89\n"
        "s_cselect_b32 s73, s89, s73\n"
        "v_mfma_f32_16x16x32_bf16 v[40:43], a[220:223], v[16:19], v[40:43]\n"
        "buffer_store_dword v5, v3, s[40:43], s88 offen sc1\n"
        "v_mfma_f32_16x16x32_bf16 v[44:47], a[224:227], v[16:19], v[44:47]\n"
        "s_getreg_b32 s88, hwreg(HW_REG_IB_STS, 0, 4)\n"
        "ds_read_b128 v[16:19], v0 offset:22528\n"
        "v_mfma_f32_16x16x32_bf16 v[36:39], a[228:231], v[20:23], v[36:39]\n"
        "s_cmp_le_u32 s88, 15\n"
        "s_cselect_b32 s88, s73, s89\n"
        "s_cselect_b32 s73, s89, s73\n"
        "v_mfma_f32_16x16x32_bf16 v[40:43], a[232:235], v[20:23], v[40:43]\n"
        "buffer_store_dword v5, v3, s[40:43], s88 offen sc1\n"
        "v_mfma_f32_16x16x32_bf16 v[44:47], a[236:239], v[20:23], v[44:47]\n"
        "s_waitcnt lgkmcnt(1)\n"
        "ds_read_b128 v[20:23], v0 offset:23552\n"
        "v_mfma_f32_16x16x32_bf16 v[36:39], a[240:243], v[8:11], v[36:39]\n"
        "v_mfma_f32_16x16x32_bf16 v[40:43], a[244:247], v[8:11], v[40:43]\n"
        "v_mfma_f32_16x16x32_bf16 v[44:47], a[248:251], v[8:11], v[44:47]\n"
        "ds_read_b128 v[8:11], v0 offset:24576\n"
        "v_mfma_f32_16x16x32_bf16 v[36:39], a[252:255], v[12:15], v[36:39]\n"
        "v_mfma_f32_16x16x32_bf16 v[40:43], v[80:83], v[12:15], v[40:43]\n"
        "v_mfma_f32_16x16x32_bf16 v[44:47], v[84:87], v[12:15], v[44:47]\n"
        "s_waitcnt lgkmcnt(1)\n"
        "ds_read_b128 v[12:15], v0 offset:25600\n"
        "v_mfma_f32_16x16x32_bf16 v[36:39], v[88:91], v[16:19], v[36:39]\n"
        "v_mfma_f32_16x16x32_bf16 v[40:43], v[92:95], v[16:19], v[40:43]\n"
        "v_mfma_f32_16x16x32_bf16 v[44:47], v[96:99], v[16:19], v[44:47]\n"
        "ds_read_b128 v[16:19], v0 offset:26624\n"
        "v_mfma_f32_16x16x32_bf16 v[36:39], v[100:103], v[20:23], v[36:39]\n"
        "v_mfma_f32_16x16x32_bf16 v[40:43], v[104:107], v[20:23], v[40:43]\n"
        "buffer_store_dword v5, v3, s[40:43], s73 offen sc1\n"
        "s_add_u32 s74, s74, 1\n"
        "v_mfma_f32_16x16x32_bf16 v[44:47], v[108:111], v[20:23], v[44:47]\n"
        "s_waitcnt lgkmcnt(1)\n"
        "ds_read_b128 v[20:23], v0 offset:27648\n"
        "s_mov_b32 s84, s89\n"
        "v_mfma_f32_16x16x32_bf16 v[36:39], v[112:115], v[8:11], v[36:39]\n"
        "s_mov_b32 s73, s60\n"
        "s_add_u32 s72, s75, s59\n"
        "s_add_u32 s72, s72, 147456\n"
        "v_mfma_f32_16x16x32_bf16 v[40:43], v[116:119], v[8:11], v[40:43]\n"
        "v_mfma_f32_16x16x32_bf16 v[44:47], v[120:123], v[8:11], v[44:47]\n"
        "ds_read_b128 v[8:11], v0 offset:28672\n"
        "v_mfma_f32_16x16x32_bf16 v[36:39], v[124:127], v[12:15], v[36:39]\n"
        "v_mfma_f32_16x16x32_bf16 v[40:43], v[128:131], v[12:15], v[40:43]\n"
        "v_mfma_f32_16x16x32_bf16 v[44:47], v[132:135], v[12:15], v[44:47]\n"
        "s_waitcnt lgkmcnt(1)\n"
        "ds_read_b128 v[12:15], v0 offset:29696\n"
        "v_mfma_f32_16x16x32_bf16 v[36:39], v[136:139], v[16:19], v[36:39]\n"
        "v_mfma_f32_16x16x32_bf16 v[40:43], v[140:143], v[16:19], v[40:43]\n"
        "v_mfma_f32_16x16x32_bf16 v[44:47], v[144:147], v[16:19], v[44:47]\n"
        "ds_read_b128 v[16:19], v0 offset:30720\n"
        "v_mfma_f32_16x16x32_bf16 v[36:39], v[148:151], v[20:23], v[36:39]\n"
        "s_waitcnt vmcnt(3)\n"
        "v_cmp_gt_i32 vcc, s70, v4\n"
        "v_mfma_f32_16x16x32_bf16 v[40:43], v[152:155], v[20:23], v[40:43]\n"
        "v_mfma_f32_16x16x32_bf16 v[44:47], v[156:159], v[20:23], v[44:47]\n"
        "s_waitcnt lgkmcnt(1)\n"
        "ds_read_b128 v[20:23], v0 offset:31744\n"
        "s_and_b64 vcc, vcc, 3\n"
        "v_mfma_f32_16x16x32_bf16 v[36:39], v[160:163], v[8:11], v[36:39]\n"
        "s_cbranch_vccnz .Lop_i12_%=\n"
        "v_mfma_f32_16x16x32_bf16 v[40:43], v[164:167], v[8:11], v[40:43]\n"
        "buffer_load_dwordx4 v1, s[40:43], s71 offen offset:0 sc1 lds\n"
        "v_mfma_f32_16x16x32_bf16 v[44:47], v[168:171], v[8:11], v[44:47]\n"
        "v_mfma_f32_16x16x32_bf16 v[36:39], v[172:175], v[12:15], v[36:39]\n"
        "buffer_load_dwordx4 v1, s[40:43], s71 offen offset:1024 sc1 lds\n"
        "v_mfma_f32_16x16x32_bf16 v[40:43], v[176:179], v[12:15], v[40:43]\n"
        "v_mfma_f32_16x16x32_bf16 v[44:47], v[180:183], v[12:15], v[44:47]\n"
        "s_waitcnt lgkmcnt(0)\n"
        "buffer_load_dwordx4 v1, s[40:43], s71 offen offset:2048 sc1 lds\n"
        "v_mfma_f32_16x16x32_bf16 v[36:39], v[184:187], v[16:19], v[36:39]\n"
        "v_mfma_f32_16x16x32_bf16 v[40:43], v[188:191], v[16:19], v[40:43]\n"
        "buffer_load_dwordx4 v1, s[40:43], s71 offen offset:3072 sc1 lds\n"
        "s_add_u32 m0, m0, 16384\n"
        "s_add_u32 s71, s71, 65536\n"
        "v_mfma_f32_16x16x32_bf16 v[44:47], v[192:195], v[16:19], v[44:47]\n"
        "v_mfma_f32_16x16x32_bf16 v[36:39], v[196:199], v[20:23], v[36:39]\n"
        "buffer_load_dwordx4 v1, s[40:43], s71 offen offset:0 sc1 lds\n"
        "v_mfma_f32_16x16x32_bf16 v[40:43], v[200:203], v[20:23], v[40:43]\n"
        "v_mfma_f32_16x16x32_bf16 v[44:47], v[204:207], v[20:23], v[44:47]\n"
        "buffer_load_dwordx4 v1, s[40:43], s71 offen offset:1024 sc1 lds\n"
        "buffer_load_dwordx4 v1, s[40:43], s71 offen offset:2048 sc1 lds\n"
        "buffer_load_dwordx4 v1, s[40:43], s71 offen offset:3072 sc1 lds\n"
        "s_add_u32 s69, s69, 1\n"
        "s_nop 2\n"
        "v_cvt_pk_bf16_f32 v36, v36, v37\n"
        "v_cvt_pk_bf16_f32 v37, v38, v39\n"
        "v_cvt_pk_bf16_f32 v38, v40, v41\n"
        "v_cvt_pk_bf16_f32 v39, v42, v43\n"
        "buffer_store_dwordx4 v[36:39], v48, s[40:43], s72 offen offset:0 sc1\n"
        "v_cvt_pk_bf16_f32 v44, v44, v45\n"
        "v_cvt_pk_bf16_f32 v45, v46, v47\n"
        "s_nop 0\n"
        "v_permlane16_swap_b32 v44, v46\n"
        "v_permlane16_swap_b32 v45, v47\n"
        "buffer_store_dwordx4 v[44:47], v7, s[40:43], s72 offen offset:0 sc1\n"
        "v_mov_b32 v5, s74\n"
        "s_mov_b32 s84, s60\n"
        "s_waitcnt vmcnt(6)\n"
        "s_branch .Lop_go1_%=\n"
        ".Lop_i12_%=:\n"
        "v_mfma_f32_16x16x32_bf16 v[40:43], v[164:167], v[8:11], v[40:43]\n"
        "buffer_load_dword v4, v2, s[40:43], s86 offen sc1\n"
        "v_mfma_f32_16x16x32_bf16 v[44:47], v[168:171], v[8:11], v[44:47]\n"
        "v_mfma_f32_16x16x32_bf16 v[36:39], v[172:175], v[12:15], v[36:39]\n"
        "v_mfma_f32_16x16x32_bf16 v[40:43], v[176:179], v[12:15], v[40:43]\n"
        "v_mfma_f32_16x16x32_bf16 v[44:47], v[180:183], v[12:15], v[44:47]\n"
        "s_waitcnt lgkmcnt(0)\n"
        "v_mfma_f32_16x16x32_bf16 v[36:39], v[184:187], v[16:19], v[36:39]\n"
        "v_mfma_f32_16x16x32_bf16 v[40:43], v[188:191], v[16:19], v[40:43]\n"
        "v_mfma_f32_16x16x32_bf16 v[44:47], v[192:195], v[16:19], v[44:47]\n"
        "v_mfma_f32_16x16x32_bf16 v[36:39], v[196:199], v[20:23], v[36:39]\n"
        "v_mfma_f32_16x16x32_bf16 v[40:43], v[200:203], v[20:23], v[40:43]\n"
        "v_mfma_f32_16x16x32_bf16 v[44:47], v[204:207], v[20:23], v[44:47]\n"
        "s_nop 2\n"
        "v_cvt_pk_bf16_f32 v36, v36, v37\n"
        "v_cvt_pk_bf16_f32 v37, v38, v39\n"
        "v_cvt_pk_bf16_f32 v38, v40, v41\n"
        "v_cvt_pk_bf16_f32 v39, v42, v43\n"
        "buffer_store_dwordx4 v[36:39], v48, s[40:43], s72 offen offset:0 sc1\n"
        "v_cvt_pk_bf16_f32 v44, v44, v45\n"
        "v_cvt_pk_bf16_f32 v45, v46, v47\n"
        "s_nop 0\n"
        "v_permlane16_swap_b32 v44, v46\n"
        "v_permlane16_swap_b32 v45, v47\n"
        "buffer_store_dwordx4 v[44:47], v7, s[40:43], s72 offen offset:0 sc1\n"
        "s_waitcnt vmcnt(2)\n"
        "v_cmp_gt_i32 vcc, s70, v4\n"
        "s_nop 0\n"
        "s_and_b64 vcc, vcc, 3\n"
        "s_cbranch_vccnz .Lop_late1_%=\n"
        "buffer_load_dwordx4 v1, s[40:43], s71 offen offset:0 sc1 lds\n"
        "buffer_load_dwordx4 v1, s[40:43], s71 offen offset:1024 sc1 lds\n"
        "buffer_load_dwordx4 v1, s[40:43], s71 offen offset:2048 sc1 lds\n"
        "buffer_load_dwordx4 v1, s[40:43], s71 offen offset:3072 sc1 lds\n"
        "s_add_u32 m0, m0, 16384\n"
        "s_add_u32 s71, s71, 65536\n"
        "buffer_load_dwordx4 v1, s[40:43], s71 offen offset:0 sc1 lds\n"
        "buffer_load_dwordx4 v1, s[40:43], s71 offen offset:1024 sc1 lds\n"
        "buffer_load_dwordx4 v1, s[40:43], s71 offen offset:2048 sc1 lds\n"
        "buffer_load_dwordx4 v1, s[40:43], s71 offen offset:3072 sc1 lds\n"
        "s_add_u32 s69, s69, 1\n"
        "s_waitcnt vmcnt(8)\n"
        "v_mov_b32 v5, s74\n"
        "buffer_store_dword v5, v3, s[40:43], s73 offen sc1\n"
        "s_waitcnt vmcnt(5)\n"
        "s_branch .Lop_go1_%=\n"
        ".Lop_late1_%=:\n"
        "s_waitcnt vmcnt(0)\n"
        "v_mov_b32 v5, s74\n"
        "buffer_store_dword v5, v3, s[40:43], s73 offen sc1\n"
        "s_cmp_eq_u32 s69, 128\n"
        "s_cbranch_scc0 .Lop_wait1_%=\n"
        "s_endpgm\n"
        ".Lop_wait1_%=:\n"
        "s_mov_b32 s64, 0\n"
        ".Lop_in1_top_%=:\n"
        "buffer_load_dword v4, v2, s[40:43], s86 offen sc1\n"
        "s_waitcnt vmcnt(0)\n"
        "v_cmp_gt_i32 vcc, s70, v4\n"
        "s_nop 0\n"
        "s_and_b64 vcc, vcc, 3\n"
        "s_cbranch_vccz .Lop_in1_ok_%=\n"
        "s_add_u32 s64, s64, 1\n"
        "s_and_b32 s65, s64, 255\n"
        "s_cbranch_scc1 .Lop_in1_go_%=\n"
        "s_mov_b32 s65, 49152\n"
        "s_cmp_lt_u32 s64, 1048576\n"
        "s_cbranch_scc1 .Lop_in1_chk_%=\n"
        "v_mov_b32 v4, 1\n"
        "buffer_store_dword v4, off, s[40:43], s65 sc1\n"
        "s_waitcnt vmcnt(0)\n"
        ".Lop_in1_chk_%=:\n"
        "buffer_load_dword v4, off, s[40:43], s65 sc1\n"
        "s_waitcnt vmcnt(0)\n"
        "v_readfirstlane_b32 s65, v4\n"
        "s_nop 0\n"
        "s_cmp_lg_u32 s65, 1\n"
        "s_cbranch_scc1 .Lop_in1_go_%=\n"
        "s_endpgm\n"
        ".Lop_in1_go_%=:\n"
        "s_branch .Lop_in1_top_%=\n"
        ".Lop_in1_ok_%=:\n"
        "buffer_load_dwordx4 v1, s[40:43], s71 offen offset:0 sc1 lds\n"
        "buffer_load_dwordx4 v1, s[40:43], s71 offen offset:1024 sc1 lds\n"
        "buffer_load_dwordx4 v1, s[40:43], s71 offen offset:2048 sc1 lds\n"
        "buffer_load_dwordx4 v1, s[40:43], s71 offen offset:3072 sc1 lds\n"
        "s_add_u32 m0, m0, 16384\n"
        "s_add_u32 s71, s71, 65536\n"
        "buffer_load_dwordx4 v1, s[40:43], s71 offen offset:0 sc1 lds\n"
        "buffer_load_dwordx4 v1, s[40:43], s71 offen offset:1024 sc1 lds\n"
        "buffer_load_dwordx4 v1, s[40:43], s71 offen offset:2048 sc1 lds\n"
        "buffer_load_dwordx4 v1, s[40:43], s71 offen offset:3072 sc1 lds\n"
        "s_add_u32 s69, s69, 1\n"
        "s_waitcnt vmcnt(4)\n"
        ".Lop_go1_%=:\n"
        "s_mov_b32 s72, 241172480\n"
        "s_mov_b32 s83, 241172480\n"
        "s_mov_b32 s73, 241172480\n"
        "v_add_u32 v0, s78, v6\n"
        "s_barrier\n"
        "ds_read_b128 v[8:11], v0 offset:0\n"
        "ds_read_b128 v[12:15], v0 offset:1024\n"
        "ds_read_b128 v[16:19], v0 offset:2048\n"
        "ds_read_b128 v[20:23], v0 offset:3072\n"
        "s_waitcnt lgkmcnt(2)\n"
        "s_branch .Lop_i0_%=\n"
        ".Lop_end_%=:\n"
            :: "s"(p.ws), "s"(p.Wo), "s"(p.Wo), "s"(p.Wo), "s"(pair(w, delay)), "s"(pair(x4, pr)),
               "s"(pair(PAGE + spread(A_Q) + (8 * kap + w) * 16384, 65 * PAGE + spread(B_P3))), "s"(pair(flag(F_OP + fl), w * 4096)), "s"(pair(0, 0)),
               "v"(pair(lane * 16, lane * 16)), "v"(pair(lane < 2 ? flag(F_AT + (8 * kap + w + 4 * lane) * 3 + nu) : OOB, fo4)), K_ARGS
            : "v0", "v1", "v2", "v3", "v4", "v5", "v6", "v7", "v8", "v9", "v10", "v11", "v12", "v13", "v14", "v15", "v16", "v17", "v18", "v19", "v20", "v21", "v22", "v23", "v24", "v25", "v26", "v27", "v28", "v29", "v30", "v31", "v32", "v33", "v34", "v35", "v36", "v37", "v38", "v39", "v40", "v41", "v42", "v43", "v44", "v45", "v46", "v47", "v48", "v49", "v50", "v51", "v52", "v53", "v54", "v55", "v56", "v57", "v58", "v59", "v60", "v61", "v62", "v63", "v64", "v65", "v66", "v67", "v68", "v69", "v70", "v71", "v72", "v73", "v74", "v75", "v76", "v77", "v78", "v79", "v255", "a255", "s30", "s31", "s32", "s33", "s34", "s35", "s36", "s37", "s38", "s39", "s40", "s41", "s42", "s43", "s44", "s45", "s46", "s47", "s48", "s49", "s50", "s51", "s52", "s53", "s54", "s55", "s56", "s57", "s58", "s59", "s60", "s61", "s62", "s63", "s64", "s65", "s66", "s67", "s68", "s69", "s70", "s71", "s72", "s73", "s74", "s75", "s76", "s77", "s78", "s79", "s80", "s81", "s82", "s83", "s84", "s85", "s86", "s87", "s88", "s89", "s90", "s91", "s92", "s93", "s94", "s95", "s96", "s97", "s98", "s99", "vcc", "scc", "memory");
    } else {
        const unsigned hb = 65 * PAGE + spread(B_H + kap * 73728);
        asm volatile(
        "s_nop 4\n"
        "s_mov_b64 s[40:41], %0\n"
        "s_and_b32 s41, s41, 0xffff\n"
        "s_mov_b32 s42, 236978176\n"
        "s_mov_b32 s43, 0x20000\n"
        "s_mov_b64 s[48:49], %4\n"
        "s_mov_b64 s[56:57], %5\n"
        "s_mov_b64 s[58:59], %6\n"
        "s_mov_b64 s[60:61], %7\n"
        "s_mov_b64 s[62:63], %8\n"
        "v_mov_b64 v[0:1], %9\n"
        "v_mov_b64 v[2:3], %10\n"
        "v_mov_b64 v[4:5], %11\n"
        "v_mov_b64 v[6:7], %12\n"
        "v_mov_b64 v[8:9], %13\n"
        "v_mov_b64 v[10:11], %14\n"
        "v_mov_b64 v[12:13], %15\n"
        "v_mov_b64 v[14:15], %16\n"
        "s_mov_b32 s47, 0x20000\n"
        "s_mul_i32 s79, s48, 40960\n"
        ".Lf2_start_%=:\n"
        "s_sub_u32 s49, s49, 1\n"
        "s_cbranch_scc1 .Lf2_start2_%=\n"
        "s_sleep 16\n"
        "s_branch .Lf2_start_%=\n"
        ".Lf2_start2_%=:\n"
        "s_mov_b64 s[44:45], %1\n"
        "s_and_b32 s45, s45, 0xffff\n"
        "s_mov_b32 s46, 5308416\n"
        "s_add_u32 m0, s79, 0\n"
        "s_mov_b32 s65, 0\n"
        "buffer_load_dwordx4 v8, s[44:47], s65 offen lds\n"
        "buffer_load_dwordx4 v9, s[44:47], s65 offen offset:1024 lds\n"
        "buffer_load_dwordx4 v10, s[44:47], s65 offen offset:2048 lds\n"
        "s_add_u32 m0, s79, 3072\n"
        "s_mov_b32 s65, 55296\n"
        "buffer_load_dwordx4 v8, s[44:47], s65 offen lds\n"
        "buffer_load_dwordx4 v9, s[44:47], s65 offen offset:1024 lds\n"
        "buffer_load_dwordx4 v10, s[44:47], s65 offen offset:2048 lds\n"
        "s_waitcnt vmcnt(3)\n"
        "ds_read_b64_tr_b16 a[0:1], v11 offset:0\n"
        "ds_read_b64_tr_b16 a[2:3], v11 offset:1536\n"
        "ds_read_b64_tr_b16 a[4:5], v11 offset:8\n"
        "ds_read_b64_tr_b16 a[6:7], v11 offset:1544\n"
        "ds_read_b64_tr_b16 a[8:9], v15 offset:0\n"
        "ds_read_b64_tr_b16 a[10:11], v15 offset:1536\n"
        "s_waitcnt vmcnt(0)\n"
        "ds_read_b64_tr_b16 a[12:13], v11 offset:3072\n"
        "ds_read_b64_tr_b16 a[14:15], v11 offset:4608\n"
        "ds_read_b64_tr_b16 a[16:17], v11 offset:3080\n"
        "ds_read_b64_tr_b16 a[18:19], v11 offset:4616\n"
        "ds_read_b64_tr_b16 a[20:21], v15 offset:3072\n"
        "ds_read_b64_tr_b16 a[22:23], v15 offset:4608\n"
        "s_waitcnt lgkmcnt(6)\n"
        "s_add_u32 m0, s79, 0\n"
        "s_mov_b32 s65, 110592\n"
        "buffer_load_dwordx4 v8, s[44:47], s65 offen lds\n"
        "buffer_load_dwordx4 v9, s[44:47], s65 offen offset:1024 lds\n"
        "buffer_load_dwordx4 v10, s[44:47], s65 offen offset:2048 lds\n"
        "s_waitcnt vmcnt(0)\n"
        "ds_read_b64_tr_b16 a[24:25], v11 offset:0\n"
        "ds_read_b64_tr_b16 a[26:27], v11 offset:1536\n"
        "ds_read_b64_tr_b16 a[28:29], v11 offset:8\n"
        "ds_read_b64_tr_b16 a[30:31], v11 offset:1544\n"
        "ds_read_b64_tr_b16 a[32:33], v15 offset:0\n"
        "ds_read_b64_tr_b16 a[34:35], v15 offset:1536\n"
        "s_waitcnt lgkmcnt(6)\n"
        "s_add_u32 m0, s79, 3072\n"
        "s_mov_b32 s65, 165888\n"
        "buffer_load_dwordx4 v8, s[44:47], s65 offen lds\n"
        "buffer_load_dwordx4 v9, s[44:47], s65 offen offset:1024 lds\n"
        "buffer_load_dwordx4 v10, s[44:47], s65 offen offset:2048 lds\n"
        "s_waitcnt vmcnt(0)\n"
        "ds_read_b64_tr_b16 a[36:37], v11 offset:3072\n"
        "ds_read_b64_tr_b16 a[38:39], v11 offset:4608\n"
        "ds_read_b64_tr_b16 a[40:41], v11 offset:3080\n"
        "ds_read_b64_tr_b16 a[42:43], v11 offset:4616\n"
        "ds_read_b64_tr_b16 a[44:45], v15 offset:3072\n"
        "ds_read_b64_tr_b16 a[46:47], v15 offset:4608\n"
        "s_waitcnt lgkmcnt(6)\n"
        "s_add_u32 m0, s79, 0\n"
        "s_mov_b32 s65, 221184\n"
        "buffer_load_dwordx4 v8, s[44:47], s65 offen lds\n"
        "buffer_load_dwordx4 v9, s[44:47], s65 offen offset:1024 lds\n"
        "buffer_load_dwordx4 v10, s[44:47], s65 offen offset:2048 lds\n"
        "s_waitcnt vmcnt(0)\n"
        "ds_read_b64_tr_b16 a[48:49], v11 offset:0\n"
        "ds_read_b64_tr_b16 a[50:51], v11 offset:1536\n"
        "ds_read_b64_tr_b16 a[52:53], v11 offset:8\n"
        "ds_read_b64_tr_b16 a[54:55], v11 offset:1544\n"
        "ds_read_b64_tr_b16 a[56:57], v15 offset:0\n"
        "ds_read_b64_tr_b16 a[58:59], v15 offset:1536\n"
        "s_waitcnt lgkmcnt(6)\n"
        "s_add_u32 m0, s79, 3072\n"
        "s_mov_b32 s65, 276480\n"
        "buffer_load_dwordx4 v8, s[44:47], s65 offen lds\n"
        "buffer_load_dwordx4 v9, s[44:47], s65 offen offset:1024 lds\n"
        "buffer_load_dwordx4 v10, s[44:47], s65 offen offset:2048 lds\n"
        "s_waitcnt vmcnt(0)\n"
        "ds_read_b64_tr_b16 a[60:61], v11 offset:3072\n"
        "ds_read_b64_tr_b16 a[62:63], v11 offset:4608\n"
        "ds_read_b64_tr_b16 a[64:65], v11 offset:3080\n"
        "ds_read_b64_tr_b16 a[66:67], v11 offset:4616\n"
        "ds_read_b64_tr_b16 a[68:69], v15 offset:3072\n"
        "ds_read_b64_tr_b16 a[70:71], v15 offset:4608\n"
        "s_waitcnt lgkmcnt(6)\n"
        "s_add_u32 m0, s79, 0\n"
        "s_mov_b32 s65, 331776\n"
        "buffer_load_dwordx4 v8, s[44:47], s65 offen lds\n"
        "buffer_load_dwordx4 v9, s[44:47], s65 offen offset:1024 lds\n"
        "buffer_load_dwordx4 v10, s[44:47], s65 offen offset:2048 lds\n"
        "s_waitcnt vmcnt(0)\n"
        "ds_read_b64_tr_b16 a[72:73], v11 offset:0\n"
        "ds_read_b64_tr_b16 a[74:75], v11 offset:1536\n"
        "ds_read_b64_tr_b16 a[76:77], v11 offset:8\n"
        "ds_read_b64_tr_b16 a[78:79], v11 offset:1544\n"
        "ds_read_b64_tr_b16 a[80:81], v15 offset:0\n"
        "ds_read_b64_tr_b16 a[82:83], v15 offset:1536\n"
        "s_waitcnt lgkmcnt(6)\n"
        "s_add_u32 m0, s79, 3072\n"
        "s_mov_b32 s65, 387072\n"
        "buffer_load_dwordx4 v8, s[44:47], s65 offen lds\n"
        "buffer_load_dwordx4 v9, s[44:47], s65 offen offset:1024 lds\n"
        "buffer_load_dwordx4 v10, s[44:47], s65 offen offset:2048 lds\n"
        "s_waitcnt vmcnt(0)\n"
        "ds_read_b64_tr_b16 a[84:85], v11 offset:3072\n"
        "ds_read_b64_tr_b16 a[86:87], v11 offset:4608\n"
        "ds_read_b64_tr_b16 a[88:89], v11 offset:3080\n"
        "ds_read_b64_tr_b16 a[90:91], v11 offset:4616\n"
        "ds_read_b64_tr_b16 a[92:93], v15 offset:3072\n"
        "ds_read_b64_tr_b16 a[94:95], v15 offset:4608\n"
        "s_waitcnt lgkmcnt(6)\n"
        "s_add_u32 m0, s79, 0\n"
        "s_mov_b32 s65, 0\n"
        "buffer_load_dwordx4 v12, s[44:47], s65 offen lds\n"
        "buffer_load_dwordx4 v13, s[44:47], s65 offen offset:1024 lds\n"
        "buffer_load_dwordx4 v14, s[44:47], s65 offen offset:2048 lds\n"
        "s_waitcnt vmcnt(0)\n"
        "ds_read_b64_tr_b16 a[96:97], v11 offset:0\n"
        "ds_read_b64_tr_b16 a[98:99], v11 offset:1536\n"
        "ds_read_b64_tr_b16 a[100:101], v11 offset:8\n"
        "ds_read_b64_tr_b16 a[102:103], v11 offset:1544\n"
        "ds_read_b64_tr_b16 a[104:105], v15 offset:0\n"
        "ds_read_b64_tr_b16 a[106:107], v15 offset:1536\n"
        "s_waitcnt lgkmcnt(6)\n"
        "s_add_u32 m0, s79, 3072\n"
        "s_mov_b32 s65, 110592\n"
        "buffer_load_dwordx4 v12, s[44:47], s65 offen lds\n"
        "buffer_load_dwordx4 v13, s[44:47], s65 offen offset:1024 lds\n"
        "buffer_load_dwordx4 v14, s[44:47], s65 offen offset:2048 lds\n"
        "s_waitcnt vmcnt(0)\n"
        "ds_read_b64_tr_b16 a[108:109], v11 offset:3072\n"
        "ds_read_b64_tr_b16 a[110:111], v11 offset:4608\n"
        "ds_read_b64_tr_b16 a[112:113], v11 offset:3080\n"
        "ds_read_b64_tr_b16 a[114:115], v11 offset:4616\n"
        "ds_read_b64_tr_b16 a[116:117], v15 offset:3072\n"
        "ds_read_b64_tr_b16 a[118:119], v15 offset:4608\n"
        "s_waitcnt lgkmcnt(6)\n"
        "s_add_u32 m0, s79, 0\n"
        "s_mov_b32 s65, 221184\n"
        "buffer_load_dwordx4 v12, s[44:47], s65 offen lds\n"
        "buffer_load_dwordx4 v13, s[44:47], s65 offen offset:1024 lds\n"
        "buffer_load_dwordx4 v14, s[44:47], s65 offen offset:2048 lds\n"
        "s_waitcnt vmcnt(0)\n"
        "ds_read_b64_tr_b16 a[120:121], v11 offset:0\n"
        "ds_read_b64_tr_b16 a[122:123], v11 offset:1536\n"
        "ds_read_b64_tr_b16 a[124:125], v11 offset:8\n"
        "ds_read_b64_tr_b16 a[126:127], v11 offset:1544\n"
        "ds_read_b64_tr_b16 a[128:129], v15 offset:0\n"
        "ds_read_b64_tr_b16 a[130:131], v15 offset:1536\n"
        "s_waitcnt lgkmcnt(6)\n"
        "s_add_u32 m0, s79, 3072\n"
        "s_mov_b32 s65, 331776\n"
        "buffer_load_dwordx4 v12, s[44:47], s65 offen lds\n"
        "buffer_load_dwordx4 v13, s[44:47], s65 offen offset:1024 lds\n"
        "buffer_load_dwordx4 v14, s[44:47], s65 offen offset:2048 lds\n"
        "s_waitcnt vmcnt(0)\n"
        "ds_read_b64_tr_b16 a[132:133], v11 offset:3072\n"
        "ds_read_b64_tr_b16 a[134:135], v11 offset:4608\n"
        "ds_read_b64_tr_b16 a[136:137], v11 offset:3080\n"
        "ds_read_b64_tr_b16 a[138:139], v11 offset:4616\n"
        "ds_read_b64_tr_b16 a[140:141], v15 offset:3072\n"
        "ds_read_b64_tr_b16 a[142:143], v15 offset:4608\n"
        "s_waitcnt lgkmcnt(6)\n"
        "s_add_u32 m0, s79, 0\n"
        "s_mov_b32 s65, 442368\n"
        "buffer_load_dwordx4 v8, s[44:47], s65 offen lds\n"
        "buffer_load_dwordx4 v9, s[44:47], s65 offen offset:1024 lds\n"
        "buffer_load_dwordx4 v10, s[44:47], s65 offen offset:2048 lds\n"
        "s_waitcnt vmcnt(0)\n"
        "ds_read_b64_tr_b16 a[144:145], v11 offset:0\n"
        "ds_read_b64_tr_b16 a[146:147], v11 offset:1536\n"
        "ds_read_b64_tr_b16 a[148:149], v11 offset:8\n"
        "ds_read_b64_tr_b16 a[150:151], v11 offset:1544\n"
        "ds_read_b64_tr_b16 a[152:153], v15 offset:0\n"
        "ds_read_b64_tr_b16 a[154:155], v15 offset:1536\n"
        "s_waitcnt lgkmcnt(6)\n"
        "s_add_u32 m0, s79, 3072\n"
        "s_mov_b32 s65, 497664\n"
        "buffer_load_dwordx4 v8, s[44:47], s65 offen lds\n"
        "buffer_load_dwordx4 v9, s[44:47], s65 offen offset:1024 lds\n"
        "buffer_load_dwordx4 v10, s[44:47], s65 offen offset:2048 lds\n"
        "s_waitcnt vmcnt(0)\n"
        "ds_read_b64_tr_b16 a[156:157], v11 offset:3072\n"
        "ds_read_b64_tr_b16 a[158:159], v11 offset:4608\n"
        "ds_read_b64_tr_b16 a[160:161], v11 offset:3080\n"
        "ds_read_b64_tr_b16 a[162:163], v11 offset:4616\n"
        "ds_read_b64_tr_b16 a[164:165], v15 offset:3072\n"
        "ds_read_b64_tr_b16 a[166:167], v15 offset:4608\n"
        "s_waitcnt lgkmcnt(6)\n"
        "s_add_u32 m0, s79, 0\n"
        "s_mov_b32 s65, 552960\n"
        "buffer_load_dwordx4 v8, s[44:47], s65 offen lds\n"
        "buffer_load_dwordx4 v9, s[44:47], s65 offen offset:1024 lds\n"
        "buffer_load_dwordx4 v10, s[44:47], s65 offen offset:2048 lds\n"
        "s_waitcnt vmcnt(0)\n"
        "ds_read_b64_tr_b16 a[168:169], v11 offset:0\n"
        "ds_read_b64_tr_b16 a[170:171], v11 offset:1536\n"
        "ds_read_b64_tr_b16 a[172:173], v11 offset:8\n"
        "ds_read_b64_tr_b16 a[174:175], v11 offset:1544\n"
        "ds_read_b64_tr_b16 a[176:177], v15 offset:0\n"
        "ds_read_b64_tr_b16 a[178:179], v15 offset:1536\n"
        "s_waitcnt lgkmcnt(6)\n"
        "s_add_u32 m0, s79, 3072\n"
        "s_mov_b32 s65, 608256\n"
        "buffer_load_dwordx4 v8, s[44:47], s65 offen lds\n"
        "buffer_load_dwordx4 v9, s[44:47], s65 offen offset:1024 lds\n"
        "buffer_load_dwordx4 v10, s[44:47], s65 offen offset:2048 lds\n"
        "s_waitcnt vmcnt(0)\n"
        "ds_read_b64_tr_b16 a[180:181], v11 offset:3072\n"
        "ds_read_b64_tr_b16 a[182:183], v11 offset:4608\n"
        "ds_read_b64_tr_b16 a[184:185], v11 offset:3080\n"
        "ds_read_b64_tr_b16 a[186:187], v11 offset:4616\n"
        "ds_read_b64_tr_b16 a[188:189], v15 offset:3072\n"
        "ds_read_b64_tr_b16 a[190:191], v15 offset:4608\n"
        "s_waitcnt lgkmcnt(6)\n"
        "s_add_u32 m0, s79, 0\n"
        "s_mov_b32 s65, 663552\n"
        "buffer_load_dwordx4 v8, s[44:47], s65 offen lds\n"
        "buffer_load_dwordx4 v9, s[44:47], s65 offen offset:1024 lds\n"
        "buffer_load_dwordx4 v10, s[44:47], s65 offen offset:2048 lds\n"
        "s_waitcnt vmcnt(0)\n"
        "ds_read_b64_tr_b16 a[192:193], v11 offset:0\n"
        "ds_read_b64_tr_b16 a[194:195], v11 offset:1536\n"
        "ds_read_b64_tr_b16 a[196:197], v11 offset:8\n"
        "ds_read_b64_tr_b16 a[198:199], v11 offset:1544\n"
        "ds_read_b64_tr_b16 a[200:201], v15 offset:0\n"
        "ds_read_b64_tr_b16 a[202:203], v15 offset:1536\n"
        "s_waitcnt lgkmcnt(6)\n"
        "s_add_u32 m0, s79, 3072\n"
        "s_mov_b32 s65, 718848\n"
        "buffer_load_dwordx4 v8, s[44:47], s65 offen lds\n"
        "buffer_load_dwordx4 v9, s[44:47], s65 offen offset:1024 lds\n"
        "buffer_load_dwordx4 v10, s[44:47], s65 offen offset:2048 lds\n"
        "s_waitcnt vmcnt(0)\n"
        "ds_read_b64_tr_b16 a[204:205], v11 offset:3072\n"
        "ds_read_b64_tr_b16 a[206:207], v11 offset:4608\n"
        "ds_read_b64_tr_b16 a[208:209], v11 offset:3080\n"
        "ds_read_b64_tr_b16 a[210:211], v11 offset:4616\n"
        "ds_read_b64_tr_b16 a[212:213], v15 offset:3072\n"
        "ds_read_b64_tr_b16 a[214:215], v15 offset:4608\n"
        "s_waitcnt lgkmcnt(6)\n"
        "s_add_u32 m0, s79, 0\n"
        "s_mov_b32 s65, 774144\n"
        "buffer_load_dwordx4 v8, s[44:47], s65 offen lds\n"
        "buffer_load_dwordx4 v9, s[44:47], s65 offen offset:1024 lds\n"
        "buffer_load_dwordx4 v10, s[44:47], s65 offen offset:2048 lds\n"
        "s_waitcnt vmcnt(0)\n"
        "ds_read_b64_tr_b16 a[216:217], v11 offset:0\n"
        "ds_read_b64_tr_b16 a[218:219], v11 offset:1536\n"
        "ds_read_b64_tr_b16 a[220:221], v11 offset:8\n"
        "ds_read_b64_tr_b16 a[222:223], v11 offset:1544\n"
        "ds_read_b64_tr_b16 a[224:225], v15 offset:0\n"
        "ds_read_b64_tr_b16 a[226:227], v15 offset:1536\n"
        "s_waitcnt lgkmcnt(6)\n"
        "s_add_u32 m0, s79, 3072\n"
        "s_mov_b32 s65, 829440\n"
        "buffer_load_dwordx4 v8, s[44:47], s65 offen lds\n"
        "buffer_load_dwordx4 v9, s[44:47], s65 offen offset:1024 lds\n"
        "buffer_load_dwordx4 v10, s[44:47], s65 offen offset:2048 lds\n"
        "s_waitcnt vmcnt(0)\n"
        "ds_read_b64_tr_b16 a[228:229], v11 offset:3072\n"
        "ds_read_b64_tr_b16 a[230:231], v11 offset:4608\n"
        "ds_read_b64_tr_b16 a[232:233], v11 offset:3080\n"
        "ds_read_b64_tr_b16 a[234:235], v11 offset:4616\n"
        "ds_read_b64_tr_b16 a[236:237], v15 offset:3072\n"
        "ds_read_b64_tr_b16 a[238:239], v15 offset:4608\n"
        "s_waitcnt lgkmcnt(6)\n"
        "s_add_u32 m0, s79, 0\n"
        "s_mov_b32 s65, 442368\n"
        "buffer_load_dwordx4 v12, s[44:47], s65 offen lds\n"
        "buffer_load_dwordx4 v13, s[44:47], s65 offen offset:1024 lds\n"
        "buffer_load_dwordx4 v14, s[44:47], s65 offen offset:2048 lds\n"
        "s_waitcnt vmcnt(0)\n"
        "ds_read_b64_tr_b16 a[240:241], v11 offset:0\n"
        "ds_read_b64_tr_b16 a[242:243], v11 offset:1536\n"
        "ds_read_b64_tr_b16 a[244:245], v11 offset:8\n"
        "ds_read_b64_tr_b16 a[246:247], v11 offset:1544\n"
        "ds_read_b64_tr_b16 a[248:249], v15 offset:0\n"
        "ds_read_b64_tr_b16 a[250:251], v15 offset:1536\n"
        "s_waitcnt lgkmcnt(6)\n"
        "s_add_u32 m0, s79, 3072\n"
        "s_mov_b32 s65, 552960\n"
        "buffer_load_dwordx4 v12, s[44:47], s65 offen lds\n"
        "buffer_load_dwordx4 v13, s[44:47], s65 offen offset:1024 lds\n"
        "buffer_load_dwordx4 v14, s[44:47], s65 offen offset:2048 lds\n"
        "s_waitcnt vmcnt(0)\n"
        "ds_read_b64_tr_b16 a[252:253], v11 offset:3072\n"
        "ds_read_b64_tr_b16 a[254:255], v11 offset:4608\n"
        "ds_read_b64_tr_b16 v[80:81], v11 offset:3080\n"
        "ds_read_b64_tr_b16 v[82:83], v11 offset:4616\n"
        "ds_read_b64_tr_b16 v[84:85], v15 offset:3072\n"
        "ds_read_b64_tr_b16 v[86:87], v15 offset:4608\n"
        "s_waitcnt lgkmcnt(6)\n"
        "s_add_u32 m0, s79, 0\n"
        "s_mov_b32 s65, 663552\n"
        "buffer_load_dwordx4 v12, s[44:47], s65 offen lds\n"
        "buffer_load_dwordx4 v13, s[44:47], s65 offen offset:1024 lds\n"
        "buffer_load_dwordx4 v14, s[44:47], s65 offen offset:2048 lds\n"
        "s_waitcnt vmcnt(0)\n"
        "ds_read_b64_tr_b16 v[88:89], v11 offset:0\n"
        "ds_read_b64_tr_b16 v[90:91], v11 offset:1536\n"
        "ds_read_b64_tr_b16 v[92:93], v11 offset:8\n"
        "ds_read_b64_tr_b16 v[94:95], v11 offset:1544\n"
        "ds_read_b64_tr_b16 v[96:97], v15 offset:0\n"
        "ds_read_b64_tr_b16 v[98:99], v15 offset:1536\n"
        "s_waitcnt lgkmcnt(6)\n"
        "s_add_u32 m0, s79, 3072\n"
        "s_mov_b32 s65, 774144\n"
        "buffer_load_dwordx4 v12, s[44:47], s65 offen lds\n"
        "buffer_load_dwordx4 v13, s[44:47], s65 offen offset:1024 lds\n"
        "buffer_load_dwordx4 v14, s[44:47], s65 offen offset:2048 lds\n"
        "s_waitcnt vmcnt(0)\n"
        "ds_read_b64_tr_b16 v[100:101], v11 offset:3072\n"
        "ds_read_b64_tr_b16 v[102:103], v11 offset:4608\n"
        "ds_read_b64_tr_b16 v[104:105], v11 offset:3080\n"
        "ds_read_b64_tr_b16 v[106:107], v11 offset:4616\n"
        "ds_read_b64_tr_b16 v[108:109], v15 offset:3072\n"
        "ds_read_b64_tr_b16 v[110:111], v15 offset:4608\n"
        "s_waitcnt lgkmcnt(6)\n"
        "s_add_u32 m0, s79, 0\n"
        "s_mov_b32 s65, 884736\n"
        "buffer_load_dwordx4 v8, s[44:47], s65 offen lds\n"
        "buffer_load_dwordx4 v9, s[44:47], s65 offen offset:1024 lds\n"
        "buffer_load_dwordx4 v10, s[44:47], s65 offen offset:2048 lds\n"
        "s_waitcnt vmcnt(0)\n"
        "ds_read_b64_tr_b16 v[112:113], v11 offset:0\n"
        "ds_read_b64_tr_b16 v[114:115], v11 offset:1536\n"
        "ds_read_b64_tr_b16 v[116:117], v11 offset:8\n"
        "ds_read_b64_tr_b16 v[118:119], v11 offset:1544\n"
        "ds_read_b64_tr_b16 v[120:121], v15 offset:0\n"
        "ds_read_b64_tr_b16 v[122:123], v15 offset:1536\n"
        "s_waitcnt lgkmcnt(6)\n"
        "s_add_u32 m0, s79, 3072\n"
        "s_mov_b32 s65, 940032\n"
        "buffer_load_dwordx4 v8, s[44:47], s65 offen lds\n"
        "buffer_load_dwordx4 v9, s[44:47], s65 offen offset:1024 lds\n"
        "buffer_load_dwordx4 v10, s[44:47], s65 offen offset:2048 lds\n"
        "s_waitcnt vmcnt(0)\n"
        "ds_read_b64_tr_b16 v[124:125], v11 offset:3072\n"
        "ds_read_b64_tr_b16 v[126:127], v11 offset:4608\n"
        "ds_read_b64_tr_b16 v[128:129], v11 offset:3080\n"
        "ds_read_b64_tr_b16 v[130:131], v11 offset:4616\n"
        "ds_read_b64_tr_b16 v[132:133], v15 offset:3072\n"
        "ds_read_b64_tr_b16 v[134:135], v15 offset:4608\n"
        "s_waitcnt lgkmcnt(6)\n"
        "s_add_u32 m0, s79, 0\n"
        "s_mov_b32 s65, 995328\n"
        "buffer_load_dwordx4 v8, s[44:47], s65 offen lds\n"
        "buffer_load_dwordx4 v9, s[44:47], s65 offen offset:1024 lds\n"
        "buffer_load_dwordx4 v10, s[44:47], s65 offen offset:2048 lds\n"
        "s_waitcnt vmcnt(0)\n"
        "ds_read_b64_tr_b16 v[136:137], v11 offset:0\n"
        "ds_read_b64_tr_b16 v[138:139], v11 offset:1536\n"
        "ds_read_b64_tr_b16 v[140:141], v11 offset:8\n"
        "ds_read_b64_tr_b16 v[142:143], v11 offset:1544\n"
        "ds_read_b64_tr_b16 v[144:145], v15 offset:0\n"
        "ds_read_b64_tr_b16 v[146:147], v15 offset:1536\n"
        "s_waitcnt lgkmcnt(6)\n"
        "s_add_u32 m0, s79, 3072\n"
        "s_mov_b32 s65, 1050624\n"
        "buffer_load_dwordx4 v8, s[44:47], s65 offen lds\n"
        "buffer_load_dwordx4 v9, s[44:47], s65 offen offset:1024 lds\n"
        "buffer_load_dwordx4 v10, s[44:47], s65 offen offset:2048 lds\n"
        "s_waitcnt vmcnt(0)\n"
        "ds_read_b64_tr_b16 v[148:149], v11 offset:3072\n"
        "ds_read_b64_tr_b16 v[150:151], v11 offset:4608\n"
        "ds_read_b64_tr_b16 v[152:153], v11 offset:3080\n"
        "ds_read_b64_tr_b16 v[154:155], v11 offset:4616\n"
        "ds_read_b64_tr_b16 v[156:157], v15 offset:3072\n"
        "ds_read_b64_tr_b16 v[158:159], v15 offset:4608\n"
        "s_waitcnt lgkmcnt(6)\n"
        "s_add_u32 m0, s79, 0\n"
        "s_mov_b32 s65, 1105920\n"
        "buffer_load_dwordx4 v8, s[44:47], s65 offen lds\n"
        "buffer_load_dwordx4 v9, s[44:47], s65 offen offset:1024 lds\n"
        "buffer_load_dwordx4 v10, s[44:47], s65 offen offset:2048 lds\n"
        "s_waitcnt vmcnt(0)\n"
        "ds_read_b64_tr_b16 v[160:161], v11 offset:0\n"
        "ds_read_b64_tr_b16 v[162:163], v11 offset:1536\n"
        "ds_read_b64_tr_b16 v[164:165], v11 offset:8\n"
        "ds_read_b64_tr_b16 v[166:167], v11 offset:1544\n"
        "ds_read_b64_tr_b16 v[168:169], v15 offset:0\n"
        "ds_read_b64_tr_b16 v[170:171], v15 offset:1536\n"
        "s_waitcnt lgkmcnt(6)\n"
        "s_add_u32 m0, s79, 3072\n"
        "s_mov_b32 s65, 1161216\n"
        "buffer_load_dwordx4 v8, s[44:47], s65 offen lds\n"
        "buffer_load_dwordx4 v9, s[44:47], s65 offen offset:1024 lds\n"
        "buffer_load_dwordx4 v10, s[44:47], s65 offen offset:2048 lds\n"
        "s_waitcnt vmcnt(0)\n"
        "ds_read_b64_tr_b16 v[172:173], v11 offset:3072\n"
        "ds_read_b64_tr_b16 v[174:175], v11 offset:4608\n"
        "ds_read_b64_tr_b16 v[176:177], v11 offset:3080\n"
        "ds_read_b64_tr_b16 v[178:179], v11 offset:4616\n"
        "ds_read_b64_tr_b16 v[180:181], v15 offset:3072\n"
        "ds_read_b64_tr_b16 v[182:183], v15 offset:4608\n"
        "s_waitcnt lgkmcnt(6)\n"
        "s_add_u32 m0, s79, 0\n"
        "s_mov_b32 s65, 1216512\n"
        "buffer_load_dwordx4 v8, s[44:47], s65 offen lds\n"
        "buffer_load_dwordx4 v9, s[44:47], s65 offen offset:1024 lds\n"
        "buffer_load_dwordx4 v10, s[44:47], s65 offen offset:2048 lds\n"
        "s_waitcnt vmcnt(0)\n"
        "ds_read_b64_tr_b16 v[184:185], v11 offset:0\n"
        "ds_read_b64_tr_b16 v[186:187], v11 offset:1536\n"
        "ds_read_b64_tr_b16 v[188:189], v11 offset:8\n"
        "ds_read_b64_tr_b16 v[190:191], v11 offset:1544\n"
        "ds_read_b64_tr_b16 v[192:193], v15 offset:0\n"
        "ds_read_b64_tr_b16 v[194:195], v15 offset:1536\n"
        "s_waitcnt lgkmcnt(6)\n"
        "s_add_u32 m0, s79, 3072\n"
        "s_mov_b32 s65, 1271808\n"
        "buffer_load_dwordx4 v8, s[44:47], s65 offen lds\n"
        "buffer_load_dwordx4 v9, s[44:47], s65 offen offset:1024 lds\n"
        "buffer_load_dwordx4 v10, s[44:47], s65 offen offset:2048 lds\n"
        "s_waitcnt vmcnt(0)\n"
        "ds_read_b64_tr_b16 v[196:197], v11 offset:3072\n"
        "ds_read_b64_tr_b16 v[198:199], v11 offset:4608\n"
        "ds_read_b64_tr_b16 v[200:201], v11 offset:3080\n"
        "ds_read_b64_tr_b16 v[202:203], v11 offset:4616\n"
        "ds_read_b64_tr_b16 v[204:205], v15 offset:3072\n"
        "ds_read_b64_tr_b16 v[206:207], v15 offset:4608\n"
        "s_waitcnt lgkmcnt(6)\n"
        "s_add_u32 m0, s79, 0\n"
        "s_mov_b32 s65, 884736\n"
        "buffer_load_dwordx4 v12, s[44:47], s65 offen lds\n"
        "buffer_load_dwordx4 v13, s[44:47], s65 offen offset:1024 lds\n"
        "buffer_load_dwordx4 v14, s[44:47], s65 offen offset:2048 lds\n"
        "s_waitcnt vmcnt(0)\n"
        "ds_read_b64_tr_b16 v[208:209], v11 offset:0\n"
        "ds_read_b64_tr_b16 v[210:211], v11 offset:1536\n"
        "ds_read_b64_tr_b16 v[212:213], v11 offset:8\n"
        "ds_read_b64_tr_b16 v[214:215], v11 offset:1544\n"
        "ds_read_b64_tr_b16 v[216:217], v15 offset:0\n"
        "ds_read_b64_tr_b16 v[218:219], v15 offset:1536\n"
        "s_waitcnt lgkmcnt(6)\n"
        "s_add_u32 m0, s79, 3072\n"
        "s_mov_b32 s65, 995328\n"
        "buffer_load_dwordx4 v12, s[44:47], s65 offen lds\n"
        "buffer_load_dwordx4 v13, s[44:47], s65 offen offset:1024 lds\n"
        "buffer_load_dwordx4 v14, s[44:47], s65 offen offset:2048 lds\n"
        "s_waitcnt vmcnt(0)\n"
        "ds_read_b64_tr_b16 v[220:221], v11 offset:3072\n"
        "ds_read_b64_tr_b16 v[222:223], v11 offset:4608\n"
        "ds_read_b64_tr_b16 v[224:225], v11 offset:3080\n"
        "ds_read_b64_tr_b16 v[226:227], v11 offset:4616\n"
        "ds_read_b64_tr_b16 v[228:229], v15 offset:3072\n"
        "ds_read_b64_tr_b16 v[230:231], v15 offset:4608\n"
        "s_waitcnt lgkmcnt(6)\n"
        "s_add_u32 m0, s79, 0\n"
        "s_mov_b32 s65, 1105920\n"
        "buffer_load_dwordx4 v12, s[44:47], s65 offen lds\n"
        "buffer_load_dwordx4 v13, s[44:47], s65 offen offset:1024 lds\n"
        "buffer_load_dwordx4 v14, s[44:47], s65 offen offset:2048 lds\n"
        "s_waitcnt vmcnt(0)\n"
        "ds_read_b64_tr_b16 v[232:233], v11 offset:0\n"
        "ds_read_b64_tr_b16 v[234:235], v11 offset:1536\n"
        "ds_read_b64_tr_b16 v[236:237], v11 offset:8\n"
        "ds_read_b64_tr_b16 v[238:239], v11 offset:1544\n"
        "ds_read_b64_tr_b16 v[240:241], v15 offset:0\n"
        "ds_read_b64_tr_b16 v[242:243], v15 offset:1536\n"
        "s_waitcnt lgkmcnt(6)\n"
        "s_add_u32 m0, s79, 3072\n"
        "s_mov_b32 s65, 1216512\n"
        "buffer_load_dwordx4 v12, s[44:47], s65 offen lds\n"
        "buffer_load_dwordx4 v13, s[44:47], s65 offen offset:1024 lds\n"
        "buffer_load_dwordx4 v14, s[44:47], s65 offen offset:2048 lds\n"
        "s_waitcnt vmcnt(0)\n"
        "ds_read_b64_tr_b16 v[244:245], v11 offset:3072\n"
        "ds_read_b64_tr_b16 v[246:247], v11 offset:4608\n"
        "ds_read_b64_tr_b16 v[248:249], v11 offset:3080\n"
        "ds_read_b64_tr_b16 v[250:251], v11 offset:4616\n"
        "ds_read_b64_tr_b16 v[252:253], v15 offset:3072\n"
        "ds_read_b64_tr_b16 v[254:255], v15 offset:4608\n"
        "s_waitcnt lgkmcnt(0)\n"
        "s_getpc_b64 s[44:45]\n"
        ".Lf2_pc_%=:\n"
        "s_add_u32 s44, s44, .Lf2_code_%=-.Lf2_pc_%=\n"
        "s_addc_u32 s45, s45, 0\n"
        "s_and_b32 s45, s45, 0xffff\n"
        "s_and_b32 s44, s44, -4\n"
        "s_mov_b32 s46, .Lf2_end_%=-.Lf2_code_%=\n"
        "s_mov_b32 s47, 0x20000\n"
        "s_lshl_b32 s65, s48, 10\n"
        "buffer_load_dwordx4 v[24:27], v1, s[44:47], s65 offen\n"
        "s_add_u32 s65, s65, 4096\n"
        "buffer_load_dwordx4 v[28:31], v1, s[44:47], s65 offen\n"
        "s_add_u32 s65, s65, 4096\n"
        "buffer_load_dwordx4 v[32:35], v1, s[44:47], s65 offen\n"
        "s_add_u32 s65, s65, 4096\n"
        "buffer_load_dwordx4 v[36:39], v1, s[44:47], s65 offen\n"
        "s_add_u32 s65, s65, 4096\n"
        "buffer_load_dwordx4 v[40:43], v1, s[44:47], s65 offen\n"
        "s_add_u32 s65, s65, 4096\n"
        "v_lshrrev_b32 v20, 4, v1\n"
        "s_mov_b32 s50, s56\n"
        "v_lshlrev_b32 v22, 7, v20\n"
        "s_sub_u32 s66, 0, s50\n"
        "s_mov_b32 s52, 32768\n"
        "s_mov_b32 s64, 0\n"
        ".Lf2_pg0_top_%=:\n"
        "buffer_load_dword v21, v22, s[40:43], s52 offen sc1\n"
        "s_waitcnt vmcnt(0)\n"
        "v_cmp_gt_i32 vcc, 0, v21\n"
        "s_cbranch_vccz .Lf2_pg0_ok_%=\n"
        "s_add_u32 s64, s64, 1\n"
        "s_and_b32 s65, s64, 255\n"
        "s_cbranch_scc1 .Lf2_pg0_go_%=\n"
        "s_mov_b32 s65, 49152\n"
        "s_cmp_lt_u32 s64, 1048576\n"
        "s_cbranch_scc1 .Lf2_pg0_chk_%=\n"
        "v_mov_b32 v21, 1\n"
        "buffer_store_dword v21, off, s[40:43], s65 sc1\n"
        "s_waitcnt vmcnt(0)\n"
        ".Lf2_pg0_chk_%=:\n"
        "buffer_load_dword v21, off, s[40:43], s65 sc1\n"
        "s_waitcnt vmcnt(0)\n"
        "v_readfirstlane_b32 s65, v21\n"
        "s_nop 0\n"
        "s_cmp_lg_u32 s65, 1\n"
        "s_cbranch_scc1 .Lf2_pg0_go_%=\n"
        "s_endpgm\n"
        ".Lf2_pg0_go_%=:\n"
        "s_sleep 2\n"
        "s_branch .Lf2_pg0_top_%=\n"
        ".Lf2_pg0_ok_%=:\n"
        "v_cmp_eq_u32 s[52:53], 1, v21\n"
        "s_nop 1\n"
        "s_mov_b32 s54, 40960\n"
        "s_mov_b32 s64, 0\n"
        ".Lf2_pg1_top_%=:\n"
        "buffer_load_dword v21, v22, s[40:43], s54 offen sc1\n"
        "s_waitcnt vmcnt(0)\n"
        "v_cmp_gt_i32 vcc, 0, v21\n"
        "s_cbranch_vccz .Lf2_pg1_ok_%=\n"
        "s_add_u32 s64, s64, 1\n"
        "s_and_b32 s65, s64, 255\n"
        "s_cbranch_scc1 .Lf2_pg1_go_%=\n"
        "s_mov_b32 s65, 49152\n"
        "s_cmp_lt_u32 s64, 1048576\n"
        "s_cbranch_scc1 .Lf2_pg1_chk_%=\n"
        "v_mov_b32 v21, 1\n"
        "buffer_store_dword v21, off, s[40:43], s65 sc1\n"
        "s_waitcnt vmcnt(0)\n"
        ".Lf2_pg1_chk_%=:\n"
        "buffer_load_dword v21, off, s[40:43], s65 sc1\n"
        "s_waitcnt vmcnt(0)\n"
        "v_readfirstlane_b32 s65, v21\n"
        "s_nop 0\n"
        "s_cmp_lg_u32 s65, 1\n"
        "s_cbranch_scc1 .Lf2_pg1_go_%=\n"
        "s_endpgm\n"
        ".Lf2_pg1_go_%=:\n"
        "s_sleep 2\n"
        "s_branch .Lf2_pg1_top_%=\n"
        ".Lf2_pg1_ok_%=:\n"
        "v_cmp_eq_u32 s[54:55], 1, v21\n"
        "s_nop 1\n"
        "s_mov_b32 vcc_lo, 0x55555555\n"
        "s_mov_b32 vcc_hi, 0x55555555\n"
        "s_xor_b64 vcc, vcc, s[52:53]\n"
        "s_bcnt1_i32_b64 s65, vcc\n"
        "s_sub_u32 s64, s65, 23\n"
        "s_cmp_lt_u32 s64, 19\n"
        "s_cbranch_scc1 .Lf2_rawmap_%=\n"
        "s_cmp_lt_u32 s65, 32\n"
        "s_cselect_b32 s65, 0, -1\n"
        "s_xor_b32 s52, s65, 0x55555555\n"
        "s_xor_b32 s53, s65, 0x55555555\n"
        "s_xor_b32 s54, s65, 0x55555555\n"
        "s_xor_b32 s55, s65, 0xaaaaaaaa\n"
        ".Lf2_rawmap_%=:\n"
        "s_xor_b32 s52, s52, s66\n"
        "s_xor_b32 s53, s53, s66\n"
        "s_xor_b32 s54, s54, s66\n"
        "s_xor_b32 s55, s55, s66\n"
        "s_and_b32 s76, s55, 1\n"
        "s_lshl_b32 s76, s76, 13\n"
        "s_add_u32 s60, s60, s76\n"
        "s_barrier\n"
        "v_mov_b32 v48, v6\n"
        "s_mov_b32 s68, -1\n"
        "s_mov_b32 s69, 0\n"
        "s_mov_b32 s78, 0\n"
        "s_mov_b32 s74, -1\n"
        "s_mov_b32 s86, s76\n"
        "s_lshr_b32 s85, s61, 16\n"
        "s_and_b32 s61, s61, 0xffff\n"
        "s_mov_b32 s89, 241172480\n"
        "s_mov_b32 s84, 241172480\n"
        "v_mov_b32 v6, v0\n"
        "s_mov_b32 s80, 0xbfb8aa3b\n"
        "s_lshr_b32 s67, s69, 1\n"
        "s_lshr_b32 s65, s67, 1\n"
        "s_bitcmp1_b32 s54, s65\n"
        "s_cselect_b32 s65, 8192, 0\n"
        "s_lshl_b32 s77, s67, 20\n"
        "s_add_u32 s77, s77, s65\n"
        "s_add_u32 s71, s77, s58\n"
        "s_add_u32 s81, s77, s62\n"
        "s_add_u32 s78, s78, 36864\n"
        "s_cmp_eq_u32 s78, 110592\n"
        "s_cselect_b32 s78, 0, s78\n"
        "s_add_u32 m0, s78, s61\n"
        "s_lshr_b32 s70, s69, 0\n"
        "s_branch .Lf2_wait1_%=\n"
        ".Lf2_code_%=:\n"
        ".Lf2_i0_%=:\n"
        "v_mfma_f32_16x16x32_bf16 v[24:27], a[0:3], v[8:11], 0\n"
        "v_mfma_f32_16x16x32_bf16 v[28:31], a[4:7], v[8:11], 0\n"
        "s_add_u32 s71, s77, s58\n"
        "buffer_load_dword v4, v2, s[40:43], s86 offen sc1\n"
        "s_add_u32 s71, s71, 49152\n"
        "v_mfma_f32_16x16x32_bf16 v[32:35], a[8:11], v[8:11], 0\n"
        "s_add_u32 s81, s77, s62\n"
        "ds_read_b128 v[8:11], v0 offset:4096\n"
        "s_add_u32 s81, s81, 4096\n"
        "v_mfma_f32_16x16x32_bf16 v[24:27], a[12:15], v[12:15], v[24:27]\n"
        "s_add_u32 s78, s78, 36864\n"
        "s_cmp_eq_u32 s78, 110592\n"
        "s_cselect_b32 s78, 0, s78\n"
        "v_mfma_f32_16x16x32_bf16 v[28:31], a[16:19], v[12:15], v[28:31]\n"
        "s_add_u32 m0, s78, s61\n"
        "s_lshr_b32 s70, s69, 0\n"
        "v_cvt_pk_bf16_f32 v36, v36, v37\n"
        "v_mfma_f32_16x16x32_bf16 v[32:35], a[20:23], v[12:15], v[32:35]\n"
        "s_waitcnt lgkmcnt(1)\n"
        "ds_read_b128 v[12:15], v0 offset:5120\n"
        "v_cvt_pk_bf16_f32 v37, v38, v39\n"
        "v_mfma_f32_16x16x32_bf16 v[24:27], a[24:27], v[16:19], v[24:27]\n"
        "v_cvt_pk_bf16_f32 v38, v40, v41\n"
        "v_cvt_pk_bf16_f32 v39, v42, v43\n"
        "v_mfma_f32_16x16x32_bf16 v[28:31], a[28:31], v[16:19], v[28:31]\n"
        "buffer_store_dwordx4 v[36:39], v48, s[40:43], s72 offen offset:0 sc1\n"
        "v_mfma_f32_16x16x32_bf16 v[32:35], a[32:35], v[16:19], v[32:35]\n"
        "ds_read_b128 v[16:19], v0 offset:6144\n"
        "v_cvt_pk_bf16_f32 v44, v44, v45\n"
        "v_mfma_f32_16x16x32_bf16 v[24:27], a[36:39], v[20:23], v[24:27]\n"
        "v_cvt_pk_bf16_f32 v45, v46, v47\n"
        "s_nop 0\n"
        "v_mfma_f32_16x16x32_bf16 v[28:31], a[40:43], v[20:23], v[28:31]\n"
        "v_permlane16_swap_b32 v44, v46\n"
        "v_mfma_f32_16x16x32_bf16 v[32:35], a[44:47], v[20:23], v[32:35]\n"
        "s_waitcnt lgkmcnt(1)\n"
        "ds_read_b128 v[20:23], v0 offset:7168\n"
        "v_mfma_f32_16x16x32_bf16 v[24:27], a[48:51], v[8:11], v[24:27]\n"
        "v_permlane16_swap_b32 v45, v47\n"
        "v_mfma_f32_16x16x32_bf16 v[28:31], a[52:55], v[8:11], v[28:31]\n"
        "v_mfma_f32_16x16x32_bf16 v[32:35], a[56:59], v[8:11], v[32:35]\n"
        "ds_read_b128 v[8:11], v0 offset:8192\n"
        "v_mfma_f32_16x16x32_bf16 v[24:27], a[60:63], v[12:15], v[24:27]\n"
        "buffer_store_dwordx4 v[44:47], v7, s[40:43], s72 offen offset:0 sc1\n"
        "v_mfma_f32_16x16x32_bf16 v[28:31], a[64:67], v[12:15], v[28:31]\n"
        "v_mfma_f32_16x16x32_bf16 v[32:35], a[68:71], v[12:15], v[32:35]\n"
        "s_waitcnt lgkmcnt(1)\n"
        "ds_read_b128 v[12:15], v0 offset:9216\n"
        "v_mfma_f32_16x16x32_bf16 v[24:27], a[72:75], v[16:19], v[24:27]\n"
        "v_mfma_f32_16x16x32_bf16 v[28:31], a[76:79], v[16:19], v[28:31]\n"
        "v_mfma_f32_16x16x32_bf16 v[32:35], a[80:83], v[16:19], v[32:35]\n"
        "ds_read_b128 v[16:19], v0 offset:10240\n"
        "v_mfma_f32_16x16x32_bf16 v[24:27], a[84:87], v[20:23], v[24:27]\n"
        "v_mfma_f32_16x16x32_bf16 v[28:31], a[88:91], v[20:23], v[28:31]\n"
        "v_mfma_f32_16x16x32_bf16 v[32:35], a[92:95], v[20:23], v[32:35]\n"
        "s_waitcnt lgkmcnt(1)\n"
        "ds_read_b128 v[20:23], v0 offset:11264\n"
        "v_mfma_f32_16x16x32_bf16 v[24:27], a[96:99], v[8:11], v[24:27]\n"
        "v_mfma_f32_16x16x32_bf16 v[28:31], a[100:103], v[8:11], v[28:31]\n"
        "s_waitcnt vmcnt(3)\n"
        "s_barrier\n"
        "v_mfma_f32_16x16x32_bf16 v[32:35], a[104:107], v[8:11], v[32:35]\n"
        "ds_read_b128 v[8:11], v0 offset:12288\n"
        "buffer_store_dword v5, v3, s[40:43], s84 offen sc1\n"
        "v_mfma_f32_16x16x32_bf16 v[24:27], a[108:111], v[12:15], v[24:27]\n"
        "v_mov_b32 v5, s74\n"
        "v_mfma_f32_16x16x32_bf16 v[28:31], a[112:115], v[12:15], v[28:31]\n"
        "v_mfma_f32_16x16x32_bf16 v[32:35], a[116:119], v[12:15], v[32:35]\n"
        "s_waitcnt lgkmcnt(1)\n"
        "ds_read_b128 v[12:15], v0 offset:13312\n"
        "v_mfma_f32_16x16x32_bf16 v[24:27], a[120:123], v[16:19], v[24:27]\n"
        "v_mfma_f32_16x16x32_bf16 v[28:31], a[124:127], v[16:19], v[28:31]\n"
        "s_waitcnt vmcnt(3)\n"
        "v_cmp_gt_i32 vcc, s70, v4\n"
        "v_mfma_f32_16x16x32_bf16 v[32:35], a[128:131], v[16:19], v[32:35]\n"
        "ds_read_b128 v[16:19], v0 offset:14336\n"
        "v_mfma_f32_16x16x32_bf16 v[24:27], a[132:135], v[20:23], v[24:27]\n"
        "s_and_b64 vcc, vcc, 63\n"
        "v_mfma_f32_16x16x32_bf16 v[28:31], a[136:139], v[20:23], v[28:31]\n"
        "s_cbranch_vccnz .Lf2_i00_%=\n"
        "v_mfma_f32_16x16x32_bf16 v[32:35], a[140:143], v[20:23], v[32:35]\n"
        "s_waitcnt lgkmcnt(1)\n"
        "ds_read_b128 v[20:23], v0 offset:15360\n"
        "buffer_load_dwordx4 v1, s[40:43], s71 offen offset:0 sc1 lds\n"
        "v_mfma_f32_16x16x32_bf16 v[24:27], a[144:147], v[8:11], v[24:27]\n"
        "v_mfma_f32_16x16x32_bf16 v[28:31], a[148:151], v[8:11], v[28:31]\n"
        "v_mfma_f32_16x16x32_bf16 v[32:35], a[152:155], v[8:11], v[32:35]\n"
        "ds_read_b128 v[8:11], v0 offset:16384\n"
        "v_mfma_f32_16x16x32_bf16 v[24:27], a[156:159], v[12:15], v[24:27]\n"
        "s_getreg_b32 s88, hwreg(HW_REG_IB_STS, 0, 4)\n"
        "buffer_load_dwordx4 v1, s[40:43], s71 offen offset:1024 sc1 lds\n"
        "s_add_u32 m0, m0, s85\n"
        "v_mfma_f32_16x16x32_bf16 v[28:31], a[160:163], v[12:15], v[28:31]\n"
        "s_cmp_le_u32 s88, 2\n"
        "s_cselect_b32 s88, s73, s89\n"
        "s_cselect_b32 s73, s89, s73\n"
        "v_mfma_f32_16x16x32_bf16 v[32:35], a[164:167], v[12:15], v[32:35]\n"
        "s_waitcnt lgkmcnt(1)\n"
        "ds_read_b128 v[12:15], v0 offset:17408\n"
        "buffer_store_dword v5, v3, s[40:43], s88 offen sc1\n"
        "v_mfma_f32_16x16x32_bf16 v[24:27], a[168:171], v[16:19], v[24:27]\n"
        "s_add_u32 s71, s71, 16384\n"
        "v_mfma_f32_16x16x32_bf16 v[28:31], a[172:175], v[16:19], v[28:31]\n"
        "buffer_load_dwordx4 v1, s[40:43], s81 offen offset:0 sc1 lds\n"
        "s_sub_u32 m0, m0, s85\n"
        "v_mfma_f32_16x16x32_bf16 v[32:35], a[176:179], v[16:19], v[32:35]\n"
        "s_add_u32 m0, m0, 12288\n"
        "ds_read_b128 v[16:19], v0 offset:18432\n"
        "s_add_u32 s81, s81, 16384\n"
        "v_mfma_f32_16x16x32_bf16 v[24:27], a[180:183], v[20:23], v[24:27]\n"
        "v_mfma_f32_16x16x32_bf16 v[28:31], a[184:187], v[20:23], v[28:31]\n"
        "v_mfma_f32_16x16x32_bf16 v[32:35], a[188:191], v[20:23], v[32:35]\n"
        "s_waitcnt lgkmcnt(1)\n"
        "ds_read_b128 v[20:23], v0 offset:19456\n"
        "buffer_load_dwordx4 v1, s[40:43], s71 offen offset:0 sc1 lds\n"
        "v_mfma_f32_16x16x32_bf16 v[24:27], a[192:195], v[8:11], v[24:27]\n"
        "s_getreg_b32 s88, hwreg(HW_REG_IB_STS, 0, 4)\n"
        "v_mfma_f32_16x16x32_bf16 v[28:31], a[196:199], v[8:11], v[28:31]\n"
        "s_cmp_le_u32 s88, 6\n"
        "s_cselect_b32 s88, s73, s89\n"
        "s_cselect_b32 s73, s89, s73\n"
        "v_mfma_f32_16x16x32_bf16 v[32:35], a[200:203], v[8:11], v[32:35]\n"
        "ds_read_b128 v[8:11], v0 offset:20480\n"
        "buffer_store_dword v5, v3, s[40:43], s88 offen sc1\n"
        "v_mfma_f32_16x16x32_bf16 v[24:27], a[204:207], v[12:15], v[24:27]\n"
        "buffer_load_dwordx4 v1, s[40:43], s71 offen offset:1024 sc1 lds\n"
        "s_add_u32 m0, m0, s85\n"
        "v_mfma_f32_16x16x32_bf16 v[28:31], a[208:211], v[12:15], v[28:31]\n"
        "s_add_u32 s71, s71, 16384\n"
        "v_mfma_f32_16x16x32_bf16 v[32:35], a[212:215], v[12:15], v[32:35]\n"
        "s_waitcnt lgkmcnt(1)\n"
        "ds_read_b128 v[12:15], v0 offset:21504\n"
        "v_mfma_f32_16x16x32_bf16 v[24:27], a[216:219], v[16:19], v[24:27]\n"
        "v_mfma_f32_16x16x32_bf16 v[28:31], a[220:223], v[16:19], v[28:31]\n"
        "s_getreg_b32 s88, hwreg(HW_REG_IB_STS, 0, 4)\n"
        "buffer_load_dwordx4 v1, s[40:43], s81 offen offset:0 sc1 lds\n"
        "s_sub_u32 m0, m0, s85\n"
        "v_mfma_f32_16x16x32_bf16 v[32:35], a[224:227], v[16:19], v[32:35]\n"
        "s_add_u32 m0, m0, 12288\n"
        "ds_read_b128 v[16:19], v0 offset:22528\n"
        "s_add_u32 s81, s81, 16384\n"
        "v_mfma_f32_16x16x32_bf16 v[24:27], a[228:231], v[20:23], v[24:27]\n"
        "s_cmp_le_u32 s88, 8\n"
        "s_cselect_b32 s88, s73, s89\n"
        "s_cselect_b32 s73, s89, s73\n"
        "v_mfma_f32_16x16x32_bf16 v[28:31], a[232:235], v[20:23], v[28:31]\n"
        "buffer_store_dword v5, v3, s[40:43], s88 offen sc1\n"
        "v_mfma_f32_16x16x32_bf16 v[32:35], a[236:239], v[20:23], v[32:35]\n"
        "s_waitcnt lgkmcnt(1)\n"
        "ds_read_b128 v[20:23], v0 offset:23552\n"
        "buffer_load_dwordx4 v1, s[40:43], s71 offen offset:0 sc1 lds\n"
        "v_mfma_f32_16x16x32_bf16 v[24:27], a[240:243], v[8:11], v[24:27]\n"
        "v_mfma_f32_16x16x32_bf16 v[28:31], a[244:247], v[8:11], v[28:31]\n"
        "v_mfma_f32_16x16x32_bf16 v[32:35], a[248:251], v[8:11], v[32:35]\n"
        "ds_read_b128 v[8:11], v0 offset:24576\n"
        "v_mfma_f32_16x16x32_bf16 v[24:27], a[252:255], v[12:15], v[24:27]\n"
        "buffer_load_dwordx4 v1, s[40:43], s71 offen offset:1024 sc1 lds\n"
        "s_add_u32 m0, m0, s85\n"
        "v_mfma_f32_16x16x32_bf16 v[28:31], v[80:83], v[12:15], v[28:31]\n"
        "s_add_u32 s71, s71, 16384\n"
        "v_mfma_f32_16x16x32_bf16 v[32:35], v[84:87], v[12:15], v[32:35]\n"
        "s_waitcnt lgkmcnt(1)\n"
        "ds_read_b128 v[12:15], v0 offset:25600\n"
        "v_mfma_f32_16x16x32_bf16 v[24:27], v[88:91], v[16:19], v[24:27]\n"
        "v_mfma_f32_16x16x32_bf16 v[28:31], v[92:95], v[16:19], v[28:31]\n"
        "buffer_load_dwordx4 v1, s[40:43], s81 offen offset:0 sc1 lds\n"
        "s_add_u32 s69, s69, 1\n"
        "v_mfma_f32_16x16x32_bf16 v[32:35], v[96:99], v[16:19], v[32:35]\n"
        "ds_read_b128 v[16:19], v0 offset:26624\n"
        "v_mfma_f32_16x16x32_bf16 v[24:27], v[100:103], v[20:23], v[24:27]\n"
        "s_waitcnt vmcnt(13)\n"
        "buffer_store_dword v5, v3, s[40:43], s73 offen sc1\n"
        "s_add_u32 s74, s74, 1\n"
        "v_mfma_f32_16x16x32_bf16 v[28:31], v[104:107], v[20:23], v[28:31]\n"
        "s_mov_b32 s84, s89\n"
        "s_mov_b32 s73, s60\n"
        "s_add_u32 s68, s68, 1\n"
        "v_mfma_f32_16x16x32_bf16 v[32:35], v[108:111], v[20:23], v[32:35]\n"
        "s_waitcnt lgkmcnt(1)\n"
        "ds_read_b128 v[20:23], v0 offset:27648\n"
        "s_lshr_b32 s66, s68, 2\n"
        "v_mfma_f32_16x16x32_bf16 v[24:27], v[112:115], v[8:11], v[24:27]\n"
        "s_add_u32 s66, s66, 1\n"
        "s_bitcmp1_b32 s55, s66\n"
        "s_cselect_b32 s66, 8192, 0\n"
        "v_mfma_f32_16x16x32_bf16 v[28:31], v[116:119], v[8:11], v[28:31]\n"
        "s_lshl_b32 s75, s68, 19\n"
        "s_add_u32 s75, s75, s66\n"
        "s_add_u32 s72, s75, s59\n"
        "v_mfma_f32_16x16x32_bf16 v[32:35], v[120:123], v[8:11], v[32:35]\n"
        "ds_read_b128 v[8:11], v0 offset:28672\n"
        "v_mfma_f32_16x16x32_bf16 v[24:27], v[124:127], v[12:15], v[24:27]\n"
        "v_mfma_f32_16x16x32_bf16 v[28:31], v[128:131], v[12:15], v[28:31]\n"
        "v_mfma_f32_16x16x32_bf16 v[32:35], v[132:135], v[12:15], v[32:35]\n"
        "s_waitcnt lgkmcnt(1)\n"
        "ds_read_b128 v[12:15], v0 offset:29696\n"
        "v_mfma_f32_16x16x32_bf16 v[24:27], v[136:139], v[16:19], v[24:27]\n"
        "v_mfma_f32_16x16x32_bf16 v[28:31], v[140:143], v[16:19], v[28:31]\n"
        "v_mfma_f32_16x16x32_bf16 v[32:35], v[144:147], v[16:19], v[32:35]\n"
        "ds_read_b128 v[16:19], v0 offset:30720\n"
        "v_mfma_f32_16x16x32_bf16 v[24:27], v[148:151], v[20:23], v[24:27]\n"
        "v_mfma_f32_16x16x32_bf16 v[28:31], v[152:155], v[20:23], v[28:31]\n"
        "v_mfma_f32_16x16x32_bf16 v[32:35], v[156:159], v[20:23], v[32:35]\n"
        "s_waitcnt lgkmcnt(1)\n"
        "ds_read_b128 v[20:23], v0 offset:31744\n"
        "v_mfma_f32_16x16x32_bf16 v[24:27], v[160:163], v[8:11], v[24:27]\n"
        "v_mfma_f32_16x16x32_bf16 v[28:31], v[164:167], v[8:11], v[28:31]\n"
        "v_mfma_f32_16x16x32_bf16 v[32:35], v[168:171], v[8:11], v[32:35]\n"
        "ds_read_b128 v[8:11], v0 offset:32768\n"
        "v_mfma_f32_16x16x32_bf16 v[24:27], v[172:175], v[12:15], v[24:27]\n"
        "v_mfma_f32_16x16x32_bf16 v[28:31], v[176:179], v[12:15], v[28:31]\n"
        "v_mfma_f32_16x16x32_bf16 v[32:35], v[180:183], v[12:15], v[32:35]\n"
        "s_waitcnt lgkmcnt(1)\n"
        "ds_read_b128 v[12:15], v0 offset:33792\n"
        "v_mfma_f32_16x16x32_bf16 v[24:27], v[184:187], v[16:19], v[24:27]\n"
        "v_mfma_f32_16x16x32_bf16 v[28:31], v[188:191], v[16:19], v[28:31]\n"
        "v_mfma_f32_16x16x32_bf16 v[32:35], v[192:195], v[16:19], v[32:35]\n"
        "ds_read_b128 v[16:19], v0 offset:34816\n"
        "v_mfma_f32_16x16x32_bf16 v[24:27], v[196:199], v[20:23], v[24:27]\n"
        "v_mfma_f32_16x16x32_bf16 v[28:31], v[200:203], v[20:23], v[28:31]\n"
        "v_mfma_f32_16x16x32_bf16 v[32:35], v[204:207], v[20:23], v[32:35]\n"
        "s_waitcnt lgkmcnt(1)\n"
        "ds_read_b128 v[20:23], v0 offset:35840\n"
        "v_mfma_f32_16x16x32_bf16 v[24:27], v[208:211], v[8:11], v[24:27]\n"
        "v_mfma_f32_16x16x32_bf16 v[28:31], v[212:215], v[8:11], v[28:31]\n"
        "s_waitcnt vmcnt(9)\n"
        "s_barrier\n"
        "v_add_u32 v0, s78, v6\n"
        "v_mfma_f32_16x16x32_bf16 v[32:35], v[216:219], v[8:11], v[32:35]\n"
        "ds_read_b128 v[8:11], v0 offset:0\n"
        "v_mfma_f32_16x16x32_bf16 v[24:27], v[220:223], v[12:15], v[24:27]\n"
        "v_mfma_f32_16x16x32_bf16 v[28:31], v[224:227], v[12:15], v[28:31]\n"
        "v_mfma_f32_16x16x32_bf16 v[32:35], v[228:231], v[12:15], v[32:35]\n"
        "s_waitcnt lgkmcnt(1)\n"
        "ds_read_b128 v[12:15], v0 offset:1024\n"
        "v_mfma_f32_16x16x32_bf16 v[24:27], v[232:235], v[16:19], v[24:27]\n"
        "v_mfma_f32_16x16x32_bf16 v[28:31], v[236:239], v[16:19], v[28:31]\n"
        "v_mfma_f32_16x16x32_bf16 v[32:35], v[240:243], v[16:19], v[32:35]\n"
        "ds_read_b128 v[16:19], v0 offset:2048\n"
        "v_mfma_f32_16x16x32_bf16 v[24:27], v[244:247], v[20:23], v[24:27]\n"
        "v_mfma_f32_16x16x32_bf16 v[28:31], v[248:251], v[20:23], v[28:31]\n"
        "v_mfma_f32_16x16x32_bf16 v[32:35], v[252:255], v[20:23], v[32:35]\n"
        "s_waitcnt lgkmcnt(1)\n"
        "ds_read_b128 v[20:23], v0 offset:3072\n"
        "s_branch .Lf2_i1_%=\n"
        ".Lf2_i00_%=:\n"
        "v_mfma_f32_16x16x32_bf16 v[32:35], a[140:143], v[20:23], v[32:35]\n"
        "s_waitcnt lgkmcnt(1)\n"
        "ds_read_b128 v[20:23], v0 offset:15360\n"
        "buffer_load_dword v4, v2, s[40:43], s86 offen sc1\n"
        "v_mfma_f32_16x16x32_bf16 v[24:27], a[144:147], v[8:11], v[24:27]\n"
        "v_mfma_f32_16x16x32_bf16 v[28:31], a[148:151], v[8:11], v[28:31]\n"
        "v_mfma_f32_16x16x32_bf16 v[32:35], a[152:155], v[8:11], v[32:35]\n"
        "ds_read_b128 v[8:11], v0 offset:16384\n"
        "v_mfma_f32_16x16x32_bf16 v[24:27], a[156:159], v[12:15], v[24:27]\n"
        "s_getreg_b32 s88, hwreg(HW_REG_IB_STS, 0, 4)\n"
        "v_mfma_f32_16x16x32_bf16 v[28:31], a[160:163], v[12:15], v[28:31]\n"
        "s_cmp_le_u32 s88, 2\n"
        "s_cselect_b32 s88, s73, s89\n"
        "s_cselect_b32 s73, s89, s73\n"
        "v_mfma_f32_16x16x32_bf16 v[32:35], a[164:167], v[12:15], v[32:35]\n"
        "s_waitcnt lgkmcnt(1)\n"
        "ds_read_b128 v[12:15], v0 offset:17408\n"
        "buffer_store_dword v5, v3, s[40:43], s88 offen sc1\n"
        "v_mfma_f32_16x16x32_bf16 v[24:27], a[168:171], v[16:19], v[24:27]\n"
        "v_mfma_f32_16x16x32_bf16 v[28:31], a[172:175], v[16:19], v[28:31]\n"
        "v_mfma_f32_16x16x32_bf16 v[32:35], a[176:179], v[16:19], v[32:35]\n"
        "ds_read_b128 v[16:19], v0 offset:18432\n"
        "v_mfma_f32_16x16x32_bf16 v[24:27], a[180:183], v[20:23], v[24:27]\n"
        "v_mfma_f32_16x16x32_bf16 v[28:31], a[184:187], v[20:23], v[28:31]\n"
        "v_mfma_f32_16x16x32_bf16 v[32:35], a[188:191], v[20:23], v[32:35]\n"
        "s_waitcnt lgkmcnt(1)\n"
        "ds_read_b128 v[20:23], v0 offset:19456\n"
        "v_mfma_f32_16x16x32_bf16 v[24:27], a[192:195], v[8:11], v[24:27]\n"
        "s_getreg_b32 s88, hwreg(HW_REG_IB_STS, 0, 4)\n"
        "v_mfma_f32_16x16x32_bf16 v[28:31], a[196:199], v[8:11], v[28:31]\n"
        "s_cmp_le_u32 s88, 3\n"
        "s_cselect_b32 s88, s73, s89\n"
        "s_cselect_b32 s73, s89, s73\n"
        "v_mfma_f32_16x16x32_bf16 v[32:35], a[200:203], v[8:11], v[32:35]\n"
        "ds_read_b128 v[8:11], v0 offset:20480\n"
        "buffer_store_dword v5, v3, s[40:43], s88 offen sc1\n"
        "v_mfma_f32_16x16x32_bf16 v[24:27], a[204:207], v[12:15], v[24:27]\n"
        "v_mfma_f32_16x16x32_bf16 v[28:31], a[208:211], v[12:15], v[28:31]\n"
        "v_mfma_f32_16x16x32_bf16 v[32:35], a[212:215], v[12:15], v[32:35]\n"
        "s_waitcnt lgkmcnt(1)\n"
        "ds_read_b128 v[12:15], v0 offset:21504\n"
        "v_mfma_f32_16x16x32_bf16 v[24:27], a[216:219], v[16:19], v[24:27]\n"
        "v_mfma_f32_16x16x32_bf16 v[28:31], a[220:223], v[16:19], v[28:31]\n"
        "s_getreg_b32 s88, hwreg(HW_REG_IB_STS, 0, 4)\n"
        "v_mfma_f32_16x16x32_bf16 v[32:35], a[224:227], v[16:19], v[32:35]\n"
        "ds_read_b128 v[16:19], v0 offset:22528\n"
        "v_mfma_f32_16x16x32_bf16 v[24:27], a[228:231], v[20:23], v[24:27]\n"
        "s_cmp_le_u32 s88, 4\n"
        "s_cselect_b32 s88, s73, s89\n"
        "s_cselect_b32 s73, s89, s73\n"
        "v_mfma_f32_16x16x32_bf16 v[28:31], a[232:235], v[20:23], v[28:31]\n"
        "buffer_store_dword v5, v3, s[40:43], s88 offen sc1\n"
        "v_mfma_f32_16x16x32_bf16 v[32:35], a[236:239], v[20:23], v[32:35]\n"
        "s_waitcnt lgkmcnt(1)\n"
        "ds_read_b128 v[20:23], v0 offset:23552\n"
        "v_mfma_f32_16x16x32_bf16 v[24:27], a[240:243], v[8:11], v[24:27]\n"
        "v_mfma_f32_16x16x32_bf16 v[28:31], a[244:247], v[8:11], v[28:31]\n"
        "s_waitcnt vmcnt(3)\n"
        "v_cmp_gt_i32 vcc, s70, v4\n"
        "v_mfma_f32_16x16x32_bf16 v[32:35], a[248:251], v[8:11], v[32:35]\n"
        "ds_read_b128 v[8:11], v0 offset:24576\n"
        "v_mfma_f32_16x16x32_bf16 v[24:27], a[252:255], v[12:15], v[24:27]\n"
        "s_and_b64 vcc, vcc, 63\n"
        "v_mfma_f32_16x16x32_bf16 v[28:31], v[80:83], v[12:15], v[28:31]\n"
        "s_cbranch_vccnz .Lf2_i01_%=\n"
        "v_mfma_f32_16x16x32_bf16 v[32:35], v[84:87], v[12:15], v[32:35]\n"
        "s_waitcnt lgkmcnt(1)\n"
        "ds_read_b128 v[12:15], v0 offset:25600\n"
        "buffer_load_dwordx4 v1, s[40:43], s71 offen offset:0 sc1 lds\n"
        "v_mfma_f32_16x16x32_bf16 v[24:27], v[88:91], v[16:19], v[24:27]\n"
        "v_mfma_f32_16x16x32_bf16 v[28:31], v[92:95], v[16:19], v[28:31]\n"
        "v_mfma_f32_16x16x32_bf16 v[32:35], v[96:99], v[16:19], v[32:35]\n"
        "ds_read_b128 v[16:19], v0 offset:26624\n"
        "buffer_load_dwordx4 v1, s[40:43], s71 offen offset:1024 sc1 lds\n"
        "v_mfma_f32_16x16x32_bf16 v[24:27], v[100:103], v[20:23], v[24:27]\n"
        "buffer_store_dword v5, v3, s[40:43], s73 offen sc1\n"
        "s_add_u32 s74, s74, 1\n"
        "v_mfma_f32_16x16x32_bf16 v[28:31], v[104:107], v[20:23], v[28:31]\n"
        "s_mov_b32 s84, s89\n"
        "s_mov_b32 s73, s60\n"
        "s_add_u32 s68, s68, 1\n"
        "v_mfma_f32_16x16x32_bf16 v[32:35], v[108:111], v[20:23], v[32:35]\n"
        "s_waitcnt lgkmcnt(1)\n"
        "ds_read_b128 v[20:23], v0 offset:27648\n"
        "s_lshr_b32 s66, s68, 2\n"
        "v_mfma_f32_16x16x32_bf16 v[24:27], v[112:115], v[8:11], v[24:27]\n"
        "s_add_u32 s66, s66, 1\n"
        "s_bitcmp1_b32 s55, s66\n"
        "s_cselect_b32 s66, 8192, 0\n"
        "v_mfma_f32_16x16x32_bf16 v[28:31], v[116:119], v[8:11], v[28:31]\n"
        "s_lshl_b32 s75, s68, 19\n"
        "s_add_u32 s75, s75, s66\n"
        "s_add_u32 s72, s75, s59\n"
        "v_mfma_f32_16x16x32_bf16 v[32:35], v[120:123], v[8:11], v[32:35]\n"
        "s_add_u32 m0, m0, s85\n"
        "ds_read_b128 v[8:11], v0 offset:28672\n"
        "s_add_u32 s71, s71, 16384\n"
        "v_mfma_f32_16x16x32_bf16 v[24:27], v[124:127], v[12:15], v[24:27]\n"
        "buffer_load_dwordx4 v1, s[40:43], s81 offen offset:0 sc1 lds\n"
        "s_sub_u32 m0, m0, s85\n"
        "v_mfma_f32_16x16x32_bf16 v[28:31], v[128:131], v[12:15], v[28:31]\n"
        "s_add_u32 m0, m0, 12288\n"
        "s_add_u32 s81, s81, 16384\n"
        "buffer_load_dwordx4 v1, s[40:43], s71 offen offset:0 sc1 lds\n"
        "v_mfma_f32_16x16x32_bf16 v[32:35], v[132:135], v[12:15], v[32:35]\n"
        "s_waitcnt lgkmcnt(1)\n"
        "ds_read_b128 v[12:15], v0 offset:29696\n"
        "buffer_load_dwordx4 v1, s[40:43], s71 offen offset:1024 sc1 lds\n"
        "v_mfma_f32_16x16x32_bf16 v[24:27], v[136:139], v[16:19], v[24:27]\n"
        "s_add_u32 m0, m0, s85\n"
        "s_add_u32 s71, s71, 16384\n"
        "v_mfma_f32_16x16x32_bf16 v[28:31], v[140:143], v[16:19], v[28:31]\n"
        "v_mfma_f32_16x16x32_bf16 v[32:35], v[144:147], v[16:19], v[32:35]\n"
        "ds_read_b128 v[16:19], v0 offset:30720\n"
        "v_mfma_f32_16x16x32_bf16 v[24:27], v[148:151], v[20:23], v[24:27]\n"
        "v_mfma_f32_16x16x32_bf16 v[28:31], v[152:155], v[20:23], v[28:31]\n"
        "v_mfma_f32_16x16x32_bf16 v[32:35], v[156:159], v[20:23], v[32:35]\n"
        "s_waitcnt lgkmcnt(1)\n"
        "ds_read_b128 v[20:23], v0 offset:31744\n"
        "v_mfma_f32_16x16x32_bf16 v[24:27], v[160:163], v[8:11], v[24:27]\n"
        "v_mfma_f32_16x16x32_bf16 v[28:31], v[164:167], v[8:11], v[28:31]\n"
        "buffer_load_dwordx4 v1, s[40:43], s81 offen offset:0 sc1 lds\n"
        "s_sub_u32 m0, m0, s85\n"
        "v_mfma_f32_16x16x32_bf16 v[32:35], v[168:171], v[8:11], v[32:35]\n"
        "s_add_u32 m0, m0, 12288\n"
        "ds_read_b128 v[8:11], v0 offset:32768\n"
        "s_add_u32 s81, s81, 16384\n"
        "v_mfma_f32_16x16x32_bf16 v[24:27], v[172:175], v[12:15], v[24:27]\n"
        "v_mfma_f32_16x16x32_bf16 v[28:31], v[176:179], v[12:15], v[28:31]\n"
        "v_mfma_f32_16x16x32_bf16 v[32:35], v[180:183], v[12:15], v[32:35]\n"
        "s_waitcnt lgkmcnt(1)\n"
        "ds_read_b128 v[12:15], v0 offset:33792\n"
        "buffer_load_dwordx4 v1, s[40:43], s71 offen offset:0 sc1 lds\n"
        "v_mfma_f32_16x16x32_bf16 v[24:27], v[184:187], v[16:19], v[24:27]\n"
        "v_mfma_f32_16x16x32_bf16 v[28:31], v[188:191], v[16:19], v[28:31]\n"
        "v_mfma_f32_16x16x32_bf16 v[32:35], v[192:195], v[16:19], v[32:35]\n"
        "ds_read_b128 v[16:19], v0 offset:34816\n"
        "v_mfma_f32_16x16x32_bf16 v[24:27], v[196:199], v[20:23], v[24:27]\n"
        "buffer_load_dwordx4 v1, s[40:43], s71 offen offset:1024 sc1 lds\n"
        "s_add_u32 m0, m0, s85\n"
        "v_mfma_f32_16x16x32_bf16 v[28:31], v[200:203], v[20:23], v[28:31]\n"
        "s_add_u32 s71, s71, 16384\n"
        "v_mfma_f32_16x16x32_bf16 v[32:35], v[204:207], v[20:23], v[32:35]\n"
        "s_waitcnt lgkmcnt(1)\n"
        "ds_read_b128 v[20:23], v0 offset:35840\n"
        "v_mfma_f32_16x16x32_bf16 v[24:27], v[208:211], v[8:11], v[24:27]\n"
        "v_mfma_f32_16x16x32_bf16 v[28:31], v[212:215], v[8:11], v[28:31]\n"
        "s_waitcnt vmcnt(5)\n"
        "s_barrier\n"
        "v_add_u32 v0, s78, v6\n"
        "v_mfma_f32_16x16x32_bf16 v[32:35], v[216:219], v[8:11], v[32:35]\n"
        "ds_read_b128 v[8:11], v0 offset:0\n"
        "buffer_load_dwordx4 v1, s[40:43], s81 offen offset:0 sc1 lds\n"
        "v_mfma_f32_16x16x32_bf16 v[24:27], v[220:223], v[12:15], v[24:27]\n"
        "s_add_u32 s69, s69, 1\n"
        "v_mfma_f32_16x16x32_bf16 v[28:31], v[224:227], v[12:15], v[28:31]\n"
        "v_mfma_f32_16x16x32_bf16 v[32:35], v[228:231], v[12:15], v[32:35]\n"
        "s_waitcnt lgkmcnt(1)\n"
        "ds_read_b128 v[12:15], v0 offset:1024\n"
        "v_mfma_f32_16x16x32_bf16 v[24:27], v[232:235], v[16:19], v[24:27]\n"
        "v_mfma_f32_16x16x32_bf16 v[28:31], v[236:239], v[16:19], v[28:31]\n"
        "v_mfma_f32_16x16x32_bf16 v[32:35], v[240:243], v[16:19], v[32:35]\n"
        "ds_read_b128 v[16:19], v0 offset:2048\n"
        "v_mfma_f32_16x16x32_bf16 v[24:27], v[244:247], v[20:23], v[24:27]\n"
        "v_mfma_f32_16x16x32_bf16 v[28:31], v[248:251], v[20:23], v[28:31]\n"
        "v_mfma_f32_16x16x32_bf16 v[32:35], v[252:255], v[20:23], v[32:35]\n"
        "s_waitcnt lgkmcnt(1)\n"
        "ds_read_b128 v[20:23], v0 offset:3072\n"
        "s_branch .Lf2_i1_%=\n"
        ".Lf2_i01_%=:\n"
        "v_mfma_f32_16x16x32_bf16 v[32:35], v[84:87], v[12:15], v[32:35]\n"
        "s_waitcnt lgkmcnt(1)\n"
        "ds_read_b128 v[12:15], v0 offset:25600\n"
        "buffer_load_dword v4, v2, s[40:43], s86 offen sc1\n"
        "v_mfma_f32_16x16x32_bf16 v[24:27], v[88:91], v[16:19], v[24:27]\n"
        "v_mfma_f32_16x16x32_bf16 v[28:31], v[92:95], v[16:19], v[28:31]\n"
        "v_mfma_f32_16x16x32_bf16 v[32:35], v[96:99], v[16:19], v[32:35]\n"
        "ds_read_b128 v[16:19], v0 offset:26624\n"
        "v_mfma_f32_16x16x32_bf16 v[24:27], v[100:103], v[20:23], v[24:27]\n"
        "buffer_store_dword v5, v3, s[40:43], s73 offen sc1\n"
        "s_add_u32 s74, s74, 1\n"
        "v_mfma_f32_16x16x32_bf16 v[28:31], v[104:107], v[20:23], v[28:31]\n"
        "s_mov_b32 s84, s89\n"
        "s_mov_b32 s73, s60\n"
        "s_add_u32 s68, s68, 1\n"
        "v_mfma_f32_16x16x32_bf16 v[32:35], v[108:111], v[20:23], v[32:35]\n"
        "s_waitcnt lgkmcnt(1)\n"
        "ds_read_b128 v[20:23], v0 offset:27648\n"
        "s_lshr_b32 s66, s68, 2\n"
        "v_mfma_f32_16x16x32_bf16 v[24:27], v[112:115], v[8:11], v[24:27]\n"
        "s_add_u32 s66, s66, 1\n"
        "s_bitcmp1_b32 s55, s66\n"
        "s_cselect_b32 s66, 8192, 0\n"
        "v_mfma_f32_16x16x32_bf16 v[28:31], v[116:119], v[8:11], v[28:31]\n"
        "s_lshl_b32 s75, s68, 19\n"
        "s_add_u32 s75, s75, s66\n"
        "s_add_u32 s72, s75, s59\n"
        "v_mfma_f32_16x16x32_bf16 v[32:35], v[120:123], v[8:11], v[32:35]\n"
        "ds_read_b128 v[8:11], v0 offset:28672\n"
        "v_mfma_f32_16x16x32_bf16 v[24:27], v[124:127], v[12:15], v[24:27]\n"
        "v_mfma_f32_16x16x32_bf16 v[28:31], v[128:131], v[12:15], v[28:31]\n"
        "v_mfma_f32_16x16x32_bf16 v[32:35], v[132:135], v[12:15], v[32:35]\n"
        "s_waitcnt lgkmcnt(1)\n"
        "ds_read_b128 v[12:15], v0 offset:29696\n"
        "v_mfma_f32_16x16x32_bf16 v[24:27], v[136:139], v[16:19], v[24:27]\n"
        "v_mfma_f32_16x16x32_bf16 v[28:31], v[140:143], v[16:19], v[28:31]\n"
        "v_mfma_f32_16x16x32_bf16 v[32:35], v[144:147], v[16:19], v[32:35]\n"
        "ds_read_b128 v[16:19], v0 offset:30720\n"
        "v_mfma_f32_16x16x32_bf16 v[24:27], v[148:151], v[20:23], v[24:27]\n"
        "v_mfma_f32_16x16x32_bf16 v[28:31], v[152:155], v[20:23], v[28:31]\n"
        "v_mfma_f32_16x16x32_bf16 v[32:35], v[156:159], v[20:23], v[32:35]\n"
        "s_waitcnt lgkmcnt(1)\n"
        "ds_read_b128 v[20:23], v0 offset:31744\n"
        "v_mfma_f32_16x16x32_bf16 v[24:27], v[160:163], v[8:11], v[24:27]\n"
        "v_mfma_f32_16x16x32_bf16 v[28:31], v[164:167], v[8:11], v[28:31]\n"
        "v_mfma_f32_16x16x32_bf16 v[32:35], v[168:171], v[8:11], v[32:35]\n"
        "ds_read_b128 v[8:11], v0 offset:32768\n"
        "v_mfma_f32_16x16x32_bf16 v[24:27], v[172:175], v[12:15], v[24:27]\n"
        "v_mfma_f32_16x16x32_bf16 v[28:31], v[176:179], v[12:15], v[28:31]\n"
        "v_mfma_f32_16x16x32_bf16 v[32:35], v[180:183], v[12:15], v[32:35]\n"
        "s_waitcnt lgkmcnt(1)\n"
        "ds_read_b128 v[12:15], v0 offset:33792\n"
        "v_mfma_f32_16x16x32_bf16 v[24:27], v[184:187], v[16:19], v[24:27]\n"
        "v_mfma_f32_16x16x32_bf16 v[28:31], v[188:191], v[16:19], v[28:31]\n"
        "v_mfma_f32_16x16x32_bf16 v[32:35], v[192:195], v[16:19], v[32:35]\n"
        "ds_read_b128 v[16:19], v0 offset:34816\n"
        "v_mfma_f32_16x16x32_bf16 v[24:27], v[196:199], v[20:23], v[24:27]\n"
        "s_waitcnt vmcnt(1)\n"
        "v_cmp_gt_i32 vcc, s70, v4\n"
        "v_mfma_f32_16x16x32_bf16 v[28:31], v[200:203], v[20:23], v[28:31]\n"
        "v_mfma_f32_16x16x32_bf16 v[32:35], v[204:207], v[20:23], v[32:35]\n"
        "s_waitcnt lgkmcnt(1)\n"
        "ds_read_b128 v[20:23], v0 offset:35840\n"
        "s_and_b64 vcc, vcc, 63\n"
        "v_mfma_f32_16x16x32_bf16 v[24:27], v[208:211], v[8:11], v[24:27]\n"
        "s_cbranch_vccnz .Lf2_i02_%=\n"
        "v_mfma_f32_16x16x32_bf16 v[28:31], v[212:215], v[8:11], v[28:31]\n"
        "buffer_load_dwordx4 v1, s[40:43], s71 offen offset:0 sc1 lds\n"
        "v_mfma_f32_16x16x32_bf16 v[32:35], v[216:219], v[8:11], v[32:35]\n"
        "v_mfma_f32_16x16x32_bf16 v[24:27], v[220:223], v[12:15], v[24:27]\n"
        "buffer_load_dwordx4 v1, s[40:43], s71 offen offset:1024 sc1 lds\n"
        "s_add_u32 m0, m0, s85\n"
        "s_add_u32 s71, s71, 16384\n"
        "v_mfma_f32_16x16x32_bf16 v[28:31], v[224:227], v[12:15], v[28:31]\n"
        "v_mfma_f32_16x16x32_bf16 v[32:35], v[228:231], v[12:15], v[32:35]\n"
        "s_waitcnt lgkmcnt(0)\n"
        "buffer_load_dwordx4 v1, s[40:43], s81 offen offset:0 sc1 lds\n"
        "s_sub_u32 m0, m0, s85\n"
        "s_add_u32 m0, m0, 12288\n"
        "s_add_u32 s81, s81, 16384\n"
        "v_mfma_f32_16x16x32_bf16 v[24:27], v[232:235], v[16:19], v[24:27]\n"
        "v_mfma_f32_16x16x32_bf16 v[28:31], v[236:239], v[16:19], v[28:31]\n"
        "buffer_load_dwordx4 v1, s[40:43], s71 offen offset:0 sc1 lds\n"
        "v_mfma_f32_16x16x32_bf16 v[32:35], v[240:243], v[16:19], v[32:35]\n"
        "v_mfma_f32_16x16x32_bf16 v[24:27], v[244:247], v[20:23], v[24:27]\n"
        "buffer_load_dwordx4 v1, s[40:43], s71 offen offset:1024 sc1 lds\n"
        "s_add_u32 m0, m0, s85\n"
        "s_add_u32 s71, s71, 16384\n"
        "v_mfma_f32_16x16x32_bf16 v[28:31], v[248:251], v[20:23], v[28:31]\n"
        "v_mfma_f32_16x16x32_bf16 v[32:35], v[252:255], v[20:23], v[32:35]\n"
        "buffer_load_dwordx4 v1, s[40:43], s81 offen offset:0 sc1 lds\n"
        "s_sub_u32 m0, m0, s85\n"
        "s_add_u32 m0, m0, 12288\n"
        "s_add_u32 s81, s81, 16384\n"
        "buffer_load_dwordx4 v1, s[40:43], s71 offen offset:0 sc1 lds\n"
        "buffer_load_dwordx4 v1, s[40:43], s71 offen offset:1024 sc1 lds\n"
        "s_add_u32 m0, m0, s85\n"
        "s_add_u32 s71, s71, 16384\n"
        "buffer_load_dwordx4 v1, s[40:43], s81 offen offset:0 sc1 lds\n"
        "s_add_u32 s69, s69, 1\n"
        "s_nop 2\n"
        "v_cvt_pk_bf16_f32 v24, v24, v25\n"
        "v_cvt_pk_bf16_f32 v25, v26, v27\n"
        "v_cvt_pk_bf16_f32 v26, v28, v29\n"
        "v_cvt_pk_bf16_f32 v27, v30, v31\n"
        "buffer_store_dwordx4 v[24:27], v48, s[40:43], s72 offen offset:0 sc1\n"
        "v_cvt_pk_bf16_f32 v32, v32, v33\n"
        "v_cvt_pk_bf16_f32 v33, v34, v35\n"
        "s_nop 0\n"
        "v_permlane16_swap_b32 v32, v34\n"
        "v_permlane16_swap_b32 v33, v35\n"
        "buffer_store_dwordx4 v[32:35], v7, s[40:43], s72 offen offset:0 sc1\n"
        "v_mov_b32 v5, s74\n"
        "s_mov_b32 s84, s60\n"
        "s_waitcnt vmcnt(8)\n"
        "s_branch .Lf2_go0_%=\n"
        ".Lf2_i02_%=:\n"
        "v_mfma_f32_16x16x32_bf16 v[28:31], v[212:215], v[8:11], v[28:31]\n"
        "buffer_load_dword v4, v2, s[40:43], s86 offen sc1\n"
        "v_mfma_f32_16x16x32_bf16 v[32:35], v[216:219], v[8:11], v[32:35]\n"
        "v_mfma_f32_16x16x32_bf16 v[24:27], v[220:223], v[12:15], v[24:27]\n"
        "v_mfma_f32_16x16x32_bf16 v[28:31], v[224:227], v[12:15], v[28:31]\n"
        "v_mfma_f32_16x16x32_bf16 v[32:35], v[228:231], v[12:15], v[32:35]\n"
        "s_waitcnt lgkmcnt(0)\n"
        "v_mfma_f32_16x16x32_bf16 v[24:27], v[232:235], v[16:19], v[24:27]\n"
        "v_mfma_f32_16x16x32_bf16 v[28:31], v[236:239], v[16:19], v[28:31]\n"
        "v_mfma_f32_16x16x32_bf16 v[32:35], v[240:243], v[16:19], v[32:35]\n"
        "v_mfma_f32_16x16x32_bf16 v[24:27], v[244:247], v[20:23], v[24:27]\n"
        "v_mfma_f32_16x16x32_bf16 v[28:31], v[248:251], v[20:23], v[28:31]\n"
        "v_mfma_f32_16x16x32_bf16 v[32:35], v[252:255], v[20:23], v[32:35]\n"
        "s_nop 2\n"
        "v_cvt_pk_bf16_f32 v24, v24, v25\n"
        "v_cvt_pk_bf16_f32 v25, v26, v27\n"
        "v_cvt_pk_bf16_f32 v26, v28, v29\n"
        "v_cvt_pk_bf16_f32 v27, v30, v31\n"
        "buffer_store_dwordx4 v[24:27], v48, s[40:43], s72 offen offset:0 sc1\n"
        "v_cvt_pk_bf16_f32 v32, v32, v33\n"
        "v_cvt_pk_bf16_f32 v33, v34, v35\n"
        "s_nop 0\n"
        "v_permlane16_swap_b32 v32, v34\n"
        "v_permlane16_swap_b32 v33, v35\n"
        "buffer_store_dwordx4 v[32:35], v7, s[40:43], s72 offen offset:0 sc1\n"
        "s_waitcnt vmcnt(2)\n"
        "v_cmp_gt_i32 vcc, s70, v4\n"
        "s_nop 0\n"
        "s_and_b64 vcc, vcc, 63\n"
        "s_cbranch_vccnz .Lf2_late0_%=\n"
        "buffer_load_dwordx4 v1, s[40:43], s71 offen offset:0 sc1 lds\n"
        "buffer_load_dwordx4 v1, s[40:43], s71 offen offset:1024 sc1 lds\n"
        "s_add_u32 m0, m0, s85\n"
        "s_add_u32 s71, s71, 16384\n"
        "buffer_load_dwordx4 v1, s[40:43], s81 offen offset:0 sc1 lds\n"
        "s_sub_u32 m0, m0, s85\n"
        "s_add_u32 m0, m0, 12288\n"
        "s_add_u32 s81, s81, 16384\n"
        "buffer_load_dwordx4 v1, s[40:43], s71 offen offset:0 sc1 lds\n"
        "buffer_load_dwordx4 v1, s[40:43], s71 offen offset:1024 sc1 lds\n"
        "s_add_u32 m0, m0, s85\n"
        "s_add_u32 s71, s71, 16384\n"
        "buffer_load_dwordx4 v1, s[40:43], s81 offen offset:0 sc1 lds\n"
        "s_sub_u32 m0, m0, s85\n"
        "s_add_u32 m0, m0, 12288\n"
        "s_add_u32 s81, s81, 16384\n"
        "buffer_load_dwordx4 v1, s[40:43], s71 offen offset:0 sc1 lds\n"
        "buffer_load_dwordx4 v1, s[40:43], s71 offen offset:1024 sc1 lds\n"
        "s_add_u32 m0, m0, s85\n"
        "s_add_u32 s71, s71, 16384\n"
        "buffer_load_dwordx4 v1, s[40:43], s81 offen offset:0 sc1 lds\n"
        "s_add_u32 s69, s69, 1\n"
        "s_waitcnt vmcnt(9)\n"
        "v_mov_b32 v5, s74\n"
        "buffer_store_dword v5, v3, s[40:43], s73 offen sc1\n"
        "s_waitcnt vmcnt(7)\n"
        "s_branch .Lf2_go0_%=\n"
        ".Lf2_late0_%=:\n"
        "s_waitcnt vmcnt(0)\n"
        "v_mov_b32 v5, s74\n"
        "buffer_store_dword v5, v3, s[40:43], s73 offen sc1\n"
        "s_mov_b32 s64, 0\n"
        ".Lf2_in0_top_%=:\n"
        "buffer_load_dword v4, v2, s[40:43], s86 offen sc1\n"
        "s_waitcnt vmcnt(0)\n"
        "v_cmp_gt_i32 vcc, s70, v4\n"
        "s_nop 0\n"
        "s_and_b64 vcc, vcc, 63\n"
        "s_cbranch_vccz .Lf2_in0_ok_%=\n"
        "s_add_u32 s64, s64, 1\n"
        "s_and_b32 s65, s64, 255\n"
        "s_cbranch_scc1 .Lf2_in0_go_%=\n"
        "s_mov_b32 s65, 49152\n"
        "s_cmp_lt_u32 s64, 1048576\n"
        "s_cbranch_scc1 .Lf2_in0_chk_%=\n"
        "v_mov_b32 v4, 1\n"
        "buffer_store_dword v4, off, s[40:43], s65 sc1\n"
        "s_waitcnt vmcnt(0)\n"
        ".Lf2_in0_chk_%=:\n"
        "buffer_load_dword v4, off, s[40:43], s65 sc1\n"
        "s_waitcnt vmcnt(0)\n"
        "v_readfirstlane_b32 s65, v4\n"
        "s_nop 0\n"
        "s_cmp_lg_u32 s65, 1\n"
        "s_cbranch_scc1 .Lf2_in0_go_%=\n"
        "s_endpgm\n"
        ".Lf2_in0_go_%=:\n"
        "s_branch .Lf2_in0_top_%=\n"
        ".Lf2_in0_ok_%=:\n"
        "buffer_load_dwordx4 v1, s[40:43], s71 offen offset:0 sc1 lds\n"
        "buffer_load_dwordx4 v1, s[40:43], s71 offen offset:1024 sc1 lds\n"
        "s_add_u32 m0, m0, s85\n"
        "s_add_u32 s71, s71, 16384\n"
        "buffer_load_dwordx4 v1, s[40:43], s81 offen offset:0 sc1 lds\n"
        "s_sub_u32 m0, m0, s85\n"
        "s_add_u32 m0, m0, 12288\n"
        "s_add_u32 s81, s81, 16384\n"
        "buffer_load_dwordx4 v1, s[40:43], s71 offen offset:0 sc1 lds\n"
        "buffer_load_dwordx4 v1, s[40:43], s71 offen offset:1024 sc1 lds\n"
        "s_add_u32 m0, m0, s85\n"
        "s_add_u32 s71, s71, 16384\n"
        "buffer_load_dwordx4 v1, s[40:43], s81 offen offset:0 sc1 lds\n"
        "s_sub_u32 m0, m0, s85\n"
        "s_add_u32 m0, m0, 12288\n"
        "s_add_u32 s81, s81, 16384\n"
        "buffer_load_dwordx4 v1, s[40:43], s71 offen offset:0 sc1 lds\n"
        "buffer_load_dwordx4 v1, s[40:43], s71 offen offset:1024 sc1 lds\n"
        "s_add_u32 m0, m0, s85\n"
        "s_add_u32 s71, s71, 16384\n"
        "buffer_load_dwordx4 v1, s[40:43], s81 offen offset:0 sc1 lds\n"
        "s_add_u32 s69, s69, 1\n"
        "s_waitcnt vmcnt(6)\n"
        ".Lf2_go0_%=:\n"
        "s_mov_b32 s72, 241172480\n"
        "s_mov_b32 s83, 241172480\n"
        "s_mov_b32 s73, 241172480\n"
        "v_add_u32 v0, s78, v6\n"
        "s_barrier\n"
        "ds_read_b128 v[8:11], v0 offset:0\n"
        "ds_read_b128 v[12:15], v0 offset:1024\n"
        "ds_read_b128 v[16:19], v0 offset:2048\n"
        "ds_read_b128 v[20:23], v0 offset:3072\n"
        "s_waitcnt lgkmcnt(2)\n"
        "s_branch .Lf2_i1_%=\n"
        ".Lf2_i1_%=:\n"
        "v_mfma_f32_16x16x32_bf16 v[36:39], a[0:3], v[8:11], 0\n"
        "v_mfma_f32_16x16x32_bf16 v[40:43], a[4:7], v[8:11], 0\n"
        "s_lshr_b32 s67, s69, 1\n"
        "buffer_load_dword v4, v2, s[40:43], s86 offen sc1\n"
        "s_lshr_b32 s65, s67, 1\n"
        "v_mfma_f32_16x16x32_bf16 v[44:47], a[8:11], v[8:11], 0\n"
        "s_bitcmp1_b32 s54, s65\n"
        "s_cselect_b32 s65, 8192, 0\n"
        "ds_read_b128 v[8:11], v0 offset:4096\n"
        "v_mfma_f32_16x16x32_bf16 v[36:39], a[12:15], v[12:15], v[36:39]\n"
        "s_lshl_b32 s77, s67, 20\n"
        "s_add_u32 s77, s77, s65\n"
        "s_add_u32 s71, s77, s58\n"
        "v_mfma_f32_16x16x32_bf16 v[40:43], a[16:19], v[12:15], v[40:43]\n"
        "s_add_u32 s81, s77, s62\n"
        "s_add_u32 s78, s78, 36864\n"
        "v_cvt_pk_bf16_f32 v24, v24, v25\n"
        "v_mfma_f32_16x16x32_bf16 v[44:47], a[20:23], v[12:15], v[44:47]\n"
        "s_waitcnt lgkmcnt(1)\n"
        "ds_read_b128 v[12:15], v0 offset:5120\n"
        "v_cvt_pk_bf16_f32 v25, v26, v27\n"
        "v_mfma_f32_16x16x32_bf16 v[36:39], a[24:27], v[16:19], v[36:39]\n"
        "s_cmp_eq_u32 s78, 110592\n"
        "s_cselect_b32 s78, 0, s78\n"
        "s_add_u32 m0, s78, s61\n"
        "v_mfma_f32_16x16x32_bf16 v[40:43], a[28:31], v[16:19], v[40:43]\n"
        "s_lshr_b32 s70, s69, 0\n"
        "v_cvt_pk_bf16_f32 v26, v28, v29\n"
        "v_cvt_pk_bf16_f32 v27, v30, v31\n"
        "v_mfma_f32_16x16x32_bf16 v[44:47], a[32:35], v[16:19], v[44:47]\n"
        "ds_read_b128 v[16:19], v0 offset:6144\n"
        "v_mfma_f32_16x16x32_bf16 v[36:39], a[36:39], v[20:23], v[36:39]\n"
        "buffer_store_dwordx4 v[24:27], v48, s[40:43], s72 offen offset:0 sc1\n"
        "v_mfma_f32_16x16x32_bf16 v[40:43], a[40:43], v[20:23], v[40:43]\n"
        "v_cvt_pk_bf16_f32 v32, v32, v33\n"
        "v_cvt_pk_bf16_f32 v33, v34, v35\n"
        "v_mfma_f32_16x16x32_bf16 v[44:47], a[44:47], v[20:23], v[44:47]\n"
        "s_waitcnt lgkmcnt(1)\n"
        "ds_read_b128 v[20:23], v0 offset:7168\n"
        "s_nop 0\n"
        "v_mfma_f32_16x16x32_bf16 v[36:39], a[48:51], v[8:11], v[36:39]\n"
        "v_permlane16_swap_b32 v32, v34\n"
        "v_mfma_f32_16x16x32_bf16 v[40:43], a[52:55], v[8:11], v[40:43]\n"
        "v_permlane16_swap_b32 v33, v35\n"
        "v_mfma_f32_16x16x32_bf16 v[44:47], a[56:59], v[8:11], v[44:47]\n"
        "ds_read_b128 v[8:11], v0 offset:8192\n"
        "v_mfma_f32_16x16x32_bf16 v[36:39], a[60:63], v[12:15], v[36:39]\n"
        "v_mfma_f32_16x16x32_bf16 v[40:43], a[64:67], v[12:15], v[40:43]\n"
        "buffer_store_dwordx4 v[32:35], v7, s[40:43], s72 offen offset:0 sc1\n"
        "v_mfma_f32_16x16x32_bf16 v[44:47], a[68:71], v[12:15], v[44:47]\n"
        "s_waitcnt lgkmcnt(1)\n"
        "ds_read_b128 v[12:15], v0 offset:9216\n"
        "v_mfma_f32_16x16x32_bf16 v[36:39], a[72:75], v[16:19], v[36:39]\n"
        "v_mfma_f32_16x16x32_bf16 v[40:43], a[76:79], v[16:19], v[40:43]\n"
        "v_mfma_f32_16x16x32_bf16 v[44:47], a[80:83], v[16:19], v[44:47]\n"
        "ds_read_b128 v[16:19], v0 offset:10240\n"
        "v_mfma_f32_16x16x32_bf16 v[36:39], a[84:87], v[20:23], v[36:39]\n"
        "v_mfma_f32_16x16x32_bf16 v[40:43], a[88:91], v[20:23], v[40:43]\n"
        "v_mfma_f32_16x16x32_bf16 v[44:47], a[92:95], v[20:23], v[44:47]\n"
        "s_waitcnt lgkmcnt(1)\n"
        "ds_read_b128 v[20:23], v0 offset:11264\n"
        "v_mfma_f32_16x16x32_bf16 v[36:39], a[96:99], v[8:11], v[36:39]\n"
        "v_mfma_f32_16x16x32_bf16 v[40:43], a[100:103], v[8:11], v[40:43]\n"
        "s_waitcnt vmcnt(3)\n"
        "s_barrier\n"
        "v_mfma_f32_16x16x32_bf16 v[44:47], a[104:107], v[8:11], v[44:47]\n"
        "ds_read_b128 v[8:11], v0 offset:12288\n"
        "buffer_store_dword v5, v3, s[40:43], s84 offen sc1\n"
        "v_mfma_f32_16x16x32_bf16 v[36:39], a[108:111], v[12:15], v[36:39]\n"
        "v_mov_b32 v5, s74\n"
        "v_mfma_f32_16x16x32_bf16 v[40:43], a[112:115], v[12:15], v[40:43]\n"
        "v_mfma_f32_16x16x32_bf16 v[44:47], a[116:119], v[12:15], v[44:47]\n"
        "s_waitcnt lgkmcnt(1)\n"
        "ds_read_b128 v[12:15], v0 offset:13312\n"
        "v_mfma_f32_16x16x32_bf16 v[36:39], a[120:123], v[16:19], v[36:39]\n"
        "v_mfma_f32_16x16x32_bf16 v[40:43], a[124:127], v[16:19], v[40:43]\n"
        "s_waitcnt vmcnt(3)\n"
        "v_cmp_gt_i32 vcc, s70, v4\n"
        "v_mfma_f32_16x16x32_bf16 v[44:47], a[128:131], v[16:19], v[44:47]\n"
        "ds_read_b128 v[16:19], v0 offset:14336\n"
        "v_mfma_f32_16x16x32_bf16 v[36:39], a[132:135], v[20:23], v[36:39]\n"
        "s_and_b64 vcc, vcc, 63\n"
        "v_mfma_f32_16x16x32_bf16 v[40:43], a[136:139], v[20:23], v[40:43]\n"
        "s_cbranch_vccnz .Lf2_i10_%=\n"
        "v_mfma_f32_16x16x32_bf16 v[44:47], a[140:143], v[20:23], v[44:47]\n"
        "s_waitcnt lgkmcnt(1)\n"
        "ds_read_b128 v[20:23], v0 offset:15360\n"
        "buffer_load_dwordx4 v1, s[40:43], s71 offen offset:0 sc1 lds\n"
        "v_mfma_f32_16x16x32_bf16 v[36:39], a[144:147], v[8:11], v[36:39]\n"
        "v_mfma_f32_16x16x32_bf16 v[40:43], a[148:151], v[8:11], v[40:43]\n"
        "v_mfma_f32_16x16x32_bf16 v[44:47], a[152:155], v[8:11], v[44:47]\n"
        "ds_read_b128 v[8:11], v0 offset:16384\n"
        "v_mfma_f32_16x16x32_bf16 v[36:39], a[156:159], v[12:15], v[36:39]\n"
        "buffer_load_dwordx4 v1, s[40:43], s71 offen offset:1024 sc1 lds\n"
        "s_add_u32 m0, m0, s85\n"
        "v_mfma_f32_16x16x32_bf16 v[40:43], a[160:163], v[12:15], v[40:43]\n"
        "s_getreg_b32 s88, hwreg(HW_REG_IB_STS, 0, 4)\n"
        "s_add_u32 s71, s71, 16384\n"
        "v_mfma_f32_16x16x32_bf16 v[44:47], a[164:167], v[12:15], v[44:47]\n"
        "s_waitcnt lgkmcnt(1)\n"
        "ds_read_b128 v[12:15], v0 offset:17408\n"
        "v_mfma_f32_16x16x32_bf16 v[36:39], a[168:171], v[16:19], v[36:39]\n"
        "s_cmp_le_u32 s88, 3\n"
        "s_cselect_b32 s88, s73, s89\n"
        "s_cselect_b32 s73, s89, s73\n"
        "v_mfma_f32_16x16x32_bf16 v[40:43], a[172:175], v[16:19], v[40:43]\n"
        "buffer_store_dword v5, v3, s[40:43], s88 offen sc1\n"
        "buffer_load_dwordx4 v1, s[40:43], s81 offen offset:0 sc1 lds\n"
        "v_mfma_f32_16x16x32_bf16 v[44:47], a[176:179], v[16:19], v[44:47]\n"
        "s_sub_u32 m0, m0, s85\n"
        "ds_read_b128 v[16:19], v0 offset:18432\n"
        "s_add_u32 m0, m0, 12288\n"
        "v_mfma_f32_16x16x32_bf16 v[36:39], a[180:183], v[20:23], v[36:39]\n"
        "s_add_u32 s81, s81, 16384\n"
        "v_mfma_f32_16x16x32_bf16 v[40:43], a[184:187], v[20:23], v[40:43]\n"
        "v_mfma_f32_16x16x32_bf16 v[44:47], a[188:191], v[20:23], v[44:47]\n"
        "s_waitcnt lgkmcnt(1)\n"
        "ds_read_b128 v[20:23], v0 offset:19456\n"
        "buffer_load_dwordx4 v1, s[40:43], s71 offen offset:0 sc1 lds\n"
        "v_mfma_f32_16x16x32_bf16 v[36:39], a[192:195], v[8:11], v[36:39]\n"
        "s_getreg_b32 s88, hwreg(HW_REG_IB_STS, 0, 4)\n"
        "v_mfma_f32_16x16x32_bf16 v[40:43], a[196:199], v[8:11], v[40:43]\n"
        "s_cmp_le_u32 s88, 6\n"
        "s_cselect_b32 s88, s73, s89\n"
        "s_cselect_b32 s73, s89, s73\n"
        "v_mfma_f32_16x16x32_bf16 v[44:47], a[200:203], v[8:11], v[44:47]\n"
        "ds_read_b128 v[8:11], v0 offset:20480\n"
        "buffer_store_dword v5, v3, s[40:43], s88 offen sc1\n"
        "v_mfma_f32_16x16x32_bf16 v[36:39], a[204:207], v[12:15], v[36:39]\n"
        "buffer_load_dwordx4 v1, s[40:43], s71 offen offset:1024 sc1 lds\n"
        "s_add_u32 m0, m0, s85\n"
        "v_mfma_f32_16x16x32_bf16 v[40:43], a[208:211], v[12:15], v[40:43]\n"
        "s_add_u32 s71, s71, 16384\n"
        "v_mfma_f32_16x16x32_bf16 v[44:47], a[212:215], v[12:15], v[44:47]\n"
        "s_waitcnt lgkmcnt(1)\n"
        "ds_read_b128 v[12:15], v0 offset:21504\n"
        "v_mfma_f32_16x16x32_bf16 v[36:39], a[216:219], v[16:19], v[36:39]\n"
        "v_mfma_f32_16x16x32_bf16 v[40:43], a[220:223], v[16:19], v[40:43]\n"
        "buffer_load_dwordx4 v1, s[40:43], s81 offen offset:0 sc1 lds\n"
        "s_sub_u32 m0, m0, s85\n"
        "v_mfma_f32_16x16x32_bf16 v[44:47], a[224:227], v[16:19], v[44:47]\n"
        "s_getreg_b32 s88, hwreg(HW_REG_IB_STS, 0, 4)\n"
        "ds_read_b128 v[16:19], v0 offset:22528\n"
        "s_add_u32 m0, m0, 12288\n"
        "v_mfma_f32_16x16x32_bf16 v[36:39], a[228:231], v[20:23], v[36:39]\n"
        "s_cmp_le_u32 s88, 9\n"
        "s_cselect_b32 s88, s73, s89\n"
        "s_cselect_b32 s73, s89, s73\n"
        "v_mfma_f32_16x16x32_bf16 v[40:43], a[232:235], v[20:23], v[40:43]\n"
        "s_add_u32 s81, s81, 16384\n"
        "buffer_store_dword v5, v3, s[40:43], s88 offen sc1\n"
        "v_mfma_f32_16x16x32_bf16 v[44:47], a[236:239], v[20:23], v[44:47]\n"
        "s_waitcnt lgkmcnt(1)\n"
        "ds_read_b128 v[20:23], v0 offset:23552\n"
        "buffer_load_dwordx4 v1, s[40:43], s71 offen offset:0 sc1 lds\n"
        "v_mfma_f32_16x16x32_bf16 v[36:39], a[240:243], v[8:11], v[36:39]\n"
        "v_mfma_f32_16x16x32_bf16 v[40:43], a[244:247], v[8:11], v[40:43]\n"
        "v_mfma_f32_16x16x32_bf16 v[44:47], a[248:251], v[8:11], v[44:47]\n"
        "ds_read_b128 v[8:11], v0 offset:24576\n"
        "v_mfma_f32_16x16x32_bf16 v[36:39], a[252:255], v[12:15], v[36:39]\n"
        "buffer_load_dwordx4 v1, s[40:43], s71 offen offset:1024 sc1 lds\n"
        "s_add_u32 m0, m0, s85\n"
        "v_mfma_f32_16x16x32_bf16 v[40:43], v[80:83], v[12:15], v[40:43]\n"
        "s_add_u32 s71, s71, 16384\n"
        "v_mfma_f32_16x16x32_bf16 v[44:47], v[84:87], v[12:15], v[44:47]\n"
        "s_waitcnt lgkmcnt(1)\n"
        "ds_read_b128 v[12:15], v0 offset:25600\n"
        "v_mfma_f32_16x16x32_bf16 v[36:39], v[88:91], v[16:19], v[36:39]\n"
        "v_mfma_f32_16x16x32_bf16 v[40:43], v[92:95], v[16:19], v[40:43]\n"
        "buffer_load_dwordx4 v1, s[40:43], s81 offen offset:0 sc1 lds\n"
        "s_add_u32 s69, s69, 1\n"
        "v_mfma_f32_16x16x32_bf16 v[44:47], v[96:99], v[16:19], v[44:47]\n"
        "ds_read_b128 v[16:19], v0 offset:26624\n"
        "v_mfma_f32_16x16x32_bf16 v[36:39], v[100:103], v[20:23], v[36:39]\n"
        "v_mfma_f32_16x16x32_bf16 v[40:43], v[104:107], v[20:23], v[40:43]\n"
        "s_waitcnt vmcnt(13)\n"
        "buffer_store_dword v5, v3, s[40:43], s73 offen sc1\n"
        "s_add_u32 s74, s74, 1\n"
        "v_mfma_f32_16x16x32_bf16 v[44:47], v[108:111], v[20:23], v[44:47]\n"
        "s_waitcnt lgkmcnt(1)\n"
        "ds_read_b128 v[20:23], v0 offset:27648\n"
        "s_mov_b32 s84, s89\n"
        "v_mfma_f32_16x16x32_bf16 v[36:39], v[112:115], v[8:11], v[36:39]\n"
        "s_mov_b32 s73, s60\n"
        "s_add_u32 s72, s75, s59\n"
        "s_add_u32 s72, s72, 147456\n"
        "v_mfma_f32_16x16x32_bf16 v[40:43], v[116:119], v[8:11], v[40:43]\n"
        "v_mfma_f32_16x16x32_bf16 v[44:47], v[120:123], v[8:11], v[44:47]\n"
        "ds_read_b128 v[8:11], v0 offset:28672\n"
        "v_mfma_f32_16x16x32_bf16 v[36:39], v[124:127], v[12:15], v[36:39]\n"
        "v_mfma_f32_16x16x32_bf16 v[40:43], v[128:131], v[12:15], v[40:43]\n"
        "v_mfma_f32_16x16x32_bf16 v[44:47], v[132:135], v[12:15], v[44:47]\n"
        "s_waitcnt lgkmcnt(1)\n"
        "ds_read_b128 v[12:15], v0 offset:29696\n"
        "v_mfma_f32_16x16x32_bf16 v[36:39], v[136:139], v[16:19], v[36:39]\n"
        "v_mfma_f32_16x16x32_bf16 v[40:43], v[140:143], v[16:19], v[40:43]\n"
        "v_mfma_f32_16x16x32_bf16 v[44:47], v[144:147], v[16:19], v[44:47]\n"
        "ds_read_b128 v[16:19], v0 offset:30720\n"
        "v_mfma_f32_16x16x32_bf16 v[36:39], v[148:151], v[20:23], v[36:39]\n"
        "v_mfma_f32_16x16x32_bf16 v[40:43], v[152:155], v[20:23], v[40:43]\n"
        "v_mfma_f32_16x16x32_bf16 v[44:47], v[156:159], v[20:23], v[44:47]\n"
        "s_waitcnt lgkmcnt(1)\n"
        "ds_read_b128 v[20:23], v0 offset:31744\n"
        "v_mfma_f32_16x16x32_bf16 v[36:39], v[160:163], v[8:11], v[36:39]\n"
        "v_mfma_f32_16x16x32_bf16 v[40:43], v[164:167], v[8:11], v[40:43]\n"
        "v_mfma_f32_16x16x32_bf16 v[44:47], v[168:171], v[8:11], v[44:47]\n"
        "ds_read_b128 v[8:11], v0 offset:32768\n"
        "v_mfma_f32_16x16x32_bf16 v[36:39], v[172:175], v[12:15], v[36:39]\n"
        "v_mfma_f32_16x16x32_bf16 v[40:43], v[176:179], v[12:15], v[40:43]\n"
        "v_mfma_f32_16x16x32_bf16 v[44:47], v[180:183], v[12:15], v[44:47]\n"
        "s_waitcnt lgkmcnt(1)\n"
        "ds_read_b128 v[12:15], v0 offset:33792\n"
        "v_mfma_f32_16x16x32_bf16 v[36:39], v[184:187], v[16:19], v[36:39]\n"
        "v_mfma_f32_16x16x32_bf16 v[40:43], v[188:191], v[16:19], v[40:43]\n"
        "v_mfma_f32_16x16x32_bf16 v[44:47], v[192:195], v[16:19], v[44:47]\n"
        "ds_read_b128 v[16:19], v0 offset:34816\n"
        "v_mfma_f32_16x16x32_bf16 v[36:39], v[196:199], v[20:23], v[36:39]\n"
        "v_mfma_f32_16x16x32_bf16 v[40:43], v[200:203], v[20:23], v[40:43]\n"
        "v_mfma_f32_16x16x32_bf16 v[44:47], v[204:207], v[20:23], v[44:47]\n"
        "s_waitcnt lgkmcnt(1)\n"
        "ds_read_b128 v[20:23], v0 offset:35840\n"
        "v_mfma_f32_16x16x32_bf16 v[36:39], v[208:211], v[8:11], v[36:39]\n"
        "v_mfma_f32_16x16x32_bf16 v[40:43], v[212:215], v[8:11], v[40:43]\n"
        "s_waitcnt vmcnt(9)\n"
        "s_barrier\n"
        "v_add_u32 v0, s78, v6\n"
        "v_mfma_f32_16x16x32_bf16 v[44:47], v[216:219], v[8:11], v[44:47]\n"
        "ds_read_b128 v[8:11], v0 offset:0\n"
        "v_mfma_f32_16x16x32_bf16 v[36:39], v[220:223], v[12:15], v[36:39]\n"
        "v_mfma_f32_16x16x32_bf16 v[40:43], v[224:227], v[12:15], v[40:43]\n"
        "v_mfma_f32_16x16x32_bf16 v[44:47], v[228:231], v[12:15], v[44:47]\n"
        "s_waitcnt lgkmcnt(1)\n"
        "ds_read_b128 v[12:15], v0 offset:1024\n"
        "v_mfma_f32_16x16x32_bf16 v[36:39], v[232:235], v[16:19], v[36:39]\n"
        "v_mfma_f32_16x16x32_bf16 v[40:43], v[236:239], v[16:19], v[40:43]\n"
        "v_mfma_f32_16x16x32_bf16 v[44:47], v[240:243], v[16:19], v[44:47]\n"
        "ds_read_b128 v[16:19], v0 offset:2048\n"
        "v_mfma_f32_16x16x32_bf16 v[36:39], v[244:247], v[20:23], v[36:39]\n"
        "v_mfma_f32_16x16x32_bf16 v[40:43], v[248:251], v[20:23], v[40:43]\n"
        "v_mfma_f32_16x16x32_bf16 v[44:47], v[252:255], v[20:23], v[44:47]\n"
        "s_waitcnt lgkmcnt(1)\n"
        "ds_read_b128 v[20:23], v0 offset:3072\n"
        "s_branch .Lf2_i0_%=\n"
        ".Lf2_i10_%=:\n"
        "v_mfma_f32_16x16x32_bf16 v[44:47], a[140:143], v[20:23], v[44:47]\n"
        "s_waitcnt lgkmcnt(1)\n"
        "ds_read_b128 v[20:23], v0 offset:15360\n"
        "buffer_load_dword v4, v2, s[40:43], s86 offen sc1\n"
        "v_mfma_f32_16x16x32_bf16 v[36:39], a[144:147], v[8:11], v[36:39]\n"
        "v_mfma_f32_16x16x32_bf16 v[40:43], a[148:151], v[8:11], v[40:43]\n"
        "v_mfma_f32_16x16x32_bf16 v[44:47], a[152:155], v[8:11], v[44:47]\n"
        "ds_read_b128 v[8:11], v0 offset:16384\n"
        "v_mfma_f32_16x16x32_bf16 v[36:39], a[156:159], v[12:15], v[36:39]\n"
        "v_mfma_f32_16x16x32_bf16 v[40:43], a[160:163], v[12:15], v[40:43]\n"
        "s_getreg_b32 s88, hwreg(HW_REG_IB_STS, 0, 4)\n"
        "v_mfma_f32_16x16x32_bf16 v[44:47], a[164:167], v[12:15], v[44:47]\n"
        "s_waitcnt lgkmcnt(1)\n"
        "ds_read_b128 v[12:15], v0 offset:17408\n"
        "v_mfma_f32_16x16x32_bf16 v[36:39], a[168:171], v[16:19], v[36:39]\n"
        "s_cmp_le_u32 s88, 2\n"
        "s_cselect_b32 s88, s73, s89\n"
        "s_cselect_b32 s73, s89, s73\n"
        "v_mfma_f32_16x16x32_bf16 v[40:43], a[172:175], v[16:19], v[40:43]\n"
        "buffer_store_dword v5, v3, s[40:43], s88 offen sc1\n"
        "v_mfma_f32_16x16x32_bf16 v[44:47], a[176:179], v[16:19], v[44:47]\n"
        "ds_read_b128 v[16:19], v0 offset:18432\n"
        "v_mfma_f32_16x16x32_bf16 v[36:39], a[180:183], v[20:23], v[36:39]\n"
        "v_mfma_f32_16x16x32_bf16 v[40:43], a[184:187], v[20:23], v[40:43]\n"
        "v_mfma_f32_16x16x32_bf16 v[44:47], a[188:191], v[20:23], v[44:47]\n"
        "s_waitcnt lgkmcnt(1)\n"
        "ds_read_b128 v[20:23], v0 offset:19456\n"
        "v_mfma_f32_16x16x32_bf16 v[36:39], a[192:195], v[8:11], v[36:39]\n"
        "s_getreg_b32 s88, hwreg(HW_REG_IB_STS, 0, 4)\n"
        "v_mfma_f32_16x16x32_bf16 v[40:43], a[196:199], v[8:11], v[40:43]\n"
        "s_cmp_le_u32 s88, 3\n"
        "s_cselect_b32 s88, s73, s89\n"
        "s_cselect_b32 s73, s89, s73\n"
        "v_mfma_f32_16x16x32_bf16 v[44:47], a[200:203], v[8:11], v[44:47]\n"
        "ds_read_b128 v[8:11], v0 offset:20480\n"
        "buffer_store_dword v5, v3, s[40:43], s88 offen sc1\n"
        "v_mfma_f32_16x16x32_bf16 v[36:39], a[204:207], v[12:15], v[36:39]\n"
        "v_mfma_f32_16x16x32_bf16 v[40:43], a[208:211], v[12:15], v[40:43]\n"
        "v_mfma_f32_16x16x32_bf16 v[44:47], a[212:215], v[12:15], v[44:47]\n"
        "s_waitcnt lgkmcnt(1)\n"
        "ds_read_b128 v[12:15], v0 offset:21504\n"
        "v_mfma_f32_16x16x32_bf16 v[36:39], a[216:219], v[16:19], v[36:39]\n"
        "v_mfma_f32_16x16x32_bf16 v[40:43], a[220:223], v[16:19], v[40:43]\n"
        "v_mfma_f32_16x16x32_bf16 v[44:47], a[224:227], v[16:19], v[44:47]\n"
        "s_getreg_b32 s88, hwreg(HW_REG_IB_STS, 0, 4)\n"
        "ds_read_b128 v[16:19], v0 offset:22528\n"
        "v_mfma_f32_16x16x32_bf16 v[36:39], a[228:231], v[20:23], v[36:39]\n"
        "s_cmp_le_u32 s88, 4\n"
        "s_cselect_b32 s88, s73, s89\n"
        "s_cselect_b32 s73, s89, s73\n"
        "v_mfma_f32_16x16x32_bf16 v[40:43], a[232:235], v[20:23], v[40:43]\n"
        "buffer_store_dword v5, v3, s[40:43], s88 offen sc1\n"
        "v_mfma_f32_16x16x32_bf16 v[44:47], a[236:239], v[20:23], v[44:47]\n"
        "s_waitcnt lgkmcnt(1)\n"
        "ds_read_b128 v[20:23], v0 offset:23552\n"
        "v_mfma_f32_16x16x32_bf16 v[36:39], a[240:243], v[8:11], v[36:39]\n"
        "v_mfma_f32_16x16x32_bf16 v[40:43], a[244:247], v[8:11], v[40:43]\n"
        "s_waitcnt vmcnt(3)\n"
        "v_cmp_gt_i32 vcc, s70, v4\n"
        "v_mfma_f32_16x16x32_bf16 v[44:47], a[248:251], v[8:11], v[44:47]\n"
        "ds_read_b128 v[8:11], v0 offset:24576\n"
        "v_mfma_f32_16x16x32_bf16 v[36:39], a[252:255], v[12:15], v[36:39]\n"
        "s_and_b64 vcc, vcc, 63\n"
        "v_mfma_f32_16x16x32_bf16 v[40:43], v[80:83], v[12:15], v[40:43]\n"
        "s_cbranch_vccnz .Lf2_i11_%=\n"
        "v_mfma_f32_16x16x32_bf16 v[44:47], v[84:87], v[12:15], v[44:47]\n"
        "s_waitcnt lgkmcnt(1)\n"
        "ds_read_b128 v[12:15], v0 offset:25600\n"
        "buffer_load_dwordx4 v1, s[40:43], s71 offen offset:0 sc1 lds\n"
        "v_mfma_f32_16x16x32_bf16 v[36:39], v[88:91], v[16:19], v[36:39]\n"
        "v_mfma_f32_16x16x32_bf16 v[40:43], v[92:95], v[16:19], v[40:43]\n"
        "v_mfma_f32_16x16x32_bf16 v[44:47], v[96:99], v[16:19], v[44:47]\n"
        "ds_read_b128 v[16:19], v0 offset:26624\n"
        "buffer_load_dwordx4 v1, s[40:43], s71 offen offset:1024 sc1 lds\n"
        "v_mfma_f32_16x16x32_bf16 v[36:39], v[100:103], v[20:23], v[36:39]\n"
        "s_add_u32 m0, m0, s85\n"
        "s_add_u32 s71, s71, 16384\n"
        "v_mfma_f32_16x16x32_bf16 v[40:43], v[104:107], v[20:23], v[40:43]\n"
        "buffer_store_dword v5, v3, s[40:43], s73 offen sc1\n"
        "s_add_u32 s74, s74, 1\n"
        "v_mfma_f32_16x16x32_bf16 v[44:47], v[108:111], v[20:23], v[44:47]\n"
        "s_waitcnt lgkmcnt(1)\n"
        "ds_read_b128 v[20:23], v0 offset:27648\n"
        "s_mov_b32 s84, s89\n"
        "buffer_load_dwordx4 v1, s[40:43], s81 offen offset:0 sc1 lds\n"
        "v_mfma_f32_16x16x32_bf16 v[36:39], v[112:115], v[8:11], v[36:39]\n"
        "s_mov_b32 s73, s60\n"
        "s_add_u32 s72, s75, s59\n"
        "s_add_u32 s72, s72, 147456\n"
        "v_mfma_f32_16x16x32_bf16 v[40:43], v[116:119], v[8:11], v[40:43]\n"
        "s_sub_u32 m0, m0, s85\n"
        "s_add_u32 m0, m0, 12288\n"
        "s_add_u32 s81, s81, 16384\n"
        "v_mfma_f32_16x16x32_bf16 v[44:47], v[120:123], v[8:11], v[44:47]\n"
        "ds_read_b128 v[8:11], v0 offset:28672\n"
        "buffer_load_dwordx4 v1, s[40:43], s71 offen offset:0 sc1 lds\n"
        "v_mfma_f32_16x16x32_bf16 v[36:39], v[124:127], v[12:15], v[36:39]\n"
        "v_mfma_f32_16x16x32_bf16 v[40:43], v[128:131], v[12:15], v[40:43]\n"
        "v_mfma_f32_16x16x32_bf16 v[44:47], v[132:135], v[12:15], v[44:47]\n"
        "s_waitcnt lgkmcnt(1)\n"
        "ds_read_b128 v[12:15], v0 offset:29696\n"
        "buffer_load_dwordx4 v1, s[40:43], s71 offen offset:1024 sc1 lds\n"
        "v_mfma_f32_16x16x32_bf16 v[36:39], v[136:139], v[16:19], v[36:39]\n"
        "s_add_u32 m0, m0, s85\n"
        "s_add_u32 s71, s71, 16384\n"
        "v_mfma_f32_16x16x32_bf16 v[40:43], v[140:143], v[16:19], v[40:43]\n"
        "v_mfma_f32_16x16x32_bf16 v[44:47], v[144:147], v[16:19], v[44:47]\n"
        "ds_read_b128 v[16:19], v0 offset:30720\n"
        "v_mfma_f32_16x16x32_bf16 v[36:39], v[148:151], v[20:23], v[36:39]\n"
        "v_mfma_f32_16x16x32_bf16 v[40:43], v[152:155], v[20:23], v[40:43]\n"
        "v_mfma_f32_16x16x32_bf16 v[44:47], v[156:159], v[20:23], v[44:47]\n"
        "s_waitcnt lgkmcnt(1)\n"
        "ds_read_b128 v[20:23], v0 offset:31744\n"
        "v_mfma_f32_16x16x32_bf16 v[36:39], v[160:163], v[8:11], v[36:39]\n"
        "v_mfma_f32_16x16x32_bf16 v[40:43], v[164:167], v[8:11], v[40:43]\n"
        "buffer_load_dwordx4 v1, s[40:43], s81 offen offset:0 sc1 lds\n"
        "s_sub_u32 m0, m0, s85\n"
        "v_mfma_f32_16x16x32_bf16 v[44:47], v[168:171], v[8:11], v[44:47]\n"
        "s_add_u32 m0, m0, 12288\n"
        "ds_read_b128 v[8:11], v0 offset:32768\n"
        "s_add_u32 s81, s81, 16384\n"
        "v_mfma_f32_16x16x32_bf16 v[36:39], v[172:175], v[12:15], v[36:39]\n"
        "v_mfma_f32_16x16x32_bf16 v[40:43], v[176:179], v[12:15], v[40:43]\n"
        "v_mfma_f32_16x16x32_bf16 v[44:47], v[180:183], v[12:15], v[44:47]\n"
        "s_waitcnt lgkmcnt(1)\n"
        "ds_read_b128 v[12:15], v0 offset:33792\n"
        "buffer_load_dwordx4 v1, s[40:43], s71 offen offset:0 sc1 lds\n"
        "v_mfma_f32_16x16x32_bf16 v[36:39], v[184:187], v[16:19], v[36:39]\n"
        "v_mfma_f32_16x16x32_bf16 v[40:43], v[188:191], v[16:19], v[40:43]\n"
        "v_mfma_f32_16x16x32_bf16 v[44:47], v[192:195], v[16:19], v[44:47]\n"
        "ds_read_b128 v[16:19], v0 offset:34816\n"
        "v_mfma_f32_16x16x32_bf16 v[36:39], v[196:199], v[20:23], v[36:39]\n"
        "buffer_load_dwordx4 v1, s[40:43], s71 offen offset:1024 sc1 lds\n"
        "s_add_u32 m0, m0, s85\n"
        "v_mfma_f32_16x16x32_bf16 v[40:43], v[200:203], v[20:23], v[40:43]\n"
        "s_add_u32 s71, s71, 16384\n"
        "v_mfma_f32_16x16x32_bf16 v[44:47], v[204:207], v[20:23], v[44:47]\n"
        "s_waitcnt lgkmcnt(1)\n"
        "ds_read_b128 v[20:23], v0 offset:35840\n"
        "v_mfma_f32_16x16x32_bf16 v[36:39], v[208:211], v[8:11], v[36:39]\n"
        "v_mfma_f32_16x16x32_bf16 v[40:43], v[212:215], v[8:11], v[40:43]\n"
        "s_waitcnt vmcnt(5)\n"
        "s_barrier\n"
        "v_add_u32 v0, s78, v6\n"
        "v_mfma_f32_16x16x32_bf16 v[44:47], v[216:219], v[8:11], v[44:47]\n"
        "ds_read_b128 v[8:11], v0 offset:0\n"
        "buffer_load_dwordx4 v1, s[40:43], s81 offen offset:0 sc1 lds\n"
        "v_mfma_f32_16x16x32_bf16 v[36:39], v[220:223], v[12:15], v[36:39]\n"
        "s_add_u32 s69, s69, 1\n"
        "v_mfma_f32_16x16x32_bf16 v[40:43], v[224:227], v[12:15], v[40:43]\n"
        "v_mfma_f32_16x16x32_bf16 v[44:47], v[228:231], v[12:15], v[44:47]\n"
        "s_waitcnt lgkmcnt(1)\n"
        "ds_read_b128 v[12:15], v0 offset:1024\n"
        "v_mfma_f32_16x16x32_bf16 v[36:39], v[232:235], v[16:19], v[36:39]\n"
        "v_mfma_f32_16x16x32_bf16 v[40:43], v[236:239], v[16:19], v[40:43]\n"
        "v_mfma_f32_16x16x32_bf16 v[44:47], v[240:243], v[16:19], v[44:47]\n"
        "ds_read_b128 v[16:19], v0 offset:2048\n"
        "v_mfma_f32_16x16x32_bf16 v[36:39], v[244:247], v[20:23], v[36:39]\n"
        "v_mfma_f32_16x16x32_bf16 v[40:43], v[248:251], v[20:23], v[40:43]\n"
        "v_mfma_f32_16x16x32_bf16 v[44:47], v[252:255], v[20:23], v[44:47]\n"
        "s_waitcnt lgkmcnt(1)\n"
        "ds_read_b128 v[20:23], v0 offset:3072\n"
        "s_branch .Lf2_i0_%=\n"
        ".Lf2_i11_%=:\n"
        "v_mfma_f32_16x16x32_bf16 v[44:47], v[84:87], v[12:15], v[44:47]\n"
        "s_waitcnt lgkmcnt(1)\n"
        "ds_read_b128 v[12:15], v0 offset:25600\n"
        "buffer_load_dword v4, v2, s[40:43], s86 offen sc1\n"
        "v_mfma_f32_16x16x32_bf16 v[36:39], v[88:91], v[16:19], v[36:39]\n"
        "v_mfma_f32_16x16x32_bf16 v[40:43], v[92:95], v[16:19], v[40:43]\n"
        "v_mfma_f32_16x16x32_bf16 v[44:47], v[96:99], v[16:19], v[44:47]\n"
        "ds_read_b128 v[16:19], v0 offset:26624\n"
        "v_mfma_f32_16x16x32_bf16 v[36:39], v[100:103], v[20:23], v[36:39]\n"
        "v_mfma_f32_16x16x32_bf16 v[40:43], v[104:107], v[20:23], v[40:43]\n"
        "buffer_store_dword v5, v3, s[40:43], s73 offen sc1\n"
        "s_add_u32 s74, s74, 1\n"
        "v_mfma_f32_16x16x32_bf16 v[44:47], v[108:111], v[20:23], v[44:47]\n"
        "s_waitcnt lgkmcnt(1)\n"
        "ds_read_b128 v[20:23], v0 offset:27648\n"
        "s_mov_b32 s84, s89\n"
        "v_mfma_f32_16x16x32_bf16 v[36:39], v[112:115], v[8:11], v[36:39]\n"
        "s_mov_b32 s73, s60\n"
        "s_add_u32 s72, s75, s59\n"
        "s_add_u32 s72, s72, 147456\n"
        "v_mfma_f32_16x16x32_bf16 v[40:43], v[116:119], v[8:11], v[40:43]\n"
        "v_mfma_f32_16x16x32_bf16 v[44:47], v[120:123], v[8:11], v[44:47]\n"
        "ds_read_b128 v[8:11], v0 offset:28672\n"
        "v_mfma_f32_16x16x32_bf16 v[36:39], v[124:127], v[12:15], v[36:39]\n"
        "v_mfma_f32_16x16x32_bf16 v[40:43], v[128:131], v[12:15], v[40:43]\n"
        "v_mfma_f32_16x16x32_bf16 v[44:47], v[132:135], v[12:15], v[44:47]\n"
        "s_waitcnt lgkmcnt(1)\n"
        "ds_read_b128 v[12:15], v0 offset:29696\n"
        "v_mfma_f32_16x16x32_bf16 v[36:39], v[136:139], v[16:19], v[36:39]\n"
        "v_mfma_f32_16x16x32_bf16 v[40:43], v[140:143], v[16:19], v[40:43]\n"
        "v_mfma_f32_16x16x32_bf16 v[44:47], v[144:147], v[16:19], v[44:47]\n"
        "ds_read_b128 v[16:19], v0 offset:30720\n"
        "v_mfma_f32_16x16x32_bf16 v[36:39], v[148:151], v[20:23], v[36:39]\n"
        "v_mfma_f32_16x16x32_bf16 v[40:43], v[152:155], v[20:23], v[40:43]\n"
        "v_mfma_f32_16x16x32_bf16 v[44:47], v[156:159], v[20:23], v[44:47]\n"
        "s_waitcnt lgkmcnt(1)\n"
        "ds_read_b128 v[20:23], v0 offset:31744\n"
        "v_mfma_f32_16x16x32_bf16 v[36:39], v[160:163], v[8:11], v[36:39]\n"
        "v_mfma_f32_16x16x32_bf16 v[40:43], v[164:167], v[8:11], v[40:43]\n"
        "v_mfma_f32_16x16x32_bf16 v[44:47], v[168:171], v[8:11], v[44:47]\n"
        "ds_read_b128 v[8:11], v0 offset:32768\n"
        "v_mfma_f32_16x16x32_bf16 v[36:39], v[172:175], v[12:15], v[36:39]\n"
        "v_mfma_f32_16x16x32_bf16 v[40:43], v[176:179], v[12:15], v[40:43]\n"
        "v_mfma_f32_16x16x32_bf16 v[44:47], v[180:183], v[12:15], v[44:47]\n"
        "s_waitcnt lgkmcnt(1)\n"
        "ds_read_b128 v[12:15], v0 offset:33792\n"
        "v_mfma_f32_16x16x32_bf16 v[36:39], v[184:187], v[16:19], v[36:39]\n"
        "v_mfma_f32_16x16x32_bf16 v[40:43], v[188:191], v[16:19], v[40:43]\n"
        "v_mfma_f32_16x16x32_bf16 v[44:47], v[192:195], v[16:19], v[44:47]\n"
        "ds_read_b128 v[16:19], v0 offset:34816\n"
        "v_mfma_f32_16x16x32_bf16 v[36:39], v[196:199], v[20:23], v[36:39]\n"
        "s_waitcnt vmcnt(1)\n"
        "v_cmp_gt_i32 vcc, s70, v4\n"
        "v_mfma_f32_16x16x32_bf16 v[40:43], v[200:203], v[20:23], v[40:43]\n"
        "v_mfma_f32_16x16x32_bf16 v[44:47], v[204:207], v[20:23], v[44:47]\n"
        "s_waitcnt lgkmcnt(1)\n"
        "ds_read_b128 v[20:23], v0 offset:35840\n"
        "s_and_b64 vcc, vcc, 63\n"
        "v_mfma_f32_16x16x32_bf16 v[36:39], v[208:211], v[8:11], v[36:39]\n"
        "s_cbranch_vccnz .Lf2_i12_%=\n"
        "v_mfma_f32_16x16x32_bf16 v[40:43], v[212:215], v[8:11], v[40:43]\n"
        "buffer_load_dwordx4 v1, s[40:43], s71 offen offset:0 sc1 lds\n"
        "v_mfma_f32_16x16x32_bf16 v[44:47], v[216:219], v[8:11], v[44:47]\n"
        "v_mfma_f32_16x16x32_bf16 v[36:39], v[220:223], v[12:15], v[36:39]\n"
        "buffer_load_dwordx4 v1, s[40:43], s71 offen offset:1024 sc1 lds\n"
        "s_add_u32 m0, m0, s85\n"
        "s_add_u32 s71, s71, 16384\n"
        "v_mfma_f32_16x16x32_bf16 v[40:43], v[224:227], v[12:15], v[40:43]\n"
        "v_mfma_f32_16x16x32_bf16 v[44:47], v[228:231], v[12:15], v[44:47]\n"
        "s_waitcnt lgkmcnt(0)\n"
        "buffer_load_dwordx4 v1, s[40:43], s81 offen offset:0 sc1 lds\n"
        "s_sub_u32 m0, m0, s85\n"
        "s_add_u32 m0, m0, 12288\n"
        "s_add_u32 s81, s81, 16384\n"
        "v_mfma_f32_16x16x32_bf16 v[36:39], v[232:235], v[16:19], v[36:39]\n"
        "v_mfma_f32_16x16x32_bf16 v[40:43], v[236:239], v[16:19], v[40:43]\n"
        "buffer_load_dwordx4 v1, s[40:43], s71 offen offset:0 sc1 lds\n"
        "v_mfma_f32_16x16x32_bf16 v[44:47], v[240:243], v[16:19], v[44:47]\n"
        "v_mfma_f32_16x16x32_bf16 v[36:39], v[244:247], v[20:23], v[36:39]\n"
        "buffer_load_dwordx4 v1, s[40:43], s71 offen offset:1024 sc1 lds\n"
        "s_add_u32 m0, m0, s85\n"
        "s_add_u32 s71, s71, 16384\n"
        "v_mfma_f32_16x16x32_bf16 v[40:43], v[248:251], v[20:23], v[40:43]\n"
        "v_mfma_f32_16x16x32_bf16 v[44:47], v[252:255], v[20:23], v[44:47]\n"
        "buffer_load_dwordx4 v1, s[40:43], s81 offen offset:0 sc1 lds\n"
        "s_sub_u32 m0, m0, s85\n"
        "s_add_u32 m0, m0, 12288\n"
        "s_add_u32 s81, s81, 16384\n"
        "buffer_load_dwordx4 v1, s[40:43], s71 offen offset:0 sc1 lds\n"
        "buffer_load_dwordx4 v1, s[40:43], s71 offen offset:1024 sc1 lds\n"
        "s_add_u32 m0, m0, s85\n"
        "s_add_u32 s71, s71, 16384\n"
        "buffer_load_dwordx4 v1, s[40:43], s81 offen offset:0 sc1 lds\n"
        "s_add_u32 s69, s69, 1\n"
        "s_nop 2\n"
        "v_cvt_pk_bf16_f32 v36, v36, v37\n"
        "v_cvt_pk_bf16_f32 v37, v38, v39\n"
        "v_cvt_pk_bf16_f32 v38, v40, v41\n"
        "v_cvt_pk_bf16_f32 v39, v42, v43\n"
        "buffer_store_dwordx4 v[36:39], v48, s[40:43], s72 offen offset:0 sc1\n"
        "v_cvt_pk_bf16_f32 v44, v44, v45\n"
        "v_cvt_pk_bf16_f32 v45, v46, v47\n"
        "s_nop 0\n"
        "v_permlane16_swap_b32 v44, v46\n"
        "v_permlane16_swap_b32 v45, v47\n"
        "buffer_store_dwordx4 v[44:47], v7, s[40:43], s72 offen offset:0 sc1\n"
        "v_mov_b32 v5, s74\n"
        "s_mov_b32 s84, s60\n"
        "s_waitcnt vmcnt(8)\n"
        "s_branch .Lf2_go1_%=\n"
        ".Lf2_i12_%=:\n"
        "v_mfma_f32_16x16x32_bf16 v[40:43], v[212:215], v[8:11], v[40:43]\n"
        "buffer_load_dword v4, v2, s[40:43], s86 offen sc1\n"
        "v_mfma_f32_16x16x32_bf16 v[44:47], v[216:219], v[8:11], v[44:47]\n"
        "v_mfma_f32_16x16x32_bf16 v[36:39], v[220:223], v[12:15], v[36:39]\n"
        "v_mfma_f32_16x16x32_bf16 v[40:43], v[224:227], v[12:15], v[40:43]\n"
        "v_mfma_f32_16x16x32_bf16 v[44:47], v[228:231], v[12:15], v[44:47]\n"
        "s_waitcnt lgkmcnt(0)\n"
        "v_mfma_f32_16x16x32_bf16 v[36:39], v[232:235], v[16:19], v[36:39]\n"
        "v_mfma_f32_16x16x32_bf16 v[40:43], v[236:239], v[16:19], v[40:43]\n"
        "v_mfma_f32_16x16x32_bf16 v[44:47], v[240:243], v[16:19], v[44:47]\n"
        "v_mfma_f32_16x16x32_bf16 v[36:39], v[244:247], v[20:23], v[36:39]\n"
        "v_mfma_f32_16x16x32_bf16 v[40:43], v[248:251], v[20:23], v[40:43]\n"
        "v_mfma_f32_16x16x32_bf16 v[44:47], v[252:255], v[20:23], v[44:47]\n"
        "s_nop 2\n"
        "v_cvt_pk_bf16_f32 v36, v36, v37\n"
        "v_cvt_pk_bf16_f32 v37, v38, v39\n"
        "v_cvt_pk_bf16_f32 v38, v40, v41\n"
        "v_cvt_pk_bf16_f32 v39, v42, v43\n"
        "buffer_store_dwordx4 v[36:39], v48, s[40:43], s72 offen offset:0 sc1\n"
        "v_cvt_pk_bf16_f32 v44, v44, v45\n"
        "v_cvt_pk_bf16_f32 v45, v46, v47\n"
        "s_nop 0\n"
        "v_permlane16_swap_b32 v44, v46\n"
        "v_permlane16_swap_b32 v45, v47\n"
        "buffer_store_dwordx4 v[44:47], v7, s[40:43], s72 offen offset:0 sc1\n"
        "s_waitcnt vmcnt(2)\n"
        "v_cmp_gt_i32 vcc, s70, v4\n"
        "s_nop 0\n"
        "s_and_b64 vcc, vcc, 63\n"
        "s_cbranch_vccnz .Lf2_late1_%=\n"
        "buffer_load_dwordx4 v1, s[40:43], s71 offen offset:0 sc1 lds\n"
        "buffer_load_dwordx4 v1, s[40:43], s71 offen offset:1024 sc1 lds\n"
        "s_add_u32 m0, m0, s85\n"
        "s_add_u32 s71, s71, 16384\n"
        "buffer_load_dwordx4 v1, s[40:43], s81 offen offset:0 sc1 lds\n"
        "s_sub_u32 m0, m0, s85\n"
        "s_add_u32 m0, m0, 12288\n"
        "s_add_u32 s81, s81, 16384\n"
        "buffer_load_dwordx4 v1, s[40:43], s71 offen offset:0 sc1 lds\n"
        "buffer_load_dwordx4 v1, s[40:43], s71 offen offset:1024 sc1 lds\n"
        "s_add_u32 m0, m0, s85\n"
        "s_add_u32 s71, s71, 16384\n"
        "buffer_load_dwordx4 v1, s[40:43], s81 offen offset:0 sc1 lds\n"
        "s_sub_u32 m0, m0, s85\n"
        "s_add_u32 m0, m0, 12288\n"
        "s_add_u32 s81, s81, 16384\n"
        "buffer_load_dwordx4 v1, s[40:43], s71 offen offset:0 sc1 lds\n"
        "buffer_load_dwordx4 v1, s[40:43], s71 offen offset:1024 sc1 lds\n"
        "s_add_u32 m0, m0, s85\n"
        "s_add_u32 s71, s71, 16384\n"
        "buffer_load_dwordx4 v1, s[40:43], s81 offen offset:0 sc1 lds\n"
        "s_add_u32 s69, s69, 1\n"
        "s_waitcnt vmcnt(9)\n"
        "v_mov_b32 v5, s74\n"
        "buffer_store_dword v5, v3, s[40:43], s73 offen sc1\n"
        "s_waitcnt vmcnt(7)\n"
        "s_branch .Lf2_go1_%=\n"
        ".Lf2_late1_%=:\n"
        "s_waitcnt vmcnt(0)\n"
        "v_mov_b32 v5, s74\n"
        "buffer_store_dword v5, v3, s[40:43], s73 offen sc1\n"
        "s_cmp_eq_u32 s69, 128\n"
        "s_cbranch_scc0 .Lf2_wait1_%=\n"
        "s_endpgm\n"
        ".Lf2_wait1_%=:\n"
        "s_mov_b32 s64, 0\n"
        ".Lf2_in1_top_%=:\n"
        "buffer_load_dword v4, v2, s[40:43], s86 offen sc1\n"
        "s_waitcnt vmcnt(0)\n"
        "v_cmp_gt_i32 vcc, s70, v4\n"
        "s_nop 0\n"
        "s_and_b64 vcc, vcc, 63\n"
        "s_cbranch_vccz .Lf2_in1_ok_%=\n"
        "s_add_u32 s64, s64, 1\n"
        "s_and_b32 s65, s64, 255\n"
        "s_cbranch_scc1 .Lf2_in1_go_%=\n"
        "s_mov_b32 s65, 49152\n"
        "s_cmp_lt_u32 s64, 1048576\n"
        "s_cbranch_scc1 .Lf2_in1_chk_%=\n"
        "v_mov_b32 v4, 1\n"
        "buffer_store_dword v4, off, s[40:43], s65 sc1\n"
        "s_waitcnt vmcnt(0)\n"
        ".Lf2_in1_chk_%=:\n"
        "buffer_load_dword v4, off, s[40:43], s65 sc1\n"
        "s_waitcnt vmcnt(0)\n"
        "v_readfirstlane_b32 s65, v4\n"
        "s_nop 0\n"
        "s_cmp_lg_u32 s65, 1\n"
        "s_cbranch_scc1 .Lf2_in1_go_%=\n"
        "s_endpgm\n"
        ".Lf2_in1_go_%=:\n"
        "s_branch .Lf2_in1_top_%=\n"
        ".Lf2_in1_ok_%=:\n"
        "buffer_load_dwordx4 v1, s[40:43], s71 offen offset:0 sc1 lds\n"
        "buffer_load_dwordx4 v1, s[40:43], s71 offen offset:1024 sc1 lds\n"
        "s_add_u32 m0, m0, s85\n"
        "s_add_u32 s71, s71, 16384\n"
        "buffer_load_dwordx4 v1, s[40:43], s81 offen offset:0 sc1 lds\n"
        "s_sub_u32 m0, m0, s85\n"
        "s_add_u32 m0, m0, 12288\n"
        "s_add_u32 s81, s81, 16384\n"
        "buffer_load_dwordx4 v1, s[40:43], s71 offen offset:0 sc1 lds\n"
        "buffer_load_dwordx4 v1, s[40:43], s71 offen offset:1024 sc1 lds\n"
        "s_add_u32 m0, m0, s85\n"
        "s_add_u32 s71, s71, 16384\n"
        "buffer_load_dwordx4 v1, s[40:43], s81 offen offset:0 sc1 lds\n"
        "s_sub_u32 m0, m0, s85\n"
        "s_add_u32 m0, m0, 12288\n"
        "s_add_u32 s81, s81, 16384\n"
        "buffer_load_dwordx4 v1, s[40:43], s71 offen offset:0 sc1 lds\n"
        "buffer_load_dwordx4 v1, s[40:43], s71 offen offset:1024 sc1 lds\n"
        "s_add_u32 m0, m0, s85\n"
        "s_add_u32 s71, s71, 16384\n"
        "buffer_load_dwordx4 v1, s[40:43], s81 offen offset:0 sc1 lds\n"
        "s_add_u32 s69, s69, 1\n"
        "s_waitcnt vmcnt(6)\n"
        ".Lf2_go1_%=:\n"
        "s_mov_b32 s72, 241172480\n"
        "s_mov_b32 s83, 241172480\n"
        "s_mov_b32 s73, 241172480\n"
        "v_add_u32 v0, s78, v6\n"
        "s_barrier\n"
        "ds_read_b128 v[8:11], v0 offset:0\n"
        "ds_read_b128 v[12:15], v0 offset:1024\n"
        "ds_read_b128 v[16:19], v0 offset:2048\n"
        "ds_read_b128 v[20:23], v0 offset:3072\n"
        "s_waitcnt lgkmcnt(2)\n"
        "s_branch .Lf2_i0_%=\n"
        ".Lf2_end_%=:\n"
            :: "s"(p.ws), "s"(p.w2), "s"(p.w2), "s"(p.w2), "s"(pair(w, delay)), "s"(pair(x4, pr)),
               "s"(pair(hb + w * 2048, 97 * PAGE + spread(C_P5))), "s"(pair(flag(F_F2 + fl), (w * 2048) | (8192 - w * 1024) << 16)), "s"(pair(hb + 6 * 16384 + w * 1024, 0)),
               "v"(pair(lane * 16, lane * 16)), "v"(pair(lane < 6 ? flag(F_F1 + 128 * nu + 24 * kap + 8 * (lane >> 1) + 2 * w + (lane & 1)) : OOB, fo4)), K_ARGS
            : "v0", "v1", "v2", "v3", "v4", "v5", "v6", "v7", "v8", "v9", "v10", "v11", "v12", "v13", "v14", "v15", "v16", "v17", "v18", "v19", "v20", "v21", "v22", "v23", "v24", "v25", "v26", "v27", "v28", "v29", "v30", "v31", "v32", "v33", "v34", "v35", "v36", "v37", "v38", "v39", "v40", "v41", "v42", "v43", "v44", "v45", "v46", "v47", "v48", "v49", "v50", "v51", "v52", "v53", "v54", "v55", "v56", "v57", "v58", "v59", "v60", "v61", "v62", "v63", "v64", "v65", "v66", "v67", "v68", "v69", "v70", "v71", "v72", "v73", "v74", "v75", "v76", "v77", "v78", "v79", "v255", "a255", "s30", "s31", "s32", "s33", "s34", "s35", "s36", "s37", "s38", "s39", "s40", "s41", "s42", "s43", "s44", "s45", "s46", "s47", "s48", "s49", "s50", "s51", "s52", "s53", "s54", "s55", "s56", "s57", "s58", "s59", "s60", "s61", "s62", "s63", "s64", "s65", "s66", "s67", "s68", "s69", "s70", "s71", "s72", "s73", "s74", "s75", "s76", "s77", "s78", "s79", "s80", "s81", "s82", "s83", "s84", "s85", "s86", "s87", "s88", "s89", "s90", "s91", "s92", "s93", "s94", "s95", "s96", "s97", "s98", "s99", "vcc", "scc", "memory");
    }
    __builtin_unreachable();
}

__device__ __forceinline__ void role_attn(const Params& p, int h0, int x4, int pr) {
    const unsigned lane = threadIdx.x & 63, i = lane & 15, g = lane >> 4;
    const int w = rfl(threadIdx.x >> 6), h = h0 + w;
    asm volatile(
        "s_nop 4\n"
        "s_mov_b64 s[40:41], %0\n"
        "s_and_b32 s41, s41, 0xffff\n"
        "s_mov_b32 s42, 236978176\n"
        "s_mov_b32 s43, 0x20000\n"
        "s_mov_b64 s[56:57], %1\n"
        "s_mov_b64 s[58:59], %2\n"
        "s_mov_b64 s[60:61], %3\n"
        "v_mov_b64 v[0:1], %4\n"
        "v_mov_b64 v[2:3], %5\n"
        "v_mov_b64 v[4:5], %6\n"
        "v_cmp_ge_i32 s[76:77], 0, v5\n"
        "v_cmp_ge_i32 s[78:79], -1, v5\n"
        "v_cmp_ge_i32 s[80:81], -2, v5\n"
        "v_cmp_ge_i32 s[82:83], -3, v5\n"
        "s_mov_b32 s84, 0\n"
        "s_mov_b32 s85, -1\n"
        "s_mov_b32 s75, 0x3e0293ee\n"
        "s_getpc_b64 s[44:45]\n"
        ".Lat_pc_%=:\n"
        "s_add_u32 s44, s44, .Lat_code_%=-.Lat_pc_%=\n"
        "s_addc_u32 s45, s45, 0\n"
        "s_and_b32 s45, s45, 0xffff\n"
        "s_and_b32 s44, s44, -4\n"
        "s_mov_b32 s46, .Lat_end_%=-.Lat_code_%=\n"
        "s_mov_b32 s47, 0x20000\n"
        "s_mov_b32 s65, 0\n"
        "buffer_load_dwordx4 v[24:27], v0, s[44:47], s65 offen\n"
        "s_add_u32 s65, s65, 1024\n"
        "buffer_load_dwordx4 v[28:31], v0, s[44:47], s65 offen\n"
        "s_add_u32 s65, s65, 1024\n"
        "buffer_load_dwordx4 v[32:35], v0, s[44:47], s65 offen\n"
        "s_add_u32 s65, s65, 1024\n"
        "buffer_load_dwordx4 v[36:39], v0, s[44:47], s65 offen\n"
        "s_add_u32 s65, s65, 1024\n"
        "buffer_load_dwordx4 v[40:43], v0, s[44:47], s65 offen\n"
        "s_add_u32 s65, s65, 1024\n"
        "v_lshrrev_b32 v12, 4, v0\n"
        "s_mov_b32 s50, s56\n"
        "v_lshlrev_b32 v14, 7, v12\n"
        "s_sub_u32 s66, 0, s50\n"
        "s_mov_b32 s52, 32768\n"
        "s_mov_b32 s64, 0\n"
        ".Lat_pg0_top_%=:\n"
        "buffer_load_dword v13, v14, s[40:43], s52 offen sc1\n"
        "s_waitcnt vmcnt(0)\n"
        "v_cmp_gt_i32 vcc, 0, v13\n"
        "s_cbranch_vccz .Lat_pg0_ok_%=\n"
        "s_add_u32 s64, s64, 1\n"
        "s_and_b32 s65, s64, 255\n"
        "s_cbranch_scc1 .Lat_pg0_go_%=\n"
        "s_mov_b32 s65, 49152\n"
        "s_cmp_lt_u32 s64, 1048576\n"
        "s_cbranch_scc1 .Lat_pg0_chk_%=\n"
        "v_mov_b32 v13, 1\n"
        "buffer_store_dword v13, off, s[40:43], s65 sc1\n"
        "s_waitcnt vmcnt(0)\n"
        ".Lat_pg0_chk_%=:\n"
        "buffer_load_dword v13, off, s[40:43], s65 sc1\n"
        "s_waitcnt vmcnt(0)\n"
        "v_readfirstlane_b32 s65, v13\n"
        "s_nop 0\n"
        "s_cmp_lg_u32 s65, 1\n"
        "s_cbranch_scc1 .Lat_pg0_go_%=\n"
        "s_endpgm\n"
        ".Lat_pg0_go_%=:\n"
        "s_sleep 2\n"
        "s_branch .Lat_pg0_top_%=\n"
        ".Lat_pg0_ok_%=:\n"
        "v_cmp_eq_u32 s[52:53], 1, v13\n"
        "s_nop 1\n"
        "s_mov_b32 s54, 40960\n"
        "s_mov_b32 s64, 0\n"
        ".Lat_pg1_top_%=:\n"
        "buffer_load_dword v13, v14, s[40:43], s54 offen sc1\n"
        "s_waitcnt vmcnt(0)\n"
        "v_cmp_gt_i32 vcc, 0, v13\n"
        "s_cbranch_vccz .Lat_pg1_ok_%=\n"
        "s_add_u32 s64, s64, 1\n"
        "s_and_b32 s65, s64, 255\n"
        "s_cbranch_scc1 .Lat_pg1_go_%=\n"
        "s_mov_b32 s65, 49152\n"
        "s_cmp_lt_u32 s64, 1048576\n"
        "s_cbranch_scc1 .Lat_pg1_chk_%=\n"
        "v_mov_b32 v13, 1\n"
        "buffer_store_dword v13, off, s[40:43], s65 sc1\n"
        "s_waitcnt vmcnt(0)\n"
        ".Lat_pg1_chk_%=:\n"
        "buffer_load_dword v13, off, s[40:43], s65 sc1\n"
        "s_waitcnt vmcnt(0)\n"
        "v_readfirstlane_b32 s65, v13\n"
        "s_nop 0\n"
        "s_cmp_lg_u32 s65, 1\n"
        "s_cbranch_scc1 .Lat_pg1_go_%=\n"
        "s_endpgm\n"
        ".Lat_pg1_go_%=:\n"
        "s_sleep 2\n"
        "s_branch .Lat_pg1_top_%=\n"
        ".Lat_pg1_ok_%=:\n"
        "v_cmp_eq_u32 s[54:55], 1, v13\n"
        "s_nop 1\n"
        "s_mov_b32 vcc_lo, 0x55555555\n"
        "s_mov_b32 vcc_hi, 0x55555555\n"
        "s_xor_b64 vcc, vcc, s[52:53]\n"
        "s_bcnt1_i32_b64 s65, vcc\n"
        "s_sub_u32 s64, s65, 23\n"
        "s_cmp_lt_u32 s64, 19\n"
        "s_cbranch_scc1 .Lat_rawmap_%=\n"
        "s_cmp_lt_u32 s65, 32\n"
        "s_cselect_b32 s65, 0, -1\n"
        "s_xor_b32 s52, s65, 0x55555555\n"
        "s_xor_b32 s53, s65, 0x55555555\n"
        "s_xor_b32 s54, s65, 0x55555555\n"
        "s_xor_b32 s55, s65, 0xaaaaaaaa\n"
        ".Lat_rawmap_%=:\n"
        "s_xor_b32 s52, s52, s66\n"
        "s_xor_b32 s53, s53, s66\n"
        "s_xor_b32 s54, s54, s66\n"
        "s_xor_b32 s55, s55, s66\n"
        "s_and_b32 s71, s55, 1\n"
        "s_lshl_b32 s71, s71, 13\n"
        "s_add_u32 s60, s60, s71\n"
        "v_mov_b64 v[166:167], 0\n"
        "v_mov_b64 v[172:173], 0\n"
        "v_mov_b64 v[176:177], 0\n"
        "v_mov_b64 v[178:179], 0\n"
        "v_mov_b64 v[182:183], 0\n"
        "v_mov_b32 v10, 0xff61b1e6\n"
        "s_mov_b32 s68, 0\n"
        "s_mov_b32 s72, 0\n"
        "s_bitcmp1_b64 s[52:53], s68\n"
        "s_cselect_b32 s65, 8192, 0\n"
        "s_lshl_b32 s70, s68, 21\n"
        "s_add_u32 s70, s70, s65\n"
        ".Lat_code_%=:\n"
        ".Lat_top_%=:\n"
        "s_mov_b32 s69, s70\n"
        "s_lshl_b32 s67, s68, 1\n"
        "s_mov_b32 s64, 0\n"
        "s_mov_b32 s86, s72\n"
        "s_cmp_eq_u32 s72, 7\n"
        "s_cbranch_scc1 .Lat_T_%=\n"
        ".Lat_more_%=:\n"
        "buffer_load_dword v5, v1, s[40:43], s71 offen sc1\n"
        "s_mov_b32 s86, s72\n"
        "s_waitcnt vmcnt(0)\n"
        "v_min_i32_dpp v5, v5, v5 quad_perm:[1,0,3,2] row_mask:0xf bank_mask:0xf\n"
        "s_nop 1\n"
        "v_min_i32_dpp v5, v5, v5 quad_perm:[2,3,0,1] row_mask:0xf bank_mask:0xf\n"
        "s_nop 0\n"
        "v_readlane_b32 s73, v5, 0\n"
        "v_readlane_b32 s74, v5, 4\n"
        "s_nop 0\n"
        "s_bitcmp1_b32 s72, 0\n"
        "s_cbranch_scc1 .Lat_hasK_%=\n"
        "s_add_u32 s65, s67, 1\n"
        "s_cmp_ge_i32 s73, s65\n"
        "s_cbranch_scc0 .Lat_hasK_%=\n"
        "s_add_u32 s65, s69, s58\n"
        "s_add_u32 s65, s65, 524288\n"
        "s_add_u32 s66, s65, 4096\n"
        "buffer_load_dwordx4 v[48:51], v0, s[40:43], s65 offen offset:0 sc1\n"
        "buffer_load_dwordx4 v[52:55], v0, s[40:43], s65 offen offset:1024 sc1\n"
        "buffer_load_dwordx4 v[56:59], v0, s[40:43], s65 offen offset:2048 sc1\n"
        "buffer_load_dwordx4 v[60:63], v0, s[40:43], s65 offen offset:3072 sc1\n"
        "buffer_load_dwordx4 v[64:67], v0, s[40:43], s66 offen offset:0 sc1\n"
        "buffer_load_dwordx4 v[68:71], v0, s[40:43], s66 offen offset:1024 sc1\n"
        "buffer_load_dwordx4 v[72:75], v0, s[40:43], s66 offen offset:2048 sc1\n"
        "buffer_load_dwordx4 v[76:79], v0, s[40:43], s66 offen offset:3072 sc1\n"
        "s_or_b32 s72, s72, 1\n"
        ".Lat_hasK_%=:\n"
        "s_bitcmp1_b32 s72, 1\n"
        "s_cbranch_scc1 .Lat_hasV_%=\n"
        "s_add_u32 s65, s67, 1\n"
        "s_cmp_ge_i32 s73, s65\n"
        "s_cbranch_scc0 .Lat_hasV_%=\n"
        "s_add_u32 s65, s69, s58\n"
        "s_add_u32 s65, s65, 1048576\n"
        "s_add_u32 s66, s65, 4096\n"
        "buffer_load_dwordx4 v[80:83], v0, s[40:43], s65 offen offset:0 sc1\n"
        "buffer_load_dwordx4 v[84:87], v0, s[40:43], s65 offen offset:1024 sc1\n"
        "buffer_load_dwordx4 v[88:91], v0, s[40:43], s65 offen offset:2048 sc1\n"
        "buffer_load_dwordx4 v[92:95], v0, s[40:43], s65 offen offset:3072 sc1\n"
        "buffer_load_dwordx4 v[96:99], v0, s[40:43], s66 offen offset:0 sc1\n"
        "buffer_load_dwordx4 v[100:103], v0, s[40:43], s66 offen offset:1024 sc1\n"
        "buffer_load_dwordx4 v[104:107], v0, s[40:43], s66 offen offset:2048 sc1\n"
        "buffer_load_dwordx4 v[108:111], v0, s[40:43], s66 offen offset:3072 sc1\n"
        "s_or_b32 s72, s72, 2\n"
        ".Lat_hasV_%=:\n"
        "s_bitcmp1_b32 s72, 3\n"
        "s_cbranch_scc1 .Lat_hasM_%=\n"
        "s_cmp_ge_i32 s74, s68\n"
        "s_cbranch_scc0 .Lat_hasM_%=\n"
        "s_add_u32 s65, s69, s59\n"
        "buffer_load_dwordx4 v[112:115], v3, s[40:43], s65 offen offset:0 sc1\n"
        "buffer_load_dwordx4 v[116:119], v3, s[40:43], s65 offen offset:512 sc1\n"
        "buffer_load_dwordx4 v[120:123], v3, s[40:43], s65 offen offset:1024 sc1\n"
        "buffer_load_dwordx4 v[124:127], v3, s[40:43], s65 offen offset:1536 sc1\n"
        "buffer_load_dwordx4 v[128:131], v4, s[40:43], s65 offen offset:2048 sc1\n"
        "buffer_load_dwordx4 v[132:135], v4, s[40:43], s65 offen offset:2560 sc1\n"
        "buffer_load_dwordx4 v[136:139], v4, s[40:43], s65 offen offset:3072 sc1\n"
        "buffer_load_dwordx4 v[140:143], v4, s[40:43], s65 offen offset:3584 sc1\n"
        "s_or_b32 s72, s72, 8\n"
        ".Lat_hasM_%=:\n"
        "s_bitcmp1_b32 s72, 2\n"
        "s_cbranch_scc1 .Lat_hasQ_%=\n"
        "s_add_u32 s65, s67, 1\n"
        "s_cmp_ge_i32 s73, s65\n"
        "s_cbranch_scc0 .Lat_hasQ_%=\n"
        "s_add_u32 s65, s69, s58\n"
        "s_add_u32 s65, s65, 0\n"
        "s_add_u32 s66, s65, 4096\n"
        "buffer_load_dwordx4 v[16:19], v0, s[40:43], s65 offen offset:0 sc1\n"
        "buffer_load_dwordx4 v[20:23], v0, s[40:43], s65 offen offset:1024 sc1\n"
        "buffer_load_dwordx4 v[24:27], v0, s[40:43], s65 offen offset:2048 sc1\n"
        "buffer_load_dwordx4 v[28:31], v0, s[40:43], s65 offen offset:3072 sc1\n"
        "buffer_load_dwordx4 v[32:35], v0, s[40:43], s66 offen offset:0 sc1\n"
        "buffer_load_dwordx4 v[36:39], v0, s[40:43], s66 offen offset:1024 sc1\n"
        "buffer_load_dwordx4 v[40:43], v0, s[40:43], s66 offen offset:2048 sc1\n"
        "buffer_load_dwordx4 v[44:47], v0, s[40:43], s66 offen offset:3072 sc1\n"
        "s_or_b32 s72, s72, 4\n"
        ".Lat_hasQ_%=:\n"
        "s_and_b32 s65, s72, 7\n"
        "s_cmp_eq_u32 s65, 7\n"
        "s_cbranch_scc1 .Lat_T_%=\n"
        "s_add_u32 s64, s64, 1\n"
        "s_and_b32 s65, s64, 255\n"
        "s_cbranch_scc1 .Lat_sp_go_%=\n"
        "s_mov_b32 s65, 49152\n"
        "s_cmp_lt_u32 s64, 1048576\n"
        "s_cbranch_scc1 .Lat_sp_chk_%=\n"
        "v_mov_b32 v6, 1\n"
        "buffer_store_dword v6, off, s[40:43], s65 sc1\n"
        "s_waitcnt vmcnt(0)\n"
        ".Lat_sp_chk_%=:\n"
        "buffer_load_dword v6, off, s[40:43], s65 sc1\n"
        "s_waitcnt vmcnt(0)\n"
        "v_readfirstlane_b32 s65, v6\n"
        "s_nop 0\n"
        "s_cmp_lg_u32 s65, 1\n"
        "s_cbranch_scc1 .Lat_sp_go_%=\n"
        "s_endpgm\n"
        ".Lat_sp_go_%=:\n"
        "s_branch .Lat_more_%=\n"
        ".Lat_T_%=:\n"
        "s_add_u32 s66, s68, 1\n"
        "s_bitcmp1_b64 s[52:53], s66\n"
        "s_cselect_b32 s65, 8192, 0\n"
        "s_lshl_b32 s70, s66, 21\n"
        "s_add_u32 s70, s70, s65\n"
        "s_add_u32 s67, s67, 2\n"
        "s_bitcmp1_b32 s86, 2\n"
        "s_cbranch_scc1 .Lat_T2_%=\n"
        "s_waitcnt vmcnt(0)\n"
        ".Lat_T2_%=:\n"
        "s_cmp_eq_u32 s72, 15\n"
        "s_cbranch_scc1 .Lat_T3_%=\n"
        "buffer_load_dword v5, v1, s[40:43], s71 offen sc1\n"
        ".Lat_T3_%=:\n"
        "v_mfma_f32_16x16x32_bf16 v[144:147], v[48:51], v[16:19], 0\n"
        "v_mfma_f32_16x16x32_bf16 v[152:155], v[52:55], v[20:23], 0\n"
        "v_mfma_f32_16x16x32_bf16 v[148:151], v[48:51], v[20:23], 0\n"
        "v_mfma_f32_16x16x32_bf16 v[144:147], v[56:59], v[24:27], v[144:147]\n"
        "v_mfma_f32_16x16x32_bf16 v[152:155], v[60:63], v[28:31], v[152:155]\n"
        "v_mfma_f32_16x16x32_bf16 v[148:151], v[56:59], v[28:31], v[148:151]\n"
        "v_mfma_f32_16x16x32_bf16 v[144:147], v[64:67], v[32:35], v[144:147]\n"
        "v_mfma_f32_16x16x32_bf16 v[152:155], v[68:71], v[36:39], v[152:155]\n"
        "v_mfma_f32_16x16x32_bf16 v[148:151], v[64:67], v[36:39], v[148:151]\n"
        "v_mfma_f32_16x16x32_bf16 v[144:147], v[72:75], v[40:43], v[144:147]\n"
        "v_mfma_f32_16x16x32_bf16 v[152:155], v[76:79], v[44:47], v[152:155]\n"
        "v_mfma_f32_16x16x32_bf16 v[148:151], v[72:75], v[44:47], v[148:151]\n"
        "v_cndmask_b32 v144, v10, v144, s[76:77]\n"
        "v_cndmask_b32 v145, v10, v145, s[78:79]\n"
        "v_cndmask_b32 v146, v10, v146, s[80:81]\n"
        "v_cndmask_b32 v147, v10, v147, s[82:83]\n"
        "v_cndmask_b32 v152, v10, v152, s[76:77]\n"
        "v_cndmask_b32 v153, v10, v153, s[78:79]\n"
        "v_cndmask_b32 v154, v10, v154, s[80:81]\n"
        "v_cndmask_b32 v155, v10, v155, s[82:83]\n"
        "v_max3_f32 v8, v144, v145, v146\n"
        "v_max3_f32 v9, v152, v153, v154\n"
        "v_max_f32 v8, v8, v147\n"
        "v_max3_f32 v9, v9, v155, v148\n"
        "v_max3_f32 v9, v9, v149, v150\n"
        "v_max_f32 v9, v9, v151\n"
        "v_mul_f32 v144, s75, v144\n"
        "v_mul_f32 v145, s75, v145\n"
        "v_mul_f32 v146, s75, v146\n"
        "v_mul_f32 v147, s75, v147\n"
        "v_mul_f32 v148, s75, v148\n"
        "v_mul_f32 v149, s75, v149\n"
        "v_mul_f32 v150, s75, v150\n"
        "v_mul_f32 v151, s75, v151\n"
        "v_mul_f32 v152, s75, v152\n"
        "v_mul_f32 v153, s75, v153\n"
        "v_mul_f32 v154, s75, v154\n"
        "v_mul_f32 v155, s75, v155\n"
        "s_cmp_eq_u32 s72, 15\n"
        "s_cbranch_scc1 .Lat_hasm_%=\n"
        "s_mov_b32 s64, 0\n"
        ".Lat_wm_%=:\n"
        "s_waitcnt vmcnt(0)\n"
        "v_min_i32_dpp v5, v5, v5 quad_perm:[1,0,3,2] row_mask:0xf bank_mask:0xf\n"
        "s_nop 1\n"
        "v_min_i32_dpp v5, v5, v5 quad_perm:[2,3,0,1] row_mask:0xf bank_mask:0xf\n"
        "s_nop 0\n"
        "v_readlane_b32 s73, v5, 0\n"
        "v_readlane_b32 s74, v5, 4\n"
        "s_nop 0\n"
        "s_cmp_ge_i32 s74, s68\n"
        "s_cbranch_scc1 .Lat_gm_%=\n"
        "s_add_u32 s64, s64, 1\n"
        "s_and_b32 s65, s64, 255\n"
        "s_cbranch_scc1 .Lat_sq_go_%=\n"
        "s_mov_b32 s65, 49152\n"
        "s_cmp_lt_u32 s64, 1048576\n"
        "s_cbranch_scc1 .Lat_sq_chk_%=\n"
        "v_mov_b32 v6, 1\n"
        "buffer_store_dword v6, off, s[40:43], s65 sc1\n"
        "s_waitcnt vmcnt(0)\n"
        ".Lat_sq_chk_%=:\n"
        "buffer_load_dword v6, off, s[40:43], s65 sc1\n"
        "s_waitcnt vmcnt(0)\n"
        "v_readfirstlane_b32 s65, v6\n"
        "s_nop 0\n"
        "s_cmp_lg_u32 s65, 1\n"
        "s_cbranch_scc1 .Lat_sq_go_%=\n"
        "s_endpgm\n"
        ".Lat_sq_go_%=:\n"
        "buffer_load_dword v5, v1, s[40:43], s71 offen sc1\n"
        "s_branch .Lat_wm_%=\n"
        ".Lat_gm_%=:\n"
        "s_add_u32 s65, s69, s59\n"
        "buffer_load_dwordx4 v[112:115], v3, s[40:43], s65 offen offset:0 sc1\n"
        "buffer_load_dwordx4 v[116:119], v3, s[40:43], s65 offen offset:512 sc1\n"
        "buffer_load_dwordx4 v[120:123], v3, s[40:43], s65 offen offset:1024 sc1\n"
        "buffer_load_dwordx4 v[124:127], v3, s[40:43], s65 offen offset:1536 sc1\n"
        "buffer_load_dwordx4 v[128:131], v4, s[40:43], s65 offen offset:2048 sc1\n"
        "buffer_load_dwordx4 v[132:135], v4, s[40:43], s65 offen offset:2560 sc1\n"
        "buffer_load_dwordx4 v[136:139], v4, s[40:43], s65 offen offset:3072 sc1\n"
        "buffer_load_dwordx4 v[140:143], v4, s[40:43], s65 offen offset:3584 sc1\n"
        ".Lat_hasm_%=:\n"
        "s_mov_b32 s72, 0\n"
        "s_add_u32 s65, s70, s58\n"
        "s_add_u32 s65, s65, 524288\n"
        "s_add_u32 s66, s67, 1\n"
        "s_cmp_ge_i32 s73, s66\n"
        "s_cselect_b32 s65, s65, 241172480\n"
        "s_cselect_b32 s66, 1, 0\n"
        "s_or_b32 s72, s72, s66\n"
        "s_add_u32 s66, s65, 4096\n"
        "buffer_load_dwordx4 v[48:51], v0, s[40:43], s65 offen offset:0 sc1\n"
        "buffer_load_dwordx4 v[52:55], v0, s[40:43], s65 offen offset:1024 sc1\n"
        "buffer_load_dwordx4 v[56:59], v0, s[40:43], s65 offen offset:2048 sc1\n"
        "buffer_load_dwordx4 v[60:63], v0, s[40:43], s65 offen offset:3072 sc1\n"
        "buffer_load_dwordx4 v[64:67], v0, s[40:43], s66 offen offset:0 sc1\n"
        "buffer_load_dwordx4 v[68:71], v0, s[40:43], s66 offen offset:1024 sc1\n"
        "buffer_load_dwordx4 v[72:75], v0, s[40:43], s66 offen offset:2048 sc1\n"
        "buffer_load_dwordx4 v[76:79], v0, s[40:43], s66 offen offset:3072 sc1\n"
        "s_add_u32 s65, s70, s58\n"
        "s_add_u32 s65, s65, 0\n"
        "s_add_u32 s66, s67, 1\n"
        "s_cmp_ge_i32 s73, s66\n"
        "s_cselect_b32 s65, s65, 241172480\n"
        "s_cselect_b32 s66, 4, 0\n"
        "s_or_b32 s72, s72, s66\n"
        "s_add_u32 s66, s65, 4096\n"
        "s_waitcnt vmcnt(15)\n"
        "v_mfma_f32_16x16x32_bf16 v[156:159], v[112:115], v[16:19], 0\n"
        "s_waitcnt vmcnt(14)\n"
        "v_mfma_f32_16x16x32_bf16 v[156:159], v[116:119], v[24:27], v[156:159]\n"
        "s_waitcnt vmcnt(13)\n"
        "v_mfma_f32_16x16x32_bf16 v[156:159], v[120:123], v[32:35], v[156:159]\n"
        "s_waitcnt vmcnt(12)\n"
        "v_mfma_f32_16x16x32_bf16 v[156:159], v[124:127], v[40:43], v[156:159]\n"
        "v_mfma_f32_16x16x32_bf16 v[160:163], v[112:115], v[20:23], 0\n"
        "v_mfma_f32_16x16x32_bf16 v[160:163], v[116:119], v[28:31], v[160:163]\n"
        "v_mfma_f32_16x16x32_bf16 v[160:163], v[120:123], v[36:39], v[160:163]\n"
        "v_mfma_f32_16x16x32_bf16 v[160:163], v[124:127], v[44:47], v[160:163]\n"
        "v_max3_f32 v8, v8, v156, v157\n"
        "buffer_load_dwordx4 v[16:19], v0, s[40:43], s65 offen offset:0 sc1\n"
        "v_max3_f32 v216, v8, v158, v159\n"
        "v_max3_f32 v217, v8, v158, v159\n"
        "v_mul_f32 v156, s75, v156\n"
        "v_mul_f32 v157, s75, v157\n"
        "v_permlane16_swap_b32 v216, v217\n"
        "v_max_f32 v8, v216, v217\n"
        "buffer_load_dwordx4 v[20:23], v0, s[40:43], s65 offen offset:1024 sc1\n"
        "v_max_f32 v216, v216, v217\n"
        "v_mul_f32 v158, s75, v158\n"
        "v_mul_f32 v159, s75, v159\n"
        "v_permlane32_swap_b32 v8, v216\n"
        "v_max_f32 v8, v8, v216\n"
        "v_mul_f32 v8, s75, v8\n"
        "buffer_load_dwordx4 v[24:27], v0, s[40:43], s65 offen offset:2048 sc1\n"
        "v_cndmask_b32 v216, v8, -v10, s[84:85]\n"
        "v_sub_f32 v144, v144, v8\n"
        "v_exp_f32 v144, v144\n"
        "v_sub_f32 v145, v145, v8\n"
        "v_exp_f32 v145, v145\n"
        "v_sub_f32 v146, v146, v8\n"
        "buffer_load_dwordx4 v[28:31], v0, s[40:43], s65 offen offset:3072 sc1\n"
        "v_exp_f32 v146, v146\n"
        "v_sub_f32 v147, v147, v8\n"
        "v_exp_f32 v147, v147\n"
        "v_sub_f32 v156, v156, v216\n"
        "v_exp_f32 v156, v156\n"
        "v_sub_f32 v157, v157, v216\n"
        "buffer_load_dwordx4 v[32:35], v0, s[40:43], s66 offen offset:0 sc1\n"
        "v_exp_f32 v157, v157\n"
        "v_sub_f32 v158, v158, v216\n"
        "v_exp_f32 v158, v158\n"
        "v_sub_f32 v159, v159, v216\n"
        "v_exp_f32 v159, v159\n"
        "v_add_f32 v216, v144, v145\n"
        "buffer_load_dwordx4 v[36:39], v0, s[40:43], s66 offen offset:1024 sc1\n"
        "v_add_f32 v217, v146, v147\n"
        "v_add_f32 v216, v216, v156\n"
        "v_add_f32 v217, v217, v157\n"
        "v_add_f32 v216, v216, v158\n"
        "v_add_f32 v217, v217, v159\n"
        "v_add_f32 v8, v216, v217\n"
        "buffer_load_dwordx4 v[40:43], v0, s[40:43], s66 offen offset:2048 sc1\n"
        "v_add_f32 v216, v216, v217\n"
        "s_nop 1\n"
        "v_permlane16_swap_b32 v8, v216\n"
        "v_add_f32 v217, v8, v216\n"
        "v_add_f32 v216, v8, v216\n"
        "s_nop 1\n"
        "buffer_load_dwordx4 v[44:47], v0, s[40:43], s66 offen offset:3072 sc1\n"
        "v_permlane32_swap_b32 v216, v217\n"
        "v_add_f32 v8, v216, v217\n"
        "v_rcp_f32 v8, v8\n"
        "s_nop 0\n"
        "v_mul_f32 v156, v156, v8\n"
        "v_mul_f32 v157, v157, v8\n"
        "v_mul_f32 v158, v158, v8\n"
        "v_mul_f32 v159, v159, v8\n"
        "v_cvt_pk_bf16_f32 v174, v156, v157\n"
        "v_cvt_pk_bf16_f32 v175, v158, v159\n"
        "v_mul_f32 v144, v144, v8\n"
        "v_mul_f32 v145, v145, v8\n"
        "v_cvt_pk_bf16_f32 v164, v144, v145\n"
        "v_mul_f32 v146, v146, v8\n"
        "v_mul_f32 v147, v147, v8\n"
        "v_cvt_pk_bf16_f32 v165, v146, v147\n"
        "buffer_load_dword v5, v1, s[40:43], s71 offen sc1\n"
        "s_add_u32 s65, s69, s58\n"
        "s_add_u32 s66, s65, 4096\n"
        "s_waitcnt vmcnt(17)\n"
        "v_mfma_f32_16x16x32_bf16 v[184:187], v[128:131], v[174:177], 0\n"
        "v_max3_f32 v9, v9, v160, v161\n"
        "v_max3_f32 v220, v9, v162, v163\n"
        "v_max3_f32 v221, v9, v162, v163\n"
        "v_mul_f32 v160, s75, v160\n"
        "v_mul_f32 v161, s75, v161\n"
        "v_mfma_f32_16x16x32_bf16 v[188:191], v[128:131], v[172:175], 0\n"
        "v_permlane16_swap_b32 v220, v221\n"
        "v_max_f32 v9, v220, v221\n"
        "v_max_f32 v220, v220, v221\n"
        "v_mul_f32 v162, s75, v162\n"
        "v_mul_f32 v163, s75, v163\n"
        "v_mfma_f32_16x16x32_bf16 v[184:187], v[80:83], v[164:167], v[184:187]\n"
        "v_permlane32_swap_b32 v9, v220\n"
        "v_max_f32 v9, v9, v220\n"
        "v_mul_f32 v9, s75, v9\n"
        "v_cndmask_b32 v220, v9, -v10, s[84:85]\n"
        "v_sub_f32 v148, v148, v9\n"
        "v_mfma_f32_16x16x32_bf16 v[188:191], v[84:87], v[164:167], v[188:191]\n"
        "v_exp_f32 v148, v148\n"
        "v_sub_f32 v149, v149, v9\n"
        "v_exp_f32 v149, v149\n"
        "v_sub_f32 v150, v150, v9\n"
        "v_mfma_f32_16x16x32_bf16 v[192:195], v[132:135], v[174:177], 0\n"
        "v_exp_f32 v150, v150\n"
        "v_sub_f32 v151, v151, v9\n"
        "v_exp_f32 v151, v151\n"
        "v_sub_f32 v152, v152, v9\n"
        "v_mfma_f32_16x16x32_bf16 v[196:199], v[132:135], v[172:175], 0\n"
        "v_cvt_pk_bf16_f32 v184, v184, v185\n"
        "v_cvt_pk_bf16_f32 v185, v186, v187\n"
        "v_cvt_pk_bf16_f32 v186, v188, v189\n"
        "v_cvt_pk_bf16_f32 v187, v190, v191\n"
        "buffer_store_dwordx4 v[184:187], v0, s[40:43], s65 offen offset:0 sc1\n"
        "v_mfma_f32_16x16x32_bf16 v[192:195], v[88:91], v[164:167], v[192:195]\n"
        "v_exp_f32 v152, v152\n"
        "v_sub_f32 v153, v153, v9\n"
        "v_exp_f32 v153, v153\n"
        "v_sub_f32 v154, v154, v9\n"
        "v_mfma_f32_16x16x32_bf16 v[196:199], v[92:95], v[164:167], v[196:199]\n"
        "v_exp_f32 v154, v154\n"
        "v_sub_f32 v155, v155, v9\n"
        "v_exp_f32 v155, v155\n"
        "v_sub_f32 v160, v160, v220\n"
        "v_mfma_f32_16x16x32_bf16 v[200:203], v[136:139], v[174:177], 0\n"
        "v_exp_f32 v160, v160\n"
        "v_sub_f32 v161, v161, v220\n"
        "v_exp_f32 v161, v161\n"
        "v_sub_f32 v162, v162, v220\n"
        "v_mfma_f32_16x16x32_bf16 v[204:207], v[136:139], v[172:175], 0\n"
        "v_cvt_pk_bf16_f32 v192, v192, v193\n"
        "v_cvt_pk_bf16_f32 v193, v194, v195\n"
        "v_cvt_pk_bf16_f32 v194, v196, v197\n"
        "v_cvt_pk_bf16_f32 v195, v198, v199\n"
        "buffer_store_dwordx4 v[192:195], v0, s[40:43], s65 offen offset:1024 sc1\n"
        "v_mfma_f32_16x16x32_bf16 v[200:203], v[96:99], v[164:167], v[200:203]\n"
        "v_exp_f32 v162, v162\n"
        "v_sub_f32 v163, v163, v220\n"
        "v_exp_f32 v163, v163\n"
        "v_add_f32 v220, v148, v149\n"
        "v_mfma_f32_16x16x32_bf16 v[204:207], v[100:103], v[164:167], v[204:207]\n"
        "v_add_f32 v221, v150, v151\n"
        "v_add_f32 v220, v220, v152\n"
        "v_add_f32 v221, v221, v153\n"
        "v_add_f32 v220, v220, v154\n"
        "v_add_f32 v221, v221, v155\n"
        "v_add_f32 v220, v220, v160\n"
        "v_mfma_f32_16x16x32_bf16 v[208:211], v[140:143], v[174:177], 0\n"
        "v_add_f32 v221, v221, v161\n"
        "v_add_f32 v220, v220, v162\n"
        "v_add_f32 v221, v221, v163\n"
        "v_add_f32 v9, v220, v221\n"
        "v_add_f32 v220, v220, v221\n"
        "v_mfma_f32_16x16x32_bf16 v[212:215], v[140:143], v[172:175], 0\n"
        "v_cvt_pk_bf16_f32 v200, v200, v201\n"
        "v_cvt_pk_bf16_f32 v201, v202, v203\n"
        "v_cvt_pk_bf16_f32 v202, v204, v205\n"
        "v_cvt_pk_bf16_f32 v203, v206, v207\n"
        "buffer_store_dwordx4 v[200:203], v0, s[40:43], s65 offen offset:2048 sc1\n"
        "v_mfma_f32_16x16x32_bf16 v[208:211], v[104:107], v[164:167], v[208:211]\n"
        "s_nop 1\n"
        "v_permlane16_swap_b32 v9, v220\n"
        "v_add_f32 v221, v9, v220\n"
        "v_add_f32 v220, v9, v220\n"
        "v_mfma_f32_16x16x32_bf16 v[212:215], v[108:111], v[164:167], v[212:215]\n"
        "s_nop 1\n"
        "v_permlane32_swap_b32 v220, v221\n"
        "v_add_f32 v9, v220, v221\n"
        "v_rcp_f32 v9, v9\n"
        "s_nop 0\n"
        "v_mul_f32 v160, v160, v9\n"
        "v_mul_f32 v161, v161, v9\n"
        "v_mul_f32 v162, v162, v9\n"
        "v_mul_f32 v163, v163, v9\n"
        "v_cvt_pk_bf16_f32 v180, v160, v161\n"
        "v_cvt_pk_bf16_f32 v181, v162, v163\n"
        "v_mul_f32 v148, v148, v9\n"
        "v_mul_f32 v149, v149, v9\n"
        "v_cvt_pk_bf16_f32 v168, v148, v149\n"
        "v_mul_f32 v150, v150, v9\n"
        "v_mul_f32 v151, v151, v9\n"
        "v_cvt_pk_bf16_f32 v169, v150, v151\n"
        "v_mul_f32 v152, v152, v9\n"
        "v_mul_f32 v153, v153, v9\n"
        "v_cvt_pk_bf16_f32 v170, v152, v153\n"
        "v_mul_f32 v154, v154, v9\n"
        "v_mul_f32 v155, v155, v9\n"
        "v_cvt_pk_bf16_f32 v171, v154, v155\n"
        "v_cvt_pk_bf16_f32 v208, v208, v209\n"
        "v_cvt_pk_bf16_f32 v209, v210, v211\n"
        "v_cvt_pk_bf16_f32 v210, v212, v213\n"
        "v_cvt_pk_bf16_f32 v211, v214, v215\n"
        "buffer_store_dwordx4 v[208:211], v0, s[40:43], s65 offen offset:3072 sc1\n"
        "v_mfma_f32_16x16x32_bf16 v[184:187], v[128:131], v[180:183], 0\n"
        "v_mfma_f32_16x16x32_bf16 v[188:191], v[128:131], v[178:181], 0\n"
        "v_mfma_f32_16x16x32_bf16 v[184:187], v[80:83], v[168:171], v[184:187]\n"
        "v_mfma_f32_16x16x32_bf16 v[188:191], v[84:87], v[168:171], v[188:191]\n"
        "v_mfma_f32_16x16x32_bf16 v[192:195], v[132:135], v[180:183], 0\n"
        "v_mfma_f32_16x16x32_bf16 v[196:199], v[132:135], v[178:181], 0\n"
        "v_cvt_pk_bf16_f32 v184, v184, v185\n"
        "v_cvt_pk_bf16_f32 v185, v186, v187\n"
        "v_cvt_pk_bf16_f32 v186, v188, v189\n"
        "v_mfma_f32_16x16x32_bf16 v[192:195], v[88:91], v[168:171], v[192:195]\n"
        "v_cvt_pk_bf16_f32 v187, v190, v191\n"
        "buffer_store_dwordx4 v[184:187], v0, s[40:43], s66 offen offset:0 sc1\n"
        "v_mfma_f32_16x16x32_bf16 v[196:199], v[92:95], v[168:171], v[196:199]\n"
        "v_mfma_f32_16x16x32_bf16 v[200:203], v[136:139], v[180:183], 0\n"
        "v_mfma_f32_16x16x32_bf16 v[204:207], v[136:139], v[178:181], 0\n"
        "v_cvt_pk_bf16_f32 v192, v192, v193\n"
        "v_cvt_pk_bf16_f32 v193, v194, v195\n"
        "v_cvt_pk_bf16_f32 v194, v196, v197\n"
        "v_mfma_f32_16x16x32_bf16 v[200:203], v[96:99], v[168:171], v[200:203]\n"
        "v_cvt_pk_bf16_f32 v195, v198, v199\n"
        "buffer_store_dwordx4 v[192:195], v0, s[40:43], s66 offen offset:1024 sc1\n"
        "v_mfma_f32_16x16x32_bf16 v[204:207], v[100:103], v[168:171], v[204:207]\n"
        "v_mfma_f32_16x16x32_bf16 v[208:211], v[140:143], v[180:183], 0\n"
        "v_mfma_f32_16x16x32_bf16 v[212:215], v[140:143], v[178:181], 0\n"
        "v_cvt_pk_bf16_f32 v200, v200, v201\n"
        "v_cvt_pk_bf16_f32 v201, v202, v203\n"
        "v_cvt_pk_bf16_f32 v202, v204, v205\n"
        "v_mfma_f32_16x16x32_bf16 v[208:211], v[104:107], v[168:171], v[208:211]\n"
        "v_cvt_pk_bf16_f32 v203, v206, v207\n"
        "buffer_store_dwordx4 v[200:203], v0, s[40:43], s66 offen offset:2048 sc1\n"
        "v_mfma_f32_16x16x32_bf16 v[212:215], v[108:111], v[168:171], v[212:215]\n"
        "s_waitcnt vmcnt(7)\n"
        "v_min_i32_dpp v5, v5, v5 quad_perm:[1,0,3,2] row_mask:0xf bank_mask:0xf\n"
        "s_nop 1\n"
        "v_min_i32_dpp v5, v5, v5 quad_perm:[2,3,0,1] row_mask:0xf bank_mask:0xf\n"
        "s_nop 0\n"
        "v_readlane_b32 s73, v5, 0\n"
        "v_readlane_b32 s74, v5, 4\n"
        "s_nop 0\n"
        "s_nop 0\n"
        "v_cvt_pk_bf16_f32 v208, v208, v209\n"
        "v_cvt_pk_bf16_f32 v209, v210, v211\n"
        "v_cvt_pk_bf16_f32 v210, v212, v213\n"
        "v_cvt_pk_bf16_f32 v211, v214, v215\n"
        "buffer_store_dwordx4 v[208:211], v0, s[40:43], s66 offen offset:3072 sc1\n"
        "s_lshl_b32 s65, s68, 1\n"
        "v_mov_b32 v6, s65\n"
        "s_waitcnt vmcnt(4)\n"
        "buffer_store_dword v6, v2, s[40:43], s60 offen sc1\n"
        "s_add_u32 s65, s70, s58\n"
        "s_add_u32 s65, s65, 1048576\n"
        "s_add_u32 s66, s67, 1\n"
        "s_cmp_ge_i32 s73, s66\n"
        "s_cselect_b32 s65, s65, 241172480\n"
        "s_cselect_b32 s66, 2, 0\n"
        "s_or_b32 s72, s72, s66\n"
        "s_add_u32 s66, s65, 4096\n"
        "buffer_load_dwordx4 v[80:83], v0, s[40:43], s65 offen offset:0 sc1\n"
        "buffer_load_dwordx4 v[84:87], v0, s[40:43], s65 offen offset:1024 sc1\n"
        "buffer_load_dwordx4 v[88:91], v0, s[40:43], s65 offen offset:2048 sc1\n"
        "buffer_load_dwordx4 v[92:95], v0, s[40:43], s65 offen offset:3072 sc1\n"
        "buffer_load_dwordx4 v[96:99], v0, s[40:43], s66 offen offset:0 sc1\n"
        "buffer_load_dwordx4 v[100:103], v0, s[40:43], s66 offen offset:1024 sc1\n"
        "buffer_load_dwordx4 v[104:107], v0, s[40:43], s66 offen offset:2048 sc1\n"
        "buffer_load_dwordx4 v[108:111], v0, s[40:43], s66 offen offset:3072 sc1\n"
        "v_add_u32 v6, 1, v6\n"
        "s_waitcnt vmcnt(9)\n"
        "buffer_store_dword v6, v2, s[40:43], s60 offen sc1\n"
        "s_add_u32 s68, s68, 1\n"
        "s_cmp_lt_u32 s68, 64\n"
        "s_cbranch_scc1 .Lat_top_%=\n"
        "s_cmp_eq_u32 s61, 0\n"
        "s_cbranch_scc0 .Lat_bye_%=\n"
        "s_and_b32 s66, s57, 7\n"
        "s_lshl_b32 s66, s66, 1\n"
        "s_add_u32 s66, s66, s50\n"
        "s_lshl_b32 s66, s66, 7\n"
        "s_add_u32 s66, s66, 51456\n"
        ".Lat_bye_%=:\n"
        "s_endpgm\n"
        ".Lat_end_%=:\n"
        :: "s"(p.ws), "s"(pair(x4, pr)), "s"(pair(PAGE + h * 16384, PAGE + spread(A_KM) + (h >> 1) * 16384 + (h & 1) * 4096)), "s"(pair(0, w)),
           "v"(pair(lane * 16, lane < 8 ? flag(((lane & 4) ? F_CH : F_S1 + 128) + 4 * h + (lane & 3)) : OOB)), "v"(pair(lane < 3 ? flag(F_AT + 3 * h + lane) : OOB, (g * 8 + (i & 7)) * 16)),
           "v"(pair((g & 1) * 256 + i * 16, 4 * g - i))
        : "v0", "v1", "v2", "v3", "v4", "v5", "v6", "v7", "v8", "v9", "v10", "v11", "v12", "v13", "v14", "v15", "v16", "v17", "v18", "v19", "v20", "v21", "v22", "v23", "v24", "v25", "v26", "v27", "v28", "v29", "v30", "v31", "v32", "v33", "v34", "v35", "v36", "v37", "v38", "v39", "v40", "v41", "v42", "v43", "v44", "v45", "v46", "v47", "v48", "v49", "v50", "v51", "v52", "v53", "v54", "v55", "v56", "v57", "v58", "v59", "v60", "v61", "v62", "v63", "v64", "v65", "v66", "v67", "v68", "v69", "v70", "v71", "v72", "v73", "v74", "v75", "v76", "v77", "v78", "v79", "v255", "a255", "s30", "s31", "s32", "s33", "s34", "s35", "s36", "s37", "s38", "s39", "s40", "s41", "s42", "s43", "s44", "s45", "s46", "s47", "s48", "s49", "s50", "s51", "s52", "s53", "s54", "s55", "s56", "s57", "s58", "s59", "s60", "s61", "s62", "s63", "s64", "s65", "s66", "s67", "s68", "s69", "s70", "s71", "s72", "s73", "s74", "s75", "s76", "s77", "s78", "s79", "s80", "s81", "s82", "s83", "s84", "s85", "s86", "s87", "s88", "s89", "s90", "s91", "s92", "s93", "s94", "s95", "s96", "s97", "s98", "s99", "vcc", "scc", "memory");
    __builtin_unreachable();
}

template <bool R5>
__device__ __forceinline__ void role_red(const Params& p, int a, int x4, int pr, u64 epoch) {
    const unsigned lane = threadIdx.x & 63, i = lane & 15, g = lane >> 4;
    const int w = rfl(threadIdx.x >> 6);
    if (w == 3) return;
    unsigned to[3], po[3], one[4];
#pragma unroll
    for (int n = 0; n < 3; ++n) { const unsigned b = 3 * w + n; to[n] = b < 8 ? a * 16384 + b * 1024 : 65536 + a * 1024; po[n] = part_off(a, 0, 8 * b); }
#pragma unroll
    for (int e = 0; e < 4; ++e) one[e] = ((i & 7) == 2 * e) ? 0x3f80u : (((i & 7) == 2 * e + 1) ? 0x3f800000u : 0u);
    const u64 o0 = pair(one[0], one[1]), o1 = pair(one[2], one[3]);
    const bool lo = i < 8, r0 = g == 2 * (i >> 3), r1 = g == 1 + 2 * (i >> 3);
    const unsigned fin = lane < 16 ? flag((R5 ? F_F2 : F_OP) + a * 64 + w * 16 + lane) : OOB;
    const unsigned fout = (w == 0 && lane < (R5 ? 64 : 48)) ? flag((R5 ? F_XN : F_R3) + 8 * (lane >> 1) + 2 * a + (lane & 1)) : OOB;
#define RED_ARGS :: "s"(p.ws), "s"(p.x), "s"(p.out), "s"(pair(x4 | (a >> 1) << 8, pr)), "s"(pair(to[0], to[1])), "s"(pair(to[2], po[0])), "s"(pair(po[1], po[2])), \
        "v"(pair(lane * 16, fin)), "v"(pair(fout, g * 16384 + i * 16)), "v"(pair(g * (w == 2 ? 1024 : 16384) + i * 16, (8 * a + (lane & 7)) * 1152 + w * 384 + (lane >> 3) * 16)), \
        "v"(pair(w * 64 + (lane & 7) * 8, (lane & 7) * 8)), "v"(lo ? o0 : 0), "v"(lo ? o1 : 0), "v"(lo ? 0 : o0), "v"(lo ? 0 : o1), \
        "v"(r0 ? o0 : 0), "v"(r0 ? o1 : 0), "v"(r1 ? o0 : 0), "v"(r1 ? o1 : 0), "v"(epoch + 8), "v"(pair(a == 0 && threadIdx.x == 0 ? 0u : OOB, a == 3 && threadIdx.x < 18 ? (G_DONE + threadIdx.x) * 128 : OOB))
    if constexpr (R5) {
        asm volatile(
        "s_nop 4\n"
        "s_mov_b64 s[40:41], %0\n"
        "s_and_b32 s41, s41, 0xffff\n"
        "s_mov_b32 s42, 236978176\n"
        "s_mov_b32 s43, 0x20000\n"
        "s_mov_b64 s[44:45], %1\n"
        "s_and_b32 s45, s45, 0xffff\n"
        "s_mov_b32 s46, 2359296\n"
        "s_mov_b32 s47, 0x20000\n"
        "s_mov_b64 s[56:57], %3\n"
        "s_mov_b64 s[58:59], %4\n"
        "s_mov_b64 s[60:61], %5\n"
        "s_mov_b64 s[62:63], %6\n"
        "v_mov_b64 v[0:1], %7\n"
        "v_mov_b64 v[2:3], %8\n"
        "v_mov_b64 v[4:5], %9\n"
        "v_mov_b64 v[6:7], %10\n"
        "v_mov_b64 v[8:9], %11\n"
        "v_mov_b64 v[10:11], %12\n"
        "v_mov_b64 v[12:13], %13\n"
        "v_mov_b64 v[14:15], %14\n"
        "v_mov_b64 v[16:17], %15\n"
        "v_mov_b64 v[18:19], %16\n"
        "v_mov_b64 v[20:21], %17\n"
        "v_mov_b64 v[22:23], %18\n"
        "v_mov_b64 v[24:25], %19\n"
        "v_mov_b64 v[26:27], %20\n"
        "v_mov_b64 v[200:201], v[24:25]\n"
        "v_mov_b64 v[202:203], v[26:27]\n"
        "s_lshr_b32 s74, s56, 8\n"
        "s_and_b32 s56, s56, 1\n"
        "s_mov_b32 s68, 0\n"
        "s_mul_i32 s65, s68, 36864\n"
        "buffer_load_dwordx4 v[80:83], v5, s[44:47], s65 offen offset:0\n"
        "buffer_load_dwordx4 v[84:87], v5, s[44:47], s65 offen offset:128\n"
        "buffer_load_dwordx4 v[88:91], v5, s[44:47], s65 offen offset:256\n"
        "s_getpc_b64 s[76:77]\n"
        ".Lr5_pc_%=:\n"
        "s_add_u32 s76, s76, .Lr5_code_%=-.Lr5_pc_%=\n"
        "s_addc_u32 s77, s77, 0\n"
        "s_and_b32 s77, s77, 0xffff\n"
        "s_and_b32 s76, s76, -4\n"
        "s_mov_b32 s78, .Lr5_end_%=-.Lr5_code_%=\n"
        "s_mov_b32 s79, 0x20000\n"
        "s_mov_b32 s65, 0\n"
        "buffer_load_dwordx4 v[32:35], v0, s[76:79], s65 offen\n"
        "s_add_u32 s65, s65, 1024\n"
        "buffer_load_dwordx4 v[36:39], v0, s[76:79], s65 offen\n"
        "s_add_u32 s65, s65, 1024\n"
        "buffer_load_dwordx4 v[40:43], v0, s[76:79], s65 offen\n"
        "s_add_u32 s65, s65, 1024\n"
        "buffer_load_dwordx4 v[44:47], v0, s[76:79], s65 offen\n"
        "s_add_u32 s65, s65, 1024\n"
        "buffer_load_dwordx4 v[48:51], v0, s[76:79], s65 offen\n"
        "s_add_u32 s65, s65, 1024\n"
        "v_lshrrev_b32 v26, 4, v0\n"
        "s_mov_b32 s50, s56\n"
        "v_lshlrev_b32 v28, 7, v26\n"
        "s_sub_u32 s66, 0, s50\n"
        "s_mov_b32 s52, 32768\n"
        "s_mov_b32 s64, 0\n"
        ".Lr5_pg0_top_%=:\n"
        "buffer_load_dword v27, v28, s[40:43], s52 offen sc1\n"
        "s_waitcnt vmcnt(0)\n"
        "v_cmp_gt_i32 vcc, 0, v27\n"
        "s_cbranch_vccz .Lr5_pg0_ok_%=\n"
        "s_add_u32 s64, s64, 1\n"
        "s_and_b32 s65, s64, 255\n"
        "s_cbranch_scc1 .Lr5_pg0_go_%=\n"
        "s_mov_b32 s65, 49152\n"
        "s_cmp_lt_u32 s64, 1048576\n"
        "s_cbranch_scc1 .Lr5_pg0_chk_%=\n"
        "v_mov_b32 v27, 1\n"
        "buffer_store_dword v27, off, s[40:43], s65 sc1\n"
        "s_waitcnt vmcnt(0)\n"
        ".Lr5_pg0_chk_%=:\n"
        "buffer_load_dword v27, off, s[40:43], s65 sc1\n"
        "s_waitcnt vmcnt(0)\n"
        "v_readfirstlane_b32 s65, v27\n"
        "s_nop 0\n"
        "s_cmp_lg_u32 s65, 1\n"
        "s_cbranch_scc1 .Lr5_pg0_go_%=\n"
        "s_endpgm\n"
        ".Lr5_pg0_go_%=:\n"
        "s_sleep 2\n"
        "s_branch .Lr5_pg0_top_%=\n"
        ".Lr5_pg0_ok_%=:\n"
        "v_cmp_eq_u32 s[52:53], 1, v27\n"
        "s_nop 1\n"
        "s_mov_b32 s54, 40960\n"
        "s_mov_b32 s64, 0\n"
        ".Lr5_pg1_top_%=:\n"
        "buffer_load_dword v27, v28, s[40:43], s54 offen sc1\n"
        "s_waitcnt vmcnt(0)\n"
        "v_cmp_gt_i32 vcc, 0, v27\n"
        "s_cbranch_vccz .Lr5_pg1_ok_%=\n"
        "s_add_u32 s64, s64, 1\n"
        "s_and_b32 s65, s64, 255\n"
        "s_cbranch_scc1 .Lr5_pg1_go_%=\n"
        "s_mov_b32 s65, 49152\n"
        "s_cmp_lt_u32 s64, 1048576\n"
        "s_cbranch_scc1 .Lr5_pg1_chk_%=\n"
        "v_mov_b32 v27, 1\n"
        "buffer_store_dword v27, off, s[40:43], s65 sc1\n"
        "s_waitcnt vmcnt(0)\n"
        ".Lr5_pg1_chk_%=:\n"
        "buffer_load_dword v27, off, s[40:43], s65 sc1\n"
        "s_waitcnt vmcnt(0)\n"
        "v_readfirstlane_b32 s65, v27\n"
        "s_nop 0\n"
        "s_cmp_lg_u32 s65, 1\n"
        "s_cbranch_scc1 .Lr5_pg1_go_%=\n"
        "s_endpgm\n"
        ".Lr5_pg1_go_%=:\n"
        "s_sleep 2\n"
        "s_branch .Lr5_pg1_top_%=\n"
        ".Lr5_pg1_ok_%=:\n"
        "v_cmp_eq_u32 s[54:55], 1, v27\n"
        "s_nop 1\n"
        "s_mov_b32 vcc_lo, 0x55555555\n"
        "s_mov_b32 vcc_hi, 0x55555555\n"
        "s_xor_b64 vcc, vcc, s[52:53]\n"
        "s_bcnt1_i32_b64 s65, vcc\n"
        "s_sub_u32 s64, s65, 23\n"
        "s_cmp_lt_u32 s64, 19\n"
        "s_cbranch_scc1 .Lr5_rawmap_%=\n"
        "s_cmp_lt_u32 s65, 32\n"
        "s_cselect_b32 s65, 0, -1\n"
        "s_xor_b32 s52, s65, 0x55555555\n"
        "s_xor_b32 s53, s65, 0x55555555\n"
        "s_xor_b32 s54, s65, 0x55555555\n"
        "s_xor_b32 s55, s65, 0xaaaaaaaa\n"
        ".Lr5_rawmap_%=:\n"
        "s_xor_b32 s52, s52, s66\n"
        "s_xor_b32 s53, s53, s66\n"
        "s_xor_b32 s54, s54, s66\n"
        "s_xor_b32 s55, s55, s66\n"
        "s_and_b32 s69, s55, 1\n"
        "s_lshl_b32 s69, s69, 13\n"
        "s_xor_b32 s70, s69, 8192\n"
        "s_cmp_eq_u32 s50, 0\n"
        "s_cbranch_scc0 .Lr5_l1_%=\n"
        ".Lr5_code_%=:\n"
        ".Lr5_l0_%=:\n"
        "s_bitcmp1_b64 s[52:53], s68\n"
        "s_cselect_b32 s65, 8192, 0\n"
        "s_lshl_b32 s71, s68, 21\n"
        "s_add_u32 s71, s71, s65\n"
        "s_add_u32 s71, s71, 2097152\n"
        "s_lshr_b32 s65, s68, 2\n"
        "s_add_u32 s65, s65, 1\n"
        "s_bitcmp1_b32 s55, s65\n"
        "s_cselect_b32 s65, 8192, 0\n"
        "s_lshl_b32 s72, s68, 19\n"
        "s_add_u32 s72, s72, s65\n"
        "s_add_u32 s72, s72, 203423744\n"
        "s_add_u32 s76, s72, s61\n"
        "s_add_u32 s79, s71, s58\n"
        "s_add_u32 s79, s79, 1998848\n"
        "s_add_u32 s77, s72, s62\n"
        "s_add_u32 s80, s71, s59\n"
        "s_add_u32 s80, s80, 1998848\n"
        "s_add_u32 s78, s72, s63\n"
        "s_add_u32 s81, s71, s60\n"
        "s_add_u32 s81, s81, 1998848\n"
        "s_lshl_b32 s73, s68, 1\n"
        "s_add_u32 s73, s73, s74\n"
        "s_mov_b32 s64, 0\n"
        ".Lr5_l0w_top_%=:\n"
        "buffer_load_dword v24, v1, s[40:43], s69 offen sc1\n"
        "s_waitcnt vmcnt(0)\n"
        "v_cmp_gt_i32 vcc, s73, v24\n"
        "s_nop 0\n"
        "s_and_b64 vcc, vcc, 0xffff\n"
        "s_cbranch_vccz .Lr5_l0w_ok_%=\n"
        "s_add_u32 s64, s64, 1\n"
        "s_and_b32 s65, s64, 255\n"
        "s_cbranch_scc1 .Lr5_l0w_go_%=\n"
        "s_mov_b32 s65, 49152\n"
        "s_cmp_lt_u32 s64, 1048576\n"
        "s_cbranch_scc1 .Lr5_l0w_chk_%=\n"
        "v_mov_b32 v24, 1\n"
        "buffer_store_dword v24, off, s[40:43], s65 sc1\n"
        "s_waitcnt vmcnt(0)\n"
        ".Lr5_l0w_chk_%=:\n"
        "buffer_load_dword v24, off, s[40:43], s65 sc1\n"
        "s_waitcnt vmcnt(0)\n"
        "v_readfirstlane_b32 s65, v24\n"
        "s_nop 0\n"
        "s_cmp_lg_u32 s65, 1\n"
        "s_cbranch_scc1 .Lr5_l0w_go_%=\n"
        "s_endpgm\n"
        ".Lr5_l0w_go_%=:\n"
        "s_branch .Lr5_l0w_top_%=\n"
        ".Lr5_l0w_ok_%=:\n"
        "buffer_load_dwordx4 v[32:35], v3, s[40:43], s76 offen offset:0 sc1\n"
        "buffer_load_dwordx4 v[40:43], v3, s[40:43], s76 offen offset:512 sc1\n"
        "buffer_load_dwordx4 v[36:39], v3, s[40:43], s76 offen offset:256 sc1\n"
        "buffer_load_dwordx4 v[44:47], v3, s[40:43], s76 offen offset:768 sc1\n"
        "buffer_load_dwordx4 v[80:83], v0, s[40:43], s79 offen sc1\n"
        "buffer_load_dwordx4 v[48:51], v3, s[40:43], s77 offen offset:0 sc1\n"
        "buffer_load_dwordx4 v[56:59], v3, s[40:43], s77 offen offset:512 sc1\n"
        "buffer_load_dwordx4 v[52:55], v3, s[40:43], s77 offen offset:256 sc1\n"
        "buffer_load_dwordx4 v[60:63], v3, s[40:43], s77 offen offset:768 sc1\n"
        "buffer_load_dwordx4 v[84:87], v0, s[40:43], s80 offen sc1\n"
        "buffer_load_dwordx4 v[64:67], v4, s[40:43], s78 offen offset:0 sc1\n"
        "buffer_load_dwordx4 v[72:75], v4, s[40:43], s78 offen offset:512 sc1\n"
        "buffer_load_dwordx4 v[68:71], v4, s[40:43], s78 offen offset:256 sc1\n"
        "buffer_load_dwordx4 v[76:79], v4, s[40:43], s78 offen offset:768 sc1\n"
        "buffer_load_dwordx4 v[88:91], v0, s[40:43], s81 offen sc1\n"
        "s_xor_b32 s71, s71, 8192\n"
        "s_add_u32 s72, s71, 1835008\n"
        "s_add_u32 s71, s71, 1916928\n"
        "s_add_u32 s82, s72, s58\n"
        "s_add_u32 s85, s71, s58\n"
        "s_add_u32 s83, s72, s59\n"
        "s_add_u32 s86, s71, s59\n"
        "s_add_u32 s84, s72, s60\n"
        "s_add_u32 s87, s71, s60\n"
        "s_waitcnt vmcnt(14)\n"
        "v_mfma_f32_16x16x32_bf16 v[92:95], v[8:11], v[32:35], 0\n"
        "s_waitcnt vmcnt(13)\n"
        "v_mfma_f32_16x16x32_bf16 v[92:95], v[12:15], v[40:43], v[92:95]\n"
        "s_waitcnt vmcnt(12)\n"
        "v_mfma_f32_16x16x32_bf16 v[96:99], v[8:11], v[36:39], 0\n"
        "s_waitcnt vmcnt(11)\n"
        "v_mfma_f32_16x16x32_bf16 v[96:99], v[12:15], v[44:47], v[96:99]\n"
        "s_waitcnt vmcnt(10)\n"
        "v_mfma_f32_16x16x32_bf16 v[92:95], v[16:19], v[80:83], v[92:95]\n"
        "v_mfma_f32_16x16x32_bf16 v[96:99], v[20:23], v[80:83], v[96:99]\n"
        "s_waitcnt vmcnt(9)\n"
        "v_mfma_f32_16x16x32_bf16 v[100:103], v[8:11], v[48:51], 0\n"
        "s_waitcnt vmcnt(8)\n"
        "v_mfma_f32_16x16x32_bf16 v[100:103], v[12:15], v[56:59], v[100:103]\n"
        "s_waitcnt vmcnt(7)\n"
        "v_mfma_f32_16x16x32_bf16 v[104:107], v[8:11], v[52:55], 0\n"
        "s_waitcnt vmcnt(6)\n"
        "v_mfma_f32_16x16x32_bf16 v[104:107], v[12:15], v[60:63], v[104:107]\n"
        "s_waitcnt vmcnt(5)\n"
        "v_mfma_f32_16x16x32_bf16 v[100:103], v[16:19], v[84:87], v[100:103]\n"
        "v_mfma_f32_16x16x32_bf16 v[104:107], v[20:23], v[84:87], v[104:107]\n"
        "s_waitcnt vmcnt(4)\n"
        "v_mfma_f32_16x16x32_bf16 v[108:111], v[8:11], v[64:67], 0\n"
        "s_waitcnt vmcnt(3)\n"
        "v_mfma_f32_16x16x32_bf16 v[108:111], v[12:15], v[72:75], v[108:111]\n"
        "s_waitcnt vmcnt(2)\n"
        "v_mfma_f32_16x16x32_bf16 v[112:115], v[8:11], v[68:71], 0\n"
        "s_waitcnt vmcnt(1)\n"
        "v_mfma_f32_16x16x32_bf16 v[112:115], v[12:15], v[76:79], v[112:115]\n"
        "s_waitcnt vmcnt(0)\n"
        "v_mfma_f32_16x16x32_bf16 v[108:111], v[16:19], v[88:91], v[108:111]\n"
        "v_mfma_f32_16x16x32_bf16 v[112:115], v[20:23], v[88:91], v[112:115]\n"
        "s_add_u32 s68, s68, 1\n"
        "s_sub_u32 s68, s68, 1\n"
        "v_pk_add_f32 v[164:165], v[92:93], v[94:95]\n"
        "v_pk_add_f32 v[166:167], v[96:97], v[98:99]\n"
        "v_pk_add_f32 v[168:169], v[100:101], v[102:103]\n"
        "v_pk_add_f32 v[170:171], v[104:105], v[106:107]\n"
        "v_pk_add_f32 v[164:165], v[164:165], v[166:167]\n"
        "v_pk_add_f32 v[168:169], v[168:169], v[170:171]\n"
        "v_pk_add_f32 v[164:165], v[164:165], v[168:169]\n"
        "s_nop 0\n"
        "v_pk_add_f32 v[166:167], v[108:109], v[110:111]\n"
        "v_pk_add_f32 v[168:169], v[112:113], v[114:115]\n"
        "v_pk_add_f32 v[164:165], v[164:165], v[166:167]\n"
        "v_pk_add_f32 v[164:165], v[164:165], v[168:169]\n"
        "v_add_f32 v164, v164, v165\n"
        "v_cvt_pk_bf16_f32 v140, v92, v93\n"
        "v_cvt_pk_bf16_f32 v141, v94, v95\n"
        "v_add_f32_dpp v166, v164, v164 row_ror:8 row_mask:0xf bank_mask:0xf\n"
        "v_add_f32_dpp v167, v164, v164 row_ror:8 row_mask:0xf bank_mask:0xf\n"
        "v_cvt_pk_bf16_f32 v142, v96, v97\n"
        "v_cvt_pk_bf16_f32 v143, v98, v99\n"
        "v_permlane16_swap_b32 v166, v167\n"
        "v_add_f32 v164, v166, v167\n"
        "v_add_f32 v166, v166, v167\n"
        "v_cvt_pk_bf16_f32 v144, v100, v101\n"
        "v_cvt_pk_bf16_f32 v145, v102, v103\n"
        "v_permlane32_swap_b32 v164, v166\n"
        "v_add_f32 v164, v164, v166\n"
        "v_mul_f32 v164, 0x3baaaaab, v164\n"
        "v_pk_add_f32 v[116:117], v[92:93], v[164:165] op_sel_hi:[1,0] neg_lo:[0,1] neg_hi:[0,1]\n"
        "v_pk_add_f32 v[118:119], v[94:95], v[164:165] op_sel_hi:[1,0] neg_lo:[0,1] neg_hi:[0,1]\n"
        "v_pk_add_f32 v[120:121], v[96:97], v[164:165] op_sel_hi:[1,0] neg_lo:[0,1] neg_hi:[0,1]\n"
        "v_pk_add_f32 v[122:123], v[98:99], v[164:165] op_sel_hi:[1,0] neg_lo:[0,1] neg_hi:[0,1]\n"
        "v_pk_add_f32 v[124:125], v[100:101], v[164:165] op_sel_hi:[1,0] neg_lo:[0,1] neg_hi:[0,1]\n"
        "v_pk_add_f32 v[126:127], v[102:103], v[164:165] op_sel_hi:[1,0] neg_lo:[0,1] neg_hi:[0,1]\n"
        "v_pk_add_f32 v[128:129], v[104:105], v[164:165] op_sel_hi:[1,0] neg_lo:[0,1] neg_hi:[0,1]\n"
        "v_pk_add_f32 v[130:131], v[106:107], v[164:165] op_sel_hi:[1,0] neg_lo:[0,1] neg_hi:[0,1]\n"
        "v_pk_add_f32 v[132:133], v[108:109], v[164:165] op_sel_hi:[1,0] neg_lo:[0,1] neg_hi:[0,1]\n"
        "v_pk_add_f32 v[134:135], v[110:111], v[164:165] op_sel_hi:[1,0] neg_lo:[0,1] neg_hi:[0,1]\n"
        "v_pk_add_f32 v[136:137], v[112:113], v[164:165] op_sel_hi:[1,0] neg_lo:[0,1] neg_hi:[0,1]\n"
        "v_pk_add_f32 v[138:139], v[114:115], v[164:165] op_sel_hi:[1,0] neg_lo:[0,1] neg_hi:[0,1]\n"
        "v_pk_mul_f32 v[166:167], v[116:117], v[116:117]\n"
        "v_pk_mul_f32 v[168:169], v[118:119], v[118:119]\n"
        "v_pk_fma_f32 v[166:167], v[120:121], v[120:121], v[166:167]\n"
        "v_pk_fma_f32 v[168:169], v[122:123], v[122:123], v[168:169]\n"
        "v_pk_fma_f32 v[166:167], v[124:125], v[124:125], v[166:167]\n"
        "v_pk_fma_f32 v[168:169], v[126:127], v[126:127], v[168:169]\n"
        "v_pk_fma_f32 v[166:167], v[128:129], v[128:129], v[166:167]\n"
        "v_pk_fma_f32 v[168:169], v[130:131], v[130:131], v[168:169]\n"
        "v_pk_fma_f32 v[166:167], v[132:133], v[132:133], v[166:167]\n"
        "v_pk_fma_f32 v[168:169], v[134:135], v[134:135], v[168:169]\n"
        "v_pk_fma_f32 v[166:167], v[136:137], v[136:137], v[166:167]\n"
        "v_pk_fma_f32 v[168:169], v[138:139], v[138:139], v[168:169]\n"
        "v_pk_add_f32 v[166:167], v[166:167], v[168:169]\n"
        "v_add_f32 v165, v166, v167\n"
        "v_cvt_pk_bf16_f32 v146, v104, v105\n"
        "v_cvt_pk_bf16_f32 v147, v106, v107\n"
        "v_add_f32_dpp v166, v165, v165 row_ror:8 row_mask:0xf bank_mask:0xf\n"
        "v_add_f32_dpp v167, v165, v165 row_ror:8 row_mask:0xf bank_mask:0xf\n"
        "v_cvt_pk_bf16_f32 v148, v108, v109\n"
        "v_cvt_pk_bf16_f32 v149, v110, v111\n"
        "v_permlane16_swap_b32 v166, v167\n"
        "v_add_f32 v165, v166, v167\n"
        "v_add_f32 v166, v166, v167\n"
        "v_cvt_pk_bf16_f32 v150, v112, v113\n"
        "v_cvt_pk_bf16_f32 v151, v114, v115\n"
        "v_permlane32_swap_b32 v165, v166\n"
        "v_add_f32 v165, v165, v166\n"
        "ds_write_b64 v6, v[164:165]\n"
        "v_permlane16_swap_b32 v140, v142\n"
        "v_permlane16_swap_b32 v141, v143\n"
        "v_permlane16_swap_b32 v144, v146\n"
        "v_permlane16_swap_b32 v145, v147\n"
        "s_waitcnt lgkmcnt(0)\n"
        "s_barrier\n"
        "ds_read_b64 v[166:167], v7 offset:0\n"
        "ds_read_b64 v[168:169], v7 offset:64\n"
        "ds_read_b64 v[170:171], v7 offset:128\n"
        "v_permlane16_swap_b32 v148, v150\n"
        "v_permlane16_swap_b32 v149, v151\n"
        "buffer_store_dwordx4 v[140:143], v0, s[40:43], s85 offen sc1\n"
        "buffer_store_dwordx4 v[144:147], v0, s[40:43], s86 offen sc1\n"
        "buffer_store_dwordx4 v[148:151], v0, s[40:43], s87 offen sc1\n"
        "s_waitcnt lgkmcnt(0)\n"
        "v_add_f32 v172, v166, v168\n"
        "v_add_f32 v173, v167, v169\n"
        "v_add_f32 v172, v172, v170\n"
        "v_add_f32 v173, v173, v171\n"
        "v_mul_f32 v172, 0x3eaaaaab, v172\n"
        "v_sub_f32 v166, v166, v172\n"
        "v_sub_f32 v168, v168, v172\n"
        "v_sub_f32 v170, v170, v172\n"
        "v_mul_f32 v166, v166, v166\n"
        "v_fmac_f32 v166, v168, v168\n"
        "v_fmac_f32 v166, v170, v170\n"
        "v_fmamk_f32 v173, v166, 0x43400000, v173\n"
        "v_mul_f32 v173, 0x3ae38e39, v173\n"
        "v_cvt_pk_bf16_f32 v172, v172, v173\n"
        "v_and_b32 v173, 0xffff0000, v172\n"
        "v_lshlrev_b32 v172, 16, v172\n"
        "v_add_f32 v173, 0x3727c5ac, v173\n"
        "v_cvt_pk_bf16_f32 v173, v173, v173\n"
        "v_lshlrev_b32 v173, 16, v173\n"
        "v_sqrt_f32 v173, v173\n"
        "v_sub_f32 v172, v164, v172\n"
        "v_cvt_pk_bf16_f32 v173, v173, v173\n"
        "v_lshlrev_b32 v173, 16, v173\n"
        "v_rcp_f32 v173, v173\n"
        "v_mov_b32 v25, s68\n"
        "v_mul_f32 v172, v172, v173\n"
        "v_pk_fma_f32 v[116:117], v[116:117], v[172:173], v[172:173] op_sel:[0,1,0] op_sel_hi:[1,1,0]\n"
        "v_pk_fma_f32 v[118:119], v[118:119], v[172:173], v[172:173] op_sel:[0,1,0] op_sel_hi:[1,1,0]\n"
        "v_pk_fma_f32 v[120:121], v[120:121], v[172:173], v[172:173] op_sel:[0,1,0] op_sel_hi:[1,1,0]\n"
        "v_pk_fma_f32 v[122:123], v[122:123], v[172:173], v[172:173] op_sel:[0,1,0] op_sel_hi:[1,1,0]\n"
        "v_cvt_pk_bf16_f32 v152, v116, v117\n"
        "v_cvt_pk_bf16_f32 v153, v118, v119\n"
        "v_cvt_pk_bf16_f32 v154, v120, v121\n"
        "v_cvt_pk_bf16_f32 v155, v122, v123\n"
        "s_nop 0\n"
        "v_permlane16_swap_b32 v152, v154\n"
        "v_permlane16_swap_b32 v153, v155\n"
        "buffer_store_dwordx4 v[152:155], v0, s[40:43], s82 offen sc1\n"
        "v_pk_fma_f32 v[124:125], v[124:125], v[172:173], v[172:173] op_sel:[0,1,0] op_sel_hi:[1,1,0]\n"
        "v_pk_fma_f32 v[126:127], v[126:127], v[172:173], v[172:173] op_sel:[0,1,0] op_sel_hi:[1,1,0]\n"
        "v_pk_fma_f32 v[128:129], v[128:129], v[172:173], v[172:173] op_sel:[0,1,0] op_sel_hi:[1,1,0]\n"
        "v_pk_fma_f32 v[130:131], v[130:131], v[172:173], v[172:173] op_sel:[0,1,0] op_sel_hi:[1,1,0]\n"
        "v_cvt_pk_bf16_f32 v156, v124, v125\n"
        "v_cvt_pk_bf16_f32 v157, v126, v127\n"
        "v_cvt_pk_bf16_f32 v158, v128, v129\n"
        "v_cvt_pk_bf16_f32 v159, v130, v131\n"
        "s_nop 0\n"
        "v_permlane16_swap_b32 v156, v158\n"
        "v_permlane16_swap_b32 v157, v159\n"
        "buffer_store_dwordx4 v[156:159], v0, s[40:43], s83 offen sc1\n"
        "v_pk_fma_f32 v[132:133], v[132:133], v[172:173], v[172:173] op_sel:[0,1,0] op_sel_hi:[1,1,0]\n"
        "v_pk_fma_f32 v[134:135], v[134:135], v[172:173], v[172:173] op_sel:[0,1,0] op_sel_hi:[1,1,0]\n"
        "v_pk_fma_f32 v[136:137], v[136:137], v[172:173], v[172:173] op_sel:[0,1,0] op_sel_hi:[1,1,0]\n"
        "v_pk_fma_f32 v[138:139], v[138:139], v[172:173], v[172:173] op_sel:[0,1,0] op_sel_hi:[1,1,0]\n"
        "v_cvt_pk_bf16_f32 v160, v132, v133\n"
        "v_cvt_pk_bf16_f32 v161, v134, v135\n"
        "v_cvt_pk_bf16_f32 v162, v136, v137\n"
        "v_cvt_pk_bf16_f32 v163, v138, v139\n"
        "s_nop 0\n"
        "v_permlane16_swap_b32 v160, v162\n"
        "v_permlane16_swap_b32 v161, v163\n"
        "buffer_store_dwordx4 v[160:163], v0, s[40:43], s84 offen sc1\n"
        "s_waitcnt vmcnt(0)\n"
        "s_barrier\n"
        "buffer_store_dword v25, v2, s[40:43], s70 offen sc1\n"
        "s_add_u32 s68, s68, 1\n"
        "s_cmp_lt_u32 s68, 64\n"
        "s_cbranch_scc1 .Lr5_l0_%=\n"
        "s_endpgm\n"
        ".Lr5_l1_%=:\n"
        "s_bitcmp1_b64 s[52:53], s68\n"
        "s_cselect_b32 s65, 8192, 0\n"
        "s_lshl_b32 s71, s68, 21\n"
        "s_add_u32 s71, s71, s65\n"
        "s_add_u32 s71, s71, 2097152\n"
        "s_xor_b32 s71, s71, 8192\n"
        "s_add_u32 s72, s71, 1835008\n"
        "s_add_u32 s71, s71, 1916928\n"
        "s_add_u32 s82, s72, s58\n"
        "s_add_u32 s85, s71, s58\n"
        "s_add_u32 s83, s72, s59\n"
        "s_add_u32 s86, s71, s59\n"
        "s_add_u32 s84, s72, s60\n"
        "s_add_u32 s87, s71, s60\n"
        "s_waitcnt vmcnt(0)\n"
        "v_mfma_f32_16x16x32_bf16 v[92:95], v[16:19], v[80:83], 0\n"
        "v_mfma_f32_16x16x32_bf16 v[96:99], v[20:23], v[80:83], 0\n"
        "v_mfma_f32_16x16x32_bf16 v[100:103], v[16:19], v[84:87], 0\n"
        "v_mfma_f32_16x16x32_bf16 v[104:107], v[20:23], v[84:87], 0\n"
        "v_mfma_f32_16x16x32_bf16 v[108:111], v[16:19], v[88:91], 0\n"
        "v_mfma_f32_16x16x32_bf16 v[112:115], v[20:23], v[88:91], 0\n"
        "s_add_u32 s68, s68, 1\n"
        "s_mul_i32 s65, s68, 36864\n"
        "buffer_load_dwordx4 v[80:83], v5, s[44:47], s65 offen offset:0\n"
        "buffer_load_dwordx4 v[84:87], v5, s[44:47], s65 offen offset:128\n"
        "buffer_load_dwordx4 v[88:91], v5, s[44:47], s65 offen offset:256\n"
        "s_sub_u32 s68, s68, 1\n"
        "v_pk_add_f32 v[164:165], v[92:93], v[94:95]\n"
        "v_pk_add_f32 v[166:167], v[96:97], v[98:99]\n"
        "v_pk_add_f32 v[168:169], v[100:101], v[102:103]\n"
        "v_pk_add_f32 v[170:171], v[104:105], v[106:107]\n"
        "v_pk_add_f32 v[164:165], v[164:165], v[166:167]\n"
        "v_pk_add_f32 v[168:169], v[168:169], v[170:171]\n"
        "v_pk_add_f32 v[164:165], v[164:165], v[168:169]\n"
        "v_pk_add_f32 v[166:167], v[108:109], v[110:111]\n"
        "v_pk_add_f32 v[168:169], v[112:113], v[114:115]\n"
        "v_pk_add_f32 v[164:165], v[164:165], v[166:167]\n"
        "v_pk_add_f32 v[164:165], v[164:165], v[168:169]\n"
        "v_add_f32 v164, v164, v165\n"
        "s_nop 1\n"
        "v_add_f32_dpp v166, v164, v164 row_ror:8 row_mask:0xf bank_mask:0xf\n"
        "v_add_f32_dpp v167, v164, v164 row_ror:8 row_mask:0xf bank_mask:0xf\n"
        "s_nop 1\n"
        "v_permlane16_swap_b32 v166, v167\n"
        "v_add_f32 v164, v166, v167\n"
        "v_add_f32 v166, v166, v167\n"
        "s_nop 1\n"
        "v_permlane32_swap_b32 v164, v166\n"
        "v_add_f32 v164, v164, v166\n"
        "v_mul_f32 v164, 0x3baaaaab, v164\n"
        "v_pk_add_f32 v[116:117], v[92:93], v[164:165] op_sel_hi:[1,0] neg_lo:[0,1] neg_hi:[0,1]\n"
        "v_pk_add_f32 v[118:119], v[94:95], v[164:165] op_sel_hi:[1,0] neg_lo:[0,1] neg_hi:[0,1]\n"
        "v_pk_add_f32 v[120:121], v[96:97], v[164:165] op_sel_hi:[1,0] neg_lo:[0,1] neg_hi:[0,1]\n"
        "v_pk_add_f32 v[122:123], v[98:99], v[164:165] op_sel_hi:[1,0] neg_lo:[0,1] neg_hi:[0,1]\n"
        "v_pk_add_f32 v[124:125], v[100:101], v[164:165] op_sel_hi:[1,0] neg_lo:[0,1] neg_hi:[0,1]\n"
        "v_pk_add_f32 v[126:127], v[102:103], v[164:165] op_sel_hi:[1,0] neg_lo:[0,1] neg_hi:[0,1]\n"
        "v_pk_add_f32 v[128:129], v[104:105], v[164:165] op_sel_hi:[1,0] neg_lo:[0,1] neg_hi:[0,1]\n"
        "v_pk_add_f32 v[130:131], v[106:107], v[164:165] op_sel_hi:[1,0] neg_lo:[0,1] neg_hi:[0,1]\n"
        "v_pk_add_f32 v[132:133], v[108:109], v[164:165] op_sel_hi:[1,0] neg_lo:[0,1] neg_hi:[0,1]\n"
        "v_pk_add_f32 v[134:135], v[110:111], v[164:165] op_sel_hi:[1,0] neg_lo:[0,1] neg_hi:[0,1]\n"
        "v_pk_add_f32 v[136:137], v[112:113], v[164:165] op_sel_hi:[1,0] neg_lo:[0,1] neg_hi:[0,1]\n"
        "v_pk_add_f32 v[138:139], v[114:115], v[164:165] op_sel_hi:[1,0] neg_lo:[0,1] neg_hi:[0,1]\n"
        "v_pk_mul_f32 v[166:167], v[116:117], v[116:117]\n"
        "v_pk_mul_f32 v[168:169], v[118:119], v[118:119]\n"
        "v_pk_fma_f32 v[166:167], v[120:121], v[120:121], v[166:167]\n"
        "v_pk_fma_f32 v[168:169], v[122:123], v[122:123], v[168:169]\n"
        "v_pk_fma_f32 v[166:167], v[124:125], v[124:125], v[166:167]\n"
        "v_pk_fma_f32 v[168:169], v[126:127], v[126:127], v[168:169]\n"
        "v_pk_fma_f32 v[166:167], v[128:129], v[128:129], v[166:167]\n"
        "v_pk_fma_f32 v[168:169], v[130:131], v[130:131], v[168:169]\n"
        "v_pk_fma_f32 v[166:167], v[132:133], v[132:133], v[166:167]\n"
        "v_pk_fma_f32 v[168:169], v[134:135], v[134:135], v[168:169]\n"
        "v_pk_fma_f32 v[166:167], v[136:137], v[136:137], v[166:167]\n"
        "v_pk_fma_f32 v[168:169], v[138:139], v[138:139], v[168:169]\n"
        "v_pk_add_f32 v[166:167], v[166:167], v[168:169]\n"
        "v_add_f32 v165, v166, v167\n"
        "s_nop 1\n"
        "v_add_f32_dpp v166, v165, v165 row_ror:8 row_mask:0xf bank_mask:0xf\n"
        "v_add_f32_dpp v167, v165, v165 row_ror:8 row_mask:0xf bank_mask:0xf\n"
        "s_nop 1\n"
        "v_permlane16_swap_b32 v166, v167\n"
        "v_add_f32 v165, v166, v167\n"
        "v_add_f32 v166, v166, v167\n"
        "s_nop 1\n"
        "v_permlane32_swap_b32 v165, v166\n"
        "v_add_f32 v165, v165, v166\n"
        "ds_write_b64 v6, v[164:165]\n"
        "s_waitcnt lgkmcnt(0)\n"
        "s_barrier\n"
        "ds_read_b64 v[166:167], v7 offset:0\n"
        "ds_read_b64 v[168:169], v7 offset:64\n"
        "ds_read_b64 v[170:171], v7 offset:128\n"
        "s_waitcnt lgkmcnt(0)\n"
        "v_add_f32 v172, v166, v168\n"
        "v_add_f32 v173, v167, v169\n"
        "v_add_f32 v172, v172, v170\n"
        "v_add_f32 v173, v173, v171\n"
        "v_mul_f32 v172, 0x3eaaaaab, v172\n"
        "v_sub_f32 v166, v166, v172\n"
        "v_sub_f32 v168, v168, v172\n"
        "v_sub_f32 v170, v170, v172\n"
        "v_mul_f32 v166, v166, v166\n"
        "v_fmac_f32 v166, v168, v168\n"
        "v_fmac_f32 v166, v170, v170\n"
        "v_fmamk_f32 v173, v166, 0x43400000, v173\n"
        "v_mul_f32 v173, 0x3ae38e39, v173\n"
        "v_cvt_pk_bf16_f32 v172, v172, v173\n"
        "v_and_b32 v173, 0xffff0000, v172\n"
        "v_lshlrev_b32 v172, 16, v172\n"
        "v_add_f32 v173, 0x3727c5ac, v173\n"
        "v_cvt_pk_bf16_f32 v173, v173, v173\n"
        "v_lshlrev_b32 v173, 16, v173\n"
        "v_sqrt_f32 v173, v173\n"
        "v_sub_f32 v172, v164, v172\n"
        "v_cvt_pk_bf16_f32 v173, v173, v173\n"
        "v_lshlrev_b32 v173, 16, v173\n"
        "v_rcp_f32 v173, v173\n"
        "v_mov_b32 v25, s68\n"
        "v_mul_f32 v172, v172, v173\n"
        "v_pk_fma_f32 v[116:117], v[116:117], v[172:173], v[172:173] op_sel:[0,1,0] op_sel_hi:[1,1,0]\n"
        "v_pk_fma_f32 v[118:119], v[118:119], v[172:173], v[172:173] op_sel:[0,1,0] op_sel_hi:[1,1,0]\n"
        "v_pk_fma_f32 v[120:121], v[120:121], v[172:173], v[172:173] op_sel:[0,1,0] op_sel_hi:[1,1,0]\n"
        "v_pk_fma_f32 v[122:123], v[122:123], v[172:173], v[172:173] op_sel:[0,1,0] op_sel_hi:[1,1,0]\n"
        "v_cvt_pk_bf16_f32 v152, v116, v117\n"
        "v_cvt_pk_bf16_f32 v153, v118, v119\n"
        "v_cvt_pk_bf16_f32 v154, v120, v121\n"
        "v_cvt_pk_bf16_f32 v155, v122, v123\n"
        "s_nop 0\n"
        "v_permlane16_swap_b32 v152, v154\n"
        "v_permlane16_swap_b32 v153, v155\n"
        "buffer_store_dwordx4 v[152:155], v0, s[40:43], s82 offen sc1\n"
        "v_pk_fma_f32 v[124:125], v[124:125], v[172:173], v[172:173] op_sel:[0,1,0] op_sel_hi:[1,1,0]\n"
        "v_pk_fma_f32 v[126:127], v[126:127], v[172:173], v[172:173] op_sel:[0,1,0] op_sel_hi:[1,1,0]\n"
        "v_pk_fma_f32 v[128:129], v[128:129], v[172:173], v[172:173] op_sel:[0,1,0] op_sel_hi:[1,1,0]\n"
        "v_pk_fma_f32 v[130:131], v[130:131], v[172:173], v[172:173] op_sel:[0,1,0] op_sel_hi:[1,1,0]\n"
        "v_cvt_pk_bf16_f32 v156, v124, v125\n"
        "v_cvt_pk_bf16_f32 v157, v126, v127\n"
        "v_cvt_pk_bf16_f32 v158, v128, v129\n"
        "v_cvt_pk_bf16_f32 v159, v130, v131\n"
        "s_nop 0\n"
        "v_permlane16_swap_b32 v156, v158\n"
        "v_permlane16_swap_b32 v157, v159\n"
        "buffer_store_dwordx4 v[156:159], v0, s[40:43], s83 offen sc1\n"
        "v_pk_fma_f32 v[132:133], v[132:133], v[172:173], v[172:173] op_sel:[0,1,0] op_sel_hi:[1,1,0]\n"
        "v_pk_fma_f32 v[134:135], v[134:135], v[172:173], v[172:173] op_sel:[0,1,0] op_sel_hi:[1,1,0]\n"
        "v_pk_fma_f32 v[136:137], v[136:137], v[172:173], v[172:173] op_sel:[0,1,0] op_sel_hi:[1,1,0]\n"
        "v_pk_fma_f32 v[138:139], v[138:139], v[172:173], v[172:173] op_sel:[0,1,0] op_sel_hi:[1,1,0]\n"
        "v_cvt_pk_bf16_f32 v160, v132, v133\n"
        "v_cvt_pk_bf16_f32 v161, v134, v135\n"
        "v_cvt_pk_bf16_f32 v162, v136, v137\n"
        "v_cvt_pk_bf16_f32 v163, v138, v139\n"
        "s_nop 0\n"
        "v_permlane16_swap_b32 v160, v162\n"
        "v_permlane16_swap_b32 v161, v163\n"
        "buffer_store_dwordx4 v[160:163], v0, s[40:43], s84 offen sc1\n"
        "s_waitcnt vmcnt(0)\n"
        "s_barrier\n"
        "buffer_store_dword v25, v2, s[40:43], s70 offen sc1\n"
        "s_add_u32 s68, s68, 1\n"
        "s_cmp_lt_u32 s68, 64\n"
        "s_cbranch_scc1 .Lr5_l1_%=\n"
        "s_mov_b32 s68, 0\n"
        "s_mov_b64 s[44:45], %2\n"
        "s_and_b32 s45, s45, 0xffff\n"
        ".Lr5_fin_%=:\n"
        "s_bitcmp1_b64 s[52:53], s68\n"
        "s_cselect_b32 s65, 8192, 0\n"
        "s_lshl_b32 s71, s68, 21\n"
        "s_add_u32 s71, s71, s65\n"
        "s_add_u32 s71, s71, 2097152\n"
        "s_lshr_b32 s65, s68, 2\n"
        "s_add_u32 s65, s65, 1\n"
        "s_bitcmp1_b32 s55, s65\n"
        "s_cselect_b32 s65, 8192, 0\n"
        "s_lshl_b32 s72, s68, 19\n"
        "s_add_u32 s72, s72, s65\n"
        "s_add_u32 s72, s72, 203423744\n"
        "s_add_u32 s76, s72, s61\n"
        "s_add_u32 s79, s71, s58\n"
        "s_add_u32 s79, s79, 1998848\n"
        "s_add_u32 s77, s72, s62\n"
        "s_add_u32 s80, s71, s59\n"
        "s_add_u32 s80, s80, 1998848\n"
        "s_add_u32 s78, s72, s63\n"
        "s_add_u32 s81, s71, s60\n"
        "s_add_u32 s81, s81, 1998848\n"
        "s_lshl_b32 s73, s68, 1\n"
        "s_add_u32 s73, s73, s74\n"
        "s_mov_b32 s64, 0\n"
        ".Lr5_finw_top_%=:\n"
        "buffer_load_dword v24, v1, s[40:43], s69 offen sc1\n"
        "s_waitcnt vmcnt(0)\n"
        "v_cmp_gt_i32 vcc, s73, v24\n"
        "s_nop 0\n"
        "s_and_b64 vcc, vcc, 0xffff\n"
        "s_cbranch_vccz .Lr5_finw_ok_%=\n"
        "s_add_u32 s64, s64, 1\n"
        "s_and_b32 s65, s64, 255\n"
        "s_cbranch_scc1 .Lr5_finw_go_%=\n"
        "s_mov_b32 s65, 49152\n"
        "s_cmp_lt_u32 s64, 1048576\n"
        "s_cbranch_scc1 .Lr5_finw_chk_%=\n"
        "v_mov_b32 v24, 1\n"
        "buffer_store_dword v24, off, s[40:43], s65 sc1\n"
        "s_waitcnt vmcnt(0)\n"
        ".Lr5_finw_chk_%=:\n"
        "buffer_load_dword v24, off, s[40:43], s65 sc1\n"
        "s_waitcnt vmcnt(0)\n"
        "v_readfirstlane_b32 s65, v24\n"
        "s_nop 0\n"
        "s_cmp_lg_u32 s65, 1\n"
        "s_cbranch_scc1 .Lr5_finw_go_%=\n"
        "s_endpgm\n"
        ".Lr5_finw_go_%=:\n"
        "s_branch .Lr5_finw_top_%=\n"
        ".Lr5_finw_ok_%=:\n"
        "s_cmp_eq_u32 s68, 63\n"
        "s_cselect_b32 s65, 0, 241172480\n"
        "v_mov_b32 v25, 1\n"
        "buffer_store_dword v25, v203, s[40:43], s65 offen sc1\n"
        "buffer_load_dwordx4 v[32:35], v3, s[40:43], s76 offen offset:0 sc1\n"
        "buffer_load_dwordx4 v[40:43], v3, s[40:43], s76 offen offset:512 sc1\n"
        "buffer_load_dwordx4 v[36:39], v3, s[40:43], s76 offen offset:256 sc1\n"
        "buffer_load_dwordx4 v[44:47], v3, s[40:43], s76 offen offset:768 sc1\n"
        "buffer_load_dwordx4 v[80:83], v0, s[40:43], s79 offen sc1\n"
        "buffer_load_dwordx4 v[48:51], v3, s[40:43], s77 offen offset:0 sc1\n"
        "buffer_load_dwordx4 v[56:59], v3, s[40:43], s77 offen offset:512 sc1\n"
        "buffer_load_dwordx4 v[52:55], v3, s[40:43], s77 offen offset:256 sc1\n"
        "buffer_load_dwordx4 v[60:63], v3, s[40:43], s77 offen offset:768 sc1\n"
        "buffer_load_dwordx4 v[84:87], v0, s[40:43], s80 offen sc1\n"
        "buffer_load_dwordx4 v[64:67], v4, s[40:43], s78 offen offset:0 sc1\n"
        "buffer_load_dwordx4 v[72:75], v4, s[40:43], s78 offen offset:512 sc1\n"
        "buffer_load_dwordx4 v[68:71], v4, s[40:43], s78 offen offset:256 sc1\n"
        "buffer_load_dwordx4 v[76:79], v4, s[40:43], s78 offen offset:768 sc1\n"
        "buffer_load_dwordx4 v[88:91], v0, s[40:43], s81 offen sc1\n"
        "s_waitcnt vmcnt(14)\n"
        "v_mfma_f32_16x16x32_bf16 v[92:95], v[8:11], v[32:35], 0\n"
        "s_waitcnt vmcnt(13)\n"
        "v_mfma_f32_16x16x32_bf16 v[92:95], v[12:15], v[40:43], v[92:95]\n"
        "s_waitcnt vmcnt(12)\n"
        "v_mfma_f32_16x16x32_bf16 v[96:99], v[8:11], v[36:39], 0\n"
        "s_waitcnt vmcnt(11)\n"
        "v_mfma_f32_16x16x32_bf16 v[96:99], v[12:15], v[44:47], v[96:99]\n"
        "s_waitcnt vmcnt(10)\n"
        "v_mfma_f32_16x16x32_bf16 v[92:95], v[16:19], v[80:83], v[92:95]\n"
        "v_mfma_f32_16x16x32_bf16 v[96:99], v[20:23], v[80:83], v[96:99]\n"
        "s_waitcnt vmcnt(9)\n"
        "v_mfma_f32_16x16x32_bf16 v[100:103], v[8:11], v[48:51], 0\n"
        "s_waitcnt vmcnt(8)\n"
        "v_mfma_f32_16x16x32_bf16 v[100:103], v[12:15], v[56:59], v[100:103]\n"
        "s_waitcnt vmcnt(7)\n"
        "v_mfma_f32_16x16x32_bf16 v[104:107], v[8:11], v[52:55], 0\n"
        "s_waitcnt vmcnt(6)\n"
        "v_mfma_f32_16x16x32_bf16 v[104:107], v[12:15], v[60:63], v[104:107]\n"
        "s_waitcnt vmcnt(5)\n"
        "v_mfma_f32_16x16x32_bf16 v[100:103], v[16:19], v[84:87], v[100:103]\n"
        "v_mfma_f32_16x16x32_bf16 v[104:107], v[20:23], v[84:87], v[104:107]\n"
        "s_waitcnt vmcnt(4)\n"
        "v_mfma_f32_16x16x32_bf16 v[108:111], v[8:11], v[64:67], 0\n"
        "s_waitcnt vmcnt(3)\n"
        "v_mfma_f32_16x16x32_bf16 v[108:111], v[12:15], v[72:75], v[108:111]\n"
        "s_waitcnt vmcnt(2)\n"
        "v_mfma_f32_16x16x32_bf16 v[112:115], v[8:11], v[68:71], 0\n"
        "s_waitcnt vmcnt(1)\n"
        "v_mfma_f32_16x16x32_bf16 v[112:115], v[12:15], v[76:79], v[112:115]\n"
        "s_waitcnt vmcnt(0)\n"
        "v_mfma_f32_16x16x32_bf16 v[108:111], v[16:19], v[88:91], v[108:111]\n"
        "v_mfma_f32_16x16x32_bf16 v[112:115], v[20:23], v[88:91], v[112:115]\n"
        "s_mul_i32 s65, s68, 36864\n"
        "v_cvt_pk_bf16_f32 v140, v92, v93\n"
        "v_cvt_pk_bf16_f32 v141, v94, v95\n"
        "v_cvt_pk_bf16_f32 v142, v96, v97\n"
        "v_cvt_pk_bf16_f32 v143, v98, v99\n"
        "s_nop 0\n"
        "v_permlane16_swap_b32 v140, v142\n"
        "v_permlane16_swap_b32 v141, v143\n"
        "buffer_store_dwordx4 v[140:143], v5, s[44:47], s65 offen offset:0\n"
        "v_cvt_pk_bf16_f32 v144, v100, v101\n"
        "v_cvt_pk_bf16_f32 v145, v102, v103\n"
        "v_cvt_pk_bf16_f32 v146, v104, v105\n"
        "v_cvt_pk_bf16_f32 v147, v106, v107\n"
        "s_nop 0\n"
        "v_permlane16_swap_b32 v144, v146\n"
        "v_permlane16_swap_b32 v145, v147\n"
        "buffer_store_dwordx4 v[144:147], v5, s[44:47], s65 offen offset:128\n"
        "s_nop 0\n"
        "v_cvt_pk_bf16_f32 v148, v108, v109\n"
        "v_cvt_pk_bf16_f32 v149, v110, v111\n"
        "v_cvt_pk_bf16_f32 v150, v112, v113\n"
        "v_cvt_pk_bf16_f32 v151, v114, v115\n"
        "s_nop 0\n"
        "v_permlane16_swap_b32 v148, v150\n"
        "v_permlane16_swap_b32 v149, v151\n"
        "buffer_store_dwordx4 v[148:151], v5, s[44:47], s65 offen offset:256\n"
        "s_add_u32 s68, s68, 1\n"
        "s_cmp_lt_u32 s68, 64\n"
        "s_cbranch_scc1 .Lr5_fin_%=\n"
        "s_barrier\n"
        "s_mov_b32 s65, 49280\n"
        "buffer_store_dwordx2 v[200:201], v202, s[40:43], s65 offen sc1\n"
        "s_endpgm\n"
        ".Lr5_end_%=:\n"
            RED_ARGS : "v0", "v1", "v2", "v3", "v4", "v5", "v6", "v7", "v8", "v9", "v10", "v11", "v12", "v13", "v14", "v15", "v16", "v17", "v18", "v19", "v20", "v21", "v22", "v23", "v24", "v25", "v26", "v27", "v28", "v29", "v30", "v31", "v32", "v33", "v34", "v35", "v36", "v37", "v38", "v39", "v40", "v41", "v42", "v43", "v44", "v45", "v46", "v47", "v48", "v49", "v50", "v51", "v52", "v53", "v54", "v55", "v56", "v57", "v58", "v59", "v60", "v61", "v62", "v63", "v64", "v65", "v66", "v67", "v68", "v69", "v70", "v71", "v72", "v73", "v74", "v75", "v76", "v77", "v78", "v79", "v255", "a255", "s30", "s31", "s32", "s33", "s34", "s35", "s36", "s37", "s38", "s39", "s40", "s41", "s42", "s43", "s44", "s45", "s46", "s47", "s48", "s49", "s50", "s51", "s52", "s53", "s54", "s55", "s56", "s57", "s58", "s59", "s60", "s61", "s62", "s63", "s64", "s65", "s66", "s67", "s68", "s69", "s70", "s71", "s72", "s73", "s74", "s75", "s76", "s77", "s78", "s79", "s80", "s81", "s82", "s83", "s84", "s85", "s86", "s87", "s88", "s89", "s90", "s91", "s92", "s93", "s94", "s95", "s96", "s97", "s98", "s99", "vcc", "scc", "memory");
    } else {
        asm volatile(
        "s_nop 4\n"
        "s_mov_b64 s[40:41], %0\n"
        "s_and_b32 s41, s41, 0xffff\n"
        "s_mov_b32 s42, 236978176\n"
        "s_mov_b32 s43, 0x20000\n"
        "s_mov_b64 s[44:45], %1\n"
        "s_and_b32 s45, s45, 0xffff\n"
        "s_mov_b32 s46, 2359296\n"
        "s_mov_b32 s47, 0x20000\n"
        "s_mov_b64 s[56:57], %3\n"
        "s_mov_b64 s[58:59], %4\n"
        "s_mov_b64 s[60:61], %5\n"
        "s_mov_b64 s[62:63], %6\n"
        "v_mov_b64 v[0:1], %7\n"
        "v_mov_b64 v[2:3], %8\n"
        "v_mov_b64 v[4:5], %9\n"
        "v_mov_b64 v[6:7], %10\n"
        "v_mov_b64 v[8:9], %11\n"
        "v_mov_b64 v[10:11], %12\n"
        "v_mov_b64 v[12:13], %13\n"
        "v_mov_b64 v[14:15], %14\n"
        "v_mov_b64 v[16:17], %15\n"
        "v_mov_b64 v[18:19], %16\n"
        "v_mov_b64 v[20:21], %17\n"
        "v_mov_b64 v[22:23], %18\n"
        "v_mov_b64 v[24:25], %19\n"
        "v_mov_b64 v[26:27], %20\n"
        "v_mov_b64 v[200:201], v[24:25]\n"
        "v_mov_b64 v[202:203], v[26:27]\n"
        "s_lshr_b32 s74, s56, 8\n"
        "s_and_b32 s56, s56, 1\n"
        "s_mov_b32 s68, 0\n"
        "s_mul_i32 s65, s68, 36864\n"
        "buffer_load_dwordx4 v[80:83], v5, s[44:47], s65 offen offset:0\n"
        "buffer_load_dwordx4 v[84:87], v5, s[44:47], s65 offen offset:128\n"
        "buffer_load_dwordx4 v[88:91], v5, s[44:47], s65 offen offset:256\n"
        "s_getpc_b64 s[76:77]\n"
        ".Lr3_pc_%=:\n"
        "s_add_u32 s76, s76, .Lr3_code_%=-.Lr3_pc_%=\n"
        "s_addc_u32 s77, s77, 0\n"
        "s_and_b32 s77, s77, 0xffff\n"
        "s_and_b32 s76, s76, -4\n"
        "s_mov_b32 s78, .Lr3_end_%=-.Lr3_code_%=\n"
        "s_mov_b32 s79, 0x20000\n"
        "s_mov_b32 s65, 0\n"
        "buffer_load_dwordx4 v[32:35], v0, s[76:79], s65 offen\n"
        "s_add_u32 s65, s65, 1024\n"
        "buffer_load_dwordx4 v[36:39], v0, s[76:79], s65 offen\n"
        "s_add_u32 s65, s65, 1024\n"
        "buffer_load_dwordx4 v[40:43], v0, s[76:79], s65 offen\n"
        "s_add_u32 s65, s65, 1024\n"
        "buffer_load_dwordx4 v[44:47], v0, s[76:79], s65 offen\n"
        "s_add_u32 s65, s65, 1024\n"
        "buffer_load_dwordx4 v[48:51], v0, s[76:79], s65 offen\n"
        "s_add_u32 s65, s65, 1024\n"
        "v_lshrrev_b32 v26, 4, v0\n"
        "s_mov_b32 s50, s56\n"
        "v_lshlrev_b32 v28, 7, v26\n"
        "s_sub_u32 s66, 0, s50\n"
        "s_mov_b32 s52, 32768\n"
        "s_mov_b32 s64, 0\n"
        ".Lr3_pg0_top_%=:\n"
        "buffer_load_dword v27, v28, s[40:43], s52 offen sc1\n"
        "s_waitcnt vmcnt(0)\n"
        "v_cmp_gt_i32 vcc, 0, v27\n"
        "s_cbranch_vccz .Lr3_pg0_ok_%=\n"
        "s_add_u32 s64, s64, 1\n"
        "s_and_b32 s65, s64, 255\n"
        "s_cbranch_scc1 .Lr3_pg0_go_%=\n"
        "s_mov_b32 s65, 49152\n"
        "s_cmp_lt_u32 s64, 1048576\n"
        "s_cbranch_scc1 .Lr3_pg0_chk_%=\n"
        "v_mov_b32 v27, 1\n"
        "buffer_store_dword v27, off, s[40:43], s65 sc1\n"
        "s_waitcnt vmcnt(0)\n"
        ".Lr3_pg0_chk_%=:\n"
        "buffer_load_dword v27, off, s[40:43], s65 sc1\n"
        "s_waitcnt vmcnt(0)\n"
        "v_readfirstlane_b32 s65, v27\n"
        "s_nop 0\n"
        "s_cmp_lg_u32 s65, 1\n"
        "s_cbranch_scc1 .Lr3_pg0_go_%=\n"
        "s_endpgm\n"
        ".Lr3_pg0_go_%=:\n"
        "s_sleep 2\n"
        "s_branch .Lr3_pg0_top_%=\n"
        ".Lr3_pg0_ok_%=:\n"
        "v_cmp_eq_u32 s[52:53], 1, v27\n"
        "s_nop 1\n"
        "s_mov_b32 s54, 40960\n"
        "s_mov_b32 s64, 0\n"
        ".Lr3_pg1_top_%=:\n"
        "buffer_load_dword v27, v28, s[40:43], s54 offen sc1\n"
        "s_waitcnt vmcnt(0)\n"
        "v_cmp_gt_i32 vcc, 0, v27\n"
        "s_cbranch_vccz .Lr3_pg1_ok_%=\n"
        "s_add_u32 s64, s64, 1\n"
        "s_and_b32 s65, s64, 255\n"
        "s_cbranch_scc1 .Lr3_pg1_go_%=\n"
        "s_mov_b32 s65, 49152\n"
        "s_cmp_lt_u32 s64, 1048576\n"
        "s_cbranch_scc1 .Lr3_pg1_chk_%=\n"
        "v_mov_b32 v27, 1\n"
        "buffer_store_dword v27, off, s[40:43], s65 sc1\n"
        "s_waitcnt vmcnt(0)\n"
        ".Lr3_pg1_chk_%=:\n"
        "buffer_load_dword v27, off, s[40:43], s65 sc1\n"
        "s_waitcnt vmcnt(0)\n"
        "v_readfirstlane_b32 s65, v27\n"
        "s_nop 0\n"
        "s_cmp_lg_u32 s65, 1\n"
        "s_cbranch_scc1 .Lr3_pg1_go_%=\n"
        "s_endpgm\n"
        ".Lr3_pg1_go_%=:\n"
        "s_sleep 2\n"
        "s_branch .Lr3_pg1_top_%=\n"
        ".Lr3_pg1_ok_%=:\n"
        "v_cmp_eq_u32 s[54:55], 1, v27\n"
        "s_nop 1\n"
        "s_mov_b32 vcc_lo, 0x55555555\n"
        "s_mov_b32 vcc_hi, 0x55555555\n"
        "s_xor_b64 vcc, vcc, s[52:53]\n"
        "s_bcnt1_i32_b64 s65, vcc\n"
        "s_sub_u32 s64, s65, 23\n"
        "s_cmp_lt_u32 s64, 19\n"
        "s_cbranch_scc1 .Lr3_rawmap_%=\n"
        "s_cmp_lt_u32 s65, 32\n"
        "s_cselect_b32 s65, 0, -1\n"
        "s_xor_b32 s52, s65, 0x55555555\n"
        "s_xor_b32 s53, s65, 0x55555555\n"
        "s_xor_b32 s54, s65, 0x55555555\n"
        "s_xor_b32 s55, s65, 0xaaaaaaaa\n"
        ".Lr3_rawmap_%=:\n"
        "s_xor_b32 s52, s52, s66\n"
        "s_xor_b32 s53, s53, s66\n"
        "s_xor_b32 s54, s54, s66\n"
        "s_xor_b32 s55, s55, s66\n"
        "s_and_b32 s69, s55, 1\n"
        "s_lshl_b32 s69, s69, 13\n"
        "s_xor_b32 s70, s69, 0\n"
        "s_cmp_eq_u32 s50, 0\n"
        "s_cbranch_scc0 .Lr3_l1_%=\n"
        ".Lr3_code_%=:\n"
        ".Lr3_l0_%=:\n"
        "s_bitcmp1_b64 s[52:53], s68\n"
        "s_cselect_b32 s65, 8192, 0\n"
        "s_lshl_b32 s71, s68, 21\n"
        "s_add_u32 s71, s71, s65\n"
        "s_add_u32 s71, s71, 2097152\n"
        "s_lshr_b32 s65, s68, 1\n"
        "s_bitcmp1_b32 s54, s65\n"
        "s_cselect_b32 s65, 8192, 0\n"
        "s_lshl_b32 s72, s68, 20\n"
        "s_add_u32 s72, s72, s65\n"
        "s_add_u32 s72, s72, 136904704\n"
        "s_add_u32 s76, s72, s61\n"
        "s_add_u32 s79, s71, s58\n"
        "s_add_u32 s79, s79, 1916928\n"
        "s_add_u32 s77, s72, s62\n"
        "s_add_u32 s80, s71, s59\n"
        "s_add_u32 s80, s80, 1916928\n"
        "s_add_u32 s78, s72, s63\n"
        "s_add_u32 s81, s71, s60\n"
        "s_add_u32 s81, s81, 1916928\n"
        "s_lshl_b32 s73, s68, 1\n"
        "s_add_u32 s73, s73, s74\n"
        "s_mov_b32 s64, 0\n"
        ".Lr3_l0w_top_%=:\n"
        "buffer_load_dword v24, v1, s[40:43], s69 offen sc1\n"
        "s_waitcnt vmcnt(0)\n"
        "v_cmp_gt_i32 vcc, s73, v24\n"
        "s_nop 0\n"
        "s_and_b64 vcc, vcc, 0xffff\n"
        "s_cbranch_vccz .Lr3_l0w_ok_%=\n"
        "s_add_u32 s64, s64, 1\n"
        "s_and_b32 s65, s64, 255\n"
        "s_cbranch_scc1 .Lr3_l0w_go_%=\n"
        "s_mov_b32 s65, 49152\n"
        "s_cmp_lt_u32 s64, 1048576\n"
        "s_cbranch_scc1 .Lr3_l0w_chk_%=\n"
        "v_mov_b32 v24, 1\n"
        "buffer_store_dword v24, off, s[40:43], s65 sc1\n"
        "s_waitcnt vmcnt(0)\n"
        ".Lr3_l0w_chk_%=:\n"
        "buffer_load_dword v24, off, s[40:43], s65 sc1\n"
        "s_waitcnt vmcnt(0)\n"
        "v_readfirstlane_b32 s65, v24\n"
        "s_nop 0\n"
        "s_cmp_lg_u32 s65, 1\n"
        "s_cbranch_scc1 .Lr3_l0w_go_%=\n"
        "s_endpgm\n"
        ".Lr3_l0w_go_%=:\n"
        "s_branch .Lr3_l0w_top_%=\n"
        ".Lr3_l0w_ok_%=:\n"
        "buffer_load_dwordx4 v[32:35], v3, s[40:43], s76 offen offset:0 sc1\n"
        "buffer_load_dwordx4 v[40:43], v3, s[40:43], s76 offen offset:512 sc1\n"
        "buffer_load_dwordx4 v[36:39], v3, s[40:43], s76 offen offset:256 sc1\n"
        "buffer_load_dwordx4 v[44:47], v3, s[40:43], s76 offen offset:768 sc1\n"
        "buffer_load_dwordx4 v[48:51], v3, s[40:43], s77 offen offset:0 sc1\n"
        "buffer_load_dwordx4 v[56:59], v3, s[40:43], s77 offen offset:512 sc1\n"
        "buffer_load_dwordx4 v[52:55], v3, s[40:43], s77 offen offset:256 sc1\n"
        "buffer_load_dwordx4 v[60:63], v3, s[40:43], s77 offen offset:768 sc1\n"
        "buffer_load_dwordx4 v[64:67], v4, s[40:43], s78 offen offset:0 sc1\n"
        "buffer_load_dwordx4 v[72:75], v4, s[40:43], s78 offen offset:512 sc1\n"
        "buffer_load_dwordx4 v[68:71], v4, s[40:43], s78 offen offset:256 sc1\n"
        "buffer_load_dwordx4 v[76:79], v4, s[40:43], s78 offen offset:768 sc1\n"
        "s_add_u32 s72, s72, 294912\n"
        "s_add_u32 s71, s71, 1998848\n"
        "s_add_u32 s82, s72, s58\n"
        "s_add_u32 s85, s71, s58\n"
        "s_add_u32 s83, s72, s59\n"
        "s_add_u32 s86, s71, s59\n"
        "s_add_u32 s84, s72, s60\n"
        "s_add_u32 s87, s71, s60\n"
        "s_waitcnt vmcnt(11)\n"
        "v_mfma_f32_16x16x32_bf16 v[92:95], v[8:11], v[32:35], 0\n"
        "s_waitcnt vmcnt(10)\n"
        "v_mfma_f32_16x16x32_bf16 v[92:95], v[12:15], v[40:43], v[92:95]\n"
        "s_waitcnt vmcnt(9)\n"
        "v_mfma_f32_16x16x32_bf16 v[96:99], v[8:11], v[36:39], 0\n"
        "s_waitcnt vmcnt(8)\n"
        "v_mfma_f32_16x16x32_bf16 v[96:99], v[12:15], v[44:47], v[96:99]\n"
        "v_mfma_f32_16x16x32_bf16 v[92:95], v[16:19], v[80:83], v[92:95]\n"
        "v_mfma_f32_16x16x32_bf16 v[96:99], v[20:23], v[80:83], v[96:99]\n"
        "s_waitcnt vmcnt(7)\n"
        "v_mfma_f32_16x16x32_bf16 v[100:103], v[8:11], v[48:51], 0\n"
        "s_waitcnt vmcnt(6)\n"
        "v_mfma_f32_16x16x32_bf16 v[100:103], v[12:15], v[56:59], v[100:103]\n"
        "s_waitcnt vmcnt(5)\n"
        "v_mfma_f32_16x16x32_bf16 v[104:107], v[8:11], v[52:55], 0\n"
        "s_waitcnt vmcnt(4)\n"
        "v_mfma_f32_16x16x32_bf16 v[104:107], v[12:15], v[60:63], v[104:107]\n"
        "v_mfma_f32_16x16x32_bf16 v[100:103], v[16:19], v[84:87], v[100:103]\n"
        "v_mfma_f32_16x16x32_bf16 v[104:107], v[20:23], v[84:87], v[104:107]\n"
        "s_waitcnt vmcnt(3)\n"
        "v_mfma_f32_16x16x32_bf16 v[108:111], v[8:11], v[64:67], 0\n"
        "s_waitcnt vmcnt(2)\n"
        "v_mfma_f32_16x16x32_bf16 v[108:111], v[12:15], v[72:75], v[108:111]\n"
        "s_waitcnt vmcnt(1)\n"
        "v_mfma_f32_16x16x32_bf16 v[112:115], v[8:11], v[68:71], 0\n"
        "s_waitcnt vmcnt(0)\n"
        "v_mfma_f32_16x16x32_bf16 v[112:115], v[12:15], v[76:79], v[112:115]\n"
        "v_mfma_f32_16x16x32_bf16 v[108:111], v[16:19], v[88:91], v[108:111]\n"
        "v_mfma_f32_16x16x32_bf16 v[112:115], v[20:23], v[88:91], v[112:115]\n"
        "s_add_u32 s68, s68, 1\n"
        "s_mul_i32 s65, s68, 36864\n"
        "buffer_load_dwordx4 v[80:83], v5, s[44:47], s65 offen offset:0\n"
        "buffer_load_dwordx4 v[84:87], v5, s[44:47], s65 offen offset:128\n"
        "buffer_load_dwordx4 v[88:91], v5, s[44:47], s65 offen offset:256\n"
        "s_sub_u32 s68, s68, 1\n"
        "v_pk_add_f32 v[164:165], v[92:93], v[94:95]\n"
        "v_pk_add_f32 v[166:167], v[96:97], v[98:99]\n"
        "v_pk_add_f32 v[168:169], v[100:101], v[102:103]\n"
        "v_pk_add_f32 v[170:171], v[104:105], v[106:107]\n"
        "v_pk_add_f32 v[164:165], v[164:165], v[166:167]\n"
        "v_pk_add_f32 v[168:169], v[168:169], v[170:171]\n"
        "v_pk_add_f32 v[164:165], v[164:165], v[168:169]\n"
        "v_pk_add_f32 v[166:167], v[108:109], v[110:111]\n"
        "v_pk_add_f32 v[168:169], v[112:113], v[114:115]\n"
        "v_pk_add_f32 v[164:165], v[164:165], v[166:167]\n"
        "v_pk_add_f32 v[164:165], v[164:165], v[168:169]\n"
        "v_add_f32 v164, v164, v165\n"
        "v_cvt_pk_bf16_f32 v140, v92, v93\n"
        "v_cvt_pk_bf16_f32 v141, v94, v95\n"
        "v_add_f32_dpp v166, v164, v164 row_ror:8 row_mask:0xf bank_mask:0xf\n"
        "v_add_f32_dpp v167, v164, v164 row_ror:8 row_mask:0xf bank_mask:0xf\n"
        "v_cvt_pk_bf16_f32 v142, v96, v97\n"
        "v_cvt_pk_bf16_f32 v143, v98, v99\n"
        "v_permlane16_swap_b32 v166, v167\n"
        "v_add_f32 v164, v166, v167\n"
        "v_add_f32 v166, v166, v167\n"
        "v_cvt_pk_bf16_f32 v144, v100, v101\n"
        "v_cvt_pk_bf16_f32 v145, v102, v103\n"
        "v_permlane32_swap_b32 v164, v166\n"
        "v_add_f32 v164, v164, v166\n"
        "v_mul_f32 v164, 0x3baaaaab, v164\n"
        "v_pk_add_f32 v[116:117], v[92:93], v[164:165] op_sel_hi:[1,0] neg_lo:[0,1] neg_hi:[0,1]\n"
        "v_pk_add_f32 v[118:119], v[94:95], v[164:165] op_sel_hi:[1,0] neg_lo:[0,1] neg_hi:[0,1]\n"
        "v_pk_add_f32 v[120:121], v[96:97], v[164:165] op_sel_hi:[1,0] neg_lo:[0,1] neg_hi:[0,1]\n"
        "v_pk_add_f32 v[122:123], v[98:99], v[164:165] op_sel_hi:[1,0] neg_lo:[0,1] neg_hi:[0,1]\n"
        "v_pk_add_f32 v[124:125], v[100:101], v[164:165] op_sel_hi:[1,0] neg_lo:[0,1] neg_hi:[0,1]\n"
        "v_pk_add_f32 v[126:127], v[102:103], v[164:165] op_sel_hi:[1,0] neg_lo:[0,1] neg_hi:[0,1]\n"
        "v_pk_add_f32 v[128:129], v[104:105], v[164:165] op_sel_hi:[1,0] neg_lo:[0,1] neg_hi:[0,1]\n"
        "v_pk_add_f32 v[130:131], v[106:107], v[164:165] op_sel_hi:[1,0] neg_lo:[0,1] neg_hi:[0,1]\n"
        "v_pk_add_f32 v[132:133], v[108:109], v[164:165] op_sel_hi:[1,0] neg_lo:[0,1] neg_hi:[0,1]\n"
        "v_pk_add_f32 v[134:135], v[110:111], v[164:165] op_sel_hi:[1,0] neg_lo:[0,1] neg_hi:[0,1]\n"
        "v_pk_add_f32 v[136:137], v[112:113], v[164:165] op_sel_hi:[1,0] neg_lo:[0,1] neg_hi:[0,1]\n"
        "v_pk_add_f32 v[138:139], v[114:115], v[164:165] op_sel_hi:[1,0] neg_lo:[0,1] neg_hi:[0,1]\n"
        "v_pk_mul_f32 v[166:167], v[116:117], v[116:117]\n"
        "v_pk_mul_f32 v[168:169], v[118:119], v[118:119]\n"
        "v_pk_fma_f32 v[166:167], v[120:121], v[120:121], v[166:167]\n"
        "v_pk_fma_f32 v[168:169], v[122:123], v[122:123], v[168:169]\n"
        "v_pk_fma_f32 v[166:167], v[124:125], v[124:125], v[166:167]\n"
        "v_pk_fma_f32 v[168:169], v[126:127], v[126:127], v[168:169]\n"
        "v_pk_fma_f32 v[166:167], v[128:129], v[128:129], v[166:167]\n"
        "v_pk_fma_f32 v[168:169], v[130:131], v[130:131], v[168:169]\n"
        "v_pk_fma_f32 v[166:167], v[132:133], v[132:133], v[166:167]\n"
        "v_pk_fma_f32 v[168:169], v[134:135], v[134:135], v[168:169]\n"
        "v_pk_fma_f32 v[166:167], v[136:137], v[136:137], v[166:167]\n"
        "v_pk_fma_f32 v[168:169], v[138:139], v[138:139], v[168:169]\n"
        "v_pk_add_f32 v[166:167], v[166:167], v[168:169]\n"
        "v_add_f32 v165, v166, v167\n"
        "v_cvt_pk_bf16_f32 v146, v104, v105\n"
        "v_cvt_pk_bf16_f32 v147, v106, v107\n"
        "v_add_f32_dpp v166, v165, v165 row_ror:8 row_mask:0xf bank_mask:0xf\n"
        "v_add_f32_dpp v167, v165, v165 row_ror:8 row_mask:0xf bank_mask:0xf\n"
        "v_cvt_pk_bf16_f32 v148, v108, v109\n"
        "v_cvt_pk_bf16_f32 v149, v110, v111\n"
        "v_permlane16_swap_b32 v166, v167\n"
        "v_add_f32 v165, v166, v167\n"
        "v_add_f32 v166, v166, v167\n"
        "v_cvt_pk_bf16_f32 v150, v112, v113\n"
        "v_cvt_pk_bf16_f32 v151, v114, v115\n"
        "v_permlane32_swap_b32 v165, v166\n"
        "v_add_f32 v165, v165, v166\n"
        "ds_write_b64 v6, v[164:165]\n"
        "v_permlane16_swap_b32 v140, v142\n"
        "v_permlane16_swap_b32 v141, v143\n"
        "v_permlane16_swap_b32 v144, v146\n"
        "v_permlane16_swap_b32 v145, v147\n"
        "s_waitcnt lgkmcnt(0)\n"
        "s_barrier\n"
        "ds_read_b64 v[166:167], v7 offset:0\n"
        "ds_read_b64 v[168:169], v7 offset:64\n"
        "ds_read_b64 v[170:171], v7 offset:128\n"
        "v_permlane16_swap_b32 v148, v150\n"
        "v_permlane16_swap_b32 v149, v151\n"
        "buffer_store_dwordx4 v[140:143], v0, s[40:43], s85 offen sc1\n"
        "buffer_store_dwordx4 v[144:147], v0, s[40:43], s86 offen sc1\n"
        "buffer_store_dwordx4 v[148:151], v0, s[40:43], s87 offen sc1\n"
        "s_waitcnt lgkmcnt(0)\n"
        "v_add_f32 v172, v166, v168\n"
        "v_add_f32 v173, v167, v169\n"
        "v_add_f32 v172, v172, v170\n"
        "v_add_f32 v173, v173, v171\n"
        "v_mul_f32 v172, 0x3eaaaaab, v172\n"
        "v_sub_f32 v166, v166, v172\n"
        "v_sub_f32 v168, v168, v172\n"
        "v_sub_f32 v170, v170, v172\n"
        "v_mul_f32 v166, v166, v166\n"
        "v_fmac_f32 v166, v168, v168\n"
        "v_fmac_f32 v166, v170, v170\n"
        "v_fmamk_f32 v173, v166, 0x43400000, v173\n"
        "v_mul_f32 v173, 0x3ae38e39, v173\n"
        "v_cvt_pk_bf16_f32 v172, v172, v173\n"
        "v_and_b32 v173, 0xffff0000, v172\n"
        "v_lshlrev_b32 v172, 16, v172\n"
        "v_add_f32 v173, 0x3727c5ac, v173\n"
        "v_cvt_pk_bf16_f32 v173, v173, v173\n"
        "v_lshlrev_b32 v173, 16, v173\n"
        "v_sqrt_f32 v173, v173\n"
        "v_sub_f32 v172, v164, v172\n"
        "v_cvt_pk_bf16_f32 v173, v173, v173\n"
        "v_lshlrev_b32 v173, 16, v173\n"
        "v_rcp_f32 v173, v173\n"
        "v_mov_b32 v25, s68\n"
        "v_mul_f32 v172, v172, v173\n"
        "v_pk_fma_f32 v[116:117], v[116:117], v[172:173], v[172:173] op_sel:[0,1,0] op_sel_hi:[1,1,0]\n"
        "v_pk_fma_f32 v[118:119], v[118:119], v[172:173], v[172:173] op_sel:[0,1,0] op_sel_hi:[1,1,0]\n"
        "v_pk_fma_f32 v[120:121], v[120:121], v[172:173], v[172:173] op_sel:[0,1,0] op_sel_hi:[1,1,0]\n"
        "v_pk_fma_f32 v[122:123], v[122:123], v[172:173], v[172:173] op_sel:[0,1,0] op_sel_hi:[1,1,0]\n"
        "v_cvt_pk_bf16_f32 v152, v116, v117\n"
        "v_cvt_pk_bf16_f32 v153, v118, v119\n"
        "v_cvt_pk_bf16_f32 v154, v120, v121\n"
        "v_cvt_pk_bf16_f32 v155, v122, v123\n"
        "s_nop 0\n"
        "v_permlane16_swap_b32 v152, v154\n"
        "v_permlane16_swap_b32 v153, v155\n"
        "buffer_store_dwordx4 v[152:155], v0, s[40:43], s82 offen sc1\n"
        "v_pk_fma_f32 v[124:125], v[124:125], v[172:173], v[172:173] op_sel:[0,1,0] op_sel_hi:[1,1,0]\n"
        "v_pk_fma_f32 v[126:127], v[126:127], v[172:173], v[172:173] op_sel:[0,1,0] op_sel_hi:[1,1,0]\n"
        "v_pk_fma_f32 v[128:129], v[128:129], v[172:173], v[172:173] op_sel:[0,1,0] op_sel_hi:[1,1,0]\n"
        "v_pk_fma_f32 v[130:131], v[130:131], v[172:173], v[172:173] op_sel:[0,1,0] op_sel_hi:[1,1,0]\n"
        "v_cvt_pk_bf16_f32 v156, v124, v125\n"
        "v_cvt_pk_bf16_f32 v157, v126, v127\n"
        "v_cvt_pk_bf16_f32 v158, v128, v129\n"
        "v_cvt_pk_bf16_f32 v159, v130, v131\n"
        "s_nop 0\n"
        "v_permlane16_swap_b32 v156, v158\n"
        "v_permlane16_swap_b32 v157, v159\n"
        "buffer_store_dwordx4 v[156:159], v0, s[40:43], s83 offen sc1\n"
        "v_pk_fma_f32 v[132:133], v[132:133], v[172:173], v[172:173] op_sel:[0,1,0] op_sel_hi:[1,1,0]\n"
        "v_pk_fma_f32 v[134:135], v[134:135], v[172:173], v[172:173] op_sel:[0,1,0] op_sel_hi:[1,1,0]\n"
        "v_pk_fma_f32 v[136:137], v[136:137], v[172:173], v[172:173] op_sel:[0,1,0] op_sel_hi:[1,1,0]\n"
        "v_pk_fma_f32 v[138:139], v[138:139], v[172:173], v[172:173] op_sel:[0,1,0] op_sel_hi:[1,1,0]\n"
        "v_cvt_pk_bf16_f32 v160, v132, v133\n"
        "v_cvt_pk_bf16_f32 v161, v134, v135\n"
        "v_cvt_pk_bf16_f32 v162, v136, v137\n"
        "v_cvt_pk_bf16_f32 v163, v138, v139\n"
        "s_nop 0\n"
        "v_permlane16_swap_b32 v160, v162\n"
        "v_permlane16_swap_b32 v161, v163\n"
        "buffer_store_dwordx4 v[160:163], v0, s[40:43], s84 offen sc1\n"
        "s_waitcnt vmcnt(0)\n"
        "s_barrier\n"
        "buffer_store_dword v25, v2, s[40:43], s70 offen sc1\n"
        "s_add_u32 s68, s68, 1\n"
        "s_cmp_lt_u32 s68, 64\n"
        "s_cbranch_scc1 .Lr3_l0_%=\n"
        "s_endpgm\n"
        ".Lr3_l1_%=:\n"
        "s_bitcmp1_b64 s[52:53], s68\n"
        "s_cselect_b32 s65, 8192, 0\n"
        "s_lshl_b32 s71, s68, 21\n"
        "s_add_u32 s71, s71, s65\n"
        "s_add_u32 s71, s71, 2097152\n"
        "s_lshr_b32 s65, s68, 1\n"
        "s_bitcmp1_b32 s54, s65\n"
        "s_cselect_b32 s65, 8192, 0\n"
        "s_lshl_b32 s72, s68, 20\n"
        "s_add_u32 s72, s72, s65\n"
        "s_add_u32 s72, s72, 136904704\n"
        "s_add_u32 s76, s72, s61\n"
        "s_add_u32 s79, s71, s58\n"
        "s_add_u32 s79, s79, 1916928\n"
        "s_add_u32 s77, s72, s62\n"
        "s_add_u32 s80, s71, s59\n"
        "s_add_u32 s80, s80, 1916928\n"
        "s_add_u32 s78, s72, s63\n"
        "s_add_u32 s81, s71, s60\n"
        "s_add_u32 s81, s81, 1916928\n"
        "s_lshl_b32 s73, s68, 1\n"
        "s_add_u32 s73, s73, s74\n"
        "s_mov_b32 s64, 0\n"
        ".Lr3_l1w_top_%=:\n"
        "buffer_load_dword v24, v1, s[40:43], s69 offen sc1\n"
        "s_waitcnt vmcnt(0)\n"
        "v_cmp_gt_i32 vcc, s73, v24\n"
        "s_nop 0\n"
        "s_and_b64 vcc, vcc, 0xffff\n"
        "s_cbranch_vccz .Lr3_l1w_ok_%=\n"
        "s_add_u32 s64, s64, 1\n"
        "s_and_b32 s65, s64, 255\n"
        "s_cbranch_scc1 .Lr3_l1w_go_%=\n"
        "s_mov_b32 s65, 49152\n"
        "s_cmp_lt_u32 s64, 1048576\n"
        "s_cbranch_scc1 .Lr3_l1w_chk_%=\n"
        "v_mov_b32 v24, 1\n"
        "buffer_store_dword v24, off, s[40:43], s65 sc1\n"
        "s_waitcnt vmcnt(0)\n"
        ".Lr3_l1w_chk_%=:\n"
        "buffer_load_dword v24, off, s[40:43], s65 sc1\n"
        "s_waitcnt vmcnt(0)\n"
        "v_readfirstlane_b32 s65, v24\n"
        "s_nop 0\n"
        "s_cmp_lg_u32 s65, 1\n"
        "s_cbranch_scc1 .Lr3_l1w_go_%=\n"
        "s_endpgm\n"
        ".Lr3_l1w_go_%=:\n"
        "s_branch .Lr3_l1w_top_%=\n"
        ".Lr3_l1w_ok_%=:\n"
        "buffer_load_dwordx4 v[32:35], v3, s[40:43], s76 offen offset:0 sc1\n"
        "buffer_load_dwordx4 v[40:43], v3, s[40:43], s76 offen offset:512 sc1\n"
        "buffer_load_dwordx4 v[36:39], v3, s[40:43], s76 offen offset:256 sc1\n"
        "buffer_load_dwordx4 v[44:47], v3, s[40:43], s76 offen offset:768 sc1\n"
        "buffer_load_dwordx4 v[80:83], v0, s[40:43], s79 offen sc1\n"
        "buffer_load_dwordx4 v[48:51], v3, s[40:43], s77 offen offset:0 sc1\n"
        "buffer_load_dwordx4 v[56:59], v3, s[40:43], s77 offen offset:512 sc1\n"
        "buffer_load_dwordx4 v[52:55], v3, s[40:43], s77 offen offset:256 sc1\n"
        "buffer_load_dwordx4 v[60:63], v3, s[40:43], s77 offen offset:768 sc1\n"
        "buffer_load_dwordx4 v[84:87], v0, s[40:43], s80 offen sc1\n"
        "buffer_load_dwordx4 v[64:67], v4, s[40:43], s78 offen offset:0 sc1\n"
        "buffer_load_dwordx4 v[72:75], v4, s[40:43], s78 offen offset:512 sc1\n"
        "buffer_load_dwordx4 v[68:71], v4, s[40:43], s78 offen offset:256 sc1\n"
        "buffer_load_dwordx4 v[76:79], v4, s[40:43], s78 offen offset:768 sc1\n"
        "buffer_load_dwordx4 v[88:91], v0, s[40:43], s81 offen sc1\n"
        "s_add_u32 s72, s72, 294912\n"
        "s_add_u32 s71, s71, 1998848\n"
        "s_add_u32 s82, s72, s58\n"
        "s_add_u32 s85, s71, s58\n"
        "s_add_u32 s83, s72, s59\n"
        "s_add_u32 s86, s71, s59\n"
        "s_add_u32 s84, s72, s60\n"
        "s_add_u32 s87, s71, s60\n"
        "s_waitcnt vmcnt(14)\n"
        "v_mfma_f32_16x16x32_bf16 v[92:95], v[8:11], v[32:35], 0\n"
        "s_waitcnt vmcnt(13)\n"
        "v_mfma_f32_16x16x32_bf16 v[92:95], v[12:15], v[40:43], v[92:95]\n"
        "s_waitcnt vmcnt(12)\n"
        "v_mfma_f32_16x16x32_bf16 v[96:99], v[8:11], v[36:39], 0\n"
        "s_waitcnt vmcnt(11)\n"
        "v_mfma_f32_16x16x32_bf16 v[96:99], v[12:15], v[44:47], v[96:99]\n"
        "s_waitcnt vmcnt(10)\n"
        "v_mfma_f32_16x16x32_bf16 v[92:95], v[16:19], v[80:83], v[92:95]\n"
        "v_mfma_f32_16x16x32_bf16 v[96:99], v[20:23], v[80:83], v[96:99]\n"
        "s_waitcnt vmcnt(9)\n"
        "v_mfma_f32_16x16x32_bf16 v[100:103], v[8:11], v[48:51], 0\n"
        "s_waitcnt vmcnt(8)\n"
        "v_mfma_f32_16x16x32_bf16 v[100:103], v[12:15], v[56:59], v[100:103]\n"
        "s_waitcnt vmcnt(7)\n"
        "v_mfma_f32_16x16x32_bf16 v[104:107], v[8:11], v[52:55], 0\n"
        "s_waitcnt vmcnt(6)\n"
        "v_mfma_f32_16x16x32_bf16 v[104:107], v[12:15], v[60:63], v[104:107]\n"
        "s_waitcnt vmcnt(5)\n"
        "v_mfma_f32_16x16x32_bf16 v[100:103], v[16:19], v[84:87], v[100:103]\n"
        "v_mfma_f32_16x16x32_bf16 v[104:107], v[20:23], v[84:87], v[104:107]\n"
        "s_waitcnt vmcnt(4)\n"
        "v_mfma_f32_16x16x32_bf16 v[108:111], v[8:11], v[64:67], 0\n"
        "s_waitcnt vmcnt(3)\n"
        "v_mfma_f32_16x16x32_bf16 v[108:111], v[12:15], v[72:75], v[108:111]\n"
        "s_waitcnt vmcnt(2)\n"
        "v_mfma_f32_16x16x32_bf16 v[112:115], v[8:11], v[68:71], 0\n"
        "s_waitcnt vmcnt(1)\n"
        "v_mfma_f32_16x16x32_bf16 v[112:115], v[12:15], v[76:79], v[112:115]\n"
        "s_waitcnt vmcnt(0)\n"
        "v_mfma_f32_16x16x32_bf16 v[108:111], v[16:19], v[88:91], v[108:111]\n"
        "v_mfma_f32_16x16x32_bf16 v[112:115], v[20:23], v[88:91], v[112:115]\n"
        "s_add_u32 s68, s68, 1\n"
        "s_sub_u32 s68, s68, 1\n"
        "v_pk_add_f32 v[164:165], v[92:93], v[94:95]\n"
        "v_pk_add_f32 v[166:167], v[96:97], v[98:99]\n"
        "v_pk_add_f32 v[168:169], v[100:101], v[102:103]\n"
        "v_pk_add_f32 v[170:171], v[104:105], v[106:107]\n"
        "v_pk_add_f32 v[164:165], v[164:165], v[166:167]\n"
        "v_pk_add_f32 v[168:169], v[168:169], v[170:171]\n"
        "v_pk_add_f32 v[164:165], v[164:165], v[168:169]\n"
        "s_nop 0\n"
        "v_pk_add_f32 v[166:167], v[108:109], v[110:111]\n"
        "v_pk_add_f32 v[168:169], v[112:113], v[114:115]\n"
        "v_pk_add_f32 v[164:165], v[164:165], v[166:167]\n"
        "v_pk_add_f32 v[164:165], v[164:165], v[168:169]\n"
        "v_add_f32 v164, v164, v165\n"
        "v_cvt_pk_bf16_f32 v140, v92, v93\n"
        "v_cvt_pk_bf16_f32 v141, v94, v95\n"
        "v_add_f32_dpp v166, v164, v164 row_ror:8 row_mask:0xf bank_mask:0xf\n"
        "v_add_f32_dpp v167, v164, v164 row_ror:8 row_mask:0xf bank_mask:0xf\n"
        "v_cvt_pk_bf16_f32 v142, v96, v97\n"
        "v_cvt_pk_bf16_f32 v143, v98, v99\n"
        "v_permlane16_swap_b32 v166, v167\n"
        "v_add_f32 v164, v166, v167\n"
        "v_add_f32 v166, v166, v167\n"
        "v_cvt_pk_bf16_f32 v144, v100, v101\n"
        "v_cvt_pk_bf16_f32 v145, v102, v103\n"
        "v_permlane32_swap_b32 v164, v166\n"
        "v_add_f32 v164, v164, v166\n"
        "v_mul_f32 v164, 0x3baaaaab, v164\n"
        "v_pk_add_f32 v[116:117], v[92:93], v[164:165] op_sel_hi:[1,0] neg_lo:[0,1] neg_hi:[0,1]\n"
        "v_pk_add_f32 v[118:119], v[94:95], v[164:165] op_sel_hi:[1,0] neg_lo:[0,1] neg_hi:[0,1]\n"
        "v_pk_add_f32 v[120:121], v[96:97], v[164:165] op_sel_hi:[1,0] neg_lo:[0,1] neg_hi:[0,1]\n"
        "v_pk_add_f32 v[122:123], v[98:99], v[164:165] op_sel_hi:[1,0] neg_lo:[0,1] neg_hi:[0,1]\n"
        "v_pk_add_f32 v[124:125], v[100:101], v[164:165] op_sel_hi:[1,0] neg_lo:[0,1] neg_hi:[0,1]\n"
        "v_pk_add_f32 v[126:127], v[102:103], v[164:165] op_sel_hi:[1,0] neg_lo:[0,1] neg_hi:[0,1]\n"
        "v_pk_add_f32 v[128:129], v[104:105], v[164:165] op_sel_hi:[1,0] neg_lo:[0,1] neg_hi:[0,1]\n"
        "v_pk_add_f32 v[130:131], v[106:107], v[164:165] op_sel_hi:[1,0] neg_lo:[0,1] neg_hi:[0,1]\n"
        "v_pk_add_f32 v[132:133], v[108:109], v[164:165] op_sel_hi:[1,0] neg_lo:[0,1] neg_hi:[0,1]\n"
        "v_pk_add_f32 v[134:135], v[110:111], v[164:165] op_sel_hi:[1,0] neg_lo:[0,1] neg_hi:[0,1]\n"
        "v_pk_add_f32 v[136:137], v[112:113], v[164:165] op_sel_hi:[1,0] neg_lo:[0,1] neg_hi:[0,1]\n"
        "v_pk_add_f32 v[138:139], v[114:115], v[164:165] op_sel_hi:[1,0] neg_lo:[0,1] neg_hi:[0,1]\n"
        "v_pk_mul_f32 v[166:167], v[116:117], v[116:117]\n"
        "v_pk_mul_f32 v[168:169], v[118:119], v[118:119]\n"
        "v_pk_fma_f32 v[166:167], v[120:121], v[120:121], v[166:167]\n"
        "v_pk_fma_f32 v[168:169], v[122:123], v[122:123], v[168:169]\n"
        "v_pk_fma_f32 v[166:167], v[124:125], v[124:125], v[166:167]\n"
        "v_pk_fma_f32 v[168:169], v[126:127], v[126:127], v[168:169]\n"
        "v_pk_fma_f32 v[166:167], v[128:129], v[128:129], v[166:167]\n"
        "v_pk_fma_f32 v[168:169], v[130:131], v[130:131], v[168:169]\n"
        "v_pk_fma_f32 v[166:167], v[132:133], v[132:133], v[166:167]\n"
        "v_pk_fma_f32 v[168:169], v[134:135], v[134:135], v[168:169]\n"
        "v_pk_fma_f32 v[166:167], v[136:137], v[136:137], v[166:167]\n"
        "v_pk_fma_f32 v[168:169], v[138:139], v[138:139], v[168:169]\n"
        "v_pk_add_f32 v[166:167], v[166:167], v[168:169]\n"
        "v_add_f32 v165, v166, v167\n"
        "v_cvt_pk_bf16_f32 v146, v104, v105\n"
        "v_cvt_pk_bf16_f32 v147, v106, v107\n"
        "v_add_f32_dpp v166, v165, v165 row_ror:8 row_mask:0xf bank_mask:0xf\n"
        "v_add_f32_dpp v167, v165, v165 row_ror:8 row_mask:0xf bank_mask:0xf\n"
        "v_cvt_pk_bf16_f32 v148, v108, v109\n"
        "v_cvt_pk_bf16_f32 v149, v110, v111\n"
        "v_permlane16_swap_b32 v166, v167\n"
        "v_add_f32 v165, v166, v167\n"
        "v_add_f32 v166, v166, v167\n"
        "v_cvt_pk_bf16_f32 v150, v112, v113\n"
        "v_cvt_pk_bf16_f32 v151, v114, v115\n"
        "v_permlane32_swap_b32 v165, v166\n"
        "v_add_f32 v165, v165, v166\n"
        "ds_write_b64 v6, v[164:165]\n"
        "v_permlane16_swap_b32 v140, v142\n"
        "v_permlane16_swap_b32 v141, v143\n"
        "v_permlane16_swap_b32 v144, v146\n"
        "v_permlane16_swap_b32 v145, v147\n"
        "s_waitcnt lgkmcnt(0)\n"
        "s_barrier\n"
        "ds_read_b64 v[166:167], v7 offset:0\n"
        "ds_read_b64 v[168:169], v7 offset:64\n"
        "ds_read_b64 v[170:171], v7 offset:128\n"
        "v_permlane16_swap_b32 v148, v150\n"
        "v_permlane16_swap_b32 v149, v151\n"
        "buffer_store_dwordx4 v[140:143], v0, s[40:43], s85 offen sc1\n"
        "buffer_store_dwordx4 v[144:147], v0, s[40:43], s86 offen sc1\n"
        "buffer_store_dwordx4 v[148:151], v0, s[40:43], s87 offen sc1\n"
        "s_waitcnt lgkmcnt(0)\n"
        "v_add_f32 v172, v166, v168\n"
        "v_add_f32 v173, v167, v169\n"
        "v_add_f32 v172, v172, v170\n"
        "v_add_f32 v173, v173, v171\n"
        "v_mul_f32 v172, 0x3eaaaaab, v172\n"
        "v_sub_f32 v166, v166, v172\n"
        "v_sub_f32 v168, v168, v172\n"
        "v_sub_f32 v170, v170, v172\n"
        "v_mul_f32 v166, v166, v166\n"
        "v_fmac_f32 v166, v168, v168\n"
        "v_fmac_f32 v166, v170, v170\n"
        "v_fmamk_f32 v173, v166, 0x43400000, v173\n"
        "v_mul_f32 v173, 0x3ae38e39, v173\n"
        "v_cvt_pk_bf16_f32 v172, v172, v173\n"
        "v_and_b32 v173, 0xffff0000, v172\n"
        "v_lshlrev_b32 v172, 16, v172\n"
        "v_add_f32 v173, 0x3727c5ac, v173\n"
        "v_cvt_pk_bf16_f32 v173, v173, v173\n"
        "v_lshlrev_b32 v173, 16, v173\n"
        "v_sqrt_f32 v173, v173\n"
        "v_sub_f32 v172, v164, v172\n"
        "v_cvt_pk_bf16_f32 v173, v173, v173\n"
        "v_lshlrev_b32 v173, 16, v173\n"
        "v_rcp_f32 v173, v173\n"
        "v_mov_b32 v25, s68\n"
        "v_mul_f32 v172, v172, v173\n"
        "v_pk_fma_f32 v[116:117], v[116:117], v[172:173], v[172:173] op_sel:[0,1,0] op_sel_hi:[1,1,0]\n"
        "v_pk_fma_f32 v[118:119], v[118:119], v[172:173], v[172:173] op_sel:[0,1,0] op_sel_hi:[1,1,0]\n"
        "v_pk_fma_f32 v[120:121], v[120:121], v[172:173], v[172:173] op_sel:[0,1,0] op_sel_hi:[1,1,0]\n"
        "v_pk_fma_f32 v[122:123], v[122:123], v[172:173], v[172:173] op_sel:[0,1,0] op_sel_hi:[1,1,0]\n"
        "v_cvt_pk_bf16_f32 v152, v116, v117\n"
        "v_cvt_pk_bf16_f32 v153, v118, v119\n"
        "v_cvt_pk_bf16_f32 v154, v120, v121\n"
        "v_cvt_pk_bf16_f32 v155, v122, v123\n"
        "s_nop 0\n"
        "v_permlane16_swap_b32 v152, v154\n"
        "v_permlane16_swap_b32 v153, v155\n"
        "buffer_store_dwordx4 v[152:155], v0, s[40:43], s82 offen sc1\n"
        "v_pk_fma_f32 v[124:125], v[124:125], v[172:173], v[172:173] op_sel:[0,1,0] op_sel_hi:[1,1,0]\n"
        "v_pk_fma_f32 v[126:127], v[126:127], v[172:173], v[172:173] op_sel:[0,1,0] op_sel_hi:[1,1,0]\n"
        "v_pk_fma_f32 v[128:129], v[128:129], v[172:173], v[172:173] op_sel:[0,1,0] op_sel_hi:[1,1,0]\n"
        "v_pk_fma_f32 v[130:131], v[130:131], v[172:173], v[172:173] op_sel:[0,1,0] op_sel_hi:[1,1,0]\n"
        "v_cvt_pk_bf16_f32 v156, v124, v125\n"
        "v_cvt_pk_bf16_f32 v157, v126, v127\n"
        "v_cvt_pk_bf16_f32 v158, v128, v129\n"
        "v_cvt_pk_bf16_f32 v159, v130, v131\n"
        "s_nop 0\n"
        "v_permlane16_swap_b32 v156, v158\n"
        "v_permlane16_swap_b32 v157, v159\n"
        "buffer_store_dwordx4 v[156:159], v0, s[40:43], s83 offen sc1\n"
        "v_pk_fma_f32 v[132:133], v[132:133], v[172:173], v[172:173] op_sel:[0,1,0] op_sel_hi:[1,1,0]\n"
        "v_pk_fma_f32 v[134:135], v[134:135], v[172:173], v[172:173] op_sel:[0,1,0] op_sel_hi:[1,1,0]\n"
        "v_pk_fma_f32 v[136:137], v[136:137], v[172:173], v[172:173] op_sel:[0,1,0] op_sel_hi:[1,1,0]\n"
        "v_pk_fma_f32 v[138:139], v[138:139], v[172:173], v[172:173] op_sel:[0,1,0] op_sel_hi:[1,1,0]\n"
        "v_cvt_pk_bf16_f32 v160, v132, v133\n"
        "v_cvt_pk_bf16_f32 v161, v134, v135\n"
        "v_cvt_pk_bf16_f32 v162, v136, v137\n"
        "v_cvt_pk_bf16_f32 v163, v138, v139\n"
        "s_nop 0\n"
        "v_permlane16_swap_b32 v160, v162\n"
        "v_permlane16_swap_b32 v161, v163\n"
        "buffer_store_dwordx4 v[160:163], v0, s[40:43], s84 offen sc1\n"
        "s_waitcnt vmcnt(0)\n"
        "s_barrier\n"
        "buffer_store_dword v25, v2, s[40:43], s70 offen sc1\n"
        "s_add_u32 s68, s68, 1\n"
        "s_cmp_lt_u32 s68, 64\n"
        "s_cbranch_scc1 .Lr3_l1_%=\n"
        "s_endpgm\n"
        ".Lr3_end_%=:\n"
            RED_ARGS : "v0", "v1", "v2", "v3", "v4", "v5", "v6", "v7", "v8", "v9", "v10", "v11", "v12", "v13", "v14", "v15", "v16", "v17", "v18", "v19", "v20", "v21", "v22", "v23", "v24", "v25", "v26", "v27", "v28", "v29", "v30", "v31", "v32", "v33", "v34", "v35", "v36", "v37", "v38", "v39", "v40", "v41", "v42", "v43", "v44", "v45", "v46", "v47", "v48", "v49", "v50", "v51", "v52", "v53", "v54", "v55", "v56", "v57", "v58", "v59", "v60", "v61", "v62", "v63", "v64", "v65", "v66", "v67", "v68", "v69", "v70", "v71", "v72", "v73", "v74", "v75", "v76", "v77", "v78", "v79", "v255", "a255", "s30", "s31", "s32", "s33", "s34", "s35", "s36", "s37", "s38", "s39", "s40", "s41", "s42", "s43", "s44", "s45", "s46", "s47", "s48", "s49", "s50", "s51", "s52", "s53", "s54", "s55", "s56", "s57", "s58", "s59", "s60", "s61", "s62", "s63", "s64", "s65", "s66", "s67", "s68", "s69", "s70", "s71", "s72", "s73", "s74", "s75", "s76", "s77", "s78", "s79", "s80", "s81", "s82", "s83", "s84", "s85", "s86", "s87", "s88", "s89", "s90", "s91", "s92", "s93", "s94", "s95", "s96", "s97", "s98", "s99", "vcc", "scc", "memory");
    }
    __builtin_unreachable();
}

__global__ __launch_bounds__(256, 1) void mega_kernel(Params p) {
    extern __shared__ __attribute__((aligned(16))) char smem[];
    const rsrc_t rs = __builtin_amdgcn_make_buffer_rsrc(p.ws, 0, WS_USED, 0x00020000);
    const v2u ev = __builtin_amdgcn_raw_buffer_load_b64(rs, G_EPOCH * 128, 0, 16);
    const int roles = role_tab[(threadIdx.x & 3) * 32 + (blockIdx.x >> 3)];
    if (blockIdx.x == 0) st4(rs, threadIdx.x <= 128 || (threadIdx.x >= G_DONE - G_PG && threadIdx.x < G_DONE - G_PG + 18) ? (G_PG + threadIdx.x) * 128 : OOB, -1);
    const u64 epoch = pair(rfl(ev[0]), rfl(ev[1])), tag = (epoch + 0x9e3779b97f4a7c15ull) & ~7ull;
    if (blockIdx.x == 0) {
        DRAIN();
        BAR();
        __builtin_amdgcn_raw_buffer_store_b64(v2u{(unsigned)tag | my_xcc(), (unsigned)(tag >> 32)}, rs, (G_ROT + threadIdx.x) * 128, 0, 16);
    }
    unsigned* const srot = reinterpret_cast<unsigned*>(smem + CH_IMG);
    if (threadIdx.x == 0) {
        unsigned rot = 8;
#pragma unroll 1
        for (unsigned spins = 0; spins < SPIN_CAP; ++spins) {
            const v2u v = __builtin_amdgcn_raw_buffer_load_b64(rs, (G_ROT + blockIdx.x) * 128, 0, 16);
            if ((pair(v[0], v[1]) & ~7ull) == tag) { rot = v[0] & 7; break; }
            asm volatile("" ::: "memory");
        }
        *srot = rot;
    }
    BAR();
    const unsigned rot = rfl(*srot);
    if (rot == 8) return;
    BAR();
    const unsigned xb = (blockIdx.x + rot) & 7;
    const int x4 = xb >> 2, half = x4, pr = __builtin_amdgcn_readlane(roles, xb & 3), k = pr >> 2, grp = pr & 3, x = x4 * 4 + grp;
    if (k < 28 && (half == 1 || k >= 16) && !((p.tune2 >> 60) & 1)) {
        const unsigned w = rfl(threadIdx.x >> 6), first = ((half ? pr : pr + 48) * 4 + w) * 1024;
        warm(p.Wq, 4718592, first, 640); warm(p.x, 2359296, first, 640); warm(p.Wk, 4718592, first, 640); warm(p.Wo, 4718592, first, 640); warm(p.Wv, 4718592, first, 640);
        if (half == 1 && k >= 8) { warm(p.w1, 5308416, first - 131072, 320); warm(p.w3, 5308416, first - 131072, 320); warm(p.w2, 5308416, first - 131072, 320); }
        DRAIN();
        BAR();
    }
    auto dl = [&](int r) __attribute__((always_inline)) -> int { return (int)(p.tune2 >> (6 * (r + 5 * half))) & 63; };
    if (k < 8) role_chain(p, grp * 8 + k, x4 | dl(0) << 8, pr, smem);
    else if (k < 16) role_s1(p, grp * 8 + k - 8, x4, pr, dl(1));
    else if (k < 22) role_f1(p, grp, k - 16, x4, pr, dl(3));
    else if (k < 25) role_k<true>(p, grp, k - 22, x4, pr, dl(2));
    else if (k < 28) role_k<false>(p, grp, k - 25, x4, pr, dl(4));
    else if (k >= 28) {
        startup(rs, ((k - 28) * 8 + x) * 4 + rfl(threadIdx.x >> 6));
        if (k < 30) role_attn(p, grp * 8 + (k - 28) * 4, x4, pr);
        else if (k == 30) role_red<false>(p, grp, x4, pr, epoch);
        else role_red<true>(p, grp, x4, pr, epoch);
    }
}

}

extern "C" void kernel_launch(void* const* d_in, const int* in_sizes, int n_in, void* d_out, int out_size,
                              void* d_ws, size_t ws_size, hipStream_t stream) {
    if (ws_size < (size_t)WS_USED + 2 * PAGE) return;
    Params p;
    p.x = (const u16*)d_in[0]; p.mem0 = (const u16*)d_in[1];
    p.Wq = (const u16*)d_in[2]; p.Wk = (const u16*)d_in[3]; p.Wv = (const u16*)d_in[4]; p.Wo = (const u16*)d_in[5];
    p.w1 = (const u16*)d_in[8]; p.w3 = (const u16*)d_in[9]; p.w2 = (const u16*)d_in[10];
    p.out = (u16*)d_out;
    p.ws = (char*)d_ws + ((0 - (size_t)d_ws) & (PAGE - 1));
    p.tune0 = (u64)D1 | (u64)D2 << 32; p.tune1 = (u64)D3 | (u64)D4 << 32;
    p.tune2 = (u64)DL0 | (u64)DL1 << 30;
    (void)hipFuncSetAttribute((const void*)mega_kernel, hipFuncAttributeMaxDynamicSharedMemorySize, LDS_BYTES);
    mega_kernel<<<256, 256, LDS_BYTES, stream>>>(p);
}
```

```cpp
#include <hip/hip_runtime.h>
#include <stdint.h>

typedef unsigned short u16;
typedef unsigned long long u64;
typedef unsigned v4u __attribute__((ext_vector_type(4)));
typedef unsigned v2u __attribute__((ext_vector_type(2)));
typedef short bf16x8 __attribute__((ext_vector_type(8)));
typedef float f32x4 __attribute__((ext_vector_type(4)));
typedef __bf16 bf2_t __attribute__((ext_vector_type(2)));
typedef __amdgpu_buffer_rsrc_t rsrc_t;

#define XF2L 0
#define XF2S 0
#define XF2M 24
#define XFN 1
#define WARMD 4
#define TCAL 200
#define RSLEEP 1
namespace {

constexpr unsigned A_K = 262144u;
constexpr unsigned A_KM = 786432u;
constexpr unsigned A_PROBE = 1044480u;
constexpr unsigned A_Q = 0u;
constexpr unsigned A_ST = 1036288u;
constexpr unsigned A_V = 524288u;
constexpr unsigned A_X1 = 999424u;
constexpr unsigned A_XN = 917504u;
constexpr unsigned A_XR = 958464u;
constexpr unsigned B_H = 0u;
constexpr unsigned B_P3 = 294912u;
constexpr unsigned B_XN1 = 442368u;
constexpr unsigned C_P5 = 0u;
constexpr unsigned DBG = 236978176u;
constexpr unsigned DBG2 = 238026752u;
constexpr unsigned EXB = 294912u;
constexpr unsigned EXL = 270336u;
constexpr unsigned F_AT = 832u;
constexpr unsigned F_CH = 704u;
constexpr unsigned F_F1 = 960u;
constexpr unsigned F_F2 = 1600u;
constexpr unsigned F_LINES = 1856u;
constexpr unsigned F_OP = 1344u;
constexpr unsigned F_R3 = 256u;
constexpr unsigned F_S1 = 448u;
constexpr unsigned F_XN = 0u;
constexpr unsigned G_ABORT = 384u;
constexpr unsigned G_DONE = 400u;
constexpr unsigned G_EPOCH = 385u;
constexpr unsigned G_LINES = 512u;
constexpr unsigned G_PG = 256u;
constexpr unsigned G_ROT = 0u;
constexpr unsigned K1 = 1024u;
constexpr unsigned LF0 = 32768u;
constexpr unsigned MEMB = 40960u;
constexpr unsigned MEML = 565248u;
constexpr unsigned NPAGES = 113u;
constexpr unsigned OOB = 2147483648u;
constexpr unsigned PAGE = 2097152u;
constexpr unsigned SPIN_CAP = 1048576u;
constexpr unsigned WS_USED = 236978176u;
constexpr unsigned CH_WB0 = 0u;
constexpr unsigned CH_WB1 = 40960u;
constexpr unsigned CH_WB2 = 81920u;
constexpr unsigned CH_WB3 = 116736u;
constexpr unsigned CH_IMG = 151552u;
constexpr unsigned CH_SO = 156672u;
constexpr unsigned CH_SELT = 160768u;
constexpr unsigned NPROBE = 6u;
constexpr unsigned NCOPY = 4u;
constexpr unsigned DL0 = 169877504u;
constexpr unsigned DL1 = 20u;
constexpr unsigned D1 = 1598029934u;
constexpr unsigned D2 = 2843934720u;
constexpr unsigned D3 = 1007419392u;
constexpr unsigned D4 = 402472u;
constexpr int LDS_BYTES = 160 * 1024;
constexpr unsigned spread(unsigned o) { return (o >> 13) * 16384u + (o & 8191u); }

__device__ const unsigned char role_tab[128] = {0, 4, 8, 12, 16, 20, 24, 28, 1, 5, 9, 13, 17, 21, 25, 29, 2, 6, 10, 14, 18, 22, 26, 30, 3, 7, 11, 15, 19, 23, 27, 31, 32, 36, 40, 44, 48, 52, 56, 60, 33, 37, 64, 68, 72, 76, 80, 84, 65, 69, 73, 77, 100, 104, 108, 101, 105, 109, 112, 116, 113, 117, 114, 118, 41, 45, 49, 53, 57, 61, 34, 38, 42, 46, 50, 54, 81, 85, 66, 70, 74, 78, 82, 86, 67, 71, 75, 79, 102, 106, 110, 103, 107, 111, 115, 119, 58, 62, 35, 39, 43, 47, 51, 55, 59, 63, 83, 87, 88, 92, 96, 89, 93, 97, 90, 94, 98, 91, 95, 99, 120, 124, 121, 125, 122, 126, 123, 127};

struct Params {
    const u16* x; const u16* mem0; const u16* Wq; const u16* Wk; const u16* Wv; const u16* Wo;
    const u16* w1; const u16* w3; const u16* w2; u16* out; char* ws;
    u64 tune0, tune1, tune2;
};

__device__ __forceinline__ v4u ld16(rsrc_t rs, unsigned voff, unsigned soff) { return __builtin_amdgcn_raw_buffer_load_b128(rs, voff, soff, 16); }
__device__ __forceinline__ int ld4(rsrc_t rs, unsigned off) { return (int)__builtin_amdgcn_raw_buffer_load_b32(rs, off, 0, 16); }
__device__ __forceinline__ void st16(rsrc_t rs, unsigned voff, unsigned soff, v4u v) { __builtin_amdgcn_raw_buffer_store_b128(v, rs, voff, soff, 16); }
__device__ __forceinline__ void st4(rsrc_t rs, unsigned off, int v) { __builtin_amdgcn_raw_buffer_store_b32((unsigned)v, rs, off, 0, 16); }
#define DRAIN() asm volatile("s_waitcnt vmcnt(0)" ::: "memory")
#define BAR() do { asm volatile("s_waitcnt lgkmcnt(0)" ::: "memory"); __builtin_amdgcn_s_barrier(); asm volatile("" ::: "memory"); } while (0)
__device__ __forceinline__ int rfl(int v) { return __builtin_amdgcn_readfirstlane(v); }
__device__ __forceinline__ u64 pair(unsigned lo, unsigned hi) { return (u64)lo | (u64)hi << 32; }
__device__ __forceinline__ unsigned my_xcc() {
    unsigned id;
    asm volatile("s_getreg_b32 %0, hwreg(HW_REG_XCC_ID)" : "=s"(id));
    return id & 7;
}

__device__ __forceinline__ void startup(rsrc_t rs, int hw) {
    const int lane = threadIdx.x & 63;
    const v4u ones = {~0u, ~0u, ~0u, ~0u};
    const unsigned o = (unsigned)(hw < 96 ? hw + 1 : (hw == 96 ? 0 : hw)) * PAGE + spread(A_PROBE);
    unsigned best[2] = {~0u, ~0u};
    if (hw < NPAGES) {
#pragma unroll 1
        for (int r = 0; r < 2 * NPROBE; ++r) {
            const unsigned t0 = (unsigned)__builtin_amdgcn_s_memtime();
            st16(rs, lane < 8 ? lane * 16 : OOB, o + (r & 1) * 8192, ones);
            DRAIN();
            const unsigned t = (unsigned)__builtin_amdgcn_s_memtime() - t0;
            if (r & 1) best[1] = min(best[1], t); else best[0] = min(best[0], t);
        }
    }
#pragma unroll
    for (int q = 0; q < 11; ++q) st16(rs, lane * 16, 2 * LF0 + (hw * 11 + q) * 1024, ones);
    static_assert(2 * LF0 + 128 * 11 * 1024 >= 2 * (MEML + 4 * MEMB) && 2 * LF0 + 128 * 11 * 1024 <= 2 * A_PROBE, "flags and exchange area");
#pragma unroll
    for (int q = 0; q < 4; ++q) st16(rs, lane * 16, (unsigned)(1 + (hw >> 1)) * PAGE + spread(A_ST + q * 1024) + (hw & 1) * 8192, ones);
    DRAIN();
    if (lane == 0) st4(rs, (G_PG + hw) * 128, (best[1] < best[0] ? 1 : 0) ^ (my_xcc() >> 2));
}

__device__ __forceinline__ void warm(const u16* W, unsigned bytes, unsigned first, unsigned step) {
    const rsrc_t r = __builtin_amdgcn_make_buffer_rsrc(const_cast<u16*>(W), 0, bytes, 0x00020000);
    const unsigned vo = (threadIdx.x & 63) * 16;
#pragma unroll 1
    for (unsigned o = first; o < bytes; o += step * 1024)
        asm volatile("s_mov_b32 m0, 0\n\ts_nop 0\n\tbuffer_load_dwordx4 %0, %1, %2 offen lds\n\ts_waitcnt vmcnt(%3)" :: "v"(vo), "s"(r), "s"(o), "n"(WARMD) : "memory");
}

__device__ __forceinline__ void role_chain(const Params& p, int h, int x4, int pr, char* smem) {
    const unsigned tid = threadIdx.x, lane = tid & 63, c = lane & 15, g = lane >> 4;
    const int w = rfl(tid >> 6);
    const unsigned wb = w == 0 ? CH_WB0 : (w == 1 ? CH_WB1 : (w == 2 ? CH_WB2 : CH_WB3));
    const unsigned slot = (g * 8 + (c & 7)) * 16;
    const unsigned spr = CH_IMG + (c >> 3) * 512 + slot;
    const unsigned gam = lane >> 4, nh = (lane >> 3) & 1, ksl = (lane >> 2) & 1, tau = ((lane ^ gam) >> 1) & 1, rho = lane & 1;
    auto img = [&](unsigned r) __attribute__((always_inline)) -> unsigned { return (2 * (r & 3) + (g & 1)) * 128 + (((g ^ r) & 2) + (r >> 2)) * 16; };
    if (w >= 2) {
        v4u sel;
#pragma unroll
        for (int e = 0; e < 4; ++e) sel[e] = ((c & 7) == 2 * e) ? 0x3f80u : (((c & 7) == 2 * e + 1) ? 0x3f800000u : 0u);
        const unsigned g3 = ksl * 512 + (2 * (gam & 1) + nh) * 128 + (4 * (gam >> 1) + 2 * tau + rho) * 16;
        *reinterpret_cast<v4u*>(smem + wb + 32768 + lane * 16) = sel;
        *reinterpret_cast<v4u*>(smem + wb + 32768 + 1024 + lane * 16) =
            v4u{lane < 8 * NCOPY ? (lane >> 3) * 1024 + (lane & 7) * 16 : OOB, g3, lane < 8 * NCOPY ? (lane >> 3) * 16384 + ((lane & 7) ^ ((h >> 1) & 2)) * 16 : OOB, 0u};
    } else if (w == 0) {
        const bool onq = g == (c & 3), upq = (c & 4) != 0, onk = g == (c >> 2) && (c & 3) < 2, upk = (c & 1) != 0;
        *reinterpret_cast<v4u*>(smem + spr + CH_SELT - CH_IMG) = v4u{onq ? (upq ? 0x3f800000u : 0x3f80u) : 0u, onq ? (upq ? 0x3f80u : 0x3f800000u) : 0u,
                                                                     onk ? (upk ? 0x3f800000u : 0x3f80u) : 0u, onk ? (upk ? 0x3f80u : 0x3f800000u) : 0u};
    } else {
        const unsigned pub = (lane >> 4) * 16384 + ((lane >> 3) & 1) * 4096 + (lane & 7) * 16;
        const unsigned kmo = (c & 3) < 2 ? (g * 8 + 2 * (c >> 2) + (c & 3)) * 16 : OOB;
        const unsigned vmo = (g & 1) ? OOB : (g >> 1) * 256 + c * 16;
        *reinterpret_cast<v4u*>(smem + spr + CH_SELT - CH_IMG + 1024) = v4u{kmo, vmo, pub, CH_SO + img(c & 7) + (c >> 3) * 8};
    }
    if (w == 0) *reinterpret_cast<unsigned*>(smem + spr + CH_SELT - CH_IMG + 2048) = ~0u;
    BAR();
    const unsigned idx = (lane & 2) ? 0xeeeeu : 0x4444u;
    const unsigned va = CH_IMG + img((c >> 2) + 4 * ((((c & 3) + 1) >> 1) & 1));
    const unsigned pp = lane >> 2, row = (pp & 9) | ((pp & 2) << 1) | ((pp & 4) >> 1);
    auto qo = [&](unsigned hb) __attribute__((always_inline)) -> unsigned { return (16 * w + g) * 8192 + 256 * h + (c ^ (2 * hb + 4 * (g & 1) + 8 * (w >> 1))) * 16; };
    const unsigned o2 = (128 * h + 16 * (lane >> 5) + ((lane >> 3) & 3)) * 1152 + w * 128 + ((lane & 7) ^ (4 * (lane >> 5))) * 16;
    const unsigned oo = (128 * h + 16 * ((row >> 2) & 1) + 4 * (row >> 3) + (row & 3)) * 1152 + w * 128 + (lane & 3) * 16;
    const unsigned q = c >> 2;
    const unsigned tr = wb + ((q & 1) + 2 * (g & 1) + 8 * (g >> 1)) * 64 + (lane & 3) * 8;
    auto to = [&](unsigned t) __attribute__((always_inline)) -> unsigned { return wb + (4 * g + (q & 1)) * 128 + ((t ^ (g & 1)) * 64) + (q >> 1) * 32 + (lane & 3) * 8; };
    auto tq = [&](unsigned t, unsigned a) __attribute__((always_inline)) -> unsigned {
        const unsigned part = 2 * (q >> 1) + a;
        return (part == 3 ? CH_WB3 : part * CH_WB1) + (4 * g + (q & 1)) * 256 + (((4 * w + 2 * t) ^ (2 * (g & 1) + 4 * (q & 1) + 8 * (q >> 1))) * 16) + (lane & 3) * 8;
    };
    const unsigned m0 = CH_IMG + (gam + 4 * rho) * 1152 + (4 * ksl + 2 * tau + nh) * 16 + (w == 3 ? 2 : w) * 384;
    asm volatile(
        "s_nop 4\n"
        "s_mov_b64 s[40:41], %0\n"
        "s_and_b32 s41, s41, 0xffff\n"
        "s_mov_b32 s42, 236978176\n"
        "s_mov_b32 s43, 0x20000\n"
        "s_mov_b64 s[48:49], %6\n"
        "s_mov_b64 s[56:57], %7\n"
        "s_mov_b64 s[60:61], %8\n"
        "s_mov_b64 s[62:63], %9\n"
        "v_mov_b64 v[0:1], %10\n"
        "v_mov_b64 v[2:3], %11\n"
        "v_mov_b64 v[4:5], %12\n"
        "v_mov_b64 v[6:7], %13\n"
        "v_mov_b64 v[8:9], %14\n"
        "v_mov_b64 v[10:11], %15\n"
        "v_mov_b64 v[12:13], %16\n"
        "v_mov_b64 v[14:15], %17\n"
        "v_mov_b64 v[16:17], %18\n"
        "s_mov_b32 s47, 0x20000\n"
        "v_readfirstlane_b32 s82, v2\n"
        "s_lshr_b32 s65, s56, 8\n"
        "s_and_b32 s56, s56, 1\n"
        "s_mov_b32 s64, s65\n"
        "s_cmp_eq_u32 s65, 0\n"
        "s_cbranch_scc1 .Lch_start2_%=\n"
        "s_memrealtime s[36:37]\n"
        "s_waitcnt lgkmcnt(0)\n"
        "s_sub_u32 s66, s36, s57\n"
        "s_cmp_gt_u32 s66, -2000\n"
        "s_cselect_b32 s67, s57, s36\n"
        "s_subb_u32 s67, s67, -1\n"
        "s_mov_b32 s35, 0\n"
        "s_mov_b32 s34, 0\n"
        ".Lch_cala_%=:\n"
        "s_memrealtime s[36:37]\n"
        "s_memtime s[90:91]\n"
        "s_waitcnt lgkmcnt(0)\n"
        "s_sub_u32 s66, s36, s67\n"
        "s_cmp_lt_i32 s66, 0\n"
        "s_cbranch_scc1 .Lch_cala_%=\n"
        "s_add_u32 s67, s36, 5\n"
        "s_sub_u32 s66, s36, s57\n"
        "s_mul_i32 s66, s66, 23\n"
        "s_sub_u32 s66, s90, s66\n"
        "s_cmp_eq_u32 s34, 0\n"
        "s_cselect_b32 s38, s66, s38\n"
        "s_sub_u32 s66, s66, s38\n"
        "s_add_u32 s35, s35, s66\n"
        "s_add_u32 s34, s34, 1\n"
        "s_cmp_lt_u32 s34, 8\n"
        "s_cbranch_scc1 .Lch_cala_%=\n"
        "s_ashr_i32 s35, s35, 3\n"
        "s_add_u32 s38, s38, s35\n"
        ".Lch_start_%=:\n"
        "s_sub_u32 s65, s65, 1\n"
        "s_cbranch_scc1 .Lch_start2_%=\n"
        "s_sleep 16\n"
        "s_branch .Lch_start_%=\n"
        ".Lch_start2_%=:\n"
        "s_mov_b64 s[44:45], %1\n"
        "s_and_b32 s45, s45, 0xffff\n"
        "s_mov_b32 s46, 4718592\n"
        "s_add_u32 m0, s82, 0\n"
        "s_mov_b32 s65, 0\n"
        "buffer_load_dwordx4 v6, s[44:47], s65 offen offset:0 lds\n"
        "s_mov_b32 s65, 31744\n"
        "buffer_load_dwordx4 v7, s[44:47], s65 offen offset:1024 lds\n"
        "s_mov_b32 s65, 63488\n"
        "buffer_load_dwordx4 v6, s[44:47], s65 offen offset:2048 lds\n"
        "s_mov_b32 s65, 95232\n"
        "buffer_load_dwordx4 v7, s[44:47], s65 offen offset:3072 lds\n"
        "s_add_u32 m0, s82, 4096\n"
        "s_mov_b32 s65, 524288\n"
        "buffer_load_dwordx4 v6, s[44:47], s65 offen offset:0 lds\n"
        "s_mov_b32 s65, 556032\n"
        "buffer_load_dwordx4 v7, s[44:47], s65 offen offset:1024 lds\n"
        "s_mov_b32 s65, 587776\n"
        "buffer_load_dwordx4 v6, s[44:47], s65 offen offset:2048 lds\n"
        "s_mov_b32 s65, 619520\n"
        "buffer_load_dwordx4 v7, s[44:47], s65 offen offset:3072 lds\n"
        "s_add_u32 m0, s82, 8192\n"
        "s_mov_b32 s65, 1048576\n"
        "buffer_load_dwordx4 v6, s[44:47], s65 offen offset:0 lds\n"
        "s_mov_b32 s65, 1080320\n"
        "buffer_load_dwordx4 v7, s[44:47], s65 offen offset:1024 lds\n"
        "s_mov_b32 s65, 1112064\n"
        "buffer_load_dwordx4 v6, s[44:47], s65 offen offset:2048 lds\n"
        "s_mov_b32 s65, 1143808\n"
        "buffer_load_dwordx4 v7, s[44:47], s65 offen offset:3072 lds\n"
        "s_cmp_eq_u32 s64, 0\n"
        "s_cbranch_scc0 .Lch_start3_%=\n"
        "s_memrealtime s[36:37]\n"
        "s_waitcnt lgkmcnt(0)\n"
        "s_sub_u32 s66, s36, s57\n"
        "s_cmp_gt_u32 s66, -2000\n"
        "s_cselect_b32 s67, s57, s36\n"
        "s_subb_u32 s67, s67, -1\n"
        "s_mov_b32 s35, 0\n"
        "s_mov_b32 s34, 0\n"
        ".Lch_calb_%=:\n"
        "s_memrealtime s[36:37]\n"
        "s_memtime s[90:91]\n"
        "s_waitcnt lgkmcnt(0)\n"
        "s_sub_u32 s66, s36, s67\n"
        "s_cmp_lt_i32 s66, 0\n"
        "s_cbranch_scc1 .Lch_calb_%=\n"
        "s_add_u32 s67, s36, 5\n"
        "s_sub_u32 s66, s36, s57\n"
        "s_mul_i32 s66, s66, 23\n"
        "s_sub_u32 s66, s90, s66\n"
        "s_cmp_eq_u32 s34, 0\n"
        "s_cselect_b32 s38, s66, s38\n"
        "s_sub_u32 s66, s66, s38\n"
        "s_add_u32 s35, s35, s66\n"
        "s_add_u32 s34, s34, 1\n"
        "s_cmp_lt_u32 s34, 8\n"
        "s_cbranch_scc1 .Lch_calb_%=\n"
        "s_ashr_i32 s35, s35, 3\n"
        "s_add_u32 s38, s38, s35\n"
        ".Lch_start3_%=:\n"
        "s_waitcnt vmcnt(8)\n"
        "s_waitcnt lgkmcnt(0)\n"
        "s_barrier\n"
        "ds_read_b64_tr_b16 a[0:1], v14 offset:0\n"
        "ds_read_b64_tr_b16 a[2:3], v14 offset:512\n"
        "ds_read_b64_tr_b16 a[4:5], v15 offset:0\n"
        "ds_read_b64_tr_b16 a[6:7], v15 offset:512\n"
        "ds_read_b64_tr_b16 a[8:9], v16 offset:0\n"
        "ds_read_b64_tr_b16 a[10:11], v16 offset:512\n"
        "ds_read_b64_tr_b16 a[12:13], v17 offset:0\n"
        "ds_read_b64_tr_b16 a[14:15], v17 offset:512\n"
        "s_waitcnt vmcnt(4)\n"
        "s_waitcnt lgkmcnt(0)\n"
        "s_barrier\n"
        "s_add_u32 m0, s82, 12288\n"
        "s_mov_b32 s65, 1572864\n"
        "buffer_load_dwordx4 v6, s[44:47], s65 offen offset:0 lds\n"
        "s_mov_b32 s65, 1604608\n"
        "buffer_load_dwordx4 v7, s[44:47], s65 offen offset:1024 lds\n"
        "s_mov_b32 s65, 1636352\n"
        "buffer_load_dwordx4 v6, s[44:47], s65 offen offset:2048 lds\n"
        "s_mov_b32 s65, 1668096\n"
        "buffer_load_dwordx4 v7, s[44:47], s65 offen offset:3072 lds\n"
        "ds_read_b64_tr_b16 a[16:17], v14 offset:4096\n"
        "ds_read_b64_tr_b16 a[18:19], v14 offset:4608\n"
        "ds_read_b64_tr_b16 a[20:21], v15 offset:4096\n"
        "ds_read_b64_tr_b16 a[22:23], v15 offset:4608\n"
        "ds_read_b64_tr_b16 a[24:25], v16 offset:4096\n"
        "ds_read_b64_tr_b16 a[26:27], v16 offset:4608\n"
        "ds_read_b64_tr_b16 a[28:29], v17 offset:4096\n"
        "ds_read_b64_tr_b16 a[30:31], v17 offset:4608\n"
        "s_waitcnt vmcnt(4)\n"
        "s_waitcnt lgkmcnt(0)\n"
        "s_barrier\n"
        "s_add_u32 m0, s82, 16384\n"
        "s_mov_b32 s65, 2097152\n"
        "buffer_load_dwordx4 v6, s[44:47], s65 offen offset:0 lds\n"
        "s_mov_b32 s65, 2128896\n"
        "buffer_load_dwordx4 v7, s[44:47], s65 offen offset:1024 lds\n"
        "s_mov_b32 s65, 2160640\n"
        "buffer_load_dwordx4 v6, s[44:47], s65 offen offset:2048 lds\n"
        "s_mov_b32 s65, 2192384\n"
        "buffer_load_dwordx4 v7, s[44:47], s65 offen offset:3072 lds\n"
        "s_mov_b64 s[44:45], %5\n"
        "s_and_b32 s45, s45, 0xffff\n"
        "s_mov_b32 s46, 9216\n"
        "s_mul_i32 s65, s48, 3072\n"
        "s_add_u32 m0, s65, 151552\n"
        "s_cmp_eq_u32 s48, 3\n"
        "s_cbranch_scc1 .Lch_nom_%=\n"
        "buffer_load_dwordx4 v3, s[44:47], s65 offen offset:0 lds\n"
        "buffer_load_dwordx4 v3, s[44:47], s65 offen offset:1024 lds\n"
        "buffer_load_dwordx4 v3, s[44:47], s65 offen offset:2048 lds\n"
        ".Lch_nom_%=:\n"
        "ds_read_b64_tr_b16 a[32:33], v14 offset:8192\n"
        "ds_read_b64_tr_b16 a[34:35], v14 offset:8704\n"
        "ds_read_b64_tr_b16 a[36:37], v15 offset:8192\n"
        "ds_read_b64_tr_b16 a[38:39], v15 offset:8704\n"
        "ds_read_b64_tr_b16 a[40:41], v16 offset:8192\n"
        "ds_read_b64_tr_b16 a[42:43], v16 offset:8704\n"
        "ds_read_b64_tr_b16 a[44:45], v17 offset:8192\n"
        "ds_read_b64_tr_b16 a[46:47], v17 offset:8704\n"
        "s_waitcnt vmcnt(4)\n"
        "s_waitcnt lgkmcnt(0)\n"
        "s_barrier\n"
        "s_mov_b64 s[44:45], %1\n"
        "s_and_b32 s45, s45, 0xffff\n"
        "s_mov_b32 s46, 4718592\n"
        "s_add_u32 m0, s82, 20480\n"
        "s_mov_b32 s65, 2621440\n"
        "buffer_load_dwordx4 v6, s[44:47], s65 offen offset:0 lds\n"
        "s_mov_b32 s65, 2653184\n"
        "buffer_load_dwordx4 v7, s[44:47], s65 offen offset:1024 lds\n"
        "s_mov_b32 s65, 2684928\n"
        "buffer_load_dwordx4 v6, s[44:47], s65 offen offset:2048 lds\n"
        "s_mov_b32 s65, 2716672\n"
        "buffer_load_dwordx4 v7, s[44:47], s65 offen offset:3072 lds\n"
        "ds_read_b64_tr_b16 a[48:49], v14 offset:12288\n"
        "ds_read_b64_tr_b16 a[50:51], v14 offset:12800\n"
        "ds_read_b64_tr_b16 a[52:53], v15 offset:12288\n"
        "ds_read_b64_tr_b16 a[54:55], v15 offset:12800\n"
        "ds_read_b64_tr_b16 a[56:57], v16 offset:12288\n"
        "ds_read_b64_tr_b16 a[58:59], v16 offset:12800\n"
        "ds_read_b64_tr_b16 a[60:61], v17 offset:12288\n"
        "ds_read_b64_tr_b16 a[62:63], v17 offset:12800\n"
        "s_waitcnt vmcnt(4)\n"
        "s_waitcnt lgkmcnt(0)\n"
        "s_barrier\n"
        "s_add_u32 m0, s82, 24576\n"
        "s_mov_b32 s65, 3145728\n"
        "buffer_load_dwordx4 v6, s[44:47], s65 offen offset:0 lds\n"
        "s_mov_b32 s65, 3177472\n"
        "buffer_load_dwordx4 v7, s[44:47], s65 offen offset:1024 lds\n"
        "s_mov_b32 s65, 3209216\n"
        "buffer_load_dwordx4 v6, s[44:47], s65 offen offset:2048 lds\n"
        "s_mov_b32 s65, 3240960\n"
        "buffer_load_dwordx4 v7, s[44:47], s65 offen offset:3072 lds\n"
        "s_getpc_b64 s[44:45]\n"
        ".Lch_pc_%=:\n"
        "s_add_u32 s44, s44, .Lch_code_%=-.Lch_pc_%=\n"
        "s_addc_u32 s45, s45, 0\n"
        "s_and_b32 s45, s45, 0xffff\n"
        "s_and_b32 s44, s44, -4\n"
        "s_mov_b32 s46, .Lch_end_%=-.Lch_code_%=\n"
        "s_mov_b32 s47, 0x20000\n"
        "s_lshl_b32 s65, s48, 10\n"
        "buffer_load_dwordx4 v[64:67], v3, s[44:47], s65 offen\n"
        "s_add_u32 s65, s65, 4096\n"
        "buffer_load_dwordx4 v[68:71], v3, s[44:47], s65 offen\n"
        "s_add_u32 s65, s65, 4096\n"
        "ds_read_b64_tr_b16 a[64:65], v14 offset:16384\n"
        "ds_read_b64_tr_b16 a[66:67], v14 offset:16896\n"
        "ds_read_b64_tr_b16 a[68:69], v15 offset:16384\n"
        "ds_read_b64_tr_b16 a[70:71], v15 offset:16896\n"
        "ds_read_b64_tr_b16 a[72:73], v16 offset:16384\n"
        "ds_read_b64_tr_b16 a[74:75], v16 offset:16896\n"
        "ds_read_b64_tr_b16 a[76:77], v17 offset:16384\n"
        "ds_read_b64_tr_b16 a[78:79], v17 offset:16896\n"
        "s_waitcnt vmcnt(6)\n"
        "s_waitcnt lgkmcnt(0)\n"
        "s_barrier\n"
        "s_mov_b64 s[44:45], %1\n"
        "s_and_b32 s45, s45, 0xffff\n"
        "s_mov_b32 s46, 4718592\n"
        "s_add_u32 m0, s82, 28672\n"
        "s_mov_b32 s65, 3670016\n"
        "buffer_load_dwordx4 v6, s[44:47], s65 offen offset:0 lds\n"
        "s_mov_b32 s65, 3701760\n"
        "buffer_load_dwordx4 v7, s[44:47], s65 offen offset:1024 lds\n"
        "s_mov_b32 s65, 3733504\n"
        "buffer_load_dwordx4 v6, s[44:47], s65 offen offset:2048 lds\n"
        "s_mov_b32 s65, 3765248\n"
        "buffer_load_dwordx4 v7, s[44:47], s65 offen offset:3072 lds\n"
        "ds_read_b64_tr_b16 a[80:81], v14 offset:20480\n"
        "ds_read_b64_tr_b16 a[82:83], v14 offset:20992\n"
        "ds_read_b64_tr_b16 a[84:85], v15 offset:20480\n"
        "ds_read_b64_tr_b16 a[86:87], v15 offset:20992\n"
        "ds_read_b64_tr_b16 a[88:89], v16 offset:20480\n"
        "ds_read_b64_tr_b16 a[90:91], v16 offset:20992\n"
        "ds_read_b64_tr_b16 a[92:93], v17 offset:20480\n"
        "ds_read_b64_tr_b16 a[94:95], v17 offset:20992\n"
        "s_waitcnt vmcnt(6)\n"
        "s_waitcnt lgkmcnt(0)\n"
        "s_barrier\n"
        "s_add_u32 m0, s82, 0\n"
        "s_mov_b32 s65, 4194304\n"
        "buffer_load_dwordx4 v6, s[44:47], s65 offen offset:0 lds\n"
        "s_mov_b32 s65, 4226048\n"
        "buffer_load_dwordx4 v7, s[44:47], s65 offen offset:1024 lds\n"
        "s_mov_b32 s65, 4257792\n"
        "buffer_load_dwordx4 v6, s[44:47], s65 offen offset:2048 lds\n"
        "s_mov_b32 s65, 4289536\n"
        "buffer_load_dwordx4 v7, s[44:47], s65 offen offset:3072 lds\n"
        "ds_read_b64_tr_b16 a[96:97], v14 offset:24576\n"
        "ds_read_b64_tr_b16 a[98:99], v14 offset:25088\n"
        "ds_read_b64_tr_b16 a[100:101], v15 offset:24576\n"
        "ds_read_b64_tr_b16 a[102:103], v15 offset:25088\n"
        "ds_read_b64_tr_b16 a[104:105], v16 offset:24576\n"
        "ds_read_b64_tr_b16 a[106:107], v16 offset:25088\n"
        "ds_read_b64_tr_b16 a[108:109], v17 offset:24576\n"
        "ds_read_b64_tr_b16 a[110:111], v17 offset:25088\n"
        "s_waitcnt vmcnt(4)\n"
        "s_waitcnt lgkmcnt(0)\n"
        "s_barrier\n"
        "s_mov_b64 s[44:45], %2\n"
        "s_and_b32 s45, s45, 0xffff\n"
        "s_mov_b32 s46, 4718592\n"
        "s_add_u32 m0, s82, 4096\n"
        "s_mov_b32 s65, 0\n"
        "buffer_load_dwordx4 v6, s[44:47], s65 offen offset:0 lds\n"
        "s_mov_b32 s65, 31744\n"
        "buffer_load_dwordx4 v7, s[44:47], s65 offen offset:1024 lds\n"
        "s_mov_b32 s65, 63488\n"
        "buffer_load_dwordx4 v6, s[44:47], s65 offen offset:2048 lds\n"
        "s_mov_b32 s65, 95232\n"
        "buffer_load_dwordx4 v7, s[44:47], s65 offen offset:3072 lds\n"
        "ds_read_b64_tr_b16 a[112:113], v14 offset:28672\n"
        "ds_read_b64_tr_b16 a[114:115], v14 offset:29184\n"
        "ds_read_b64_tr_b16 a[116:117], v15 offset:28672\n"
        "ds_read_b64_tr_b16 a[118:119], v15 offset:29184\n"
        "ds_read_b64_tr_b16 a[120:121], v16 offset:28672\n"
        "ds_read_b64_tr_b16 a[122:123], v16 offset:29184\n"
        "ds_read_b64_tr_b16 a[124:125], v17 offset:28672\n"
        "ds_read_b64_tr_b16 a[126:127], v17 offset:29184\n"
        "s_waitcnt vmcnt(4)\n"
        "s_waitcnt lgkmcnt(0)\n"
        "s_barrier\n"
        "s_add_u32 m0, s82, 8192\n"
        "s_mov_b32 s65, 524288\n"
        "buffer_load_dwordx4 v6, s[44:47], s65 offen offset:0 lds\n"
        "s_mov_b32 s65, 556032\n"
        "buffer_load_dwordx4 v7, s[44:47], s65 offen offset:1024 lds\n"
        "s_mov_b32 s65, 587776\n"
        "buffer_load_dwordx4 v6, s[44:47], s65 offen offset:2048 lds\n"
        "s_mov_b32 s65, 619520\n"
        "buffer_load_dwordx4 v7, s[44:47], s65 offen offset:3072 lds\n"
        "ds_read_b64_tr_b16 a[128:129], v14 offset:0\n"
        "ds_read_b64_tr_b16 a[130:131], v14 offset:512\n"
        "ds_read_b64_tr_b16 a[132:133], v15 offset:0\n"
        "ds_read_b64_tr_b16 a[134:135], v15 offset:512\n"
        "ds_read_b64_tr_b16 a[136:137], v16 offset:0\n"
        "ds_read_b64_tr_b16 a[138:139], v16 offset:512\n"
        "ds_read_b64_tr_b16 a[140:141], v17 offset:0\n"
        "ds_read_b64_tr_b16 a[142:143], v17 offset:512\n"
        "s_waitcnt vmcnt(4)\n"
        "s_waitcnt lgkmcnt(0)\n"
        "s_barrier\n"
        "s_add_u32 m0, s82, 12288\n"
        "s_mov_b32 s65, 1048576\n"
        "buffer_load_dwordx4 v6, s[44:47], s65 offen offset:0 lds\n"
        "s_mov_b32 s65, 1080320\n"
        "buffer_load_dwordx4 v7, s[44:47], s65 offen offset:1024 lds\n"
        "s_mov_b32 s65, 1112064\n"
        "buffer_load_dwordx4 v6, s[44:47], s65 offen offset:2048 lds\n"
        "s_mov_b32 s65, 1143808\n"
        "buffer_load_dwordx4 v7, s[44:47], s65 offen offset:3072 lds\n"
        "ds_read_b64_tr_b16 a[144:145], v14 offset:4096\n"
        "ds_read_b64_tr_b16 a[146:147], v14 offset:4608\n"
        "ds_read_b64_tr_b16 a[148:149], v15 offset:4096\n"
        "ds_read_b64_tr_b16 a[150:151], v15 offset:4608\n"
        "ds_read_b64_tr_b16 a[152:153], v16 offset:4096\n"
        "ds_read_b64_tr_b16 a[154:155], v16 offset:4608\n"
        "ds_read_b64_tr_b16 a[156:157], v17 offset:4096\n"
        "ds_read_b64_tr_b16 a[158:159], v17 offset:4608\n"
        "s_waitcnt vmcnt(4)\n"
        "s_waitcnt lgkmcnt(0)\n"
        "s_barrier\n"
        "s_add_u32 m0, s82, 16384\n"
        "s_mov_b32 s65, 1572864\n"
        "buffer_load_dwordx4 v6, s[44:47], s65 offen offset:0 lds\n"
        "s_mov_b32 s65, 1604608\n"
        "buffer_load_dwordx4 v7, s[44:47], s65 offen offset:1024 lds\n"
        "s_mov_b32 s65, 1636352\n"
        "buffer_load_dwordx4 v6, s[44:47], s65 offen offset:2048 lds\n"
        "s_mov_b32 s65, 1668096\n"
        "buffer_load_dwordx4 v7, s[44:47], s65 offen offset:3072 lds\n"
        "ds_read_b64_tr_b16 a[160:161], v14 offset:8192\n"
        "ds_read_b64_tr_b16 a[162:163], v14 offset:8704\n"
        "ds_read_b64_tr_b16 a[164:165], v15 offset:8192\n"
        "ds_read_b64_tr_b16 a[166:167], v15 offset:8704\n"
        "ds_read_b64_tr_b16 a[168:169], v16 offset:8192\n"
        "ds_read_b64_tr_b16 a[170:171], v16 offset:8704\n"
        "ds_read_b64_tr_b16 a[172:173], v17 offset:8192\n"
        "ds_read_b64_tr_b16 a[174:175], v17 offset:8704\n"
        "s_waitcnt vmcnt(4)\n"
        "s_waitcnt lgkmcnt(0)\n"
        "s_barrier\n"
        "s_add_u32 m0, s82, 20480\n"
        "s_mov_b32 s65, 2097152\n"
        "buffer_load_dwordx4 v6, s[44:47], s65 offen offset:0 lds\n"
        "s_mov_b32 s65, 2128896\n"
        "buffer_load_dwordx4 v7, s[44:47], s65 offen offset:1024 lds\n"
        "s_mov_b32 s65, 2160640\n"
        "buffer_load_dwordx4 v6, s[44:47], s65 offen offset:2048 lds\n"
        "s_mov_b32 s65, 2192384\n"
        "buffer_load_dwordx4 v7, s[44:47], s65 offen offset:3072 lds\n"
        "ds_read_b64_tr_b16 a[176:177], v14 offset:12288\n"
        "ds_read_b64_tr_b16 a[178:179], v14 offset:12800\n"
        "ds_read_b64_tr_b16 a[180:181], v15 offset:12288\n"
        "ds_read_b64_tr_b16 a[182:183], v15 offset:12800\n"
        "ds_read_b64_tr_b16 a[184:185], v16 offset:12288\n"
        "ds_read_b64_tr_b16 a[186:187], v16 offset:12800\n"
        "ds_read_b64_tr_b16 a[188:189], v17 offset:12288\n"
        "ds_read_b64_tr_b16 a[190:191], v17 offset:12800\n"
        "s_waitcnt vmcnt(4)\n"
        "s_waitcnt lgkmcnt(0)\n"
        "s_barrier\n"
        "s_add_u32 m0, s82, 24576\n"
        "s_mov_b32 s65, 2621440\n"
        "buffer_load_dwordx4 v6, s[44:47], s65 offen offset:0 lds\n"
        "s_mov_b32 s65, 2653184\n"
        "buffer_load_dwordx4 v7, s[44:47], s65 offen offset:1024 lds\n"
        "s_mov_b32 s65, 2684928\n"
        "buffer_load_dwordx4 v6, s[44:47], s65 offen offset:2048 lds\n"
        "s_mov_b32 s65, 2716672\n"
        "buffer_load_dwordx4 v7, s[44:47], s65 offen offset:3072 lds\n"
        "ds_read_b128 v[24:27], v11 offset:0\n"
        "ds_read_b128 v[28:31], v11 offset:128\n"
        "ds_read_b128 v[32:35], v11 offset:256\n"
        "s_min_u32 s65, s48, 2\n"
        "s_mul_i32 s65, s65, 3072\n"
        "s_add_u32 s65, s65, 151552\n"
        "v_add_u32 v36, s65, v3\n"
        "ds_read_b64_tr_b16 a[192:193], v14 offset:16384\n"
        "ds_read_b64_tr_b16 a[194:195], v14 offset:16896\n"
        "ds_read_b64_tr_b16 a[196:197], v15 offset:16384\n"
        "ds_read_b64_tr_b16 a[198:199], v15 offset:16896\n"
        "ds_read_b64_tr_b16 a[200:201], v16 offset:16384\n"
        "ds_read_b64_tr_b16 a[202:203], v16 offset:16896\n"
        "ds_read_b64_tr_b16 a[204:205], v17 offset:16384\n"
        "ds_read_b64_tr_b16 a[206:207], v17 offset:16896\n"
        "s_waitcnt vmcnt(4)\n"
        "s_waitcnt lgkmcnt(0)\n"
        "s_barrier\n"
        "s_add_u32 m0, s82, 28672\n"
        "s_mov_b32 s65, 3145728\n"
        "buffer_load_dwordx4 v6, s[44:47], s65 offen offset:0 lds\n"
        "s_mov_b32 s65, 3177472\n"
        "buffer_load_dwordx4 v7, s[44:47], s65 offen offset:1024 lds\n"
        "s_mov_b32 s65, 3209216\n"
        "buffer_load_dwordx4 v6, s[44:47], s65 offen offset:2048 lds\n"
        "s_mov_b32 s65, 3240960\n"
        "buffer_load_dwordx4 v7, s[44:47], s65 offen offset:3072 lds\n"
        "ds_write_b128 v36, v[24:27] offset:0\n"
        "ds_write_b128 v36, v[28:31] offset:1024\n"
        "ds_write_b128 v36, v[32:35] offset:2048\n"
        "ds_read_b64_tr_b16 a[208:209], v14 offset:20480\n"
        "ds_read_b64_tr_b16 a[210:211], v14 offset:20992\n"
        "ds_read_b64_tr_b16 a[212:213], v15 offset:20480\n"
        "ds_read_b64_tr_b16 a[214:215], v15 offset:20992\n"
        "ds_read_b64_tr_b16 a[216:217], v16 offset:20480\n"
        "ds_read_b64_tr_b16 a[218:219], v16 offset:20992\n"
        "ds_read_b64_tr_b16 a[220:221], v17 offset:20480\n"
        "ds_read_b64_tr_b16 a[222:223], v17 offset:20992\n"
        "s_waitcnt vmcnt(4)\n"
        "s_waitcnt lgkmcnt(0)\n"
        "s_barrier\n"
        "s_add_u32 m0, s82, 0\n"
        "s_mov_b32 s65, 3670016\n"
        "buffer_load_dwordx4 v6, s[44:47], s65 offen offset:0 lds\n"
        "s_mov_b32 s65, 3701760\n"
        "buffer_load_dwordx4 v7, s[44:47], s65 offen offset:1024 lds\n"
        "s_mov_b32 s65, 3733504\n"
        "buffer_load_dwordx4 v6, s[44:47], s65 offen offset:2048 lds\n"
        "s_mov_b32 s65, 3765248\n"
        "buffer_load_dwordx4 v7, s[44:47], s65 offen offset:3072 lds\n"
        "ds_read_b64_tr_b16 a[224:225], v14 offset:24576\n"
        "ds_read_b64_tr_b16 a[226:227], v14 offset:25088\n"
        "ds_read_b64_tr_b16 a[228:229], v15 offset:24576\n"
        "ds_read_b64_tr_b16 a[230:231], v15 offset:25088\n"
        "ds_read_b64_tr_b16 a[232:233], v16 offset:24576\n"
        "ds_read_b64_tr_b16 a[234:235], v16 offset:25088\n"
        "ds_read_b64_tr_b16 a[236:237], v17 offset:24576\n"
        "ds_read_b64_tr_b16 a[238:239], v17 offset:25088\n"
        "s_waitcnt vmcnt(4)\n"
        "s_waitcnt lgkmcnt(0)\n"
        "s_barrier\n"
        "s_add_u32 m0, s82, 4096\n"
        "s_mov_b32 s65, 4194304\n"
        "buffer_load_dwordx4 v6, s[44:47], s65 offen offset:0 lds\n"
        "s_mov_b32 s65, 4226048\n"
        "buffer_load_dwordx4 v7, s[44:47], s65 offen offset:1024 lds\n"
        "s_mov_b32 s65, 4257792\n"
        "buffer_load_dwordx4 v6, s[44:47], s65 offen offset:2048 lds\n"
        "s_mov_b32 s65, 4289536\n"
        "buffer_load_dwordx4 v7, s[44:47], s65 offen offset:3072 lds\n"
        "ds_read_b64_tr_b16 a[240:241], v14 offset:28672\n"
        "ds_read_b64_tr_b16 a[242:243], v14 offset:29184\n"
        "ds_read_b64_tr_b16 a[244:245], v15 offset:28672\n"
        "ds_read_b64_tr_b16 a[246:247], v15 offset:29184\n"
        "ds_read_b64_tr_b16 a[248:249], v16 offset:28672\n"
        "ds_read_b64_tr_b16 a[250:251], v16 offset:29184\n"
        "ds_read_b64_tr_b16 a[252:253], v17 offset:28672\n"
        "ds_read_b64_tr_b16 a[254:255], v17 offset:29184\n"
        "s_waitcnt vmcnt(4)\n"
        "s_waitcnt lgkmcnt(0)\n"
        "s_barrier\n"
        "s_mov_b64 s[44:45], %4\n"
        "s_and_b32 s45, s45, 0xffff\n"
        "s_mov_b32 s46, 4718592\n"
        "s_add_u32 m0, s82, 8192\n"
        "s_mov_b32 s65, 0\n"
        "buffer_load_dwordx4 v9, s[44:47], s65 offen offset:0 lds\n"
        "s_mov_b32 s65, 3584\n"
        "buffer_load_dwordx4 v9, s[44:47], s65 offen offset:1024 lds\n"
        "s_mov_b32 s65, 7168\n"
        "buffer_load_dwordx4 v9, s[44:47], s65 offen offset:2048 lds\n"
        "s_mov_b32 s65, 10752\n"
        "buffer_load_dwordx4 v9, s[44:47], s65 offen offset:3072 lds\n"
        "ds_read_b64_tr_b16 v[80:81], v14 offset:0\n"
        "ds_read_b64_tr_b16 v[82:83], v14 offset:512\n"
        "ds_read_b64_tr_b16 v[84:85], v15 offset:0\n"
        "ds_read_b64_tr_b16 v[86:87], v15 offset:512\n"
        "ds_read_b64_tr_b16 v[88:89], v16 offset:0\n"
        "ds_read_b64_tr_b16 v[90:91], v16 offset:512\n"
        "ds_read_b64_tr_b16 v[92:93], v17 offset:0\n"
        "ds_read_b64_tr_b16 v[94:95], v17 offset:512\n"
        "s_waitcnt vmcnt(4)\n"
        "s_waitcnt lgkmcnt(0)\n"
        "s_barrier\n"
        "s_add_u32 m0, s82, 12288\n"
        "s_mov_b32 s65, 36864\n"
        "buffer_load_dwordx4 v9, s[44:47], s65 offen offset:0 lds\n"
        "s_mov_b32 s65, 40448\n"
        "buffer_load_dwordx4 v9, s[44:47], s65 offen offset:1024 lds\n"
        "s_mov_b32 s65, 44032\n"
        "buffer_load_dwordx4 v9, s[44:47], s65 offen offset:2048 lds\n"
        "s_mov_b32 s65, 47616\n"
        "buffer_load_dwordx4 v9, s[44:47], s65 offen offset:3072 lds\n"
        "ds_read_b64_tr_b16 v[96:97], v14 offset:4096\n"
        "ds_read_b64_tr_b16 v[98:99], v14 offset:4608\n"
        "ds_read_b64_tr_b16 v[100:101], v15 offset:4096\n"
        "ds_read_b64_tr_b16 v[102:103], v15 offset:4608\n"
        "ds_read_b64_tr_b16 v[104:105], v16 offset:4096\n"
        "ds_read_b64_tr_b16 v[106:107], v16 offset:4608\n"
        "ds_read_b64_tr_b16 v[108:109], v17 offset:4096\n"
        "ds_read_b64_tr_b16 v[110:111], v17 offset:4608\n"
        "s_cmp_lt_u32 s48, 2\n"
        "s_cbranch_scc0 .Lch_noex_%=\n"
        "s_lshl_b32 s66, s48, 6\n"
        "s_add_u32 m0, s82, 32768\n"
        "s_sub_u32 s65, 1024, s66\n"
        "buffer_load_dwordx4 v8, s[44:47], s65 offen lds\n"
        "s_sub_u32 s65, 9216, s66\n"
        "buffer_load_dwordx4 v8, s[44:47], s65 offen offset:1024 lds\n"
        "s_add_u32 m0, s82, 34816\n"
        "s_sub_u32 s65, 37888, s66\n"
        "buffer_load_dwordx4 v8, s[44:47], s65 offen lds\n"
        "s_sub_u32 s65, 46080, s66\n"
        "buffer_load_dwordx4 v8, s[44:47], s65 offen offset:1024 lds\n"
        "s_add_u32 m0, s82, 36864\n"
        "s_sub_u32 s65, 74752, s66\n"
        "buffer_load_dwordx4 v8, s[44:47], s65 offen lds\n"
        "s_sub_u32 s65, 82944, s66\n"
        "buffer_load_dwordx4 v8, s[44:47], s65 offen offset:1024 lds\n"
        "s_add_u32 m0, s82, 38912\n"
        "s_sub_u32 s65, 111616, s66\n"
        "buffer_load_dwordx4 v8, s[44:47], s65 offen lds\n"
        "s_sub_u32 s65, 119808, s66\n"
        "buffer_load_dwordx4 v8, s[44:47], s65 offen offset:1024 lds\n"
        ".Lch_noex_%=:\n"
        "s_waitcnt vmcnt(4)\n"
        "s_waitcnt lgkmcnt(0)\n"
        "s_barrier\n"
        "s_add_u32 m0, s82, 16384\n"
        "s_mov_b32 s65, 73728\n"
        "buffer_load_dwordx4 v9, s[44:47], s65 offen offset:0 lds\n"
        "s_mov_b32 s65, 77312\n"
        "buffer_load_dwordx4 v9, s[44:47], s65 offen offset:1024 lds\n"
        "s_mov_b32 s65, 80896\n"
        "buffer_load_dwordx4 v9, s[44:47], s65 offen offset:2048 lds\n"
        "s_mov_b32 s65, 84480\n"
        "buffer_load_dwordx4 v9, s[44:47], s65 offen offset:3072 lds\n"
        "ds_read_b64_tr_b16 v[112:113], v12 offset:8192\n"
        "ds_read_b64_tr_b16 v[114:115], v12 offset:8448\n"
        "ds_read_b64_tr_b16 v[116:117], v12 offset:10240\n"
        "ds_read_b64_tr_b16 v[118:119], v12 offset:10496\n"
        "ds_read_b64_tr_b16 v[144:145], v13 offset:8192\n"
        "ds_read_b64_tr_b16 v[146:147], v13 offset:8448\n"
        "ds_read_b64_tr_b16 v[148:149], v13 offset:10240\n"
        "ds_read_b64_tr_b16 v[150:151], v13 offset:10496\n"
        "s_waitcnt vmcnt(4)\n"
        "s_waitcnt lgkmcnt(0)\n"
        "s_barrier\n"
        "s_add_u32 m0, s82, 20480\n"
        "s_mov_b32 s65, 110592\n"
        "buffer_load_dwordx4 v9, s[44:47], s65 offen offset:0 lds\n"
        "s_mov_b32 s65, 114176\n"
        "buffer_load_dwordx4 v9, s[44:47], s65 offen offset:1024 lds\n"
        "s_mov_b32 s65, 117760\n"
        "buffer_load_dwordx4 v9, s[44:47], s65 offen offset:2048 lds\n"
        "s_mov_b32 s65, 121344\n"
        "buffer_load_dwordx4 v9, s[44:47], s65 offen offset:3072 lds\n"
        "ds_read_b64_tr_b16 v[120:121], v12 offset:12288\n"
        "ds_read_b64_tr_b16 v[122:123], v12 offset:12544\n"
        "ds_read_b64_tr_b16 v[124:125], v12 offset:14336\n"
        "ds_read_b64_tr_b16 v[126:127], v12 offset:14592\n"
        "ds_read_b64_tr_b16 v[152:153], v13 offset:12288\n"
        "ds_read_b64_tr_b16 v[154:155], v13 offset:12544\n"
        "ds_read_b64_tr_b16 v[156:157], v13 offset:14336\n"
        "ds_read_b64_tr_b16 v[158:159], v13 offset:14592\n"
        "s_waitcnt vmcnt(4)\n"
        "s_waitcnt lgkmcnt(0)\n"
        "s_barrier\n"
        "s_add_u32 m0, s82, 24576\n"
        "s_mov_b32 s65, 512\n"
        "buffer_load_dwordx4 v9, s[44:47], s65 offen offset:0 lds\n"
        "s_mov_b32 s65, 4096\n"
        "buffer_load_dwordx4 v9, s[44:47], s65 offen offset:1024 lds\n"
        "s_mov_b32 s65, 7680\n"
        "buffer_load_dwordx4 v9, s[44:47], s65 offen offset:2048 lds\n"
        "s_mov_b32 s65, 11264\n"
        "buffer_load_dwordx4 v9, s[44:47], s65 offen offset:3072 lds\n"
        "ds_read_b64_tr_b16 v[128:129], v12 offset:16384\n"
        "ds_read_b64_tr_b16 v[130:131], v12 offset:16640\n"
        "ds_read_b64_tr_b16 v[132:133], v12 offset:18432\n"
        "ds_read_b64_tr_b16 v[134:135], v12 offset:18688\n"
        "ds_read_b64_tr_b16 v[160:161], v13 offset:16384\n"
        "ds_read_b64_tr_b16 v[162:163], v13 offset:16640\n"
        "ds_read_b64_tr_b16 v[164:165], v13 offset:18432\n"
        "ds_read_b64_tr_b16 v[166:167], v13 offset:18688\n"
        "s_waitcnt vmcnt(4)\n"
        "s_waitcnt lgkmcnt(0)\n"
        "s_barrier\n"
        "s_add_u32 m0, s82, 28672\n"
        "s_mov_b32 s65, 37376\n"
        "buffer_load_dwordx4 v9, s[44:47], s65 offen offset:0 lds\n"
        "s_mov_b32 s65, 40960\n"
        "buffer_load_dwordx4 v9, s[44:47], s65 offen offset:1024 lds\n"
        "s_mov_b32 s65, 44544\n"
        "buffer_load_dwordx4 v9, s[44:47], s65 offen offset:2048 lds\n"
        "s_mov_b32 s65, 48128\n"
        "buffer_load_dwordx4 v9, s[44:47], s65 offen offset:3072 lds\n"
        "ds_read_b64_tr_b16 v[136:137], v12 offset:20480\n"
        "ds_read_b64_tr_b16 v[138:139], v12 offset:20736\n"
        "ds_read_b64_tr_b16 v[140:141], v12 offset:22528\n"
        "ds_read_b64_tr_b16 v[142:143], v12 offset:22784\n"
        "ds_read_b64_tr_b16 v[168:169], v13 offset:20480\n"
        "ds_read_b64_tr_b16 v[170:171], v13 offset:20736\n"
        "ds_read_b64_tr_b16 v[172:173], v13 offset:22528\n"
        "ds_read_b64_tr_b16 v[174:175], v13 offset:22784\n"
        "s_waitcnt vmcnt(4)\n"
        "s_waitcnt lgkmcnt(0)\n"
        "s_barrier\n"
        "s_add_u32 m0, s82, 0\n"
        "s_mov_b32 s65, 74240\n"
        "buffer_load_dwordx4 v9, s[44:47], s65 offen offset:0 lds\n"
        "s_mov_b32 s65, 77824\n"
        "buffer_load_dwordx4 v9, s[44:47], s65 offen offset:1024 lds\n"
        "s_mov_b32 s65, 81408\n"
        "buffer_load_dwordx4 v9, s[44:47], s65 offen offset:2048 lds\n"
        "s_mov_b32 s65, 84992\n"
        "buffer_load_dwordx4 v9, s[44:47], s65 offen offset:3072 lds\n"
        "ds_read_b64_tr_b16 v[176:177], v12 offset:24576\n"
        "ds_read_b64_tr_b16 v[178:179], v12 offset:24832\n"
        "ds_read_b64_tr_b16 v[180:181], v12 offset:26624\n"
        "ds_read_b64_tr_b16 v[182:183], v12 offset:26880\n"
        "ds_read_b64_tr_b16 v[208:209], v13 offset:24576\n"
        "ds_read_b64_tr_b16 v[210:211], v13 offset:24832\n"
        "ds_read_b64_tr_b16 v[212:213], v13 offset:26624\n"
        "ds_read_b64_tr_b16 v[214:215], v13 offset:26880\n"
        "s_waitcnt vmcnt(4)\n"
        "s_waitcnt lgkmcnt(0)\n"
        "s_barrier\n"
        "s_add_u32 m0, s82, 4096\n"
        "s_mov_b32 s65, 111104\n"
        "buffer_load_dwordx4 v9, s[44:47], s65 offen offset:0 lds\n"
        "s_mov_b32 s65, 114688\n"
        "buffer_load_dwordx4 v9, s[44:47], s65 offen offset:1024 lds\n"
        "s_mov_b32 s65, 118272\n"
        "buffer_load_dwordx4 v9, s[44:47], s65 offen offset:2048 lds\n"
        "s_mov_b32 s65, 121856\n"
        "buffer_load_dwordx4 v9, s[44:47], s65 offen offset:3072 lds\n"
        "ds_read_b64_tr_b16 v[184:185], v12 offset:28672\n"
        "ds_read_b64_tr_b16 v[186:187], v12 offset:28928\n"
        "ds_read_b64_tr_b16 v[188:189], v12 offset:30720\n"
        "ds_read_b64_tr_b16 v[190:191], v12 offset:30976\n"
        "ds_read_b64_tr_b16 v[216:217], v13 offset:28672\n"
        "ds_read_b64_tr_b16 v[218:219], v13 offset:28928\n"
        "ds_read_b64_tr_b16 v[220:221], v13 offset:30720\n"
        "ds_read_b64_tr_b16 v[222:223], v13 offset:30976\n"
        "s_waitcnt vmcnt(4)\n"
        "s_waitcnt lgkmcnt(0)\n"
        "s_barrier\n"
        "s_mov_b64 s[44:45], %3\n"
        "s_and_b32 s45, s45, 0xffff\n"
        "s_mov_b32 s46, 4718592\n"
        "s_add_u32 m0, s82, 8192\n"
        "s_mov_b32 s65, 4194304\n"
        "buffer_load_dwordx4 v6, s[44:47], s65 offen offset:0 lds\n"
        "s_mov_b32 s65, 4226048\n"
        "buffer_load_dwordx4 v7, s[44:47], s65 offen offset:1024 lds\n"
        "s_mov_b32 s65, 4257792\n"
        "buffer_load_dwordx4 v6, s[44:47], s65 offen offset:2048 lds\n"
        "s_mov_b32 s65, 4289536\n"
        "buffer_load_dwordx4 v7, s[44:47], s65 offen offset:3072 lds\n"
        "ds_read_b64_tr_b16 v[192:193], v12 offset:0\n"
        "ds_read_b64_tr_b16 v[194:195], v12 offset:256\n"
        "ds_read_b64_tr_b16 v[196:197], v12 offset:2048\n"
        "ds_read_b64_tr_b16 v[198:199], v12 offset:2304\n"
        "ds_read_b64_tr_b16 v[224:225], v13 offset:0\n"
        "ds_read_b64_tr_b16 v[226:227], v13 offset:256\n"
        "ds_read_b64_tr_b16 v[228:229], v13 offset:2048\n"
        "ds_read_b64_tr_b16 v[230:231], v13 offset:2304\n"
        "s_cmp_lt_u32 s48, 2\n"
        "s_cbranch_scc0 .Lch_noex2_%=\n"
        "ds_read_b64_tr_b16 v[24:25], v10 offset:32768\n"
        "ds_read_b64_tr_b16 v[26:27], v10 offset:33024\n"
        "ds_read_b64_tr_b16 v[28:29], v10 offset:33792\n"
        "ds_read_b64_tr_b16 v[30:31], v10 offset:34048\n"
        "ds_read_b64_tr_b16 v[32:33], v10 offset:34816\n"
        "ds_read_b64_tr_b16 v[34:35], v10 offset:35072\n"
        "ds_read_b64_tr_b16 v[36:37], v10 offset:35840\n"
        "ds_read_b64_tr_b16 v[38:39], v10 offset:36096\n"
        "ds_read_b64_tr_b16 v[40:41], v10 offset:36864\n"
        "ds_read_b64_tr_b16 v[42:43], v10 offset:37120\n"
        "ds_read_b64_tr_b16 v[44:45], v10 offset:37888\n"
        "ds_read_b64_tr_b16 v[46:47], v10 offset:38144\n"
        "ds_read_b64_tr_b16 v[48:49], v10 offset:38912\n"
        "ds_read_b64_tr_b16 v[50:51], v10 offset:39168\n"
        "ds_read_b64_tr_b16 v[52:53], v10 offset:39936\n"
        "ds_read_b64_tr_b16 v[54:55], v10 offset:40192\n"
        "s_waitcnt lgkmcnt(0)\n"
        "ds_write_b128 v2, v[24:27] offset:32768\n"
        "ds_write_b128 v2, v[28:31] offset:33792\n"
        "ds_write_b128 v2, v[32:35] offset:34816\n"
        "ds_write_b128 v2, v[36:39] offset:35840\n"
        "ds_write_b128 v2, v[40:43] offset:36864\n"
        "ds_write_b128 v2, v[44:47] offset:37888\n"
        "ds_write_b128 v2, v[48:51] offset:38912\n"
        "ds_write_b128 v2, v[52:55] offset:39936\n"
        ".Lch_noex2_%=:\n"
        "s_waitcnt vmcnt(4)\n"
        "s_waitcnt lgkmcnt(0)\n"
        "s_barrier\n"
        "s_add_u32 m0, s82, 12288\n"
        "s_mov_b32 s65, 1572864\n"
        "buffer_load_dwordx4 v6, s[44:47], s65 offen offset:0 lds\n"
        "s_mov_b32 s65, 1604608\n"
        "buffer_load_dwordx4 v7, s[44:47], s65 offen offset:1024 lds\n"
        "s_mov_b32 s65, 1636352\n"
        "buffer_load_dwordx4 v6, s[44:47], s65 offen offset:2048 lds\n"
        "s_mov_b32 s65, 1668096\n"
        "buffer_load_dwordx4 v7, s[44:47], s65 offen offset:3072 lds\n"
        "ds_read_b64_tr_b16 v[200:201], v12 offset:4096\n"
        "ds_read_b64_tr_b16 v[202:203], v12 offset:4352\n"
        "ds_read_b64_tr_b16 v[204:205], v12 offset:6144\n"
        "ds_read_b64_tr_b16 v[206:207], v12 offset:6400\n"
        "ds_read_b64_tr_b16 v[232:233], v13 offset:4096\n"
        "ds_read_b64_tr_b16 v[234:235], v13 offset:4352\n"
        "ds_read_b64_tr_b16 v[236:237], v13 offset:6144\n"
        "ds_read_b64_tr_b16 v[238:239], v13 offset:6400\n"
        "s_waitcnt vmcnt(4)\n"
        "s_waitcnt lgkmcnt(0)\n"
        "s_barrier\n"
        "s_add_u32 m0, s82, 16384\n"
        "s_mov_b32 s65, 2097152\n"
        "buffer_load_dwordx4 v6, s[44:47], s65 offen offset:0 lds\n"
        "s_mov_b32 s65, 2128896\n"
        "buffer_load_dwordx4 v7, s[44:47], s65 offen offset:1024 lds\n"
        "s_mov_b32 s65, 2160640\n"
        "buffer_load_dwordx4 v6, s[44:47], s65 offen offset:2048 lds\n"
        "s_mov_b32 s65, 2192384\n"
        "buffer_load_dwordx4 v7, s[44:47], s65 offen offset:3072 lds\n"
        "ds_read_b64_tr_b16 v[240:241], v14 offset:8192\n"
        "ds_read_b64_tr_b16 v[242:243], v14 offset:8704\n"
        "ds_read_b64_tr_b16 v[244:245], v15 offset:8192\n"
        "ds_read_b64_tr_b16 v[246:247], v15 offset:8704\n"
        "ds_read_b64_tr_b16 v[248:249], v16 offset:8192\n"
        "ds_read_b64_tr_b16 v[250:251], v16 offset:8704\n"
        "ds_read_b64_tr_b16 v[252:253], v17 offset:8192\n"
        "ds_read_b64_tr_b16 v[254:255], v17 offset:8704\n"
        "s_waitcnt vmcnt(4)\n"
        "s_waitcnt lgkmcnt(0)\n"
        "s_barrier\n"
        "s_add_u32 m0, s82, 20480\n"
        "s_mov_b32 s65, 2621440\n"
        "buffer_load_dwordx4 v6, s[44:47], s65 offen offset:0 lds\n"
        "s_mov_b32 s65, 2653184\n"
        "buffer_load_dwordx4 v7, s[44:47], s65 offen offset:1024 lds\n"
        "s_mov_b32 s65, 2684928\n"
        "buffer_load_dwordx4 v6, s[44:47], s65 offen offset:2048 lds\n"
        "s_mov_b32 s65, 2716672\n"
        "buffer_load_dwordx4 v7, s[44:47], s65 offen offset:3072 lds\n"
        "ds_read_b64_tr_b16 v[40:41], v14 offset:12288\n"
        "ds_read_b64_tr_b16 v[42:43], v14 offset:12800\n"
        "ds_read_b64_tr_b16 v[44:45], v15 offset:12288\n"
        "ds_read_b64_tr_b16 v[46:47], v15 offset:12800\n"
        "ds_read_b64_tr_b16 v[48:49], v16 offset:12288\n"
        "ds_read_b64_tr_b16 v[50:51], v16 offset:12800\n"
        "ds_read_b64_tr_b16 v[52:53], v17 offset:12288\n"
        "ds_read_b64_tr_b16 v[54:55], v17 offset:12800\n"
        "s_waitcnt vmcnt(4)\n"
        "s_waitcnt lgkmcnt(0)\n"
        "s_barrier\n"
        "ds_write_b128 v2, v[40:43] offset:12288\n"
        "ds_write_b128 v2, v[44:47] offset:13312\n"
        "ds_write_b128 v2, v[48:51] offset:14336\n"
        "ds_write_b128 v2, v[52:55] offset:15360\n"
        "s_add_u32 m0, s82, 24576\n"
        "s_mov_b32 s65, 3145728\n"
        "buffer_load_dwordx4 v6, s[44:47], s65 offen offset:0 lds\n"
        "s_mov_b32 s65, 3177472\n"
        "buffer_load_dwordx4 v7, s[44:47], s65 offen offset:1024 lds\n"
        "s_mov_b32 s65, 3209216\n"
        "buffer_load_dwordx4 v6, s[44:47], s65 offen offset:2048 lds\n"
        "s_mov_b32 s65, 3240960\n"
        "buffer_load_dwordx4 v7, s[44:47], s65 offen offset:3072 lds\n"
        "ds_read_b64_tr_b16 v[24:25], v14 offset:16384\n"
        "ds_read_b64_tr_b16 v[26:27], v14 offset:16896\n"
        "ds_read_b64_tr_b16 v[28:29], v15 offset:16384\n"
        "ds_read_b64_tr_b16 v[30:31], v15 offset:16896\n"
        "ds_read_b64_tr_b16 v[32:33], v16 offset:16384\n"
        "ds_read_b64_tr_b16 v[34:35], v16 offset:16896\n"
        "ds_read_b64_tr_b16 v[36:37], v17 offset:16384\n"
        "ds_read_b64_tr_b16 v[38:39], v17 offset:16896\n"
        "s_waitcnt vmcnt(4)\n"
        "s_waitcnt lgkmcnt(0)\n"
        "s_barrier\n"
        "ds_write_b128 v2, v[24:27] offset:16384\n"
        "ds_write_b128 v2, v[28:31] offset:17408\n"
        "ds_write_b128 v2, v[32:35] offset:18432\n"
        "ds_write_b128 v2, v[36:39] offset:19456\n"
        "s_add_u32 m0, s82, 28672\n"
        "s_mov_b32 s65, 3670016\n"
        "buffer_load_dwordx4 v6, s[44:47], s65 offen offset:0 lds\n"
        "s_mov_b32 s65, 3701760\n"
        "buffer_load_dwordx4 v7, s[44:47], s65 offen offset:1024 lds\n"
        "s_mov_b32 s65, 3733504\n"
        "buffer_load_dwordx4 v6, s[44:47], s65 offen offset:2048 lds\n"
        "s_mov_b32 s65, 3765248\n"
        "buffer_load_dwordx4 v7, s[44:47], s65 offen offset:3072 lds\n"
        "ds_read_b64_tr_b16 v[40:41], v14 offset:20480\n"
        "ds_read_b64_tr_b16 v[42:43], v14 offset:20992\n"
        "ds_read_b64_tr_b16 v[44:45], v15 offset:20480\n"
        "ds_read_b64_tr_b16 v[46:47], v15 offset:20992\n"
        "ds_read_b64_tr_b16 v[48:49], v16 offset:20480\n"
        "ds_read_b64_tr_b16 v[50:51], v16 offset:20992\n"
        "ds_read_b64_tr_b16 v[52:53], v17 offset:20480\n"
        "ds_read_b64_tr_b16 v[54:55], v17 offset:20992\n"
        "s_waitcnt vmcnt(4)\n"
        "s_waitcnt lgkmcnt(0)\n"
        "s_barrier\n"
        "ds_write_b128 v2, v[40:43] offset:20480\n"
        "ds_write_b128 v2, v[44:47] offset:21504\n"
        "ds_write_b128 v2, v[48:51] offset:22528\n"
        "ds_write_b128 v2, v[52:55] offset:23552\n"
        "s_add_u32 m0, s82, 0\n"
        "s_mov_b32 s65, 0\n"
        "buffer_load_dwordx4 v6, s[44:47], s65 offen offset:0 lds\n"
        "s_mov_b32 s65, 31744\n"
        "buffer_load_dwordx4 v7, s[44:47], s65 offen offset:1024 lds\n"
        "s_mov_b32 s65, 63488\n"
        "buffer_load_dwordx4 v6, s[44:47], s65 offen offset:2048 lds\n"
        "s_mov_b32 s65, 95232\n"
        "buffer_load_dwordx4 v7, s[44:47], s65 offen offset:3072 lds\n"
        "ds_read_b64_tr_b16 v[24:25], v14 offset:24576\n"
        "ds_read_b64_tr_b16 v[26:27], v14 offset:25088\n"
        "ds_read_b64_tr_b16 v[28:29], v15 offset:24576\n"
        "ds_read_b64_tr_b16 v[30:31], v15 offset:25088\n"
        "ds_read_b64_tr_b16 v[32:33], v16 offset:24576\n"
        "ds_read_b64_tr_b16 v[34:35], v16 offset:25088\n"
        "ds_read_b64_tr_b16 v[36:37], v17 offset:24576\n"
        "ds_read_b64_tr_b16 v[38:39], v17 offset:25088\n"
        "s_waitcnt vmcnt(4)\n"
        "s_waitcnt lgkmcnt(0)\n"
        "s_barrier\n"
        "ds_write_b128 v2, v[24:27] offset:24576\n"
        "ds_write_b128 v2, v[28:31] offset:25600\n"
        "ds_write_b128 v2, v[32:35] offset:26624\n"
        "ds_write_b128 v2, v[36:39] offset:27648\n"
        "s_add_u32 m0, s82, 4096\n"
        "s_mov_b32 s65, 524288\n"
        "buffer_load_dwordx4 v6, s[44:47], s65 offen offset:0 lds\n"
        "s_mov_b32 s65, 556032\n"
        "buffer_load_dwordx4 v7, s[44:47], s65 offen offset:1024 lds\n"
        "s_mov_b32 s65, 587776\n"
        "buffer_load_dwordx4 v6, s[44:47], s65 offen offset:2048 lds\n"
        "s_mov_b32 s65, 619520\n"
        "buffer_load_dwordx4 v7, s[44:47], s65 offen offset:3072 lds\n"
        "ds_read_b64_tr_b16 v[40:41], v14 offset:28672\n"
        "ds_read_b64_tr_b16 v[42:43], v14 offset:29184\n"
        "ds_read_b64_tr_b16 v[44:45], v15 offset:28672\n"
        "ds_read_b64_tr_b16 v[46:47], v15 offset:29184\n"
        "ds_read_b64_tr_b16 v[48:49], v16 offset:28672\n"
        "ds_read_b64_tr_b16 v[50:51], v16 offset:29184\n"
        "ds_read_b64_tr_b16 v[52:53], v17 offset:28672\n"
        "ds_read_b64_tr_b16 v[54:55], v17 offset:29184\n"
        "s_waitcnt vmcnt(4)\n"
        "s_waitcnt lgkmcnt(0)\n"
        "s_barrier\n"
        "ds_write_b128 v2, v[40:43] offset:28672\n"
        "ds_write_b128 v2, v[44:47] offset:29696\n"
        "ds_write_b128 v2, v[48:51] offset:30720\n"
        "ds_write_b128 v2, v[52:55] offset:31744\n"
        "s_add_u32 m0, s82, 8192\n"
        "s_mov_b32 s65, 1048576\n"
        "buffer_load_dwordx4 v6, s[44:47], s65 offen offset:0 lds\n"
        "s_mov_b32 s65, 1080320\n"
        "buffer_load_dwordx4 v7, s[44:47], s65 offen offset:1024 lds\n"
        "s_mov_b32 s65, 1112064\n"
        "buffer_load_dwordx4 v6, s[44:47], s65 offen offset:2048 lds\n"
        "s_mov_b32 s65, 1143808\n"
        "buffer_load_dwordx4 v7, s[44:47], s65 offen offset:3072 lds\n"
        "ds_read_b64_tr_b16 v[24:25], v14 offset:0\n"
        "ds_read_b64_tr_b16 v[26:27], v14 offset:512\n"
        "ds_read_b64_tr_b16 v[28:29], v15 offset:0\n"
        "ds_read_b64_tr_b16 v[30:31], v15 offset:512\n"
        "ds_read_b64_tr_b16 v[32:33], v16 offset:0\n"
        "ds_read_b64_tr_b16 v[34:35], v16 offset:512\n"
        "ds_read_b64_tr_b16 v[36:37], v17 offset:0\n"
        "ds_read_b64_tr_b16 v[38:39], v17 offset:512\n"
        "s_waitcnt vmcnt(4)\n"
        "s_waitcnt lgkmcnt(0)\n"
        "s_barrier\n"
        "ds_write_b128 v2, v[24:27] offset:0\n"
        "ds_write_b128 v2, v[28:31] offset:1024\n"
        "ds_write_b128 v2, v[32:35] offset:2048\n"
        "ds_write_b128 v2, v[36:39] offset:3072\n"
        "v_lshlrev_b32 v60, 3, v3\n"
        "s_mov_b32 s65, 32768\n"
        "buffer_load_dword v61, v60, s[40:43], s65 offen sc1\n"
        "s_mov_b32 s65, 40960\n"
        "buffer_load_dword v62, v60, s[40:43], s65 offen sc1\n"
        "ds_read_b64_tr_b16 v[40:41], v14 offset:4096\n"
        "ds_read_b64_tr_b16 v[42:43], v14 offset:4608\n"
        "ds_read_b64_tr_b16 v[44:45], v15 offset:4096\n"
        "ds_read_b64_tr_b16 v[46:47], v15 offset:4608\n"
        "ds_read_b64_tr_b16 v[48:49], v16 offset:4096\n"
        "ds_read_b64_tr_b16 v[50:51], v16 offset:4608\n"
        "ds_read_b64_tr_b16 v[52:53], v17 offset:4096\n"
        "ds_read_b64_tr_b16 v[54:55], v17 offset:4608\n"
        "s_waitcnt vmcnt(2)\n"
        "s_waitcnt lgkmcnt(0)\n"
        "s_barrier\n"
        "ds_write_b128 v2, v[40:43] offset:4096\n"
        "ds_write_b128 v2, v[44:47] offset:5120\n"
        "ds_write_b128 v2, v[48:51] offset:6144\n"
        "ds_write_b128 v2, v[52:55] offset:7168\n"
        "ds_read_b64_tr_b16 v[24:25], v14 offset:8192\n"
        "ds_read_b64_tr_b16 v[26:27], v14 offset:8704\n"
        "ds_read_b64_tr_b16 v[28:29], v15 offset:8192\n"
        "ds_read_b64_tr_b16 v[30:31], v15 offset:8704\n"
        "ds_read_b64_tr_b16 v[32:33], v16 offset:8192\n"
        "ds_read_b64_tr_b16 v[34:35], v16 offset:8704\n"
        "ds_read_b64_tr_b16 v[36:37], v17 offset:8192\n"
        "ds_read_b64_tr_b16 v[38:39], v17 offset:8704\n"
        "s_waitcnt lgkmcnt(0)\n"
        "s_barrier\n"
        "ds_write_b128 v2, v[24:27] offset:8192\n"
        "ds_write_b128 v2, v[28:31] offset:9216\n"
        "ds_write_b128 v2, v[32:35] offset:10240\n"
        "ds_write_b128 v2, v[36:39] offset:11264\n"
        "s_waitcnt vmcnt(0) lgkmcnt(0)\n"
        "v_lshrrev_b32 v12, 4, v3\n"
        "s_mov_b32 s50, s56\n"
        "v_lshlrev_b32 v14, 7, v12\n"
        "s_sub_u32 s66, 0, s50\n"
        "s_mov_b32 s52, 32768\n"
        "v_cmp_gt_i32 vcc, 0, v61\n"
        "s_cbranch_vccz .Lch_pg0_ok_%=\n"
        "s_mov_b32 s64, 0\n"
        ".Lch_pg0_top_%=:\n"
        "buffer_load_dword v61, v14, s[40:43], s52 offen sc1\n"
        "s_waitcnt vmcnt(0)\n"
        "v_cmp_gt_i32 vcc, 0, v61\n"
        "s_cbranch_vccz .Lch_pg0_ok_%=\n"
        "s_add_u32 s64, s64, 1\n"
        "s_and_b32 s65, s64, 255\n"
        "s_cbranch_scc1 .Lch_pg0_go_%=\n"
        "s_mov_b32 s65, 49152\n"
        "s_cmp_lt_u32 s64, 1048576\n"
        "s_cbranch_scc1 .Lch_pg0_chk_%=\n"
        "v_mov_b32 v61, 1\n"
        "buffer_store_dword v61, off, s[40:43], s65 sc1\n"
        "s_waitcnt vmcnt(0)\n"
        ".Lch_pg0_chk_%=:\n"
        "buffer_load_dword v61, off, s[40:43], s65 sc1\n"
        "s_waitcnt vmcnt(0)\n"
        "v_readfirstlane_b32 s65, v61\n"
        "s_nop 0\n"
        "s_cmp_lg_u32 s65, 1\n"
        "s_cbranch_scc1 .Lch_pg0_go_%=\n"
        "s_endpgm\n"
        ".Lch_pg0_go_%=:\n"
        "s_sleep 2\n"
        "s_branch .Lch_pg0_top_%=\n"
        ".Lch_pg0_ok_%=:\n"
        "v_cmp_eq_u32 s[52:53], 1, v61\n"
        "s_nop 1\n"
        "s_mov_b32 s54, 40960\n"
        "v_cmp_gt_i32 vcc, 0, v62\n"
        "s_cbranch_vccz .Lch_pg1_ok_%=\n"
        "s_mov_b32 s64, 0\n"
        ".Lch_pg1_top_%=:\n"
        "buffer_load_dword v62, v14, s[40:43], s54 offen sc1\n"
        "s_waitcnt vmcnt(0)\n"
        "v_cmp_gt_i32 vcc, 0, v62\n"
        "s_cbranch_vccz .Lch_pg1_ok_%=\n"
        "s_add_u32 s64, s64, 1\n"
        "s_and_b32 s65, s64, 255\n"
        "s_cbranch_scc1 .Lch_pg1_go_%=\n"
        "s_mov_b32 s65, 49152\n"
        "s_cmp_lt_u32 s64, 1048576\n"
        "s_cbranch_scc1 .Lch_pg1_chk_%=\n"
        "v_mov_b32 v62, 1\n"
        "buffer_store_dword v62, off, s[40:43], s65 sc1\n"
        "s_waitcnt vmcnt(0)\n"
        ".Lch_pg1_chk_%=:\n"
        "buffer_load_dword v62, off, s[40:43], s65 sc1\n"
        "s_waitcnt vmcnt(0)\n"
        "v_readfirstlane_b32 s65, v62\n"
        "s_nop 0\n"
        "s_cmp_lg_u32 s65, 1\n"
        "s_cbranch_scc1 .Lch_pg1_go_%=\n"
        "s_endpgm\n"
        ".Lch_pg1_go_%=:\n"
        "s_sleep 2\n"
        "s_branch .Lch_pg1_top_%=\n"
        ".Lch_pg1_ok_%=:\n"
        "v_cmp_eq_u32 s[54:55], 1, v62\n"
        "s_nop 1\n"
        "s_mov_b32 vcc_lo, 0x55555555\n"
        "s_mov_b32 vcc_hi, 0x55555555\n"
        "s_xor_b64 vcc, vcc, s[52:53]\n"
        "s_bcnt1_i32_b64 s65, vcc\n"
        "s_sub_u32 s64, s65, 23\n"
        "s_cmp_lt_u32 s64, 19\n"
        "s_cbranch_scc1 .Lch_rawmap_%=\n"
        "s_cmp_lt_u32 s65, 32\n"
        "s_cselect_b32 s65, 0, -1\n"
        "s_xor_b32 s52, s65, 0x55555555\n"
        "s_xor_b32 s53, s65, 0x55555555\n"
        "s_xor_b32 s54, s65, 0x55555555\n"
        "s_xor_b32 s55, s65, 0xaaaaaaaa\n"
        ".Lch_rawmap_%=:\n"
        "s_xor_b32 s52, s52, s66\n"
        "s_xor_b32 s53, s53, s66\n"
        "s_xor_b32 s54, s54, s66\n"
        "s_xor_b32 s55, s55, s66\n"
        "s_mov_b32 s58, 0xff00ff00\n"
        "s_mov_b32 s59, 0xff00ff00\n"
        "s_and_b32 s67, s55, 1\n"
        "s_lshl_b32 s67, s67, 13\n"
        "s_lshl_b32 s65, s49, 2\n"
        "s_add_u32 s65, s65, s48\n"
        "s_lshl_b32 s65, s65, 7\n"
        "s_lshr_b32 s66, s65, 13\n"
        "s_lshl_b32 s66, s66, 13\n"
        "s_add_u32 s66, s66, s65\n"
        "s_add_u32 s66, s66, s67\n"
        "s_add_u32 s78, s66, 180224\n"
        "s_add_u32 s79, s66, 245760\n"
        "s_lshl_b32 s83, s49, 14\n"
        "s_lshl_b32 s65, s48, 11\n"
        "s_add_u32 s83, s83, s65\n"
        "s_add_u32 s83, s83, 524288\n"
        "s_lshl_b32 s65, s49, 12\n"
        "s_lshr_b32 s84, s65, 13\n"
        "s_lshl_b32 s84, s84, 13\n"
        "s_add_u32 s84, s84, s65\n"
        "s_lshl_b32 s65, s48, 9\n"
        "s_add_u32 s84, s84, s65\n"
        "s_add_u32 s84, s84, 1572864\n"
        "s_lshr_b32 s65, s49, 2\n"
        "s_add_u32 s65, s65, 64\n"
        "s_cmp_eq_u32 s48, 2\n"
        "s_cselect_b32 s65, s49, s65\n"
        "s_add_u32 s66, s49, 32\n"
        "s_cmp_eq_u32 s48, 3\n"
        "s_cselect_b32 s65, s66, s65\n"
        "s_lshl_b32 s85, s65, 7\n"
        "s_cmp_eq_u32 s48, 0\n"
        "s_cselect_b32 s66, 57344, 0\n"
        "s_add_u32 s85, s85, s66\n"
        "s_add_u32 s85, s85, s67\n"
        "s_add_u32 s85, s85, 1130496\n"
        "s_lshr_b32 s66, s49, 3\n"
        "s_lshl_b32 s54, s66, 14\n"
        "s_lshl_b32 s55, s66, 10\n"
        "s_and_b32 s66, s48, 1\n"
        "s_lshl_b32 s66, s66, 12\n"
        "s_add_u32 s54, s54, s66\n"
        "s_add_u32 s66, s67, 1130496\n"
        "s_add_u32 s54, s54, s66\n"
        "s_add_u32 s55, s55, s66\n"
        "s_add_u32 s55, s55, 65536\n"
        "s_add_u32 s72, s85, 245760\n"
        "s_lshl_b32 s66, s65, 12\n"
        "s_lshr_b32 s87, s66, 13\n"
        "s_lshl_b32 s87, s87, 13\n"
        "s_add_u32 s87, s87, s66\n"
        "s_and_b32 s65, s49, 3\n"
        "s_or_b32 s88, s65, s48\n"
        "s_lshl_b32 s86, s48, 16\n"
        "s_lshl_b32 s65, s49, 7\n"
        "s_add_u32 s86, s86, s65\n"
        "s_add_u32 s67, s67, 540672\n"
        "s_add_u32 s86, s86, s67\n"
        "s_add_u32 s87, s87, s67\n"
        "s_mov_b32 s73, s86\n"
        "s_add_u32 s74, s73, 262144\n"
        "s_mov_b32 s76, s87\n"
        "s_lshl_b32 s65, s48, 15\n"
        "s_sub_u32 s75, s73, s65\n"
        "s_add_u32 s75, s75, 524288\n"
        "s_cmp_lt_u32 s48, 2\n"
        "s_cselect_b32 s65, s88, 0\n"
        "s_cmp_eq_u32 s65, 0\n"
        "s_cselect_b32 s86, 0, 2147483648\n"
        "s_lshl_b32 s65, s48, 3\n"
        "s_lshr_b32 s60, s60, s65\n"
        "s_and_b32 s60, s60, 255\n"
        "s_lshl_b32 s60, s60, 3\n"
        "s_add_u32 s60, s60, 1280\n"
        "s_add_u32 s60, s60, s38\n"
        "s_lshr_b32 s34, s61, s65\n"
        "s_and_b32 s34, s34, 255\n"
        "s_lshl_b32 s34, s34, 3\n"
        "s_add_u32 s34, s34, 2048\n"
        "s_add_u32 s34, s34, s38\n"
        "s_mov_b32 s87, s60\n"
        "s_bfe_u32 s35, s62, 0x80010\n"
        "s_lshr_b32 s61, s62, 24\n"
        "s_memrealtime s[66:67]\n"
        "s_memtime s[90:91]\n"
        "s_waitcnt lgkmcnt(0)\n"
        "s_mov_b32 s29, s90\n"
        "s_mov_b32 s28, s66\n"
        "s_mov_b32 s27, 0\n"
        "s_mov_b32 s26, 0\n"
        "s_lshl_b32 s33, s48, 10\n"
        "s_lshl_b32 s32, s48, 2\n"
        "s_lshr_b32 s65, s48, 1\n"
        "s_lshl_b32 s31, s65, 10\n"
        "s_and_b32 s65, s48, 1\n"
        "s_lshl_b32 s65, s65, 6\n"
        "s_add_u32 s31, s31, s65\n"
        "s_mov_b32 s51, -1\n"
        "s_mov_b32 s80, -1\n"
        "s_lshl_b32 s62, s49, 7\n"
        "s_cmp_eq_u32 s48, 1\n"
        "s_cselect_b32 s62, s62, 2147483648\n"
        "s_bfe_u32 s56, s63, 0x80008\n"
        "s_lshl_b32 s56, s56, 4\n"
        "s_lshr_b32 s57, s63, 16\n"
        "s_lshl_b32 s57, s57, 4\n"
        "s_and_b32 s63, s63, 255\n"
        "s_mov_b32 s44, 0x3f803f80\n"
        "s_mov_b32 s45, 0x3f803f80\n"
        "s_branch .Lch_idle2_%=\n"
        ".Lch_code_%=:\n"
        ".Lch_A2_%=:\n"
        "s_waitcnt vmcnt(0)\n"
        ".Lch_A_%=:\n"
        "s_waitcnt lgkmcnt(0)\n"
        "s_barrier\n"
        "ds_read_b128 v[32:35], v1 offset:0\n"
        "ds_read_b32 v7, v5 offset:0\n"
        "ds_read_b128 v[36:39], v1 offset:1024\n"
        "s_waitcnt lgkmcnt(2)\n"
        "v_smfmac_f32_16x16x64_bf16 v[8:11], v[32:35], a[0:7], v0\n"
        "v_max3_u32 v6, v6, v32, v33\n"
        "v_smfmac_f32_16x16x64_bf16 v[16:19], v[32:35], a[144:151], v0\n"
        "s_waitcnt lgkmcnt(1)\n"
        "v_cmp_eq_u32 vcc, -1, v7\n"
        "v_smfmac_f32_16x16x64_bf16 v[24:27], v[32:35], v[40:47], v0\n"
        "ds_read_b128 v[40:43], v2 offset:6144\n"
        "v_max3_u32 v6, v6, v34, v35\n"
        "v_smfmac_f32_16x16x64_bf16 v[12:15], v[32:35], a[8:15], v0\n"
        "s_cbranch_vccnz .Lch_rt0_%=\n"
        "v_smfmac_f32_16x16x64_bf16 v[20:23], v[32:35], a[152:159], v0\n"
        "s_waitcnt lgkmcnt(1)\n"
        "ds_read_b128 v[44:47], v2 offset:7168\n"
        "v_smfmac_f32_16x16x64_bf16 v[28:31], v[32:35], v[48:55], v0\n"
        "ds_read_b128 v[48:51], v2 offset:8192\n"
        "v_smfmac_f32_16x16x64_bf16 v[8:11], v[36:39], a[16:23], v0\n"
        "ds_read_b128 v[32:35], v1 offset:2048\n"
        "v_max3_u32 v6, v6, v36, v37\n"
        "v_smfmac_f32_16x16x64_bf16 v[16:19], v[36:39], a[160:167], v0\n"
        "ds_read_b128 v[52:55], v2 offset:9216\n"
        "v_smfmac_f32_16x16x64_bf16 v[24:27], v[36:39], v[56:63], v0\n"
        "ds_read_b128 v[56:59], v2 offset:10240\n"
        "v_max3_u32 v6, v6, v38, v39\n"
        "v_smfmac_f32_16x16x64_bf16 v[12:15], v[36:39], a[24:31], v0\n"
        "ds_read_b128 v[60:63], v2 offset:11264\n"
        "v_smfmac_f32_16x16x64_bf16 v[20:23], v[36:39], a[168:175], v0\n"
        "s_waitcnt lgkmcnt(3)\n"
        "v_smfmac_f32_16x16x64_bf16 v[28:31], v[36:39], v[40:47], v0\n"
        "ds_read_b128 v[40:43], v2 offset:12288\n"
        "v_smfmac_f32_16x16x64_bf16 v[8:11], v[32:35], a[32:39], v0\n"
        "ds_read_b128 v[36:39], v1 offset:3072\n"
        "v_max3_u32 v6, v6, v32, v33\n"
        "v_smfmac_f32_16x16x64_bf16 v[16:19], v[32:35], a[176:183], v0\n"
        "s_waitcnt lgkmcnt(4)\n"
        "ds_read_b128 v[44:47], v2 offset:13312\n"
        "buffer_load_dwordx4 v[64:67], v3, s[40:43], s30 offen offset:0 sc1\n"
        "v_smfmac_f32_16x16x64_bf16 v[24:27], v[32:35], v[48:55], v0\n"
        "ds_read_b128 v[48:51], v2 offset:14336\n"
        "v_max3_u32 v6, v6, v34, v35\n"
        "v_smfmac_f32_16x16x64_bf16 v[12:15], v[32:35], a[40:47], v0\n"
        "ds_read_b128 v[52:55], v2 offset:15360\n"
        "buffer_load_dwordx4 v[68:71], v3, s[40:43], s30 offen offset:1024 sc1\n"
        "v_smfmac_f32_16x16x64_bf16 v[20:23], v[32:35], a[184:191], v0\n"
        "s_waitcnt lgkmcnt(3)\n"
        "s_waitcnt vmcnt(3)\n"
        "s_barrier\n"
        "v_smfmac_f32_16x16x64_bf16 v[28:31], v[32:35], v[56:63], v0\n"
        "ds_read_b128 v[56:59], v2 offset:16384\n"
        "ds_read_b32 v7, v5 offset:4096\n"
        "v_smfmac_f32_16x16x64_bf16 v[8:11], v[36:39], a[48:55], v0\n"
        "ds_read_b128 v[32:35], v1 offset:4096\n"
        "v_max3_u32 v6, v6, v36, v37\n"
        "v_smfmac_f32_16x16x64_bf16 v[16:19], v[36:39], a[192:199], v0\n"
        "s_waitcnt lgkmcnt(5)\n"
        "ds_read_b128 v[60:63], v2 offset:17408\n"
        "v_smfmac_f32_16x16x64_bf16 v[24:27], v[36:39], v[40:47], v0\n"
        "ds_read_b128 v[40:43], v2 offset:18432\n"
        "v_max3_u32 v6, v6, v38, v39\n"
        "v_smfmac_f32_16x16x64_bf16 v[12:15], v[36:39], a[56:63], v0\n"
        "ds_read_b128 v[44:47], v2 offset:19456\n"
        "v_smfmac_f32_16x16x64_bf16 v[20:23], v[36:39], a[200:207], v0\n"
        "s_waitcnt lgkmcnt(3)\n"
        "v_cmp_eq_u32 vcc, -1, v7\n"
        "v_smfmac_f32_16x16x64_bf16 v[28:31], v[36:39], v[48:55], v0\n"
        "s_cbranch_vccnz .Lch_rt1_%=\n"
        ".Lch_ok1_%=:\n"
        "v_smfmac_f32_16x16x64_bf16 v[8:11], v[32:35], a[64:71], v0\n"
        "ds_read_b128 v[36:39], v1 offset:5120\n"
        "v_max3_u32 v6, v6, v32, v33\n"
        "v_smfmac_f32_16x16x64_bf16 v[16:19], v[32:35], a[208:215], v0\n"
        "s_waitcnt lgkmcnt(3)\n"
        "ds_read_b128 v[52:55], v2 offset:21504\n"
        "ds_read_b128 v[48:51], v2 offset:20480\n"
        "v_smfmac_f32_16x16x64_bf16 v[24:27], v[32:35], v[56:63], v0\n"
        "ds_read_b128 v[56:59], v2 offset:22528\n"
        "v_max3_u32 v6, v6, v34, v35\n"
        "v_smfmac_f32_16x16x64_bf16 v[12:15], v[32:35], a[72:79], v0\n"
        "ds_read_b128 v[60:63], v2 offset:23552\n"
        "v_smfmac_f32_16x16x64_bf16 v[20:23], v[32:35], a[216:223], v0\n"
        "s_waitcnt lgkmcnt(4)\n"
        "buffer_load_dwordx4 v[72:75], v3, s[40:43], s89 offen offset:0 sc1\n"
        "v_smfmac_f32_16x16x64_bf16 v[28:31], v[32:35], v[40:47], v0\n"
        "ds_read_b128 v[40:43], v2 offset:24576\n"
        "v_smfmac_f32_16x16x64_bf16 v[8:11], v[36:39], a[80:87], v0\n"
        "ds_read_b128 v[32:35], v1 offset:6144\n"
        "v_max3_u32 v6, v6, v36, v37\n"
        "v_smfmac_f32_16x16x64_bf16 v[16:19], v[36:39], a[224:231], v0\n"
        "s_waitcnt lgkmcnt(4)\n"
        "ds_read_b128 v[44:47], v2 offset:25600\n"
        "v_smfmac_f32_16x16x64_bf16 v[24:27], v[36:39], v[48:55], v0\n"
        "ds_read_b128 v[48:51], v2 offset:26624\n"
        "v_max3_u32 v6, v6, v38, v39\n"
        "v_smfmac_f32_16x16x64_bf16 v[12:15], v[36:39], a[88:95], v0\n"
        "ds_read_b128 v[52:55], v2 offset:27648\n"
        "buffer_load_dwordx4 v[76:79], v3, s[40:43], s77 offen offset:1024 sc1\n"
        "v_smfmac_f32_16x16x64_bf16 v[20:23], v[36:39], a[232:239], v0\n"
        "s_waitcnt lgkmcnt(3)\n"
        "v_smfmac_f32_16x16x64_bf16 v[28:31], v[36:39], v[56:63], v0\n"
        "s_waitcnt lgkmcnt(2)\n"
        "ds_read_b128 v[56:59], v2 offset:28672\n"
        "v_smfmac_f32_16x16x64_bf16 v[24:27], v[32:35], v[40:47], v0\n"
        "s_waitcnt lgkmcnt(1)\n"
        "ds_read_b128 v[36:39], v1 offset:7168\n"
        "ds_read_b32 v40, v4 offset:11264\n"
        "v_smfmac_f32_16x16x64_bf16 v[28:31], v[32:35], v[48:55], v0\n"
        "s_waitcnt vmcnt(4)\n"
        "s_barrier\n"
        "ds_read_b32 v7, v5 offset:8192\n"
        "v_smfmac_f32_16x16x64_bf16 v[8:11], v[32:35], a[96:103], v0\n"
        "s_memtime s[36:37]\n"
        "ds_read_b128 v[48:51], v1 offset:8192\n"
        "v_max3_u32 v6, v6, v32, v33\n"
        "v_smfmac_f32_16x16x64_bf16 v[16:19], v[32:35], a[240:247], v0\n"
        "ds_read_b128 v[52:55], v4 offset:9216\n"
        "v_max3_u32 v6, v6, v34, v35\n"
        "v_smfmac_f32_16x16x64_bf16 v[12:15], v[32:35], a[104:111], v0\n"
        "v_smfmac_f32_16x16x64_bf16 v[20:23], v[32:35], a[248:255], v0\n"
        "s_waitcnt lgkmcnt(2)\n"
        "s_nop 0\n"
        "v_cmp_eq_u32 vcc, -1, v7\n"
        "v_smfmac_f32_16x16x64_bf16 v[8:11], v[36:39], a[112:119], v0\n"
        "s_waitcnt lgkmcnt(0)\n"
        "s_and_b64 vcc, vcc, 0xffff\n"
        "v_max3_u32 v6, v6, v36, v37\n"
        "v_smfmac_f32_16x16x64_bf16 v[12:15], v[36:39], a[120:127], v0\n"
        "s_cbranch_vccnz .Lch_rt2_%=\n"
        ".Lch_ok2_%=:\n"
        "v_smfmac_f32_16x16x64_bf16 v[8:11], v[48:51], a[128:135], v0\n"
        "s_sub_u32 s37, s36, s38\n"
        "ds_read_b128 v[44:47], v2 offset:31744\n"
        "v_readfirstlane_b32 s81, v40\n"
        "v_smfmac_f32_16x16x64_bf16 v[12:15], v[48:51], a[136:143], v0\n"
        "s_cmp_eq_u32 s81, s51\n"
        "v_and_b32 v7, -2, s37\n"
        "v_smfmac_f32_16x16x64_bf16 v[16:19], v[36:39], v[80:87], v0\n"
        "s_cselect_b32 s65, 2147483648, s46\n"
        "v_cvt_pk_bf16_f32 v8, v8, v9\n"
        "v_smfmac_f32_16x16x64_bf16 v[16:19], v[48:51], v[96:103], v0\n"
        "s_mov_b64 exec, 0xff\n"
        "buffer_store_dwordx4 v[4:7], v3, s[40:43], s65 offen sc1\n"
        "s_mov_b64 exec, -1\n"
        "v_cvt_pk_bf16_f32 v9, v10, v11\n"
        "v_smfmac_f32_16x16x64_bf16 v[20:23], v[36:39], v[88:95], v0\n"
        "ds_read_b128 v[60:63], v2 offset:29696\n"
        "v_cvt_pk_bf16_f32 v12, v12, v13\n"
        "v_smfmac_f32_16x16x64_bf16 v[20:23], v[48:51], v[104:111], v0\n"
        "ds_read_b128 v[40:43], v2 offset:30720\n"
        "v_cvt_pk_bf16_f32 v13, v14, v15\n"
        "v_mfma_f32_16x16x16_bf16 v[32:35], v[8:9], v[52:53], 0\n"
        "v_cvt_pk_bf16_f32 v16, v16, v17\n"
        "v_cvt_pk_bf16_f32 v17, v18, v19\n"
        "v_mfma_f32_16x16x16_bf16 v[8:11], v[12:13], v[52:53], 0\n"
        "v_cvt_pk_bf16_f32 v20, v20, v21\n"
        "v_cvt_pk_bf16_f32 v21, v22, v23\n"
        "v_mfma_f32_16x16x16_bf16 v[12:15], v[16:17], v[54:55], 0\n"
        "v_cvt_pk_bf16_f32 v32, v32, v33\n"
        "v_cvt_pk_bf16_f32 v33, v34, v35\n"
        "v_mfma_f32_16x16x16_bf16 v[16:19], v[20:21], v[54:55], 0\n"
        "v_cvt_pk_bf16_f32 v34, v8, v9\n"
        "v_cvt_pk_bf16_f32 v35, v10, v11\n"
        "s_cbranch_scc1 .Lch_slow_%=\n"
        ".Lch_back_%=:\n"
        "s_waitcnt vmcnt(3)\n"
        "v_mfma_f32_16x16x32_bf16 v[8:11], v[64:67], v[32:35], 0\n"
        "v_cvt_pk_bf16_f32 v12, v12, v13\n"
        "v_cvt_pk_bf16_f32 v13, v14, v15\n"
        "v_mfma_f32_16x16x32_bf16 v[20:23], v[68:71], v[32:35], 0\n"
        "v_cvt_pk_bf16_f32 v14, v16, v17\n"
        "v_cvt_pk_bf16_f32 v15, v18, v19\n"
        "v_add_u32 v52, s33, v4\n"
        "v_add_u32 v53, s32, v4\n"
        "v_mfma_f32_16x16x32_bf16 v[16:19], v[12:15], v[32:35], 0\n"
        "ds_read_b128 v[68:71], v4 offset:10240\n"
        "v_max3_u32 v6, v6, v38, v39\n"
        "v_max3_u32 v6, v6, v48, v49\n"
        "v_mov_b32_dpp v8, v20 quad_perm:[0,1,2,3] row_mask:0xf bank_mask:0xc\n"
        "v_mov_b32_dpp v9, v21 quad_perm:[0,1,2,3] row_mask:0xf bank_mask:0xc\n"
        "v_mov_b32_dpp v10, v22 quad_perm:[0,1,2,3] row_mask:0xf bank_mask:0xc\n"
        "v_mov_b32_dpp v11, v23 quad_perm:[0,1,2,3] row_mask:0xf bank_mask:0xc\n"
        "ds_write_b128 v52, v[8:11]\n"
        "v_mov_b32_dpp v16, v17 quad_perm:[0,1,2,3] row_mask:0xf bank_mask:0xc\n"
        "ds_write_b32 v53, v16 offset:4096\n"
        "s_waitcnt lgkmcnt(3)\n"
        "v_smfmac_f32_16x16x64_bf16 v[24:27], v[36:39], v[56:63], v0\n"
        "v_xor_b32 v20, 0x400, v52\n"
        "v_smfmac_f32_16x16x64_bf16 v[28:31], v[36:39], v[40:47], v0\n"
        "v_xor_b32 v21, 0x800, v52\n"
        "v_smfmac_f32_16x16x64_bf16 v[24:27], v[48:51], v[240:247], v0\n"
        "v_xor_b32 v22, 0xc00, v52\n"
        "v_smfmac_f32_16x16x64_bf16 v[28:31], v[48:51], v[248:255], v0\n"
        "s_waitcnt lgkmcnt(0)\n"
        "s_barrier\n"
        "ds_read_b128 v[32:35], v20\n"
        "ds_read_b128 v[36:39], v21\n"
        "ds_read_b128 v[40:43], v22\n"
        "ds_read_b128 v[44:47], v4 offset:4096\n"
        "v_max3_u32 v6, v6, v50, v51\n"
        "buffer_store_dwordx4 v[12:15], v68, s[40:43], s70 offen sc1\n"
        "v_cmp_eq_u32 vcc, -1, v6\n"
        "v_add_f32 v24, v24, v27\n"
        "v_add_f32 v25, v25, v26\n"
        "v_add_f32 v28, v28, v31\n"
        "v_add_f32 v29, v29, v30\n"
        "s_cbranch_vccnz .Lch_redo_%=\n"
        "s_waitcnt lgkmcnt(3)\n"
        "v_pk_add_f32 v[32:33], v[32:33], v[8:9]\n"
        "v_pk_add_f32 v[34:35], v[34:35], v[10:11]\n"
        "s_waitcnt lgkmcnt(1)\n"
        "v_pk_add_f32 v[36:37], v[36:37], v[40:41]\n"
        "v_pk_add_f32 v[38:39], v[38:39], v[42:43]\n"
        "v_pk_add_f32 v[32:33], v[32:33], v[36:37]\n"
        "v_pk_add_f32 v[34:35], v[34:35], v[38:39]\n"
        "s_waitcnt lgkmcnt(0)\n"
        "v_pk_add_f32 v[48:49], v[44:45], v[46:47]\n"
        "v_max3_f32 v36, v32, v33, v34\n"
        "v_add_f32 v48, v48, v49\n"
        "v_max3_f32 v36, v36, v35, v48\n"
        "v_cvt_pk_bf16_f32 v30, v24, v25\n"
        "v_cvt_pk_bf16_f32 v31, v29, v28\n"
        "v_max_f32_dpp v37, v36, v36 row_ror:8 row_mask:0xf bank_mask:0xf\n"
        "v_max_f32_dpp v38, v36, v36 row_ror:8 row_mask:0xf bank_mask:0xf\n"
        "v_mov_b64 v[52:53], s[44:45]\n"
        "v_mov_b64 v[54:55], s[44:45]\n"
        "v_permlane16_swap_b32 v37, v38\n"
        "v_max_f32 v36, v37, v38\n"
        "v_max_f32 v39, v37, v38\n"
        "v_add_u32 v71, s31, v71\n"
        "v_lshlrev_b32 v6, 3, v3\n"
        "v_permlane32_swap_b32 v36, v39\n"
        "v_max_f32 v36, v36, v39\n"
        "v_mul_f32 v36, 0xbe0293ee, v36\n"
        "v_fmamk_f32 v48, v48, 0x3e0293ee, v36\n"
        "v_exp_f32 v48, v48\n"
        "v_fmamk_f32 v32, v32, 0x3e0293ee, v36\n"
        "v_fmamk_f32 v33, v33, 0x3e0293ee, v36\n"
        "v_mov_b32_dpp v49, v48 row_ror:8 row_mask:0xf bank_mask:0xf\n"
        "v_exp_f32 v32, v32\n"
        "v_cvt_pk_bf16_f32 v49, v48, v49\n"
        "v_exp_f32 v33, v33\n"
        "v_cndmask_b32 v44, v49, 0, s[58:59]\n"
        "v_cndmask_b32 v45, 0, v49, s[58:59]\n"
        "v_fmamk_f32 v34, v34, 0x3e0293ee, v36\n"
        "v_fmamk_f32 v35, v35, 0x3e0293ee, v36\n"
        "v_mfma_f32_16x16x16_bf16 v[56:59], v[52:53], v[44:45], 0\n"
        "v_exp_f32 v34, v34\n"
        "v_exp_f32 v35, v35\n"
        "v_cvt_pk_bf16_f32 v40, v32, v33\n"
        "v_mfma_f32_16x16x16_bf16 v[60:63], v[30:31], v[44:45], 0\n"
        "v_cvt_pk_bf16_f32 v41, v34, v35\n"
        "v_mov_b32_dpp v42, v40 row_ror:8 row_mask:0xf bank_mask:0xf\n"
        "s_waitcnt vmcnt(2)\n"
        "v_mov_b32_dpp v43, v41 row_ror:8 row_mask:0xf bank_mask:0xf\n"
        "v_swap_b32 v76, v78\n"
        "v_swap_b32 v77, v79\n"
        "v_mfma_f32_16x16x32_bf16 v[56:59], v[52:55], v[40:43], v[56:59]\n"
        "v_mov_b64 v[10:11], 0\n"
        "v_mov_b64 v[12:13], 0\n"
        "v_mfma_f32_16x16x32_bf16 v[32:35], v[72:75], v[40:43], v[60:63]\n"
        "s_mov_b32 exec_hi, 0\n"
        "buffer_load_dword v6, v6, s[40:43], s47 offen offset:12 sc1\n"
        "s_mov_b32 exec_hi, -1\n"
        "v_mfma_f32_16x16x32_bf16 v[48:51], v[76:79], v[40:43], v[60:63]\n"
        "v_rcp_f32 v36, v56\n"
        "v_mov_b64 v[14:15], 0\n"
        "v_mov_b64 v[16:17], 0\n"
        "v_mov_b64 v[18:19], 0\n"
        "v_mul_f32 v32, v32, v36\n"
        "v_mul_f32 v33, v33, v36\n"
        "v_mul_f32 v34, v34, v36\n"
        "v_mul_f32 v35, v35, v36\n"
        "v_mul_f32_dpp v32, v48, v36 quad_perm:[0,1,2,3] row_mask:0xf bank_mask:0xc\n"
        "v_mul_f32_dpp v33, v49, v36 quad_perm:[0,1,2,3] row_mask:0xf bank_mask:0xc\n"
        "v_mul_f32_dpp v34, v50, v36 quad_perm:[0,1,2,3] row_mask:0xf bank_mask:0xc\n"
        "v_mul_f32_dpp v35, v51, v36 quad_perm:[0,1,2,3] row_mask:0xf bank_mask:0xc\n"
        "v_cvt_pk_bf16_f32 v44, v32, v33\n"
        "v_cvt_pk_bf16_f32 v45, v34, v35\n"
        "ds_write_b64 v71, v[44:45]\n"
        "v_and_b32 v7, 0xffffffbf, v1\n"
        "v_cvt_pk_bf16_f32 v24, v24, v25\n"
        "v_cvt_pk_bf16_f32 v26, v28, v29\n"
        "v_mov_b64 v[8:9], 0\n"
        "v_mov_b64 v[20:21], 0\n"
        "v_mov_b64 v[22:23], 0\n"
        "s_waitcnt lgkmcnt(0)\n"
        "s_barrier\n"
        "ds_read_b128 v[28:31], v7 offset:5120\n"
        "ds_read_b128 v[32:35], v7 offset:5184\n"
        "ds_read_b128 v[36:39], v7 offset:6144\n"
        "ds_read_b128 v[40:43], v7 offset:6208\n"
        "v_permlane16_swap_b32 v24, v25\n"
        "v_permlane16_swap_b32 v26, v27\n"
        "buffer_store_dwordx4 v[24:27], v69, s[40:43], s70 offen offset:2048 sc1\n"
        "s_waitcnt lgkmcnt(3)\n"
        "v_smfmac_f32_16x16x64_bf16 v[8:11], v[28:31], v[112:119], v0\n"
        "s_waitcnt lgkmcnt(2)\n"
        "v_smfmac_f32_16x16x64_bf16 v[8:11], v[32:35], v[120:127], v0\n"
        "v_mov_b64 v[24:25], 0\n"
        "s_waitcnt lgkmcnt(1)\n"
        "v_smfmac_f32_16x16x64_bf16 v[8:11], v[36:39], v[128:135], v0\n"
        "v_mov_b64 v[26:27], 0\n"
        "s_waitcnt lgkmcnt(0)\n"
        "v_smfmac_f32_16x16x64_bf16 v[8:11], v[40:43], v[136:143], v0\n"
        "s_barrier\n"
        "v_smfmac_f32_16x16x64_bf16 v[12:15], v[28:31], v[144:151], v0\n"
        "ds_read_b128 v[44:47], v2 offset:32768\n"
        "v_smfmac_f32_16x16x64_bf16 v[12:15], v[32:35], v[152:159], v0\n"
        "ds_read_b128 v[48:51], v2 offset:33792\n"
        "v_cvt_pk_bf16_f32 v8, v8, v9\n"
        "v_smfmac_f32_16x16x64_bf16 v[12:15], v[36:39], v[160:167], v0\n"
        "ds_read_b128 v[52:55], v2 offset:34816\n"
        "v_cvt_pk_bf16_f32 v9, v11, v10\n"
        "v_smfmac_f32_16x16x64_bf16 v[12:15], v[40:43], v[168:175], v0\n"
        "ds_read_b128 v[56:59], v2 offset:35840\n"
        "v_bfi_b32 v8, -2, v8, s51\n"
        "v_smfmac_f32_16x16x64_bf16 v[16:19], v[28:31], v[176:183], v0\n"
        "ds_read_b128 v[60:63], v2 offset:36864\n"
        "v_smfmac_f32_16x16x64_bf16 v[16:19], v[32:35], v[184:191], v0\n"
        "v_cvt_pk_bf16_f32 v10, v12, v13\n"
        "v_cvt_pk_bf16_f32 v11, v15, v14\n"
        "v_bfi_b32 v10, -2, v10, s51\n"
        "buffer_store_dwordx4 v[8:11], v70, s[40:43], s73 offen sc1\n"
        "v_smfmac_f32_16x16x64_bf16 v[16:19], v[36:39], v[192:199], v0\n"
        "ds_read_b128 v[64:67], v2 offset:37888\n"
        "v_smfmac_f32_16x16x64_bf16 v[16:19], v[40:43], v[200:207], v0\n"
        "ds_read_b128 v[72:75], v2 offset:38912\n"
        "v_smfmac_f32_16x16x64_bf16 v[20:23], v[28:31], v[208:215], v0\n"
        "ds_read_b128 v[76:79], v2 offset:39936\n"
        "v_smfmac_f32_16x16x64_bf16 v[20:23], v[32:35], v[216:223], v0\n"
        "v_cvt_pk_bf16_f32 v16, v16, v17\n"
        "v_smfmac_f32_16x16x64_bf16 v[20:23], v[36:39], v[224:231], v0\n"
        "s_cmp_lt_u32 s48, 2\n"
        "v_cvt_pk_bf16_f32 v17, v19, v18\n"
        "v_smfmac_f32_16x16x64_bf16 v[20:23], v[40:43], v[232:239], v0\n"
        "v_bfi_b32 v16, -2, v16, s51\n"
        "s_cbranch_scc0 .Lch_R23_%=\n"
        "s_waitcnt lgkmcnt(0)\n"
        "v_smfmac_f32_16x16x64_bf16 v[24:27], v[28:31], v[44:51], v0\n"
        "v_smfmac_f32_16x16x64_bf16 v[24:27], v[32:35], v[52:59], v0\n"
        "v_cvt_pk_bf16_f32 v18, v20, v21\n"
        "v_cvt_pk_bf16_f32 v19, v23, v22\n"
        "v_bfi_b32 v18, -2, v18, s51\n"
        "buffer_store_dwordx4 v[16:19], v70, s[40:43], s74 offen sc1\n"
        "v_smfmac_f32_16x16x64_bf16 v[24:27], v[36:39], v[60:67], v0\n"
        "v_smfmac_f32_16x16x64_bf16 v[24:27], v[40:43], v[72:79], v0\n"
        "v_add_u32 v12, 114688, v3\n"
        "ds_read_b128 v[44:47], v12\n"
        "ds_read_b32 v50, v12 offset:1024\n"
        "s_nop 4\n"
        "v_cvt_pk_bf16_f32 v24, v24, v25\n"
        "v_bfi_b32 v24, -2, v24, s51\n"
        "v_cvt_pk_bf16_f32 v25, v27, v26\n"
        "s_nop 0\n"
        "v_permlane32_swap_b32 v24, v26\n"
        "v_permlane32_swap_b32 v25, v27\n"
        "s_mov_b32 exec_hi, 0\n"
        "buffer_store_dwordx4 v[24:27], v70, s[40:43], s75 offen sc1\n"
        "s_memtime s[90:91]\n"
        "s_waitcnt vmcnt(4)\n"
        "s_cmp_eq_u32 s88, 0\n"
        "s_cbranch_scc0 .Lch_idle_%=\n"
        "s_mov_b64 exec, 1\n"
        "buffer_load_dword v7, off, s[40:43], s78 sc1\n"
        "s_mov_b32 exec_lo, -1\n"
        "s_waitcnt vmcnt(4)\n"
        "s_branch .Lch_R0_%=\n"
        ".Lch_idle_%=:\n"
        "s_mov_b32 exec_hi, -1\n"
        "s_waitcnt lgkmcnt(0)\n"
        "s_waitcnt vmcnt(3)\n"
        "v_mov_b32 v12, s51\n"
        "s_mov_b64 exec, 1\n"
        "buffer_store_dword v12, off, s[40:43], s79 sc1\n"
        "s_mov_b64 exec, -1\n"
        "s_mov_b32 s65, s63\n"
        ".Lch_d4_%=:\n"
        "s_sub_u32 s65, s65, 1\n"
        "s_cbranch_scc0 .Lch_d4_%=\n"
        ".Lch_idle2_%=:\n"
        "s_mov_b64 exec, 1\n"
        "buffer_load_dword v7, off, s[40:43], s78 sc1\n"
        "s_mov_b64 exec, -1\n"
        "s_branch .Lch_nog_%=\n"
        ".Lch_R23_%=:\n"
        "s_nop 5\n"
        "v_cvt_pk_bf16_f32 v18, v20, v21\n"
        "v_cvt_pk_bf16_f32 v19, v23, v22\n"
        "v_bfi_b32 v18, -2, v18, s51\n"
        "buffer_store_dwordx4 v[16:19], v70, s[40:43], s74 offen sc1\n"
        "s_memtime s[90:91]\n"
        "s_mov_b32 exec_hi, 0\n"
        "s_waitcnt vmcnt(3)\n"
        "s_cmp_eq_u32 s48, 2\n"
        "s_cbranch_scc1 .Lch_R_%=\n"
        "s_waitcnt vmcnt(2)\n"
        ".Lch_R0_%=:\n"
        "v_mov_b32 v10, s51\n"
        "s_mov_b32 exec_lo, 1\n"
        "buffer_store_dword v10, off, s[40:43], s79 sc1\n"
        "s_mov_b32 exec_lo, -1\n"
        ".Lch_R_%=:\n"
        ".Lch_st1_%=:\n"
        "v_cmp_eq_u32 vcc, -1, v6\n"
        "s_cbranch_vccnz .Lch_stwait_%=\n"
        ".Lch_st2_%=:\n"
        "s_waitcnt lgkmcnt(0)\n"
        "s_sub_u32 s65, s90, s60\n"
        "s_memtime s[90:91]\n"
        "v_sub_u32 v12, s65, v6\n"
        "v_cmp_lt_u32 vcc, 0xffffb1e0, v12\n"
        "s_cbranch_vccnz .Lch_st2_%=\n"
        "s_mov_b32 exec_hi, -1\n"
        ".Lch_rl_%=:\n"
        "buffer_load_dwordx4 v[28:31], v3, s[40:43], s76 offen offset:0 sc1\n"
        "buffer_load_dwordx4 v[32:35], v3, s[40:43], s76 offen offset:1024 sc1\n"
        "buffer_load_dwordx4 v[36:39], v3, s[40:43], s76 offen offset:2048 sc1\n"
        "buffer_load_dwordx4 v[40:43], v3, s[40:43], s76 offen offset:3072 sc1\n"
        "v_mov_b32 v10, s51\n"
        "s_waitcnt vmcnt(6)\n"
        "s_cmp_eq_u32 s48, 2\n"
        "s_cselect_b32 s65, s79, 2147483648\n"
        "s_mov_b64 exec, 1\n"
        "buffer_store_dword v10, off, s[40:43], s65 sc1\n"
        "s_mov_b64 exec, -1\n"
        "v_subrev_u32 v8, s37, v6\n"
        "v_subrev_u32 v9, s37, v6\n"
        "s_nop 0\n"
        "v_max_i32_dpp v8, v8, v8 row_ror:8 row_mask:0xf bank_mask:0xf\n"
        "v_min_i32_dpp v9, v9, v9 row_ror:8 row_mask:0xf bank_mask:0xf\n"
        "s_nop 0\n"
        "v_max_i32_dpp v8, v8, v8 row_ror:4 row_mask:0xf bank_mask:0xf\n"
        "v_min_i32_dpp v9, v9, v9 row_ror:4 row_mask:0xf bank_mask:0xf\n"
        "s_nop 0\n"
        "v_max_i32_dpp v8, v8, v8 row_ror:2 row_mask:0xf bank_mask:0xf\n"
        "v_min_i32_dpp v9, v9, v9 row_ror:2 row_mask:0xf bank_mask:0xf\n"
        "s_nop 0\n"
        "v_max_i32_dpp v8, v8, v8 row_ror:1 row_mask:0xf bank_mask:0xf\n"
        "v_min_i32_dpp v9, v9, v9 row_ror:1 row_mask:0xf bank_mask:0xf\n"
        "s_waitcnt lgkmcnt(0)\n"
        "s_memrealtime s[66:67]\n"
        "s_memtime s[90:91]\n"
        "s_waitcnt lgkmcnt(0)\n"
        "s_sub_u32 s91, s90, s29\n"
        "s_mov_b32 s29, s90\n"
        "s_sub_u32 s67, s66, s28\n"
        "s_mov_b32 s28, s66\n"
        "v_cvt_f32_u32 v10, s91\n"
        "v_cvt_f32_u32 v11, s67\n"
        "v_fma_f32 v10, s27, 0.5, v10\n"
        "v_fma_f32 v11, s26, 0.5, v11\n"
        "s_nop 0\n"
        "v_readfirstlane_b32 s27, v10\n"
        "v_readfirstlane_b32 s26, v11\n"
        "v_rcp_f32 v11, v11\n"
        "s_nop 0\n"
        "v_mul_f32 v10, v10, v11\n"
        "v_mul_f32 v10, 0x41800000, v10\n"
        "v_cvt_u32_f32 v10, v10\n"
        "s_nop 0\n"
        "v_readfirstlane_b32 s66, v10\n"
        "s_nop 0\n"
        "s_min_u32 s66, s66, 400\n"
        "s_max_u32 s66, s66, 256\n"
        "s_sub_u32 s66, s66, 336\n"
        "s_mul_i32 s67, s66, s35\n"
        "s_ashr_i32 s67, s67, 4\n"
        "s_add_u32 s60, s87, s67\n"
        "s_mul_i32 s67, s66, s61\n"
        "s_ashr_i32 s67, s67, 4\n"
        "s_add_u32 s67, s67, s34\n"
        "v_readlane_b32 s65, v8, 0\n"
        "v_readlane_b32 s66, v8, 16\n"
        "v_readlane_b32 s90, v9, 0\n"
        "v_readlane_b32 s91, v9, 16\n"
        "s_max_i32 s65, s65, s66\n"
        "s_min_i32 s90, s90, s91\n"
        "s_sub_u32 s90, s65, s90\n"
        "s_add_u32 s65, s65, s37\n"
        "s_add_u32 s65, s65, s67\n"
        "s_cmp_gt_u32 s90, s56\n"
        "s_cselect_b32 s90, s57, 0\n"
        "s_add_u32 s82, s65, s90\n"
        "s_and_b32 s71, s51, 3\n"
        "s_mul_i32 s71, s71, 81920\n"
        "s_add_u32 s39, s71, s85\n"
        "v_mov_b64 v[64:65], -1\n"
        "v_mov_b64 v[66:67], -1\n"
        "s_waitcnt vmcnt(4)\n"
        "v_mfma_f32_16x16x32_bf16 v[12:15], v[28:31], v[44:47], 0\n"
        "v_bitop3_b32 v8, v28, v30, s51 bitop3:0x7e\n"
        "s_waitcnt vmcnt(3)\n"
        "v_mfma_f32_16x16x32_bf16 v[12:15], v[32:35], v[44:47], v[12:15]\n"
        "v_bitop3_b32 v10, v32, v34, s51 bitop3:0x7e\n"
        "v_or_b32 v8, v8, v10\n"
        "s_waitcnt vmcnt(2)\n"
        "v_mfma_f32_16x16x32_bf16 v[12:15], v[36:39], v[44:47], v[12:15]\n"
        "v_bitop3_b32 v10, v36, v38, s51 bitop3:0x7e\n"
        "v_or_b32 v8, v8, v10\n"
        "s_waitcnt vmcnt(1)\n"
        "v_mfma_f32_16x16x32_bf16 v[12:15], v[40:43], v[44:47], v[12:15]\n"
        "v_bitop3_b32 v10, v40, v42, s51 bitop3:0x7e\n"
        "v_or_b32 v8, v8, v10\n"
        "v_and_b32 v8, 1, v8\n"
        "v_cmp_ne_u32 vcc, 0, v8\n"
        "s_nop 2\n"
        "s_cbranch_vccnz .Lch_rretry_%=\n"
        "v_permlane32_swap_b32 v12, v14\n"
        "v_permlane32_swap_b32 v13, v15\n"
        "v_add_f32 v12, v12, v14\n"
        "v_add_f32 v13, v13, v15\n"
        "v_cvt_pk_bf16_f32 v16, v12, v13\n"
        "v_cvt_pk_bf16_f32 v18, v12, v13\n"
        "s_nop 1\n"
        "v_permlane16_swap_b32 v16, v18\n"
        "s_nop 1\n"
        "v_permlane32_swap_b32 v16, v17\n"
        "v_permlane32_swap_b32 v18, v19\n"
        "buffer_store_dwordx4 v[16:19], v50, s[40:43], s39 offen sc1\n"
        "buffer_store_dwordx4 v[64:67], v50, s[40:43], s72 offen sc1\n"
        "s_mov_b32 s72, s39\n"
        "s_cmp_eq_u32 s48, 0\n"
        "s_cbranch_scc0 .Lch_w23_%=\n"
        "s_add_u32 s51, s51, 1\n"
        "s_add_u32 s80, s80, 2\n"
        "s_bitcmp1_b64 s[52:53], s51\n"
        "s_cselect_b32 s65, 8192, 0\n"
        "s_lshl_b32 s68, s51, 21\n"
        "s_add_u32 s68, s68, s65\n"
        "s_add_u32 s68, s68, 2097152\n"
        "s_add_u32 s70, s68, s84\n"
        "s_add_u32 s69, s68, s83\n"
        "s_add_u32 s47, s68, 2068480\n"
        "s_add_u32 s46, s47, s62\n"
        "s_or_b32 s47, s47, s86\n"
        "ds_read_b128 v[40:43], v2 offset:0\n"
        "ds_read_b128 v[44:47], v2 offset:1024\n"
        "ds_read_b128 v[48:51], v2 offset:2048\n"
        "ds_read_b128 v[52:55], v2 offset:3072\n"
        "ds_read_b128 v[56:59], v2 offset:4096\n"
        "ds_read_b128 v[60:63], v2 offset:5120\n"
        "v_mov_b64 v[8:9], 0\n"
        "v_mov_b64 v[10:11], 0\n"
        "v_mov_b64 v[12:13], 0\n"
        "v_mov_b64 v[14:15], 0\n"
        "v_mov_b64 v[16:17], 0\n"
        "v_mov_b64 v[18:19], 0\n"
        "v_mov_b64 v[20:21], 0\n"
        "v_mov_b64 v[22:23], 0\n"
        "v_mov_b64 v[24:25], 0\n"
        "v_mov_b64 v[26:27], 0\n"
        "v_mov_b64 v[28:29], 0\n"
        "v_mov_b64 v[30:31], 0\n"
        "v_mov_b32 v6, 0\n"
        "s_add_u32 s89, s69, 524288\n"
        "s_mov_b32 s30, s69\n"
        "s_mov_b32 s77, s89\n"
        "s_cmp_eq_u32 s51, 63\n"
        "s_cselect_b32 s67, 2147483647, s80\n"
        "v_readfirstlane_b32 s65, v7\n"
        "s_nop 0\n"
        "s_cmp_ge_i32 s65, s67\n"
        "s_cbranch_scc1 .Lch_A_%=\n"
        "s_mov_b32 s30, 2147483648\n"
        "s_mov_b32 s89, 2147483648\n"
        "s_sub_u32 s77, s78, 1024\n"
        "v_mov_b32 v7, s51\n"
        "ds_write_b32 v4, v7 offset:11264\n"
        "s_branch .Lch_A_%=\n"
        ".Lch_w23_%=:\n"
        "s_mov_b64 exec, 1\n"
        "buffer_load_dword v7, off, s[40:43], s78 sc1\n"
        "s_mov_b64 exec, -1\n"
        "v_mov_b32 v32, v49\n"
        "s_cmp_eq_u32 s48, 2\n"
        "s_cbranch_scc0 .Lch_nog_%=\n"
        "s_and_b32 s66, s33, 1024\n"
        "s_lshl_b32 s66, s66, 2\n"
        "s_add_u32 m0, s66, 151552\n"
        "s_add_u32 s66, s54, s71\n"
        "s_memtime s[90:91]\n"
        "s_waitcnt lgkmcnt(0)\n"
        "s_sub_u32 s65, s82, s90\n"
        "s_cmp_lt_u32 s65, 20000\n"
        "s_cselect_b32 s65, s65, 0\n"
        ".Lch_gwa_%=:\n"
        "s_sub_u32 s65, s65, 28\n"
        "s_cbranch_scc0 .Lch_gwa_%=\n"
        "buffer_load_dwordx4 v3, s[40:43], s66 offen offset:0 sc1 lds\n"
        "buffer_load_dwordx4 v3, s[40:43], s66 offen offset:1024 sc1 lds\n"
        "buffer_load_dwordx4 v3, s[40:43], s66 offen offset:2048 sc1 lds\n"
        "buffer_load_dwordx4 v3, s[40:43], s66 offen offset:3072 sc1 lds\n"
        "s_add_u32 s51, s51, 1\n"
        "s_add_u32 s80, s80, 2\n"
        "s_bitcmp1_b64 s[52:53], s51\n"
        "s_cselect_b32 s65, 8192, 0\n"
        "s_lshl_b32 s68, s51, 21\n"
        "s_add_u32 s68, s68, s65\n"
        "s_add_u32 s68, s68, 2097152\n"
        "s_add_u32 s70, s68, s84\n"
        "s_add_u32 s69, s68, s83\n"
        "s_add_u32 s47, s68, 2068480\n"
        "s_add_u32 s46, s47, s62\n"
        "s_or_b32 s47, s47, s86\n"
        "ds_read_b128 v[40:43], v2 offset:0\n"
        "ds_read_b128 v[44:47], v2 offset:1024\n"
        "ds_read_b128 v[48:51], v2 offset:2048\n"
        "ds_read_b128 v[52:55], v2 offset:3072\n"
        "ds_read_b128 v[56:59], v2 offset:4096\n"
        "ds_read_b128 v[60:63], v2 offset:5120\n"
        "v_mov_b64 v[8:9], 0\n"
        "v_mov_b64 v[10:11], 0\n"
        "v_mov_b64 v[12:13], 0\n"
        "v_mov_b64 v[14:15], 0\n"
        "v_mov_b64 v[16:17], 0\n"
        "v_mov_b64 v[18:19], 0\n"
        "v_mov_b64 v[20:21], 0\n"
        "v_mov_b64 v[22:23], 0\n"
        "v_mov_b64 v[24:25], 0\n"
        "v_mov_b64 v[26:27], 0\n"
        "v_mov_b64 v[28:29], 0\n"
        "v_mov_b64 v[30:31], 0\n"
        "v_mov_b32 v6, 0\n"
        "s_add_u32 s89, s69, 524288\n"
        "s_mov_b32 s30, s69\n"
        "s_mov_b32 s77, s89\n"
        "s_cmp_eq_u32 s51, 63\n"
        "s_cselect_b32 s67, 2147483647, s80\n"
        "s_waitcnt vmcnt(4)\n"
        "v_readfirstlane_b32 s65, v7\n"
        "s_nop 0\n"
        "s_cmp_ge_i32 s65, s67\n"
        "s_cbranch_scc1 .Lch_A2_%=\n"
        "s_mov_b32 s30, 2147483648\n"
        "s_mov_b32 s89, 2147483648\n"
        "s_sub_u32 s77, s78, 1024\n"
        "v_mov_b32 v7, s51\n"
        "ds_write_b32 v4, v7 offset:11264\n"
        "s_branch .Lch_A2_%=\n"
        ".Lch_nog_%=:\n"
        "s_add_u32 s51, s51, 1\n"
        "s_add_u32 s80, s80, 2\n"
        "s_bitcmp1_b64 s[52:53], s51\n"
        "s_cselect_b32 s65, 8192, 0\n"
        "s_lshl_b32 s68, s51, 21\n"
        "s_add_u32 s68, s68, s65\n"
        "s_add_u32 s68, s68, 2097152\n"
        "s_add_u32 s70, s68, s84\n"
        "s_add_u32 s69, s68, s83\n"
        "s_add_u32 s47, s68, 2068480\n"
        "s_add_u32 s46, s47, s62\n"
        "s_or_b32 s47, s47, s86\n"
        "ds_read_b128 v[40:43], v2 offset:0\n"
        "ds_read_b128 v[44:47], v2 offset:1024\n"
        "ds_read_b128 v[48:51], v2 offset:2048\n"
        "ds_read_b128 v[52:55], v2 offset:3072\n"
        "ds_read_b128 v[56:59], v2 offset:4096\n"
        "ds_read_b128 v[60:63], v2 offset:5120\n"
        "v_mov_b64 v[8:9], 0\n"
        "v_mov_b64 v[10:11], 0\n"
        "v_mov_b64 v[12:13], 0\n"
        "v_mov_b64 v[14:15], 0\n"
        "v_mov_b64 v[16:17], 0\n"
        "v_mov_b64 v[18:19], 0\n"
        "v_mov_b64 v[20:21], 0\n"
        "v_mov_b64 v[22:23], 0\n"
        "v_mov_b64 v[24:25], 0\n"
        "v_mov_b64 v[26:27], 0\n"
        "v_mov_b64 v[28:29], 0\n"
        "v_mov_b64 v[30:31], 0\n"
        "v_mov_b32 v6, 0\n"
        "s_add_u32 s89, s69, 524288\n"
        "s_mov_b32 s30, s69\n"
        "s_mov_b32 s77, s89\n"
        "s_cmp_eq_u32 s51, 63\n"
        "s_cselect_b32 s67, 2147483647, s80\n"
        "s_cmp_eq_u32 s48, 3\n"
        "s_cselect_b32 s65, s51, 0\n"
        "s_cmp_eq_u32 s65, 0\n"
        "s_cbranch_scc0 .Lch_g3_%=\n"
        "s_waitcnt vmcnt(0)\n"
        "v_readfirstlane_b32 s65, v7\n"
        "s_nop 0\n"
        "s_cmp_ge_i32 s65, s67\n"
        "s_cbranch_scc1 .Lch_A_%=\n"
        "s_mov_b32 s30, 2147483648\n"
        "s_mov_b32 s89, 2147483648\n"
        "s_sub_u32 s77, s78, 1024\n"
        "v_mov_b32 v7, s51\n"
        "ds_write_b32 v4, v7 offset:11264\n"
        "s_branch .Lch_A_%=\n"
        ".Lch_g3_%=:\n"
        "s_and_b32 s66, s33, 1024\n"
        "s_lshl_b32 s66, s66, 2\n"
        "s_add_u32 m0, s66, 151552\n"
        "s_add_u32 s66, s54, s71\n"
        "s_memtime s[90:91]\n"
        "s_waitcnt lgkmcnt(0)\n"
        "s_sub_u32 s65, s82, s90\n"
        "s_cmp_lt_u32 s65, 20000\n"
        "s_cselect_b32 s65, s65, 0\n"
        ".Lch_gwb_%=:\n"
        "s_sub_u32 s65, s65, 28\n"
        "s_cbranch_scc0 .Lch_gwb_%=\n"
        "buffer_load_dwordx4 v3, s[40:43], s66 offen offset:0 sc1 lds\n"
        "buffer_load_dwordx4 v3, s[40:43], s66 offen offset:1024 sc1 lds\n"
        "buffer_load_dwordx4 v3, s[40:43], s66 offen offset:2048 sc1 lds\n"
        "buffer_load_dwordx4 v3, s[40:43], s66 offen offset:3072 sc1 lds\n"
        "s_mov_b32 m0, 159744\n"
        "s_add_u32 s66, s71, s55\n"
        "buffer_load_dwordx4 v32, s[40:43], s66 offen sc1 lds\n"
        "s_waitcnt vmcnt(5)\n"
        "v_readfirstlane_b32 s65, v7\n"
        "s_nop 0\n"
        "s_cmp_ge_i32 s65, s67\n"
        "s_cbranch_scc1 .Lch_A_%=\n"
        "s_mov_b32 s30, 2147483648\n"
        "s_mov_b32 s89, 2147483648\n"
        "s_sub_u32 s77, s78, 1024\n"
        "v_mov_b32 v7, s51\n"
        "ds_write_b32 v4, v7 offset:11264\n"
        "s_branch .Lch_A_%=\n"
        ".Lch_rretry_%=:\n"
        "s_waitcnt vmcnt(0)\n"
        "s_add_u32 s64, s64, 1\n"
        "s_and_b32 s65, s64, 255\n"
        "s_cbranch_scc1 .Lch_rr_go_%=\n"
        "s_mov_b32 s65, 49152\n"
        "s_cmp_lt_u32 s64, 1048576\n"
        "s_cbranch_scc1 .Lch_rr_chk_%=\n"
        "v_mov_b32 v8, 1\n"
        "buffer_store_dword v8, off, s[40:43], s65 sc1\n"
        "s_waitcnt vmcnt(0)\n"
        ".Lch_rr_chk_%=:\n"
        "buffer_load_dword v8, off, s[40:43], s65 sc1\n"
        "s_waitcnt vmcnt(0)\n"
        "v_readfirstlane_b32 s65, v8\n"
        "s_nop 0\n"
        "s_cmp_lg_u32 s65, 1\n"
        "s_cbranch_scc1 .Lch_rr_go_%=\n"
        "s_endpgm\n"
        ".Lch_rr_go_%=:\n"
        "s_branch .Lch_rl_%=\n"
        ".Lch_stwait_%=:\n"
        "s_add_u32 s64, s64, 1\n"
        "s_and_b32 s65, s64, 255\n"
        "s_cbranch_scc1 .Lch_sw_go_%=\n"
        "s_mov_b32 s65, 49152\n"
        "s_cmp_lt_u32 s64, 1048576\n"
        "s_cbranch_scc1 .Lch_sw_chk_%=\n"
        "v_mov_b32 v12, 1\n"
        "buffer_store_dword v12, off, s[40:43], s65 sc1\n"
        "s_waitcnt vmcnt(0)\n"
        ".Lch_sw_chk_%=:\n"
        "buffer_load_dword v12, off, s[40:43], s65 sc1\n"
        "s_waitcnt vmcnt(0)\n"
        "v_readfirstlane_b32 s65, v12\n"
        "s_nop 0\n"
        "s_cmp_lg_u32 s65, 1\n"
        "s_cbranch_scc1 .Lch_sw_go_%=\n"
        "s_endpgm\n"
        ".Lch_sw_go_%=:\n"
        "s_sleep 1\n"
        "v_lshlrev_b32 v6, 3, v3\n"
        "buffer_load_dword v6, v6, s[40:43], s47 offen offset:12 sc1\n"
        "s_waitcnt vmcnt(0)\n"
        "s_branch .Lch_st1_%=\n"
        ".Lch_rt0_%=:\n"
        "s_waitcnt lgkmcnt(0)\n"
        "s_barrier\n"
        "s_add_u32 s64, s64, 1\n"
        "s_and_b32 s65, s64, 255\n"
        "s_cbranch_scc1 .Lch_rt0_go_%=\n"
        "s_mov_b32 s65, 49152\n"
        "s_cmp_lt_u32 s64, 1048576\n"
        "s_cbranch_scc1 .Lch_rt0_chk_%=\n"
        "v_mov_b32 v7, 1\n"
        "buffer_store_dword v7, off, s[40:43], s65 sc1\n"
        "s_waitcnt vmcnt(0)\n"
        ".Lch_rt0_chk_%=:\n"
        "buffer_load_dword v7, off, s[40:43], s65 sc1\n"
        "s_waitcnt vmcnt(0)\n"
        "v_readfirstlane_b32 s65, v7\n"
        "s_nop 0\n"
        "s_cmp_lg_u32 s65, 1\n"
        "s_cbranch_scc1 .Lch_rt0_go_%=\n"
        "s_endpgm\n"
        ".Lch_rt0_go_%=:\n"
        "s_cmp_eq_u32 s48, 2\n"
        "s_cbranch_scc0 .Lch_rw0_%=\n"
        "s_and_b32 s66, s33, 1024\n"
        "s_lshl_b32 s66, s66, 2\n"
        "s_add_u32 m0, s66, 151552\n"
        "s_add_u32 s66, s54, s71\n"
        "buffer_load_dwordx4 v3, s[40:43], s66 offen offset:0 sc1 lds\n"
        "buffer_load_dwordx4 v3, s[40:43], s66 offen offset:1024 sc1 lds\n"
        "buffer_load_dwordx4 v3, s[40:43], s66 offen offset:2048 sc1 lds\n"
        "buffer_load_dwordx4 v3, s[40:43], s66 offen offset:3072 sc1 lds\n"
        "s_waitcnt vmcnt(0)\n"
        ".Lch_rw0_%=:\n"
        "s_waitcnt vmcnt(0)\n"
        "ds_read_b128 v[40:43], v2 offset:0\n"
        "ds_read_b128 v[44:47], v2 offset:1024\n"
        "ds_read_b128 v[48:51], v2 offset:2048\n"
        "ds_read_b128 v[52:55], v2 offset:3072\n"
        "ds_read_b128 v[56:59], v2 offset:4096\n"
        "ds_read_b128 v[60:63], v2 offset:5120\n"
        "v_mov_b64 v[8:9], 0\n"
        "v_mov_b64 v[10:11], 0\n"
        "v_mov_b64 v[12:13], 0\n"
        "v_mov_b64 v[14:15], 0\n"
        "v_mov_b64 v[16:17], 0\n"
        "v_mov_b64 v[18:19], 0\n"
        "v_mov_b64 v[20:21], 0\n"
        "v_mov_b64 v[22:23], 0\n"
        "v_mov_b64 v[24:25], 0\n"
        "v_mov_b64 v[26:27], 0\n"
        "v_mov_b64 v[28:29], 0\n"
        "v_mov_b64 v[30:31], 0\n"
        "v_mov_b32 v6, 0\n"
        "s_branch .Lch_A_%=\n"
        ".Lch_rt1_%=:\n"
        "s_waitcnt lgkmcnt(0)\n"
        "s_barrier\n"
        "s_add_u32 s64, s64, 1\n"
        "s_and_b32 s65, s64, 255\n"
        "s_cbranch_scc1 .Lch_rt1_go_%=\n"
        "s_mov_b32 s65, 49152\n"
        "s_cmp_lt_u32 s64, 1048576\n"
        "s_cbranch_scc1 .Lch_rt1_chk_%=\n"
        "v_mov_b32 v7, 1\n"
        "buffer_store_dword v7, off, s[40:43], s65 sc1\n"
        "s_waitcnt vmcnt(0)\n"
        ".Lch_rt1_chk_%=:\n"
        "buffer_load_dword v7, off, s[40:43], s65 sc1\n"
        "s_waitcnt vmcnt(0)\n"
        "v_readfirstlane_b32 s65, v7\n"
        "s_nop 0\n"
        "s_cmp_lg_u32 s65, 1\n"
        "s_cbranch_scc1 .Lch_rt1_go_%=\n"
        "s_endpgm\n"
        ".Lch_rt1_go_%=:\n"
        "s_cmp_eq_u32 s48, 3\n"
        "s_cbranch_scc0 .Lch_rw1_%=\n"
        "s_and_b32 s66, s33, 1024\n"
        "s_lshl_b32 s66, s66, 2\n"
        "s_add_u32 m0, s66, 151552\n"
        "s_add_u32 s66, s54, s71\n"
        "buffer_load_dwordx4 v3, s[40:43], s66 offen offset:0 sc1 lds\n"
        "buffer_load_dwordx4 v3, s[40:43], s66 offen offset:1024 sc1 lds\n"
        "buffer_load_dwordx4 v3, s[40:43], s66 offen offset:2048 sc1 lds\n"
        "buffer_load_dwordx4 v3, s[40:43], s66 offen offset:3072 sc1 lds\n"
        "s_waitcnt vmcnt(0)\n"
        ".Lch_rw1_%=:\n"
        "s_barrier\n"
        "ds_read_b32 v7, v5 offset:4096\n"
        "ds_read_b128 v[32:35], v1 offset:4096\n"
        "s_waitcnt lgkmcnt(0)\n"
        "v_cmp_eq_u32 vcc, -1, v7\n"
        "s_cbranch_vccnz .Lch_rt1_%=\n"
        "s_branch .Lch_ok1_%=\n"
        ".Lch_rt2_%=:\n"
        "s_waitcnt lgkmcnt(0)\n"
        "s_barrier\n"
        "s_add_u32 s64, s64, 1\n"
        "s_and_b32 s65, s64, 255\n"
        "s_cbranch_scc1 .Lch_rt2_go_%=\n"
        "s_mov_b32 s65, 49152\n"
        "s_cmp_lt_u32 s64, 1048576\n"
        "s_cbranch_scc1 .Lch_rt2_chk_%=\n"
        "v_mov_b32 v7, 1\n"
        "buffer_store_dword v7, off, s[40:43], s65 sc1\n"
        "s_waitcnt vmcnt(0)\n"
        ".Lch_rt2_chk_%=:\n"
        "buffer_load_dword v7, off, s[40:43], s65 sc1\n"
        "s_waitcnt vmcnt(0)\n"
        "v_readfirstlane_b32 s65, v7\n"
        "s_nop 0\n"
        "s_cmp_lg_u32 s65, 1\n"
        "s_cbranch_scc1 .Lch_rt2_go_%=\n"
        "s_endpgm\n"
        ".Lch_rt2_go_%=:\n"
        "s_cmp_eq_u32 s48, 3\n"
        "s_cbranch_scc0 .Lch_rw2_%=\n"
        "ds_read_b32 v7, v2 offset:33796\n"
        "s_waitcnt lgkmcnt(0)\n"
        "s_mov_b32 m0, 159744\n"
        "s_add_u32 s66, s71, s55\n"
        "buffer_load_dwordx4 v7, s[40:43], s66 offen sc1 lds\n"
        "s_waitcnt vmcnt(0)\n"
        ".Lch_rw2_%=:\n"
        "s_barrier\n"
        "ds_read_b32 v7, v5 offset:8192\n"
        "ds_read_b128 v[48:51], v1 offset:8192\n"
        "s_memtime s[36:37]\n"
        "s_waitcnt lgkmcnt(0)\n"
        "v_cmp_eq_u32 vcc, -1, v7\n"
        "s_nop 0\n"
        "s_and_b64 vcc, vcc, 0xffff\n"
        "s_cbranch_vccnz .Lch_rt2_%=\n"
        "s_branch .Lch_ok2_%=\n"
        ".Lch_redo_%=:\n"
        "s_waitcnt vmcnt(0) lgkmcnt(0)\n"
        "s_barrier\n"
        "s_add_u32 s64, s64, 1\n"
        "s_and_b32 s65, s64, 255\n"
        "s_cbranch_scc1 .Lch_rd_go_%=\n"
        "s_mov_b32 s65, 49152\n"
        "s_cmp_lt_u32 s64, 1048576\n"
        "s_cbranch_scc1 .Lch_rd_chk_%=\n"
        "v_mov_b32 v7, 1\n"
        "buffer_store_dword v7, off, s[40:43], s65 sc1\n"
        "s_waitcnt vmcnt(0)\n"
        ".Lch_rd_chk_%=:\n"
        "buffer_load_dword v7, off, s[40:43], s65 sc1\n"
        "s_waitcnt vmcnt(0)\n"
        "v_readfirstlane_b32 s65, v7\n"
        "s_nop 0\n"
        "s_cmp_lg_u32 s65, 1\n"
        "s_cbranch_scc1 .Lch_rd_go_%=\n"
        "s_endpgm\n"
        ".Lch_rd_go_%=:\n"
        "s_cmp_lt_u32 s48, 2\n"
        "s_cbranch_scc1 .Lch_rd2_%=\n"
        "s_and_b32 s66, s33, 1024\n"
        "s_lshl_b32 s66, s66, 2\n"
        "s_add_u32 m0, s66, 151552\n"
        "s_add_u32 s66, s54, s71\n"
        "buffer_load_dwordx4 v3, s[40:43], s66 offen offset:0 sc1 lds\n"
        "buffer_load_dwordx4 v3, s[40:43], s66 offen offset:1024 sc1 lds\n"
        "buffer_load_dwordx4 v3, s[40:43], s66 offen offset:2048 sc1 lds\n"
        "buffer_load_dwordx4 v3, s[40:43], s66 offen offset:3072 sc1 lds\n"
        "ds_read_b32 v7, v2 offset:33796\n"
        "s_waitcnt lgkmcnt(0)\n"
        "s_mov_b32 m0, 159744\n"
        "s_add_u32 s66, s71, s55\n"
        "buffer_load_dwordx4 v7, s[40:43], s66 offen sc1 lds\n"
        ".Lch_rd2_%=:\n"
        "s_nop 7\n"
        "ds_read_b128 v[40:43], v2 offset:0\n"
        "ds_read_b128 v[44:47], v2 offset:1024\n"
        "ds_read_b128 v[48:51], v2 offset:2048\n"
        "ds_read_b128 v[52:55], v2 offset:3072\n"
        "ds_read_b128 v[56:59], v2 offset:4096\n"
        "ds_read_b128 v[60:63], v2 offset:5120\n"
        "v_mov_b64 v[8:9], 0\n"
        "v_mov_b64 v[10:11], 0\n"
        "v_mov_b64 v[12:13], 0\n"
        "v_mov_b64 v[14:15], 0\n"
        "v_mov_b64 v[16:17], 0\n"
        "v_mov_b64 v[18:19], 0\n"
        "v_mov_b64 v[20:21], 0\n"
        "v_mov_b64 v[22:23], 0\n"
        "v_mov_b64 v[24:25], 0\n"
        "v_mov_b64 v[26:27], 0\n"
        "v_mov_b64 v[28:29], 0\n"
        "v_mov_b64 v[30:31], 0\n"
        "v_mov_b32 v6, 0\n"
        "s_waitcnt vmcnt(0)\n"
        "s_branch .Lch_A_%=\n"
        ".Lch_slow_%=:\n"
        "v_max3_u32 v6, v6, v38, v39\n"
        "v_max3_u32 v6, v6, v48, v49\n"
        "v_max3_u32 v6, v6, v50, v51\n"
        "v_cmp_eq_u32 vcc, -1, v6\n"
        "s_cbranch_vccnz .Lch_redo_%=\n"
        "s_waitcnt vmcnt(0) lgkmcnt(0)\n"
        "s_cmp_eq_u32 s30, s69\n"
        "s_cbranch_scc1 .Lch_slow3_%=\n"
        "v_readfirstlane_b32 s65, v76\n"
        "s_cmp_eq_u32 s51, 63\n"
        "s_cselect_b32 s65, -2, s65\n"
        "s_cmp_ge_i32 s65, s80\n"
        "s_cbranch_scc1 .Lch_kvw_ok_%=\n"
        "ds_read_b128 v[68:71], v4 offset:10240\n"
        "v_mov_b64 v[72:73], v[24:25]\n"
        "v_mov_b64 v[74:75], v[26:27]\n"
        "v_mov_b64 v[76:77], v[28:29]\n"
        "v_mov_b64 v[78:79], v[30:31]\n"
        "v_cvt_pk_bf16_f32 v64, v12, v13\n"
        "v_cvt_pk_bf16_f32 v65, v14, v15\n"
        "v_cvt_pk_bf16_f32 v66, v16, v17\n"
        "v_cvt_pk_bf16_f32 v67, v18, v19\n"
        "v_smfmac_f32_16x16x64_bf16 v[72:75], v[36:39], v[56:63], v0\n"
        "v_smfmac_f32_16x16x64_bf16 v[76:79], v[36:39], v[40:47], v0\n"
        "v_smfmac_f32_16x16x64_bf16 v[72:75], v[48:51], v[240:247], v0\n"
        "v_smfmac_f32_16x16x64_bf16 v[76:79], v[48:51], v[248:255], v0\n"
        "s_waitcnt lgkmcnt(0)\n"
        "buffer_store_dwordx4 v[64:67], v68, s[40:43], s70 offen sc1\n"
        "s_nop 5\n"
        "v_add_f32 v72, v72, v75\n"
        "v_add_f32 v73, v73, v74\n"
        "v_add_f32 v76, v76, v79\n"
        "v_add_f32 v77, v77, v78\n"
        "v_cvt_pk_bf16_f32 v72, v72, v73\n"
        "v_cvt_pk_bf16_f32 v74, v76, v77\n"
        "s_nop 1\n"
        "v_permlane16_swap_b32 v72, v73\n"
        "v_permlane16_swap_b32 v74, v75\n"
        "buffer_store_dwordx4 v[72:75], v69, s[40:43], s70 offen offset:2048 sc1\n"
        "s_mov_b64 exec, 1\n"
        "buffer_load_dword v64, off, s[40:43], s78 sc1\n"
        "s_mov_b64 exec, -1\n"
        "s_waitcnt vmcnt(0)\n"
        "v_mov_b32 v7, s51\n"
        "s_mov_b64 exec, 1\n"
        "buffer_store_dword v7, off, s[40:43], s79 sc1\n"
        "s_mov_b64 exec, -1\n"
        "s_cmp_eq_u32 s51, 63\n"
        "s_cbranch_scc0 .Lch_slow2_%=\n"
        "s_or_b32 s65, s49, s48\n"
        "s_cbranch_scc1 .Lch_bye_%=\n"
        "s_lshl_b32 s66, s50, 7\n"
        "s_add_u32 s66, s66, 51200\n"
        ".Lch_bye_%=:\n"
        "s_endpgm\n"
        ".Lch_slow2_%=:\n"
        "s_mov_b32 s70, 241172480\n"
        "v_readfirstlane_b32 s65, v64\n"
        "s_nop 0\n"
        "s_cmp_ge_i32 s65, s80\n"
        "s_cbranch_scc1 .Lch_kvw_ok_%=\n"
        "s_mov_b32 s64, 0\n"
        ".Lch_kvw_top_%=:\n"
        "s_mov_b64 exec, 1\n"
        "buffer_load_dword v7, off, s[40:43], s78 sc1\n"
        "s_mov_b64 exec, -1\n"
        "s_waitcnt vmcnt(0)\n"
        "v_cmp_gt_i32 vcc, s80, v7\n"
        "s_nop 0\n"
        "s_and_b64 vcc, vcc, 1\n"
        "s_cbranch_vccz .Lch_kvw_ok_%=\n"
        "s_add_u32 s64, s64, 1\n"
        "s_and_b32 s65, s64, 255\n"
        "s_cbranch_scc1 .Lch_kvw_go_%=\n"
        "s_mov_b32 s65, 49152\n"
        "s_cmp_lt_u32 s64, 1048576\n"
        "s_cbranch_scc1 .Lch_kvw_chk_%=\n"
        "v_mov_b32 v7, 1\n"
        "buffer_store_dword v7, off, s[40:43], s65 sc1\n"
        "s_waitcnt vmcnt(0)\n"
        ".Lch_kvw_chk_%=:\n"
        "buffer_load_dword v7, off, s[40:43], s65 sc1\n"
        "s_waitcnt vmcnt(0)\n"
        "v_readfirstlane_b32 s65, v7\n"
        "s_nop 0\n"
        "s_cmp_lg_u32 s65, 1\n"
        "s_cbranch_scc1 .Lch_kvw_go_%=\n"
        "s_endpgm\n"
        ".Lch_kvw_go_%=:\n"
        "s_branch .Lch_kvw_top_%=\n"
        ".Lch_kvw_ok_%=:\n"
        "s_add_u32 s89, s69, 524288\n"
        "buffer_load_dwordx4 v[68:71], v3, s[40:43], s69 offen offset:1024 sc1\n"
        "buffer_load_dwordx4 v[64:67], v3, s[40:43], s69 offen sc1\n"
        "buffer_load_dwordx4 v[72:75], v3, s[40:43], s89 offen sc1\n"
        "buffer_load_dwordx4 v[76:79], v3, s[40:43], s89 offen offset:1024 sc1\n"
        "s_mov_b32 s77, s89\n"
        ".Lch_slow3_%=:\n"
        "s_barrier\n"
        "s_memtime s[36:37]\n"
        "s_waitcnt lgkmcnt(0)\n"
        "s_add_u32 s36, s36, 256\n"
        "s_sub_u32 s37, s36, s38\n"
        "v_and_b32 v7, -2, s37\n"
        "s_mov_b64 exec, 0xff\n"
        "buffer_store_dwordx4 v[4:7], v3, s[40:43], s46 offen sc1\n"
        "s_mov_b64 exec, -1\n"
        "s_mov_b32 s30, s69\n"
        "s_branch .Lch_back_%=\n"
        ".Lch_end_%=:\n"
        :: "s"(p.ws), "s"(p.Wq), "s"(p.Wk), "s"(p.Wv), "s"(p.Wo), "s"(p.mem0), "s"(pair(w, h)), "s"(pair(x4, pr)), "s"(p.tune0), "s"(p.tune1),
           "v"(pair(idx, va + (lane & 2) * 32)), "v"(pair(wb + lane * 16, lane * 16)), "v"(pair(spr, CH_IMG + lane * 64 + c * 4)),
           "v"(pair(qo(0), qo(1))), "v"(pair(oo, o2)), "v"(pair(tr + (q >> 1) * 32, m0)), "v"(pair(to(0), to(1))), "v"(pair(tq(0, 0), tq(0, 1))), "v"(pair(tq(1, 0), tq(1, 1)))
        : "v0", "v1", "v2", "v3", "v4", "v5", "v6", "v7", "v8", "v9", "v10", "v11", "v12", "v13", "v14", "v15", "v16", "v17", "v18", "v19", "v20", "v21", "v22", "v23", "v24", "v25", "v26", "v27", "v28", "v29", "v30", "v31", "v32", "v33", "v34", "v35", "v36", "v37", "v38", "v39", "v40", "v41", "v42", "v43", "v44", "v45", "v46", "v47", "v48", "v49", "v50", "v51", "v52", "v53", "v54", "v55", "v56", "v57", "v58", "v59", "v60", "v61", "v62", "v63", "v64", "v65", "v66", "v67", "v68", "v69", "v70", "v71", "v72", "v73", "v74", "v75", "v76", "v77", "v78", "v79", "v255", "a255", "s26", "s27", "s28", "s29", "s30", "s31", "s32", "s33", "s34", "s35", "s36", "s37", "s38", "s39", "s40", "s41", "s42", "s43", "s44", "s45", "s46", "s47", "s48", "s49", "s50", "s51", "s52", "s53", "s54", "s55", "s56", "s57", "s58", "s59", "s60", "s61", "s62", "s63", "s64", "s65", "s66", "s67", "s68", "s69", "s70", "s71", "s72", "s73", "s74", "s75", "s76", "s77", "s78", "s79", "s80", "s81", "s82", "s83", "s84", "s85", "s86", "s87", "s88", "s89", "s90", "s91", "s92", "s93", "s94", "s95", "s96", "s97", "s98", "s99", "vcc", "scc", "memory");
    __builtin_unreachable();
}

constexpr unsigned flag(unsigned i) { return spread(LF0 + i * 128); }
#define GEMM_HEAD const unsigned lane = threadIdx.x & 63, i = lane & 15, g = lane >> 4, q = i >> 2, pp = lane & 3; const int w = rfl(threadIdx.x >> 6)
#define TILE_ARGS(base) "s"(pair(base + (w >> 1) * 16384 + (w & 1) * 4096, out1)), "s"(pair(fout, ((w >> 1) * 9216 + (w & 1) * 4096) | ((w >> 1) * 9216 + 8192) << 16)), \
        "s"(pair(base + 65536 + (w >> 1) * 1024, out2)), "v"(pair((i >> 3) * 9216 + g * 128 + (i & 7) * 16, lane * 16))
__device__ __forceinline__ void role_s1(const Params& p, int h, int x4, int pr, int delay) {
    GEMM_HEAD;
    const unsigned vo = (8 * g + q) * 8192 + (128 * h + 32 * w) * 2 + (pp ^ (2 * (g & 1))) * 16;
    const unsigned tr = w * 40960 + (4 * g + q) * 64 + pp * 8;
    const unsigned out1 = PAGE + h * 16384 + w * 2048, out2 = 0, fout = flag(F_S1 + 4 * h + w);
    asm volatile(
        "s_nop 4\n"
        "s_mov_b64 s[40:41], %0\n"
        "s_and_b32 s41, s41, 0xffff\n"
        "s_mov_b32 s42, 236978176\n"
        "s_mov_b32 s43, 0x20000\n"
        "s_mov_b64 s[48:49], %4\n"
        "s_mov_b64 s[56:57], %5\n"
        "s_mov_b64 s[58:59], %6\n"
        "s_mov_b64 s[60:61], %7\n"
        "s_mov_b64 s[62:63], %8\n"
        "v_mov_b64 v[0:1], %9\n"
        "v_mov_b64 v[2:3], %10\n"
        "v_mov_b64 v[4:5], %11\n"
        "v_mov_b64 v[6:7], %12\n"
        "v_mov_b64 v[8:9], %13\n"
        "v_mov_b64 v[10:11], %14\n"
        "s_mov_b32 s47, 0x20000\n"
        "s_mul_i32 s79, s48, 40960\n"
        ".Ls1_start_%=:\n"
        "s_sub_u32 s49, s49, 1\n"
        "s_cbranch_scc1 .Ls1_start2_%=\n"
        "s_sleep 16\n"
        "s_branch .Ls1_start_%=\n"
        ".Ls1_start2_%=:\n"
        "s_mov_b64 s[44:45], %3\n"
        "s_and_b32 s45, s45, 0xffff\n"
        "s_mov_b32 s46, 4718592\n"
        "s_add_u32 m0, s79, 0\n"
        "s_mov_b32 s65, 0\n"
        "buffer_load_dwordx4 v8, s[44:47], s65 offen lds\n"
        "s_mov_b32 s65, 31744\n"
        "buffer_load_dwordx4 v8, s[44:47], s65 offen offset:1024 lds\n"
        "s_add_u32 m0, s79, 2048\n"
        "s_mov_b32 s65, 262144\n"
        "buffer_load_dwordx4 v8, s[44:47], s65 offen lds\n"
        "s_mov_b32 s65, 293888\n"
        "buffer_load_dwordx4 v8, s[44:47], s65 offen offset:1024 lds\n"
        "s_add_u32 m0, s79, 4096\n"
        "s_mov_b32 s65, 524288\n"
        "buffer_load_dwordx4 v8, s[44:47], s65 offen lds\n"
        "s_mov_b32 s65, 556032\n"
        "buffer_load_dwordx4 v8, s[44:47], s65 offen offset:1024 lds\n"
        "s_waitcnt vmcnt(4)\n"
        "ds_read_b64_tr_b16 a[16:17], v10 offset:0\n"
        "ds_read_b64_tr_b16 a[18:19], v10 offset:1024\n"
        "ds_read_b64_tr_b16 a[20:21], v11 offset:0\n"
        "ds_read_b64_tr_b16 a[22:23], v11 offset:1024\n"
        "s_waitcnt vmcnt(2)\n"
        "ds_read_b64_tr_b16 a[40:41], v10 offset:2048\n"
        "ds_read_b64_tr_b16 a[42:43], v10 offset:3072\n"
        "ds_read_b64_tr_b16 a[44:45], v11 offset:2048\n"
        "ds_read_b64_tr_b16 a[46:47], v11 offset:3072\n"
        "s_waitcnt lgkmcnt(4)\n"
        "s_add_u32 m0, s79, 0\n"
        "s_mov_b32 s65, 786432\n"
        "buffer_load_dwordx4 v8, s[44:47], s65 offen lds\n"
        "s_mov_b32 s65, 818176\n"
        "buffer_load_dwordx4 v8, s[44:47], s65 offen offset:1024 lds\n"
        "s_waitcnt vmcnt(2)\n"
        "ds_read_b64_tr_b16 a[64:65], v10 offset:4096\n"
        "ds_read_b64_tr_b16 a[66:67], v10 offset:5120\n"
        "ds_read_b64_tr_b16 a[68:69], v11 offset:4096\n"
        "ds_read_b64_tr_b16 a[70:71], v11 offset:5120\n"
        "s_waitcnt lgkmcnt(4)\n"
        "s_add_u32 m0, s79, 2048\n"
        "s_mov_b32 s65, 1048576\n"
        "buffer_load_dwordx4 v8, s[44:47], s65 offen lds\n"
        "s_mov_b32 s65, 1080320\n"
        "buffer_load_dwordx4 v8, s[44:47], s65 offen offset:1024 lds\n"
        "s_waitcnt vmcnt(2)\n"
        "ds_read_b64_tr_b16 a[88:89], v10 offset:0\n"
        "ds_read_b64_tr_b16 a[90:91], v10 offset:1024\n"
        "ds_read_b64_tr_b16 a[92:93], v11 offset:0\n"
        "ds_read_b64_tr_b16 a[94:95], v11 offset:1024\n"
        "s_waitcnt lgkmcnt(4)\n"
        "s_add_u32 m0, s79, 4096\n"
        "s_mov_b32 s65, 1310720\n"
        "buffer_load_dwordx4 v8, s[44:47], s65 offen lds\n"
        "s_mov_b32 s65, 1342464\n"
        "buffer_load_dwordx4 v8, s[44:47], s65 offen offset:1024 lds\n"
        "s_waitcnt vmcnt(2)\n"
        "ds_read_b64_tr_b16 a[112:113], v10 offset:2048\n"
        "ds_read_b64_tr_b16 a[114:115], v10 offset:3072\n"
        "ds_read_b64_tr_b16 a[116:117], v11 offset:2048\n"
        "ds_read_b64_tr_b16 a[118:119], v11 offset:3072\n"
        "s_waitcnt lgkmcnt(4)\n"
        "s_add_u32 m0, s79, 0\n"
        "s_mov_b32 s65, 1572864\n"
        "buffer_load_dwordx4 v8, s[44:47], s65 offen lds\n"
        "s_mov_b32 s65, 1604608\n"
        "buffer_load_dwordx4 v8, s[44:47], s65 offen offset:1024 lds\n"
        "s_waitcnt vmcnt(2)\n"
        "ds_read_b64_tr_b16 a[136:137], v10 offset:4096\n"
        "ds_read_b64_tr_b16 a[138:139], v10 offset:5120\n"
        "ds_read_b64_tr_b16 a[140:141], v11 offset:4096\n"
        "ds_read_b64_tr_b16 a[142:143], v11 offset:5120\n"
        "s_waitcnt lgkmcnt(4)\n"
        "s_add_u32 m0, s79, 2048\n"
        "s_mov_b32 s65, 1835008\n"
        "buffer_load_dwordx4 v8, s[44:47], s65 offen lds\n"
        "s_mov_b32 s65, 1866752\n"
        "buffer_load_dwordx4 v8, s[44:47], s65 offen offset:1024 lds\n"
        "s_waitcnt vmcnt(2)\n"
        "ds_read_b64_tr_b16 a[160:161], v10 offset:0\n"
        "ds_read_b64_tr_b16 a[162:163], v10 offset:1024\n"
        "ds_read_b64_tr_b16 a[164:165], v11 offset:0\n"
        "ds_read_b64_tr_b16 a[166:167], v11 offset:1024\n"
        "s_waitcnt lgkmcnt(4)\n"
        "s_add_u32 m0, s79, 4096\n"
        "s_mov_b32 s65, 2097152\n"
        "buffer_load_dwordx4 v8, s[44:47], s65 offen lds\n"
        "s_mov_b32 s65, 2128896\n"
        "buffer_load_dwordx4 v8, s[44:47], s65 offen offset:1024 lds\n"
        "s_waitcnt vmcnt(2)\n"
        "ds_read_b64_tr_b16 a[184:185], v10 offset:2048\n"
        "ds_read_b64_tr_b16 a[186:187], v10 offset:3072\n"
        "ds_read_b64_tr_b16 a[188:189], v11 offset:2048\n"
        "ds_read_b64_tr_b16 a[190:191], v11 offset:3072\n"
        "s_waitcnt lgkmcnt(4)\n"
        "s_add_u32 m0, s79, 0\n"
        "s_mov_b32 s65, 2359296\n"
        "buffer_load_dwordx4 v8, s[44:47], s65 offen lds\n"
        "s_mov_b32 s65, 2391040\n"
        "buffer_load_dwordx4 v8, s[44:47], s65 offen offset:1024 lds\n"
        "s_waitcnt vmcnt(2)\n"
        "ds_read_b64_tr_b16 a[208:209], v10 offset:4096\n"
        "ds_read_b64_tr_b16 a[210:211], v10 offset:5120\n"
        "ds_read_b64_tr_b16 a[212:213], v11 offset:4096\n"
        "ds_read_b64_tr_b16 a[214:215], v11 offset:5120\n"
        "s_waitcnt lgkmcnt(4)\n"
        "s_add_u32 m0, s79, 2048\n"
        "s_mov_b32 s65, 2621440\n"
        "buffer_load_dwordx4 v8, s[44:47], s65 offen lds\n"
        "s_mov_b32 s65, 2653184\n"
        "buffer_load_dwordx4 v8, s[44:47], s65 offen offset:1024 lds\n"
        "s_waitcnt vmcnt(2)\n"
        "ds_read_b64_tr_b16 a[232:233], v10 offset:0\n"
        "ds_read_b64_tr_b16 a[234:235], v10 offset:1024\n"
        "ds_read_b64_tr_b16 a[236:237], v11 offset:0\n"
        "ds_read_b64_tr_b16 a[238:239], v11 offset:1024\n"
        "s_waitcnt lgkmcnt(4)\n"
        "s_add_u32 m0, s79, 4096\n"
        "s_mov_b32 s65, 2883584\n"
        "buffer_load_dwordx4 v8, s[44:47], s65 offen lds\n"
        "s_mov_b32 s65, 2915328\n"
        "buffer_load_dwordx4 v8, s[44:47], s65 offen offset:1024 lds\n"
        "s_waitcnt vmcnt(2)\n"
        "ds_read_b64_tr_b16 v[80:81], v10 offset:2048\n"
        "ds_read_b64_tr_b16 v[82:83], v10 offset:3072\n"
        "ds_read_b64_tr_b16 v[84:85], v11 offset:2048\n"
        "ds_read_b64_tr_b16 v[86:87], v11 offset:3072\n"
        "s_waitcnt lgkmcnt(4)\n"
        "s_add_u32 m0, s79, 0\n"
        "s_mov_b32 s65, 3145728\n"
        "buffer_load_dwordx4 v8, s[44:47], s65 offen lds\n"
        "s_mov_b32 s65, 3177472\n"
        "buffer_load_dwordx4 v8, s[44:47], s65 offen offset:1024 lds\n"
        "s_waitcnt vmcnt(2)\n"
        "ds_read_b64_tr_b16 v[104:105], v10 offset:4096\n"
        "ds_read_b64_tr_b16 v[106:107], v10 offset:5120\n"
        "ds_read_b64_tr_b16 v[108:109], v11 offset:4096\n"
        "ds_read_b64_tr_b16 v[110:111], v11 offset:5120\n"
        "s_waitcnt lgkmcnt(4)\n"
        "s_add_u32 m0, s79, 2048\n"
        "s_mov_b32 s65, 3407872\n"
        "buffer_load_dwordx4 v8, s[44:47], s65 offen lds\n"
        "s_mov_b32 s65, 3439616\n"
        "buffer_load_dwordx4 v8, s[44:47], s65 offen offset:1024 lds\n"
        "s_waitcnt vmcnt(2)\n"
        "ds_read_b64_tr_b16 v[128:129], v10 offset:0\n"
        "ds_read_b64_tr_b16 v[130:131], v10 offset:1024\n"
        "ds_read_b64_tr_b16 v[132:133], v11 offset:0\n"
        "ds_read_b64_tr_b16 v[134:135], v11 offset:1024\n"
        "s_waitcnt lgkmcnt(4)\n"
        "s_add_u32 m0, s79, 4096\n"
        "s_mov_b32 s65, 3670016\n"
        "buffer_load_dwordx4 v8, s[44:47], s65 offen lds\n"
        "s_mov_b32 s65, 3701760\n"
        "buffer_load_dwordx4 v8, s[44:47], s65 offen offset:1024 lds\n"
        "s_waitcnt vmcnt(2)\n"
        "ds_read_b64_tr_b16 v[152:153], v10 offset:2048\n"
        "ds_read_b64_tr_b16 v[154:155], v10 offset:3072\n"
        "ds_read_b64_tr_b16 v[156:157], v11 offset:2048\n"
        "ds_read_b64_tr_b16 v[158:159], v11 offset:3072\n"
        "s_waitcnt lgkmcnt(4)\n"
        "s_add_u32 m0, s79, 0\n"
        "s_mov_b32 s65, 3932160\n"
        "buffer_load_dwordx4 v8, s[44:47], s65 offen lds\n"
        "s_mov_b32 s65, 3963904\n"
        "buffer_load_dwordx4 v8, s[44:47], s65 offen offset:1024 lds\n"
        "s_waitcnt vmcnt(2)\n"
        "ds_read_b64_tr_b16 v[176:177], v10 offset:4096\n"
        "ds_read_b64_tr_b16 v[178:179], v10 offset:5120\n"
        "ds_read_b64_tr_b16 v[180:181], v11 offset:4096\n"
        "ds_read_b64_tr_b16 v[182:183], v11 offset:5120\n"
        "s_waitcnt lgkmcnt(4)\n"
        "s_add_u32 m0, s79, 2048\n"
        "s_mov_b32 s65, 4194304\n"
        "buffer_load_dwordx4 v8, s[44:47], s65 offen lds\n"
        "s_mov_b32 s65, 4226048\n"
        "buffer_load_dwordx4 v8, s[44:47], s65 offen offset:1024 lds\n"
        "s_waitcnt vmcnt(2)\n"
        "ds_read_b64_tr_b16 v[200:201], v10 offset:0\n"
        "ds_read_b64_tr_b16 v[202:203], v10 offset:1024\n"
        "ds_read_b64_tr_b16 v[204:205], v11 offset:0\n"
        "ds_read_b64_tr_b16 v[206:207], v11 offset:1024\n"
        "s_waitcnt lgkmcnt(4)\n"
        "s_add_u32 m0, s79, 4096\n"
        "s_mov_b32 s65, 4456448\n"
        "buffer_load_dwordx4 v8, s[44:47], s65 offen lds\n"
        "s_mov_b32 s65, 4488192\n"
        "buffer_load_dwordx4 v8, s[44:47], s65 offen offset:1024 lds\n"
        "s_waitcnt vmcnt(2)\n"
        "ds_read_b64_tr_b16 v[224:225], v10 offset:2048\n"
        "ds_read_b64_tr_b16 v[226:227], v10 offset:3072\n"
        "ds_read_b64_tr_b16 v[228:229], v11 offset:2048\n"
        "ds_read_b64_tr_b16 v[230:231], v11 offset:3072\n"
        "s_waitcnt lgkmcnt(4)\n"
        "s_mov_b64 s[44:45], %1\n"
        "s_and_b32 s45, s45, 0xffff\n"
        "s_mov_b32 s46, 4718592\n"
        "s_add_u32 m0, s79, 0\n"
        "s_mov_b32 s65, 0\n"
        "buffer_load_dwordx4 v8, s[44:47], s65 offen lds\n"
        "s_mov_b32 s65, 31744\n"
        "buffer_load_dwordx4 v8, s[44:47], s65 offen offset:1024 lds\n"
        "s_waitcnt vmcnt(2)\n"
        "ds_read_b64_tr_b16 v[248:249], v10 offset:4096\n"
        "ds_read_b64_tr_b16 v[250:251], v10 offset:5120\n"
        "ds_read_b64_tr_b16 v[252:253], v11 offset:4096\n"
        "ds_read_b64_tr_b16 v[254:255], v11 offset:5120\n"
        "s_waitcnt lgkmcnt(4)\n"
        "s_add_u32 m0, s79, 2048\n"
        "s_mov_b32 s65, 262144\n"
        "buffer_load_dwordx4 v8, s[44:47], s65 offen lds\n"
        "s_mov_b32 s65, 293888\n"
        "buffer_load_dwordx4 v8, s[44:47], s65 offen offset:1024 lds\n"
        "s_waitcnt vmcnt(2)\n"
        "ds_read_b64_tr_b16 a[0:1], v10 offset:0\n"
        "ds_read_b64_tr_b16 a[2:3], v10 offset:1024\n"
        "ds_read_b64_tr_b16 a[4:5], v11 offset:0\n"
        "ds_read_b64_tr_b16 a[6:7], v11 offset:1024\n"
        "s_waitcnt lgkmcnt(4)\n"
        "s_add_u32 m0, s79, 4096\n"
        "s_mov_b32 s65, 524288\n"
        "buffer_load_dwordx4 v8, s[44:47], s65 offen lds\n"
        "s_mov_b32 s65, 556032\n"
        "buffer_load_dwordx4 v8, s[44:47], s65 offen offset:1024 lds\n"
        "s_waitcnt vmcnt(2)\n"
        "ds_read_b64_tr_b16 a[24:25], v10 offset:2048\n"
        "ds_read_b64_tr_b16 a[26:27], v10 offset:3072\n"
        "ds_read_b64_tr_b16 a[28:29], v11 offset:2048\n"
        "ds_read_b64_tr_b16 a[30:31], v11 offset:3072\n"
        "s_waitcnt lgkmcnt(4)\n"
        "s_add_u32 m0, s79, 0\n"
        "s_mov_b32 s65, 786432\n"
        "buffer_load_dwordx4 v8, s[44:47], s65 offen lds\n"
        "s_mov_b32 s65, 818176\n"
        "buffer_load_dwordx4 v8, s[44:47], s65 offen offset:1024 lds\n"
        "s_waitcnt vmcnt(2)\n"
        "ds_read_b64_tr_b16 a[48:49], v10 offset:4096\n"
        "ds_read_b64_tr_b16 a[50:51], v10 offset:5120\n"
        "ds_read_b64_tr_b16 a[52:53], v11 offset:4096\n"
        "ds_read_b64_tr_b16 a[54:55], v11 offset:5120\n"
        "s_waitcnt lgkmcnt(4)\n"
        "s_add_u32 m0, s79, 2048\n"
        "s_mov_b32 s65, 1048576\n"
        "buffer_load_dwordx4 v8, s[44:47], s65 offen lds\n"
        "s_mov_b32 s65, 1080320\n"
        "buffer_load_dwordx4 v8, s[44:47], s65 offen offset:1024 lds\n"
        "s_waitcnt vmcnt(2)\n"
        "ds_read_b64_tr_b16 a[72:73], v10 offset:0\n"
        "ds_read_b64_tr_b16 a[74:75], v10 offset:1024\n"
        "ds_read_b64_tr_b16 a[76:77], v11 offset:0\n"
        "ds_read_b64_tr_b16 a[78:79], v11 offset:1024\n"
        "s_waitcnt lgkmcnt(4)\n"
        "s_add_u32 m0, s79, 4096\n"
        "s_mov_b32 s65, 1310720\n"
        "buffer_load_dwordx4 v8, s[44:47], s65 offen lds\n"
        "s_mov_b32 s65, 1342464\n"
        "buffer_load_dwordx4 v8, s[44:47], s65 offen offset:1024 lds\n"
        "s_waitcnt vmcnt(2)\n"
        "ds_read_b64_tr_b16 a[96:97], v10 offset:2048\n"
        "ds_read_b64_tr_b16 a[98:99], v10 offset:3072\n"
        "ds_read_b64_tr_b16 a[100:101], v11 offset:2048\n"
        "ds_read_b64_tr_b16 a[102:103], v11 offset:3072\n"
        "s_waitcnt lgkmcnt(4)\n"
        "s_add_u32 m0, s79, 0\n"
        "s_mov_b32 s65, 1572864\n"
        "buffer_load_dwordx4 v8, s[44:47], s65 offen lds\n"
        "s_mov_b32 s65, 1604608\n"
        "buffer_load_dwordx4 v8, s[44:47], s65 offen offset:1024 lds\n"
        "s_waitcnt vmcnt(2)\n"
        "ds_read_b64_tr_b16 a[120:121], v10 offset:4096\n"
        "ds_read_b64_tr_b16 a[122:123], v10 offset:5120\n"
        "ds_read_b64_tr_b16 a[124:125], v11 offset:4096\n"
        "ds_read_b64_tr_b16 a[126:127], v11 offset:5120\n"
        "s_waitcnt lgkmcnt(4)\n"
        "s_add_u32 m0, s79, 2048\n"
        "s_mov_b32 s65, 1835008\n"
        "buffer_load_dwordx4 v8, s[44:47], s65 offen lds\n"
        "s_mov_b32 s65, 1866752\n"
        "buffer_load_dwordx4 v8, s[44:47], s65 offen offset:1024 lds\n"
        "s_waitcnt vmcnt(2)\n"
        "ds_read_b64_tr_b16 a[144:145], v10 offset:0\n"
        "ds_read_b64_tr_b16 a[146:147], v10 offset:1024\n"
        "ds_read_b64_tr_b16 a[148:149], v11 offset:0\n"
        "ds_read_b64_tr_b16 a[150:151], v11 offset:1024\n"
        "s_waitcnt lgkmcnt(4)\n"
        "s_add_u32 m0, s79, 4096\n"
        "s_mov_b32 s65, 2097152\n"
        "buffer_load_dwordx4 v8, s[44:47], s65 offen lds\n"
        "s_mov_b32 s65, 2128896\n"
        "buffer_load_dwordx4 v8, s[44:47], s65 offen offset:1024 lds\n"
        "s_waitcnt vmcnt(2)\n"
        "ds_read_b64_tr_b16 a[168:169], v10 offset:2048\n"
        "ds_read_b64_tr_b16 a[170:171], v10 offset:3072\n"
        "ds_read_b64_tr_b16 a[172:173], v11 offset:2048\n"
        "ds_read_b64_tr_b16 a[174:175], v11 offset:3072\n"
        "s_waitcnt lgkmcnt(4)\n"
        "s_add_u32 m0, s79, 0\n"
        "s_mov_b32 s65, 2359296\n"
        "buffer_load_dwordx4 v8, s[44:47], s65 offen lds\n"
        "s_mov_b32 s65, 2391040\n"
        "buffer_load_dwordx4 v8, s[44:47], s65 offen offset:1024 lds\n"
        "s_waitcnt vmcnt(2)\n"
        "ds_read_b64_tr_b16 a[192:193], v10 offset:4096\n"
        "ds_read_b64_tr_b16 a[194:195], v10 offset:5120\n"
        "ds_read_b64_tr_b16 a[196:197], v11 offset:4096\n"
        "ds_read_b64_tr_b16 a[198:199], v11 offset:5120\n"
        "s_waitcnt lgkmcnt(4)\n"
        "s_add_u32 m0, s79, 2048\n"
        "s_mov_b32 s65, 2621440\n"
        "buffer_load_dwordx4 v8, s[44:47], s65 offen lds\n"
        "s_mov_b32 s65, 2653184\n"
        "buffer_load_dwordx4 v8, s[44:47], s65 offen offset:1024 lds\n"
        "s_waitcnt vmcnt(2)\n"
        "ds_read_b64_tr_b16 a[216:217], v10 offset:0\n"
        "ds_read_b64_tr_b16 a[218:219], v10 offset:1024\n"
        "ds_read_b64_tr_b16 a[220:221], v11 offset:0\n"
        "ds_read_b64_tr_b16 a[222:223], v11 offset:1024\n"
        "s_waitcnt lgkmcnt(4)\n"
        "s_add_u32 m0, s79, 4096\n"
        "s_mov_b32 s65, 2883584\n"
        "buffer_load_dwordx4 v8, s[44:47], s65 offen lds\n"
        "s_mov_b32 s65, 2915328\n"
        "buffer_load_dwordx4 v8, s[44:47], s65 offen offset:1024 lds\n"
        "s_waitcnt vmcnt(2)\n"
        "ds_read_b64_tr_b16 a[240:241], v10 offset:2048\n"
        "ds_read_b64_tr_b16 a[242:243], v10 offset:3072\n"
        "ds_read_b64_tr_b16 a[244:245], v11 offset:2048\n"
        "ds_read_b64_tr_b16 a[246:247], v11 offset:3072\n"
        "s_waitcnt lgkmcnt(4)\n"
        "s_add_u32 m0, s79, 0\n"
        "s_mov_b32 s65, 3145728\n"
        "buffer_load_dwordx4 v8, s[44:47], s65 offen lds\n"
        "s_mov_b32 s65, 3177472\n"
        "buffer_load_dwordx4 v8, s[44:47], s65 offen offset:1024 lds\n"
        "s_waitcnt vmcnt(2)\n"
        "ds_read_b64_tr_b16 v[88:89], v10 offset:4096\n"
        "ds_read_b64_tr_b16 v[90:91], v10 offset:5120\n"
        "ds_read_b64_tr_b16 v[92:93], v11 offset:4096\n"
        "ds_read_b64_tr_b16 v[94:95], v11 offset:5120\n"
        "s_waitcnt lgkmcnt(4)\n"
        "s_add_u32 m0, s79, 2048\n"
        "s_mov_b32 s65, 3407872\n"
        "buffer_load_dwordx4 v8, s[44:47], s65 offen lds\n"
        "s_mov_b32 s65, 3439616\n"
        "buffer_load_dwordx4 v8, s[44:47], s65 offen offset:1024 lds\n"
        "s_waitcnt vmcnt(2)\n"
        "ds_read_b64_tr_b16 v[112:113], v10 offset:0\n"
        "ds_read_b64_tr_b16 v[114:115], v10 offset:1024\n"
        "ds_read_b64_tr_b16 v[116:117], v11 offset:0\n"
        "ds_read_b64_tr_b16 v[118:119], v11 offset:1024\n"
        "s_waitcnt lgkmcnt(4)\n"
        "s_add_u32 m0, s79, 4096\n"
        "s_mov_b32 s65, 3670016\n"
        "buffer_load_dwordx4 v8, s[44:47], s65 offen lds\n"
        "s_mov_b32 s65, 3701760\n"
        "buffer_load_dwordx4 v8, s[44:47], s65 offen offset:1024 lds\n"
        "s_waitcnt vmcnt(2)\n"
        "ds_read_b64_tr_b16 v[136:137], v10 offset:2048\n"
        "ds_read_b64_tr_b16 v[138:139], v10 offset:3072\n"
        "ds_read_b64_tr_b16 v[140:141], v11 offset:2048\n"
        "ds_read_b64_tr_b16 v[142:143], v11 offset:3072\n"
        "s_waitcnt lgkmcnt(4)\n"
        "s_add_u32 m0, s79, 0\n"
        "s_mov_b32 s65, 3932160\n"
        "buffer_load_dwordx4 v8, s[44:47], s65 offen lds\n"
        "s_mov_b32 s65, 3963904\n"
        "buffer_load_dwordx4 v8, s[44:47], s65 offen offset:1024 lds\n"
        "s_waitcnt vmcnt(2)\n"
        "ds_read_b64_tr_b16 v[160:161], v10 offset:4096\n"
        "ds_read_b64_tr_b16 v[162:163], v10 offset:5120\n"
        "ds_read_b64_tr_b16 v[164:165], v11 offset:4096\n"
        "ds_read_b64_tr_b16 v[166:167], v11 offset:5120\n"
        "s_waitcnt lgkmcnt(4)\n"
        "s_add_u32 m0, s79, 2048\n"
        "s_mov_b32 s65, 4194304\n"
        "buffer_load_dwordx4 v8, s[44:47], s65 offen lds\n"
        "s_mov_b32 s65, 4226048\n"
        "buffer_load_dwordx4 v8, s[44:47], s65 offen offset:1024 lds\n"
        "s_waitcnt vmcnt(2)\n"
        "ds_read_b64_tr_b16 v[184:185], v10 offset:0\n"
        "ds_read_b64_tr_b16 v[186:187], v10 offset:1024\n"
        "ds_read_b64_tr_b16 v[188:189], v11 offset:0\n"
        "ds_read_b64_tr_b16 v[190:191], v11 offset:1024\n"
        "s_waitcnt lgkmcnt(4)\n"
        "s_add_u32 m0, s79, 4096\n"
        "s_mov_b32 s65, 4456448\n"
        "buffer_load_dwordx4 v8, s[44:47], s65 offen lds\n"
        "s_mov_b32 s65, 4488192\n"
        "buffer_load_dwordx4 v8, s[44:47], s65 offen offset:1024 lds\n"
        "s_waitcnt vmcnt(2)\n"
        "ds_read_b64_tr_b16 v[208:209], v10 offset:2048\n"
        "ds_read_b64_tr_b16 v[210:211], v10 offset:3072\n"
        "ds_read_b64_tr_b16 v[212:213], v11 offset:2048\n"
        "ds_read_b64_tr_b16 v[214:215], v11 offset:3072\n"
        "s_waitcnt lgkmcnt(4)\n"
        "s_mov_b64 s[44:45], %2\n"
        "s_and_b32 s45, s45, 0xffff\n"
        "s_mov_b32 s46, 4718592\n"
        "s_add_u32 m0, s79, 0\n"
        "s_mov_b32 s65, 0\n"
        "buffer_load_dwordx4 v8, s[44:47], s65 offen lds\n"
        "s_mov_b32 s65, 31744\n"
        "buffer_load_dwordx4 v8, s[44:47], s65 offen offset:1024 lds\n"
        "s_waitcnt vmcnt(2)\n"
        "ds_read_b64_tr_b16 v[232:233], v10 offset:4096\n"
        "ds_read_b64_tr_b16 v[234:235], v10 offset:5120\n"
        "ds_read_b64_tr_b16 v[236:237], v11 offset:4096\n"
        "ds_read_b64_tr_b16 v[238:239], v11 offset:5120\n"
        "s_waitcnt lgkmcnt(4)\n"
        "s_add_u32 m0, s79, 2048\n"
        "s_mov_b32 s65, 262144\n"
        "buffer_load_dwordx4 v8, s[44:47], s65 offen lds\n"
        "s_mov_b32 s65, 293888\n"
        "buffer_load_dwordx4 v8, s[44:47], s65 offen offset:1024 lds\n"
        "s_waitcnt vmcnt(2)\n"
        "ds_read_b64_tr_b16 a[8:9], v10 offset:0\n"
        "ds_read_b64_tr_b16 a[10:11], v10 offset:1024\n"
        "ds_read_b64_tr_b16 a[12:13], v11 offset:0\n"
        "ds_read_b64_tr_b16 a[14:15], v11 offset:1024\n"
        "s_waitcnt lgkmcnt(4)\n"
        "s_add_u32 m0, s79, 4096\n"
        "s_mov_b32 s65, 524288\n"
        "buffer_load_dwordx4 v8, s[44:47], s65 offen lds\n"
        "s_mov_b32 s65, 556032\n"
        "buffer_load_dwordx4 v8, s[44:47], s65 offen offset:1024 lds\n"
        "s_waitcnt vmcnt(2)\n"
        "ds_read_b64_tr_b16 a[32:33], v10 offset:2048\n"
        "ds_read_b64_tr_b16 a[34:35], v10 offset:3072\n"
        "ds_read_b64_tr_b16 a[36:37], v11 offset:2048\n"
        "ds_read_b64_tr_b16 a[38:39], v11 offset:3072\n"
        "s_waitcnt lgkmcnt(4)\n"
        "s_add_u32 m0, s79, 0\n"
        "s_mov_b32 s65, 786432\n"
        "buffer_load_dwordx4 v8, s[44:47], s65 offen lds\n"
        "s_mov_b32 s65, 818176\n"
        "buffer_load_dwordx4 v8, s[44:47], s65 offen offset:1024 lds\n"
        "s_waitcnt vmcnt(2)\n"
        "ds_read_b64_tr_b16 a[56:57], v10 offset:4096\n"
        "ds_read_b64_tr_b16 a[58:59], v10 offset:5120\n"
        "ds_read_b64_tr_b16 a[60:61], v11 offset:4096\n"
        "ds_read_b64_tr_b16 a[62:63], v11 offset:5120\n"
        "s_waitcnt lgkmcnt(4)\n"
        "s_add_u32 m0, s79, 2048\n"
        "s_mov_b32 s65, 1048576\n"
        "buffer_load_dwordx4 v8, s[44:47], s65 offen lds\n"
        "s_mov_b32 s65, 1080320\n"
        "buffer_load_dwordx4 v8, s[44:47], s65 offen offset:1024 lds\n"
        "s_waitcnt vmcnt(2)\n"
        "ds_read_b64_tr_b16 a[80:81], v10 offset:0\n"
        "ds_read_b64_tr_b16 a[82:83], v10 offset:1024\n"
        "ds_read_b64_tr_b16 a[84:85], v11 offset:0\n"
        "ds_read_b64_tr_b16 a[86:87], v11 offset:1024\n"
        "s_waitcnt lgkmcnt(4)\n"
        "s_add_u32 m0, s79, 4096\n"
        "s_mov_b32 s65, 1310720\n"
        "buffer_load_dwordx4 v8, s[44:47], s65 offen lds\n"
        "s_mov_b32 s65, 1342464\n"
        "buffer_load_dwordx4 v8, s[44:47], s65 offen offset:1024 lds\n"
        "s_waitcnt vmcnt(2)\n"
        "ds_read_b64_tr_b16 a[104:105], v10 offset:2048\n"
        "ds_read_b64_tr_b16 a[106:107], v10 offset:3072\n"
        "ds_read_b64_tr_b16 a[108:109], v11 offset:2048\n"
        "ds_read_b64_tr_b16 a[110:111], v11 offset:3072\n"
        "s_waitcnt lgkmcnt(4)\n"
        "s_add_u32 m0, s79, 0\n"
        "s_mov_b32 s65, 1572864\n"
        "buffer_load_dwordx4 v8, s[44:47], s65 offen lds\n"
        "s_mov_b32 s65, 1604608\n"
        "buffer_load_dwordx4 v8, s[44:47], s65 offen offset:1024 lds\n"
        "s_waitcnt vmcnt(2)\n"
        "ds_read_b64_tr_b16 a[128:129], v10 offset:4096\n"
        "ds_read_b64_tr_b16 a[130:131], v10 offset:5120\n"
        "ds_read_b64_tr_b16 a[132:133], v11 offset:4096\n"
        "ds_read_b64_tr_b16 a[134:135], v11 offset:5120\n"
        "s_waitcnt lgkmcnt(4)\n"
        "s_add_u32 m0, s79, 2048\n"
        "s_mov_b32 s65, 1835008\n"
        "buffer_load_dwordx4 v8, s[44:47], s65 offen lds\n"
        "s_mov_b32 s65, 1866752\n"
        "buffer_load_dwordx4 v8, s[44:47], s65 offen offset:1024 lds\n"
        "s_waitcnt vmcnt(2)\n"
        "ds_read_b64_tr_b16 a[152:153], v10 offset:0\n"
        "ds_read_b64_tr_b16 a[154:155], v10 offset:1024\n"
        "ds_read_b64_tr_b16 a[156:157], v11 offset:0\n"
        "ds_read_b64_tr_b16 a[158:159], v11 offset:1024\n"
        "s_waitcnt lgkmcnt(4)\n"
        "s_add_u32 m0, s79, 4096\n"
        "s_mov_b32 s65, 2097152\n"
        "buffer_load_dwordx4 v8, s[44:47], s65 offen lds\n"
        "s_mov_b32 s65, 2128896\n"
        "buffer_load_dwordx4 v8, s[44:47], s65 offen offset:1024 lds\n"
        "s_waitcnt vmcnt(2)\n"
        "ds_read_b64_tr_b16 a[176:177], v10 offset:2048\n"
        "ds_read_b64_tr_b16 a[178:179], v10 offset:3072\n"
        "ds_read_b64_tr_b16 a[180:181], v11 offset:2048\n"
        "ds_read_b64_tr_b16 a[182:183], v11 offset:3072\n"
        "s_waitcnt lgkmcnt(4)\n"
        "s_add_u32 m0, s79, 0\n"
        "s_mov_b32 s65, 2359296\n"
        "buffer_load_dwordx4 v8, s[44:47], s65 offen lds\n"
        "s_mov_b32 s65, 2391040\n"
        "buffer_load_dwordx4 v8, s[44:47], s65 offen offset:1024 lds\n"
        "s_waitcnt vmcnt(2)\n"
        "ds_read_b64_tr_b16 a[200:201], v10 offset:4096\n"
        "ds_read_b64_tr_b16 a[202:203], v10 offset:5120\n"
        "ds_read_b64_tr_b16 a[204:205], v11 offset:4096\n"
        "ds_read_b64_tr_b16 a[206:207], v11 offset:5120\n"
        "s_waitcnt lgkmcnt(4)\n"
        "s_add_u32 m0, s79, 2048\n"
        "s_mov_b32 s65, 2621440\n"
        "buffer_load_dwordx4 v8, s[44:47], s65 offen lds\n"
        "s_mov_b32 s65, 2653184\n"
        "buffer_load_dwordx4 v8, s[44:47], s65 offen offset:1024 lds\n"
        "s_waitcnt vmcnt(2)\n"
        "ds_read_b64_tr_b16 a[224:225], v10 offset:0\n"
        "ds_read_b64_tr_b16 a[226:227], v10 offset:1024\n"
        "ds_read_b64_tr_b16 a[228:229], v11 offset:0\n"
        "ds_read_b64_tr_b16 a[230:231], v11 offset:1024\n"
        "s_waitcnt lgkmcnt(4)\n"
        "s_add_u32 m0, s79, 4096\n"
        "s_mov_b32 s65, 2883584\n"
        "buffer_load_dwordx4 v8, s[44:47], s65 offen lds\n"
        "s_mov_b32 s65, 2915328\n"
        "buffer_load_dwordx4 v8, s[44:47], s65 offen offset:1024 lds\n"
        "s_waitcnt vmcnt(2)\n"
        "ds_read_b64_tr_b16 a[248:249], v10 offset:2048\n"
        "ds_read_b64_tr_b16 a[250:251], v10 offset:3072\n"
        "ds_read_b64_tr_b16 a[252:253], v11 offset:2048\n"
        "ds_read_b64_tr_b16 a[254:255], v11 offset:3072\n"
        "s_waitcnt lgkmcnt(4)\n"
        "s_add_u32 m0, s79, 0\n"
        "s_mov_b32 s65, 3145728\n"
        "buffer_load_dwordx4 v8, s[44:47], s65 offen lds\n"
        "s_mov_b32 s65, 3177472\n"
        "buffer_load_dwordx4 v8, s[44:47], s65 offen offset:1024 lds\n"
        "s_waitcnt vmcnt(2)\n"
        "ds_read_b64_tr_b16 v[96:97], v10 offset:4096\n"
        "ds_read_b64_tr_b16 v[98:99], v10 offset:5120\n"
        "ds_read_b64_tr_b16 v[100:101], v11 offset:4096\n"
        "ds_read_b64_tr_b16 v[102:103], v11 offset:5120\n"
        "s_waitcnt lgkmcnt(4)\n"
        "s_add_u32 m0, s79, 2048\n"
        "s_mov_b32 s65, 3407872\n"
        "buffer_load_dwordx4 v8, s[44:47], s65 offen lds\n"
        "s_mov_b32 s65, 3439616\n"
        "buffer_load_dwordx4 v8, s[44:47], s65 offen offset:1024 lds\n"
        "s_waitcnt vmcnt(2)\n"
        "ds_read_b64_tr_b16 v[120:121], v10 offset:0\n"
        "ds_read_b64_tr_b16 v[122:123], v10 offset:1024\n"
        "ds_read_b64_tr_b16 v[124:125], v11 offset:0\n"
        "ds_read_b64_tr_b16 v[126:127], v11 offset:1024\n"
        "s_waitcnt lgkmcnt(4)\n"
        "s_add_u32 m0, s79, 4096\n"
        "s_mov_b32 s65, 3670016\n"
        "buffer_load_dwordx4 v8, s[44:47], s65 offen lds\n"
        "s_mov_b32 s65, 3701760\n"
        "buffer_load_dwordx4 v8, s[44:47], s65 offen offset:1024 lds\n"
        "s_waitcnt vmcnt(2)\n"
        "ds_read_b64_tr_b16 v[144:145], v10 offset:2048\n"
        "ds_read_b64_tr_b16 v[146:147], v10 offset:3072\n"
        "ds_read_b64_tr_b16 v[148:149], v11 offset:2048\n"
        "ds_read_b64_tr_b16 v[150:151], v11 offset:3072\n"
        "s_waitcnt lgkmcnt(4)\n"
        "s_add_u32 m0, s79, 0\n"
        "s_mov_b32 s65, 3932160\n"
        "buffer_load_dwordx4 v8, s[44:47], s65 offen lds\n"
        "s_mov_b32 s65, 3963904\n"
        "buffer_load_dwordx4 v8, s[44:47], s65 offen offset:1024 lds\n"
        "s_waitcnt vmcnt(2)\n"
        "ds_read_b64_tr_b16 v[168:169], v10 offset:4096\n"
        "ds_read_b64_tr_b16 v[170:171], v10 offset:5120\n"
        "ds_read_b64_tr_b16 v[172:173], v11 offset:4096\n"
        "ds_read_b64_tr_b16 v[174:175], v11 offset:5120\n"
        "s_waitcnt lgkmcnt(4)\n"
        "s_add_u32 m0, s79, 2048\n"
        "s_mov_b32 s65, 4194304\n"
        "buffer_load_dwordx4 v8, s[44:47], s65 offen lds\n"
        "s_mov_b32 s65, 4226048\n"
        "buffer_load_dwordx4 v8, s[44:47], s65 offen offset:1024 lds\n"
        "s_waitcnt vmcnt(2)\n"
        "ds_read_b64_tr_b16 v[192:193], v10 offset:0\n"
        "ds_read_b64_tr_b16 v[194:195], v10 offset:1024\n"
        "ds_read_b64_tr_b16 v[196:197], v11 offset:0\n"
        "ds_read_b64_tr_b16 v[198:199], v11 offset:1024\n"
        "s_waitcnt lgkmcnt(4)\n"
        "s_add_u32 m0, s79, 4096\n"
        "s_mov_b32 s65, 4456448\n"
        "buffer_load_dwordx4 v8, s[44:47], s65 offen lds\n"
        "s_mov_b32 s65, 4488192\n"
        "buffer_load_dwordx4 v8, s[44:47], s65 offen offset:1024 lds\n"
        "s_waitcnt vmcnt(2)\n"
        "ds_read_b64_tr_b16 v[216:217], v10 offset:2048\n"
        "ds_read_b64_tr_b16 v[218:219], v10 offset:3072\n"
        "ds_read_b64_tr_b16 v[220:221], v11 offset:2048\n"
        "ds_read_b64_tr_b16 v[222:223], v11 offset:3072\n"
        "s_waitcnt vmcnt(0)\n"
        "ds_read_b64_tr_b16 v[240:241], v10 offset:4096\n"
        "ds_read_b64_tr_b16 v[242:243], v10 offset:5120\n"
        "ds_read_b64_tr_b16 v[244:245], v11 offset:4096\n"
        "ds_read_b64_tr_b16 v[246:247], v11 offset:5120\n"
        "s_waitcnt lgkmcnt(0)\n"
        "s_getpc_b64 s[44:45]\n"
        ".Ls1_pc_%=:\n"
        "s_add_u32 s44, s44, .Ls1_code_%=-.Ls1_pc_%=\n"
        "s_addc_u32 s45, s45, 0\n"
        "s_and_b32 s45, s45, 0xffff\n"
        "s_and_b32 s44, s44, -4\n"
        "s_mov_b32 s46, .Ls1_end_%=-.Ls1_code_%=\n"
        "s_mov_b32 s47, 0x20000\n"
        "s_lshl_b32 s65, s48, 10\n"
        "buffer_load_dwordx4 v[24:27], v1, s[44:47], s65 offen\n"
        "s_add_u32 s65, s65, 4096\n"
        "buffer_load_dwordx4 v[28:31], v1, s[44:47], s65 offen\n"
        "s_add_u32 s65, s65, 4096\n"
        "buffer_load_dwordx4 v[32:35], v1, s[44:47], s65 offen\n"
        "s_add_u32 s65, s65, 4096\n"
        "buffer_load_dwordx4 v[36:39], v1, s[44:47], s65 offen\n"
        "s_add_u32 s65, s65, 4096\n"
        "buffer_load_dwordx4 v[40:43], v1, s[44:47], s65 offen\n"
        "s_add_u32 s65, s65, 4096\n"
        "v_lshrrev_b32 v20, 4, v1\n"
        "s_mov_b32 s50, s56\n"
        "v_lshlrev_b32 v22, 7, v20\n"
        "s_sub_u32 s66, 0, s50\n"
        "s_mov_b32 s52, 32768\n"
        "s_mov_b32 s64, 0\n"
        ".Ls1_pg0_top_%=:\n"
        "buffer_load_dword v21, v22, s[40:43], s52 offen sc1\n"
        "s_waitcnt vmcnt(0)\n"
        "v_cmp_gt_i32 vcc, 0, v21\n"
        "s_cbranch_vccz .Ls1_pg0_ok_%=\n"
        "s_add_u32 s64, s64, 1\n"
        "s_and_b32 s65, s64, 255\n"
        "s_cbranch_scc1 .Ls1_pg0_go_%=\n"
        "s_mov_b32 s65, 49152\n"
        "s_cmp_lt_u32 s64, 1048576\n"
        "s_cbranch_scc1 .Ls1_pg0_chk_%=\n"
        "v_mov_b32 v21, 1\n"
        "buffer_store_dword v21, off, s[40:43], s65 sc1\n"
        "s_waitcnt vmcnt(0)\n"
        ".Ls1_pg0_chk_%=:\n"
        "buffer_load_dword v21, off, s[40:43], s65 sc1\n"
        "s_waitcnt vmcnt(0)\n"
        "v_readfirstlane_b32 s65, v21\n"
        "s_nop 0\n"
        "s_cmp_lg_u32 s65, 1\n"
        "s_cbranch_scc1 .Ls1_pg0_go_%=\n"
        "s_endpgm\n"
        ".Ls1_pg0_go_%=:\n"
        "s_sleep 2\n"
        "s_branch .Ls1_pg0_top_%=\n"
        ".Ls1_pg0_ok_%=:\n"
        "v_cmp_eq_u32 s[52:53], 1, v21\n"
        "s_nop 1\n"
        "s_mov_b32 s54, 40960\n"
        "s_mov_b32 s64, 0\n"
        ".Ls1_pg1_top_%=:\n"
        "buffer_load_dword v21, v22, s[40:43], s54 offen sc1\n"
        "s_waitcnt vmcnt(0)\n"
        "v_cmp_gt_i32 vcc, 0, v21\n"
        "s_cbranch_vccz .Ls1_pg1_ok_%=\n"
        "s_add_u32 s64, s64, 1\n"
        "s_and_b32 s65, s64, 255\n"
        "s_cbranch_scc1 .Ls1_pg1_go_%=\n"
        "s_mov_b32 s65, 49152\n"
        "s_cmp_lt_u32 s64, 1048576\n"
        "s_cbranch_scc1 .Ls1_pg1_chk_%=\n"
        "v_mov_b32 v21, 1\n"
        "buffer_store_dword v21, off, s[40:43], s65 sc1\n"
        "s_waitcnt vmcnt(0)\n"
        ".Ls1_pg1_chk_%=:\n"
        "buffer_load_dword v21, off, s[40:43], s65 sc1\n"
        "s_waitcnt vmcnt(0)\n"
        "v_readfirstlane_b32 s65, v21\n"
        "s_nop 0\n"
        "s_cmp_lg_u32 s65, 1\n"
        "s_cbranch_scc1 .Ls1_pg1_go_%=\n"
        "s_endpgm\n"
        ".Ls1_pg1_go_%=:\n"
        "s_sleep 2\n"
        "s_branch .Ls1_pg1_top_%=\n"
        ".Ls1_pg1_ok_%=:\n"
        "v_cmp_eq_u32 s[54:55], 1, v21\n"
        "s_nop 1\n"
        "s_mov_b32 vcc_lo, 0x55555555\n"
        "s_mov_b32 vcc_hi, 0x55555555\n"
        "s_xor_b64 vcc, vcc, s[52:53]\n"
        "s_bcnt1_i32_b64 s65, vcc\n"
        "s_sub_u32 s64, s65, 23\n"
        "s_cmp_lt_u32 s64, 19\n"
        "s_cbranch_scc1 .Ls1_rawmap_%=\n"
        "s_cmp_lt_u32 s65, 32\n"
        "s_cselect_b32 s65, 0, -1\n"
        "s_xor_b32 s52, s65, 0x55555555\n"
        "s_xor_b32 s53, s65, 0x55555555\n"
        "s_xor_b32 s54, s65, 0x55555555\n"
        "s_xor_b32 s55, s65, 0xaaaaaaaa\n"
        ".Ls1_rawmap_%=:\n"
        "s_xor_b32 s52, s52, s66\n"
        "s_xor_b32 s53, s53, s66\n"
        "s_xor_b32 s54, s54, s66\n"
        "s_xor_b32 s55, s55, s66\n"
        "s_and_b32 s76, s55, 1\n"
        "s_lshl_b32 s76, s76, 13\n"
        "s_add_u32 s60, s60, s76\n"
        "s_barrier\n"
        "s_mov_b32 s68, -1\n"
        "s_mov_b32 s69, 0\n"
        "s_mov_b32 s78, 0\n"
        "s_mov_b32 s74, -1\n"
        "s_mov_b32 s86, s76\n"
        "s_lshr_b32 s85, s61, 16\n"
        "s_and_b32 s61, s61, 0xffff\n"
        "s_mov_b32 s89, 241172480\n"
        "s_mov_b32 s84, 241172480\n"
        "v_mov_b32 v6, v0\n"
        "s_mov_b32 s80, 0xbfb8aa3b\n"
        "s_lshr_b32 s67, s69, 1\n"
        "s_bitcmp1_b64 s[52:53], s67\n"
        "s_cselect_b32 s65, 8192, 0\n"
        "s_lshl_b32 s77, s67, 21\n"
        "s_add_u32 s77, s77, s65\n"
        "s_add_u32 s71, s77, s58\n"
        "s_add_u32 s81, s77, s62\n"
        "s_add_u32 s78, s78, 18432\n"
        "s_cmp_eq_u32 s78, 55296\n"
        "s_cselect_b32 s78, 0, s78\n"
        "s_add_u32 m0, s78, s61\n"
        "s_lshr_b32 s70, s69, 1\n"
        "s_branch .Ls1_wait1_%=\n"
        ".Ls1_code_%=:\n"
        ".Ls1_i0_%=:\n"
        "v_mfma_f32_16x16x32_bf16 v[24:27], a[0:3], v[8:11], 0\n"
        "s_add_u32 s86, s76, 512\n"
        "buffer_load_dword v4, v2, s[40:43], s86 offen sc1\n"
        "v_mfma_f32_16x16x32_bf16 v[28:31], a[4:7], v[8:11], 0\n"
        "s_add_u32 s71, s77, s58\n"
        "v_cvt_pk_bf16_f32 v48, v48, v49\n"
        "v_cvt_pk_bf16_f32 v49, v50, v51\n"
        "v_mfma_f32_16x16x32_bf16 v[32:35], v[8:11], a[8:11], 0\n"
        "s_add_u32 s71, s71, 32768\n"
        "v_cvt_pk_bf16_f32 v50, v52, v53\n"
        "v_cvt_pk_bf16_f32 v51, v54, v55\n"
        "v_mfma_f32_16x16x32_bf16 v[36:39], v[8:11], a[12:15], 0\n"
        "s_add_u32 s81, s77, s62\n"
        "s_add_u32 s66, s72, 524288\n"
        "v_mfma_f32_16x16x32_bf16 v[40:43], a[16:19], v[8:11], 0\n"
        "s_add_u32 s81, s81, 2048\n"
        "buffer_store_dwordx4 v[48:51], v1, s[40:43], s66 offen offset:1024 sc1\n"
        "v_mfma_f32_16x16x32_bf16 v[44:47], a[20:23], v[8:11], 0\n"
        "s_waitcnt lgkmcnt(1)\n"
        "ds_read_b128 v[8:11], v0 offset:1536\n"
        "v_cvt_pk_bf16_f32 v74, v56, v57\n"
        "v_mfma_f32_16x16x32_bf16 v[24:27], a[24:27], v[12:15], v[24:27]\n"
        "s_add_u32 s78, s78, 18432\n"
        "v_cvt_pk_bf16_f32 v75, v58, v59\n"
        "s_add_u32 s66, s72, 1048576\n"
        "v_mfma_f32_16x16x32_bf16 v[28:31], a[28:31], v[12:15], v[28:31]\n"
        "s_cmp_eq_u32 s78, 55296\n"
        "s_cselect_b32 s78, 0, s78\n"
        "v_mfma_f32_16x16x32_bf16 v[32:35], v[12:15], a[32:35], v[32:35]\n"
        "s_add_u32 m0, s78, s61\n"
        "v_mfma_f32_16x16x32_bf16 v[36:39], v[12:15], a[36:39], v[36:39]\n"
        "s_lshr_b32 s70, s69, 1\n"
        "v_mfma_f32_16x16x32_bf16 v[40:43], a[40:43], v[12:15], v[40:43]\n"
        "v_mfma_f32_16x16x32_bf16 v[44:47], a[44:47], v[12:15], v[44:47]\n"
        "s_waitcnt lgkmcnt(1)\n"
        "ds_read_b128 v[12:15], v0 offset:2048\n"
        "v_mfma_f32_16x16x32_bf16 v[24:27], a[48:51], v[16:19], v[24:27]\n"
        "buffer_store_dwordx4 v[72:75], v1, s[40:43], s66 offen offset:0 sc1\n"
        "v_mfma_f32_16x16x32_bf16 v[28:31], a[52:55], v[16:19], v[28:31]\n"
        "v_cvt_pk_bf16_f32 v78, v60, v61\n"
        "v_cvt_pk_bf16_f32 v79, v62, v63\n"
        "v_mfma_f32_16x16x32_bf16 v[32:35], v[16:19], a[56:59], v[32:35]\n"
        "s_waitcnt vmcnt(3)\n"
        "v_mfma_f32_16x16x32_bf16 v[36:39], v[16:19], a[60:63], v[36:39]\n"
        "buffer_store_dword v5, v3, s[40:43], s84 offen sc1\n"
        "v_mfma_f32_16x16x32_bf16 v[40:43], a[64:67], v[16:19], v[40:43]\n"
        "v_mfma_f32_16x16x32_bf16 v[44:47], a[68:71], v[16:19], v[44:47]\n"
        "s_waitcnt lgkmcnt(1)\n"
        "ds_read_b128 v[16:19], v0 offset:2560\n"
        "v_mfma_f32_16x16x32_bf16 v[24:27], a[72:75], v[8:11], v[24:27]\n"
        "v_mfma_f32_16x16x32_bf16 v[28:31], a[76:79], v[8:11], v[28:31]\n"
        "buffer_store_dwordx4 v[76:79], v1, s[40:43], s66 offen offset:1024 sc1\n"
        "v_mfma_f32_16x16x32_bf16 v[32:35], v[8:11], a[80:83], v[32:35]\n"
        "v_cvt_pk_bf16_f32 v64, v64, v65\n"
        "v_cvt_pk_bf16_f32 v65, v66, v67\n"
        "v_mfma_f32_16x16x32_bf16 v[36:39], v[8:11], a[84:87], v[36:39]\n"
        "v_cvt_pk_bf16_f32 v66, v68, v69\n"
        "v_cvt_pk_bf16_f32 v67, v70, v71\n"
        "v_mfma_f32_16x16x32_bf16 v[40:43], a[88:91], v[8:11], v[40:43]\n"
        "v_mfma_f32_16x16x32_bf16 v[44:47], a[92:95], v[8:11], v[44:47]\n"
        "s_waitcnt lgkmcnt(1)\n"
        "ds_read_b128 v[8:11], v0 offset:3072\n"
        "v_mfma_f32_16x16x32_bf16 v[24:27], a[96:99], v[12:15], v[24:27]\n"
        "v_mfma_f32_16x16x32_bf16 v[28:31], a[100:103], v[12:15], v[28:31]\n"
        "v_mfma_f32_16x16x32_bf16 v[32:35], v[12:15], a[104:107], v[32:35]\n"
        "buffer_store_dwordx4 v[64:67], v1, s[40:43], s72 offen offset:1024 sc1\n"
        "v_mfma_f32_16x16x32_bf16 v[36:39], v[12:15], a[108:111], v[36:39]\n"
        "v_mov_b32 v5, s74\n"
        "v_mfma_f32_16x16x32_bf16 v[40:43], a[112:115], v[12:15], v[40:43]\n"
        "s_waitcnt vmcnt(5)\n"
        "v_cmp_gt_i32 vcc, s70, v4\n"
        "v_mfma_f32_16x16x32_bf16 v[44:47], a[116:119], v[12:15], v[44:47]\n"
        "s_waitcnt lgkmcnt(1)\n"
        "ds_read_b128 v[12:15], v0 offset:3584\n"
        "v_mfma_f32_16x16x32_bf16 v[24:27], a[120:123], v[16:19], v[24:27]\n"
        "s_and_b64 vcc, vcc, 1\n"
        "v_mfma_f32_16x16x32_bf16 v[28:31], a[124:127], v[16:19], v[28:31]\n"
        "s_cbranch_vccnz .Ls1_i00_%=\n"
        "v_mfma_f32_16x16x32_bf16 v[32:35], v[16:19], a[128:131], v[32:35]\n"
        "buffer_load_dwordx4 v1, s[40:43], s71 offen offset:0 sc1 lds\n"
        "v_mfma_f32_16x16x32_bf16 v[36:39], v[16:19], a[132:135], v[36:39]\n"
        "v_mfma_f32_16x16x32_bf16 v[40:43], a[136:139], v[16:19], v[40:43]\n"
        "v_mfma_f32_16x16x32_bf16 v[44:47], a[140:143], v[16:19], v[44:47]\n"
        "s_waitcnt lgkmcnt(1)\n"
        "ds_read_b128 v[16:19], v0 offset:4096\n"
        "v_mfma_f32_16x16x32_bf16 v[24:27], a[144:147], v[8:11], v[24:27]\n"
        "buffer_load_dwordx4 v1, s[40:43], s71 offen offset:1024 sc1 lds\n"
        "v_mfma_f32_16x16x32_bf16 v[28:31], a[148:151], v[8:11], v[28:31]\n"
        "v_mfma_f32_16x16x32_bf16 v[32:35], v[8:11], a[152:155], v[32:35]\n"
        "v_mfma_f32_16x16x32_bf16 v[36:39], v[8:11], a[156:159], v[36:39]\n"
        "v_mfma_f32_16x16x32_bf16 v[40:43], a[160:163], v[8:11], v[40:43]\n"
        "buffer_load_dwordx4 v1, s[40:43], s71 offen offset:2048 sc1 lds\n"
        "v_mfma_f32_16x16x32_bf16 v[44:47], a[164:167], v[8:11], v[44:47]\n"
        "s_waitcnt lgkmcnt(1)\n"
        "ds_read_b128 v[8:11], v0 offset:4608\n"
        "v_mfma_f32_16x16x32_bf16 v[24:27], a[168:171], v[12:15], v[24:27]\n"
        "v_mfma_f32_16x16x32_bf16 v[28:31], a[172:175], v[12:15], v[28:31]\n"
        "v_mfma_f32_16x16x32_bf16 v[32:35], v[12:15], a[176:179], v[32:35]\n"
        "buffer_load_dwordx4 v1, s[40:43], s71 offen offset:3072 sc1 lds\n"
        "v_mfma_f32_16x16x32_bf16 v[36:39], v[12:15], a[180:183], v[36:39]\n"
        "s_add_u32 m0, s78, s85\n"
        "s_add_u32 s69, s69, 1\n"
        "v_mfma_f32_16x16x32_bf16 v[40:43], a[184:187], v[12:15], v[40:43]\n"
        "v_mfma_f32_16x16x32_bf16 v[44:47], a[188:191], v[12:15], v[44:47]\n"
        "s_waitcnt lgkmcnt(1)\n"
        "ds_read_b128 v[12:15], v0 offset:5120\n"
        "v_mfma_f32_16x16x32_bf16 v[24:27], a[192:195], v[16:19], v[24:27]\n"
        "buffer_load_dwordx4 v1, s[40:43], s81 offen offset:0 sc1 lds\n"
        "v_mfma_f32_16x16x32_bf16 v[28:31], a[196:199], v[16:19], v[28:31]\n"
        "v_mfma_f32_16x16x32_bf16 v[32:35], v[16:19], a[200:203], v[32:35]\n"
        "s_getreg_b32 s88, hwreg(HW_REG_IB_STS, 0, 4)\n"
        "v_mfma_f32_16x16x32_bf16 v[36:39], v[16:19], a[204:207], v[36:39]\n"
        "s_cmp_le_u32 s88, 5\n"
        "s_cselect_b32 s88, s73, s89\n"
        "s_cselect_b32 s73, s89, s73\n"
        "v_mfma_f32_16x16x32_bf16 v[40:43], a[208:211], v[16:19], v[40:43]\n"
        "buffer_store_dword v5, v3, s[40:43], s88 offen sc1\n"
        "v_mfma_f32_16x16x32_bf16 v[44:47], a[212:215], v[16:19], v[44:47]\n"
        "s_waitcnt lgkmcnt(1)\n"
        "ds_read_b128 v[16:19], v0 offset:5632\n"
        "v_mfma_f32_16x16x32_bf16 v[24:27], a[216:219], v[8:11], v[24:27]\n"
        "v_mfma_f32_16x16x32_bf16 v[28:31], a[220:223], v[8:11], v[28:31]\n"
        "v_mfma_f32_16x16x32_bf16 v[32:35], v[8:11], a[224:227], v[32:35]\n"
        "v_mfma_f32_16x16x32_bf16 v[36:39], v[8:11], a[228:231], v[36:39]\n"
        "v_mfma_f32_16x16x32_bf16 v[40:43], a[232:235], v[8:11], v[40:43]\n"
        "s_getreg_b32 s88, hwreg(HW_REG_IB_STS, 0, 4)\n"
        "v_mfma_f32_16x16x32_bf16 v[44:47], a[236:239], v[8:11], v[44:47]\n"
        "s_waitcnt lgkmcnt(1)\n"
        "ds_read_b128 v[8:11], v0 offset:6144\n"
        "v_mfma_f32_16x16x32_bf16 v[24:27], a[240:243], v[12:15], v[24:27]\n"
        "s_cmp_le_u32 s88, 6\n"
        "s_cselect_b32 s88, s73, s89\n"
        "s_cselect_b32 s73, s89, s73\n"
        "v_mfma_f32_16x16x32_bf16 v[28:31], a[244:247], v[12:15], v[28:31]\n"
        "buffer_store_dword v5, v3, s[40:43], s88 offen sc1\n"
        "v_mfma_f32_16x16x32_bf16 v[32:35], v[12:15], a[248:251], v[32:35]\n"
        "v_mfma_f32_16x16x32_bf16 v[36:39], v[12:15], a[252:255], v[36:39]\n"
        "v_mfma_f32_16x16x32_bf16 v[40:43], v[80:83], v[12:15], v[40:43]\n"
        "v_mfma_f32_16x16x32_bf16 v[44:47], v[84:87], v[12:15], v[44:47]\n"
        "s_waitcnt lgkmcnt(1)\n"
        "ds_read_b128 v[12:15], v0 offset:6656\n"
        "v_mfma_f32_16x16x32_bf16 v[24:27], v[88:91], v[16:19], v[24:27]\n"
        "s_getreg_b32 s88, hwreg(HW_REG_IB_STS, 0, 4)\n"
        "v_mfma_f32_16x16x32_bf16 v[28:31], v[92:95], v[16:19], v[28:31]\n"
        "s_cmp_le_u32 s88, 7\n"
        "s_cselect_b32 s88, s73, s89\n"
        "s_cselect_b32 s73, s89, s73\n"
        "v_mfma_f32_16x16x32_bf16 v[32:35], v[16:19], v[96:99], v[32:35]\n"
        "buffer_store_dword v5, v3, s[40:43], s88 offen sc1\n"
        "v_mfma_f32_16x16x32_bf16 v[36:39], v[16:19], v[100:103], v[36:39]\n"
        "v_mfma_f32_16x16x32_bf16 v[40:43], v[104:107], v[16:19], v[40:43]\n"
        "v_mfma_f32_16x16x32_bf16 v[44:47], v[108:111], v[16:19], v[44:47]\n"
        "s_waitcnt lgkmcnt(1)\n"
        "ds_read_b128 v[16:19], v0 offset:7168\n"
        "v_mfma_f32_16x16x32_bf16 v[24:27], v[112:115], v[8:11], v[24:27]\n"
        "v_mfma_f32_16x16x32_bf16 v[28:31], v[116:119], v[8:11], v[28:31]\n"
        "v_mfma_f32_16x16x32_bf16 v[32:35], v[8:11], v[120:123], v[32:35]\n"
        "v_mfma_f32_16x16x32_bf16 v[36:39], v[8:11], v[124:127], v[36:39]\n"
        "v_mfma_f32_16x16x32_bf16 v[40:43], v[128:131], v[8:11], v[40:43]\n"
        "v_mfma_f32_16x16x32_bf16 v[44:47], v[132:135], v[8:11], v[44:47]\n"
        "s_waitcnt lgkmcnt(1)\n"
        "ds_read_b128 v[8:11], v0 offset:7680\n"
        "v_mfma_f32_16x16x32_bf16 v[24:27], v[136:139], v[12:15], v[24:27]\n"
        "v_mfma_f32_16x16x32_bf16 v[28:31], v[140:143], v[12:15], v[28:31]\n"
        "v_mfma_f32_16x16x32_bf16 v[32:35], v[12:15], v[144:147], v[32:35]\n"
        "s_waitcnt vmcnt(8)\n"
        "buffer_store_dword v5, v3, s[40:43], s73 offen sc1\n"
        "v_mfma_f32_16x16x32_bf16 v[36:39], v[12:15], v[148:151], v[36:39]\n"
        "s_mov_b32 s84, s89\n"
        "s_mov_b32 s73, s60\n"
        "s_add_u32 s74, s74, 1\n"
        "v_mfma_f32_16x16x32_bf16 v[40:43], v[152:155], v[12:15], v[40:43]\n"
        "s_add_u32 s68, s68, 1\n"
        "s_bitcmp1_b64 s[52:53], s68\n"
        "s_cselect_b32 s66, 8192, 0\n"
        "v_mfma_f32_16x16x32_bf16 v[44:47], v[156:159], v[12:15], v[44:47]\n"
        "s_waitcnt lgkmcnt(1)\n"
        "ds_read_b128 v[12:15], v0 offset:8192\n"
        "s_lshl_b32 s75, s68, 21\n"
        "v_mfma_f32_16x16x32_bf16 v[24:27], v[160:163], v[16:19], v[24:27]\n"
        "s_add_u32 s75, s75, s66\n"
        "s_add_u32 s72, s75, s59\n"
        "v_mfma_f32_16x16x32_bf16 v[28:31], v[164:167], v[16:19], v[28:31]\n"
        "v_mfma_f32_16x16x32_bf16 v[32:35], v[16:19], v[168:171], v[32:35]\n"
        "v_mfma_f32_16x16x32_bf16 v[36:39], v[16:19], v[172:175], v[36:39]\n"
        "v_mfma_f32_16x16x32_bf16 v[40:43], v[176:179], v[16:19], v[40:43]\n"
        "v_mfma_f32_16x16x32_bf16 v[44:47], v[180:183], v[16:19], v[44:47]\n"
        "s_waitcnt lgkmcnt(1)\n"
        "ds_read_b128 v[16:19], v0 offset:8704\n"
        "v_mfma_f32_16x16x32_bf16 v[24:27], v[184:187], v[8:11], v[24:27]\n"
        "v_mfma_f32_16x16x32_bf16 v[28:31], v[188:191], v[8:11], v[28:31]\n"
        "v_mfma_f32_16x16x32_bf16 v[32:35], v[8:11], v[192:195], v[32:35]\n"
        "v_mfma_f32_16x16x32_bf16 v[36:39], v[8:11], v[196:199], v[36:39]\n"
        "v_mfma_f32_16x16x32_bf16 v[40:43], v[200:203], v[8:11], v[40:43]\n"
        "s_waitcnt vmcnt(4)\n"
        "s_barrier\n"
        "v_add_u32 v0, s78, v6\n"
        "v_mfma_f32_16x16x32_bf16 v[44:47], v[204:207], v[8:11], v[44:47]\n"
        "s_waitcnt lgkmcnt(1)\n"
        "ds_read_b128 v[8:11], v0 offset:0\n"
        "v_mfma_f32_16x16x32_bf16 v[24:27], v[208:211], v[12:15], v[24:27]\n"
        "v_mfma_f32_16x16x32_bf16 v[28:31], v[212:215], v[12:15], v[28:31]\n"
        "v_mfma_f32_16x16x32_bf16 v[32:35], v[12:15], v[216:219], v[32:35]\n"
        "v_mfma_f32_16x16x32_bf16 v[36:39], v[12:15], v[220:223], v[36:39]\n"
        "v_mfma_f32_16x16x32_bf16 v[40:43], v[224:227], v[12:15], v[40:43]\n"
        "v_mfma_f32_16x16x32_bf16 v[44:47], v[228:231], v[12:15], v[44:47]\n"
        "s_waitcnt lgkmcnt(1)\n"
        "ds_read_b128 v[12:15], v0 offset:512\n"
        "v_mfma_f32_16x16x32_bf16 v[24:27], v[232:235], v[16:19], v[24:27]\n"
        "v_mfma_f32_16x16x32_bf16 v[28:31], v[236:239], v[16:19], v[28:31]\n"
        "v_mfma_f32_16x16x32_bf16 v[32:35], v[16:19], v[240:243], v[32:35]\n"
        "v_mfma_f32_16x16x32_bf16 v[36:39], v[16:19], v[244:247], v[36:39]\n"
        "v_mfma_f32_16x16x32_bf16 v[40:43], v[248:251], v[16:19], v[40:43]\n"
        "v_mfma_f32_16x16x32_bf16 v[44:47], v[252:255], v[16:19], v[44:47]\n"
        "s_waitcnt lgkmcnt(1)\n"
        "ds_read_b128 v[16:19], v0 offset:1024\n"
        "s_branch .Ls1_i1_%=\n"
        ".Ls1_i00_%=:\n"
        "v_mfma_f32_16x16x32_bf16 v[32:35], v[16:19], a[128:131], v[32:35]\n"
        "buffer_load_dword v4, v2, s[40:43], s86 offen sc1\n"
        "v_mfma_f32_16x16x32_bf16 v[36:39], v[16:19], a[132:135], v[36:39]\n"
        "v_mfma_f32_16x16x32_bf16 v[40:43], a[136:139], v[16:19], v[40:43]\n"
        "v_mfma_f32_16x16x32_bf16 v[44:47], a[140:143], v[16:19], v[44:47]\n"
        "s_waitcnt lgkmcnt(1)\n"
        "ds_read_b128 v[16:19], v0 offset:4096\n"
        "v_mfma_f32_16x16x32_bf16 v[24:27], a[144:147], v[8:11], v[24:27]\n"
        "v_mfma_f32_16x16x32_bf16 v[28:31], a[148:151], v[8:11], v[28:31]\n"
        "v_mfma_f32_16x16x32_bf16 v[32:35], v[8:11], a[152:155], v[32:35]\n"
        "v_mfma_f32_16x16x32_bf16 v[36:39], v[8:11], a[156:159], v[36:39]\n"
        "v_mfma_f32_16x16x32_bf16 v[40:43], a[160:163], v[8:11], v[40:43]\n"
        "v_mfma_f32_16x16x32_bf16 v[44:47], a[164:167], v[8:11], v[44:47]\n"
        "s_waitcnt lgkmcnt(1)\n"
        "ds_read_b128 v[8:11], v0 offset:4608\n"
        "v_mfma_f32_16x16x32_bf16 v[24:27], a[168:171], v[12:15], v[24:27]\n"
        "v_mfma_f32_16x16x32_bf16 v[28:31], a[172:175], v[12:15], v[28:31]\n"
        "v_mfma_f32_16x16x32_bf16 v[32:35], v[12:15], a[176:179], v[32:35]\n"
        "v_mfma_f32_16x16x32_bf16 v[36:39], v[12:15], a[180:183], v[36:39]\n"
        "v_mfma_f32_16x16x32_bf16 v[40:43], a[184:187], v[12:15], v[40:43]\n"
        "v_mfma_f32_16x16x32_bf16 v[44:47], a[188:191], v[12:15], v[44:47]\n"
        "s_waitcnt lgkmcnt(1)\n"
        "ds_read_b128 v[12:15], v0 offset:5120\n"
        "v_mfma_f32_16x16x32_bf16 v[24:27], a[192:195], v[16:19], v[24:27]\n"
        "v_mfma_f32_16x16x32_bf16 v[28:31], a[196:199], v[16:19], v[28:31]\n"
        "v_mfma_f32_16x16x32_bf16 v[32:35], v[16:19], a[200:203], v[32:35]\n"
        "s_getreg_b32 s88, hwreg(HW_REG_IB_STS, 0, 4)\n"
        "v_mfma_f32_16x16x32_bf16 v[36:39], v[16:19], a[204:207], v[36:39]\n"
        "s_cmp_le_u32 s88, 1\n"
        "s_cselect_b32 s88, s73, s89\n"
        "s_cselect_b32 s73, s89, s73\n"
        "v_mfma_f32_16x16x32_bf16 v[40:43], a[208:211], v[16:19], v[40:43]\n"
        "buffer_store_dword v5, v3, s[40:43], s88 offen sc1\n"
        "v_mfma_f32_16x16x32_bf16 v[44:47], a[212:215], v[16:19], v[44:47]\n"
        "s_waitcnt lgkmcnt(1)\n"
        "ds_read_b128 v[16:19], v0 offset:5632\n"
        "v_mfma_f32_16x16x32_bf16 v[24:27], a[216:219], v[8:11], v[24:27]\n"
        "v_mfma_f32_16x16x32_bf16 v[28:31], a[220:223], v[8:11], v[28:31]\n"
        "v_mfma_f32_16x16x32_bf16 v[32:35], v[8:11], a[224:227], v[32:35]\n"
        "v_mfma_f32_16x16x32_bf16 v[36:39], v[8:11], a[228:231], v[36:39]\n"
        "v_mfma_f32_16x16x32_bf16 v[40:43], a[232:235], v[8:11], v[40:43]\n"
        "s_getreg_b32 s88, hwreg(HW_REG_IB_STS, 0, 4)\n"
        "v_mfma_f32_16x16x32_bf16 v[44:47], a[236:239], v[8:11], v[44:47]\n"
        "s_waitcnt lgkmcnt(1)\n"
        "ds_read_b128 v[8:11], v0 offset:6144\n"
        "s_waitcnt vmcnt(1)\n"
        "v_cmp_gt_i32 vcc, s70, v4\n"
        "v_mfma_f32_16x16x32_bf16 v[24:27], a[240:243], v[12:15], v[24:27]\n"
        "s_cmp_le_u32 s88, 2\n"
        "s_cselect_b32 s88, s73, s89\n"
        "s_cselect_b32 s73, s89, s73\n"
        "v_mfma_f32_16x16x32_bf16 v[28:31], a[244:247], v[12:15], v[28:31]\n"
        "s_and_b64 vcc, vcc, 1\n"
        "v_mfma_f32_16x16x32_bf16 v[32:35], v[12:15], a[248:251], v[32:35]\n"
        "s_cbranch_vccnz .Ls1_i01_%=\n"
        "v_mfma_f32_16x16x32_bf16 v[36:39], v[12:15], a[252:255], v[36:39]\n"
        "buffer_load_dwordx4 v1, s[40:43], s71 offen offset:0 sc1 lds\n"
        "v_mfma_f32_16x16x32_bf16 v[40:43], v[80:83], v[12:15], v[40:43]\n"
        "buffer_store_dword v5, v3, s[40:43], s88 offen sc1\n"
        "v_mfma_f32_16x16x32_bf16 v[44:47], v[84:87], v[12:15], v[44:47]\n"
        "s_waitcnt lgkmcnt(1)\n"
        "ds_read_b128 v[12:15], v0 offset:6656\n"
        "v_mfma_f32_16x16x32_bf16 v[24:27], v[88:91], v[16:19], v[24:27]\n"
        "s_getreg_b32 s88, hwreg(HW_REG_IB_STS, 0, 4)\n"
        "buffer_load_dwordx4 v1, s[40:43], s71 offen offset:1024 sc1 lds\n"
        "v_mfma_f32_16x16x32_bf16 v[28:31], v[92:95], v[16:19], v[28:31]\n"
        "s_cmp_le_u32 s88, 15\n"
        "s_cselect_b32 s88, s73, s89\n"
        "s_cselect_b32 s73, s89, s73\n"
        "v_mfma_f32_16x16x32_bf16 v[32:35], v[16:19], v[96:99], v[32:35]\n"
        "buffer_store_dword v5, v3, s[40:43], s88 offen sc1\n"
        "v_mfma_f32_16x16x32_bf16 v[36:39], v[16:19], v[100:103], v[36:39]\n"
        "buffer_load_dwordx4 v1, s[40:43], s71 offen offset:2048 sc1 lds\n"
        "v_mfma_f32_16x16x32_bf16 v[40:43], v[104:107], v[16:19], v[40:43]\n"
        "v_mfma_f32_16x16x32_bf16 v[44:47], v[108:111], v[16:19], v[44:47]\n"
        "s_waitcnt lgkmcnt(1)\n"
        "ds_read_b128 v[16:19], v0 offset:7168\n"
        "v_mfma_f32_16x16x32_bf16 v[24:27], v[112:115], v[8:11], v[24:27]\n"
        "buffer_load_dwordx4 v1, s[40:43], s71 offen offset:3072 sc1 lds\n"
        "v_mfma_f32_16x16x32_bf16 v[28:31], v[116:119], v[8:11], v[28:31]\n"
        "s_add_u32 m0, s78, s85\n"
        "s_add_u32 s69, s69, 1\n"
        "v_mfma_f32_16x16x32_bf16 v[32:35], v[8:11], v[120:123], v[32:35]\n"
        "v_mfma_f32_16x16x32_bf16 v[36:39], v[8:11], v[124:127], v[36:39]\n"
        "buffer_load_dwordx4 v1, s[40:43], s81 offen offset:0 sc1 lds\n"
        "v_mfma_f32_16x16x32_bf16 v[40:43], v[128:131], v[8:11], v[40:43]\n"
        "v_mfma_f32_16x16x32_bf16 v[44:47], v[132:135], v[8:11], v[44:47]\n"
        "s_waitcnt lgkmcnt(1)\n"
        "ds_read_b128 v[8:11], v0 offset:7680\n"
        "v_mfma_f32_16x16x32_bf16 v[24:27], v[136:139], v[12:15], v[24:27]\n"
        "v_mfma_f32_16x16x32_bf16 v[28:31], v[140:143], v[12:15], v[28:31]\n"
        "v_mfma_f32_16x16x32_bf16 v[32:35], v[12:15], v[144:147], v[32:35]\n"
        "buffer_store_dword v5, v3, s[40:43], s73 offen sc1\n"
        "v_mfma_f32_16x16x32_bf16 v[36:39], v[12:15], v[148:151], v[36:39]\n"
        "s_mov_b32 s84, s89\n"
        "s_mov_b32 s73, s60\n"
        "s_add_u32 s74, s74, 1\n"
        "v_mfma_f32_16x16x32_bf16 v[40:43], v[152:155], v[12:15], v[40:43]\n"
        "s_add_u32 s68, s68, 1\n"
        "s_bitcmp1_b64 s[52:53], s68\n"
        "s_cselect_b32 s66, 8192, 0\n"
        "v_mfma_f32_16x16x32_bf16 v[44:47], v[156:159], v[12:15], v[44:47]\n"
        "s_waitcnt lgkmcnt(1)\n"
        "ds_read_b128 v[12:15], v0 offset:8192\n"
        "s_lshl_b32 s75, s68, 21\n"
        "v_mfma_f32_16x16x32_bf16 v[24:27], v[160:163], v[16:19], v[24:27]\n"
        "s_add_u32 s75, s75, s66\n"
        "s_add_u32 s72, s75, s59\n"
        "v_mfma_f32_16x16x32_bf16 v[28:31], v[164:167], v[16:19], v[28:31]\n"
        "v_mfma_f32_16x16x32_bf16 v[32:35], v[16:19], v[168:171], v[32:35]\n"
        "v_mfma_f32_16x16x32_bf16 v[36:39], v[16:19], v[172:175], v[36:39]\n"
        "v_mfma_f32_16x16x32_bf16 v[40:43], v[176:179], v[16:19], v[40:43]\n"
        "v_mfma_f32_16x16x32_bf16 v[44:47], v[180:183], v[16:19], v[44:47]\n"
        "s_waitcnt lgkmcnt(1)\n"
        "ds_read_b128 v[16:19], v0 offset:8704\n"
        "v_mfma_f32_16x16x32_bf16 v[24:27], v[184:187], v[8:11], v[24:27]\n"
        "v_mfma_f32_16x16x32_bf16 v[28:31], v[188:191], v[8:11], v[28:31]\n"
        "v_mfma_f32_16x16x32_bf16 v[32:35], v[8:11], v[192:195], v[32:35]\n"
        "v_mfma_f32_16x16x32_bf16 v[36:39], v[8:11], v[196:199], v[36:39]\n"
        "v_mfma_f32_16x16x32_bf16 v[40:43], v[200:203], v[8:11], v[40:43]\n"
        "s_waitcnt vmcnt(1)\n"
        "s_barrier\n"
        "v_add_u32 v0, s78, v6\n"
        "v_mfma_f32_16x16x32_bf16 v[44:47], v[204:207], v[8:11], v[44:47]\n"
        "s_waitcnt lgkmcnt(1)\n"
        "ds_read_b128 v[8:11], v0 offset:0\n"
        "v_mfma_f32_16x16x32_bf16 v[24:27], v[208:211], v[12:15], v[24:27]\n"
        "v_mfma_f32_16x16x32_bf16 v[28:31], v[212:215], v[12:15], v[28:31]\n"
        "v_mfma_f32_16x16x32_bf16 v[32:35], v[12:15], v[216:219], v[32:35]\n"
        "v_mfma_f32_16x16x32_bf16 v[36:39], v[12:15], v[220:223], v[36:39]\n"
        "v_mfma_f32_16x16x32_bf16 v[40:43], v[224:227], v[12:15], v[40:43]\n"
        "v_mfma_f32_16x16x32_bf16 v[44:47], v[228:231], v[12:15], v[44:47]\n"
        "s_waitcnt lgkmcnt(1)\n"
        "ds_read_b128 v[12:15], v0 offset:512\n"
        "v_mfma_f32_16x16x32_bf16 v[24:27], v[232:235], v[16:19], v[24:27]\n"
        "v_mfma_f32_16x16x32_bf16 v[28:31], v[236:239], v[16:19], v[28:31]\n"
        "v_mfma_f32_16x16x32_bf16 v[32:35], v[16:19], v[240:243], v[32:35]\n"
        "v_mfma_f32_16x16x32_bf16 v[36:39], v[16:19], v[244:247], v[36:39]\n"
        "v_mfma_f32_16x16x32_bf16 v[40:43], v[248:251], v[16:19], v[40:43]\n"
        "v_mfma_f32_16x16x32_bf16 v[44:47], v[252:255], v[16:19], v[44:47]\n"
        "s_waitcnt lgkmcnt(1)\n"
        "ds_read_b128 v[16:19], v0 offset:1024\n"
        "s_branch .Ls1_i1_%=\n"
        ".Ls1_i01_%=:\n"
        "v_mfma_f32_16x16x32_bf16 v[36:39], v[12:15], a[252:255], v[36:39]\n"
        "buffer_load_dword v4, v2, s[40:43], s86 offen sc1\n"
        "v_mfma_f32_16x16x32_bf16 v[40:43], v[80:83], v[12:15], v[40:43]\n"
        "buffer_store_dword v5, v3, s[40:43], s88 offen sc1\n"
        "v_mfma_f32_16x16x32_bf16 v[44:47], v[84:87], v[12:15], v[44:47]\n"
        "s_waitcnt lgkmcnt(1)\n"
        "ds_read_b128 v[12:15], v0 offset:6656\n"
        "v_mfma_f32_16x16x32_bf16 v[24:27], v[88:91], v[16:19], v[24:27]\n"
        "s_getreg_b32 s88, hwreg(HW_REG_IB_STS, 0, 4)\n"
        "v_mfma_f32_16x16x32_bf16 v[28:31], v[92:95], v[16:19], v[28:31]\n"
        "s_cmp_le_u32 s88, 15\n"
        "s_cselect_b32 s88, s73, s89\n"
        "s_cselect_b32 s73, s89, s73\n"
        "v_mfma_f32_16x16x32_bf16 v[32:35], v[16:19], v[96:99], v[32:35]\n"
        "buffer_store_dword v5, v3, s[40:43], s88 offen sc1\n"
        "v_mfma_f32_16x16x32_bf16 v[36:39], v[16:19], v[100:103], v[36:39]\n"
        "v_mfma_f32_16x16x32_bf16 v[40:43], v[104:107], v[16:19], v[40:43]\n"
        "v_mfma_f32_16x16x32_bf16 v[44:47], v[108:111], v[16:19], v[44:47]\n"
        "s_waitcnt lgkmcnt(1)\n"
        "ds_read_b128 v[16:19], v0 offset:7168\n"
        "v_mfma_f32_16x16x32_bf16 v[24:27], v[112:115], v[8:11], v[24:27]\n"
        "v_mfma_f32_16x16x32_bf16 v[28:31], v[116:119], v[8:11], v[28:31]\n"
        "v_mfma_f32_16x16x32_bf16 v[32:35], v[8:11], v[120:123], v[32:35]\n"
        "v_mfma_f32_16x16x32_bf16 v[36:39], v[8:11], v[124:127], v[36:39]\n"
        "v_mfma_f32_16x16x32_bf16 v[40:43], v[128:131], v[8:11], v[40:43]\n"
        "v_mfma_f32_16x16x32_bf16 v[44:47], v[132:135], v[8:11], v[44:47]\n"
        "s_waitcnt lgkmcnt(1)\n"
        "ds_read_b128 v[8:11], v0 offset:7680\n"
        "v_mfma_f32_16x16x32_bf16 v[24:27], v[136:139], v[12:15], v[24:27]\n"
        "v_mfma_f32_16x16x32_bf16 v[28:31], v[140:143], v[12:15], v[28:31]\n"
        "v_mfma_f32_16x16x32_bf16 v[32:35], v[12:15], v[144:147], v[32:35]\n"
        "buffer_store_dword v5, v3, s[40:43], s73 offen sc1\n"
        "v_mfma_f32_16x16x32_bf16 v[36:39], v[12:15], v[148:151], v[36:39]\n"
        "s_mov_b32 s84, s89\n"
        "s_mov_b32 s73, s60\n"
        "s_add_u32 s74, s74, 1\n"
        "v_mfma_f32_16x16x32_bf16 v[40:43], v[152:155], v[12:15], v[40:43]\n"
        "s_add_u32 s68, s68, 1\n"
        "s_bitcmp1_b64 s[52:53], s68\n"
        "s_cselect_b32 s66, 8192, 0\n"
        "v_mfma_f32_16x16x32_bf16 v[44:47], v[156:159], v[12:15], v[44:47]\n"
        "s_waitcnt lgkmcnt(1)\n"
        "ds_read_b128 v[12:15], v0 offset:8192\n"
        "s_lshl_b32 s75, s68, 21\n"
        "v_mfma_f32_16x16x32_bf16 v[24:27], v[160:163], v[16:19], v[24:27]\n"
        "s_add_u32 s75, s75, s66\n"
        "s_add_u32 s72, s75, s59\n"
        "v_mfma_f32_16x16x32_bf16 v[28:31], v[164:167], v[16:19], v[28:31]\n"
        "v_mfma_f32_16x16x32_bf16 v[32:35], v[16:19], v[168:171], v[32:35]\n"
        "v_mfma_f32_16x16x32_bf16 v[36:39], v[16:19], v[172:175], v[36:39]\n"
        "v_mfma_f32_16x16x32_bf16 v[40:43], v[176:179], v[16:19], v[40:43]\n"
        "v_mfma_f32_16x16x32_bf16 v[44:47], v[180:183], v[16:19], v[44:47]\n"
        "s_waitcnt lgkmcnt(1)\n"
        "ds_read_b128 v[16:19], v0 offset:8704\n"
        "v_mfma_f32_16x16x32_bf16 v[24:27], v[184:187], v[8:11], v[24:27]\n"
        "s_waitcnt vmcnt(3)\n"
        "v_cmp_gt_i32 vcc, s70, v4\n"
        "v_mfma_f32_16x16x32_bf16 v[28:31], v[188:191], v[8:11], v[28:31]\n"
        "v_mfma_f32_16x16x32_bf16 v[32:35], v[8:11], v[192:195], v[32:35]\n"
        "s_and_b64 vcc, vcc, 1\n"
        "v_mfma_f32_16x16x32_bf16 v[36:39], v[8:11], v[196:199], v[36:39]\n"
        "s_cbranch_vccnz .Ls1_i02_%=\n"
        "v_mfma_f32_16x16x32_bf16 v[40:43], v[200:203], v[8:11], v[40:43]\n"
        "buffer_load_dwordx4 v1, s[40:43], s71 offen offset:0 sc1 lds\n"
        "v_mfma_f32_16x16x32_bf16 v[44:47], v[204:207], v[8:11], v[44:47]\n"
        "s_waitcnt lgkmcnt(1)\n"
        "v_mfma_f32_16x16x32_bf16 v[24:27], v[208:211], v[12:15], v[24:27]\n"
        "buffer_load_dwordx4 v1, s[40:43], s71 offen offset:1024 sc1 lds\n"
        "v_mfma_f32_16x16x32_bf16 v[28:31], v[212:215], v[12:15], v[28:31]\n"
        "v_mfma_f32_16x16x32_bf16 v[32:35], v[12:15], v[216:219], v[32:35]\n"
        "buffer_load_dwordx4 v1, s[40:43], s71 offen offset:2048 sc1 lds\n"
        "v_mfma_f32_16x16x32_bf16 v[36:39], v[12:15], v[220:223], v[36:39]\n"
        "v_mfma_f32_16x16x32_bf16 v[40:43], v[224:227], v[12:15], v[40:43]\n"
        "buffer_load_dwordx4 v1, s[40:43], s71 offen offset:3072 sc1 lds\n"
        "v_mfma_f32_16x16x32_bf16 v[44:47], v[228:231], v[12:15], v[44:47]\n"
        "s_waitcnt lgkmcnt(0)\n"
        "s_add_u32 m0, s78, s85\n"
        "s_add_u32 s69, s69, 1\n"
        "v_mfma_f32_16x16x32_bf16 v[24:27], v[232:235], v[16:19], v[24:27]\n"
        "buffer_load_dwordx4 v1, s[40:43], s81 offen offset:0 sc1 lds\n"
        "v_mfma_f32_16x16x32_bf16 v[28:31], v[236:239], v[16:19], v[28:31]\n"
        "v_mfma_f32_16x16x32_bf16 v[32:35], v[16:19], v[240:243], v[32:35]\n"
        "v_mfma_f32_16x16x32_bf16 v[36:39], v[16:19], v[244:247], v[36:39]\n"
        "v_mfma_f32_16x16x32_bf16 v[40:43], v[248:251], v[16:19], v[40:43]\n"
        "v_mfma_f32_16x16x32_bf16 v[44:47], v[252:255], v[16:19], v[44:47]\n"
        "v_cvt_pk_bf16_f32 v24, v24, v25\n"
        "v_cvt_pk_bf16_f32 v25, v26, v27\n"
        "v_cvt_pk_bf16_f32 v26, v28, v29\n"
        "v_cvt_pk_bf16_f32 v27, v30, v31\n"
        "s_add_u32 s66, s72, 524288\n"
        "buffer_store_dwordx4 v[24:27], v1, s[40:43], s66 offen offset:0 sc1\n"
        "v_cvt_pk_bf16_f32 v72, v32, v33\n"
        "v_cvt_pk_bf16_f32 v73, v34, v35\n"
        "v_cvt_pk_bf16_f32 v76, v36, v37\n"
        "v_cvt_pk_bf16_f32 v77, v38, v39\n"
        "v_cvt_pk_bf16_f32 v40, v40, v41\n"
        "v_cvt_pk_bf16_f32 v41, v42, v43\n"
        "v_cvt_pk_bf16_f32 v42, v44, v45\n"
        "v_cvt_pk_bf16_f32 v43, v46, v47\n"
        "buffer_store_dwordx4 v[40:43], v1, s[40:43], s72 offen offset:0 sc1\n"
        "v_mov_b32 v5, s74\n"
        "s_mov_b32 s84, s60\n"
        "s_waitcnt vmcnt(2)\n"
        "s_branch .Ls1_go0_%=\n"
        ".Ls1_i02_%=:\n"
        "v_mfma_f32_16x16x32_bf16 v[40:43], v[200:203], v[8:11], v[40:43]\n"
        "buffer_load_dword v4, v2, s[40:43], s86 offen sc1\n"
        "v_mfma_f32_16x16x32_bf16 v[44:47], v[204:207], v[8:11], v[44:47]\n"
        "s_waitcnt lgkmcnt(1)\n"
        "v_mfma_f32_16x16x32_bf16 v[24:27], v[208:211], v[12:15], v[24:27]\n"
        "v_mfma_f32_16x16x32_bf16 v[28:31], v[212:215], v[12:15], v[28:31]\n"
        "v_mfma_f32_16x16x32_bf16 v[32:35], v[12:15], v[216:219], v[32:35]\n"
        "v_mfma_f32_16x16x32_bf16 v[36:39], v[12:15], v[220:223], v[36:39]\n"
        "v_mfma_f32_16x16x32_bf16 v[40:43], v[224:227], v[12:15], v[40:43]\n"
        "v_mfma_f32_16x16x32_bf16 v[44:47], v[228:231], v[12:15], v[44:47]\n"
        "s_waitcnt lgkmcnt(0)\n"
        "v_mfma_f32_16x16x32_bf16 v[24:27], v[232:235], v[16:19], v[24:27]\n"
        "v_mfma_f32_16x16x32_bf16 v[28:31], v[236:239], v[16:19], v[28:31]\n"
        "v_mfma_f32_16x16x32_bf16 v[32:35], v[16:19], v[240:243], v[32:35]\n"
        "v_mfma_f32_16x16x32_bf16 v[36:39], v[16:19], v[244:247], v[36:39]\n"
        "v_mfma_f32_16x16x32_bf16 v[40:43], v[248:251], v[16:19], v[40:43]\n"
        "v_mfma_f32_16x16x32_bf16 v[44:47], v[252:255], v[16:19], v[44:47]\n"
        "v_cvt_pk_bf16_f32 v24, v24, v25\n"
        "v_cvt_pk_bf16_f32 v25, v26, v27\n"
        "v_cvt_pk_bf16_f32 v26, v28, v29\n"
        "v_cvt_pk_bf16_f32 v27, v30, v31\n"
        "s_add_u32 s66, s72, 524288\n"
        "buffer_store_dwordx4 v[24:27], v1, s[40:43], s66 offen offset:0 sc1\n"
        "v_cvt_pk_bf16_f32 v72, v32, v33\n"
        "v_cvt_pk_bf16_f32 v73, v34, v35\n"
        "v_cvt_pk_bf16_f32 v76, v36, v37\n"
        "v_cvt_pk_bf16_f32 v77, v38, v39\n"
        "v_cvt_pk_bf16_f32 v40, v40, v41\n"
        "v_cvt_pk_bf16_f32 v41, v42, v43\n"
        "v_cvt_pk_bf16_f32 v42, v44, v45\n"
        "v_cvt_pk_bf16_f32 v43, v46, v47\n"
        "buffer_store_dwordx4 v[40:43], v1, s[40:43], s72 offen offset:0 sc1\n"
        "s_waitcnt vmcnt(2)\n"
        "v_cmp_gt_i32 vcc, s70, v4\n"
        "s_nop 0\n"
        "s_and_b64 vcc, vcc, 1\n"
        "s_cbranch_vccnz .Ls1_late0_%=\n"
        "buffer_load_dwordx4 v1, s[40:43], s71 offen offset:0 sc1 lds\n"
        "buffer_load_dwordx4 v1, s[40:43], s71 offen offset:1024 sc1 lds\n"
        "buffer_load_dwordx4 v1, s[40:43], s71 offen offset:2048 sc1 lds\n"
        "buffer_load_dwordx4 v1, s[40:43], s71 offen offset:3072 sc1 lds\n"
        "s_add_u32 m0, s78, s85\n"
        "s_add_u32 s69, s69, 1\n"
        "buffer_load_dwordx4 v1, s[40:43], s81 offen offset:0 sc1 lds\n"
        "s_waitcnt vmcnt(5)\n"
        "v_mov_b32 v5, s74\n"
        "buffer_store_dword v5, v3, s[40:43], s73 offen sc1\n"
        "s_waitcnt vmcnt(1)\n"
        "s_branch .Ls1_go0_%=\n"
        ".Ls1_late0_%=:\n"
        "s_waitcnt vmcnt(0)\n"
        "v_mov_b32 v5, s74\n"
        "buffer_store_dword v5, v3, s[40:43], s73 offen sc1\n"
        "s_mov_b32 s64, 0\n"
        ".Ls1_in0_top_%=:\n"
        "buffer_load_dword v4, v2, s[40:43], s86 offen sc1\n"
        "s_waitcnt vmcnt(0)\n"
        "v_cmp_gt_i32 vcc, s70, v4\n"
        "s_nop 0\n"
        "s_and_b64 vcc, vcc, 1\n"
        "s_cbranch_vccz .Ls1_in0_ok_%=\n"
        "s_add_u32 s64, s64, 1\n"
        "s_and_b32 s65, s64, 255\n"
        "s_cbranch_scc1 .Ls1_in0_go_%=\n"
        "s_mov_b32 s65, 49152\n"
        "s_cmp_lt_u32 s64, 1048576\n"
        "s_cbranch_scc1 .Ls1_in0_chk_%=\n"
        "v_mov_b32 v4, 1\n"
        "buffer_store_dword v4, off, s[40:43], s65 sc1\n"
        "s_waitcnt vmcnt(0)\n"
        ".Ls1_in0_chk_%=:\n"
        "buffer_load_dword v4, off, s[40:43], s65 sc1\n"
        "s_waitcnt vmcnt(0)\n"
        "v_readfirstlane_b32 s65, v4\n"
        "s_nop 0\n"
        "s_cmp_lg_u32 s65, 1\n"
        "s_cbranch_scc1 .Ls1_in0_go_%=\n"
        "s_endpgm\n"
        ".Ls1_in0_go_%=:\n"
        "s_branch .Ls1_in0_top_%=\n"
        ".Ls1_in0_ok_%=:\n"
        "buffer_load_dwordx4 v1, s[40:43], s71 offen offset:0 sc1 lds\n"
        "buffer_load_dwordx4 v1, s[40:43], s71 offen offset:1024 sc1 lds\n"
        "buffer_load_dwordx4 v1, s[40:43], s71 offen offset:2048 sc1 lds\n"
        "buffer_load_dwordx4 v1, s[40:43], s71 offen offset:3072 sc1 lds\n"
        "s_add_u32 m0, s78, s85\n"
        "s_add_u32 s69, s69, 1\n"
        "buffer_load_dwordx4 v1, s[40:43], s81 offen offset:0 sc1 lds\n"
        "s_waitcnt vmcnt(0)\n"
        ".Ls1_go0_%=:\n"
        "s_mov_b32 s72, 241172480\n"
        "s_mov_b32 s83, 241172480\n"
        "s_mov_b32 s73, 241172480\n"
        "v_add_u32 v0, s78, v6\n"
        "s_barrier\n"
        "ds_read_b128 v[8:11], v0 offset:0\n"
        "ds_read_b128 v[12:15], v0 offset:512\n"
        "ds_read_b128 v[16:19], v0 offset:1024\n"
        "s_waitcnt lgkmcnt(2)\n"
        "s_branch .Ls1_i1_%=\n"
        ".Ls1_i1_%=:\n"
        "v_mfma_f32_16x16x32_bf16 v[48:51], a[0:3], v[8:11], 0\n"
        "s_add_u32 s86, s76, 0\n"
        "buffer_load_dword v4, v2, s[40:43], s86 offen sc1\n"
        "v_mfma_f32_16x16x32_bf16 v[52:55], a[4:7], v[8:11], 0\n"
        "s_lshr_b32 s67, s69, 1\n"
        "v_cvt_pk_bf16_f32 v24, v24, v25\n"
        "v_cvt_pk_bf16_f32 v25, v26, v27\n"
        "v_mfma_f32_16x16x32_bf16 v[56:59], v[8:11], a[8:11], 0\n"
        "s_bitcmp1_b64 s[52:53], s67\n"
        "s_cselect_b32 s65, 8192, 0\n"
        "v_cvt_pk_bf16_f32 v26, v28, v29\n"
        "v_mfma_f32_16x16x32_bf16 v[60:63], v[8:11], a[12:15], 0\n"
        "s_lshl_b32 s77, s67, 21\n"
        "v_cvt_pk_bf16_f32 v27, v30, v31\n"
        "s_add_u32 s66, s72, 524288\n"
        "v_mfma_f32_16x16x32_bf16 v[64:67], a[16:19], v[8:11], 0\n"
        "s_add_u32 s77, s77, s65\n"
        "buffer_store_dwordx4 v[24:27], v1, s[40:43], s66 offen offset:0 sc1\n"
        "v_mfma_f32_16x16x32_bf16 v[68:71], a[20:23], v[8:11], 0\n"
        "s_waitcnt lgkmcnt(1)\n"
        "ds_read_b128 v[8:11], v0 offset:1536\n"
        "v_cvt_pk_bf16_f32 v72, v32, v33\n"
        "v_mfma_f32_16x16x32_bf16 v[48:51], a[24:27], v[12:15], v[48:51]\n"
        "s_add_u32 s71, s77, s58\n"
        "v_cvt_pk_bf16_f32 v73, v34, v35\n"
        "v_cvt_pk_bf16_f32 v76, v36, v37\n"
        "v_mfma_f32_16x16x32_bf16 v[52:55], a[28:31], v[12:15], v[52:55]\n"
        "s_add_u32 s81, s77, s62\n"
        "v_cvt_pk_bf16_f32 v77, v38, v39\n"
        "v_cvt_pk_bf16_f32 v40, v40, v41\n"
        "v_mfma_f32_16x16x32_bf16 v[56:59], v[12:15], a[32:35], v[56:59]\n"
        "s_add_u32 s78, s78, 18432\n"
        "v_cvt_pk_bf16_f32 v41, v42, v43\n"
        "v_cvt_pk_bf16_f32 v42, v44, v45\n"
        "v_mfma_f32_16x16x32_bf16 v[60:63], v[12:15], a[36:39], v[60:63]\n"
        "s_cmp_eq_u32 s78, 55296\n"
        "s_cselect_b32 s78, 0, s78\n"
        "v_cvt_pk_bf16_f32 v43, v46, v47\n"
        "v_mfma_f32_16x16x32_bf16 v[64:67], a[40:43], v[12:15], v[64:67]\n"
        "s_add_u32 m0, s78, s61\n"
        "v_mfma_f32_16x16x32_bf16 v[68:71], a[44:47], v[12:15], v[68:71]\n"
        "s_waitcnt lgkmcnt(1)\n"
        "ds_read_b128 v[12:15], v0 offset:2048\n"
        "v_mfma_f32_16x16x32_bf16 v[48:51], a[48:51], v[16:19], v[48:51]\n"
        "s_lshr_b32 s70, s69, 1\n"
        "buffer_store_dwordx4 v[40:43], v1, s[40:43], s72 offen offset:0 sc1\n"
        "v_mfma_f32_16x16x32_bf16 v[52:55], a[52:55], v[16:19], v[52:55]\n"
        "v_mfma_f32_16x16x32_bf16 v[56:59], v[16:19], a[56:59], v[56:59]\n"
        "s_waitcnt vmcnt(3)\n"
        "v_mfma_f32_16x16x32_bf16 v[60:63], v[16:19], a[60:63], v[60:63]\n"
        "buffer_store_dword v5, v3, s[40:43], s84 offen sc1\n"
        "v_mfma_f32_16x16x32_bf16 v[64:67], a[64:67], v[16:19], v[64:67]\n"
        "v_mov_b32 v5, s74\n"
        "v_mfma_f32_16x16x32_bf16 v[68:71], a[68:71], v[16:19], v[68:71]\n"
        "s_waitcnt lgkmcnt(1)\n"
        "ds_read_b128 v[16:19], v0 offset:2560\n"
        "v_mfma_f32_16x16x32_bf16 v[48:51], a[72:75], v[8:11], v[48:51]\n"
        "v_mfma_f32_16x16x32_bf16 v[52:55], a[76:79], v[8:11], v[52:55]\n"
        "v_mfma_f32_16x16x32_bf16 v[56:59], v[8:11], a[80:83], v[56:59]\n"
        "v_mfma_f32_16x16x32_bf16 v[60:63], v[8:11], a[84:87], v[60:63]\n"
        "v_mfma_f32_16x16x32_bf16 v[64:67], a[88:91], v[8:11], v[64:67]\n"
        "v_mfma_f32_16x16x32_bf16 v[68:71], a[92:95], v[8:11], v[68:71]\n"
        "s_waitcnt lgkmcnt(1)\n"
        "ds_read_b128 v[8:11], v0 offset:3072\n"
        "v_mfma_f32_16x16x32_bf16 v[48:51], a[96:99], v[12:15], v[48:51]\n"
        "v_mfma_f32_16x16x32_bf16 v[52:55], a[100:103], v[12:15], v[52:55]\n"
        "v_mfma_f32_16x16x32_bf16 v[56:59], v[12:15], a[104:107], v[56:59]\n"
        "v_mfma_f32_16x16x32_bf16 v[60:63], v[12:15], a[108:111], v[60:63]\n"
        "v_mfma_f32_16x16x32_bf16 v[64:67], a[112:115], v[12:15], v[64:67]\n"
        "s_waitcnt vmcnt(3)\n"
        "v_cmp_gt_i32 vcc, s70, v4\n"
        "v_mfma_f32_16x16x32_bf16 v[68:71], a[116:119], v[12:15], v[68:71]\n"
        "s_waitcnt lgkmcnt(1)\n"
        "ds_read_b128 v[12:15], v0 offset:3584\n"
        "v_mfma_f32_16x16x32_bf16 v[48:51], a[120:123], v[16:19], v[48:51]\n"
        "s_and_b64 vcc, vcc, 1\n"
        "v_mfma_f32_16x16x32_bf16 v[52:55], a[124:127], v[16:19], v[52:55]\n"
        "s_cbranch_vccnz .Ls1_i10_%=\n"
        "v_mfma_f32_16x16x32_bf16 v[56:59], v[16:19], a[128:131], v[56:59]\n"
        "buffer_load_dwordx4 v1, s[40:43], s71 offen offset:0 sc1 lds\n"
        "v_mfma_f32_16x16x32_bf16 v[60:63], v[16:19], a[132:135], v[60:63]\n"
        "v_mfma_f32_16x16x32_bf16 v[64:67], a[136:139], v[16:19], v[64:67]\n"
        "v_mfma_f32_16x16x32_bf16 v[68:71], a[140:143], v[16:19], v[68:71]\n"
        "s_waitcnt lgkmcnt(1)\n"
        "ds_read_b128 v[16:19], v0 offset:4096\n"
        "v_mfma_f32_16x16x32_bf16 v[48:51], a[144:147], v[8:11], v[48:51]\n"
        "s_getreg_b32 s88, hwreg(HW_REG_IB_STS, 0, 4)\n"
        "buffer_load_dwordx4 v1, s[40:43], s71 offen offset:1024 sc1 lds\n"
        "v_mfma_f32_16x16x32_bf16 v[52:55], a[148:151], v[8:11], v[52:55]\n"
        "s_cmp_le_u32 s88, 2\n"
        "s_cselect_b32 s88, s73, s89\n"
        "s_cselect_b32 s73, s89, s73\n"
        "v_mfma_f32_16x16x32_bf16 v[56:59], v[8:11], a[152:155], v[56:59]\n"
        "buffer_store_dword v5, v3, s[40:43], s88 offen sc1\n"
        "v_mfma_f32_16x16x32_bf16 v[60:63], v[8:11], a[156:159], v[60:63]\n"
        "v_mfma_f32_16x16x32_bf16 v[64:67], a[160:163], v[8:11], v[64:67]\n"
        "buffer_load_dwordx4 v1, s[40:43], s71 offen offset:2048 sc1 lds\n"
        "v_mfma_f32_16x16x32_bf16 v[68:71], a[164:167], v[8:11], v[68:71]\n"
        "s_waitcnt lgkmcnt(1)\n"
        "ds_read_b128 v[8:11], v0 offset:4608\n"
        "v_mfma_f32_16x16x32_bf16 v[48:51], a[168:171], v[12:15], v[48:51]\n"
        "v_mfma_f32_16x16x32_bf16 v[52:55], a[172:175], v[12:15], v[52:55]\n"
        "v_mfma_f32_16x16x32_bf16 v[56:59], v[12:15], a[176:179], v[56:59]\n"
        "s_getreg_b32 s88, hwreg(HW_REG_IB_STS, 0, 4)\n"
        "buffer_load_dwordx4 v1, s[40:43], s71 offen offset:3072 sc1 lds\n"
        "v_mfma_f32_16x16x32_bf16 v[60:63], v[12:15], a[180:183], v[60:63]\n"
        "s_add_u32 m0, s78, s85\n"
        "s_add_u32 s69, s69, 1\n"
        "v_mfma_f32_16x16x32_bf16 v[64:67], a[184:187], v[12:15], v[64:67]\n"
        "s_cmp_le_u32 s88, 5\n"
        "s_cselect_b32 s88, s73, s89\n"
        "s_cselect_b32 s73, s89, s73\n"
        "v_mfma_f32_16x16x32_bf16 v[68:71], a[188:191], v[12:15], v[68:71]\n"
        "s_waitcnt lgkmcnt(1)\n"
        "ds_read_b128 v[12:15], v0 offset:5120\n"
        "v_mfma_f32_16x16x32_bf16 v[48:51], a[192:195], v[16:19], v[48:51]\n"
        "buffer_load_dwordx4 v1, s[40:43], s81 offen offset:0 sc1 lds\n"
        "v_mfma_f32_16x16x32_bf16 v[52:55], a[196:199], v[16:19], v[52:55]\n"
        "buffer_store_dword v5, v3, s[40:43], s88 offen sc1\n"
        "v_mfma_f32_16x16x32_bf16 v[56:59], v[16:19], a[200:203], v[56:59]\n"
        "v_mfma_f32_16x16x32_bf16 v[60:63], v[16:19], a[204:207], v[60:63]\n"
        "v_mfma_f32_16x16x32_bf16 v[64:67], a[208:211], v[16:19], v[64:67]\n"
        "s_getreg_b32 s88, hwreg(HW_REG_IB_STS, 0, 4)\n"
        "v_mfma_f32_16x16x32_bf16 v[68:71], a[212:215], v[16:19], v[68:71]\n"
        "s_waitcnt lgkmcnt(1)\n"
        "ds_read_b128 v[16:19], v0 offset:5632\n"
        "v_mfma_f32_16x16x32_bf16 v[48:51], a[216:219], v[8:11], v[48:51]\n"
        "s_cmp_le_u32 s88, 8\n"
        "s_cselect_b32 s88, s73, s89\n"
        "s_cselect_b32 s73, s89, s73\n"
        "v_mfma_f32_16x16x32_bf16 v[52:55], a[220:223], v[8:11], v[52:55]\n"
        "buffer_store_dword v5, v3, s[40:43], s88 offen sc1\n"
        "v_mfma_f32_16x16x32_bf16 v[56:59], v[8:11], a[224:227], v[56:59]\n"
        "v_mfma_f32_16x16x32_bf16 v[60:63], v[8:11], a[228:231], v[60:63]\n"
        "v_mfma_f32_16x16x32_bf16 v[64:67], a[232:235], v[8:11], v[64:67]\n"
        "v_mfma_f32_16x16x32_bf16 v[68:71], a[236:239], v[8:11], v[68:71]\n"
        "s_waitcnt lgkmcnt(1)\n"
        "ds_read_b128 v[8:11], v0 offset:6144\n"
        "v_mfma_f32_16x16x32_bf16 v[48:51], a[240:243], v[12:15], v[48:51]\n"
        "v_mfma_f32_16x16x32_bf16 v[52:55], a[244:247], v[12:15], v[52:55]\n"
        "v_mfma_f32_16x16x32_bf16 v[56:59], v[12:15], a[248:251], v[56:59]\n"
        "v_mfma_f32_16x16x32_bf16 v[60:63], v[12:15], a[252:255], v[60:63]\n"
        "v_mfma_f32_16x16x32_bf16 v[64:67], v[80:83], v[12:15], v[64:67]\n"
        "v_mfma_f32_16x16x32_bf16 v[68:71], v[84:87], v[12:15], v[68:71]\n"
        "s_waitcnt lgkmcnt(1)\n"
        "ds_read_b128 v[12:15], v0 offset:6656\n"
        "v_mfma_f32_16x16x32_bf16 v[48:51], v[88:91], v[16:19], v[48:51]\n"
        "s_waitcnt vmcnt(9)\n"
        "buffer_store_dword v5, v3, s[40:43], s73 offen sc1\n"
        "v_mfma_f32_16x16x32_bf16 v[52:55], v[92:95], v[16:19], v[52:55]\n"
        "s_mov_b32 s84, s89\n"
        "s_mov_b32 s73, s60\n"
        "s_add_u32 s74, s74, 1\n"
        "v_mfma_f32_16x16x32_bf16 v[56:59], v[16:19], v[96:99], v[56:59]\n"
        "s_add_u32 s72, s75, s59\n"
        "v_mfma_f32_16x16x32_bf16 v[60:63], v[16:19], v[100:103], v[60:63]\n"
        "v_mfma_f32_16x16x32_bf16 v[64:67], v[104:107], v[16:19], v[64:67]\n"
        "v_mfma_f32_16x16x32_bf16 v[68:71], v[108:111], v[16:19], v[68:71]\n"
        "s_waitcnt lgkmcnt(1)\n"
        "ds_read_b128 v[16:19], v0 offset:7168\n"
        "v_mfma_f32_16x16x32_bf16 v[48:51], v[112:115], v[8:11], v[48:51]\n"
        "v_mfma_f32_16x16x32_bf16 v[52:55], v[116:119], v[8:11], v[52:55]\n"
        "v_mfma_f32_16x16x32_bf16 v[56:59], v[8:11], v[120:123], v[56:59]\n"
        "v_mfma_f32_16x16x32_bf16 v[60:63], v[8:11], v[124:127], v[60:63]\n"
        "v_mfma_f32_16x16x32_bf16 v[64:67], v[128:131], v[8:11], v[64:67]\n"
        "v_mfma_f32_16x16x32_bf16 v[68:71], v[132:135], v[8:11], v[68:71]\n"
        "s_waitcnt lgkmcnt(1)\n"
        "ds_read_b128 v[8:11], v0 offset:7680\n"
        "v_mfma_f32_16x16x32_bf16 v[48:51], v[136:139], v[12:15], v[48:51]\n"
        "v_mfma_f32_16x16x32_bf16 v[52:55], v[140:143], v[12:15], v[52:55]\n"
        "v_mfma_f32_16x16x32_bf16 v[56:59], v[12:15], v[144:147], v[56:59]\n"
        "v_mfma_f32_16x16x32_bf16 v[60:63], v[12:15], v[148:151], v[60:63]\n"
        "v_mfma_f32_16x16x32_bf16 v[64:67], v[152:155], v[12:15], v[64:67]\n"
        "v_mfma_f32_16x16x32_bf16 v[68:71], v[156:159], v[12:15], v[68:71]\n"
        "s_waitcnt lgkmcnt(1)\n"
        "ds_read_b128 v[12:15], v0 offset:8192\n"
        "v_mfma_f32_16x16x32_bf16 v[48:51], v[160:163], v[16:19], v[48:51]\n"
        "v_mfma_f32_16x16x32_bf16 v[52:55], v[164:167], v[16:19], v[52:55]\n"
        "v_mfma_f32_16x16x32_bf16 v[56:59], v[16:19], v[168:171], v[56:59]\n"
        "v_mfma_f32_16x16x32_bf16 v[60:63], v[16:19], v[172:175], v[60:63]\n"
        "v_mfma_f32_16x16x32_bf16 v[64:67], v[176:179], v[16:19], v[64:67]\n"
        "v_mfma_f32_16x16x32_bf16 v[68:71], v[180:183], v[16:19], v[68:71]\n"
        "s_waitcnt lgkmcnt(1)\n"
        "ds_read_b128 v[16:19], v0 offset:8704\n"
        "v_mfma_f32_16x16x32_bf16 v[48:51], v[184:187], v[8:11], v[48:51]\n"
        "v_mfma_f32_16x16x32_bf16 v[52:55], v[188:191], v[8:11], v[52:55]\n"
        "v_mfma_f32_16x16x32_bf16 v[56:59], v[8:11], v[192:195], v[56:59]\n"
        "v_mfma_f32_16x16x32_bf16 v[60:63], v[8:11], v[196:199], v[60:63]\n"
        "v_mfma_f32_16x16x32_bf16 v[64:67], v[200:203], v[8:11], v[64:67]\n"
        "s_waitcnt vmcnt(3)\n"
        "s_barrier\n"
        "v_add_u32 v0, s78, v6\n"
        "v_mfma_f32_16x16x32_bf16 v[68:71], v[204:207], v[8:11], v[68:71]\n"
        "s_waitcnt lgkmcnt(1)\n"
        "ds_read_b128 v[8:11], v0 offset:0\n"
        "v_mfma_f32_16x16x32_bf16 v[48:51], v[208:211], v[12:15], v[48:51]\n"
        "v_mfma_f32_16x16x32_bf16 v[52:55], v[212:215], v[12:15], v[52:55]\n"
        "v_mfma_f32_16x16x32_bf16 v[56:59], v[12:15], v[216:219], v[56:59]\n"
        "v_mfma_f32_16x16x32_bf16 v[60:63], v[12:15], v[220:223], v[60:63]\n"
        "v_mfma_f32_16x16x32_bf16 v[64:67], v[224:227], v[12:15], v[64:67]\n"
        "v_mfma_f32_16x16x32_bf16 v[68:71], v[228:231], v[12:15], v[68:71]\n"
        "s_waitcnt lgkmcnt(1)\n"
        "ds_read_b128 v[12:15], v0 offset:512\n"
        "v_mfma_f32_16x16x32_bf16 v[48:51], v[232:235], v[16:19], v[48:51]\n"
        "v_mfma_f32_16x16x32_bf16 v[52:55], v[236:239], v[16:19], v[52:55]\n"
        "v_mfma_f32_16x16x32_bf16 v[56:59], v[16:19], v[240:243], v[56:59]\n"
        "v_mfma_f32_16x16x32_bf16 v[60:63], v[16:19], v[244:247], v[60:63]\n"
        "v_mfma_f32_16x16x32_bf16 v[64:67], v[248:251], v[16:19], v[64:67]\n"
        "v_mfma_f32_16x16x32_bf16 v[68:71], v[252:255], v[16:19], v[68:71]\n"
        "s_waitcnt lgkmcnt(1)\n"
        "ds_read_b128 v[16:19], v0 offset:1024\n"
        "s_branch .Ls1_i0_%=\n"
        ".Ls1_i10_%=:\n"
        "v_mfma_f32_16x16x32_bf16 v[56:59], v[16:19], a[128:131], v[56:59]\n"
        "buffer_load_dword v4, v2, s[40:43], s86 offen sc1\n"
        "v_mfma_f32_16x16x32_bf16 v[60:63], v[16:19], a[132:135], v[60:63]\n"
        "v_mfma_f32_16x16x32_bf16 v[64:67], a[136:139], v[16:19], v[64:67]\n"
        "v_mfma_f32_16x16x32_bf16 v[68:71], a[140:143], v[16:19], v[68:71]\n"
        "s_waitcnt lgkmcnt(1)\n"
        "ds_read_b128 v[16:19], v0 offset:4096\n"
        "v_mfma_f32_16x16x32_bf16 v[48:51], a[144:147], v[8:11], v[48:51]\n"
        "s_getreg_b32 s88, hwreg(HW_REG_IB_STS, 0, 4)\n"
        "v_mfma_f32_16x16x32_bf16 v[52:55], a[148:151], v[8:11], v[52:55]\n"
        "s_cmp_le_u32 s88, 2\n"
        "s_cselect_b32 s88, s73, s89\n"
        "s_cselect_b32 s73, s89, s73\n"
        "v_mfma_f32_16x16x32_bf16 v[56:59], v[8:11], a[152:155], v[56:59]\n"
        "buffer_store_dword v5, v3, s[40:43], s88 offen sc1\n"
        "v_mfma_f32_16x16x32_bf16 v[60:63], v[8:11], a[156:159], v[60:63]\n"
        "v_mfma_f32_16x16x32_bf16 v[64:67], a[160:163], v[8:11], v[64:67]\n"
        "v_mfma_f32_16x16x32_bf16 v[68:71], a[164:167], v[8:11], v[68:71]\n"
        "s_waitcnt lgkmcnt(1)\n"
        "ds_read_b128 v[8:11], v0 offset:4608\n"
        "v_mfma_f32_16x16x32_bf16 v[48:51], a[168:171], v[12:15], v[48:51]\n"
        "v_mfma_f32_16x16x32_bf16 v[52:55], a[172:175], v[12:15], v[52:55]\n"
        "v_mfma_f32_16x16x32_bf16 v[56:59], v[12:15], a[176:179], v[56:59]\n"
        "s_getreg_b32 s88, hwreg(HW_REG_IB_STS, 0, 4)\n"
        "v_mfma_f32_16x16x32_bf16 v[60:63], v[12:15], a[180:183], v[60:63]\n"
        "s_cmp_le_u32 s88, 3\n"
        "s_cselect_b32 s88, s73, s89\n"
        "s_cselect_b32 s73, s89, s73\n"
        "v_mfma_f32_16x16x32_bf16 v[64:67], a[184:187], v[12:15], v[64:67]\n"
        "buffer_store_dword v5, v3, s[40:43], s88 offen sc1\n"
        "v_mfma_f32_16x16x32_bf16 v[68:71], a[188:191], v[12:15], v[68:71]\n"
        "s_waitcnt lgkmcnt(1)\n"
        "ds_read_b128 v[12:15], v0 offset:5120\n"
        "v_mfma_f32_16x16x32_bf16 v[48:51], a[192:195], v[16:19], v[48:51]\n"
        "v_mfma_f32_16x16x32_bf16 v[52:55], a[196:199], v[16:19], v[52:55]\n"
        "v_mfma_f32_16x16x32_bf16 v[56:59], v[16:19], a[200:203], v[56:59]\n"
        "v_mfma_f32_16x16x32_bf16 v[60:63], v[16:19], a[204:207], v[60:63]\n"
        "v_mfma_f32_16x16x32_bf16 v[64:67], a[208:211], v[16:19], v[64:67]\n"
        "s_getreg_b32 s88, hwreg(HW_REG_IB_STS, 0, 4)\n"
        "v_mfma_f32_16x16x32_bf16 v[68:71], a[212:215], v[16:19], v[68:71]\n"
        "s_waitcnt lgkmcnt(1)\n"
        "ds_read_b128 v[16:19], v0 offset:5632\n"
        "v_mfma_f32_16x16x32_bf16 v[48:51], a[216:219], v[8:11], v[48:51]\n"
        "s_cmp_le_u32 s88, 4\n"
        "s_cselect_b32 s88, s73, s89\n"
        "s_cselect_b32 s73, s89, s73\n"
        "v_mfma_f32_16x16x32_bf16 v[52:55], a[220:223], v[8:11], v[52:55]\n"
        "buffer_store_dword v5, v3, s[40:43], s88 offen sc1\n"
        "v_mfma_f32_16x16x32_bf16 v[56:59], v[8:11], a[224:227], v[56:59]\n"
        "v_mfma_f32_16x16x32_bf16 v[60:63], v[8:11], a[228:231], v[60:63]\n"
        "v_mfma_f32_16x16x32_bf16 v[64:67], a[232:235], v[8:11], v[64:67]\n"
        "v_mfma_f32_16x16x32_bf16 v[68:71], a[236:239], v[8:11], v[68:71]\n"
        "s_waitcnt lgkmcnt(1)\n"
        "ds_read_b128 v[8:11], v0 offset:6144\n"
        "s_waitcnt vmcnt(3)\n"
        "v_cmp_gt_i32 vcc, s70, v4\n"
        "v_mfma_f32_16x16x32_bf16 v[48:51], a[240:243], v[12:15], v[48:51]\n"
        "v_mfma_f32_16x16x32_bf16 v[52:55], a[244:247], v[12:15], v[52:55]\n"
        "s_and_b64 vcc, vcc, 1\n"
        "v_mfma_f32_16x16x32_bf16 v[56:59], v[12:15], a[248:251], v[56:59]\n"
        "s_cbranch_vccnz .Ls1_i11_%=\n"
        "v_mfma_f32_16x16x32_bf16 v[60:63], v[12:15], a[252:255], v[60:63]\n"
        "buffer_load_dwordx4 v1, s[40:43], s71 offen offset:0 sc1 lds\n"
        "v_mfma_f32_16x16x32_bf16 v[64:67], v[80:83], v[12:15], v[64:67]\n"
        "v_mfma_f32_16x16x32_bf16 v[68:71], v[84:87], v[12:15], v[68:71]\n"
        "s_waitcnt lgkmcnt(1)\n"
        "ds_read_b128 v[12:15], v0 offset:6656\n"
        "v_mfma_f32_16x16x32_bf16 v[48:51], v[88:91], v[16:19], v[48:51]\n"
        "buffer_load_dwordx4 v1, s[40:43], s71 offen offset:1024 sc1 lds\n"
        "v_mfma_f32_16x16x32_bf16 v[52:55], v[92:95], v[16:19], v[52:55]\n"
        "buffer_store_dword v5, v3, s[40:43], s73 offen sc1\n"
        "v_mfma_f32_16x16x32_bf16 v[56:59], v[16:19], v[96:99], v[56:59]\n"
        "s_mov_b32 s84, s89\n"
        "s_mov_b32 s73, s60\n"
        "s_add_u32 s74, s74, 1\n"
        "v_mfma_f32_16x16x32_bf16 v[60:63], v[16:19], v[100:103], v[60:63]\n"
        "s_add_u32 s72, s75, s59\n"
        "buffer_load_dwordx4 v1, s[40:43], s71 offen offset:2048 sc1 lds\n"
        "v_mfma_f32_16x16x32_bf16 v[64:67], v[104:107], v[16:19], v[64:67]\n"
        "v_mfma_f32_16x16x32_bf16 v[68:71], v[108:111], v[16:19], v[68:71]\n"
        "s_waitcnt lgkmcnt(1)\n"
        "ds_read_b128 v[16:19], v0 offset:7168\n"
        "v_mfma_f32_16x16x32_bf16 v[48:51], v[112:115], v[8:11], v[48:51]\n"
        "buffer_load_dwordx4 v1, s[40:43], s71 offen offset:3072 sc1 lds\n"
        "v_mfma_f32_16x16x32_bf16 v[52:55], v[116:119], v[8:11], v[52:55]\n"
        "s_add_u32 m0, s78, s85\n"
        "s_add_u32 s69, s69, 1\n"
        "v_mfma_f32_16x16x32_bf16 v[56:59], v[8:11], v[120:123], v[56:59]\n"
        "v_mfma_f32_16x16x32_bf16 v[60:63], v[8:11], v[124:127], v[60:63]\n"
        "buffer_load_dwordx4 v1, s[40:43], s81 offen offset:0 sc1 lds\n"
        "v_mfma_f32_16x16x32_bf16 v[64:67], v[128:131], v[8:11], v[64:67]\n"
        "v_mfma_f32_16x16x32_bf16 v[68:71], v[132:135], v[8:11], v[68:71]\n"
        "s_waitcnt lgkmcnt(1)\n"
        "ds_read_b128 v[8:11], v0 offset:7680\n"
        "v_mfma_f32_16x16x32_bf16 v[48:51], v[136:139], v[12:15], v[48:51]\n"
        "v_mfma_f32_16x16x32_bf16 v[52:55], v[140:143], v[12:15], v[52:55]\n"
        "v_mfma_f32_16x16x32_bf16 v[56:59], v[12:15], v[144:147], v[56:59]\n"
        "v_mfma_f32_16x16x32_bf16 v[60:63], v[12:15], v[148:151], v[60:63]\n"
        "v_mfma_f32_16x16x32_bf16 v[64:67], v[152:155], v[12:15], v[64:67]\n"
        "v_mfma_f32_16x16x32_bf16 v[68:71], v[156:159], v[12:15], v[68:71]\n"
        "s_waitcnt lgkmcnt(1)\n"
        "ds_read_b128 v[12:15], v0 offset:8192\n"
        "v_mfma_f32_16x16x32_bf16 v[48:51], v[160:163], v[16:19], v[48:51]\n"
        "v_mfma_f32_16x16x32_bf16 v[52:55], v[164:167], v[16:19], v[52:55]\n"
        "v_mfma_f32_16x16x32_bf16 v[56:59], v[16:19], v[168:171], v[56:59]\n"
        "v_mfma_f32_16x16x32_bf16 v[60:63], v[16:19], v[172:175], v[60:63]\n"
        "v_mfma_f32_16x16x32_bf16 v[64:67], v[176:179], v[16:19], v[64:67]\n"
        "v_mfma_f32_16x16x32_bf16 v[68:71], v[180:183], v[16:19], v[68:71]\n"
        "s_waitcnt lgkmcnt(1)\n"
        "ds_read_b128 v[16:19], v0 offset:8704\n"
        "v_mfma_f32_16x16x32_bf16 v[48:51], v[184:187], v[8:11], v[48:51]\n"
        "v_mfma_f32_16x16x32_bf16 v[52:55], v[188:191], v[8:11], v[52:55]\n"
        "v_mfma_f32_16x16x32_bf16 v[56:59], v[8:11], v[192:195], v[56:59]\n"
        "v_mfma_f32_16x16x32_bf16 v[60:63], v[8:11], v[196:199], v[60:63]\n"
        "v_mfma_f32_16x16x32_bf16 v[64:67], v[200:203], v[8:11], v[64:67]\n"
        "s_waitcnt vmcnt(0)\n"
        "s_barrier\n"
        "v_add_u32 v0, s78, v6\n"
        "v_mfma_f32_16x16x32_bf16 v[68:71], v[204:207], v[8:11], v[68:71]\n"
        "s_waitcnt lgkmcnt(1)\n"
        "ds_read_b128 v[8:11], v0 offset:0\n"
        "v_mfma_f32_16x16x32_bf16 v[48:51], v[208:211], v[12:15], v[48:51]\n"
        "v_mfma_f32_16x16x32_bf16 v[52:55], v[212:215], v[12:15], v[52:55]\n"
        "v_mfma_f32_16x16x32_bf16 v[56:59], v[12:15], v[216:219], v[56:59]\n"
        "v_mfma_f32_16x16x32_bf16 v[60:63], v[12:15], v[220:223], v[60:63]\n"
        "v_mfma_f32_16x16x32_bf16 v[64:67], v[224:227], v[12:15], v[64:67]\n"
        "v_mfma_f32_16x16x32_bf16 v[68:71], v[228:231], v[12:15], v[68:71]\n"
        "s_waitcnt lgkmcnt(1)\n"
        "ds_read_b128 v[12:15], v0 offset:512\n"
        "v_mfma_f32_16x16x32_bf16 v[48:51], v[232:235], v[16:19], v[48:51]\n"
        "v_mfma_f32_16x16x32_bf16 v[52:55], v[236:239], v[16:19], v[52:55]\n"
        "v_mfma_f32_16x16x32_bf16 v[56:59], v[16:19], v[240:243], v[56:59]\n"
        "v_mfma_f32_16x16x32_bf16 v[60:63], v[16:19], v[244:247], v[60:63]\n"
        "v_mfma_f32_16x16x32_bf16 v[64:67], v[248:251], v[16:19], v[64:67]\n"
        "v_mfma_f32_16x16x32_bf16 v[68:71], v[252:255], v[16:19], v[68:71]\n"
        "s_waitcnt lgkmcnt(1)\n"
        "ds_read_b128 v[16:19], v0 offset:1024\n"
        "s_branch .Ls1_i0_%=\n"
        ".Ls1_i11_%=:\n"
        "v_mfma_f32_16x16x32_bf16 v[60:63], v[12:15], a[252:255], v[60:63]\n"
        "buffer_load_dword v4, v2, s[40:43], s86 offen sc1\n"
        "v_mfma_f32_16x16x32_bf16 v[64:67], v[80:83], v[12:15], v[64:67]\n"
        "v_mfma_f32_16x16x32_bf16 v[68:71], v[84:87], v[12:15], v[68:71]\n"
        "s_waitcnt lgkmcnt(1)\n"
        "ds_read_b128 v[12:15], v0 offset:6656\n"
        "v_mfma_f32_16x16x32_bf16 v[48:51], v[88:91], v[16:19], v[48:51]\n"
        "buffer_store_dword v5, v3, s[40:43], s73 offen sc1\n"
        "v_mfma_f32_16x16x32_bf16 v[52:55], v[92:95], v[16:19], v[52:55]\n"
        "s_mov_b32 s84, s89\n"
        "s_mov_b32 s73, s60\n"
        "s_add_u32 s74, s74, 1\n"
        "v_mfma_f32_16x16x32_bf16 v[56:59], v[16:19], v[96:99], v[56:59]\n"
        "s_add_u32 s72, s75, s59\n"
        "v_mfma_f32_16x16x32_bf16 v[60:63], v[16:19], v[100:103], v[60:63]\n"
        "v_mfma_f32_16x16x32_bf16 v[64:67], v[104:107], v[16:19], v[64:67]\n"
        "v_mfma_f32_16x16x32_bf16 v[68:71], v[108:111], v[16:19], v[68:71]\n"
        "s_waitcnt lgkmcnt(1)\n"
        "ds_read_b128 v[16:19], v0 offset:7168\n"
        "v_mfma_f32_16x16x32_bf16 v[48:51], v[112:115], v[8:11], v[48:51]\n"
        "v_mfma_f32_16x16x32_bf16 v[52:55], v[116:119], v[8:11], v[52:55]\n"
        "v_mfma_f32_16x16x32_bf16 v[56:59], v[8:11], v[120:123], v[56:59]\n"
        "v_mfma_f32_16x16x32_bf16 v[60:63], v[8:11], v[124:127], v[60:63]\n"
        "v_mfma_f32_16x16x32_bf16 v[64:67], v[128:131], v[8:11], v[64:67]\n"
        "v_mfma_f32_16x16x32_bf16 v[68:71], v[132:135], v[8:11], v[68:71]\n"
        "s_waitcnt lgkmcnt(1)\n"
        "ds_read_b128 v[8:11], v0 offset:7680\n"
        "v_mfma_f32_16x16x32_bf16 v[48:51], v[136:139], v[12:15], v[48:51]\n"
        "v_mfma_f32_16x16x32_bf16 v[52:55], v[140:143], v[12:15], v[52:55]\n"
        "v_mfma_f32_16x16x32_bf16 v[56:59], v[12:15], v[144:147], v[56:59]\n"
        "v_mfma_f32_16x16x32_bf16 v[60:63], v[12:15], v[148:151], v[60:63]\n"
        "v_mfma_f32_16x16x32_bf16 v[64:67], v[152:155], v[12:15], v[64:67]\n"
        "v_mfma_f32_16x16x32_bf16 v[68:71], v[156:159], v[12:15], v[68:71]\n"
        "s_waitcnt lgkmcnt(1)\n"
        "ds_read_b128 v[12:15], v0 offset:8192\n"
        "v_mfma_f32_16x16x32_bf16 v[48:51], v[160:163], v[16:19], v[48:51]\n"
        "v_mfma_f32_16x16x32_bf16 v[52:55], v[164:167], v[16:19], v[52:55]\n"
        "v_mfma_f32_16x16x32_bf16 v[56:59], v[16:19], v[168:171], v[56:59]\n"
        "v_mfma_f32_16x16x32_bf16 v[60:63], v[16:19], v[172:175], v[60:63]\n"
        "v_mfma_f32_16x16x32_bf16 v[64:67], v[176:179], v[16:19], v[64:67]\n"
        "v_mfma_f32_16x16x32_bf16 v[68:71], v[180:183], v[16:19], v[68:71]\n"
        "s_waitcnt lgkmcnt(1)\n"
        "ds_read_b128 v[16:19], v0 offset:8704\n"
        "v_mfma_f32_16x16x32_bf16 v[48:51], v[184:187], v[8:11], v[48:51]\n"
        "s_waitcnt vmcnt(1)\n"
        "v_cmp_gt_i32 vcc, s70, v4\n"
        "v_mfma_f32_16x16x32_bf16 v[52:55], v[188:191], v[8:11], v[52:55]\n"
        "v_mfma_f32_16x16x32_bf16 v[56:59], v[8:11], v[192:195], v[56:59]\n"
        "s_and_b64 vcc, vcc, 1\n"
        "v_mfma_f32_16x16x32_bf16 v[60:63], v[8:11], v[196:199], v[60:63]\n"
        "s_cbranch_vccnz .Ls1_i12_%=\n"
        "v_mfma_f32_16x16x32_bf16 v[64:67], v[200:203], v[8:11], v[64:67]\n"
        "buffer_load_dwordx4 v1, s[40:43], s71 offen offset:0 sc1 lds\n"
        "v_mfma_f32_16x16x32_bf16 v[68:71], v[204:207], v[8:11], v[68:71]\n"
        "s_waitcnt lgkmcnt(1)\n"
        "v_mfma_f32_16x16x32_bf16 v[48:51], v[208:211], v[12:15], v[48:51]\n"
        "buffer_load_dwordx4 v1, s[40:43], s71 offen offset:1024 sc1 lds\n"
        "v_mfma_f32_16x16x32_bf16 v[52:55], v[212:215], v[12:15], v[52:55]\n"
        "v_mfma_f32_16x16x32_bf16 v[56:59], v[12:15], v[216:219], v[56:59]\n"
        "buffer_load_dwordx4 v1, s[40:43], s71 offen offset:2048 sc1 lds\n"
        "v_mfma_f32_16x16x32_bf16 v[60:63], v[12:15], v[220:223], v[60:63]\n"
        "v_mfma_f32_16x16x32_bf16 v[64:67], v[224:227], v[12:15], v[64:67]\n"
        "buffer_load_dwordx4 v1, s[40:43], s71 offen offset:3072 sc1 lds\n"
        "v_mfma_f32_16x16x32_bf16 v[68:71], v[228:231], v[12:15], v[68:71]\n"
        "s_waitcnt lgkmcnt(0)\n"
        "s_add_u32 m0, s78, s85\n"
        "s_add_u32 s69, s69, 1\n"
        "v_mfma_f32_16x16x32_bf16 v[48:51], v[232:235], v[16:19], v[48:51]\n"
        "buffer_load_dwordx4 v1, s[40:43], s81 offen offset:0 sc1 lds\n"
        "v_mfma_f32_16x16x32_bf16 v[52:55], v[236:239], v[16:19], v[52:55]\n"
        "v_mfma_f32_16x16x32_bf16 v[56:59], v[16:19], v[240:243], v[56:59]\n"
        "v_mfma_f32_16x16x32_bf16 v[60:63], v[16:19], v[244:247], v[60:63]\n"
        "v_mfma_f32_16x16x32_bf16 v[64:67], v[248:251], v[16:19], v[64:67]\n"
        "v_mfma_f32_16x16x32_bf16 v[68:71], v[252:255], v[16:19], v[68:71]\n"
        "v_cvt_pk_bf16_f32 v48, v48, v49\n"
        "v_cvt_pk_bf16_f32 v49, v50, v51\n"
        "v_cvt_pk_bf16_f32 v50, v52, v53\n"
        "v_cvt_pk_bf16_f32 v51, v54, v55\n"
        "s_add_u32 s66, s72, 524288\n"
        "buffer_store_dwordx4 v[48:51], v1, s[40:43], s66 offen offset:1024 sc1\n"
        "v_cvt_pk_bf16_f32 v74, v56, v57\n"
        "v_cvt_pk_bf16_f32 v75, v58, v59\n"
        "s_add_u32 s66, s72, 1048576\n"
        "buffer_store_dwordx4 v[72:75], v1, s[40:43], s66 offen offset:0 sc1\n"
        "v_cvt_pk_bf16_f32 v78, v60, v61\n"
        "v_cvt_pk_bf16_f32 v79, v62, v63\n"
        "buffer_store_dwordx4 v[76:79], v1, s[40:43], s66 offen offset:1024 sc1\n"
        "v_cvt_pk_bf16_f32 v64, v64, v65\n"
        "v_cvt_pk_bf16_f32 v65, v66, v67\n"
        "v_cvt_pk_bf16_f32 v66, v68, v69\n"
        "v_cvt_pk_bf16_f32 v67, v70, v71\n"
        "buffer_store_dwordx4 v[64:67], v1, s[40:43], s72 offen offset:1024 sc1\n"
        "v_mov_b32 v5, s74\n"
        "s_mov_b32 s84, s60\n"
        "s_waitcnt vmcnt(4)\n"
        "s_branch .Ls1_go1_%=\n"
        ".Ls1_i12_%=:\n"
        "v_mfma_f32_16x16x32_bf16 v[64:67], v[200:203], v[8:11], v[64:67]\n"
        "buffer_load_dword v4, v2, s[40:43], s86 offen sc1\n"
        "v_mfma_f32_16x16x32_bf16 v[68:71], v[204:207], v[8:11], v[68:71]\n"
        "s_waitcnt lgkmcnt(1)\n"
        "v_mfma_f32_16x16x32_bf16 v[48:51], v[208:211], v[12:15], v[48:51]\n"
        "v_mfma_f32_16x16x32_bf16 v[52:55], v[212:215], v[12:15], v[52:55]\n"
        "v_mfma_f32_16x16x32_bf16 v[56:59], v[12:15], v[216:219], v[56:59]\n"
        "v_mfma_f32_16x16x32_bf16 v[60:63], v[12:15], v[220:223], v[60:63]\n"
        "v_mfma_f32_16x16x32_bf16 v[64:67], v[224:227], v[12:15], v[64:67]\n"
        "v_mfma_f32_16x16x32_bf16 v[68:71], v[228:231], v[12:15], v[68:71]\n"
        "s_waitcnt lgkmcnt(0)\n"
        "v_mfma_f32_16x16x32_bf16 v[48:51], v[232:235], v[16:19], v[48:51]\n"
        "v_mfma_f32_16x16x32_bf16 v[52:55], v[236:239], v[16:19], v[52:55]\n"
        "v_mfma_f32_16x16x32_bf16 v[56:59], v[16:19], v[240:243], v[56:59]\n"
        "v_mfma_f32_16x16x32_bf16 v[60:63], v[16:19], v[244:247], v[60:63]\n"
        "v_mfma_f32_16x16x32_bf16 v[64:67], v[248:251], v[16:19], v[64:67]\n"
        "v_mfma_f32_16x16x32_bf16 v[68:71], v[252:255], v[16:19], v[68:71]\n"
        "v_cvt_pk_bf16_f32 v48, v48, v49\n"
        "v_cvt_pk_bf16_f32 v49, v50, v51\n"
        "v_cvt_pk_bf16_f32 v50, v52, v53\n"
        "v_cvt_pk_bf16_f32 v51, v54, v55\n"
        "s_add_u32 s66, s72, 524288\n"
        "buffer_store_dwordx4 v[48:51], v1, s[40:43], s66 offen offset:1024 sc1\n"
        "v_cvt_pk_bf16_f32 v74, v56, v57\n"
        "v_cvt_pk_bf16_f32 v75, v58, v59\n"
        "s_add_u32 s66, s72, 1048576\n"
        "buffer_store_dwordx4 v[72:75], v1, s[40:43], s66 offen offset:0 sc1\n"
        "v_cvt_pk_bf16_f32 v78, v60, v61\n"
        "v_cvt_pk_bf16_f32 v79, v62, v63\n"
        "buffer_store_dwordx4 v[76:79], v1, s[40:43], s66 offen offset:1024 sc1\n"
        "v_cvt_pk_bf16_f32 v64, v64, v65\n"
        "v_cvt_pk_bf16_f32 v65, v66, v67\n"
        "v_cvt_pk_bf16_f32 v66, v68, v69\n"
        "v_cvt_pk_bf16_f32 v67, v70, v71\n"
        "buffer_store_dwordx4 v[64:67], v1, s[40:43], s72 offen offset:1024 sc1\n"
        "s_waitcnt vmcnt(4)\n"
        "v_cmp_gt_i32 vcc, s70, v4\n"
        "s_nop 0\n"
        "s_and_b64 vcc, vcc, 1\n"
        "s_cbranch_vccnz .Ls1_late1_%=\n"
        "buffer_load_dwordx4 v1, s[40:43], s71 offen offset:0 sc1 lds\n"
        "buffer_load_dwordx4 v1, s[40:43], s71 offen offset:1024 sc1 lds\n"
        "buffer_load_dwordx4 v1, s[40:43], s71 offen offset:2048 sc1 lds\n"
        "buffer_load_dwordx4 v1, s[40:43], s71 offen offset:3072 sc1 lds\n"
        "s_add_u32 m0, s78, s85\n"
        "s_add_u32 s69, s69, 1\n"
        "buffer_load_dwordx4 v1, s[40:43], s81 offen offset:0 sc1 lds\n"
        "s_waitcnt vmcnt(5)\n"
        "v_mov_b32 v5, s74\n"
        "buffer_store_dword v5, v3, s[40:43], s73 offen sc1\n"
        "s_waitcnt vmcnt(1)\n"
        "s_branch .Ls1_go1_%=\n"
        ".Ls1_late1_%=:\n"
        "s_waitcnt vmcnt(0)\n"
        "v_mov_b32 v5, s74\n"
        "buffer_store_dword v5, v3, s[40:43], s73 offen sc1\n"
        "s_cmp_eq_u32 s69, 128\n"
        "s_cbranch_scc0 .Ls1_wait1_%=\n"
        "s_endpgm\n"
        ".Ls1_wait1_%=:\n"
        "s_mov_b32 s64, 0\n"
        ".Ls1_in1_top_%=:\n"
        "buffer_load_dword v4, v2, s[40:43], s86 offen sc1\n"
        "s_waitcnt vmcnt(0)\n"
        "v_cmp_gt_i32 vcc, s70, v4\n"
        "s_nop 0\n"
        "s_and_b64 vcc, vcc, 1\n"
        "s_cbranch_vccz .Ls1_in1_ok_%=\n"
        "s_add_u32 s64, s64, 1\n"
        "s_and_b32 s65, s64, 255\n"
        "s_cbranch_scc1 .Ls1_in1_go_%=\n"
        "s_mov_b32 s65, 49152\n"
        "s_cmp_lt_u32 s64, 1048576\n"
        "s_cbranch_scc1 .Ls1_in1_chk_%=\n"
        "v_mov_b32 v4, 1\n"
        "buffer_store_dword v4, off, s[40:43], s65 sc1\n"
        "s_waitcnt vmcnt(0)\n"
        ".Ls1_in1_chk_%=:\n"
        "buffer_load_dword v4, off, s[40:43], s65 sc1\n"
        "s_waitcnt vmcnt(0)\n"
        "v_readfirstlane_b32 s65, v4\n"
        "s_nop 0\n"
        "s_cmp_lg_u32 s65, 1\n"
        "s_cbranch_scc1 .Ls1_in1_go_%=\n"
        "s_endpgm\n"
        ".Ls1_in1_go_%=:\n"
        "s_branch .Ls1_in1_top_%=\n"
        ".Ls1_in1_ok_%=:\n"
        "buffer_load_dwordx4 v1, s[40:43], s71 offen offset:0 sc1 lds\n"
        "buffer_load_dwordx4 v1, s[40:43], s71 offen offset:1024 sc1 lds\n"
        "buffer_load_dwordx4 v1, s[40:43], s71 offen offset:2048 sc1 lds\n"
        "buffer_load_dwordx4 v1, s[40:43], s71 offen offset:3072 sc1 lds\n"
        "s_add_u32 m0, s78, s85\n"
        "s_add_u32 s69, s69, 1\n"
        "buffer_load_dwordx4 v1, s[40:43], s81 offen offset:0 sc1 lds\n"
        "s_waitcnt vmcnt(0)\n"
        ".Ls1_go1_%=:\n"
        "s_mov_b32 s72, 241172480\n"
        "s_mov_b32 s83, 241172480\n"
        "s_mov_b32 s73, 241172480\n"
        "v_add_u32 v0, s78, v6\n"
        "s_barrier\n"
        "ds_read_b128 v[8:11], v0 offset:0\n"
        "ds_read_b128 v[12:15], v0 offset:512\n"
        "ds_read_b128 v[16:19], v0 offset:1024\n"
        "s_waitcnt lgkmcnt(2)\n"
        "s_branch .Ls1_i0_%=\n"
        ".Ls1_end_%=:\n"
        :: "s"(p.ws), "s"(p.Wk), "s"(p.Wv), "s"(p.Wq), "s"(pair(w, delay)), "s"(pair(x4, pr)), TILE_ARGS(PAGE + spread(A_XN)),
           "v"(pair(lane == 0 ? flag(F_XN + 8 * h + w) : OOB, lane < 2 ? lane * 32768 : OOB)), "v"(pair(0, 0)), "v"(pair(0, 0)),
           "v"(pair(vo, 0)), "v"(pair(tr + (g & 1) * 32, tr + (1 - (g & 1)) * 32))
        : "v0", "v1", "v2", "v3", "v4", "v5", "v6", "v7", "v8", "v9", "v10", "v11", "v12", "v13", "v14", "v15", "v16", "v17", "v18", "v19", "v20", "v21", "v22", "v23", "v24", "v25", "v26", "v27", "v28", "v29", "v30", "v31", "v32", "v33", "v34", "v35", "v36", "v37", "v38", "v39", "v40", "v41", "v42", "v43", "v44", "v45", "v46", "v47", "v48", "v49", "v50", "v51", "v52", "v53", "v54", "v55", "v56", "v57", "v58", "v59", "v60", "v61", "v62", "v63", "v64", "v65", "v66", "v67", "v68", "v69", "v70", "v71", "v72", "v73", "v74", "v75", "v76", "v77", "v78", "v79", "v255", "a255", "s26", "s27", "s28", "s29", "s30", "s31", "s32", "s33", "s34", "s35", "s36", "s37", "s38", "s39", "s40", "s41", "s42", "s43", "s44", "s45", "s46", "s47", "s48", "s49", "s50", "s51", "s52", "s53", "s54", "s55", "s56", "s57", "s58", "s59", "s60", "s61", "s62", "s63", "s64", "s65", "s66", "s67", "s68", "s69", "s70", "s71", "s72", "s73", "s74", "s75", "s76", "s77", "s78", "s79", "s80", "s81", "s82", "s83", "s84", "s85", "s86", "s87", "s88", "s89", "s90", "s91", "s92", "s93", "s94", "s95", "s96", "s97", "s98", "s99", "vcc", "scc", "memory");
    __builtin_unreachable();
}
__device__ __forceinline__ void role_f1(const Params& p, int kap, int jj, int x4, int pr, int delay) {
    GEMM_HEAD;
    const unsigned u = jj * 4 + w;
    unsigned vo[3];
#pragma unroll
    for (int n = 0; n < 3; ++n) {
        const unsigned ps = (64 * n + lane) / 6;
        vo[n] = (8 * ((ps >> 2) & 3) + 4 * (ps >> 4) + (ps & 3)) * 9216 + (1152 * kap + 48 * u) * 2 + ((64 * n + lane) % 6) * 16 - 1024 * n;
    }
    const unsigned hb = 65 * PAGE + spread(B_H + kap * 73728);
    const unsigned out1 = hb + spread(u * 1024), out2 = hb + (6 + (u >> 3)) * 16384 + (u & 7) * 512, fout = flag(F_F1 + 24 * kap + u);
    asm volatile(
        "s_nop 4\n"
        "s_mov_b64 s[40:41], %0\n"
        "s_and_b32 s41, s41, 0xffff\n"
        "s_mov_b32 s42, 236978176\n"
        "s_mov_b32 s43, 0x20000\n"
        "s_mov_b64 s[48:49], %4\n"
        "s_mov_b64 s[56:57], %5\n"
        "s_mov_b64 s[58:59], %6\n"
        "s_mov_b64 s[60:61], %7\n"
        "s_mov_b64 s[62:63], %8\n"
        "v_mov_b64 v[0:1], %9\n"
        "v_mov_b64 v[2:3], %10\n"
        "v_mov_b64 v[4:5], %11\n"
        "v_mov_b64 v[6:7], %12\n"
        "v_mov_b64 v[8:9], %13\n"
        "v_mov_b64 v[10:11], %14\n"
        "s_mov_b32 s47, 0x20000\n"
        "s_mul_i32 s79, s48, 40960\n"
        ".Lf1_start_%=:\n"
        "s_sub_u32 s49, s49, 1\n"
        "s_cbranch_scc1 .Lf1_start2_%=\n"
        "s_sleep 16\n"
        "s_branch .Lf1_start_%=\n"
        ".Lf1_start2_%=:\n"
        "s_mov_b64 s[44:45], %1\n"
        "s_and_b32 s45, s45, 0xffff\n"
        "s_mov_b32 s46, 5308416\n"
        "s_add_u32 m0, s79, 0\n"
        "s_mov_b32 s65, 0\n"
        "buffer_load_dwordx4 v8, s[44:47], s65 offen lds\n"
        "buffer_load_dwordx4 v9, s[44:47], s65 offen offset:1024 lds\n"
        "buffer_load_dwordx4 v10, s[44:47], s65 offen offset:2048 lds\n"
        "s_add_u32 m0, s79, 3072\n"
        "s_mov_b32 s65, 294912\n"
        "buffer_load_dwordx4 v8, s[44:47], s65 offen lds\n"
        "buffer_load_dwordx4 v9, s[44:47], s65 offen offset:1024 lds\n"
        "buffer_load_dwordx4 v10, s[44:47], s65 offen offset:2048 lds\n"
        "s_waitcnt vmcnt(3)\n"
        "ds_read_b64_tr_b16 a[0:1], v11 offset:0\n"
        "ds_read_b64_tr_b16 a[2:3], v11 offset:1536\n"
        "ds_read_b64_tr_b16 a[8:9], v11 offset:32\n"
        "ds_read_b64_tr_b16 a[10:11], v11 offset:1568\n"
        "ds_read_b64_tr_b16 a[16:17], v11 offset:64\n"
        "ds_read_b64_tr_b16 a[18:19], v11 offset:1600\n"
        "s_waitcnt vmcnt(0)\n"
        "ds_read_b64_tr_b16 a[24:25], v11 offset:3072\n"
        "ds_read_b64_tr_b16 a[26:27], v11 offset:4608\n"
        "ds_read_b64_tr_b16 a[32:33], v11 offset:3104\n"
        "ds_read_b64_tr_b16 a[34:35], v11 offset:4640\n"
        "ds_read_b64_tr_b16 a[40:41], v11 offset:3136\n"
        "ds_read_b64_tr_b16 a[42:43], v11 offset:4672\n"
        "s_waitcnt lgkmcnt(6)\n"
        "s_add_u32 m0, s79, 0\n"
        "s_mov_b32 s65, 589824\n"
        "buffer_load_dwordx4 v8, s[44:47], s65 offen lds\n"
        "buffer_load_dwordx4 v9, s[44:47], s65 offen offset:1024 lds\n"
        "buffer_load_dwordx4 v10, s[44:47], s65 offen offset:2048 lds\n"
        "s_waitcnt vmcnt(0)\n"
        "ds_read_b64_tr_b16 a[48:49], v11 offset:0\n"
        "ds_read_b64_tr_b16 a[50:51], v11 offset:1536\n"
        "ds_read_b64_tr_b16 a[56:57], v11 offset:32\n"
        "ds_read_b64_tr_b16 a[58:59], v11 offset:1568\n"
        "ds_read_b64_tr_b16 a[64:65], v11 offset:64\n"
        "ds_read_b64_tr_b16 a[66:67], v11 offset:1600\n"
        "s_waitcnt lgkmcnt(6)\n"
        "s_add_u32 m0, s79, 3072\n"
        "s_mov_b32 s65, 884736\n"
        "buffer_load_dwordx4 v8, s[44:47], s65 offen lds\n"
        "buffer_load_dwordx4 v9, s[44:47], s65 offen offset:1024 lds\n"
        "buffer_load_dwordx4 v10, s[44:47], s65 offen offset:2048 lds\n"
        "s_waitcnt vmcnt(0)\n"
        "ds_read_b64_tr_b16 a[72:73], v11 offset:3072\n"
        "ds_read_b64_tr_b16 a[74:75], v11 offset:4608\n"
        "ds_read_b64_tr_b16 a[80:81], v11 offset:3104\n"
        "ds_read_b64_tr_b16 a[82:83], v11 offset:4640\n"
        "ds_read_b64_tr_b16 a[88:89], v11 offset:3136\n"
        "ds_read_b64_tr_b16 a[90:91], v11 offset:4672\n"
        "s_waitcnt lgkmcnt(6)\n"
        "s_add_u32 m0, s79, 0\n"
        "s_mov_b32 s65, 1179648\n"
        "buffer_load_dwordx4 v8, s[44:47], s65 offen lds\n"
        "buffer_load_dwordx4 v9, s[44:47], s65 offen offset:1024 lds\n"
        "buffer_load_dwordx4 v10, s[44:47], s65 offen offset:2048 lds\n"
        "s_waitcnt vmcnt(0)\n"
        "ds_read_b64_tr_b16 a[96:97], v11 offset:0\n"
        "ds_read_b64_tr_b16 a[98:99], v11 offset:1536\n"
        "ds_read_b64_tr_b16 a[104:105], v11 offset:32\n"
        "ds_read_b64_tr_b16 a[106:107], v11 offset:1568\n"
        "ds_read_b64_tr_b16 a[112:113], v11 offset:64\n"
        "ds_read_b64_tr_b16 a[114:115], v11 offset:1600\n"
        "s_waitcnt lgkmcnt(6)\n"
        "s_add_u32 m0, s79, 3072\n"
        "s_mov_b32 s65, 1474560\n"
        "buffer_load_dwordx4 v8, s[44:47], s65 offen lds\n"
        "buffer_load_dwordx4 v9, s[44:47], s65 offen offset:1024 lds\n"
        "buffer_load_dwordx4 v10, s[44:47], s65 offen offset:2048 lds\n"
        "s_waitcnt vmcnt(0)\n"
        "ds_read_b64_tr_b16 a[120:121], v11 offset:3072\n"
        "ds_read_b64_tr_b16 a[122:123], v11 offset:4608\n"
        "ds_read_b64_tr_b16 a[128:129], v11 offset:3104\n"
        "ds_read_b64_tr_b16 a[130:131], v11 offset:4640\n"
        "ds_read_b64_tr_b16 a[136:137], v11 offset:3136\n"
        "ds_read_b64_tr_b16 a[138:139], v11 offset:4672\n"
        "s_waitcnt lgkmcnt(6)\n"
        "s_add_u32 m0, s79, 0\n"
        "s_mov_b32 s65, 1769472\n"
        "buffer_load_dwordx4 v8, s[44:47], s65 offen lds\n"
        "buffer_load_dwordx4 v9, s[44:47], s65 offen offset:1024 lds\n"
        "buffer_load_dwordx4 v10, s[44:47], s65 offen offset:2048 lds\n"
        "s_waitcnt vmcnt(0)\n"
        "ds_read_b64_tr_b16 a[144:145], v11 offset:0\n"
        "ds_read_b64_tr_b16 a[146:147], v11 offset:1536\n"
        "ds_read_b64_tr_b16 a[152:153], v11 offset:32\n"
        "ds_read_b64_tr_b16 a[154:155], v11 offset:1568\n"
        "ds_read_b64_tr_b16 a[160:161], v11 offset:64\n"
        "ds_read_b64_tr_b16 a[162:163], v11 offset:1600\n"
        "s_waitcnt lgkmcnt(6)\n"
        "s_add_u32 m0, s79, 3072\n"
        "s_mov_b32 s65, 2064384\n"
        "buffer_load_dwordx4 v8, s[44:47], s65 offen lds\n"
        "buffer_load_dwordx4 v9, s[44:47], s65 offen offset:1024 lds\n"
        "buffer_load_dwordx4 v10, s[44:47], s65 offen offset:2048 lds\n"
        "s_waitcnt vmcnt(0)\n"
        "ds_read_b64_tr_b16 a[168:169], v11 offset:3072\n"
        "ds_read_b64_tr_b16 a[170:171], v11 offset:4608\n"
        "ds_read_b64_tr_b16 a[176:177], v11 offset:3104\n"
        "ds_read_b64_tr_b16 a[178:179], v11 offset:4640\n"
        "ds_read_b64_tr_b16 a[184:185], v11 offset:3136\n"
        "ds_read_b64_tr_b16 a[186:187], v11 offset:4672\n"
        "s_waitcnt lgkmcnt(6)\n"
        "s_add_u32 m0, s79, 0\n"
        "s_mov_b32 s65, 2359296\n"
        "buffer_load_dwordx4 v8, s[44:47], s65 offen lds\n"
        "buffer_load_dwordx4 v9, s[44:47], s65 offen offset:1024 lds\n"
        "buffer_load_dwordx4 v10, s[44:47], s65 offen offset:2048 lds\n"
        "s_waitcnt vmcnt(0)\n"
        "ds_read_b64_tr_b16 a[192:193], v11 offset:0\n"
        "ds_read_b64_tr_b16 a[194:195], v11 offset:1536\n"
        "ds_read_b64_tr_b16 a[200:201], v11 offset:32\n"
        "ds_read_b64_tr_b16 a[202:203], v11 offset:1568\n"
        "ds_read_b64_tr_b16 a[208:209], v11 offset:64\n"
        "ds_read_b64_tr_b16 a[210:211], v11 offset:1600\n"
        "s_waitcnt lgkmcnt(6)\n"
        "s_add_u32 m0, s79, 3072\n"
        "s_mov_b32 s65, 2654208\n"
        "buffer_load_dwordx4 v8, s[44:47], s65 offen lds\n"
        "buffer_load_dwordx4 v9, s[44:47], s65 offen offset:1024 lds\n"
        "buffer_load_dwordx4 v10, s[44:47], s65 offen offset:2048 lds\n"
        "s_waitcnt vmcnt(0)\n"
        "ds_read_b64_tr_b16 a[216:217], v11 offset:3072\n"
        "ds_read_b64_tr_b16 a[218:219], v11 offset:4608\n"
        "ds_read_b64_tr_b16 a[224:225], v11 offset:3104\n"
        "ds_read_b64_tr_b16 a[226:227], v11 offset:4640\n"
        "ds_read_b64_tr_b16 a[232:233], v11 offset:3136\n"
        "ds_read_b64_tr_b16 a[234:235], v11 offset:4672\n"
        "s_waitcnt lgkmcnt(6)\n"
        "s_add_u32 m0, s79, 0\n"
        "s_mov_b32 s65, 2949120\n"
        "buffer_load_dwordx4 v8, s[44:47], s65 offen lds\n"
        "buffer_load_dwordx4 v9, s[44:47], s65 offen offset:1024 lds\n"
        "buffer_load_dwordx4 v10, s[44:47], s65 offen offset:2048 lds\n"
        "s_waitcnt vmcnt(0)\n"
        "ds_read_b64_tr_b16 a[240:241], v11 offset:0\n"
        "ds_read_b64_tr_b16 a[242:243], v11 offset:1536\n"
        "ds_read_b64_tr_b16 a[248:249], v11 offset:32\n"
        "ds_read_b64_tr_b16 a[250:251], v11 offset:1568\n"
        "ds_read_b64_tr_b16 v[80:81], v11 offset:64\n"
        "ds_read_b64_tr_b16 v[82:83], v11 offset:1600\n"
        "s_waitcnt lgkmcnt(6)\n"
        "s_add_u32 m0, s79, 3072\n"
        "s_mov_b32 s65, 3244032\n"
        "buffer_load_dwordx4 v8, s[44:47], s65 offen lds\n"
        "buffer_load_dwordx4 v9, s[44:47], s65 offen offset:1024 lds\n"
        "buffer_load_dwordx4 v10, s[44:47], s65 offen offset:2048 lds\n"
        "s_waitcnt vmcnt(0)\n"
        "ds_read_b64_tr_b16 v[88:89], v11 offset:3072\n"
        "ds_read_b64_tr_b16 v[90:91], v11 offset:4608\n"
        "ds_read_b64_tr_b16 v[96:97], v11 offset:3104\n"
        "ds_read_b64_tr_b16 v[98:99], v11 offset:4640\n"
        "ds_read_b64_tr_b16 v[104:105], v11 offset:3136\n"
        "ds_read_b64_tr_b16 v[106:107], v11 offset:4672\n"
        "s_waitcnt lgkmcnt(6)\n"
        "s_add_u32 m0, s79, 0\n"
        "s_mov_b32 s65, 3538944\n"
        "buffer_load_dwordx4 v8, s[44:47], s65 offen lds\n"
        "buffer_load_dwordx4 v9, s[44:47], s65 offen offset:1024 lds\n"
        "buffer_load_dwordx4 v10, s[44:47], s65 offen offset:2048 lds\n"
        "s_waitcnt vmcnt(0)\n"
        "ds_read_b64_tr_b16 v[112:113], v11 offset:0\n"
        "ds_read_b64_tr_b16 v[114:115], v11 offset:1536\n"
        "ds_read_b64_tr_b16 v[120:121], v11 offset:32\n"
        "ds_read_b64_tr_b16 v[122:123], v11 offset:1568\n"
        "ds_read_b64_tr_b16 v[128:129], v11 offset:64\n"
        "ds_read_b64_tr_b16 v[130:131], v11 offset:1600\n"
        "s_waitcnt lgkmcnt(6)\n"
        "s_add_u32 m0, s79, 3072\n"
        "s_mov_b32 s65, 3833856\n"
        "buffer_load_dwordx4 v8, s[44:47], s65 offen lds\n"
        "buffer_load_dwordx4 v9, s[44:47], s65 offen offset:1024 lds\n"
        "buffer_load_dwordx4 v10, s[44:47], s65 offen offset:2048 lds\n"
        "s_waitcnt vmcnt(0)\n"
        "ds_read_b64_tr_b16 v[136:137], v11 offset:3072\n"
        "ds_read_b64_tr_b16 v[138:139], v11 offset:4608\n"
        "ds_read_b64_tr_b16 v[144:145], v11 offset:3104\n"
        "ds_read_b64_tr_b16 v[146:147], v11 offset:4640\n"
        "ds_read_b64_tr_b16 v[152:153], v11 offset:3136\n"
        "ds_read_b64_tr_b16 v[154:155], v11 offset:4672\n"
        "s_waitcnt lgkmcnt(6)\n"
        "s_add_u32 m0, s79, 0\n"
        "s_mov_b32 s65, 4128768\n"
        "buffer_load_dwordx4 v8, s[44:47], s65 offen lds\n"
        "buffer_load_dwordx4 v9, s[44:47], s65 offen offset:1024 lds\n"
        "buffer_load_dwordx4 v10, s[44:47], s65 offen offset:2048 lds\n"
        "s_waitcnt vmcnt(0)\n"
        "ds_read_b64_tr_b16 v[160:161], v11 offset:0\n"
        "ds_read_b64_tr_b16 v[162:163], v11 offset:1536\n"
        "ds_read_b64_tr_b16 v[168:169], v11 offset:32\n"
        "ds_read_b64_tr_b16 v[170:171], v11 offset:1568\n"
        "ds_read_b64_tr_b16 v[176:177], v11 offset:64\n"
        "ds_read_b64_tr_b16 v[178:179], v11 offset:1600\n"
        "s_waitcnt lgkmcnt(6)\n"
        "s_add_u32 m0, s79, 3072\n"
        "s_mov_b32 s65, 4423680\n"
        "buffer_load_dwordx4 v8, s[44:47], s65 offen lds\n"
        "buffer_load_dwordx4 v9, s[44:47], s65 offen offset:1024 lds\n"
        "buffer_load_dwordx4 v10, s[44:47], s65 offen offset:2048 lds\n"
        "s_waitcnt vmcnt(0)\n"
        "ds_read_b64_tr_b16 v[184:185], v11 offset:3072\n"
        "ds_read_b64_tr_b16 v[186:187], v11 offset:4608\n"
        "ds_read_b64_tr_b16 v[192:193], v11 offset:3104\n"
        "ds_read_b64_tr_b16 v[194:195], v11 offset:4640\n"
        "ds_read_b64_tr_b16 v[200:201], v11 offset:3136\n"
        "ds_read_b64_tr_b16 v[202:203], v11 offset:4672\n"
        "s_waitcnt lgkmcnt(6)\n"
        "s_add_u32 m0, s79, 0\n"
        "s_mov_b32 s65, 4718592\n"
        "buffer_load_dwordx4 v8, s[44:47], s65 offen lds\n"
        "buffer_load_dwordx4 v9, s[44:47], s65 offen offset:1024 lds\n"
        "buffer_load_dwordx4 v10, s[44:47], s65 offen offset:2048 lds\n"
        "s_waitcnt vmcnt(0)\n"
        "ds_read_b64_tr_b16 v[208:209], v11 offset:0\n"
        "ds_read_b64_tr_b16 v[210:211], v11 offset:1536\n"
        "ds_read_b64_tr_b16 v[216:217], v11 offset:32\n"
        "ds_read_b64_tr_b16 v[218:219], v11 offset:1568\n"
        "ds_read_b64_tr_b16 v[224:225], v11 offset:64\n"
        "ds_read_b64_tr_b16 v[226:227], v11 offset:1600\n"
        "s_waitcnt lgkmcnt(6)\n"
        "s_add_u32 m0, s79, 3072\n"
        "s_mov_b32 s65, 5013504\n"
        "buffer_load_dwordx4 v8, s[44:47], s65 offen lds\n"
        "buffer_load_dwordx4 v9, s[44:47], s65 offen offset:1024 lds\n"
        "buffer_load_dwordx4 v10, s[44:47], s65 offen offset:2048 lds\n"
        "s_waitcnt vmcnt(0)\n"
        "ds_read_b64_tr_b16 v[232:233], v11 offset:3072\n"
        "ds_read_b64_tr_b16 v[234:235], v11 offset:4608\n"
        "ds_read_b64_tr_b16 v[240:241], v11 offset:3104\n"
        "ds_read_b64_tr_b16 v[242:243], v11 offset:4640\n"
        "ds_read_b64_tr_b16 v[248:249], v11 offset:3136\n"
        "ds_read_b64_tr_b16 v[250:251], v11 offset:4672\n"
        "s_waitcnt lgkmcnt(6)\n"
        "s_mov_b64 s[44:45], %2\n"
        "s_and_b32 s45, s45, 0xffff\n"
        "s_mov_b32 s46, 5308416\n"
        "s_add_u32 m0, s79, 0\n"
        "s_mov_b32 s65, 0\n"
        "buffer_load_dwordx4 v8, s[44:47], s65 offen lds\n"
        "buffer_load_dwordx4 v9, s[44:47], s65 offen offset:1024 lds\n"
        "buffer_load_dwordx4 v10, s[44:47], s65 offen offset:2048 lds\n"
        "s_waitcnt vmcnt(0)\n"
        "ds_read_b64_tr_b16 a[4:5], v11 offset:0\n"
        "ds_read_b64_tr_b16 a[6:7], v11 offset:1536\n"
        "ds_read_b64_tr_b16 a[12:13], v11 offset:32\n"
        "ds_read_b64_tr_b16 a[14:15], v11 offset:1568\n"
        "ds_read_b64_tr_b16 a[20:21], v11 offset:64\n"
        "ds_read_b64_tr_b16 a[22:23], v11 offset:1600\n"
        "s_waitcnt lgkmcnt(6)\n"
        "s_add_u32 m0, s79, 3072\n"
        "s_mov_b32 s65, 294912\n"
        "buffer_load_dwordx4 v8, s[44:47], s65 offen lds\n"
        "buffer_load_dwordx4 v9, s[44:47], s65 offen offset:1024 lds\n"
        "buffer_load_dwordx4 v10, s[44:47], s65 offen offset:2048 lds\n"
        "s_waitcnt vmcnt(0)\n"
        "ds_read_b64_tr_b16 a[28:29], v11 offset:3072\n"
        "ds_read_b64_tr_b16 a[30:31], v11 offset:4608\n"
        "ds_read_b64_tr_b16 a[36:37], v11 offset:3104\n"
        "ds_read_b64_tr_b16 a[38:39], v11 offset:4640\n"
        "ds_read_b64_tr_b16 a[44:45], v11 offset:3136\n"
        "ds_read_b64_tr_b16 a[46:47], v11 offset:4672\n"
        "s_waitcnt lgkmcnt(6)\n"
        "s_add_u32 m0, s79, 0\n"
        "s_mov_b32 s65, 589824\n"
        "buffer_load_dwordx4 v8, s[44:47], s65 offen lds\n"
        "buffer_load_dwordx4 v9, s[44:47], s65 offen offset:1024 lds\n"
        "buffer_load_dwordx4 v10, s[44:47], s65 offen offset:2048 lds\n"
        "s_waitcnt vmcnt(0)\n"
        "ds_read_b64_tr_b16 a[52:53], v11 offset:0\n"
        "ds_read_b64_tr_b16 a[54:55], v11 offset:1536\n"
        "ds_read_b64_tr_b16 a[60:61], v11 offset:32\n"
        "ds_read_b64_tr_b16 a[62:63], v11 offset:1568\n"
        "ds_read_b64_tr_b16 a[68:69], v11 offset:64\n"
        "ds_read_b64_tr_b16 a[70:71], v11 offset:1600\n"
        "s_waitcnt lgkmcnt(6)\n"
        "s_add_u32 m0, s79, 3072\n"
        "s_mov_b32 s65, 884736\n"
        "buffer_load_dwordx4 v8, s[44:47], s65 offen lds\n"
        "buffer_load_dwordx4 v9, s[44:47], s65 offen offset:1024 lds\n"
        "buffer_load_dwordx4 v10, s[44:47], s65 offen offset:2048 lds\n"
        "s_waitcnt vmcnt(0)\n"
        "ds_read_b64_tr_b16 a[76:77], v11 offset:3072\n"
        "ds_read_b64_tr_b16 a[78:79], v11 offset:4608\n"
        "ds_read_b64_tr_b16 a[84:85], v11 offset:3104\n"
        "ds_read_b64_tr_b16 a[86:87], v11 offset:4640\n"
        "ds_read_b64_tr_b16 a[92:93], v11 offset:3136\n"
        "ds_read_b64_tr_b16 a[94:95], v11 offset:4672\n"
        "s_waitcnt lgkmcnt(6)\n"
        "s_add_u32 m0, s79, 0\n"
        "s_mov_b32 s65, 1179648\n"
        "buffer_load_dwordx4 v8, s[44:47], s65 offen lds\n"
        "buffer_load_dwordx4 v9, s[44:47], s65 offen offset:1024 lds\n"
        "buffer_load_dwordx4 v10, s[44:47], s65 offen offset:2048 lds\n"
        "s_waitcnt vmcnt(0)\n"
        "ds_read_b64_tr_b16 a[100:101], v11 offset:0\n"
        "ds_read_b64_tr_b16 a[102:103], v11 offset:1536\n"
        "ds_read_b64_tr_b16 a[108:109], v11 offset:32\n"
        "ds_read_b64_tr_b16 a[110:111], v11 offset:1568\n"
        "ds_read_b64_tr_b16 a[116:117], v11 offset:64\n"
        "ds_read_b64_tr_b16 a[118:119], v11 offset:1600\n"
        "s_waitcnt lgkmcnt(6)\n"
        "s_add_u32 m0, s79, 3072\n"
        "s_mov_b32 s65, 1474560\n"
        "buffer_load_dwordx4 v8, s[44:47], s65 offen lds\n"
        "buffer_load_dwordx4 v9, s[44:47], s65 offen offset:1024 lds\n"
        "buffer_load_dwordx4 v10, s[44:47], s65 offen offset:2048 lds\n"
        "s_waitcnt vmcnt(0)\n"
        "ds_read_b64_tr_b16 a[124:125], v11 offset:3072\n"
        "ds_read_b64_tr_b16 a[126:127], v11 offset:4608\n"
        "ds_read_b64_tr_b16 a[132:133], v11 offset:3104\n"
        "ds_read_b64_tr_b16 a[134:135], v11 offset:4640\n"
        "ds_read_b64_tr_b16 a[140:141], v11 offset:3136\n"
        "ds_read_b64_tr_b16 a[142:143], v11 offset:4672\n"
        "s_waitcnt lgkmcnt(6)\n"
        "s_add_u32 m0, s79, 0\n"
        "s_mov_b32 s65, 1769472\n"
        "buffer_load_dwordx4 v8, s[44:47], s65 offen lds\n"
        "buffer_load_dwordx4 v9, s[44:47], s65 offen offset:1024 lds\n"
        "buffer_load_dwordx4 v10, s[44:47], s65 offen offset:2048 lds\n"
        "s_waitcnt vmcnt(0)\n"
        "ds_read_b64_tr_b16 a[148:149], v11 offset:0\n"
        "ds_read_b64_tr_b16 a[150:151], v11 offset:1536\n"
        "ds_read_b64_tr_b16 a[156:157], v11 offset:32\n"
        "ds_read_b64_tr_b16 a[158:159], v11 offset:1568\n"
        "ds_read_b64_tr_b16 a[164:165], v11 offset:64\n"
        "ds_read_b64_tr_b16 a[166:167], v11 offset:1600\n"
        "s_waitcnt lgkmcnt(6)\n"
        "s_add_u32 m0, s79, 3072\n"
        "s_mov_b32 s65, 2064384\n"
        "buffer_load_dwordx4 v8, s[44:47], s65 offen lds\n"
        "buffer_load_dwordx4 v9, s[44:47], s65 offen offset:1024 lds\n"
        "buffer_load_dwordx4 v10, s[44:47], s65 offen offset:2048 lds\n"
        "s_waitcnt vmcnt(0)\n"
        "ds_read_b64_tr_b16 a[172:173], v11 offset:3072\n"
        "ds_read_b64_tr_b16 a[174:175], v11 offset:4608\n"
        "ds_read_b64_tr_b16 a[180:181], v11 offset:3104\n"
        "ds_read_b64_tr_b16 a[182:183], v11 offset:4640\n"
        "ds_read_b64_tr_b16 a[188:189], v11 offset:3136\n"
        "ds_read_b64_tr_b16 a[190:191], v11 offset:4672\n"
        "s_waitcnt lgkmcnt(6)\n"
        "s_add_u32 m0, s79, 0\n"
        "s_mov_b32 s65, 2359296\n"
        "buffer_load_dwordx4 v8, s[44:47], s65 offen lds\n"
        "buffer_load_dwordx4 v9, s[44:47], s65 offen offset:1024 lds\n"
        "buffer_load_dwordx4 v10, s[44:47], s65 offen offset:2048 lds\n"
        "s_waitcnt vmcnt(0)\n"
        "ds_read_b64_tr_b16 a[196:197], v11 offset:0\n"
        "ds_read_b64_tr_b16 a[198:199], v11 offset:1536\n"
        "ds_read_b64_tr_b16 a[204:205], v11 offset:32\n"
        "ds_read_b64_tr_b16 a[206:207], v11 offset:1568\n"
        "ds_read_b64_tr_b16 a[212:213], v11 offset:64\n"
        "ds_read_b64_tr_b16 a[214:215], v11 offset:1600\n"
        "s_waitcnt lgkmcnt(6)\n"
        "s_add_u32 m0, s79, 3072\n"
        "s_mov_b32 s65, 2654208\n"
        "buffer_load_dwordx4 v8, s[44:47], s65 offen lds\n"
        "buffer_load_dwordx4 v9, s[44:47], s65 offen offset:1024 lds\n"
        "buffer_load_dwordx4 v10, s[44:47], s65 offen offset:2048 lds\n"
        "s_waitcnt vmcnt(0)\n"
        "ds_read_b64_tr_b16 a[220:221], v11 offset:3072\n"
        "ds_read_b64_tr_b16 a[222:223], v11 offset:4608\n"
        "ds_read_b64_tr_b16 a[228:229], v11 offset:3104\n"
        "ds_read_b64_tr_b16 a[230:231], v11 offset:4640\n"
        "ds_read_b64_tr_b16 a[236:237], v11 offset:3136\n"
        "ds_read_b64_tr_b16 a[238:239], v11 offset:4672\n"
        "s_waitcnt lgkmcnt(6)\n"
        "s_add_u32 m0, s79, 0\n"
        "s_mov_b32 s65, 2949120\n"
        "buffer_load_dwordx4 v8, s[44:47], s65 offen lds\n"
        "buffer_load_dwordx4 v9, s[44:47], s65 offen offset:1024 lds\n"
        "buffer_load_dwordx4 v10, s[44:47], s65 offen offset:2048 lds\n"
        "s_waitcnt vmcnt(0)\n"
        "ds_read_b64_tr_b16 a[244:245], v11 offset:0\n"
        "ds_read_b64_tr_b16 a[246:247], v11 offset:1536\n"
        "ds_read_b64_tr_b16 a[252:253], v11 offset:32\n"
        "ds_read_b64_tr_b16 a[254:255], v11 offset:1568\n"
        "ds_read_b64_tr_b16 v[84:85], v11 offset:64\n"
        "ds_read_b64_tr_b16 v[86:87], v11 offset:1600\n"
        "s_waitcnt lgkmcnt(6)\n"
        "s_add_u32 m0, s79, 3072\n"
        "s_mov_b32 s65, 3244032\n"
        "buffer_load_dwordx4 v8, s[44:47], s65 offen lds\n"
        "buffer_load_dwordx4 v9, s[44:47], s65 offen offset:1024 lds\n"
        "buffer_load_dwordx4 v10, s[44:47], s65 offen offset:2048 lds\n"
        "s_waitcnt vmcnt(0)\n"
        "ds_read_b64_tr_b16 v[92:93], v11 offset:3072\n"
        "ds_read_b64_tr_b16 v[94:95], v11 offset:4608\n"
        "ds_read_b64_tr_b16 v[100:101], v11 offset:3104\n"
        "ds_read_b64_tr_b16 v[102:103], v11 offset:4640\n"
        "ds_read_b64_tr_b16 v[108:109], v11 offset:3136\n"
        "ds_read_b64_tr_b16 v[110:111], v11 offset:4672\n"
        "s_waitcnt lgkmcnt(6)\n"
        "s_add_u32 m0, s79, 0\n"
        "s_mov_b32 s65, 3538944\n"
        "buffer_load_dwordx4 v8, s[44:47], s65 offen lds\n"
        "buffer_load_dwordx4 v9, s[44:47], s65 offen offset:1024 lds\n"
        "buffer_load_dwordx4 v10, s[44:47], s65 offen offset:2048 lds\n"
        "s_waitcnt vmcnt(0)\n"
        "ds_read_b64_tr_b16 v[116:117], v11 offset:0\n"
        "ds_read_b64_tr_b16 v[118:119], v11 offset:1536\n"
        "ds_read_b64_tr_b16 v[124:125], v11 offset:32\n"
        "ds_read_b64_tr_b16 v[126:127], v11 offset:1568\n"
        "ds_read_b64_tr_b16 v[132:133], v11 offset:64\n"
        "ds_read_b64_tr_b16 v[134:135], v11 offset:1600\n"
        "s_waitcnt lgkmcnt(6)\n"
        "s_add_u32 m0, s79, 3072\n"
        "s_mov_b32 s65, 3833856\n"
        "buffer_load_dwordx4 v8, s[44:47], s65 offen lds\n"
        "buffer_load_dwordx4 v9, s[44:47], s65 offen offset:1024 lds\n"
        "buffer_load_dwordx4 v10, s[44:47], s65 offen offset:2048 lds\n"
        "s_waitcnt vmcnt(0)\n"
        "ds_read_b64_tr_b16 v[140:141], v11 offset:3072\n"
        "ds_read_b64_tr_b16 v[142:143], v11 offset:4608\n"
        "ds_read_b64_tr_b16 v[148:149], v11 offset:3104\n"
        "ds_read_b64_tr_b16 v[150:151], v11 offset:4640\n"
        "ds_read_b64_tr_b16 v[156:157], v11 offset:3136\n"
        "ds_read_b64_tr_b16 v[158:159], v11 offset:4672\n"
        "s_waitcnt lgkmcnt(6)\n"
        "s_add_u32 m0, s79, 0\n"
        "s_mov_b32 s65, 4128768\n"
        "buffer_load_dwordx4 v8, s[44:47], s65 offen lds\n"
        "buffer_load_dwordx4 v9, s[44:47], s65 offen offset:1024 lds\n"
        "buffer_load_dwordx4 v10, s[44:47], s65 offen offset:2048 lds\n"
        "s_waitcnt vmcnt(0)\n"
        "ds_read_b64_tr_b16 v[164:165], v11 offset:0\n"
        "ds_read_b64_tr_b16 v[166:167], v11 offset:1536\n"
        "ds_read_b64_tr_b16 v[172:173], v11 offset:32\n"
        "ds_read_b64_tr_b16 v[174:175], v11 offset:1568\n"
        "ds_read_b64_tr_b16 v[180:181], v11 offset:64\n"
        "ds_read_b64_tr_b16 v[182:183], v11 offset:1600\n"
        "s_waitcnt lgkmcnt(6)\n"
        "s_add_u32 m0, s79, 3072\n"
        "s_mov_b32 s65, 4423680\n"
        "buffer_load_dwordx4 v8, s[44:47], s65 offen lds\n"
        "buffer_load_dwordx4 v9, s[44:47], s65 offen offset:1024 lds\n"
        "buffer_load_dwordx4 v10, s[44:47], s65 offen offset:2048 lds\n"
        "s_waitcnt vmcnt(0)\n"
        "ds_read_b64_tr_b16 v[188:189], v11 offset:3072\n"
        "ds_read_b64_tr_b16 v[190:191], v11 offset:4608\n"
        "ds_read_b64_tr_b16 v[196:197], v11 offset:3104\n"
        "ds_read_b64_tr_b16 v[198:199], v11 offset:4640\n"
        "ds_read_b64_tr_b16 v[204:205], v11 offset:3136\n"
        "ds_read_b64_tr_b16 v[206:207], v11 offset:4672\n"
        "s_waitcnt lgkmcnt(6)\n"
        "s_add_u32 m0, s79, 0\n"
        "s_mov_b32 s65, 4718592\n"
        "buffer_load_dwordx4 v8, s[44:47], s65 offen lds\n"
        "buffer_load_dwordx4 v9, s[44:47], s65 offen offset:1024 lds\n"
        "buffer_load_dwordx4 v10, s[44:47], s65 offen offset:2048 lds\n"
        "s_waitcnt vmcnt(0)\n"
        "ds_read_b64_tr_b16 v[212:213], v11 offset:0\n"
        "ds_read_b64_tr_b16 v[214:215], v11 offset:1536\n"
        "ds_read_b64_tr_b16 v[220:221], v11 offset:32\n"
        "ds_read_b64_tr_b16 v[222:223], v11 offset:1568\n"
        "ds_read_b64_tr_b16 v[228:229], v11 offset:64\n"
        "ds_read_b64_tr_b16 v[230:231], v11 offset:1600\n"
        "s_waitcnt lgkmcnt(6)\n"
        "s_add_u32 m0, s79, 3072\n"
        "s_mov_b32 s65, 5013504\n"
        "buffer_load_dwordx4 v8, s[44:47], s65 offen lds\n"
        "buffer_load_dwordx4 v9, s[44:47], s65 offen offset:1024 lds\n"
        "buffer_load_dwordx4 v10, s[44:47], s65 offen offset:2048 lds\n"
        "s_waitcnt vmcnt(0)\n"
        "ds_read_b64_tr_b16 v[236:237], v11 offset:3072\n"
        "ds_read_b64_tr_b16 v[238:239], v11 offset:4608\n"
        "ds_read_b64_tr_b16 v[244:245], v11 offset:3104\n"
        "ds_read_b64_tr_b16 v[246:247], v11 offset:4640\n"
        "ds_read_b64_tr_b16 v[252:253], v11 offset:3136\n"
        "ds_read_b64_tr_b16 v[254:255], v11 offset:4672\n"
        "s_waitcnt lgkmcnt(0)\n"
        "s_getpc_b64 s[44:45]\n"
        ".Lf1_pc_%=:\n"
        "s_add_u32 s44, s44, .Lf1_code_%=-.Lf1_pc_%=\n"
        "s_addc_u32 s45, s45, 0\n"
        "s_and_b32 s45, s45, 0xffff\n"
        "s_and_b32 s44, s44, -4\n"
        "s_mov_b32 s46, .Lf1_end_%=-.Lf1_code_%=\n"
        "s_mov_b32 s47, 0x20000\n"
        "s_lshl_b32 s65, s48, 10\n"
        "buffer_load_dwordx4 v[24:27], v1, s[44:47], s65 offen\n"
        "s_add_u32 s65, s65, 4096\n"
        "buffer_load_dwordx4 v[28:31], v1, s[44:47], s65 offen\n"
        "s_add_u32 s65, s65, 4096\n"
        "buffer_load_dwordx4 v[32:35], v1, s[44:47], s65 offen\n"
        "s_add_u32 s65, s65, 4096\n"
        "buffer_load_dwordx4 v[36:39], v1, s[44:47], s65 offen\n"
        "s_add_u32 s65, s65, 4096\n"
        "buffer_load_dwordx4 v[40:43], v1, s[44:47], s65 offen\n"
        "s_add_u32 s65, s65, 4096\n"
        "v_lshrrev_b32 v20, 4, v1\n"
        "s_mov_b32 s50, s56\n"
        "v_lshlrev_b32 v22, 7, v20\n"
        "s_sub_u32 s66, 0, s50\n"
        "s_mov_b32 s52, 32768\n"
        "s_mov_b32 s64, 0\n"
        ".Lf1_pg0_top_%=:\n"
        "buffer_load_dword v21, v22, s[40:43], s52 offen sc1\n"
        "s_waitcnt vmcnt(0)\n"
        "v_cmp_gt_i32 vcc, 0, v21\n"
        "s_cbranch_vccz .Lf1_pg0_ok_%=\n"
        "s_add_u32 s64, s64, 1\n"
        "s_and_b32 s65, s64, 255\n"
        "s_cbranch_scc1 .Lf1_pg0_go_%=\n"
        "s_mov_b32 s65, 49152\n"
        "s_cmp_lt_u32 s64, 1048576\n"
        "s_cbranch_scc1 .Lf1_pg0_chk_%=\n"
        "v_mov_b32 v21, 1\n"
        "buffer_store_dword v21, off, s[40:43], s65 sc1\n"
        "s_waitcnt vmcnt(0)\n"
        ".Lf1_pg0_chk_%=:\n"
        "buffer_load_dword v21, off, s[40:43], s65 sc1\n"
        "s_waitcnt vmcnt(0)\n"
        "v_readfirstlane_b32 s65, v21\n"
        "s_nop 0\n"
        "s_cmp_lg_u32 s65, 1\n"
        "s_cbranch_scc1 .Lf1_pg0_go_%=\n"
        "s_endpgm\n"
        ".Lf1_pg0_go_%=:\n"
        "s_sleep 2\n"
        "s_branch .Lf1_pg0_top_%=\n"
        ".Lf1_pg0_ok_%=:\n"
        "v_cmp_eq_u32 s[52:53], 1, v21\n"
        "s_nop 1\n"
        "s_mov_b32 s54, 40960\n"
        "s_mov_b32 s64, 0\n"
        ".Lf1_pg1_top_%=:\n"
        "buffer_load_dword v21, v22, s[40:43], s54 offen sc1\n"
        "s_waitcnt vmcnt(0)\n"
        "v_cmp_gt_i32 vcc, 0, v21\n"
        "s_cbranch_vccz .Lf1_pg1_ok_%=\n"
        "s_add_u32 s64, s64, 1\n"
        "s_and_b32 s65, s64, 255\n"
        "s_cbranch_scc1 .Lf1_pg1_go_%=\n"
        "s_mov_b32 s65, 49152\n"
        "s_cmp_lt_u32 s64, 1048576\n"
        "s_cbranch_scc1 .Lf1_pg1_chk_%=\n"
        "v_mov_b32 v21, 1\n"
        "buffer_store_dword v21, off, s[40:43], s65 sc1\n"
        "s_waitcnt vmcnt(0)\n"
        ".Lf1_pg1_chk_%=:\n"
        "buffer_load_dword v21, off, s[40:43], s65 sc1\n"
        "s_waitcnt vmcnt(0)\n"
        "v_readfirstlane_b32 s65, v21\n"
        "s_nop 0\n"
        "s_cmp_lg_u32 s65, 1\n"
        "s_cbranch_scc1 .Lf1_pg1_go_%=\n"
        "s_endpgm\n"
        ".Lf1_pg1_go_%=:\n"
        "s_sleep 2\n"
        "s_branch .Lf1_pg1_top_%=\n"
        ".Lf1_pg1_ok_%=:\n"
        "v_cmp_eq_u32 s[54:55], 1, v21\n"
        "s_nop 1\n"
        "s_mov_b32 vcc_lo, 0x55555555\n"
        "s_mov_b32 vcc_hi, 0x55555555\n"
        "s_xor_b64 vcc, vcc, s[52:53]\n"
        "s_bcnt1_i32_b64 s65, vcc\n"
        "s_sub_u32 s64, s65, 23\n"
        "s_cmp_lt_u32 s64, 19\n"
        "s_cbranch_scc1 .Lf1_rawmap_%=\n"
        "s_cmp_lt_u32 s65, 32\n"
        "s_cselect_b32 s65, 0, -1\n"
        "s_xor_b32 s52, s65, 0x55555555\n"
        "s_xor_b32 s53, s65, 0x55555555\n"
        "s_xor_b32 s54, s65, 0x55555555\n"
        "s_xor_b32 s55, s65, 0xaaaaaaaa\n"
        ".Lf1_rawmap_%=:\n"
        "s_xor_b32 s52, s52, s66\n"
        "s_xor_b32 s53, s53, s66\n"
        "s_xor_b32 s54, s54, s66\n"
        "s_xor_b32 s55, s55, s66\n"
        "s_and_b32 s76, s55, 1\n"
        "s_lshl_b32 s76, s76, 13\n"
        "s_add_u32 s60, s60, s76\n"
        "s_barrier\n"
        "s_mov_b32 s68, -1\n"
        "s_mov_b32 s69, 0\n"
        "s_mov_b32 s78, 0\n"
        "s_mov_b32 s74, -1\n"
        "s_mov_b32 s86, s76\n"
        "s_lshr_b32 s85, s61, 16\n"
        "s_and_b32 s61, s61, 0xffff\n"
        "s_mov_b32 s89, 241172480\n"
        "s_mov_b32 s84, 241172480\n"
        "v_mov_b32 v6, v0\n"
        "s_mov_b32 s80, 0xbfb8aa3b\n"
        "s_lshr_b32 s67, s69, 1\n"
        "s_lshr_b32 s65, s67, 1\n"
        "s_bitcmp1_b32 s54, s65\n"
        "s_cselect_b32 s65, 8192, 0\n"
        "s_lshl_b32 s77, s67, 20\n"
        "s_add_u32 s77, s77, s65\n"
        "s_add_u32 s71, s77, s58\n"
        "s_add_u32 s81, s77, s62\n"
        "s_add_u32 s78, s78, 18432\n"
        "s_cmp_eq_u32 s78, 55296\n"
        "s_cselect_b32 s78, 0, s78\n"
        "s_add_u32 m0, s78, s61\n"
        "s_lshr_b32 s70, s69, 1\n"
        "s_branch .Lf1_wait1_%=\n"
        ".Lf1_code_%=:\n"
        ".Lf1_i0_%=:\n"
        "v_mfma_f32_16x16x32_bf16 v[24:27], a[0:3], v[8:11], 0\n"
        "s_add_u32 s86, s76, 512\n"
        "buffer_load_dword v4, v2, s[40:43], s86 offen sc1\n"
        "v_mfma_f32_16x16x32_bf16 v[28:31], a[4:7], v[8:11], 0\n"
        "s_add_u32 s71, s77, s58\n"
        "v_mul_f32 v72, s80, v48\n"
        "v_mul_f32 v73, s80, v49\n"
        "v_mfma_f32_16x16x32_bf16 v[32:35], a[8:11], v[8:11], 0\n"
        "s_add_u32 s71, s71, 32768\n"
        "v_mul_f32 v74, s80, v50\n"
        "v_mul_f32 v75, s80, v51\n"
        "v_mfma_f32_16x16x32_bf16 v[36:39], a[12:15], v[8:11], 0\n"
        "s_add_u32 s81, s77, s62\n"
        "v_exp_f32 v72, v72\n"
        "v_mfma_f32_16x16x32_bf16 v[40:43], a[16:19], v[8:11], 0\n"
        "s_add_u32 s81, s81, 2048\n"
        "v_exp_f32 v73, v73\n"
        "v_mfma_f32_16x16x32_bf16 v[44:47], a[20:23], v[8:11], 0\n"
        "s_waitcnt lgkmcnt(1)\n"
        "ds_read_b128 v[8:11], v0 offset:1536\n"
        "v_mul_f32 v76, s80, v56\n"
        "v_mfma_f32_16x16x32_bf16 v[24:27], a[24:27], v[12:15], v[24:27]\n"
        "s_add_u32 s78, s78, 18432\n"
        "v_exp_f32 v74, v74\n"
        "v_mfma_f32_16x16x32_bf16 v[28:31], a[28:31], v[12:15], v[28:31]\n"
        "s_cmp_eq_u32 s78, 55296\n"
        "s_cselect_b32 s78, 0, s78\n"
        "v_mul_f32 v77, s80, v57\n"
        "v_mfma_f32_16x16x32_bf16 v[32:35], a[32:35], v[12:15], v[32:35]\n"
        "s_add_u32 m0, s78, s61\n"
        "v_exp_f32 v75, v75\n"
        "v_mfma_f32_16x16x32_bf16 v[36:39], a[36:39], v[12:15], v[36:39]\n"
        "s_lshr_b32 s70, s69, 1\n"
        "v_mul_f32 v48, v48, v52\n"
        "v_mul_f32 v49, v49, v53\n"
        "v_mfma_f32_16x16x32_bf16 v[40:43], a[40:43], v[12:15], v[40:43]\n"
        "v_mul_f32 v50, v50, v54\n"
        "v_mul_f32 v51, v51, v55\n"
        "v_mfma_f32_16x16x32_bf16 v[44:47], a[44:47], v[12:15], v[44:47]\n"
        "s_waitcnt lgkmcnt(1)\n"
        "ds_read_b128 v[12:15], v0 offset:2048\n"
        "v_add_f32 v72, 1.0, v72\n"
        "v_mfma_f32_16x16x32_bf16 v[24:27], a[48:51], v[16:19], v[24:27]\n"
        "v_add_f32 v73, 1.0, v73\n"
        "v_add_f32 v74, 1.0, v74\n"
        "v_mfma_f32_16x16x32_bf16 v[28:31], a[52:55], v[16:19], v[28:31]\n"
        "v_add_f32 v75, 1.0, v75\n"
        "v_mul_f32 v78, s80, v58\n"
        "v_mfma_f32_16x16x32_bf16 v[32:35], a[56:59], v[16:19], v[32:35]\n"
        "s_waitcnt vmcnt(3)\n"
        "v_rcp_f32 v72, v72\n"
        "v_mfma_f32_16x16x32_bf16 v[36:39], a[60:63], v[16:19], v[36:39]\n"
        "buffer_store_dword v5, v3, s[40:43], s84 offen sc1\n"
        "v_mfma_f32_16x16x32_bf16 v[40:43], a[64:67], v[16:19], v[40:43]\n"
        "v_rcp_f32 v73, v73\n"
        "v_mfma_f32_16x16x32_bf16 v[44:47], a[68:71], v[16:19], v[44:47]\n"
        "s_waitcnt lgkmcnt(1)\n"
        "ds_read_b128 v[16:19], v0 offset:2560\n"
        "v_mul_f32 v79, s80, v59\n"
        "v_mfma_f32_16x16x32_bf16 v[24:27], a[72:75], v[8:11], v[24:27]\n"
        "v_rcp_f32 v74, v74\n"
        "v_mfma_f32_16x16x32_bf16 v[28:31], a[76:79], v[8:11], v[28:31]\n"
        "v_rcp_f32 v75, v75\n"
        "v_mfma_f32_16x16x32_bf16 v[32:35], a[80:83], v[8:11], v[32:35]\n"
        "v_mul_f32 v48, v48, v72\n"
        "v_mul_f32 v49, v49, v73\n"
        "v_mfma_f32_16x16x32_bf16 v[36:39], a[84:87], v[8:11], v[36:39]\n"
        "v_mul_f32 v50, v50, v74\n"
        "v_mul_f32 v51, v51, v75\n"
        "v_mfma_f32_16x16x32_bf16 v[40:43], a[88:91], v[8:11], v[40:43]\n"
        "v_cvt_pk_bf16_f32 v52, v48, v49\n"
        "v_cvt_pk_bf16_f32 v53, v50, v51\n"
        "v_mfma_f32_16x16x32_bf16 v[44:47], a[92:95], v[8:11], v[44:47]\n"
        "s_waitcnt lgkmcnt(1)\n"
        "ds_read_b128 v[8:11], v0 offset:3072\n"
        "v_mul_f32 v72, s80, v64\n"
        "v_mfma_f32_16x16x32_bf16 v[24:27], a[96:99], v[12:15], v[24:27]\n"
        "v_exp_f32 v76, v76\n"
        "v_mfma_f32_16x16x32_bf16 v[28:31], a[100:103], v[12:15], v[28:31]\n"
        "v_exp_f32 v77, v77\n"
        "v_mfma_f32_16x16x32_bf16 v[32:35], a[104:107], v[12:15], v[32:35]\n"
        "v_exp_f32 v78, v78\n"
        "v_mfma_f32_16x16x32_bf16 v[36:39], a[108:111], v[12:15], v[36:39]\n"
        "v_exp_f32 v79, v79\n"
        "v_mfma_f32_16x16x32_bf16 v[40:43], a[112:115], v[12:15], v[40:43]\n"
        "s_waitcnt vmcnt(1)\n"
        "v_cmp_gt_i32 vcc, s70, v4\n"
        "v_mul_f32 v56, v56, v60\n"
        "v_mfma_f32_16x16x32_bf16 v[44:47], a[116:119], v[12:15], v[44:47]\n"
        "s_waitcnt lgkmcnt(1)\n"
        "ds_read_b128 v[12:15], v0 offset:3584\n"
        "v_mul_f32 v57, v57, v61\n"
        "v_mfma_f32_16x16x32_bf16 v[24:27], a[120:123], v[16:19], v[24:27]\n"
        "s_and_b64 vcc, vcc, 1\n"
        "v_mul_f32 v58, v58, v62\n"
        "v_mfma_f32_16x16x32_bf16 v[28:31], a[124:127], v[16:19], v[28:31]\n"
        "s_cbranch_vccnz .Lf1_i00_%=\n"
        "v_mfma_f32_16x16x32_bf16 v[32:35], a[128:131], v[16:19], v[32:35]\n"
        "buffer_load_dwordx4 v1, s[40:43], s71 offen offset:0 sc1 lds\n"
        "v_mfma_f32_16x16x32_bf16 v[36:39], a[132:135], v[16:19], v[36:39]\n"
        "v_mul_f32 v59, v59, v63\n"
        "v_add_f32 v76, 1.0, v76\n"
        "v_mfma_f32_16x16x32_bf16 v[40:43], a[136:139], v[16:19], v[40:43]\n"
        "v_add_f32 v77, 1.0, v77\n"
        "v_add_f32 v78, 1.0, v78\n"
        "v_mfma_f32_16x16x32_bf16 v[44:47], a[140:143], v[16:19], v[44:47]\n"
        "s_waitcnt lgkmcnt(1)\n"
        "ds_read_b128 v[16:19], v0 offset:4096\n"
        "v_add_f32 v79, 1.0, v79\n"
        "v_mfma_f32_16x16x32_bf16 v[24:27], a[144:147], v[8:11], v[24:27]\n"
        "buffer_load_dwordx4 v1, s[40:43], s71 offen offset:1024 sc1 lds\n"
        "v_mfma_f32_16x16x32_bf16 v[28:31], a[148:151], v[8:11], v[28:31]\n"
        "v_rcp_f32 v76, v76\n"
        "v_mfma_f32_16x16x32_bf16 v[32:35], a[152:155], v[8:11], v[32:35]\n"
        "v_rcp_f32 v77, v77\n"
        "v_mfma_f32_16x16x32_bf16 v[36:39], a[156:159], v[8:11], v[36:39]\n"
        "v_rcp_f32 v78, v78\n"
        "v_mfma_f32_16x16x32_bf16 v[40:43], a[160:163], v[8:11], v[40:43]\n"
        "buffer_load_dwordx4 v1, s[40:43], s71 offen offset:2048 sc1 lds\n"
        "v_mfma_f32_16x16x32_bf16 v[44:47], a[164:167], v[8:11], v[44:47]\n"
        "s_waitcnt lgkmcnt(1)\n"
        "ds_read_b128 v[8:11], v0 offset:4608\n"
        "v_mul_f32 v73, s80, v65\n"
        "v_mfma_f32_16x16x32_bf16 v[24:27], a[168:171], v[12:15], v[24:27]\n"
        "v_rcp_f32 v79, v79\n"
        "v_mfma_f32_16x16x32_bf16 v[28:31], a[172:175], v[12:15], v[28:31]\n"
        "v_mul_f32 v56, v56, v76\n"
        "v_mul_f32 v57, v57, v77\n"
        "v_mfma_f32_16x16x32_bf16 v[32:35], a[176:179], v[12:15], v[32:35]\n"
        "buffer_load_dwordx4 v1, s[40:43], s71 offen offset:3072 sc1 lds\n"
        "v_mfma_f32_16x16x32_bf16 v[36:39], a[180:183], v[12:15], v[36:39]\n"
        "s_add_u32 m0, s78, s85\n"
        "s_add_u32 s69, s69, 1\n"
        "v_mul_f32 v58, v58, v78\n"
        "v_mfma_f32_16x16x32_bf16 v[40:43], a[184:187], v[12:15], v[40:43]\n"
        "v_mul_f32 v59, v59, v79\n"
        "v_cvt_pk_bf16_f32 v54, v56, v57\n"
        "v_mfma_f32_16x16x32_bf16 v[44:47], a[188:191], v[12:15], v[44:47]\n"
        "s_waitcnt lgkmcnt(1)\n"
        "ds_read_b128 v[12:15], v0 offset:5120\n"
        "v_cvt_pk_bf16_f32 v55, v58, v59\n"
        "v_mfma_f32_16x16x32_bf16 v[24:27], a[192:195], v[16:19], v[24:27]\n"
        "buffer_load_dwordx4 v1, s[40:43], s81 offen offset:0 sc1 lds\n"
        "v_mfma_f32_16x16x32_bf16 v[28:31], a[196:199], v[16:19], v[28:31]\n"
        "buffer_store_dwordx4 v[52:55], v1, s[40:43], s72 offen offset:0 sc1\n"
        "v_mfma_f32_16x16x32_bf16 v[32:35], a[200:203], v[16:19], v[32:35]\n"
        "v_mul_f32 v74, s80, v66\n"
        "v_mul_f32 v75, s80, v67\n"
        "v_mfma_f32_16x16x32_bf16 v[36:39], a[204:207], v[16:19], v[36:39]\n"
        "v_exp_f32 v72, v72\n"
        "v_mfma_f32_16x16x32_bf16 v[40:43], a[208:211], v[16:19], v[40:43]\n"
        "v_exp_f32 v73, v73\n"
        "v_mfma_f32_16x16x32_bf16 v[44:47], a[212:215], v[16:19], v[44:47]\n"
        "s_waitcnt lgkmcnt(1)\n"
        "ds_read_b128 v[16:19], v0 offset:5632\n"
        "v_mfma_f32_16x16x32_bf16 v[24:27], a[216:219], v[8:11], v[24:27]\n"
        "v_exp_f32 v74, v74\n"
        "v_mfma_f32_16x16x32_bf16 v[28:31], a[220:223], v[8:11], v[28:31]\n"
        "v_exp_f32 v75, v75\n"
        "v_mfma_f32_16x16x32_bf16 v[32:35], a[224:227], v[8:11], v[32:35]\n"
        "v_mul_f32 v64, v64, v68\n"
        "v_mul_f32 v65, v65, v69\n"
        "v_mfma_f32_16x16x32_bf16 v[36:39], a[228:231], v[8:11], v[36:39]\n"
        "v_mul_f32 v66, v66, v70\n"
        "v_mul_f32 v67, v67, v71\n"
        "v_mfma_f32_16x16x32_bf16 v[40:43], a[232:235], v[8:11], v[40:43]\n"
        "v_add_f32 v72, 1.0, v72\n"
        "v_add_f32 v73, 1.0, v73\n"
        "v_mfma_f32_16x16x32_bf16 v[44:47], a[236:239], v[8:11], v[44:47]\n"
        "s_waitcnt lgkmcnt(1)\n"
        "ds_read_b128 v[8:11], v0 offset:6144\n"
        "v_add_f32 v74, 1.0, v74\n"
        "v_mfma_f32_16x16x32_bf16 v[24:27], a[240:243], v[12:15], v[24:27]\n"
        "v_add_f32 v75, 1.0, v75\n"
        "v_mfma_f32_16x16x32_bf16 v[28:31], a[244:247], v[12:15], v[28:31]\n"
        "v_rcp_f32 v72, v72\n"
        "v_mfma_f32_16x16x32_bf16 v[32:35], a[248:251], v[12:15], v[32:35]\n"
        "v_rcp_f32 v73, v73\n"
        "v_mfma_f32_16x16x32_bf16 v[36:39], a[252:255], v[12:15], v[36:39]\n"
        "v_rcp_f32 v74, v74\n"
        "v_mfma_f32_16x16x32_bf16 v[40:43], v[80:83], v[12:15], v[40:43]\n"
        "v_rcp_f32 v75, v75\n"
        "v_mfma_f32_16x16x32_bf16 v[44:47], v[84:87], v[12:15], v[44:47]\n"
        "s_waitcnt lgkmcnt(1)\n"
        "ds_read_b128 v[12:15], v0 offset:6656\n"
        "v_mul_f32 v64, v64, v72\n"
        "v_mfma_f32_16x16x32_bf16 v[24:27], v[88:91], v[16:19], v[24:27]\n"
        "v_mul_f32 v65, v65, v73\n"
        "v_mul_f32 v66, v66, v74\n"
        "v_mfma_f32_16x16x32_bf16 v[28:31], v[92:95], v[16:19], v[28:31]\n"
        "v_mul_f32 v67, v67, v75\n"
        "v_cvt_pk_bf16_f32 v68, v64, v65\n"
        "v_mfma_f32_16x16x32_bf16 v[32:35], v[96:99], v[16:19], v[32:35]\n"
        "v_cvt_pk_bf16_f32 v69, v66, v67\n"
        "s_nop 0\n"
        "v_mfma_f32_16x16x32_bf16 v[36:39], v[100:103], v[16:19], v[36:39]\n"
        "v_permlane16_swap_b32 v68, v70\n"
        "v_mfma_f32_16x16x32_bf16 v[40:43], v[104:107], v[16:19], v[40:43]\n"
        "v_permlane16_swap_b32 v69, v71\n"
        "v_mfma_f32_16x16x32_bf16 v[44:47], v[108:111], v[16:19], v[44:47]\n"
        "s_waitcnt lgkmcnt(1)\n"
        "ds_read_b128 v[16:19], v0 offset:7168\n"
        "v_mfma_f32_16x16x32_bf16 v[24:27], v[112:115], v[8:11], v[24:27]\n"
        "buffer_store_dwordx4 v[68:71], v7, s[40:43], s83 offen offset:0 sc1\n"
        "v_mfma_f32_16x16x32_bf16 v[28:31], v[116:119], v[8:11], v[28:31]\n"
        "v_mov_b32 v5, s74\n"
        "v_mfma_f32_16x16x32_bf16 v[32:35], v[120:123], v[8:11], v[32:35]\n"
        "s_add_u32 s74, s74, 1\n"
        "v_mfma_f32_16x16x32_bf16 v[36:39], v[124:127], v[8:11], v[36:39]\n"
        "s_add_u32 s68, s68, 1\n"
        "v_mfma_f32_16x16x32_bf16 v[40:43], v[128:131], v[8:11], v[40:43]\n"
        "s_lshr_b32 s66, s68, 1\n"
        "v_mfma_f32_16x16x32_bf16 v[44:47], v[132:135], v[8:11], v[44:47]\n"
        "s_waitcnt lgkmcnt(1)\n"
        "ds_read_b128 v[8:11], v0 offset:7680\n"
        "v_mfma_f32_16x16x32_bf16 v[24:27], v[136:139], v[12:15], v[24:27]\n"
        "s_bitcmp1_b32 s54, s66\n"
        "s_cselect_b32 s66, 8192, 0\n"
        "v_mfma_f32_16x16x32_bf16 v[28:31], v[140:143], v[12:15], v[28:31]\n"
        "s_lshl_b32 s75, s68, 20\n"
        "v_mfma_f32_16x16x32_bf16 v[32:35], v[144:147], v[12:15], v[32:35]\n"
        "s_add_u32 s75, s75, s66\n"
        "v_mfma_f32_16x16x32_bf16 v[36:39], v[148:151], v[12:15], v[36:39]\n"
        "s_add_u32 s72, s75, s59\n"
        "v_mfma_f32_16x16x32_bf16 v[40:43], v[152:155], v[12:15], v[40:43]\n"
        "s_add_u32 s83, s75, s63\n"
        "v_mfma_f32_16x16x32_bf16 v[44:47], v[156:159], v[12:15], v[44:47]\n"
        "s_waitcnt lgkmcnt(1)\n"
        "ds_read_b128 v[12:15], v0 offset:8192\n"
        "v_mfma_f32_16x16x32_bf16 v[24:27], v[160:163], v[16:19], v[24:27]\n"
        "v_mfma_f32_16x16x32_bf16 v[28:31], v[164:167], v[16:19], v[28:31]\n"
        "v_mfma_f32_16x16x32_bf16 v[32:35], v[168:171], v[16:19], v[32:35]\n"
        "v_mfma_f32_16x16x32_bf16 v[36:39], v[172:175], v[16:19], v[36:39]\n"
        "v_mfma_f32_16x16x32_bf16 v[40:43], v[176:179], v[16:19], v[40:43]\n"
        "v_mfma_f32_16x16x32_bf16 v[44:47], v[180:183], v[16:19], v[44:47]\n"
        "s_waitcnt lgkmcnt(1)\n"
        "ds_read_b128 v[16:19], v0 offset:8704\n"
        "v_mfma_f32_16x16x32_bf16 v[24:27], v[184:187], v[8:11], v[24:27]\n"
        "v_mfma_f32_16x16x32_bf16 v[28:31], v[188:191], v[8:11], v[28:31]\n"
        "v_mfma_f32_16x16x32_bf16 v[32:35], v[192:195], v[8:11], v[32:35]\n"
        "v_mfma_f32_16x16x32_bf16 v[36:39], v[196:199], v[8:11], v[36:39]\n"
        "v_mfma_f32_16x16x32_bf16 v[40:43], v[200:203], v[8:11], v[40:43]\n"
        "s_waitcnt vmcnt(2)\n"
        "s_barrier\n"
        "v_add_u32 v0, s78, v6\n"
        "v_mfma_f32_16x16x32_bf16 v[44:47], v[204:207], v[8:11], v[44:47]\n"
        "s_waitcnt lgkmcnt(1)\n"
        "ds_read_b128 v[8:11], v0 offset:0\n"
        "v_mfma_f32_16x16x32_bf16 v[24:27], v[208:211], v[12:15], v[24:27]\n"
        "s_getreg_b32 s88, hwreg(HW_REG_IB_STS, 0, 4)\n"
        "v_mfma_f32_16x16x32_bf16 v[28:31], v[212:215], v[12:15], v[28:31]\n"
        "s_cmp_le_u32 s88, 0\n"
        "s_cselect_b32 s88, s73, s89\n"
        "s_cselect_b32 s73, s89, s73\n"
        "v_mfma_f32_16x16x32_bf16 v[32:35], v[216:219], v[12:15], v[32:35]\n"
        "buffer_store_dword v5, v3, s[40:43], s88 offen sc1\n"
        "v_mfma_f32_16x16x32_bf16 v[36:39], v[220:223], v[12:15], v[36:39]\n"
        "v_mfma_f32_16x16x32_bf16 v[40:43], v[224:227], v[12:15], v[40:43]\n"
        "v_mfma_f32_16x16x32_bf16 v[44:47], v[228:231], v[12:15], v[44:47]\n"
        "s_waitcnt lgkmcnt(1)\n"
        "ds_read_b128 v[12:15], v0 offset:512\n"
        "v_mfma_f32_16x16x32_bf16 v[24:27], v[232:235], v[16:19], v[24:27]\n"
        "v_mfma_f32_16x16x32_bf16 v[28:31], v[236:239], v[16:19], v[28:31]\n"
        "v_mfma_f32_16x16x32_bf16 v[32:35], v[240:243], v[16:19], v[32:35]\n"
        "s_getreg_b32 s88, hwreg(HW_REG_IB_STS, 0, 4)\n"
        "v_mfma_f32_16x16x32_bf16 v[36:39], v[244:247], v[16:19], v[36:39]\n"
        "s_cmp_le_u32 s88, 1\n"
        "s_cselect_b32 s88, s73, s89\n"
        "s_cselect_b32 s73, s89, s73\n"
        "v_mfma_f32_16x16x32_bf16 v[40:43], v[248:251], v[16:19], v[40:43]\n"
        "buffer_store_dword v5, v3, s[40:43], s88 offen sc1\n"
        "v_mfma_f32_16x16x32_bf16 v[44:47], v[252:255], v[16:19], v[44:47]\n"
        "s_waitcnt lgkmcnt(1)\n"
        "ds_read_b128 v[16:19], v0 offset:1024\n"
        "s_mov_b32 s84, s73\n"
        "s_mov_b32 s73, s60\n"
        "s_branch .Lf1_i1_%=\n"
        ".Lf1_i00_%=:\n"
        "v_mfma_f32_16x16x32_bf16 v[32:35], a[128:131], v[16:19], v[32:35]\n"
        "buffer_load_dword v4, v2, s[40:43], s86 offen sc1\n"
        "v_mfma_f32_16x16x32_bf16 v[36:39], a[132:135], v[16:19], v[36:39]\n"
        "v_mul_f32 v59, v59, v63\n"
        "v_add_f32 v76, 1.0, v76\n"
        "v_mfma_f32_16x16x32_bf16 v[40:43], a[136:139], v[16:19], v[40:43]\n"
        "v_add_f32 v77, 1.0, v77\n"
        "v_add_f32 v78, 1.0, v78\n"
        "v_mfma_f32_16x16x32_bf16 v[44:47], a[140:143], v[16:19], v[44:47]\n"
        "s_waitcnt lgkmcnt(1)\n"
        "ds_read_b128 v[16:19], v0 offset:4096\n"
        "v_add_f32 v79, 1.0, v79\n"
        "v_mfma_f32_16x16x32_bf16 v[24:27], a[144:147], v[8:11], v[24:27]\n"
        "v_rcp_f32 v76, v76\n"
        "v_mfma_f32_16x16x32_bf16 v[28:31], a[148:151], v[8:11], v[28:31]\n"
        "v_rcp_f32 v77, v77\n"
        "v_mfma_f32_16x16x32_bf16 v[32:35], a[152:155], v[8:11], v[32:35]\n"
        "v_rcp_f32 v78, v78\n"
        "v_mfma_f32_16x16x32_bf16 v[36:39], a[156:159], v[8:11], v[36:39]\n"
        "v_rcp_f32 v79, v79\n"
        "v_mfma_f32_16x16x32_bf16 v[40:43], a[160:163], v[8:11], v[40:43]\n"
        "v_mul_f32 v56, v56, v76\n"
        "v_mul_f32 v57, v57, v77\n"
        "v_mfma_f32_16x16x32_bf16 v[44:47], a[164:167], v[8:11], v[44:47]\n"
        "s_waitcnt lgkmcnt(1)\n"
        "ds_read_b128 v[8:11], v0 offset:4608\n"
        "v_mul_f32 v58, v58, v78\n"
        "v_mfma_f32_16x16x32_bf16 v[24:27], a[168:171], v[12:15], v[24:27]\n"
        "v_mul_f32 v59, v59, v79\n"
        "v_cvt_pk_bf16_f32 v54, v56, v57\n"
        "v_mfma_f32_16x16x32_bf16 v[28:31], a[172:175], v[12:15], v[28:31]\n"
        "v_cvt_pk_bf16_f32 v55, v58, v59\n"
        "v_mul_f32 v73, s80, v65\n"
        "v_mfma_f32_16x16x32_bf16 v[32:35], a[176:179], v[12:15], v[32:35]\n"
        "buffer_store_dwordx4 v[52:55], v1, s[40:43], s72 offen offset:0 sc1\n"
        "v_mfma_f32_16x16x32_bf16 v[36:39], a[180:183], v[12:15], v[36:39]\n"
        "v_mul_f32 v74, s80, v66\n"
        "v_mul_f32 v75, s80, v67\n"
        "v_mfma_f32_16x16x32_bf16 v[40:43], a[184:187], v[12:15], v[40:43]\n"
        "v_exp_f32 v72, v72\n"
        "v_mfma_f32_16x16x32_bf16 v[44:47], a[188:191], v[12:15], v[44:47]\n"
        "s_waitcnt lgkmcnt(1)\n"
        "ds_read_b128 v[12:15], v0 offset:5120\n"
        "v_mfma_f32_16x16x32_bf16 v[24:27], a[192:195], v[16:19], v[24:27]\n"
        "v_exp_f32 v73, v73\n"
        "v_mfma_f32_16x16x32_bf16 v[28:31], a[196:199], v[16:19], v[28:31]\n"
        "v_exp_f32 v74, v74\n"
        "v_mfma_f32_16x16x32_bf16 v[32:35], a[200:203], v[16:19], v[32:35]\n"
        "v_exp_f32 v75, v75\n"
        "v_mfma_f32_16x16x32_bf16 v[36:39], a[204:207], v[16:19], v[36:39]\n"
        "v_mul_f32 v64, v64, v68\n"
        "v_mul_f32 v65, v65, v69\n"
        "v_mfma_f32_16x16x32_bf16 v[40:43], a[208:211], v[16:19], v[40:43]\n"
        "v_mul_f32 v66, v66, v70\n"
        "v_mul_f32 v67, v67, v71\n"
        "v_mfma_f32_16x16x32_bf16 v[44:47], a[212:215], v[16:19], v[44:47]\n"
        "s_waitcnt lgkmcnt(1)\n"
        "ds_read_b128 v[16:19], v0 offset:5632\n"
        "v_add_f32 v72, 1.0, v72\n"
        "v_mfma_f32_16x16x32_bf16 v[24:27], a[216:219], v[8:11], v[24:27]\n"
        "v_add_f32 v73, 1.0, v73\n"
        "v_add_f32 v74, 1.0, v74\n"
        "v_mfma_f32_16x16x32_bf16 v[28:31], a[220:223], v[8:11], v[28:31]\n"
        "v_add_f32 v75, 1.0, v75\n"
        "v_mfma_f32_16x16x32_bf16 v[32:35], a[224:227], v[8:11], v[32:35]\n"
        "v_rcp_f32 v72, v72\n"
        "v_mfma_f32_16x16x32_bf16 v[36:39], a[228:231], v[8:11], v[36:39]\n"
        "v_rcp_f32 v73, v73\n"
        "v_mfma_f32_16x16x32_bf16 v[40:43], a[232:235], v[8:11], v[40:43]\n"
        "v_rcp_f32 v74, v74\n"
        "v_mfma_f32_16x16x32_bf16 v[44:47], a[236:239], v[8:11], v[44:47]\n"
        "s_waitcnt lgkmcnt(1)\n"
        "ds_read_b128 v[8:11], v0 offset:6144\n"
        "s_waitcnt vmcnt(1)\n"
        "v_cmp_gt_i32 vcc, s70, v4\n"
        "v_mfma_f32_16x16x32_bf16 v[24:27], a[240:243], v[12:15], v[24:27]\n"
        "v_rcp_f32 v75, v75\n"
        "v_mfma_f32_16x16x32_bf16 v[28:31], a[244:247], v[12:15], v[28:31]\n"
        "s_and_b64 vcc, vcc, 1\n"
        "v_mul_f32 v64, v64, v72\n"
        "v_mfma_f32_16x16x32_bf16 v[32:35], a[248:251], v[12:15], v[32:35]\n"
        "s_cbranch_vccnz .Lf1_i01_%=\n"
        "v_mfma_f32_16x16x32_bf16 v[36:39], a[252:255], v[12:15], v[36:39]\n"
        "buffer_load_dwordx4 v1, s[40:43], s71 offen offset:0 sc1 lds\n"
        "v_mfma_f32_16x16x32_bf16 v[40:43], v[80:83], v[12:15], v[40:43]\n"
        "v_mul_f32 v65, v65, v73\n"
        "v_mul_f32 v66, v66, v74\n"
        "v_mfma_f32_16x16x32_bf16 v[44:47], v[84:87], v[12:15], v[44:47]\n"
        "s_waitcnt lgkmcnt(1)\n"
        "ds_read_b128 v[12:15], v0 offset:6656\n"
        "v_mul_f32 v67, v67, v75\n"
        "v_mfma_f32_16x16x32_bf16 v[24:27], v[88:91], v[16:19], v[24:27]\n"
        "buffer_load_dwordx4 v1, s[40:43], s71 offen offset:1024 sc1 lds\n"
        "v_mfma_f32_16x16x32_bf16 v[28:31], v[92:95], v[16:19], v[28:31]\n"
        "v_cvt_pk_bf16_f32 v68, v64, v65\n"
        "v_cvt_pk_bf16_f32 v69, v66, v67\n"
        "v_mfma_f32_16x16x32_bf16 v[32:35], v[96:99], v[16:19], v[32:35]\n"
        "s_nop 0\n"
        "v_permlane16_swap_b32 v68, v70\n"
        "v_mfma_f32_16x16x32_bf16 v[36:39], v[100:103], v[16:19], v[36:39]\n"
        "buffer_load_dwordx4 v1, s[40:43], s71 offen offset:2048 sc1 lds\n"
        "v_mfma_f32_16x16x32_bf16 v[40:43], v[104:107], v[16:19], v[40:43]\n"
        "v_permlane16_swap_b32 v69, v71\n"
        "v_mfma_f32_16x16x32_bf16 v[44:47], v[108:111], v[16:19], v[44:47]\n"
        "s_waitcnt lgkmcnt(1)\n"
        "ds_read_b128 v[16:19], v0 offset:7168\n"
        "v_mfma_f32_16x16x32_bf16 v[24:27], v[112:115], v[8:11], v[24:27]\n"
        "buffer_load_dwordx4 v1, s[40:43], s71 offen offset:3072 sc1 lds\n"
        "v_mfma_f32_16x16x32_bf16 v[28:31], v[116:119], v[8:11], v[28:31]\n"
        "s_add_u32 m0, s78, s85\n"
        "s_add_u32 s69, s69, 1\n"
        "v_mfma_f32_16x16x32_bf16 v[32:35], v[120:123], v[8:11], v[32:35]\n"
        "buffer_store_dwordx4 v[68:71], v7, s[40:43], s83 offen offset:0 sc1\n"
        "v_mfma_f32_16x16x32_bf16 v[36:39], v[124:127], v[8:11], v[36:39]\n"
        "buffer_load_dwordx4 v1, s[40:43], s81 offen offset:0 sc1 lds\n"
        "v_mfma_f32_16x16x32_bf16 v[40:43], v[128:131], v[8:11], v[40:43]\n"
        "v_mov_b32 v5, s74\n"
        "v_mfma_f32_16x16x32_bf16 v[44:47], v[132:135], v[8:11], v[44:47]\n"
        "s_waitcnt lgkmcnt(1)\n"
        "ds_read_b128 v[8:11], v0 offset:7680\n"
        "v_mfma_f32_16x16x32_bf16 v[24:27], v[136:139], v[12:15], v[24:27]\n"
        "s_add_u32 s74, s74, 1\n"
        "v_mfma_f32_16x16x32_bf16 v[28:31], v[140:143], v[12:15], v[28:31]\n"
        "s_add_u32 s68, s68, 1\n"
        "v_mfma_f32_16x16x32_bf16 v[32:35], v[144:147], v[12:15], v[32:35]\n"
        "s_lshr_b32 s66, s68, 1\n"
        "v_mfma_f32_16x16x32_bf16 v[36:39], v[148:151], v[12:15], v[36:39]\n"
        "s_bitcmp1_b32 s54, s66\n"
        "s_cselect_b32 s66, 8192, 0\n"
        "v_mfma_f32_16x16x32_bf16 v[40:43], v[152:155], v[12:15], v[40:43]\n"
        "s_lshl_b32 s75, s68, 20\n"
        "v_mfma_f32_16x16x32_bf16 v[44:47], v[156:159], v[12:15], v[44:47]\n"
        "s_waitcnt lgkmcnt(1)\n"
        "ds_read_b128 v[12:15], v0 offset:8192\n"
        "v_mfma_f32_16x16x32_bf16 v[24:27], v[160:163], v[16:19], v[24:27]\n"
        "s_add_u32 s75, s75, s66\n"
        "v_mfma_f32_16x16x32_bf16 v[28:31], v[164:167], v[16:19], v[28:31]\n"
        "s_add_u32 s72, s75, s59\n"
        "v_mfma_f32_16x16x32_bf16 v[32:35], v[168:171], v[16:19], v[32:35]\n"
        "s_add_u32 s83, s75, s63\n"
        "v_mfma_f32_16x16x32_bf16 v[36:39], v[172:175], v[16:19], v[36:39]\n"
        "v_mfma_f32_16x16x32_bf16 v[40:43], v[176:179], v[16:19], v[40:43]\n"
        "v_mfma_f32_16x16x32_bf16 v[44:47], v[180:183], v[16:19], v[44:47]\n"
        "s_waitcnt lgkmcnt(1)\n"
        "ds_read_b128 v[16:19], v0 offset:8704\n"
        "v_mfma_f32_16x16x32_bf16 v[24:27], v[184:187], v[8:11], v[24:27]\n"
        "v_mfma_f32_16x16x32_bf16 v[28:31], v[188:191], v[8:11], v[28:31]\n"
        "v_mfma_f32_16x16x32_bf16 v[32:35], v[192:195], v[8:11], v[32:35]\n"
        "v_mfma_f32_16x16x32_bf16 v[36:39], v[196:199], v[8:11], v[36:39]\n"
        "v_mfma_f32_16x16x32_bf16 v[40:43], v[200:203], v[8:11], v[40:43]\n"
        "s_waitcnt vmcnt(0)\n"
        "s_barrier\n"
        "v_add_u32 v0, s78, v6\n"
        "v_mfma_f32_16x16x32_bf16 v[44:47], v[204:207], v[8:11], v[44:47]\n"
        "s_waitcnt lgkmcnt(1)\n"
        "ds_read_b128 v[8:11], v0 offset:0\n"
        "v_mfma_f32_16x16x32_bf16 v[24:27], v[208:211], v[12:15], v[24:27]\n"
        "v_mfma_f32_16x16x32_bf16 v[28:31], v[212:215], v[12:15], v[28:31]\n"
        "v_mfma_f32_16x16x32_bf16 v[32:35], v[216:219], v[12:15], v[32:35]\n"
        "s_getreg_b32 s88, hwreg(HW_REG_IB_STS, 0, 4)\n"
        "v_mfma_f32_16x16x32_bf16 v[36:39], v[220:223], v[12:15], v[36:39]\n"
        "s_cmp_le_u32 s88, 15\n"
        "s_cselect_b32 s88, s73, s89\n"
        "s_cselect_b32 s73, s89, s73\n"
        "v_mfma_f32_16x16x32_bf16 v[40:43], v[224:227], v[12:15], v[40:43]\n"
        "buffer_store_dword v5, v3, s[40:43], s88 offen sc1\n"
        "v_mfma_f32_16x16x32_bf16 v[44:47], v[228:231], v[12:15], v[44:47]\n"
        "s_waitcnt lgkmcnt(1)\n"
        "ds_read_b128 v[12:15], v0 offset:512\n"
        "v_mfma_f32_16x16x32_bf16 v[24:27], v[232:235], v[16:19], v[24:27]\n"
        "v_mfma_f32_16x16x32_bf16 v[28:31], v[236:239], v[16:19], v[28:31]\n"
        "v_mfma_f32_16x16x32_bf16 v[32:35], v[240:243], v[16:19], v[32:35]\n"
        "s_getreg_b32 s88, hwreg(HW_REG_IB_STS, 0, 4)\n"
        "v_mfma_f32_16x16x32_bf16 v[36:39], v[244:247], v[16:19], v[36:39]\n"
        "s_cmp_le_u32 s88, 15\n"
        "s_cselect_b32 s88, s73, s89\n"
        "s_cselect_b32 s73, s89, s73\n"
        "v_mfma_f32_16x16x32_bf16 v[40:43], v[248:251], v[16:19], v[40:43]\n"
        "buffer_store_dword v5, v3, s[40:43], s88 offen sc1\n"
        "v_mfma_f32_16x16x32_bf16 v[44:47], v[252:255], v[16:19], v[44:47]\n"
        "s_waitcnt lgkmcnt(1)\n"
        "ds_read_b128 v[16:19], v0 offset:1024\n"
        "s_mov_b32 s84, s73\n"
        "s_mov_b32 s73, s60\n"
        "s_branch .Lf1_i1_%=\n"
        ".Lf1_i01_%=:\n"
        "v_mfma_f32_16x16x32_bf16 v[36:39], a[252:255], v[12:15], v[36:39]\n"
        "buffer_load_dword v4, v2, s[40:43], s86 offen sc1\n"
        "v_mfma_f32_16x16x32_bf16 v[40:43], v[80:83], v[12:15], v[40:43]\n"
        "v_mul_f32 v65, v65, v73\n"
        "v_mul_f32 v66, v66, v74\n"
        "v_mfma_f32_16x16x32_bf16 v[44:47], v[84:87], v[12:15], v[44:47]\n"
        "s_waitcnt lgkmcnt(1)\n"
        "ds_read_b128 v[12:15], v0 offset:6656\n"
        "v_mul_f32 v67, v67, v75\n"
        "v_mfma_f32_16x16x32_bf16 v[24:27], v[88:91], v[16:19], v[24:27]\n"
        "v_cvt_pk_bf16_f32 v68, v64, v65\n"
        "v_cvt_pk_bf16_f32 v69, v66, v67\n"
        "v_mfma_f32_16x16x32_bf16 v[28:31], v[92:95], v[16:19], v[28:31]\n"
        "s_nop 0\n"
        "v_permlane16_swap_b32 v68, v70\n"
        "v_mfma_f32_16x16x32_bf16 v[32:35], v[96:99], v[16:19], v[32:35]\n"
        "v_permlane16_swap_b32 v69, v71\n"
        "v_mfma_f32_16x16x32_bf16 v[36:39], v[100:103], v[16:19], v[36:39]\n"
        "buffer_store_dwordx4 v[68:71], v7, s[40:43], s83 offen offset:0 sc1\n"
        "v_mfma_f32_16x16x32_bf16 v[40:43], v[104:107], v[16:19], v[40:43]\n"
        "v_mov_b32 v5, s74\n"
        "v_mfma_f32_16x16x32_bf16 v[44:47], v[108:111], v[16:19], v[44:47]\n"
        "s_waitcnt lgkmcnt(1)\n"
        "ds_read_b128 v[16:19], v0 offset:7168\n"
        "v_mfma_f32_16x16x32_bf16 v[24:27], v[112:115], v[8:11], v[24:27]\n"
        "s_add_u32 s74, s74, 1\n"
        "v_mfma_f32_16x16x32_bf16 v[28:31], v[116:119], v[8:11], v[28:31]\n"
        "s_add_u32 s68, s68, 1\n"
        "v_mfma_f32_16x16x32_bf16 v[32:35], v[120:123], v[8:11], v[32:35]\n"
        "s_lshr_b32 s66, s68, 1\n"
        "v_mfma_f32_16x16x32_bf16 v[36:39], v[124:127], v[8:11], v[36:39]\n"
        "s_bitcmp1_b32 s54, s66\n"
        "s_cselect_b32 s66, 8192, 0\n"
        "v_mfma_f32_16x16x32_bf16 v[40:43], v[128:131], v[8:11], v[40:43]\n"
        "s_lshl_b32 s75, s68, 20\n"
        "v_mfma_f32_16x16x32_bf16 v[44:47], v[132:135], v[8:11], v[44:47]\n"
        "s_waitcnt lgkmcnt(1)\n"
        "ds_read_b128 v[8:11], v0 offset:7680\n"
        "v_mfma_f32_16x16x32_bf16 v[24:27], v[136:139], v[12:15], v[24:27]\n"
        "s_add_u32 s75, s75, s66\n"
        "v_mfma_f32_16x16x32_bf16 v[28:31], v[140:143], v[12:15], v[28:31]\n"
        "s_add_u32 s72, s75, s59\n"
        "v_mfma_f32_16x16x32_bf16 v[32:35], v[144:147], v[12:15], v[32:35]\n"
        "s_add_u32 s83, s75, s63\n"
        "v_mfma_f32_16x16x32_bf16 v[36:39], v[148:151], v[12:15], v[36:39]\n"
        "v_mfma_f32_16x16x32_bf16 v[40:43], v[152:155], v[12:15], v[40:43]\n"
        "v_mfma_f32_16x16x32_bf16 v[44:47], v[156:159], v[12:15], v[44:47]\n"
        "s_waitcnt lgkmcnt(1)\n"
        "ds_read_b128 v[12:15], v0 offset:8192\n"
        "v_mfma_f32_16x16x32_bf16 v[24:27], v[160:163], v[16:19], v[24:27]\n"
        "v_mfma_f32_16x16x32_bf16 v[28:31], v[164:167], v[16:19], v[28:31]\n"
        "v_mfma_f32_16x16x32_bf16 v[32:35], v[168:171], v[16:19], v[32:35]\n"
        "v_mfma_f32_16x16x32_bf16 v[36:39], v[172:175], v[16:19], v[36:39]\n"
        "v_mfma_f32_16x16x32_bf16 v[40:43], v[176:179], v[16:19], v[40:43]\n"
        "v_mfma_f32_16x16x32_bf16 v[44:47], v[180:183], v[16:19], v[44:47]\n"
        "s_waitcnt lgkmcnt(1)\n"
        "ds_read_b128 v[16:19], v0 offset:8704\n"
        "v_mfma_f32_16x16x32_bf16 v[24:27], v[184:187], v[8:11], v[24:27]\n"
        "s_waitcnt vmcnt(1)\n"
        "v_cmp_gt_i32 vcc, s70, v4\n"
        "v_mfma_f32_16x16x32_bf16 v[28:31], v[188:191], v[8:11], v[28:31]\n"
        "v_mfma_f32_16x16x32_bf16 v[32:35], v[192:195], v[8:11], v[32:35]\n"
        "s_and_b64 vcc, vcc, 1\n"
        "v_mfma_f32_16x16x32_bf16 v[36:39], v[196:199], v[8:11], v[36:39]\n"
        "s_cbranch_vccnz .Lf1_i02_%=\n"
        "v_mfma_f32_16x16x32_bf16 v[40:43], v[200:203], v[8:11], v[40:43]\n"
        "s_getreg_b32 s88, hwreg(HW_REG_IB_STS, 0, 4)\n"
        "buffer_load_dwordx4 v1, s[40:43], s71 offen offset:0 sc1 lds\n"
        "v_mfma_f32_16x16x32_bf16 v[44:47], v[204:207], v[8:11], v[44:47]\n"
        "s_waitcnt lgkmcnt(1)\n"
        "v_mfma_f32_16x16x32_bf16 v[24:27], v[208:211], v[12:15], v[24:27]\n"
        "buffer_load_dwordx4 v1, s[40:43], s71 offen offset:1024 sc1 lds\n"
        "v_mfma_f32_16x16x32_bf16 v[28:31], v[212:215], v[12:15], v[28:31]\n"
        "s_cmp_le_u32 s88, 0\n"
        "s_cselect_b32 s88, s73, s89\n"
        "s_cselect_b32 s73, s89, s73\n"
        "v_mfma_f32_16x16x32_bf16 v[32:35], v[216:219], v[12:15], v[32:35]\n"
        "buffer_load_dwordx4 v1, s[40:43], s71 offen offset:2048 sc1 lds\n"
        "v_mfma_f32_16x16x32_bf16 v[36:39], v[220:223], v[12:15], v[36:39]\n"
        "buffer_store_dword v5, v3, s[40:43], s88 offen sc1\n"
        "v_mfma_f32_16x16x32_bf16 v[40:43], v[224:227], v[12:15], v[40:43]\n"
        "buffer_load_dwordx4 v1, s[40:43], s71 offen offset:3072 sc1 lds\n"
        "v_mfma_f32_16x16x32_bf16 v[44:47], v[228:231], v[12:15], v[44:47]\n"
        "s_waitcnt lgkmcnt(0)\n"
        "s_add_u32 m0, s78, s85\n"
        "s_add_u32 s69, s69, 1\n"
        "v_mfma_f32_16x16x32_bf16 v[24:27], v[232:235], v[16:19], v[24:27]\n"
        "s_getreg_b32 s88, hwreg(HW_REG_IB_STS, 0, 4)\n"
        "buffer_load_dwordx4 v1, s[40:43], s81 offen offset:0 sc1 lds\n"
        "v_mfma_f32_16x16x32_bf16 v[28:31], v[236:239], v[16:19], v[28:31]\n"
        "s_cmp_le_u32 s88, 5\n"
        "s_cselect_b32 s88, s73, s89\n"
        "s_cselect_b32 s73, s89, s73\n"
        "v_mfma_f32_16x16x32_bf16 v[32:35], v[240:243], v[16:19], v[32:35]\n"
        "buffer_store_dword v5, v3, s[40:43], s88 offen sc1\n"
        "v_mfma_f32_16x16x32_bf16 v[36:39], v[244:247], v[16:19], v[36:39]\n"
        "s_mov_b32 s84, s73\n"
        "s_mov_b32 s73, s60\n"
        "v_mfma_f32_16x16x32_bf16 v[40:43], v[248:251], v[16:19], v[40:43]\n"
        "v_mfma_f32_16x16x32_bf16 v[44:47], v[252:255], v[16:19], v[44:47]\n"
        "v_mul_f32 v72, s80, v24\n"
        "v_mul_f32 v73, s80, v25\n"
        "v_mul_f32 v74, s80, v26\n"
        "v_mul_f32 v75, s80, v27\n"
        "v_exp_f32 v72, v72\n"
        "v_exp_f32 v73, v73\n"
        "v_exp_f32 v74, v74\n"
        "v_exp_f32 v75, v75\n"
        "v_mul_f32 v24, v24, v28\n"
        "v_mul_f32 v25, v25, v29\n"
        "v_mul_f32 v26, v26, v30\n"
        "v_mul_f32 v27, v27, v31\n"
        "v_add_f32 v72, 1.0, v72\n"
        "v_add_f32 v73, 1.0, v73\n"
        "v_add_f32 v74, 1.0, v74\n"
        "v_add_f32 v75, 1.0, v75\n"
        "v_rcp_f32 v72, v72\n"
        "v_rcp_f32 v73, v73\n"
        "v_rcp_f32 v74, v74\n"
        "v_rcp_f32 v75, v75\n"
        "v_mul_f32 v24, v24, v72\n"
        "v_mul_f32 v25, v25, v73\n"
        "v_mul_f32 v26, v26, v74\n"
        "v_mul_f32 v27, v27, v75\n"
        "v_cvt_pk_bf16_f32 v28, v24, v25\n"
        "v_cvt_pk_bf16_f32 v29, v26, v27\n"
        "s_waitcnt vmcnt(7)\n"
        "buffer_store_dword v5, v3, s[40:43], s84 offen sc1\n"
        "s_mov_b32 s84, s89\n"
        "v_mul_f32 v76, s80, v32\n"
        "v_mul_f32 v77, s80, v33\n"
        "v_mul_f32 v78, s80, v34\n"
        "v_mul_f32 v79, s80, v35\n"
        "v_exp_f32 v76, v76\n"
        "v_exp_f32 v77, v77\n"
        "v_exp_f32 v78, v78\n"
        "v_exp_f32 v79, v79\n"
        "v_mul_f32 v32, v32, v36\n"
        "v_mul_f32 v33, v33, v37\n"
        "v_mul_f32 v34, v34, v38\n"
        "v_mul_f32 v35, v35, v39\n"
        "v_add_f32 v76, 1.0, v76\n"
        "v_add_f32 v77, 1.0, v77\n"
        "v_add_f32 v78, 1.0, v78\n"
        "v_add_f32 v79, 1.0, v79\n"
        "v_rcp_f32 v76, v76\n"
        "v_rcp_f32 v77, v77\n"
        "v_rcp_f32 v78, v78\n"
        "v_rcp_f32 v79, v79\n"
        "v_mul_f32 v32, v32, v76\n"
        "v_mul_f32 v33, v33, v77\n"
        "v_mul_f32 v34, v34, v78\n"
        "v_mul_f32 v35, v35, v79\n"
        "v_cvt_pk_bf16_f32 v30, v32, v33\n"
        "v_cvt_pk_bf16_f32 v31, v34, v35\n"
        "buffer_store_dwordx4 v[28:31], v1, s[40:43], s72 offen offset:0 sc1\n"
        "v_mul_f32 v72, s80, v40\n"
        "v_mul_f32 v73, s80, v41\n"
        "v_mul_f32 v74, s80, v42\n"
        "v_mul_f32 v75, s80, v43\n"
        "v_exp_f32 v72, v72\n"
        "v_exp_f32 v73, v73\n"
        "v_exp_f32 v74, v74\n"
        "v_exp_f32 v75, v75\n"
        "v_mul_f32 v40, v40, v44\n"
        "v_mul_f32 v41, v41, v45\n"
        "v_mul_f32 v42, v42, v46\n"
        "v_mul_f32 v43, v43, v47\n"
        "v_add_f32 v72, 1.0, v72\n"
        "v_add_f32 v73, 1.0, v73\n"
        "v_add_f32 v74, 1.0, v74\n"
        "v_add_f32 v75, 1.0, v75\n"
        "v_rcp_f32 v72, v72\n"
        "v_rcp_f32 v73, v73\n"
        "v_rcp_f32 v74, v74\n"
        "v_rcp_f32 v75, v75\n"
        "v_mul_f32 v40, v40, v72\n"
        "v_mul_f32 v41, v41, v73\n"
        "v_mul_f32 v42, v42, v74\n"
        "v_mul_f32 v43, v43, v75\n"
        "v_cvt_pk_bf16_f32 v44, v40, v41\n"
        "v_cvt_pk_bf16_f32 v45, v42, v43\n"
        "s_nop 0\n"
        "v_permlane16_swap_b32 v44, v46\n"
        "v_permlane16_swap_b32 v45, v47\n"
        "buffer_store_dwordx4 v[44:47], v7, s[40:43], s83 offen offset:0 sc1\n"
        "v_mov_b32 v5, s74\n"
        "s_mov_b32 s84, s60\n"
        "buffer_store_dword v5, v3, s[40:43], s89 offen sc1\n"
        "buffer_store_dword v5, v3, s[40:43], s89 offen sc1\n"
        "s_waitcnt vmcnt(6)\n"
        "s_branch .Lf1_go0_%=\n"
        ".Lf1_i02_%=:\n"
        "v_mfma_f32_16x16x32_bf16 v[40:43], v[200:203], v[8:11], v[40:43]\n"
        "s_getreg_b32 s88, hwreg(HW_REG_IB_STS, 0, 4)\n"
        "buffer_load_dword v4, v2, s[40:43], s86 offen sc1\n"
        "v_mfma_f32_16x16x32_bf16 v[44:47], v[204:207], v[8:11], v[44:47]\n"
        "s_waitcnt lgkmcnt(1)\n"
        "v_mfma_f32_16x16x32_bf16 v[24:27], v[208:211], v[12:15], v[24:27]\n"
        "s_cmp_le_u32 s88, 0\n"
        "s_cselect_b32 s88, s73, s89\n"
        "s_cselect_b32 s73, s89, s73\n"
        "v_mfma_f32_16x16x32_bf16 v[28:31], v[212:215], v[12:15], v[28:31]\n"
        "buffer_store_dword v5, v3, s[40:43], s88 offen sc1\n"
        "v_mfma_f32_16x16x32_bf16 v[32:35], v[216:219], v[12:15], v[32:35]\n"
        "v_mfma_f32_16x16x32_bf16 v[36:39], v[220:223], v[12:15], v[36:39]\n"
        "v_mfma_f32_16x16x32_bf16 v[40:43], v[224:227], v[12:15], v[40:43]\n"
        "v_mfma_f32_16x16x32_bf16 v[44:47], v[228:231], v[12:15], v[44:47]\n"
        "s_waitcnt lgkmcnt(0)\n"
        "v_mfma_f32_16x16x32_bf16 v[24:27], v[232:235], v[16:19], v[24:27]\n"
        "s_getreg_b32 s88, hwreg(HW_REG_IB_STS, 0, 4)\n"
        "v_mfma_f32_16x16x32_bf16 v[28:31], v[236:239], v[16:19], v[28:31]\n"
        "s_cmp_le_u32 s88, 2\n"
        "s_cselect_b32 s88, s73, s89\n"
        "s_cselect_b32 s73, s89, s73\n"
        "v_mfma_f32_16x16x32_bf16 v[32:35], v[240:243], v[16:19], v[32:35]\n"
        "buffer_store_dword v5, v3, s[40:43], s88 offen sc1\n"
        "v_mfma_f32_16x16x32_bf16 v[36:39], v[244:247], v[16:19], v[36:39]\n"
        "s_mov_b32 s84, s73\n"
        "s_mov_b32 s73, s60\n"
        "v_mfma_f32_16x16x32_bf16 v[40:43], v[248:251], v[16:19], v[40:43]\n"
        "v_mfma_f32_16x16x32_bf16 v[44:47], v[252:255], v[16:19], v[44:47]\n"
        "v_mul_f32 v72, s80, v24\n"
        "v_mul_f32 v73, s80, v25\n"
        "v_mul_f32 v74, s80, v26\n"
        "v_mul_f32 v75, s80, v27\n"
        "v_exp_f32 v72, v72\n"
        "v_exp_f32 v73, v73\n"
        "v_exp_f32 v74, v74\n"
        "v_exp_f32 v75, v75\n"
        "v_mul_f32 v24, v24, v28\n"
        "v_mul_f32 v25, v25, v29\n"
        "v_mul_f32 v26, v26, v30\n"
        "v_mul_f32 v27, v27, v31\n"
        "v_add_f32 v72, 1.0, v72\n"
        "v_add_f32 v73, 1.0, v73\n"
        "v_add_f32 v74, 1.0, v74\n"
        "v_add_f32 v75, 1.0, v75\n"
        "v_rcp_f32 v72, v72\n"
        "v_rcp_f32 v73, v73\n"
        "v_rcp_f32 v74, v74\n"
        "v_rcp_f32 v75, v75\n"
        "v_mul_f32 v24, v24, v72\n"
        "v_mul_f32 v25, v25, v73\n"
        "v_mul_f32 v26, v26, v74\n"
        "v_mul_f32 v27, v27, v75\n"
        "v_cvt_pk_bf16_f32 v28, v24, v25\n"
        "v_cvt_pk_bf16_f32 v29, v26, v27\n"
        "s_waitcnt vmcnt(3)\n"
        "buffer_store_dword v5, v3, s[40:43], s84 offen sc1\n"
        "s_mov_b32 s84, s89\n"
        "v_mul_f32 v76, s80, v32\n"
        "v_mul_f32 v77, s80, v33\n"
        "v_mul_f32 v78, s80, v34\n"
        "v_mul_f32 v79, s80, v35\n"
        "v_exp_f32 v76, v76\n"
        "v_exp_f32 v77, v77\n"
        "v_exp_f32 v78, v78\n"
        "v_exp_f32 v79, v79\n"
        "v_mul_f32 v32, v32, v36\n"
        "v_mul_f32 v33, v33, v37\n"
        "v_mul_f32 v34, v34, v38\n"
        "v_mul_f32 v35, v35, v39\n"
        "v_add_f32 v76, 1.0, v76\n"
        "v_add_f32 v77, 1.0, v77\n"
        "v_add_f32 v78, 1.0, v78\n"
        "v_add_f32 v79, 1.0, v79\n"
        "v_rcp_f32 v76, v76\n"
        "v_rcp_f32 v77, v77\n"
        "v_rcp_f32 v78, v78\n"
        "v_rcp_f32 v79, v79\n"
        "v_mul_f32 v32, v32, v76\n"
        "v_mul_f32 v33, v33, v77\n"
        "v_mul_f32 v34, v34, v78\n"
        "v_mul_f32 v35, v35, v79\n"
        "v_cvt_pk_bf16_f32 v30, v32, v33\n"
        "v_cvt_pk_bf16_f32 v31, v34, v35\n"
        "buffer_store_dwordx4 v[28:31], v1, s[40:43], s72 offen offset:0 sc1\n"
        "v_mul_f32 v72, s80, v40\n"
        "v_mul_f32 v73, s80, v41\n"
        "v_mul_f32 v74, s80, v42\n"
        "v_mul_f32 v75, s80, v43\n"
        "v_exp_f32 v72, v72\n"
        "v_exp_f32 v73, v73\n"
        "v_exp_f32 v74, v74\n"
        "v_exp_f32 v75, v75\n"
        "v_mul_f32 v40, v40, v44\n"
        "v_mul_f32 v41, v41, v45\n"
        "v_mul_f32 v42, v42, v46\n"
        "v_mul_f32 v43, v43, v47\n"
        "v_add_f32 v72, 1.0, v72\n"
        "v_add_f32 v73, 1.0, v73\n"
        "v_add_f32 v74, 1.0, v74\n"
        "v_add_f32 v75, 1.0, v75\n"
        "v_rcp_f32 v72, v72\n"
        "v_rcp_f32 v73, v73\n"
        "v_rcp_f32 v74, v74\n"
        "v_rcp_f32 v75, v75\n"
        "v_mul_f32 v40, v40, v72\n"
        "v_mul_f32 v41, v41, v73\n"
        "v_mul_f32 v42, v42, v74\n"
        "v_mul_f32 v43, v43, v75\n"
        "v_cvt_pk_bf16_f32 v44, v40, v41\n"
        "v_cvt_pk_bf16_f32 v45, v42, v43\n"
        "s_nop 0\n"
        "v_permlane16_swap_b32 v44, v46\n"
        "v_permlane16_swap_b32 v45, v47\n"
        "buffer_store_dwordx4 v[44:47], v7, s[40:43], s83 offen offset:0 sc1\n"
        "s_waitcnt vmcnt(5)\n"
        "v_cmp_gt_i32 vcc, s70, v4\n"
        "s_nop 0\n"
        "s_and_b64 vcc, vcc, 1\n"
        "s_cbranch_vccnz .Lf1_late0_%=\n"
        "buffer_load_dwordx4 v1, s[40:43], s71 offen offset:0 sc1 lds\n"
        "buffer_load_dwordx4 v1, s[40:43], s71 offen offset:1024 sc1 lds\n"
        "buffer_load_dwordx4 v1, s[40:43], s71 offen offset:2048 sc1 lds\n"
        "buffer_load_dwordx4 v1, s[40:43], s71 offen offset:3072 sc1 lds\n"
        "s_add_u32 m0, s78, s85\n"
        "s_add_u32 s69, s69, 1\n"
        "buffer_load_dwordx4 v1, s[40:43], s81 offen offset:0 sc1 lds\n"
        "s_waitcnt vmcnt(5)\n"
        "v_mov_b32 v5, s74\n"
        "buffer_store_dword v5, v3, s[40:43], s73 offen sc1\n"
        "s_waitcnt vmcnt(1)\n"
        "s_branch .Lf1_go0_%=\n"
        ".Lf1_late0_%=:\n"
        "s_waitcnt vmcnt(0)\n"
        "v_mov_b32 v5, s74\n"
        "buffer_store_dword v5, v3, s[40:43], s73 offen sc1\n"
        "s_mov_b32 s64, 0\n"
        ".Lf1_in0_top_%=:\n"
        "buffer_load_dword v4, v2, s[40:43], s86 offen sc1\n"
        "s_waitcnt vmcnt(0)\n"
        "v_cmp_gt_i32 vcc, s70, v4\n"
        "s_nop 0\n"
        "s_and_b64 vcc, vcc, 1\n"
        "s_cbranch_vccz .Lf1_in0_ok_%=\n"
        "s_add_u32 s64, s64, 1\n"
        "s_and_b32 s65, s64, 255\n"
        "s_cbranch_scc1 .Lf1_in0_go_%=\n"
        "s_mov_b32 s65, 49152\n"
        "s_cmp_lt_u32 s64, 1048576\n"
        "s_cbranch_scc1 .Lf1_in0_chk_%=\n"
        "v_mov_b32 v4, 1\n"
        "buffer_store_dword v4, off, s[40:43], s65 sc1\n"
        "s_waitcnt vmcnt(0)\n"
        ".Lf1_in0_chk_%=:\n"
        "buffer_load_dword v4, off, s[40:43], s65 sc1\n"
        "s_waitcnt vmcnt(0)\n"
        "v_readfirstlane_b32 s65, v4\n"
        "s_nop 0\n"
        "s_cmp_lg_u32 s65, 1\n"
        "s_cbranch_scc1 .Lf1_in0_go_%=\n"
        "s_endpgm\n"
        ".Lf1_in0_go_%=:\n"
        "s_branch .Lf1_in0_top_%=\n"
        ".Lf1_in0_ok_%=:\n"
        "buffer_load_dwordx4 v1, s[40:43], s71 offen offset:0 sc1 lds\n"
        "buffer_load_dwordx4 v1, s[40:43], s71 offen offset:1024 sc1 lds\n"
        "buffer_load_dwordx4 v1, s[40:43], s71 offen offset:2048 sc1 lds\n"
        "buffer_load_dwordx4 v1, s[40:43], s71 offen offset:3072 sc1 lds\n"
        "s_add_u32 m0, s78, s85\n"
        "s_add_u32 s69, s69, 1\n"
        "buffer_load_dwordx4 v1, s[40:43], s81 offen offset:0 sc1 lds\n"
        "s_waitcnt vmcnt(0)\n"
        ".Lf1_go0_%=:\n"
        "s_mov_b32 s72, 241172480\n"
        "s_mov_b32 s83, 241172480\n"
        "s_mov_b32 s73, 241172480\n"
        "v_add_u32 v0, s78, v6\n"
        "s_barrier\n"
        "ds_read_b128 v[8:11], v0 offset:0\n"
        "ds_read_b128 v[12:15], v0 offset:512\n"
        "ds_read_b128 v[16:19], v0 offset:1024\n"
        "s_waitcnt lgkmcnt(2)\n"
        "s_branch .Lf1_i1_%=\n"
        ".Lf1_i1_%=:\n"
        "v_mfma_f32_16x16x32_bf16 v[48:51], a[0:3], v[8:11], 0\n"
        "s_add_u32 s86, s76, 0\n"
        "buffer_load_dword v4, v2, s[40:43], s86 offen sc1\n"
        "v_mfma_f32_16x16x32_bf16 v[52:55], a[4:7], v[8:11], 0\n"
        "s_lshr_b32 s67, s69, 1\n"
        "v_mul_f32 v72, s80, v24\n"
        "v_mul_f32 v73, s80, v25\n"
        "v_mfma_f32_16x16x32_bf16 v[56:59], a[8:11], v[8:11], 0\n"
        "s_lshr_b32 s65, s67, 1\n"
        "v_mul_f32 v74, s80, v26\n"
        "v_mul_f32 v75, s80, v27\n"
        "v_mfma_f32_16x16x32_bf16 v[60:63], a[12:15], v[8:11], 0\n"
        "s_bitcmp1_b32 s54, s65\n"
        "s_cselect_b32 s65, 8192, 0\n"
        "v_mul_f32 v76, s80, v32\n"
        "v_mfma_f32_16x16x32_bf16 v[64:67], a[16:19], v[8:11], 0\n"
        "s_lshl_b32 s77, s67, 20\n"
        "v_exp_f32 v72, v72\n"
        "v_mfma_f32_16x16x32_bf16 v[68:71], a[20:23], v[8:11], 0\n"
        "s_waitcnt lgkmcnt(1)\n"
        "ds_read_b128 v[8:11], v0 offset:1536\n"
        "v_mul_f32 v77, s80, v33\n"
        "v_mfma_f32_16x16x32_bf16 v[48:51], a[24:27], v[12:15], v[48:51]\n"
        "s_add_u32 s77, s77, s65\n"
        "v_exp_f32 v73, v73\n"
        "v_mfma_f32_16x16x32_bf16 v[52:55], a[28:31], v[12:15], v[52:55]\n"
        "s_add_u32 s71, s77, s58\n"
        "v_exp_f32 v74, v74\n"
        "v_mfma_f32_16x16x32_bf16 v[56:59], a[32:35], v[12:15], v[56:59]\n"
        "s_add_u32 s81, s77, s62\n"
        "v_exp_f32 v75, v75\n"
        "v_mfma_f32_16x16x32_bf16 v[60:63], a[36:39], v[12:15], v[60:63]\n"
        "s_add_u32 s78, s78, 18432\n"
        "v_mul_f32 v24, v24, v28\n"
        "v_mul_f32 v25, v25, v29\n"
        "v_mfma_f32_16x16x32_bf16 v[64:67], a[40:43], v[12:15], v[64:67]\n"
        "s_cmp_eq_u32 s78, 55296\n"
        "s_cselect_b32 s78, 0, s78\n"
        "v_mul_f32 v26, v26, v30\n"
        "v_mfma_f32_16x16x32_bf16 v[68:71], a[44:47], v[12:15], v[68:71]\n"
        "s_waitcnt lgkmcnt(1)\n"
        "ds_read_b128 v[12:15], v0 offset:2048\n"
        "v_mul_f32 v27, v27, v31\n"
        "v_mfma_f32_16x16x32_bf16 v[48:51], a[48:51], v[16:19], v[48:51]\n"
        "s_add_u32 m0, s78, s61\n"
        "v_add_f32 v72, 1.0, v72\n"
        "v_add_f32 v73, 1.0, v73\n"
        "v_mfma_f32_16x16x32_bf16 v[52:55], a[52:55], v[16:19], v[52:55]\n"
        "s_lshr_b32 s70, s69, 1\n"
        "v_add_f32 v74, 1.0, v74\n"
        "v_add_f32 v75, 1.0, v75\n"
        "v_mfma_f32_16x16x32_bf16 v[56:59], a[56:59], v[16:19], v[56:59]\n"
        "s_waitcnt vmcnt(3)\n"
        "v_rcp_f32 v72, v72\n"
        "v_mfma_f32_16x16x32_bf16 v[60:63], a[60:63], v[16:19], v[60:63]\n"
        "buffer_store_dword v5, v3, s[40:43], s84 offen sc1\n"
        "v_mfma_f32_16x16x32_bf16 v[64:67], a[64:67], v[16:19], v[64:67]\n"
        "v_rcp_f32 v73, v73\n"
        "v_mfma_f32_16x16x32_bf16 v[68:71], a[68:71], v[16:19], v[68:71]\n"
        "s_waitcnt lgkmcnt(1)\n"
        "ds_read_b128 v[16:19], v0 offset:2560\n"
        "v_mul_f32 v78, s80, v34\n"
        "v_mfma_f32_16x16x32_bf16 v[48:51], a[72:75], v[8:11], v[48:51]\n"
        "v_rcp_f32 v74, v74\n"
        "v_mfma_f32_16x16x32_bf16 v[52:55], a[76:79], v[8:11], v[52:55]\n"
        "v_rcp_f32 v75, v75\n"
        "v_mfma_f32_16x16x32_bf16 v[56:59], a[80:83], v[8:11], v[56:59]\n"
        "v_mul_f32 v24, v24, v72\n"
        "v_mul_f32 v25, v25, v73\n"
        "v_mfma_f32_16x16x32_bf16 v[60:63], a[84:87], v[8:11], v[60:63]\n"
        "v_mul_f32 v26, v26, v74\n"
        "v_mul_f32 v27, v27, v75\n"
        "v_mfma_f32_16x16x32_bf16 v[64:67], a[88:91], v[8:11], v[64:67]\n"
        "v_cvt_pk_bf16_f32 v28, v24, v25\n"
        "v_cvt_pk_bf16_f32 v29, v26, v27\n"
        "v_mfma_f32_16x16x32_bf16 v[68:71], a[92:95], v[8:11], v[68:71]\n"
        "s_waitcnt lgkmcnt(1)\n"
        "ds_read_b128 v[8:11], v0 offset:3072\n"
        "v_mul_f32 v79, s80, v35\n"
        "v_mfma_f32_16x16x32_bf16 v[48:51], a[96:99], v[12:15], v[48:51]\n"
        "v_exp_f32 v76, v76\n"
        "v_mfma_f32_16x16x32_bf16 v[52:55], a[100:103], v[12:15], v[52:55]\n"
        "v_exp_f32 v77, v77\n"
        "v_mfma_f32_16x16x32_bf16 v[56:59], a[104:107], v[12:15], v[56:59]\n"
        "v_exp_f32 v78, v78\n"
        "v_mfma_f32_16x16x32_bf16 v[60:63], a[108:111], v[12:15], v[60:63]\n"
        "v_exp_f32 v79, v79\n"
        "v_mfma_f32_16x16x32_bf16 v[64:67], a[112:115], v[12:15], v[64:67]\n"
        "s_waitcnt vmcnt(1)\n"
        "v_cmp_gt_i32 vcc, s70, v4\n"
        "v_mul_f32 v32, v32, v36\n"
        "v_mfma_f32_16x16x32_bf16 v[68:71], a[116:119], v[12:15], v[68:71]\n"
        "s_waitcnt lgkmcnt(1)\n"
        "ds_read_b128 v[12:15], v0 offset:3584\n"
        "v_mul_f32 v33, v33, v37\n"
        "v_mfma_f32_16x16x32_bf16 v[48:51], a[120:123], v[16:19], v[48:51]\n"
        "s_and_b64 vcc, vcc, 1\n"
        "v_mul_f32 v34, v34, v38\n"
        "v_mfma_f32_16x16x32_bf16 v[52:55], a[124:127], v[16:19], v[52:55]\n"
        "s_cbranch_vccnz .Lf1_i10_%=\n"
        "v_mfma_f32_16x16x32_bf16 v[56:59], a[128:131], v[16:19], v[56:59]\n"
        "buffer_load_dwordx4 v1, s[40:43], s71 offen offset:0 sc1 lds\n"
        "v_mfma_f32_16x16x32_bf16 v[60:63], a[132:135], v[16:19], v[60:63]\n"
        "v_mul_f32 v35, v35, v39\n"
        "v_add_f32 v76, 1.0, v76\n"
        "v_mfma_f32_16x16x32_bf16 v[64:67], a[136:139], v[16:19], v[64:67]\n"
        "v_add_f32 v77, 1.0, v77\n"
        "v_add_f32 v78, 1.0, v78\n"
        "v_mfma_f32_16x16x32_bf16 v[68:71], a[140:143], v[16:19], v[68:71]\n"
        "s_waitcnt lgkmcnt(1)\n"
        "ds_read_b128 v[16:19], v0 offset:4096\n"
        "v_add_f32 v79, 1.0, v79\n"
        "v_mfma_f32_16x16x32_bf16 v[48:51], a[144:147], v[8:11], v[48:51]\n"
        "buffer_load_dwordx4 v1, s[40:43], s71 offen offset:1024 sc1 lds\n"
        "v_mfma_f32_16x16x32_bf16 v[52:55], a[148:151], v[8:11], v[52:55]\n"
        "v_rcp_f32 v76, v76\n"
        "v_mfma_f32_16x16x32_bf16 v[56:59], a[152:155], v[8:11], v[56:59]\n"
        "v_rcp_f32 v77, v77\n"
        "v_mfma_f32_16x16x32_bf16 v[60:63], a[156:159], v[8:11], v[60:63]\n"
        "v_rcp_f32 v78, v78\n"
        "v_mfma_f32_16x16x32_bf16 v[64:67], a[160:163], v[8:11], v[64:67]\n"
        "buffer_load_dwordx4 v1, s[40:43], s71 offen offset:2048 sc1 lds\n"
        "v_mfma_f32_16x16x32_bf16 v[68:71], a[164:167], v[8:11], v[68:71]\n"
        "s_waitcnt lgkmcnt(1)\n"
        "ds_read_b128 v[8:11], v0 offset:4608\n"
        "v_mul_f32 v72, s80, v40\n"
        "v_mfma_f32_16x16x32_bf16 v[48:51], a[168:171], v[12:15], v[48:51]\n"
        "v_rcp_f32 v79, v79\n"
        "v_mfma_f32_16x16x32_bf16 v[52:55], a[172:175], v[12:15], v[52:55]\n"
        "v_mul_f32 v32, v32, v76\n"
        "v_mul_f32 v33, v33, v77\n"
        "v_mfma_f32_16x16x32_bf16 v[56:59], a[176:179], v[12:15], v[56:59]\n"
        "buffer_load_dwordx4 v1, s[40:43], s71 offen offset:3072 sc1 lds\n"
        "v_mfma_f32_16x16x32_bf16 v[60:63], a[180:183], v[12:15], v[60:63]\n"
        "s_add_u32 m0, s78, s85\n"
        "s_add_u32 s69, s69, 1\n"
        "v_mul_f32 v34, v34, v78\n"
        "v_mfma_f32_16x16x32_bf16 v[64:67], a[184:187], v[12:15], v[64:67]\n"
        "v_mul_f32 v35, v35, v79\n"
        "v_cvt_pk_bf16_f32 v30, v32, v33\n"
        "v_mfma_f32_16x16x32_bf16 v[68:71], a[188:191], v[12:15], v[68:71]\n"
        "s_waitcnt lgkmcnt(1)\n"
        "ds_read_b128 v[12:15], v0 offset:5120\n"
        "v_cvt_pk_bf16_f32 v31, v34, v35\n"
        "v_mfma_f32_16x16x32_bf16 v[48:51], a[192:195], v[16:19], v[48:51]\n"
        "buffer_load_dwordx4 v1, s[40:43], s81 offen offset:0 sc1 lds\n"
        "v_mfma_f32_16x16x32_bf16 v[52:55], a[196:199], v[16:19], v[52:55]\n"
        "buffer_store_dwordx4 v[28:31], v1, s[40:43], s72 offen offset:0 sc1\n"
        "v_mfma_f32_16x16x32_bf16 v[56:59], a[200:203], v[16:19], v[56:59]\n"
        "v_mul_f32 v73, s80, v41\n"
        "v_mul_f32 v74, s80, v42\n"
        "v_mfma_f32_16x16x32_bf16 v[60:63], a[204:207], v[16:19], v[60:63]\n"
        "v_mul_f32 v75, s80, v43\n"
        "v_mfma_f32_16x16x32_bf16 v[64:67], a[208:211], v[16:19], v[64:67]\n"
        "v_exp_f32 v72, v72\n"
        "v_mfma_f32_16x16x32_bf16 v[68:71], a[212:215], v[16:19], v[68:71]\n"
        "s_waitcnt lgkmcnt(1)\n"
        "ds_read_b128 v[16:19], v0 offset:5632\n"
        "v_mfma_f32_16x16x32_bf16 v[48:51], a[216:219], v[8:11], v[48:51]\n"
        "v_exp_f32 v73, v73\n"
        "v_mfma_f32_16x16x32_bf16 v[52:55], a[220:223], v[8:11], v[52:55]\n"
        "v_exp_f32 v74, v74\n"
        "v_mfma_f32_16x16x32_bf16 v[56:59], a[224:227], v[8:11], v[56:59]\n"
        "v_exp_f32 v75, v75\n"
        "v_mfma_f32_16x16x32_bf16 v[60:63], a[228:231], v[8:11], v[60:63]\n"
        "v_mul_f32 v40, v40, v44\n"
        "v_mul_f32 v41, v41, v45\n"
        "v_mfma_f32_16x16x32_bf16 v[64:67], a[232:235], v[8:11], v[64:67]\n"
        "v_mul_f32 v42, v42, v46\n"
        "v_mul_f32 v43, v43, v47\n"
        "v_mfma_f32_16x16x32_bf16 v[68:71], a[236:239], v[8:11], v[68:71]\n"
        "s_waitcnt lgkmcnt(1)\n"
        "ds_read_b128 v[8:11], v0 offset:6144\n"
        "v_add_f32 v72, 1.0, v72\n"
        "v_mfma_f32_16x16x32_bf16 v[48:51], a[240:243], v[12:15], v[48:51]\n"
        "v_add_f32 v73, 1.0, v73\n"
        "v_add_f32 v74, 1.0, v74\n"
        "v_mfma_f32_16x16x32_bf16 v[52:55], a[244:247], v[12:15], v[52:55]\n"
        "v_add_f32 v75, 1.0, v75\n"
        "v_mfma_f32_16x16x32_bf16 v[56:59], a[248:251], v[12:15], v[56:59]\n"
        "v_rcp_f32 v72, v72\n"
        "v_mfma_f32_16x16x32_bf16 v[60:63], a[252:255], v[12:15], v[60:63]\n"
        "v_rcp_f32 v73, v73\n"
        "v_mfma_f32_16x16x32_bf16 v[64:67], v[80:83], v[12:15], v[64:67]\n"
        "v_rcp_f32 v74, v74\n"
        "v_mfma_f32_16x16x32_bf16 v[68:71], v[84:87], v[12:15], v[68:71]\n"
        "s_waitcnt lgkmcnt(1)\n"
        "ds_read_b128 v[12:15], v0 offset:6656\n"
        "v_mfma_f32_16x16x32_bf16 v[48:51], v[88:91], v[16:19], v[48:51]\n"
        "v_rcp_f32 v75, v75\n"
        "v_mfma_f32_16x16x32_bf16 v[52:55], v[92:95], v[16:19], v[52:55]\n"
        "v_mul_f32 v40, v40, v72\n"
        "v_mul_f32 v41, v41, v73\n"
        "v_mfma_f32_16x16x32_bf16 v[56:59], v[96:99], v[16:19], v[56:59]\n"
        "v_mul_f32 v42, v42, v74\n"
        "v_mul_f32 v43, v43, v75\n"
        "v_mfma_f32_16x16x32_bf16 v[60:63], v[100:103], v[16:19], v[60:63]\n"
        "v_cvt_pk_bf16_f32 v44, v40, v41\n"
        "v_cvt_pk_bf16_f32 v45, v42, v43\n"
        "v_mfma_f32_16x16x32_bf16 v[64:67], v[104:107], v[16:19], v[64:67]\n"
        "s_nop 0\n"
        "v_permlane16_swap_b32 v44, v46\n"
        "v_mfma_f32_16x16x32_bf16 v[68:71], v[108:111], v[16:19], v[68:71]\n"
        "s_waitcnt lgkmcnt(1)\n"
        "ds_read_b128 v[16:19], v0 offset:7168\n"
        "v_mfma_f32_16x16x32_bf16 v[48:51], v[112:115], v[8:11], v[48:51]\n"
        "v_permlane16_swap_b32 v45, v47\n"
        "v_mfma_f32_16x16x32_bf16 v[52:55], v[116:119], v[8:11], v[52:55]\n"
        "buffer_store_dwordx4 v[44:47], v7, s[40:43], s83 offen offset:0 sc1\n"
        "v_mfma_f32_16x16x32_bf16 v[56:59], v[120:123], v[8:11], v[56:59]\n"
        "v_mov_b32 v5, s74\n"
        "v_mfma_f32_16x16x32_bf16 v[60:63], v[124:127], v[8:11], v[60:63]\n"
        "s_add_u32 s74, s74, 1\n"
        "v_mfma_f32_16x16x32_bf16 v[64:67], v[128:131], v[8:11], v[64:67]\n"
        "s_add_u32 s72, s75, s59\n"
        "v_mfma_f32_16x16x32_bf16 v[68:71], v[132:135], v[8:11], v[68:71]\n"
        "s_waitcnt lgkmcnt(1)\n"
        "ds_read_b128 v[8:11], v0 offset:7680\n"
        "v_mfma_f32_16x16x32_bf16 v[48:51], v[136:139], v[12:15], v[48:51]\n"
        "s_add_u32 s83, s75, s63\n"
        "v_mfma_f32_16x16x32_bf16 v[52:55], v[140:143], v[12:15], v[52:55]\n"
        "s_add_u32 s72, s72, 49152\n"
        "v_mfma_f32_16x16x32_bf16 v[56:59], v[144:147], v[12:15], v[56:59]\n"
        "s_add_u32 s83, s83, 4096\n"
        "v_mfma_f32_16x16x32_bf16 v[60:63], v[148:151], v[12:15], v[60:63]\n"
        "v_mfma_f32_16x16x32_bf16 v[64:67], v[152:155], v[12:15], v[64:67]\n"
        "v_mfma_f32_16x16x32_bf16 v[68:71], v[156:159], v[12:15], v[68:71]\n"
        "s_waitcnt lgkmcnt(1)\n"
        "ds_read_b128 v[12:15], v0 offset:8192\n"
        "v_mfma_f32_16x16x32_bf16 v[48:51], v[160:163], v[16:19], v[48:51]\n"
        "v_mfma_f32_16x16x32_bf16 v[52:55], v[164:167], v[16:19], v[52:55]\n"
        "v_mfma_f32_16x16x32_bf16 v[56:59], v[168:171], v[16:19], v[56:59]\n"
        "v_mfma_f32_16x16x32_bf16 v[60:63], v[172:175], v[16:19], v[60:63]\n"
        "v_mfma_f32_16x16x32_bf16 v[64:67], v[176:179], v[16:19], v[64:67]\n"
        "v_mfma_f32_16x16x32_bf16 v[68:71], v[180:183], v[16:19], v[68:71]\n"
        "s_waitcnt lgkmcnt(1)\n"
        "ds_read_b128 v[16:19], v0 offset:8704\n"
        "v_mfma_f32_16x16x32_bf16 v[48:51], v[184:187], v[8:11], v[48:51]\n"
        "v_mfma_f32_16x16x32_bf16 v[52:55], v[188:191], v[8:11], v[52:55]\n"
        "v_mfma_f32_16x16x32_bf16 v[56:59], v[192:195], v[8:11], v[56:59]\n"
        "v_mfma_f32_16x16x32_bf16 v[60:63], v[196:199], v[8:11], v[60:63]\n"
        "v_mfma_f32_16x16x32_bf16 v[64:67], v[200:203], v[8:11], v[64:67]\n"
        "s_waitcnt vmcnt(2)\n"
        "s_barrier\n"
        "v_add_u32 v0, s78, v6\n"
        "v_mfma_f32_16x16x32_bf16 v[68:71], v[204:207], v[8:11], v[68:71]\n"
        "s_waitcnt lgkmcnt(1)\n"
        "ds_read_b128 v[8:11], v0 offset:0\n"
        "v_mfma_f32_16x16x32_bf16 v[48:51], v[208:211], v[12:15], v[48:51]\n"
        "v_mfma_f32_16x16x32_bf16 v[52:55], v[212:215], v[12:15], v[52:55]\n"
        "s_getreg_b32 s88, hwreg(HW_REG_IB_STS, 0, 4)\n"
        "v_mfma_f32_16x16x32_bf16 v[56:59], v[216:219], v[12:15], v[56:59]\n"
        "s_cmp_le_u32 s88, 0\n"
        "s_cselect_b32 s88, s73, s89\n"
        "s_cselect_b32 s73, s89, s73\n"
        "v_mfma_f32_16x16x32_bf16 v[60:63], v[220:223], v[12:15], v[60:63]\n"
        "buffer_store_dword v5, v3, s[40:43], s88 offen sc1\n"
        "v_mfma_f32_16x16x32_bf16 v[64:67], v[224:227], v[12:15], v[64:67]\n"
        "v_mfma_f32_16x16x32_bf16 v[68:71], v[228:231], v[12:15], v[68:71]\n"
        "s_waitcnt lgkmcnt(1)\n"
        "ds_read_b128 v[12:15], v0 offset:512\n"
        "v_mfma_f32_16x16x32_bf16 v[48:51], v[232:235], v[16:19], v[48:51]\n"
        "v_mfma_f32_16x16x32_bf16 v[52:55], v[236:239], v[16:19], v[52:55]\n"
        "v_mfma_f32_16x16x32_bf16 v[56:59], v[240:243], v[16:19], v[56:59]\n"
        "s_getreg_b32 s88, hwreg(HW_REG_IB_STS, 0, 4)\n"
        "v_mfma_f32_16x16x32_bf16 v[60:63], v[244:247], v[16:19], v[60:63]\n"
        "s_cmp_le_u32 s88, 1\n"
        "s_cselect_b32 s88, s73, s89\n"
        "s_cselect_b32 s73, s89, s73\n"
        "v_mfma_f32_16x16x32_bf16 v[64:67], v[248:251], v[16:19], v[64:67]\n"
        "buffer_store_dword v5, v3, s[40:43], s88 offen sc1\n"
        "v_mfma_f32_16x16x32_bf16 v[68:71], v[252:255], v[16:19], v[68:71]\n"
        "s_waitcnt lgkmcnt(1)\n"
        "ds_read_b128 v[16:19], v0 offset:1024\n"
        "s_mov_b32 s84, s73\n"
        "s_mov_b32 s73, s60\n"
        "s_branch .Lf1_i0_%=\n"
        ".Lf1_i10_%=:\n"
        "v_mfma_f32_16x16x32_bf16 v[56:59], a[128:131], v[16:19], v[56:59]\n"
        "buffer_load_dword v4, v2, s[40:43], s86 offen sc1\n"
        "v_mfma_f32_16x16x32_bf16 v[60:63], a[132:135], v[16:19], v[60:63]\n"
        "v_mul_f32 v35, v35, v39\n"
        "v_add_f32 v76, 1.0, v76\n"
        "v_mfma_f32_16x16x32_bf16 v[64:67], a[136:139], v[16:19], v[64:67]\n"
        "v_add_f32 v77, 1.0, v77\n"
        "v_add_f32 v78, 1.0, v78\n"
        "v_mfma_f32_16x16x32_bf16 v[68:71], a[140:143], v[16:19], v[68:71]\n"
        "s_waitcnt lgkmcnt(1)\n"
        "ds_read_b128 v[16:19], v0 offset:4096\n"
        "v_add_f32 v79, 1.0, v79\n"
        "v_mfma_f32_16x16x32_bf16 v[48:51], a[144:147], v[8:11], v[48:51]\n"
        "v_rcp_f32 v76, v76\n"
        "v_mfma_f32_16x16x32_bf16 v[52:55], a[148:151], v[8:11], v[52:55]\n"
        "v_rcp_f32 v77, v77\n"
        "v_mfma_f32_16x16x32_bf16 v[56:59], a[152:155], v[8:11], v[56:59]\n"
        "v_rcp_f32 v78, v78\n"
        "v_mfma_f32_16x16x32_bf16 v[60:63], a[156:159], v[8:11], v[60:63]\n"
        "v_rcp_f32 v79, v79\n"
        "v_mfma_f32_16x16x32_bf16 v[64:67], a[160:163], v[8:11], v[64:67]\n"
        "v_mul_f32 v32, v32, v76\n"
        "v_mul_f32 v33, v33, v77\n"
        "v_mfma_f32_16x16x32_bf16 v[68:71], a[164:167], v[8:11], v[68:71]\n"
        "s_waitcnt lgkmcnt(1)\n"
        "ds_read_b128 v[8:11], v0 offset:4608\n"
        "v_mul_f32 v34, v34, v78\n"
        "v_mfma_f32_16x16x32_bf16 v[48:51], a[168:171], v[12:15], v[48:51]\n"
        "v_mul_f32 v35, v35, v79\n"
        "v_cvt_pk_bf16_f32 v30, v32, v33\n"
        "v_mfma_f32_16x16x32_bf16 v[52:55], a[172:175], v[12:15], v[52:55]\n"
        "v_cvt_pk_bf16_f32 v31, v34, v35\n"
        "v_mul_f32 v72, s80, v40\n"
        "v_mfma_f32_16x16x32_bf16 v[56:59], a[176:179], v[12:15], v[56:59]\n"
        "buffer_store_dwordx4 v[28:31], v1, s[40:43], s72 offen offset:0 sc1\n"
        "v_mfma_f32_16x16x32_bf16 v[60:63], a[180:183], v[12:15], v[60:63]\n"
        "v_mul_f32 v73, s80, v41\n"
        "v_mul_f32 v74, s80, v42\n"
        "v_mfma_f32_16x16x32_bf16 v[64:67], a[184:187], v[12:15], v[64:67]\n"
        "v_mul_f32 v75, s80, v43\n"
        "v_mfma_f32_16x16x32_bf16 v[68:71], a[188:191], v[12:15], v[68:71]\n"
        "s_waitcnt lgkmcnt(1)\n"
        "ds_read_b128 v[12:15], v0 offset:5120\n"
        "v_mfma_f32_16x16x32_bf16 v[48:51], a[192:195], v[16:19], v[48:51]\n"
        "v_exp_f32 v72, v72\n"
        "v_mfma_f32_16x16x32_bf16 v[52:55], a[196:199], v[16:19], v[52:55]\n"
        "v_exp_f32 v73, v73\n"
        "v_mfma_f32_16x16x32_bf16 v[56:59], a[200:203], v[16:19], v[56:59]\n"
        "v_exp_f32 v74, v74\n"
        "v_mfma_f32_16x16x32_bf16 v[60:63], a[204:207], v[16:19], v[60:63]\n"
        "v_exp_f32 v75, v75\n"
        "v_mfma_f32_16x16x32_bf16 v[64:67], a[208:211], v[16:19], v[64:67]\n"
        "v_mul_f32 v40, v40, v44\n"
        "v_mul_f32 v41, v41, v45\n"
        "v_mfma_f32_16x16x32_bf16 v[68:71], a[212:215], v[16:19], v[68:71]\n"
        "s_waitcnt lgkmcnt(1)\n"
        "ds_read_b128 v[16:19], v0 offset:5632\n"
        "v_mul_f32 v42, v42, v46\n"
        "v_mfma_f32_16x16x32_bf16 v[48:51], a[216:219], v[8:11], v[48:51]\n"
        "v_mul_f32 v43, v43, v47\n"
        "v_add_f32 v72, 1.0, v72\n"
        "v_mfma_f32_16x16x32_bf16 v[52:55], a[220:223], v[8:11], v[52:55]\n"
        "v_add_f32 v73, 1.0, v73\n"
        "v_add_f32 v74, 1.0, v74\n"
        "v_mfma_f32_16x16x32_bf16 v[56:59], a[224:227], v[8:11], v[56:59]\n"
        "v_add_f32 v75, 1.0, v75\n"
        "v_mfma_f32_16x16x32_bf16 v[60:63], a[228:231], v[8:11], v[60:63]\n"
        "v_rcp_f32 v72, v72\n"
        "v_mfma_f32_16x16x32_bf16 v[64:67], a[232:235], v[8:11], v[64:67]\n"
        "v_rcp_f32 v73, v73\n"
        "v_mfma_f32_16x16x32_bf16 v[68:71], a[236:239], v[8:11], v[68:71]\n"
        "s_waitcnt lgkmcnt(1)\n"
        "ds_read_b128 v[8:11], v0 offset:6144\n"
        "s_waitcnt vmcnt(1)\n"
        "v_cmp_gt_i32 vcc, s70, v4\n"
        "v_mfma_f32_16x16x32_bf16 v[48:51], a[240:243], v[12:15], v[48:51]\n"
        "v_rcp_f32 v74, v74\n"
        "v_mfma_f32_16x16x32_bf16 v[52:55], a[244:247], v[12:15], v[52:55]\n"
        "s_and_b64 vcc, vcc, 1\n"
        "v_mfma_f32_16x16x32_bf16 v[56:59], a[248:251], v[12:15], v[56:59]\n"
        "s_cbranch_vccnz .Lf1_i11_%=\n"
        "v_mfma_f32_16x16x32_bf16 v[60:63], a[252:255], v[12:15], v[60:63]\n"
        "buffer_load_dwordx4 v1, s[40:43], s71 offen offset:0 sc1 lds\n"
        "v_mfma_f32_16x16x32_bf16 v[64:67], v[80:83], v[12:15], v[64:67]\n"
        "v_rcp_f32 v75, v75\n"
        "v_mfma_f32_16x16x32_bf16 v[68:71], v[84:87], v[12:15], v[68:71]\n"
        "s_waitcnt lgkmcnt(1)\n"
        "ds_read_b128 v[12:15], v0 offset:6656\n"
        "v_mul_f32 v40, v40, v72\n"
        "v_mfma_f32_16x16x32_bf16 v[48:51], v[88:91], v[16:19], v[48:51]\n"
        "buffer_load_dwordx4 v1, s[40:43], s71 offen offset:1024 sc1 lds\n"
        "v_mfma_f32_16x16x32_bf16 v[52:55], v[92:95], v[16:19], v[52:55]\n"
        "v_mul_f32 v41, v41, v73\n"
        "v_mul_f32 v42, v42, v74\n"
        "v_mfma_f32_16x16x32_bf16 v[56:59], v[96:99], v[16:19], v[56:59]\n"
        "v_mul_f32 v43, v43, v75\n"
        "v_cvt_pk_bf16_f32 v44, v40, v41\n"
        "v_mfma_f32_16x16x32_bf16 v[60:63], v[100:103], v[16:19], v[60:63]\n"
        "buffer_load_dwordx4 v1, s[40:43], s71 offen offset:2048 sc1 lds\n"
        "v_mfma_f32_16x16x32_bf16 v[64:67], v[104:107], v[16:19], v[64:67]\n"
        "v_cvt_pk_bf16_f32 v45, v42, v43\n"
        "s_nop 0\n"
        "v_mfma_f32_16x16x32_bf16 v[68:71], v[108:111], v[16:19], v[68:71]\n"
        "s_waitcnt lgkmcnt(1)\n"
        "ds_read_b128 v[16:19], v0 offset:7168\n"
        "v_mfma_f32_16x16x32_bf16 v[48:51], v[112:115], v[8:11], v[48:51]\n"
        "buffer_load_dwordx4 v1, s[40:43], s71 offen offset:3072 sc1 lds\n"
        "v_mfma_f32_16x16x32_bf16 v[52:55], v[116:119], v[8:11], v[52:55]\n"
        "s_add_u32 m0, s78, s85\n"
        "s_add_u32 s69, s69, 1\n"
        "v_mfma_f32_16x16x32_bf16 v[56:59], v[120:123], v[8:11], v[56:59]\n"
        "v_permlane16_swap_b32 v44, v46\n"
        "v_mfma_f32_16x16x32_bf16 v[60:63], v[124:127], v[8:11], v[60:63]\n"
        "buffer_load_dwordx4 v1, s[40:43], s81 offen offset:0 sc1 lds\n"
        "v_mfma_f32_16x16x32_bf16 v[64:67], v[128:131], v[8:11], v[64:67]\n"
        "v_permlane16_swap_b32 v45, v47\n"
        "v_mfma_f32_16x16x32_bf16 v[68:71], v[132:135], v[8:11], v[68:71]\n"
        "s_waitcnt lgkmcnt(1)\n"
        "ds_read_b128 v[8:11], v0 offset:7680\n"
        "v_mfma_f32_16x16x32_bf16 v[48:51], v[136:139], v[12:15], v[48:51]\n"
        "buffer_store_dwordx4 v[44:47], v7, s[40:43], s83 offen offset:0 sc1\n"
        "v_mfma_f32_16x16x32_bf16 v[52:55], v[140:143], v[12:15], v[52:55]\n"
        "v_mov_b32 v5, s74\n"
        "v_mfma_f32_16x16x32_bf16 v[56:59], v[144:147], v[12:15], v[56:59]\n"
        "s_add_u32 s74, s74, 1\n"
        "v_mfma_f32_16x16x32_bf16 v[60:63], v[148:151], v[12:15], v[60:63]\n"
        "s_add_u32 s72, s75, s59\n"
        "v_mfma_f32_16x16x32_bf16 v[64:67], v[152:155], v[12:15], v[64:67]\n"
        "s_add_u32 s83, s75, s63\n"
        "v_mfma_f32_16x16x32_bf16 v[68:71], v[156:159], v[12:15], v[68:71]\n"
        "s_waitcnt lgkmcnt(1)\n"
        "ds_read_b128 v[12:15], v0 offset:8192\n"
        "v_mfma_f32_16x16x32_bf16 v[48:51], v[160:163], v[16:19], v[48:51]\n"
        "s_add_u32 s72, s72, 49152\n"
        "v_mfma_f32_16x16x32_bf16 v[52:55], v[164:167], v[16:19], v[52:55]\n"
        "s_add_u32 s83, s83, 4096\n"
        "v_mfma_f32_16x16x32_bf16 v[56:59], v[168:171], v[16:19], v[56:59]\n"
        "v_mfma_f32_16x16x32_bf16 v[60:63], v[172:175], v[16:19], v[60:63]\n"
        "v_mfma_f32_16x16x32_bf16 v[64:67], v[176:179], v[16:19], v[64:67]\n"
        "v_mfma_f32_16x16x32_bf16 v[68:71], v[180:183], v[16:19], v[68:71]\n"
        "s_waitcnt lgkmcnt(1)\n"
        "ds_read_b128 v[16:19], v0 offset:8704\n"
        "v_mfma_f32_16x16x32_bf16 v[48:51], v[184:187], v[8:11], v[48:51]\n"
        "v_mfma_f32_16x16x32_bf16 v[52:55], v[188:191], v[8:11], v[52:55]\n"
        "v_mfma_f32_16x16x32_bf16 v[56:59], v[192:195], v[8:11], v[56:59]\n"
        "v_mfma_f32_16x16x32_bf16 v[60:63], v[196:199], v[8:11], v[60:63]\n"
        "v_mfma_f32_16x16x32_bf16 v[64:67], v[200:203], v[8:11], v[64:67]\n"
        "s_waitcnt vmcnt(1)\n"
        "s_barrier\n"
        "v_add_u32 v0, s78, v6\n"
        "v_mfma_f32_16x16x32_bf16 v[68:71], v[204:207], v[8:11], v[68:71]\n"
        "s_waitcnt lgkmcnt(1)\n"
        "ds_read_b128 v[8:11], v0 offset:0\n"
        "v_mfma_f32_16x16x32_bf16 v[48:51], v[208:211], v[12:15], v[48:51]\n"
        "v_mfma_f32_16x16x32_bf16 v[52:55], v[212:215], v[12:15], v[52:55]\n"
        "v_mfma_f32_16x16x32_bf16 v[56:59], v[216:219], v[12:15], v[56:59]\n"
        "s_getreg_b32 s88, hwreg(HW_REG_IB_STS, 0, 4)\n"
        "v_mfma_f32_16x16x32_bf16 v[60:63], v[220:223], v[12:15], v[60:63]\n"
        "s_cmp_le_u32 s88, 0\n"
        "s_cselect_b32 s88, s73, s89\n"
        "s_cselect_b32 s73, s89, s73\n"
        "v_mfma_f32_16x16x32_bf16 v[64:67], v[224:227], v[12:15], v[64:67]\n"
        "buffer_store_dword v5, v3, s[40:43], s88 offen sc1\n"
        "v_mfma_f32_16x16x32_bf16 v[68:71], v[228:231], v[12:15], v[68:71]\n"
        "s_waitcnt lgkmcnt(1)\n"
        "ds_read_b128 v[12:15], v0 offset:512\n"
        "v_mfma_f32_16x16x32_bf16 v[48:51], v[232:235], v[16:19], v[48:51]\n"
        "v_mfma_f32_16x16x32_bf16 v[52:55], v[236:239], v[16:19], v[52:55]\n"
        "v_mfma_f32_16x16x32_bf16 v[56:59], v[240:243], v[16:19], v[56:59]\n"
        "s_getreg_b32 s88, hwreg(HW_REG_IB_STS, 0, 4)\n"
        "v_mfma_f32_16x16x32_bf16 v[60:63], v[244:247], v[16:19], v[60:63]\n"
        "s_cmp_le_u32 s88, 1\n"
        "s_cselect_b32 s88, s73, s89\n"
        "s_cselect_b32 s73, s89, s73\n"
        "v_mfma_f32_16x16x32_bf16 v[64:67], v[248:251], v[16:19], v[64:67]\n"
        "buffer_store_dword v5, v3, s[40:43], s88 offen sc1\n"
        "v_mfma_f32_16x16x32_bf16 v[68:71], v[252:255], v[16:19], v[68:71]\n"
        "s_waitcnt lgkmcnt(1)\n"
        "ds_read_b128 v[16:19], v0 offset:1024\n"
        "s_mov_b32 s84, s73\n"
        "s_mov_b32 s73, s60\n"
        "s_branch .Lf1_i0_%=\n"
        ".Lf1_i11_%=:\n"
        "v_mfma_f32_16x16x32_bf16 v[60:63], a[252:255], v[12:15], v[60:63]\n"
        "buffer_load_dword v4, v2, s[40:43], s86 offen sc1\n"
        "v_mfma_f32_16x16x32_bf16 v[64:67], v[80:83], v[12:15], v[64:67]\n"
        "v_rcp_f32 v75, v75\n"
        "v_mfma_f32_16x16x32_bf16 v[68:71], v[84:87], v[12:15], v[68:71]\n"
        "s_waitcnt lgkmcnt(1)\n"
        "ds_read_b128 v[12:15], v0 offset:6656\n"
        "v_mul_f32 v40, v40, v72\n"
        "v_mfma_f32_16x16x32_bf16 v[48:51], v[88:91], v[16:19], v[48:51]\n"
        "v_mul_f32 v41, v41, v73\n"
        "v_mul_f32 v42, v42, v74\n"
        "v_mfma_f32_16x16x32_bf16 v[52:55], v[92:95], v[16:19], v[52:55]\n"
        "v_mul_f32 v43, v43, v75\n"
        "v_cvt_pk_bf16_f32 v44, v40, v41\n"
        "v_mfma_f32_16x16x32_bf16 v[56:59], v[96:99], v[16:19], v[56:59]\n"
        "v_cvt_pk_bf16_f32 v45, v42, v43\n"
        "s_nop 0\n"
        "v_mfma_f32_16x16x32_bf16 v[60:63], v[100:103], v[16:19], v[60:63]\n"
        "v_permlane16_swap_b32 v44, v46\n"
        "v_mfma_f32_16x16x32_bf16 v[64:67], v[104:107], v[16:19], v[64:67]\n"
        "v_permlane16_swap_b32 v45, v47\n"
        "v_mfma_f32_16x16x32_bf16 v[68:71], v[108:111], v[16:19], v[68:71]\n"
        "s_waitcnt lgkmcnt(1)\n"
        "ds_read_b128 v[16:19], v0 offset:7168\n"
        "v_mfma_f32_16x16x32_bf16 v[48:51], v[112:115], v[8:11], v[48:51]\n"
        "buffer_store_dwordx4 v[44:47], v7, s[40:43], s83 offen offset:0 sc1\n"
        "v_mfma_f32_16x16x32_bf16 v[52:55], v[116:119], v[8:11], v[52:55]\n"
        "v_mov_b32 v5, s74\n"
        "v_mfma_f32_16x16x32_bf16 v[56:59], v[120:123], v[8:11], v[56:59]\n"
        "s_add_u32 s74, s74, 1\n"
        "v_mfma_f32_16x16x32_bf16 v[60:63], v[124:127], v[8:11], v[60:63]\n"
        "s_add_u32 s72, s75, s59\n"
        "v_mfma_f32_16x16x32_bf16 v[64:67], v[128:131], v[8:11], v[64:67]\n"
        "s_add_u32 s83, s75, s63\n"
        "v_mfma_f32_16x16x32_bf16 v[68:71], v[132:135], v[8:11], v[68:71]\n"
        "s_waitcnt lgkmcnt(1)\n"
        "ds_read_b128 v[8:11], v0 offset:7680\n"
        "v_mfma_f32_16x16x32_bf16 v[48:51], v[136:139], v[12:15], v[48:51]\n"
        "s_add_u32 s72, s72, 49152\n"
        "v_mfma_f32_16x16x32_bf16 v[52:55], v[140:143], v[12:15], v[52:55]\n"
        "s_add_u32 s83, s83, 4096\n"
        "v_mfma_f32_16x16x32_bf16 v[56:59], v[144:147], v[12:15], v[56:59]\n"
        "v_mfma_f32_16x16x32_bf16 v[60:63], v[148:151], v[12:15], v[60:63]\n"
        "v_mfma_f32_16x16x32_bf16 v[64:67], v[152:155], v[12:15], v[64:67]\n"
        "v_mfma_f32_16x16x32_bf16 v[68:71], v[156:159], v[12:15], v[68:71]\n"
        "s_waitcnt lgkmcnt(1)\n"
        "ds_read_b128 v[12:15], v0 offset:8192\n"
        "v_mfma_f32_16x16x32_bf16 v[48:51], v[160:163], v[16:19], v[48:51]\n"
        "v_mfma_f32_16x16x32_bf16 v[52:55], v[164:167], v[16:19], v[52:55]\n"
        "v_mfma_f32_16x16x32_bf16 v[56:59], v[168:171], v[16:19], v[56:59]\n"
        "v_mfma_f32_16x16x32_bf16 v[60:63], v[172:175], v[16:19], v[60:63]\n"
        "v_mfma_f32_16x16x32_bf16 v[64:67], v[176:179], v[16:19], v[64:67]\n"
        "v_mfma_f32_16x16x32_bf16 v[68:71], v[180:183], v[16:19], v[68:71]\n"
        "s_waitcnt lgkmcnt(1)\n"
        "ds_read_b128 v[16:19], v0 offset:8704\n"
        "v_mfma_f32_16x16x32_bf16 v[48:51], v[184:187], v[8:11], v[48:51]\n"
        "s_waitcnt vmcnt(1)\n"
        "v_cmp_gt_i32 vcc, s70, v4\n"
        "v_mfma_f32_16x16x32_bf16 v[52:55], v[188:191], v[8:11], v[52:55]\n"
        "v_mfma_f32_16x16x32_bf16 v[56:59], v[192:195], v[8:11], v[56:59]\n"
        "s_and_b64 vcc, vcc, 1\n"
        "v_mfma_f32_16x16x32_bf16 v[60:63], v[196:199], v[8:11], v[60:63]\n"
        "s_cbranch_vccnz .Lf1_i12_%=\n"
        "v_mfma_f32_16x16x32_bf16 v[64:67], v[200:203], v[8:11], v[64:67]\n"
        "buffer_load_dwordx4 v1, s[40:43], s71 offen offset:0 sc1 lds\n"
        "v_mfma_f32_16x16x32_bf16 v[68:71], v[204:207], v[8:11], v[68:71]\n"
        "s_waitcnt lgkmcnt(1)\n"
        "v_mfma_f32_16x16x32_bf16 v[48:51], v[208:211], v[12:15], v[48:51]\n"
        "s_getreg_b32 s88, hwreg(HW_REG_IB_STS, 0, 4)\n"
        "buffer_load_dwordx4 v1, s[40:43], s71 offen offset:1024 sc1 lds\n"
        "v_mfma_f32_16x16x32_bf16 v[52:55], v[212:215], v[12:15], v[52:55]\n"
        "s_cmp_le_u32 s88, 1\n"
        "s_cselect_b32 s88, s73, s89\n"
        "s_cselect_b32 s73, s89, s73\n"
        "v_mfma_f32_16x16x32_bf16 v[56:59], v[216:219], v[12:15], v[56:59]\n"
        "buffer_load_dwordx4 v1, s[40:43], s71 offen offset:2048 sc1 lds\n"
        "v_mfma_f32_16x16x32_bf16 v[60:63], v[220:223], v[12:15], v[60:63]\n"
        "buffer_store_dword v5, v3, s[40:43], s88 offen sc1\n"
        "v_mfma_f32_16x16x32_bf16 v[64:67], v[224:227], v[12:15], v[64:67]\n"
        "buffer_load_dwordx4 v1, s[40:43], s71 offen offset:3072 sc1 lds\n"
        "v_mfma_f32_16x16x32_bf16 v[68:71], v[228:231], v[12:15], v[68:71]\n"
        "s_waitcnt lgkmcnt(0)\n"
        "s_add_u32 m0, s78, s85\n"
        "s_add_u32 s69, s69, 1\n"
        "v_mfma_f32_16x16x32_bf16 v[48:51], v[232:235], v[16:19], v[48:51]\n"
        "buffer_load_dwordx4 v1, s[40:43], s81 offen offset:0 sc1 lds\n"
        "v_mfma_f32_16x16x32_bf16 v[52:55], v[236:239], v[16:19], v[52:55]\n"
        "v_mfma_f32_16x16x32_bf16 v[56:59], v[240:243], v[16:19], v[56:59]\n"
        "s_getreg_b32 s88, hwreg(HW_REG_IB_STS, 0, 4)\n"
        "v_mfma_f32_16x16x32_bf16 v[60:63], v[244:247], v[16:19], v[60:63]\n"
        "s_cmp_le_u32 s88, 6\n"
        "s_cselect_b32 s88, s73, s89\n"
        "s_cselect_b32 s73, s89, s73\n"
        "v_mfma_f32_16x16x32_bf16 v[64:67], v[248:251], v[16:19], v[64:67]\n"
        "buffer_store_dword v5, v3, s[40:43], s88 offen sc1\n"
        "v_mfma_f32_16x16x32_bf16 v[68:71], v[252:255], v[16:19], v[68:71]\n"
        "s_mov_b32 s84, s73\n"
        "s_mov_b32 s73, s60\n"
        "v_mul_f32 v72, s80, v48\n"
        "v_mul_f32 v73, s80, v49\n"
        "v_mul_f32 v74, s80, v50\n"
        "v_mul_f32 v75, s80, v51\n"
        "v_exp_f32 v72, v72\n"
        "v_exp_f32 v73, v73\n"
        "v_exp_f32 v74, v74\n"
        "v_exp_f32 v75, v75\n"
        "v_mul_f32 v48, v48, v52\n"
        "v_mul_f32 v49, v49, v53\n"
        "v_mul_f32 v50, v50, v54\n"
        "v_mul_f32 v51, v51, v55\n"
        "v_add_f32 v72, 1.0, v72\n"
        "v_add_f32 v73, 1.0, v73\n"
        "v_add_f32 v74, 1.0, v74\n"
        "v_add_f32 v75, 1.0, v75\n"
        "v_rcp_f32 v72, v72\n"
        "v_rcp_f32 v73, v73\n"
        "v_rcp_f32 v74, v74\n"
        "v_rcp_f32 v75, v75\n"
        "v_mul_f32 v48, v48, v72\n"
        "v_mul_f32 v49, v49, v73\n"
        "v_mul_f32 v50, v50, v74\n"
        "v_mul_f32 v51, v51, v75\n"
        "v_cvt_pk_bf16_f32 v52, v48, v49\n"
        "v_cvt_pk_bf16_f32 v53, v50, v51\n"
        "s_waitcnt vmcnt(7)\n"
        "buffer_store_dword v5, v3, s[40:43], s84 offen sc1\n"
        "s_mov_b32 s84, s89\n"
        "v_mul_f32 v76, s80, v56\n"
        "v_mul_f32 v77, s80, v57\n"
        "v_mul_f32 v78, s80, v58\n"
        "v_mul_f32 v79, s80, v59\n"
        "v_exp_f32 v76, v76\n"
        "v_exp_f32 v77, v77\n"
        "v_exp_f32 v78, v78\n"
        "v_exp_f32 v79, v79\n"
        "v_mul_f32 v56, v56, v60\n"
        "v_mul_f32 v57, v57, v61\n"
        "v_mul_f32 v58, v58, v62\n"
        "v_mul_f32 v59, v59, v63\n"
        "v_add_f32 v76, 1.0, v76\n"
        "v_add_f32 v77, 1.0, v77\n"
        "v_add_f32 v78, 1.0, v78\n"
        "v_add_f32 v79, 1.0, v79\n"
        "v_rcp_f32 v76, v76\n"
        "v_rcp_f32 v77, v77\n"
        "v_rcp_f32 v78, v78\n"
        "v_rcp_f32 v79, v79\n"
        "v_mul_f32 v56, v56, v76\n"
        "v_mul_f32 v57, v57, v77\n"
        "v_mul_f32 v58, v58, v78\n"
        "v_mul_f32 v59, v59, v79\n"
        "v_cvt_pk_bf16_f32 v54, v56, v57\n"
        "v_cvt_pk_bf16_f32 v55, v58, v59\n"
        "buffer_store_dwordx4 v[52:55], v1, s[40:43], s72 offen offset:0 sc1\n"
        "v_mul_f32 v72, s80, v64\n"
        "v_mul_f32 v73, s80, v65\n"
        "v_mul_f32 v74, s80, v66\n"
        "v_mul_f32 v75, s80, v67\n"
        "v_exp_f32 v72, v72\n"
        "v_exp_f32 v73, v73\n"
        "v_exp_f32 v74, v74\n"
        "v_exp_f32 v75, v75\n"
        "v_mul_f32 v64, v64, v68\n"
        "v_mul_f32 v65, v65, v69\n"
        "v_mul_f32 v66, v66, v70\n"
        "v_mul_f32 v67, v67, v71\n"
        "v_add_f32 v72, 1.0, v72\n"
        "v_add_f32 v73, 1.0, v73\n"
        "v_add_f32 v74, 1.0, v74\n"
        "v_add_f32 v75, 1.0, v75\n"
        "v_rcp_f32 v72, v72\n"
        "v_rcp_f32 v73, v73\n"
        "v_rcp_f32 v74, v74\n"
        "v_rcp_f32 v75, v75\n"
        "v_mul_f32 v64, v64, v72\n"
        "v_mul_f32 v65, v65, v73\n"
        "v_mul_f32 v66, v66, v74\n"
        "v_mul_f32 v67, v67, v75\n"
        "v_cvt_pk_bf16_f32 v68, v64, v65\n"
        "v_cvt_pk_bf16_f32 v69, v66, v67\n"
        "s_nop 0\n"
        "v_permlane16_swap_b32 v68, v70\n"
        "v_permlane16_swap_b32 v69, v71\n"
        "buffer_store_dwordx4 v[68:71], v7, s[40:43], s83 offen offset:0 sc1\n"
        "v_mov_b32 v5, s74\n"
        "s_mov_b32 s84, s60\n"
        "buffer_store_dword v5, v3, s[40:43], s89 offen sc1\n"
        "buffer_store_dword v5, v3, s[40:43], s89 offen sc1\n"
        "s_waitcnt vmcnt(6)\n"
        "s_branch .Lf1_go1_%=\n"
        ".Lf1_i12_%=:\n"
        "v_mfma_f32_16x16x32_bf16 v[64:67], v[200:203], v[8:11], v[64:67]\n"
        "buffer_load_dword v4, v2, s[40:43], s86 offen sc1\n"
        "v_mfma_f32_16x16x32_bf16 v[68:71], v[204:207], v[8:11], v[68:71]\n"
        "s_waitcnt lgkmcnt(1)\n"
        "v_mfma_f32_16x16x32_bf16 v[48:51], v[208:211], v[12:15], v[48:51]\n"
        "s_getreg_b32 s88, hwreg(HW_REG_IB_STS, 0, 4)\n"
        "v_mfma_f32_16x16x32_bf16 v[52:55], v[212:215], v[12:15], v[52:55]\n"
        "s_cmp_le_u32 s88, 1\n"
        "s_cselect_b32 s88, s73, s89\n"
        "s_cselect_b32 s73, s89, s73\n"
        "v_mfma_f32_16x16x32_bf16 v[56:59], v[216:219], v[12:15], v[56:59]\n"
        "buffer_store_dword v5, v3, s[40:43], s88 offen sc1\n"
        "v_mfma_f32_16x16x32_bf16 v[60:63], v[220:223], v[12:15], v[60:63]\n"
        "v_mfma_f32_16x16x32_bf16 v[64:67], v[224:227], v[12:15], v[64:67]\n"
        "v_mfma_f32_16x16x32_bf16 v[68:71], v[228:231], v[12:15], v[68:71]\n"
        "s_waitcnt lgkmcnt(0)\n"
        "v_mfma_f32_16x16x32_bf16 v[48:51], v[232:235], v[16:19], v[48:51]\n"
        "v_mfma_f32_16x16x32_bf16 v[52:55], v[236:239], v[16:19], v[52:55]\n"
        "v_mfma_f32_16x16x32_bf16 v[56:59], v[240:243], v[16:19], v[56:59]\n"
        "s_getreg_b32 s88, hwreg(HW_REG_IB_STS, 0, 4)\n"
        "v_mfma_f32_16x16x32_bf16 v[60:63], v[244:247], v[16:19], v[60:63]\n"
        "s_cmp_le_u32 s88, 2\n"
        "s_cselect_b32 s88, s73, s89\n"
        "s_cselect_b32 s73, s89, s73\n"
        "v_mfma_f32_16x16x32_bf16 v[64:67], v[248:251], v[16:19], v[64:67]\n"
        "buffer_store_dword v5, v3, s[40:43], s88 offen sc1\n"
        "v_mfma_f32_16x16x32_bf16 v[68:71], v[252:255], v[16:19], v[68:71]\n"
        "s_mov_b32 s84, s73\n"
        "s_mov_b32 s73, s60\n"
        "v_mul_f32 v72, s80, v48\n"
        "v_mul_f32 v73, s80, v49\n"
        "v_mul_f32 v74, s80, v50\n"
        "v_mul_f32 v75, s80, v51\n"
        "v_exp_f32 v72, v72\n"
        "v_exp_f32 v73, v73\n"
        "v_exp_f32 v74, v74\n"
        "v_exp_f32 v75, v75\n"
        "v_mul_f32 v48, v48, v52\n"
        "v_mul_f32 v49, v49, v53\n"
        "v_mul_f32 v50, v50, v54\n"
        "v_mul_f32 v51, v51, v55\n"
        "v_add_f32 v72, 1.0, v72\n"
        "v_add_f32 v73, 1.0, v73\n"
        "v_add_f32 v74, 1.0, v74\n"
        "v_add_f32 v75, 1.0, v75\n"
        "v_rcp_f32 v72, v72\n"
        "v_rcp_f32 v73, v73\n"
        "v_rcp_f32 v74, v74\n"
        "v_rcp_f32 v75, v75\n"
        "v_mul_f32 v48, v48, v72\n"
        "v_mul_f32 v49, v49, v73\n"
        "v_mul_f32 v50, v50, v74\n"
        "v_mul_f32 v51, v51, v75\n"
        "v_cvt_pk_bf16_f32 v52, v48, v49\n"
        "v_cvt_pk_bf16_f32 v53, v50, v51\n"
        "s_waitcnt vmcnt(3)\n"
        "buffer_store_dword v5, v3, s[40:43], s84 offen sc1\n"
        "s_mov_b32 s84, s89\n"
        "v_mul_f32 v76, s80, v56\n"
        "v_mul_f32 v77, s80, v57\n"
        "v_mul_f32 v78, s80, v58\n"
        "v_mul_f32 v79, s80, v59\n"
        "v_exp_f32 v76, v76\n"
        "v_exp_f32 v77, v77\n"
        "v_exp_f32 v78, v78\n"
        "v_exp_f32 v79, v79\n"
        "v_mul_f32 v56, v56, v60\n"
        "v_mul_f32 v57, v57, v61\n"
        "v_mul_f32 v58, v58, v62\n"
        "v_mul_f32 v59, v59, v63\n"
        "v_add_f32 v76, 1.0, v76\n"
        "v_add_f32 v77, 1.0, v77\n"
        "v_add_f32 v78, 1.0, v78\n"
        "v_add_f32 v79, 1.0, v79\n"
        "v_rcp_f32 v76, v76\n"
        "v_rcp_f32 v77, v77\n"
        "v_rcp_f32 v78, v78\n"
        "v_rcp_f32 v79, v79\n"
        "v_mul_f32 v56, v56, v76\n"
        "v_mul_f32 v57, v57, v77\n"
        "v_mul_f32 v58, v58, v78\n"
        "v_mul_f32 v59, v59, v79\n"
        "v_cvt_pk_bf16_f32 v54, v56, v57\n"
        "v_cvt_pk_bf16_f32 v55, v58, v59\n"
        "buffer_store_dwordx4 v[52:55], v1, s[40:43], s72 offen offset:0 sc1\n"
        "v_mul_f32 v72, s80, v64\n"
        "v_mul_f32 v73, s80, v65\n"
        "v_mul_f32 v74, s80, v66\n"
        "v_mul_f32 v75, s80, v67\n"
        "v_exp_f32 v72, v72\n"
        "v_exp_f32 v73, v73\n"
        "v_exp_f32 v74, v74\n"
        "v_exp_f32 v75, v75\n"
        "v_mul_f32 v64, v64, v68\n"
        "v_mul_f32 v65, v65, v69\n"
        "v_mul_f32 v66, v66, v70\n"
        "v_mul_f32 v67, v67, v71\n"
        "v_add_f32 v72, 1.0, v72\n"
        "v_add_f32 v73, 1.0, v73\n"
        "v_add_f32 v74, 1.0, v74\n"
        "v_add_f32 v75, 1.0, v75\n"
        "v_rcp_f32 v72, v72\n"
        "v_rcp_f32 v73, v73\n"
        "v_rcp_f32 v74, v74\n"
        "v_rcp_f32 v75, v75\n"
        "v_mul_f32 v64, v64, v72\n"
        "v_mul_f32 v65, v65, v73\n"
        "v_mul_f32 v66, v66, v74\n"
        "v_mul_f32 v67, v67, v75\n"
        "v_cvt_pk_bf16_f32 v68, v64, v65\n"
        "v_cvt_pk_bf16_f32 v69, v66, v67\n"
        "s_nop 0\n"
        "v_permlane16_swap_b32 v68, v70\n"
        "v_permlane16_swap_b32 v69, v71\n"
        "buffer_store_dwordx4 v[68:71], v7, s[40:43], s83 offen offset:0 sc1\n"
        "s_waitcnt vmcnt(5)\n"
        "v_cmp_gt_i32 vcc, s70, v4\n"
        "s_nop 0\n"
        "s_and_b64 vcc, vcc, 1\n"
        "s_cbranch_vccnz .Lf1_late1_%=\n"
        "buffer_load_dwordx4 v1, s[40:43], s71 offen offset:0 sc1 lds\n"
        "buffer_load_dwordx4 v1, s[40:43], s71 offen offset:1024 sc1 lds\n"
        "buffer_load_dwordx4 v1, s[40:43], s71 offen offset:2048 sc1 lds\n"
        "buffer_load_dwordx4 v1, s[40:43], s71 offen offset:3072 sc1 lds\n"
        "s_add_u32 m0, s78, s85\n"
        "s_add_u32 s69, s69, 1\n"
        "buffer_load_dwordx4 v1, s[40:43], s81 offen offset:0 sc1 lds\n"
        "s_waitcnt vmcnt(5)\n"
        "v_mov_b32 v5, s74\n"
        "buffer_store_dword v5, v3, s[40:43], s73 offen sc1\n"
        "s_waitcnt vmcnt(1)\n"
        "s_branch .Lf1_go1_%=\n"
        ".Lf1_late1_%=:\n"
        "s_waitcnt vmcnt(0)\n"
        "v_mov_b32 v5, s74\n"
        "buffer_store_dword v5, v3, s[40:43], s73 offen sc1\n"
        "s_cmp_eq_u32 s69, 128\n"
        "s_cbranch_scc0 .Lf1_wait1_%=\n"
        "s_endpgm\n"
        ".Lf1_wait1_%=:\n"
        "s_mov_b32 s64, 0\n"
        ".Lf1_in1_top_%=:\n"
        "buffer_load_dword v4, v2, s[40:43], s86 offen sc1\n"
        "s_waitcnt vmcnt(0)\n"
        "v_cmp_gt_i32 vcc, s70, v4\n"
        "s_nop 0\n"
        "s_and_b64 vcc, vcc, 1\n"
        "s_cbranch_vccz .Lf1_in1_ok_%=\n"
        "s_add_u32 s64, s64, 1\n"
        "s_and_b32 s65, s64, 255\n"
        "s_cbranch_scc1 .Lf1_in1_go_%=\n"
        "s_mov_b32 s65, 49152\n"
        "s_cmp_lt_u32 s64, 1048576\n"
        "s_cbranch_scc1 .Lf1_in1_chk_%=\n"
        "v_mov_b32 v4, 1\n"
        "buffer_store_dword v4, off, s[40:43], s65 sc1\n"
        "s_waitcnt vmcnt(0)\n"
        ".Lf1_in1_chk_%=:\n"
        "buffer_load_dword v4, off, s[40:43], s65 sc1\n"
        "s_waitcnt vmcnt(0)\n"
        "v_readfirstlane_b32 s65, v4\n"
        "s_nop 0\n"
        "s_cmp_lg_u32 s65, 1\n"
        "s_cbranch_scc1 .Lf1_in1_go_%=\n"
        "s_endpgm\n"
        ".Lf1_in1_go_%=:\n"
        "s_branch .Lf1_in1_top_%=\n"
        ".Lf1_in1_ok_%=:\n"
        "buffer_load_dwordx4 v1, s[40:43], s71 offen offset:0 sc1 lds\n"
        "buffer_load_dwordx4 v1, s[40:43], s71 offen offset:1024 sc1 lds\n"
        "buffer_load_dwordx4 v1, s[40:43], s71 offen offset:2048 sc1 lds\n"
        "buffer_load_dwordx4 v1, s[40:43], s71 offen offset:3072 sc1 lds\n"
        "s_add_u32 m0, s78, s85\n"
        "s_add_u32 s69, s69, 1\n"
        "buffer_load_dwordx4 v1, s[40:43], s81 offen offset:0 sc1 lds\n"
        "s_waitcnt vmcnt(0)\n"
        ".Lf1_go1_%=:\n"
        "s_mov_b32 s72, 241172480\n"
        "s_mov_b32 s83, 241172480\n"
        "s_mov_b32 s73, 241172480\n"
        "v_add_u32 v0, s78, v6\n"
        "s_barrier\n"
        "ds_read_b128 v[8:11], v0 offset:0\n"
        "ds_read_b128 v[12:15], v0 offset:512\n"
        "ds_read_b128 v[16:19], v0 offset:1024\n"
        "s_waitcnt lgkmcnt(2)\n"
        "s_branch .Lf1_i0_%=\n"
        ".Lf1_end_%=:\n"
        :: "s"(p.ws), "s"(p.w1), "s"(p.w3), "s"(p.w3), "s"(pair(w, delay)), "s"(pair(x4, pr)), TILE_ARGS(65 * PAGE + spread(B_XN1)),
           "v"(pair(lane == 0 ? flag(F_R3 + 8 * (6 * kap + jj) + w) : OOB, lane < 3 ? lane * 32768 : OOB)), "v"(pair(0, 0)), "v"(pair(0, (g & 1) ? OOB : (g >> 1) * 256 + i * 16)),
           "v"(pair(vo[0], vo[1])), "v"(pair(vo[2], w * 40960 + (4 * g + q) * 96 + 8 * pp))
        : "v0", "v1", "v2", "v3", "v4", "v5", "v6", "v7", "v8", "v9", "v10", "v11", "v12", "v13", "v14", "v15", "v16", "v17", "v18", "v19", "v20", "v21", "v22", "v23", "v24", "v25", "v26", "v27", "v28", "v29", "v30", "v31", "v32", "v33", "v34", "v35", "v36", "v37", "v38", "v39", "v40", "v41", "v42", "v43", "v44", "v45", "v46", "v47", "v48", "v49", "v50", "v51", "v52", "v53", "v54", "v55", "v56", "v57", "v58", "v59", "v60", "v61", "v62", "v63", "v64", "v65", "v66", "v67", "v68", "v69", "v70", "v71", "v72", "v73", "v74", "v75", "v76", "v77", "v78", "v79", "v255", "a255", "s26", "s27", "s28", "s29", "s30", "s31", "s32", "s33", "s34", "s35", "s36", "s37", "s38", "s39", "s40", "s41", "s42", "s43", "s44", "s45", "s46", "s47", "s48", "s49", "s50", "s51", "s52", "s53", "s54", "s55", "s56", "s57", "s58", "s59", "s60", "s61", "s62", "s63", "s64", "s65", "s66", "s67", "s68", "s69", "s70", "s71", "s72", "s73", "s74", "s75", "s76", "s77", "s78", "s79", "s80", "s81", "s82", "s83", "s84", "s85", "s86", "s87", "s88", "s89", "s90", "s91", "s92", "s93", "s94", "s95", "s96", "s97", "s98", "s99", "vcc", "scc", "memory");
    __builtin_unreachable();
}
__device__ __forceinline__ unsigned part_off(unsigned a, unsigned kap, unsigned chunk) {
    const unsigned b = (a & 1) * 4 + kap;
    return (a >> 1) * (9 * 16384) + (chunk < 64 ? b * 16384 + chunk * 128 : 8 * 16384 + b * 1024 + (chunk - 64) * 128);
}
template <bool OP>
__device__ __forceinline__ void role_k(const Params& p, int kap, int nu, int x4, int pr, int delay) {
    GEMM_HEAD;
    unsigned vo[3], vt[3];
#pragma unroll
    for (int n = 0; n < 3; ++n) {
        const unsigned ps = (64 * n + lane) / 6, gg = (ps >> 2) & 3;
        const unsigned col = (192 * nu + 48 * w) * 2 + ((64 * n + lane) % 6) * 16 - 1024 * n;
        vo[n] = ((OP ? 1024 : 1152) * kap + ps) * 1152 + col;
        vt[n] = (1152 * kap + 48 * (gg >> 1) + 32 + 8 * (gg & 1) + 4 * (ps >> 4) + (ps & 3)) * 1152 + col;
    }
    const unsigned tr = w * 40960 + (4 * g + q) * 96;
    const unsigned c0 = 24 * nu + 6 * w;
    const unsigned s01 = part_off(i >> 3, kap, c0 + g) + (i & 7) * 16, s2 = (g & 1) ? OOB : part_off(i >> 3, kap, c0 + 4 + (g >> 1)) + (i & 7) * 16;
    const unsigned fl = nu * 16 + kap * 4 + w, fo4 = lane < 4 ? lane * 16384 : OOB;
#define K_ARGS "v"(pair(0, 0)), "v"(pair(s01, s2)), "v"(pair(vo[0], vo[1])), "v"(pair(vo[2], tr + 16 * pp)), "v"(pair(vt[0], vt[1])), "v"(pair(vt[2], tr + 64 + 8 * pp))
    if constexpr (OP) {
        asm volatile(
        "s_nop 4\n"
        "s_mov_b64 s[40:41], %0\n"
        "s_and_b32 s41, s41, 0xffff\n"
        "s_mov_b32 s42, 236978176\n"
        "s_mov_b32 s43, 0x20000\n"
        "s_mov_b64 s[48:49], %4\n"
        "s_mov_b64 s[56:57], %5\n"
        "s_mov_b64 s[58:59], %6\n"
        "s_mov_b64 s[60:61], %7\n"
        "s_mov_b64 s[62:63], %8\n"
        "v_mov_b64 v[0:1], %9\n"
        "v_mov_b64 v[2:3], %10\n"
        "v_mov_b64 v[4:5], %11\n"
        "v_mov_b64 v[6:7], %12\n"
        "v_mov_b64 v[8:9], %13\n"
        "v_mov_b64 v[10:11], %14\n"
        "v_mov_b64 v[12:13], %15\n"
        "v_mov_b64 v[14:15], %16\n"
        "s_mov_b32 s47, 0x20000\n"
        "s_mul_i32 s79, s48, 40960\n"
        ".Lop_start_%=:\n"
        "s_sub_u32 s49, s49, 1\n"
        "s_cbranch_scc1 .Lop_start2_%=\n"
        "s_sleep 16\n"
        "s_branch .Lop_start_%=\n"
        ".Lop_start2_%=:\n"
        "s_mov_b64 s[44:45], %1\n"
        "s_and_b32 s45, s45, 0xffff\n"
        "s_mov_b32 s46, 4718592\n"
        "s_add_u32 m0, s79, 0\n"
        "s_mov_b32 s65, 0\n"
        "buffer_load_dwordx4 v8, s[44:47], s65 offen lds\n"
        "buffer_load_dwordx4 v9, s[44:47], s65 offen offset:1024 lds\n"
        "buffer_load_dwordx4 v10, s[44:47], s65 offen offset:2048 lds\n"
        "s_add_u32 m0, s79, 3072\n"
        "s_mov_b32 s65, 36864\n"
        "buffer_load_dwordx4 v8, s[44:47], s65 offen lds\n"
        "buffer_load_dwordx4 v9, s[44:47], s65 offen offset:1024 lds\n"
        "buffer_load_dwordx4 v10, s[44:47], s65 offen offset:2048 lds\n"
        "s_waitcnt vmcnt(3)\n"
        "ds_read_b64_tr_b16 a[0:1], v11 offset:0\n"
        "ds_read_b64_tr_b16 a[2:3], v11 offset:1536\n"
        "ds_read_b64_tr_b16 a[4:5], v11 offset:8\n"
        "ds_read_b64_tr_b16 a[6:7], v11 offset:1544\n"
        "ds_read_b64_tr_b16 a[8:9], v15 offset:0\n"
        "ds_read_b64_tr_b16 a[10:11], v15 offset:1536\n"
        "s_waitcnt vmcnt(0)\n"
        "ds_read_b64_tr_b16 a[12:13], v11 offset:3072\n"
        "ds_read_b64_tr_b16 a[14:15], v11 offset:4608\n"
        "ds_read_b64_tr_b16 a[16:17], v11 offset:3080\n"
        "ds_read_b64_tr_b16 a[18:19], v11 offset:4616\n"
        "ds_read_b64_tr_b16 a[20:21], v15 offset:3072\n"
        "ds_read_b64_tr_b16 a[22:23], v15 offset:4608\n"
        "s_waitcnt lgkmcnt(6)\n"
        "s_add_u32 m0, s79, 0\n"
        "s_mov_b32 s65, 73728\n"
        "buffer_load_dwordx4 v8, s[44:47], s65 offen lds\n"
        "buffer_load_dwordx4 v9, s[44:47], s65 offen offset:1024 lds\n"
        "buffer_load_dwordx4 v10, s[44:47], s65 offen offset:2048 lds\n"
        "s_waitcnt vmcnt(0)\n"
        "ds_read_b64_tr_b16 a[24:25], v11 offset:0\n"
        "ds_read_b64_tr_b16 a[26:27], v11 offset:1536\n"
        "ds_read_b64_tr_b16 a[28:29], v11 offset:8\n"
        "ds_read_b64_tr_b16 a[30:31], v11 offset:1544\n"
        "ds_read_b64_tr_b16 a[32:33], v15 offset:0\n"
        "ds_read_b64_tr_b16 a[34:35], v15 offset:1536\n"
        "s_waitcnt lgkmcnt(6)\n"
        "s_add_u32 m0, s79, 3072\n"
        "s_mov_b32 s65, 110592\n"
        "buffer_load_dwordx4 v8, s[44:47], s65 offen lds\n"
        "buffer_load_dwordx4 v9, s[44:47], s65 offen offset:1024 lds\n"
        "buffer_load_dwordx4 v10, s[44:47], s65 offen offset:2048 lds\n"
        "s_waitcnt vmcnt(0)\n"
        "ds_read_b64_tr_b16 a[36:37], v11 offset:3072\n"
        "ds_read_b64_tr_b16 a[38:39], v11 offset:4608\n"
        "ds_read_b64_tr_b16 a[40:41], v11 offset:3080\n"
        "ds_read_b64_tr_b16 a[42:43], v11 offset:4616\n"
        "ds_read_b64_tr_b16 a[44:45], v15 offset:3072\n"
        "ds_read_b64_tr_b16 a[46:47], v15 offset:4608\n"
        "s_waitcnt lgkmcnt(6)\n"
        "s_add_u32 m0, s79, 0\n"
        "s_mov_b32 s65, 147456\n"
        "buffer_load_dwordx4 v8, s[44:47], s65 offen lds\n"
        "buffer_load_dwordx4 v9, s[44:47], s65 offen offset:1024 lds\n"
        "buffer_load_dwordx4 v10, s[44:47], s65 offen offset:2048 lds\n"
        "s_waitcnt vmcnt(0)\n"
        "ds_read_b64_tr_b16 a[48:49], v11 offset:0\n"
        "ds_read_b64_tr_b16 a[50:51], v11 offset:1536\n"
        "ds_read_b64_tr_b16 a[52:53], v11 offset:8\n"
        "ds_read_b64_tr_b16 a[54:55], v11 offset:1544\n"
        "ds_read_b64_tr_b16 a[56:57], v15 offset:0\n"
        "ds_read_b64_tr_b16 a[58:59], v15 offset:1536\n"
        "s_waitcnt lgkmcnt(6)\n"
        "s_add_u32 m0, s79, 3072\n"
        "s_mov_b32 s65, 184320\n"
        "buffer_load_dwordx4 v8, s[44:47], s65 offen lds\n"
        "buffer_load_dwordx4 v9, s[44:47], s65 offen offset:1024 lds\n"
        "buffer_load_dwordx4 v10, s[44:47], s65 offen offset:2048 lds\n"
        "s_waitcnt vmcnt(0)\n"
        "ds_read_b64_tr_b16 a[60:61], v11 offset:3072\n"
        "ds_read_b64_tr_b16 a[62:63], v11 offset:4608\n"
        "ds_read_b64_tr_b16 a[64:65], v11 offset:3080\n"
        "ds_read_b64_tr_b16 a[66:67], v11 offset:4616\n"
        "ds_read_b64_tr_b16 a[68:69], v15 offset:3072\n"
        "ds_read_b64_tr_b16 a[70:71], v15 offset:4608\n"
        "s_waitcnt lgkmcnt(6)\n"
        "s_add_u32 m0, s79, 0\n"
        "s_mov_b32 s65, 221184\n"
        "buffer_load_dwordx4 v8, s[44:47], s65 offen lds\n"
        "buffer_load_dwordx4 v9, s[44:47], s65 offen offset:1024 lds\n"
        "buffer_load_dwordx4 v10, s[44:47], s65 offen offset:2048 lds\n"
        "s_waitcnt vmcnt(0)\n"
        "ds_read_b64_tr_b16 a[72:73], v11 offset:0\n"
        "ds_read_b64_tr_b16 a[74:75], v11 offset:1536\n"
        "ds_read_b64_tr_b16 a[76:77], v11 offset:8\n"
        "ds_read_b64_tr_b16 a[78:79], v11 offset:1544\n"
        "ds_read_b64_tr_b16 a[80:81], v15 offset:0\n"
        "ds_read_b64_tr_b16 a[82:83], v15 offset:1536\n"
        "s_waitcnt lgkmcnt(6)\n"
        "s_add_u32 m0, s79, 3072\n"
        "s_mov_b32 s65, 258048\n"
        "buffer_load_dwordx4 v8, s[44:47], s65 offen lds\n"
        "buffer_load_dwordx4 v9, s[44:47], s65 offen offset:1024 lds\n"
        "buffer_load_dwordx4 v10, s[44:47], s65 offen offset:2048 lds\n"
        "s_waitcnt vmcnt(0)\n"
        "ds_read_b64_tr_b16 a[84:85], v11 offset:3072\n"
        "ds_read_b64_tr_b16 a[86:87], v11 offset:4608\n"
        "ds_read_b64_tr_b16 a[88:89], v11 offset:3080\n"
        "ds_read_b64_tr_b16 a[90:91], v11 offset:4616\n"
        "ds_read_b64_tr_b16 a[92:93], v15 offset:3072\n"
        "ds_read_b64_tr_b16 a[94:95], v15 offset:4608\n"
        "s_waitcnt lgkmcnt(6)\n"
        "s_add_u32 m0, s79, 0\n"
        "s_mov_b32 s65, 294912\n"
        "buffer_load_dwordx4 v8, s[44:47], s65 offen lds\n"
        "buffer_load_dwordx4 v9, s[44:47], s65 offen offset:1024 lds\n"
        "buffer_load_dwordx4 v10, s[44:47], s65 offen offset:2048 lds\n"
        "s_waitcnt vmcnt(0)\n"
        "ds_read_b64_tr_b16 a[96:97], v11 offset:0\n"
        "ds_read_b64_tr_b16 a[98:99], v11 offset:1536\n"
        "ds_read_b64_tr_b16 a[100:101], v11 offset:8\n"
        "ds_read_b64_tr_b16 a[102:103], v11 offset:1544\n"
        "ds_read_b64_tr_b16 a[104:105], v15 offset:0\n"
        "ds_read_b64_tr_b16 a[106:107], v15 offset:1536\n"
        "s_waitcnt lgkmcnt(6)\n"
        "s_add_u32 m0, s79, 3072\n"
        "s_mov_b32 s65, 331776\n"
        "buffer_load_dwordx4 v8, s[44:47], s65 offen lds\n"
        "buffer_load_dwordx4 v9, s[44:47], s65 offen offset:1024 lds\n"
        "buffer_load_dwordx4 v10, s[44:47], s65 offen offset:2048 lds\n"
        "s_waitcnt vmcnt(0)\n"
        "ds_read_b64_tr_b16 a[108:109], v11 offset:3072\n"
        "ds_read_b64_tr_b16 a[110:111], v11 offset:4608\n"
        "ds_read_b64_tr_b16 a[112:113], v11 offset:3080\n"
        "ds_read_b64_tr_b16 a[114:115], v11 offset:4616\n"
        "ds_read_b64_tr_b16 a[116:117], v15 offset:3072\n"
        "ds_read_b64_tr_b16 a[118:119], v15 offset:4608\n"
        "s_waitcnt lgkmcnt(6)\n"
        "s_add_u32 m0, s79, 0\n"
        "s_mov_b32 s65, 368640\n"
        "buffer_load_dwordx4 v8, s[44:47], s65 offen lds\n"
        "buffer_load_dwordx4 v9, s[44:47], s65 offen offset:1024 lds\n"
        "buffer_load_dwordx4 v10, s[44:47], s65 offen offset:2048 lds\n"
        "s_waitcnt vmcnt(0)\n"
        "ds_read_b64_tr_b16 a[120:121], v11 offset:0\n"
        "ds_read_b64_tr_b16 a[122:123], v11 offset:1536\n"
        "ds_read_b64_tr_b16 a[124:125], v11 offset:8\n"
        "ds_read_b64_tr_b16 a[126:127], v11 offset:1544\n"
        "ds_read_b64_tr_b16 a[128:129], v15 offset:0\n"
        "ds_read_b64_tr_b16 a[130:131], v15 offset:1536\n"
        "s_waitcnt lgkmcnt(6)\n"
        "s_add_u32 m0, s79, 3072\n"
        "s_mov_b32 s65, 405504\n"
        "buffer_load_dwordx4 v8, s[44:47], s65 offen lds\n"
        "buffer_load_dwordx4 v9, s[44:47], s65 offen offset:1024 lds\n"
        "buffer_load_dwordx4 v10, s[44:47], s65 offen offset:2048 lds\n"
        "s_waitcnt vmcnt(0)\n"
        "ds_read_b64_tr_b16 a[132:133], v11 offset:3072\n"
        "ds_read_b64_tr_b16 a[134:135], v11 offset:4608\n"
        "ds_read_b64_tr_b16 a[136:137], v11 offset:3080\n"
        "ds_read_b64_tr_b16 a[138:139], v11 offset:4616\n"
        "ds_read_b64_tr_b16 a[140:141], v15 offset:3072\n"
        "ds_read_b64_tr_b16 a[142:143], v15 offset:4608\n"
        "s_waitcnt lgkmcnt(6)\n"
        "s_add_u32 m0, s79, 0\n"
        "s_mov_b32 s65, 442368\n"
        "buffer_load_dwordx4 v8, s[44:47], s65 offen lds\n"
        "buffer_load_dwordx4 v9, s[44:47], s65 offen offset:1024 lds\n"
        "buffer_load_dwordx4 v10, s[44:47], s65 offen offset:2048 lds\n"
        "s_waitcnt vmcnt(0)\n"
        "ds_read_b64_tr_b16 a[144:145], v11 offset:0\n"
        "ds_read_b64_tr_b16 a[146:147], v11 offset:1536\n"
        "ds_read_b64_tr_b16 a[148:149], v11 offset:8\n"
        "ds_read_b64_tr_b16 a[150:151], v11 offset:1544\n"
        "ds_read_b64_tr_b16 a[152:153], v15 offset:0\n"
        "ds_read_b64_tr_b16 a[154:155], v15 offset:1536\n"
        "s_waitcnt lgkmcnt(6)\n"
        "s_add_u32 m0, s79, 3072\n"
        "s_mov_b32 s65, 479232\n"
        "buffer_load_dwordx4 v8, s[44:47], s65 offen lds\n"
        "buffer_load_dwordx4 v9, s[44:47], s65 offen offset:1024 lds\n"
        "buffer_load_dwordx4 v10, s[44:47], s65 offen offset:2048 lds\n"
        "s_waitcnt vmcnt(0)\n"
        "ds_read_b64_tr_b16 a[156:157], v11 offset:3072\n"
        "ds_read_b64_tr_b16 a[158:159], v11 offset:4608\n"
        "ds_read_b64_tr_b16 a[160:161], v11 offset:3080\n"
        "ds_read_b64_tr_b16 a[162:163], v11 offset:4616\n"
        "ds_read_b64_tr_b16 a[164:165], v15 offset:3072\n"
        "ds_read_b64_tr_b16 a[166:167], v15 offset:4608\n"
        "s_waitcnt lgkmcnt(6)\n"
        "s_add_u32 m0, s79, 0\n"
        "s_mov_b32 s65, 516096\n"
        "buffer_load_dwordx4 v8, s[44:47], s65 offen lds\n"
        "buffer_load_dwordx4 v9, s[44:47], s65 offen offset:1024 lds\n"
        "buffer_load_dwordx4 v10, s[44:47], s65 offen offset:2048 lds\n"
        "s_waitcnt vmcnt(0)\n"
        "ds_read_b64_tr_b16 a[168:169], v11 offset:0\n"
        "ds_read_b64_tr_b16 a[170:171], v11 offset:1536\n"
        "ds_read_b64_tr_b16 a[172:173], v11 offset:8\n"
        "ds_read_b64_tr_b16 a[174:175], v11 offset:1544\n"
        "ds_read_b64_tr_b16 a[176:177], v15 offset:0\n"
        "ds_read_b64_tr_b16 a[178:179], v15 offset:1536\n"
        "s_waitcnt lgkmcnt(6)\n"
        "s_add_u32 m0, s79, 3072\n"
        "s_mov_b32 s65, 552960\n"
        "buffer_load_dwordx4 v8, s[44:47], s65 offen lds\n"
        "buffer_load_dwordx4 v9, s[44:47], s65 offen offset:1024 lds\n"
        "buffer_load_dwordx4 v10, s[44:47], s65 offen offset:2048 lds\n"
        "s_waitcnt vmcnt(0)\n"
        "ds_read_b64_tr_b16 a[180:181], v11 offset:3072\n"
        "ds_read_b64_tr_b16 a[182:183], v11 offset:4608\n"
        "ds_read_b64_tr_b16 a[184:185], v11 offset:3080\n"
        "ds_read_b64_tr_b16 a[186:187], v11 offset:4616\n"
        "ds_read_b64_tr_b16 a[188:189], v15 offset:3072\n"
        "ds_read_b64_tr_b16 a[190:191], v15 offset:4608\n"
        "s_waitcnt lgkmcnt(6)\n"
        "s_add_u32 m0, s79, 0\n"
        "s_mov_b32 s65, 589824\n"
        "buffer_load_dwordx4 v8, s[44:47], s65 offen lds\n"
        "buffer_load_dwordx4 v9, s[44:47], s65 offen offset:1024 lds\n"
        "buffer_load_dwordx4 v10, s[44:47], s65 offen offset:2048 lds\n"
        "s_waitcnt vmcnt(0)\n"
        "ds_read_b64_tr_b16 a[192:193], v11 offset:0\n"
        "ds_read_b64_tr_b16 a[194:195], v11 offset:1536\n"
        "ds_read_b64_tr_b16 a[196:197], v11 offset:8\n"
        "ds_read_b64_tr_b16 a[198:199], v11 offset:1544\n"
        "ds_read_b64_tr_b16 a[200:201], v15 offset:0\n"
        "ds_read_b64_tr_b16 a[202:203], v15 offset:1536\n"
        "s_waitcnt lgkmcnt(6)\n"
        "s_add_u32 m0, s79, 3072\n"
        "s_mov_b32 s65, 626688\n"
        "buffer_load_dwordx4 v8, s[44:47], s65 offen lds\n"
        "buffer_load_dwordx4 v9, s[44:47], s65 offen offset:1024 lds\n"
        "buffer_load_dwordx4 v10, s[44:47], s65 offen offset:2048 lds\n"
        "s_waitcnt vmcnt(0)\n"
        "ds_read_b64_tr_b16 a[204:205], v11 offset:3072\n"
        "ds_read_b64_tr_b16 a[206:207], v11 offset:4608\n"
        "ds_read_b64_tr_b16 a[208:209], v11 offset:3080\n"
        "ds_read_b64_tr_b16 a[210:211], v11 offset:4616\n"
        "ds_read_b64_tr_b16 a[212:213], v15 offset:3072\n"
        "ds_read_b64_tr_b16 a[214:215], v15 offset:4608\n"
        "s_waitcnt lgkmcnt(6)\n"
        "s_add_u32 m0, s79, 0\n"
        "s_mov_b32 s65, 663552\n"
        "buffer_load_dwordx4 v8, s[44:47], s65 offen lds\n"
        "buffer_load_dwordx4 v9, s[44:47], s65 offen offset:1024 lds\n"
        "buffer_load_dwordx4 v10, s[44:47], s65 offen offset:2048 lds\n"
        "s_waitcnt vmcnt(0)\n"
        "ds_read_b64_tr_b16 a[216:217], v11 offset:0\n"
        "ds_read_b64_tr_b16 a[218:219], v11 offset:1536\n"
        "ds_read_b64_tr_b16 a[220:221], v11 offset:8\n"
        "ds_read_b64_tr_b16 a[222:223], v11 offset:1544\n"
        "ds_read_b64_tr_b16 a[224:225], v15 offset:0\n"
        "ds_read_b64_tr_b16 a[226:227], v15 offset:1536\n"
        "s_waitcnt lgkmcnt(6)\n"
        "s_add_u32 m0, s79, 3072\n"
        "s_mov_b32 s65, 700416\n"
        "buffer_load_dwordx4 v8, s[44:47], s65 offen lds\n"
        "buffer_load_dwordx4 v9, s[44:47], s65 offen offset:1024 lds\n"
        "buffer_load_dwordx4 v10, s[44:47], s65 offen offset:2048 lds\n"
        "s_waitcnt vmcnt(0)\n"
        "ds_read_b64_tr_b16 a[228:229], v11 offset:3072\n"
        "ds_read_b64_tr_b16 a[230:231], v11 offset:4608\n"
        "ds_read_b64_tr_b16 a[232:233], v11 offset:3080\n"
        "ds_read_b64_tr_b16 a[234:235], v11 offset:4616\n"
        "ds_read_b64_tr_b16 a[236:237], v15 offset:3072\n"
        "ds_read_b64_tr_b16 a[238:239], v15 offset:4608\n"
        "s_waitcnt lgkmcnt(6)\n"
        "s_add_u32 m0, s79, 0\n"
        "s_mov_b32 s65, 737280\n"
        "buffer_load_dwordx4 v8, s[44:47], s65 offen lds\n"
        "buffer_load_dwordx4 v9, s[44:47], s65 offen offset:1024 lds\n"
        "buffer_load_dwordx4 v10, s[44:47], s65 offen offset:2048 lds\n"
        "s_waitcnt vmcnt(0)\n"
        "ds_read_b64_tr_b16 a[240:241], v11 offset:0\n"
        "ds_read_b64_tr_b16 a[242:243], v11 offset:1536\n"
        "ds_read_b64_tr_b16 a[244:245], v11 offset:8\n"
        "ds_read_b64_tr_b16 a[246:247], v11 offset:1544\n"
        "ds_read_b64_tr_b16 a[248:249], v15 offset:0\n"
        "ds_read_b64_tr_b16 a[250:251], v15 offset:1536\n"
        "s_waitcnt lgkmcnt(6)\n"
        "s_add_u32 m0, s79, 3072\n"
        "s_mov_b32 s65, 774144\n"
        "buffer_load_dwordx4 v8, s[44:47], s65 offen lds\n"
        "buffer_load_dwordx4 v9, s[44:47], s65 offen offset:1024 lds\n"
        "buffer_load_dwordx4 v10, s[44:47], s65 offen offset:2048 lds\n"
        "s_waitcnt vmcnt(0)\n"
        "ds_read_b64_tr_b16 a[252:253], v11 offset:3072\n"
        "ds_read_b64_tr_b16 a[254:255], v11 offset:4608\n"
        "ds_read_b64_tr_b16 v[80:81], v11 offset:3080\n"
        "ds_read_b64_tr_b16 v[82:83], v11 offset:4616\n"
        "ds_read_b64_tr_b16 v[84:85], v15 offset:3072\n"
        "ds_read_b64_tr_b16 v[86:87], v15 offset:4608\n"
        "s_waitcnt lgkmcnt(6)\n"
        "s_add_u32 m0, s79, 0\n"
        "s_mov_b32 s65, 811008\n"
        "buffer_load_dwordx4 v8, s[44:47], s65 offen lds\n"
        "buffer_load_dwordx4 v9, s[44:47], s65 offen offset:1024 lds\n"
        "buffer_load_dwordx4 v10, s[44:47], s65 offen offset:2048 lds\n"
        "s_waitcnt vmcnt(0)\n"
        "ds_read_b64_tr_b16 v[88:89], v11 offset:0\n"
        "ds_read_b64_tr_b16 v[90:91], v11 offset:1536\n"
        "ds_read_b64_tr_b16 v[92:93], v11 offset:8\n"
        "ds_read_b64_tr_b16 v[94:95], v11 offset:1544\n"
        "ds_read_b64_tr_b16 v[96:97], v15 offset:0\n"
        "ds_read_b64_tr_b16 v[98:99], v15 offset:1536\n"
        "s_waitcnt lgkmcnt(6)\n"
        "s_add_u32 m0, s79, 3072\n"
        "s_mov_b32 s65, 847872\n"
        "buffer_load_dwordx4 v8, s[44:47], s65 offen lds\n"
        "buffer_load_dwordx4 v9, s[44:47], s65 offen offset:1024 lds\n"
        "buffer_load_dwordx4 v10, s[44:47], s65 offen offset:2048 lds\n"
        "s_waitcnt vmcnt(0)\n"
        "ds_read_b64_tr_b16 v[100:101], v11 offset:3072\n"
        "ds_read_b64_tr_b16 v[102:103], v11 offset:4608\n"
        "ds_read_b64_tr_b16 v[104:105], v11 offset:3080\n"
        "ds_read_b64_tr_b16 v[106:107], v11 offset:4616\n"
        "ds_read_b64_tr_b16 v[108:109], v15 offset:3072\n"
        "ds_read_b64_tr_b16 v[110:111], v15 offset:4608\n"
        "s_waitcnt lgkmcnt(6)\n"
        "s_add_u32 m0, s79, 0\n"
        "s_mov_b32 s65, 884736\n"
        "buffer_load_dwordx4 v8, s[44:47], s65 offen lds\n"
        "buffer_load_dwordx4 v9, s[44:47], s65 offen offset:1024 lds\n"
        "buffer_load_dwordx4 v10, s[44:47], s65 offen offset:2048 lds\n"
        "s_waitcnt vmcnt(0)\n"
        "ds_read_b64_tr_b16 v[112:113], v11 offset:0\n"
        "ds_read_b64_tr_b16 v[114:115], v11 offset:1536\n"
        "ds_read_b64_tr_b16 v[116:117], v11 offset:8\n"
        "ds_read_b64_tr_b16 v[118:119], v11 offset:1544\n"
        "ds_read_b64_tr_b16 v[120:121], v15 offset:0\n"
        "ds_read_b64_tr_b16 v[122:123], v15 offset:1536\n"
        "s_waitcnt lgkmcnt(6)\n"
        "s_add_u32 m0, s79, 3072\n"
        "s_mov_b32 s65, 921600\n"
        "buffer_load_dwordx4 v8, s[44:47], s65 offen lds\n"
        "buffer_load_dwordx4 v9, s[44:47], s65 offen offset:1024 lds\n"
        "buffer_load_dwordx4 v10, s[44:47], s65 offen offset:2048 lds\n"
        "s_waitcnt vmcnt(0)\n"
        "ds_read_b64_tr_b16 v[124:125], v11 offset:3072\n"
        "ds_read_b64_tr_b16 v[126:127], v11 offset:4608\n"
        "ds_read_b64_tr_b16 v[128:129], v11 offset:3080\n"
        "ds_read_b64_tr_b16 v[130:131], v11 offset:4616\n"
        "ds_read_b64_tr_b16 v[132:133], v15 offset:3072\n"
        "ds_read_b64_tr_b16 v[134:135], v15 offset:4608\n"
        "s_waitcnt lgkmcnt(6)\n"
        "s_add_u32 m0, s79, 0\n"
        "s_mov_b32 s65, 958464\n"
        "buffer_load_dwordx4 v8, s[44:47], s65 offen lds\n"
        "buffer_load_dwordx4 v9, s[44:47], s65 offen offset:1024 lds\n"
        "buffer_load_dwordx4 v10, s[44:47], s65 offen offset:2048 lds\n"
        "s_waitcnt vmcnt(0)\n"
        "ds_read_b64_tr_b16 v[136:137], v11 offset:0\n"
        "ds_read_b64_tr_b16 v[138:139], v11 offset:1536\n"
        "ds_read_b64_tr_b16 v[140:141], v11 offset:8\n"
        "ds_read_b64_tr_b16 v[142:143], v11 offset:1544\n"
        "ds_read_b64_tr_b16 v[144:145], v15 offset:0\n"
        "ds_read_b64_tr_b16 v[146:147], v15 offset:1536\n"
        "s_waitcnt lgkmcnt(6)\n"
        "s_add_u32 m0, s79, 3072\n"
        "s_mov_b32 s65, 995328\n"
        "buffer_load_dwordx4 v8, s[44:47], s65 offen lds\n"
        "buffer_load_dwordx4 v9, s[44:47], s65 offen offset:1024 lds\n"
        "buffer_load_dwordx4 v10, s[44:47], s65 offen offset:2048 lds\n"
        "s_waitcnt vmcnt(0)\n"
        "ds_read_b64_tr_b16 v[148:149], v11 offset:3072\n"
        "ds_read_b64_tr_b16 v[150:151], v11 offset:4608\n"
        "ds_read_b64_tr_b16 v[152:153], v11 offset:3080\n"
        "ds_read_b64_tr_b16 v[154:155], v11 offset:4616\n"
        "ds_read_b64_tr_b16 v[156:157], v15 offset:3072\n"
        "ds_read_b64_tr_b16 v[158:159], v15 offset:4608\n"
        "s_waitcnt lgkmcnt(6)\n"
        "s_add_u32 m0, s79, 0\n"
        "s_mov_b32 s65, 1032192\n"
        "buffer_load_dwordx4 v8, s[44:47], s65 offen lds\n"
        "buffer_load_dwordx4 v9, s[44:47], s65 offen offset:1024 lds\n"
        "buffer_load_dwordx4 v10, s[44:47], s65 offen offset:2048 lds\n"
        "s_waitcnt vmcnt(0)\n"
        "ds_read_b64_tr_b16 v[160:161], v11 offset:0\n"
        "ds_read_b64_tr_b16 v[162:163], v11 offset:1536\n"
        "ds_read_b64_tr_b16 v[164:165], v11 offset:8\n"
        "ds_read_b64_tr_b16 v[166:167], v11 offset:1544\n"
        "ds_read_b64_tr_b16 v[168:169], v15 offset:0\n"
        "ds_read_b64_tr_b16 v[170:171], v15 offset:1536\n"
        "s_waitcnt lgkmcnt(6)\n"
        "s_add_u32 m0, s79, 3072\n"
        "s_mov_b32 s65, 1069056\n"
        "buffer_load_dwordx4 v8, s[44:47], s65 offen lds\n"
        "buffer_load_dwordx4 v9, s[44:47], s65 offen offset:1024 lds\n"
        "buffer_load_dwordx4 v10, s[44:47], s65 offen offset:2048 lds\n"
        "s_waitcnt vmcnt(0)\n"
        "ds_read_b64_tr_b16 v[172:173], v11 offset:3072\n"
        "ds_read_b64_tr_b16 v[174:175], v11 offset:4608\n"
        "ds_read_b64_tr_b16 v[176:177], v11 offset:3080\n"
        "ds_read_b64_tr_b16 v[178:179], v11 offset:4616\n"
        "ds_read_b64_tr_b16 v[180:181], v15 offset:3072\n"
        "ds_read_b64_tr_b16 v[182:183], v15 offset:4608\n"
        "s_waitcnt lgkmcnt(6)\n"
        "s_add_u32 m0, s79, 0\n"
        "s_mov_b32 s65, 1105920\n"
        "buffer_load_dwordx4 v8, s[44:47], s65 offen lds\n"
        "buffer_load_dwordx4 v9, s[44:47], s65 offen offset:1024 lds\n"
        "buffer_load_dwordx4 v10, s[44:47], s65 offen offset:2048 lds\n"
        "s_waitcnt vmcnt(0)\n"
        "ds_read_b64_tr_b16 v[184:185], v11 offset:0\n"
        "ds_read_b64_tr_b16 v[186:187], v11 offset:1536\n"
        "ds_read_b64_tr_b16 v[188:189], v11 offset:8\n"
        "ds_read_b64_tr_b16 v[190:191], v11 offset:1544\n"
        "ds_read_b64_tr_b16 v[192:193], v15 offset:0\n"
        "ds_read_b64_tr_b16 v[194:195], v15 offset:1536\n"
        "s_waitcnt lgkmcnt(6)\n"
        "s_add_u32 m0, s79, 3072\n"
        "s_mov_b32 s65, 1142784\n"
        "buffer_load_dwordx4 v8, s[44:47], s65 offen lds\n"
        "buffer_load_dwordx4 v9, s[44:47], s65 offen offset:1024 lds\n"
        "buffer_load_dwordx4 v10, s[44:47], s65 offen offset:2048 lds\n"
        "s_waitcnt vmcnt(0)\n"
        "ds_read_b64_tr_b16 v[196:197], v11 offset:3072\n"
        "ds_read_b64_tr_b16 v[198:199], v11 offset:4608\n"
        "ds_read_b64_tr_b16 v[200:201], v11 offset:3080\n"
        "ds_read_b64_tr_b16 v[202:203], v11 offset:4616\n"
        "ds_read_b64_tr_b16 v[204:205], v15 offset:3072\n"
        "ds_read_b64_tr_b16 v[206:207], v15 offset:4608\n"
        "s_waitcnt lgkmcnt(0)\n"
        "s_getpc_b64 s[44:45]\n"
        ".Lop_pc_%=:\n"
        "s_add_u32 s44, s44, .Lop_code_%=-.Lop_pc_%=\n"
        "s_addc_u32 s45, s45, 0\n"
        "s_and_b32 s45, s45, 0xffff\n"
        "s_and_b32 s44, s44, -4\n"
        "s_mov_b32 s46, .Lop_end_%=-.Lop_code_%=\n"
        "s_mov_b32 s47, 0x20000\n"
        "s_lshl_b32 s65, s48, 10\n"
        "buffer_load_dwordx4 v[24:27], v1, s[44:47], s65 offen\n"
        "s_add_u32 s65, s65, 4096\n"
        "buffer_load_dwordx4 v[28:31], v1, s[44:47], s65 offen\n"
        "s_add_u32 s65, s65, 4096\n"
        "buffer_load_dwordx4 v[32:35], v1, s[44:47], s65 offen\n"
        "s_add_u32 s65, s65, 4096\n"
        "buffer_load_dwordx4 v[36:39], v1, s[44:47], s65 offen\n"
        "s_add_u32 s65, s65, 4096\n"
        "buffer_load_dwordx4 v[40:43], v1, s[44:47], s65 offen\n"
        "s_add_u32 s65, s65, 4096\n"
        "v_lshrrev_b32 v20, 4, v1\n"
        "s_mov_b32 s50, s56\n"
        "v_lshlrev_b32 v22, 7, v20\n"
        "s_sub_u32 s66, 0, s50\n"
        "s_mov_b32 s52, 32768\n"
        "s_mov_b32 s64, 0\n"
        ".Lop_pg0_top_%=:\n"
        "buffer_load_dword v21, v22, s[40:43], s52 offen sc1\n"
        "s_waitcnt vmcnt(0)\n"
        "v_cmp_gt_i32 vcc, 0, v21\n"
        "s_cbranch_vccz .Lop_pg0_ok_%=\n"
        "s_add_u32 s64, s64, 1\n"
        "s_and_b32 s65, s64, 255\n"
        "s_cbranch_scc1 .Lop_pg0_go_%=\n"
        "s_mov_b32 s65, 49152\n"
        "s_cmp_lt_u32 s64, 1048576\n"
        "s_cbranch_scc1 .Lop_pg0_chk_%=\n"
        "v_mov_b32 v21, 1\n"
        "buffer_store_dword v21, off, s[40:43], s65 sc1\n"
        "s_waitcnt vmcnt(0)\n"
        ".Lop_pg0_chk_%=:\n"
        "buffer_load_dword v21, off, s[40:43], s65 sc1\n"
        "s_waitcnt vmcnt(0)\n"
        "v_readfirstlane_b32 s65, v21\n"
        "s_nop 0\n"
        "s_cmp_lg_u32 s65, 1\n"
        "s_cbranch_scc1 .Lop_pg0_go_%=\n"
        "s_endpgm\n"
        ".Lop_pg0_go_%=:\n"
        "s_sleep 2\n"
        "s_branch .Lop_pg0_top_%=\n"
        ".Lop_pg0_ok_%=:\n"
        "v_cmp_eq_u32 s[52:53], 1, v21\n"
        "s_nop 1\n"
        "s_mov_b32 s54, 40960\n"
        "s_mov_b32 s64, 0\n"
        ".Lop_pg1_top_%=:\n"
        "buffer_load_dword v21, v22, s[40:43], s54 offen sc1\n"
        "s_waitcnt vmcnt(0)\n"
        "v_cmp_gt_i32 vcc, 0, v21\n"
        "s_cbranch_vccz .Lop_pg1_ok_%=\n"
        "s_add_u32 s64, s64, 1\n"
        "s_and_b32 s65, s64, 255\n"
        "s_cbranch_scc1 .Lop_pg1_go_%=\n"
        "s_mov_b32 s65, 49152\n"
        "s_cmp_lt_u32 s64, 1048576\n"
        "s_cbranch_scc1 .Lop_pg1_chk_%=\n"
        "v_mov_b32 v21, 1\n"
        "buffer_store_dword v21, off, s[40:43], s65 sc1\n"
        "s_waitcnt vmcnt(0)\n"
        ".Lop_pg1_chk_%=:\n"
        "buffer_load_dword v21, off, s[40:43], s65 sc1\n"
        "s_waitcnt vmcnt(0)\n"
        "v_readfirstlane_b32 s65, v21\n"
        "s_nop 0\n"
        "s_cmp_lg_u32 s65, 1\n"
        "s_cbranch_scc1 .Lop_pg1_go_%=\n"
        "s_endpgm\n"
        ".Lop_pg1_go_%=:\n"
        "s_sleep 2\n"
        "s_branch .Lop_pg1_top_%=\n"
        ".Lop_pg1_ok_%=:\n"
        "v_cmp_eq_u32 s[54:55], 1, v21\n"
        "s_nop 1\n"
        "s_mov_b32 vcc_lo, 0x55555555\n"
        "s_mov_b32 vcc_hi, 0x55555555\n"
        "s_xor_b64 vcc, vcc, s[52:53]\n"
        "s_bcnt1_i32_b64 s65, vcc\n"
        "s_sub_u32 s64, s65, 23\n"
        "s_cmp_lt_u32 s64, 19\n"
        "s_cbranch_scc1 .Lop_rawmap_%=\n"
        "s_cmp_lt_u32 s65, 32\n"
        "s_cselect_b32 s65, 0, -1\n"
        "s_xor_b32 s52, s65, 0x55555555\n"
        "s_xor_b32 s53, s65, 0x55555555\n"
        "s_xor_b32 s54, s65, 0x55555555\n"
        "s_xor_b32 s55, s65, 0xaaaaaaaa\n"
        ".Lop_rawmap_%=:\n"
        "s_xor_b32 s52, s52, s66\n"
        "s_xor_b32 s53, s53, s66\n"
        "s_xor_b32 s54, s54, s66\n"
        "s_xor_b32 s55, s55, s66\n"
        "s_and_b32 s76, s55, 1\n"
        "s_lshl_b32 s76, s76, 13\n"
        "s_add_u32 s60, s60, s76\n"
        "s_barrier\n"
        "v_mov_b32 v48, v6\n"
        "s_mov_b32 s68, -1\n"
        "s_mov_b32 s69, 0\n"
        "s_mov_b32 s78, 0\n"
        "s_mov_b32 s74, -1\n"
        "s_mov_b32 s86, s76\n"
        "s_lshr_b32 s85, s61, 16\n"
        "s_and_b32 s61, s61, 0xffff\n"
        "s_mov_b32 s89, 241172480\n"
        "s_mov_b32 s84, 241172480\n"
        "v_mov_b32 v6, v0\n"
        "s_mov_b32 s80, 0xbfb8aa3b\n"
        "s_lshr_b32 s67, s69, 1\n"
        "s_bitcmp1_b64 s[52:53], s67\n"
        "s_cselect_b32 s65, 8192, 0\n"
        "s_lshl_b32 s77, s67, 21\n"
        "s_add_u32 s77, s77, s65\n"
        "s_add_u32 s71, s77, s58\n"
        "s_add_u32 s78, s78, 32768\n"
        "s_cmp_eq_u32 s78, 98304\n"
        "s_cselect_b32 s78, 0, s78\n"
        "s_add_u32 m0, s78, s61\n"
        "s_lshr_b32 s70, s69, 0\n"
        "s_branch .Lop_wait1_%=\n"
        ".Lop_code_%=:\n"
        ".Lop_i0_%=:\n"
        "v_mfma_f32_16x16x32_bf16 v[24:27], a[0:3], v[8:11], 0\n"
        "buffer_load_dword v4, v2, s[40:43], s86 offen sc1\n"
        "v_mfma_f32_16x16x32_bf16 v[28:31], a[4:7], v[8:11], 0\n"
        "s_add_u32 s71, s77, s58\n"
        "s_add_u32 s71, s71, 4096\n"
        "s_add_u32 s78, s78, 32768\n"
        "v_mfma_f32_16x16x32_bf16 v[32:35], a[8:11], v[8:11], 0\n"
        "s_cmp_eq_u32 s78, 98304\n"
        "s_cselect_b32 s78, 0, s78\n"
        "ds_read_b128 v[8:11], v0 offset:4096\n"
        "v_mfma_f32_16x16x32_bf16 v[24:27], a[12:15], v[12:15], v[24:27]\n"
        "s_add_u32 m0, s78, s61\n"
        "s_lshr_b32 s70, s69, 0\n"
        "v_cvt_pk_bf16_f32 v36, v36, v37\n"
        "v_mfma_f32_16x16x32_bf16 v[28:31], a[16:19], v[12:15], v[28:31]\n"
        "v_cvt_pk_bf16_f32 v37, v38, v39\n"
        "v_cvt_pk_bf16_f32 v38, v40, v41\n"
        "v_mfma_f32_16x16x32_bf16 v[32:35], a[20:23], v[12:15], v[32:35]\n"
        "s_waitcnt lgkmcnt(1)\n"
        "ds_read_b128 v[12:15], v0 offset:5120\n"
        "v_cvt_pk_bf16_f32 v39, v42, v43\n"
        "v_mfma_f32_16x16x32_bf16 v[24:27], a[24:27], v[16:19], v[24:27]\n"
        "buffer_store_dwordx4 v[36:39], v48, s[40:43], s72 offen offset:0 sc1\n"
        "v_mfma_f32_16x16x32_bf16 v[28:31], a[28:31], v[16:19], v[28:31]\n"
        "v_cvt_pk_bf16_f32 v44, v44, v45\n"
        "v_cvt_pk_bf16_f32 v45, v46, v47\n"
        "v_mfma_f32_16x16x32_bf16 v[32:35], a[32:35], v[16:19], v[32:35]\n"
        "s_nop 0\n"
        "ds_read_b128 v[16:19], v0 offset:6144\n"
        "v_mfma_f32_16x16x32_bf16 v[24:27], a[36:39], v[20:23], v[24:27]\n"
        "v_permlane16_swap_b32 v44, v46\n"
        "v_mfma_f32_16x16x32_bf16 v[28:31], a[40:43], v[20:23], v[28:31]\n"
        "v_permlane16_swap_b32 v45, v47\n"
        "v_mfma_f32_16x16x32_bf16 v[32:35], a[44:47], v[20:23], v[32:35]\n"
        "s_waitcnt lgkmcnt(1)\n"
        "ds_read_b128 v[20:23], v0 offset:7168\n"
        "v_mfma_f32_16x16x32_bf16 v[24:27], a[48:51], v[8:11], v[24:27]\n"
        "v_mfma_f32_16x16x32_bf16 v[28:31], a[52:55], v[8:11], v[28:31]\n"
        "buffer_store_dwordx4 v[44:47], v7, s[40:43], s72 offen offset:0 sc1\n"
        "v_mfma_f32_16x16x32_bf16 v[32:35], a[56:59], v[8:11], v[32:35]\n"
        "ds_read_b128 v[8:11], v0 offset:8192\n"
        "v_mfma_f32_16x16x32_bf16 v[24:27], a[60:63], v[12:15], v[24:27]\n"
        "v_mfma_f32_16x16x32_bf16 v[28:31], a[64:67], v[12:15], v[28:31]\n"
        "v_mfma_f32_16x16x32_bf16 v[32:35], a[68:71], v[12:15], v[32:35]\n"
        "s_waitcnt lgkmcnt(1)\n"
        "ds_read_b128 v[12:15], v0 offset:9216\n"
        "v_mfma_f32_16x16x32_bf16 v[24:27], a[72:75], v[16:19], v[24:27]\n"
        "v_mfma_f32_16x16x32_bf16 v[28:31], a[76:79], v[16:19], v[28:31]\n"
        "s_waitcnt vmcnt(2)\n"
        "v_cmp_gt_i32 vcc, s70, v4\n"
        "v_mfma_f32_16x16x32_bf16 v[32:35], a[80:83], v[16:19], v[32:35]\n"
        "ds_read_b128 v[16:19], v0 offset:10240\n"
        "v_mfma_f32_16x16x32_bf16 v[24:27], a[84:87], v[20:23], v[24:27]\n"
        "s_and_b64 vcc, vcc, 3\n"
        "v_mfma_f32_16x16x32_bf16 v[28:31], a[88:91], v[20:23], v[28:31]\n"
        "s_cbranch_vccnz .Lop_i00_%=\n"
        "v_mfma_f32_16x16x32_bf16 v[32:35], a[92:95], v[20:23], v[32:35]\n"
        "s_waitcnt lgkmcnt(1)\n"
        "ds_read_b128 v[20:23], v0 offset:11264\n"
        "v_mfma_f32_16x16x32_bf16 v[24:27], a[96:99], v[8:11], v[24:27]\n"
        "buffer_load_dwordx4 v1, s[40:43], s71 offen offset:0 sc1 lds\n"
        "v_mfma_f32_16x16x32_bf16 v[28:31], a[100:103], v[8:11], v[28:31]\n"
        "v_mfma_f32_16x16x32_bf16 v[32:35], a[104:107], v[8:11], v[32:35]\n"
        "ds_read_b128 v[8:11], v0 offset:12288\n"
        "v_mfma_f32_16x16x32_bf16 v[24:27], a[108:111], v[12:15], v[24:27]\n"
        "buffer_load_dwordx4 v1, s[40:43], s71 offen offset:1024 sc1 lds\n"
        "v_mfma_f32_16x16x32_bf16 v[28:31], a[112:115], v[12:15], v[28:31]\n"
        "v_mfma_f32_16x16x32_bf16 v[32:35], a[116:119], v[12:15], v[32:35]\n"
        "s_waitcnt lgkmcnt(1)\n"
        "ds_read_b128 v[12:15], v0 offset:13312\n"
        "v_mfma_f32_16x16x32_bf16 v[24:27], a[120:123], v[16:19], v[24:27]\n"
        "v_mfma_f32_16x16x32_bf16 v[28:31], a[124:127], v[16:19], v[28:31]\n"
        "buffer_load_dwordx4 v1, s[40:43], s71 offen offset:2048 sc1 lds\n"
        "v_mfma_f32_16x16x32_bf16 v[32:35], a[128:131], v[16:19], v[32:35]\n"
        "ds_read_b128 v[16:19], v0 offset:14336\n"
        "v_mfma_f32_16x16x32_bf16 v[24:27], a[132:135], v[20:23], v[24:27]\n"
        "v_mfma_f32_16x16x32_bf16 v[28:31], a[136:139], v[20:23], v[28:31]\n"
        "v_mfma_f32_16x16x32_bf16 v[32:35], a[140:143], v[20:23], v[32:35]\n"
        "s_waitcnt lgkmcnt(1)\n"
        "ds_read_b128 v[20:23], v0 offset:15360\n"
        "v_mfma_f32_16x16x32_bf16 v[24:27], a[144:147], v[8:11], v[24:27]\n"
        "buffer_load_dwordx4 v1, s[40:43], s71 offen offset:3072 sc1 lds\n"
        "v_mfma_f32_16x16x32_bf16 v[28:31], a[148:151], v[8:11], v[28:31]\n"
        "s_barrier\n"
        "s_add_u32 m0, m0, 16384\n"
        "v_mfma_f32_16x16x32_bf16 v[32:35], a[152:155], v[8:11], v[32:35]\n"
        "s_add_u32 s71, s71, 65536\n"
        "ds_read_b128 v[8:11], v0 offset:16384\n"
        "v_mfma_f32_16x16x32_bf16 v[24:27], a[156:159], v[12:15], v[24:27]\n"
        "buffer_load_dwordx4 v1, s[40:43], s71 offen offset:0 sc1 lds\n"
        "v_mfma_f32_16x16x32_bf16 v[28:31], a[160:163], v[12:15], v[28:31]\n"
        "buffer_store_dword v5, v3, s[40:43], s84 offen sc1\n"
        "v_mfma_f32_16x16x32_bf16 v[32:35], a[164:167], v[12:15], v[32:35]\n"
        "s_waitcnt lgkmcnt(1)\n"
        "ds_read_b128 v[12:15], v0 offset:17408\n"
        "v_mov_b32 v5, s74\n"
        "v_mfma_f32_16x16x32_bf16 v[24:27], a[168:171], v[16:19], v[24:27]\n"
        "s_getreg_b32 s88, hwreg(HW_REG_IB_STS, 0, 4)\n"
        "v_mfma_f32_16x16x32_bf16 v[28:31], a[172:175], v[16:19], v[28:31]\n"
        "buffer_load_dwordx4 v1, s[40:43], s71 offen offset:1024 sc1 lds\n"
        "v_mfma_f32_16x16x32_bf16 v[32:35], a[176:179], v[16:19], v[32:35]\n"
        "ds_read_b128 v[16:19], v0 offset:18432\n"
        "v_mfma_f32_16x16x32_bf16 v[24:27], a[180:183], v[20:23], v[24:27]\n"
        "s_cmp_le_u32 s88, 6\n"
        "s_cselect_b32 s88, s73, s89\n"
        "s_cselect_b32 s73, s89, s73\n"
        "v_mfma_f32_16x16x32_bf16 v[28:31], a[184:187], v[20:23], v[28:31]\n"
        "buffer_store_dword v5, v3, s[40:43], s88 offen sc1\n"
        "v_mfma_f32_16x16x32_bf16 v[32:35], a[188:191], v[20:23], v[32:35]\n"
        "s_waitcnt lgkmcnt(1)\n"
        "ds_read_b128 v[20:23], v0 offset:19456\n"
        "v_mfma_f32_16x16x32_bf16 v[24:27], a[192:195], v[8:11], v[24:27]\n"
        "s_getreg_b32 s88, hwreg(HW_REG_IB_STS, 0, 4)\n"
        "buffer_load_dwordx4 v1, s[40:43], s71 offen offset:2048 sc1 lds\n"
        "v_mfma_f32_16x16x32_bf16 v[28:31], a[196:199], v[8:11], v[28:31]\n"
        "s_cmp_le_u32 s88, 8\n"
        "s_cselect_b32 s88, s73, s89\n"
        "s_cselect_b32 s73, s89, s73\n"
        "v_mfma_f32_16x16x32_bf16 v[32:35], a[200:203], v[8:11], v[32:35]\n"
        "ds_read_b128 v[8:11], v0 offset:20480\n"
        "v_mfma_f32_16x16x32_bf16 v[24:27], a[204:207], v[12:15], v[24:27]\n"
        "buffer_load_dwordx4 v1, s[40:43], s71 offen offset:3072 sc1 lds\n"
        "v_mfma_f32_16x16x32_bf16 v[28:31], a[208:211], v[12:15], v[28:31]\n"
        "s_add_u32 s69, s69, 1\n"
        "buffer_store_dword v5, v3, s[40:43], s88 offen sc1\n"
        "v_mfma_f32_16x16x32_bf16 v[32:35], a[212:215], v[12:15], v[32:35]\n"
        "s_waitcnt lgkmcnt(1)\n"
        "ds_read_b128 v[12:15], v0 offset:21504\n"
        "v_mfma_f32_16x16x32_bf16 v[24:27], a[216:219], v[16:19], v[24:27]\n"
        "s_getreg_b32 s88, hwreg(HW_REG_IB_STS, 0, 4)\n"
        "v_mfma_f32_16x16x32_bf16 v[28:31], a[220:223], v[16:19], v[28:31]\n"
        "s_cmp_le_u32 s88, 11\n"
        "s_cselect_b32 s88, s73, s89\n"
        "s_cselect_b32 s73, s89, s73\n"
        "v_mfma_f32_16x16x32_bf16 v[32:35], a[224:227], v[16:19], v[32:35]\n"
        "ds_read_b128 v[16:19], v0 offset:22528\n"
        "v_mfma_f32_16x16x32_bf16 v[24:27], a[228:231], v[20:23], v[24:27]\n"
        "buffer_store_dword v5, v3, s[40:43], s88 offen sc1\n"
        "v_mfma_f32_16x16x32_bf16 v[28:31], a[232:235], v[20:23], v[28:31]\n"
        "v_mfma_f32_16x16x32_bf16 v[32:35], a[236:239], v[20:23], v[32:35]\n"
        "s_waitcnt lgkmcnt(1)\n"
        "ds_read_b128 v[20:23], v0 offset:23552\n"
        "v_mfma_f32_16x16x32_bf16 v[24:27], a[240:243], v[8:11], v[24:27]\n"
        "v_mfma_f32_16x16x32_bf16 v[28:31], a[244:247], v[8:11], v[28:31]\n"
        "v_mfma_f32_16x16x32_bf16 v[32:35], a[248:251], v[8:11], v[32:35]\n"
        "ds_read_b128 v[8:11], v0 offset:24576\n"
        "v_mfma_f32_16x16x32_bf16 v[24:27], a[252:255], v[12:15], v[24:27]\n"
        "v_mfma_f32_16x16x32_bf16 v[28:31], v[80:83], v[12:15], v[28:31]\n"
        "v_mfma_f32_16x16x32_bf16 v[32:35], v[84:87], v[12:15], v[32:35]\n"
        "s_waitcnt lgkmcnt(1)\n"
        "ds_read_b128 v[12:15], v0 offset:25600\n"
        "v_mfma_f32_16x16x32_bf16 v[24:27], v[88:91], v[16:19], v[24:27]\n"
        "v_mfma_f32_16x16x32_bf16 v[28:31], v[92:95], v[16:19], v[28:31]\n"
        "s_waitcnt vmcnt(12)\n"
        "buffer_store_dword v5, v3, s[40:43], s73 offen sc1\n"
        "v_mfma_f32_16x16x32_bf16 v[32:35], v[96:99], v[16:19], v[32:35]\n"
        "s_mov_b32 s84, s89\n"
        "ds_read_b128 v[16:19], v0 offset:26624\n"
        "s_mov_b32 s73, s60\n"
        "v_mfma_f32_16x16x32_bf16 v[24:27], v[100:103], v[20:23], v[24:27]\n"
        "s_add_u32 s74, s74, 1\n"
        "s_add_u32 s68, s68, 1\n"
        "s_lshr_b32 s66, s68, 1\n"
        "v_mfma_f32_16x16x32_bf16 v[28:31], v[104:107], v[20:23], v[28:31]\n"
        "s_bitcmp1_b32 s54, s66\n"
        "s_cselect_b32 s66, 8192, 0\n"
        "s_lshl_b32 s75, s68, 20\n"
        "v_mfma_f32_16x16x32_bf16 v[32:35], v[108:111], v[20:23], v[32:35]\n"
        "s_waitcnt lgkmcnt(1)\n"
        "ds_read_b128 v[20:23], v0 offset:27648\n"
        "s_add_u32 s75, s75, s66\n"
        "v_mfma_f32_16x16x32_bf16 v[24:27], v[112:115], v[8:11], v[24:27]\n"
        "s_add_u32 s72, s75, s59\n"
        "v_mfma_f32_16x16x32_bf16 v[28:31], v[116:119], v[8:11], v[28:31]\n"
        "v_mfma_f32_16x16x32_bf16 v[32:35], v[120:123], v[8:11], v[32:35]\n"
        "ds_read_b128 v[8:11], v0 offset:28672\n"
        "v_mfma_f32_16x16x32_bf16 v[24:27], v[124:127], v[12:15], v[24:27]\n"
        "v_mfma_f32_16x16x32_bf16 v[28:31], v[128:131], v[12:15], v[28:31]\n"
        "v_mfma_f32_16x16x32_bf16 v[32:35], v[132:135], v[12:15], v[32:35]\n"
        "s_waitcnt lgkmcnt(1)\n"
        "ds_read_b128 v[12:15], v0 offset:29696\n"
        "v_mfma_f32_16x16x32_bf16 v[24:27], v[136:139], v[16:19], v[24:27]\n"
        "v_mfma_f32_16x16x32_bf16 v[28:31], v[140:143], v[16:19], v[28:31]\n"
        "v_mfma_f32_16x16x32_bf16 v[32:35], v[144:147], v[16:19], v[32:35]\n"
        "ds_read_b128 v[16:19], v0 offset:30720\n"
        "v_mfma_f32_16x16x32_bf16 v[24:27], v[148:151], v[20:23], v[24:27]\n"
        "v_mfma_f32_16x16x32_bf16 v[28:31], v[152:155], v[20:23], v[28:31]\n"
        "v_mfma_f32_16x16x32_bf16 v[32:35], v[156:159], v[20:23], v[32:35]\n"
        "s_waitcnt lgkmcnt(1)\n"
        "ds_read_b128 v[20:23], v0 offset:31744\n"
        "v_mfma_f32_16x16x32_bf16 v[24:27], v[160:163], v[8:11], v[24:27]\n"
        "v_mfma_f32_16x16x32_bf16 v[28:31], v[164:167], v[8:11], v[28:31]\n"
        "s_waitcnt vmcnt(9)\n"
        "s_barrier\n"
        "v_add_u32 v0, s78, v6\n"
        "v_mfma_f32_16x16x32_bf16 v[32:35], v[168:171], v[8:11], v[32:35]\n"
        "ds_read_b128 v[8:11], v0 offset:0\n"
        "v_mfma_f32_16x16x32_bf16 v[24:27], v[172:175], v[12:15], v[24:27]\n"
        "v_mfma_f32_16x16x32_bf16 v[28:31], v[176:179], v[12:15], v[28:31]\n"
        "v_mfma_f32_16x16x32_bf16 v[32:35], v[180:183], v[12:15], v[32:35]\n"
        "s_waitcnt lgkmcnt(1)\n"
        "ds_read_b128 v[12:15], v0 offset:1024\n"
        "v_mfma_f32_16x16x32_bf16 v[24:27], v[184:187], v[16:19], v[24:27]\n"
        "v_mfma_f32_16x16x32_bf16 v[28:31], v[188:191], v[16:19], v[28:31]\n"
        "v_mfma_f32_16x16x32_bf16 v[32:35], v[192:195], v[16:19], v[32:35]\n"
        "ds_read_b128 v[16:19], v0 offset:2048\n"
        "v_mfma_f32_16x16x32_bf16 v[24:27], v[196:199], v[20:23], v[24:27]\n"
        "v_mfma_f32_16x16x32_bf16 v[28:31], v[200:203], v[20:23], v[28:31]\n"
        "v_mfma_f32_16x16x32_bf16 v[32:35], v[204:207], v[20:23], v[32:35]\n"
        "s_waitcnt lgkmcnt(1)\n"
        "ds_read_b128 v[20:23], v0 offset:3072\n"
        "s_branch .Lop_i1_%=\n"
        ".Lop_i00_%=:\n"
        "v_mfma_f32_16x16x32_bf16 v[32:35], a[92:95], v[20:23], v[32:35]\n"
        "s_waitcnt lgkmcnt(1)\n"
        "ds_read_b128 v[20:23], v0 offset:11264\n"
        "v_mfma_f32_16x16x32_bf16 v[24:27], a[96:99], v[8:11], v[24:27]\n"
        "buffer_load_dword v4, v2, s[40:43], s86 offen sc1\n"
        "v_mfma_f32_16x16x32_bf16 v[28:31], a[100:103], v[8:11], v[28:31]\n"
        "v_mfma_f32_16x16x32_bf16 v[32:35], a[104:107], v[8:11], v[32:35]\n"
        "ds_read_b128 v[8:11], v0 offset:12288\n"
        "v_mfma_f32_16x16x32_bf16 v[24:27], a[108:111], v[12:15], v[24:27]\n"
        "v_mfma_f32_16x16x32_bf16 v[28:31], a[112:115], v[12:15], v[28:31]\n"
        "v_mfma_f32_16x16x32_bf16 v[32:35], a[116:119], v[12:15], v[32:35]\n"
        "s_waitcnt lgkmcnt(1)\n"
        "ds_read_b128 v[12:15], v0 offset:13312\n"
        "v_mfma_f32_16x16x32_bf16 v[24:27], a[120:123], v[16:19], v[24:27]\n"
        "v_mfma_f32_16x16x32_bf16 v[28:31], a[124:127], v[16:19], v[28:31]\n"
        "v_mfma_f32_16x16x32_bf16 v[32:35], a[128:131], v[16:19], v[32:35]\n"
        "ds_read_b128 v[16:19], v0 offset:14336\n"
        "v_mfma_f32_16x16x32_bf16 v[24:27], a[132:135], v[20:23], v[24:27]\n"
        "v_mfma_f32_16x16x32_bf16 v[28:31], a[136:139], v[20:23], v[28:31]\n"
        "v_mfma_f32_16x16x32_bf16 v[32:35], a[140:143], v[20:23], v[32:35]\n"
        "s_waitcnt lgkmcnt(1)\n"
        "ds_read_b128 v[20:23], v0 offset:15360\n"
        "v_mfma_f32_16x16x32_bf16 v[24:27], a[144:147], v[8:11], v[24:27]\n"
        "v_mfma_f32_16x16x32_bf16 v[28:31], a[148:151], v[8:11], v[28:31]\n"
        "s_barrier\n"
        "v_mfma_f32_16x16x32_bf16 v[32:35], a[152:155], v[8:11], v[32:35]\n"
        "ds_read_b128 v[8:11], v0 offset:16384\n"
        "v_mfma_f32_16x16x32_bf16 v[24:27], a[156:159], v[12:15], v[24:27]\n"
        "buffer_store_dword v5, v3, s[40:43], s84 offen sc1\n"
        "v_mfma_f32_16x16x32_bf16 v[28:31], a[160:163], v[12:15], v[28:31]\n"
        "v_mov_b32 v5, s74\n"
        "v_mfma_f32_16x16x32_bf16 v[32:35], a[164:167], v[12:15], v[32:35]\n"
        "s_waitcnt lgkmcnt(1)\n"
        "ds_read_b128 v[12:15], v0 offset:17408\n"
        "v_mfma_f32_16x16x32_bf16 v[24:27], a[168:171], v[16:19], v[24:27]\n"
        "s_getreg_b32 s88, hwreg(HW_REG_IB_STS, 0, 4)\n"
        "v_mfma_f32_16x16x32_bf16 v[28:31], a[172:175], v[16:19], v[28:31]\n"
        "s_cmp_le_u32 s88, 2\n"
        "s_cselect_b32 s88, s73, s89\n"
        "s_cselect_b32 s73, s89, s73\n"
        "v_mfma_f32_16x16x32_bf16 v[32:35], a[176:179], v[16:19], v[32:35]\n"
        "ds_read_b128 v[16:19], v0 offset:18432\n"
        "v_mfma_f32_16x16x32_bf16 v[24:27], a[180:183], v[20:23], v[24:27]\n"
        "buffer_store_dword v5, v3, s[40:43], s88 offen sc1\n"
        "v_mfma_f32_16x16x32_bf16 v[28:31], a[184:187], v[20:23], v[28:31]\n"
        "s_getreg_b32 s88, hwreg(HW_REG_IB_STS, 0, 4)\n"
        "v_mfma_f32_16x16x32_bf16 v[32:35], a[188:191], v[20:23], v[32:35]\n"
        "s_waitcnt lgkmcnt(1)\n"
        "ds_read_b128 v[20:23], v0 offset:19456\n"
        "v_mfma_f32_16x16x32_bf16 v[24:27], a[192:195], v[8:11], v[24:27]\n"
        "s_cmp_le_u32 s88, 3\n"
        "s_cselect_b32 s88, s73, s89\n"
        "s_cselect_b32 s73, s89, s73\n"
        "v_mfma_f32_16x16x32_bf16 v[28:31], a[196:199], v[8:11], v[28:31]\n"
        "s_waitcnt vmcnt(2)\n"
        "v_cmp_gt_i32 vcc, s70, v4\n"
        "v_mfma_f32_16x16x32_bf16 v[32:35], a[200:203], v[8:11], v[32:35]\n"
        "ds_read_b128 v[8:11], v0 offset:20480\n"
        "v_mfma_f32_16x16x32_bf16 v[24:27], a[204:207], v[12:15], v[24:27]\n"
        "s_and_b64 vcc, vcc, 3\n"
        "v_mfma_f32_16x16x32_bf16 v[28:31], a[208:211], v[12:15], v[28:31]\n"
        "s_cbranch_vccnz .Lop_i01_%=\n"
        "v_mfma_f32_16x16x32_bf16 v[32:35], a[212:215], v[12:15], v[32:35]\n"
        "s_waitcnt lgkmcnt(1)\n"
        "ds_read_b128 v[12:15], v0 offset:21504\n"
        "v_mfma_f32_16x16x32_bf16 v[24:27], a[216:219], v[16:19], v[24:27]\n"
        "buffer_load_dwordx4 v1, s[40:43], s71 offen offset:0 sc1 lds\n"
        "v_mfma_f32_16x16x32_bf16 v[28:31], a[220:223], v[16:19], v[28:31]\n"
        "buffer_store_dword v5, v3, s[40:43], s88 offen sc1\n"
        "v_mfma_f32_16x16x32_bf16 v[32:35], a[224:227], v[16:19], v[32:35]\n"
        "s_getreg_b32 s88, hwreg(HW_REG_IB_STS, 0, 4)\n"
        "ds_read_b128 v[16:19], v0 offset:22528\n"
        "v_mfma_f32_16x16x32_bf16 v[24:27], a[228:231], v[20:23], v[24:27]\n"
        "buffer_load_dwordx4 v1, s[40:43], s71 offen offset:1024 sc1 lds\n"
        "v_mfma_f32_16x16x32_bf16 v[28:31], a[232:235], v[20:23], v[28:31]\n"
        "s_cmp_le_u32 s88, 15\n"
        "s_cselect_b32 s88, s73, s89\n"
        "s_cselect_b32 s73, s89, s73\n"
        "v_mfma_f32_16x16x32_bf16 v[32:35], a[236:239], v[20:23], v[32:35]\n"
        "s_waitcnt lgkmcnt(1)\n"
        "ds_read_b128 v[20:23], v0 offset:23552\n"
        "v_mfma_f32_16x16x32_bf16 v[24:27], a[240:243], v[8:11], v[24:27]\n"
        "buffer_load_dwordx4 v1, s[40:43], s71 offen offset:2048 sc1 lds\n"
        "v_mfma_f32_16x16x32_bf16 v[28:31], a[244:247], v[8:11], v[28:31]\n"
        "buffer_store_dword v5, v3, s[40:43], s88 offen sc1\n"
        "v_mfma_f32_16x16x32_bf16 v[32:35], a[248:251], v[8:11], v[32:35]\n"
        "ds_read_b128 v[8:11], v0 offset:24576\n"
        "v_mfma_f32_16x16x32_bf16 v[24:27], a[252:255], v[12:15], v[24:27]\n"
        "buffer_load_dwordx4 v1, s[40:43], s71 offen offset:3072 sc1 lds\n"
        "v_mfma_f32_16x16x32_bf16 v[28:31], v[80:83], v[12:15], v[28:31]\n"
        "s_add_u32 m0, m0, 16384\n"
        "s_add_u32 s71, s71, 65536\n"
        "v_mfma_f32_16x16x32_bf16 v[32:35], v[84:87], v[12:15], v[32:35]\n"
        "s_waitcnt lgkmcnt(1)\n"
        "ds_read_b128 v[12:15], v0 offset:25600\n"
        "v_mfma_f32_16x16x32_bf16 v[24:27], v[88:91], v[16:19], v[24:27]\n"
        "buffer_load_dwordx4 v1, s[40:43], s71 offen offset:0 sc1 lds\n"
        "v_mfma_f32_16x16x32_bf16 v[28:31], v[92:95], v[16:19], v[28:31]\n"
        "buffer_store_dword v5, v3, s[40:43], s73 offen sc1\n"
        "v_mfma_f32_16x16x32_bf16 v[32:35], v[96:99], v[16:19], v[32:35]\n"
        "s_mov_b32 s84, s89\n"
        "ds_read_b128 v[16:19], v0 offset:26624\n"
        "s_mov_b32 s73, s60\n"
        "v_mfma_f32_16x16x32_bf16 v[24:27], v[100:103], v[20:23], v[24:27]\n"
        "s_add_u32 s74, s74, 1\n"
        "s_add_u32 s68, s68, 1\n"
        "s_lshr_b32 s66, s68, 1\n"
        "v_mfma_f32_16x16x32_bf16 v[28:31], v[104:107], v[20:23], v[28:31]\n"
        "s_bitcmp1_b32 s54, s66\n"
        "s_cselect_b32 s66, 8192, 0\n"
        "s_lshl_b32 s75, s68, 20\n"
        "v_mfma_f32_16x16x32_bf16 v[32:35], v[108:111], v[20:23], v[32:35]\n"
        "s_waitcnt lgkmcnt(1)\n"
        "ds_read_b128 v[20:23], v0 offset:27648\n"
        "s_add_u32 s75, s75, s66\n"
        "v_mfma_f32_16x16x32_bf16 v[24:27], v[112:115], v[8:11], v[24:27]\n"
        "s_add_u32 s72, s75, s59\n"
        "v_mfma_f32_16x16x32_bf16 v[28:31], v[116:119], v[8:11], v[28:31]\n"
        "buffer_load_dwordx4 v1, s[40:43], s71 offen offset:1024 sc1 lds\n"
        "v_mfma_f32_16x16x32_bf16 v[32:35], v[120:123], v[8:11], v[32:35]\n"
        "ds_read_b128 v[8:11], v0 offset:28672\n"
        "v_mfma_f32_16x16x32_bf16 v[24:27], v[124:127], v[12:15], v[24:27]\n"
        "v_mfma_f32_16x16x32_bf16 v[28:31], v[128:131], v[12:15], v[28:31]\n"
        "v_mfma_f32_16x16x32_bf16 v[32:35], v[132:135], v[12:15], v[32:35]\n"
        "s_waitcnt lgkmcnt(1)\n"
        "ds_read_b128 v[12:15], v0 offset:29696\n"
        "v_mfma_f32_16x16x32_bf16 v[24:27], v[136:139], v[16:19], v[24:27]\n"
        "buffer_load_dwordx4 v1, s[40:43], s71 offen offset:2048 sc1 lds\n"
        "v_mfma_f32_16x16x32_bf16 v[28:31], v[140:143], v[16:19], v[28:31]\n"
        "v_mfma_f32_16x16x32_bf16 v[32:35], v[144:147], v[16:19], v[32:35]\n"
        "ds_read_b128 v[16:19], v0 offset:30720\n"
        "v_mfma_f32_16x16x32_bf16 v[24:27], v[148:151], v[20:23], v[24:27]\n"
        "buffer_load_dwordx4 v1, s[40:43], s71 offen offset:3072 sc1 lds\n"
        "v_mfma_f32_16x16x32_bf16 v[28:31], v[152:155], v[20:23], v[28:31]\n"
        "s_add_u32 s69, s69, 1\n"
        "v_mfma_f32_16x16x32_bf16 v[32:35], v[156:159], v[20:23], v[32:35]\n"
        "s_waitcnt lgkmcnt(1)\n"
        "ds_read_b128 v[20:23], v0 offset:31744\n"
        "v_mfma_f32_16x16x32_bf16 v[24:27], v[160:163], v[8:11], v[24:27]\n"
        "v_mfma_f32_16x16x32_bf16 v[28:31], v[164:167], v[8:11], v[28:31]\n"
        "s_waitcnt vmcnt(5)\n"
        "s_barrier\n"
        "v_add_u32 v0, s78, v6\n"
        "v_mfma_f32_16x16x32_bf16 v[32:35], v[168:171], v[8:11], v[32:35]\n"
        "ds_read_b128 v[8:11], v0 offset:0\n"
        "v_mfma_f32_16x16x32_bf16 v[24:27], v[172:175], v[12:15], v[24:27]\n"
        "v_mfma_f32_16x16x32_bf16 v[28:31], v[176:179], v[12:15], v[28:31]\n"
        "v_mfma_f32_16x16x32_bf16 v[32:35], v[180:183], v[12:15], v[32:35]\n"
        "s_waitcnt lgkmcnt(1)\n"
        "ds_read_b128 v[12:15], v0 offset:1024\n"
        "v_mfma_f32_16x16x32_bf16 v[24:27], v[184:187], v[16:19], v[24:27]\n"
        "v_mfma_f32_16x16x32_bf16 v[28:31], v[188:191], v[16:19], v[28:31]\n"
        "v_mfma_f32_16x16x32_bf16 v[32:35], v[192:195], v[16:19], v[32:35]\n"
        "ds_read_b128 v[16:19], v0 offset:2048\n"
        "v_mfma_f32_16x16x32_bf16 v[24:27], v[196:199], v[20:23], v[24:27]\n"
        "v_mfma_f32_16x16x32_bf16 v[28:31], v[200:203], v[20:23], v[28:31]\n"
        "v_mfma_f32_16x16x32_bf16 v[32:35], v[204:207], v[20:23], v[32:35]\n"
        "s_waitcnt lgkmcnt(1)\n"
        "ds_read_b128 v[20:23], v0 offset:3072\n"
        "s_branch .Lop_i1_%=\n"
        ".Lop_i01_%=:\n"
        "v_mfma_f32_16x16x32_bf16 v[32:35], a[212:215], v[12:15], v[32:35]\n"
        "s_waitcnt lgkmcnt(1)\n"
        "ds_read_b128 v[12:15], v0 offset:21504\n"
        "v_mfma_f32_16x16x32_bf16 v[24:27], a[216:219], v[16:19], v[24:27]\n"
        "buffer_load_dword v4, v2, s[40:43], s86 offen sc1\n"
        "v_mfma_f32_16x16x32_bf16 v[28:31], a[220:223], v[16:19], v[28:31]\n"
        "buffer_store_dword v5, v3, s[40:43], s88 offen sc1\n"
        "v_mfma_f32_16x16x32_bf16 v[32:35], a[224:227], v[16:19], v[32:35]\n"
        "s_getreg_b32 s88, hwreg(HW_REG_IB_STS, 0, 4)\n"
        "ds_read_b128 v[16:19], v0 offset:22528\n"
        "v_mfma_f32_16x16x32_bf16 v[24:27], a[228:231], v[20:23], v[24:27]\n"
        "s_cmp_le_u32 s88, 15\n"
        "s_cselect_b32 s88, s73, s89\n"
        "s_cselect_b32 s73, s89, s73\n"
        "v_mfma_f32_16x16x32_bf16 v[28:31], a[232:235], v[20:23], v[28:31]\n"
        "buffer_store_dword v5, v3, s[40:43], s88 offen sc1\n"
        "v_mfma_f32_16x16x32_bf16 v[32:35], a[236:239], v[20:23], v[32:35]\n"
        "s_waitcnt lgkmcnt(1)\n"
        "ds_read_b128 v[20:23], v0 offset:23552\n"
        "v_mfma_f32_16x16x32_bf16 v[24:27], a[240:243], v[8:11], v[24:27]\n"
        "v_mfma_f32_16x16x32_bf16 v[28:31], a[244:247], v[8:11], v[28:31]\n"
        "v_mfma_f32_16x16x32_bf16 v[32:35], a[248:251], v[8:11], v[32:35]\n"
        "ds_read_b128 v[8:11], v0 offset:24576\n"
        "v_mfma_f32_16x16x32_bf16 v[24:27], a[252:255], v[12:15], v[24:27]\n"
        "v_mfma_f32_16x16x32_bf16 v[28:31], v[80:83], v[12:15], v[28:31]\n"
        "v_mfma_f32_16x16x32_bf16 v[32:35], v[84:87], v[12:15], v[32:35]\n"
        "s_waitcnt lgkmcnt(1)\n"
        "ds_read_b128 v[12:15], v0 offset:25600\n"
        "v_mfma_f32_16x16x32_bf16 v[24:27], v[88:91], v[16:19], v[24:27]\n"
        "v_mfma_f32_16x16x32_bf16 v[28:31], v[92:95], v[16:19], v[28:31]\n"
        "buffer_store_dword v5, v3, s[40:43], s73 offen sc1\n"
        "v_mfma_f32_16x16x32_bf16 v[32:35], v[96:99], v[16:19], v[32:35]\n"
        "s_mov_b32 s84, s89\n"
        "ds_read_b128 v[16:19], v0 offset:26624\n"
        "s_mov_b32 s73, s60\n"
        "v_mfma_f32_16x16x32_bf16 v[24:27], v[100:103], v[20:23], v[24:27]\n"
        "s_add_u32 s74, s74, 1\n"
        "s_add_u32 s68, s68, 1\n"
        "s_lshr_b32 s66, s68, 1\n"
        "v_mfma_f32_16x16x32_bf16 v[28:31], v[104:107], v[20:23], v[28:31]\n"
        "s_bitcmp1_b32 s54, s66\n"
        "s_cselect_b32 s66, 8192, 0\n"
        "s_lshl_b32 s75, s68, 20\n"
        "v_mfma_f32_16x16x32_bf16 v[32:35], v[108:111], v[20:23], v[32:35]\n"
        "s_waitcnt lgkmcnt(1)\n"
        "ds_read_b128 v[20:23], v0 offset:27648\n"
        "s_add_u32 s75, s75, s66\n"
        "v_mfma_f32_16x16x32_bf16 v[24:27], v[112:115], v[8:11], v[24:27]\n"
        "s_add_u32 s72, s75, s59\n"
        "v_mfma_f32_16x16x32_bf16 v[28:31], v[116:119], v[8:11], v[28:31]\n"
        "v_mfma_f32_16x16x32_bf16 v[32:35], v[120:123], v[8:11], v[32:35]\n"
        "ds_read_b128 v[8:11], v0 offset:28672\n"
        "v_mfma_f32_16x16x32_bf16 v[24:27], v[124:127], v[12:15], v[24:27]\n"
        "v_mfma_f32_16x16x32_bf16 v[28:31], v[128:131], v[12:15], v[28:31]\n"
        "v_mfma_f32_16x16x32_bf16 v[32:35], v[132:135], v[12:15], v[32:35]\n"
        "s_waitcnt lgkmcnt(1)\n"
        "ds_read_b128 v[12:15], v0 offset:29696\n"
        "v_mfma_f32_16x16x32_bf16 v[24:27], v[136:139], v[16:19], v[24:27]\n"
        "v_mfma_f32_16x16x32_bf16 v[28:31], v[140:143], v[16:19], v[28:31]\n"
        "v_mfma_f32_16x16x32_bf16 v[32:35], v[144:147], v[16:19], v[32:35]\n"
        "ds_read_b128 v[16:19], v0 offset:30720\n"
        "v_mfma_f32_16x16x32_bf16 v[24:27], v[148:151], v[20:23], v[24:27]\n"
        "s_waitcnt vmcnt(3)\n"
        "v_cmp_gt_i32 vcc, s70, v4\n"
        "v_mfma_f32_16x16x32_bf16 v[28:31], v[152:155], v[20:23], v[28:31]\n"
        "v_mfma_f32_16x16x32_bf16 v[32:35], v[156:159], v[20:23], v[32:35]\n"
        "s_waitcnt lgkmcnt(1)\n"
        "ds_read_b128 v[20:23], v0 offset:31744\n"
        "s_and_b64 vcc, vcc, 3\n"
        "v_mfma_f32_16x16x32_bf16 v[24:27], v[160:163], v[8:11], v[24:27]\n"
        "s_cbranch_vccnz .Lop_i02_%=\n"
        "v_mfma_f32_16x16x32_bf16 v[28:31], v[164:167], v[8:11], v[28:31]\n"
        "buffer_load_dwordx4 v1, s[40:43], s71 offen offset:0 sc1 lds\n"
        "v_mfma_f32_16x16x32_bf16 v[32:35], v[168:171], v[8:11], v[32:35]\n"
        "v_mfma_f32_16x16x32_bf16 v[24:27], v[172:175], v[12:15], v[24:27]\n"
        "buffer_load_dwordx4 v1, s[40:43], s71 offen offset:1024 sc1 lds\n"
        "v_mfma_f32_16x16x32_bf16 v[28:31], v[176:179], v[12:15], v[28:31]\n"
        "v_mfma_f32_16x16x32_bf16 v[32:35], v[180:183], v[12:15], v[32:35]\n"
        "s_waitcnt lgkmcnt(0)\n"
        "buffer_load_dwordx4 v1, s[40:43], s71 offen offset:2048 sc1 lds\n"
        "v_mfma_f32_16x16x32_bf16 v[24:27], v[184:187], v[16:19], v[24:27]\n"
        "v_mfma_f32_16x16x32_bf16 v[28:31], v[188:191], v[16:19], v[28:31]\n"
        "buffer_load_dwordx4 v1, s[40:43], s71 offen offset:3072 sc1 lds\n"
        "v_mfma_f32_16x16x32_bf16 v[32:35], v[192:195], v[16:19], v[32:35]\n"
        "s_add_u32 m0, m0, 16384\n"
        "s_add_u32 s71, s71, 65536\n"
        "v_mfma_f32_16x16x32_bf16 v[24:27], v[196:199], v[20:23], v[24:27]\n"
        "buffer_load_dwordx4 v1, s[40:43], s71 offen offset:0 sc1 lds\n"
        "v_mfma_f32_16x16x32_bf16 v[28:31], v[200:203], v[20:23], v[28:31]\n"
        "v_mfma_f32_16x16x32_bf16 v[32:35], v[204:207], v[20:23], v[32:35]\n"
        "buffer_load_dwordx4 v1, s[40:43], s71 offen offset:1024 sc1 lds\n"
        "buffer_load_dwordx4 v1, s[40:43], s71 offen offset:2048 sc1 lds\n"
        "buffer_load_dwordx4 v1, s[40:43], s71 offen offset:3072 sc1 lds\n"
        "s_add_u32 s69, s69, 1\n"
        "s_nop 2\n"
        "v_cvt_pk_bf16_f32 v24, v24, v25\n"
        "v_cvt_pk_bf16_f32 v25, v26, v27\n"
        "v_cvt_pk_bf16_f32 v26, v28, v29\n"
        "v_cvt_pk_bf16_f32 v27, v30, v31\n"
        "buffer_store_dwordx4 v[24:27], v48, s[40:43], s72 offen offset:0 sc1\n"
        "v_cvt_pk_bf16_f32 v32, v32, v33\n"
        "v_cvt_pk_bf16_f32 v33, v34, v35\n"
        "s_nop 0\n"
        "v_permlane16_swap_b32 v32, v34\n"
        "v_permlane16_swap_b32 v33, v35\n"
        "buffer_store_dwordx4 v[32:35], v7, s[40:43], s72 offen offset:0 sc1\n"
        "v_mov_b32 v5, s74\n"
        "s_mov_b32 s84, s60\n"
        "s_waitcnt vmcnt(6)\n"
        "s_branch .Lop_go0_%=\n"
        ".Lop_i02_%=:\n"
        "v_mfma_f32_16x16x32_bf16 v[28:31], v[164:167], v[8:11], v[28:31]\n"
        "buffer_load_dword v4, v2, s[40:43], s86 offen sc1\n"
        "v_mfma_f32_16x16x32_bf16 v[32:35], v[168:171], v[8:11], v[32:35]\n"
        "v_mfma_f32_16x16x32_bf16 v[24:27], v[172:175], v[12:15], v[24:27]\n"
        "v_mfma_f32_16x16x32_bf16 v[28:31], v[176:179], v[12:15], v[28:31]\n"
        "v_mfma_f32_16x16x32_bf16 v[32:35], v[180:183], v[12:15], v[32:35]\n"
        "s_waitcnt lgkmcnt(0)\n"
        "v_mfma_f32_16x16x32_bf16 v[24:27], v[184:187], v[16:19], v[24:27]\n"
        "v_mfma_f32_16x16x32_bf16 v[28:31], v[188:191], v[16:19], v[28:31]\n"
        "v_mfma_f32_16x16x32_bf16 v[32:35], v[192:195], v[16:19], v[32:35]\n"
        "v_mfma_f32_16x16x32_bf16 v[24:27], v[196:199], v[20:23], v[24:27]\n"
        "v_mfma_f32_16x16x32_bf16 v[28:31], v[200:203], v[20:23], v[28:31]\n"
        "v_mfma_f32_16x16x32_bf16 v[32:35], v[204:207], v[20:23], v[32:35]\n"
        "s_nop 2\n"
        "v_cvt_pk_bf16_f32 v24, v24, v25\n"
        "v_cvt_pk_bf16_f32 v25, v26, v27\n"
        "v_cvt_pk_bf16_f32 v26, v28, v29\n"
        "v_cvt_pk_bf16_f32 v27, v30, v31\n"
        "buffer_store_dwordx4 v[24:27], v48, s[40:43], s72 offen offset:0 sc1\n"
        "v_cvt_pk_bf16_f32 v32, v32, v33\n"
        "v_cvt_pk_bf16_f32 v33, v34, v35\n"
        "s_nop 0\n"
        "v_permlane16_swap_b32 v32, v34\n"
        "v_permlane16_swap_b32 v33, v35\n"
        "buffer_store_dwordx4 v[32:35], v7, s[40:43], s72 offen offset:0 sc1\n"
        "s_waitcnt vmcnt(2)\n"
        "v_cmp_gt_i32 vcc, s70, v4\n"
        "s_nop 0\n"
        "s_and_b64 vcc, vcc, 3\n"
        "s_cbranch_vccnz .Lop_late0_%=\n"
        "buffer_load_dwordx4 v1, s[40:43], s71 offen offset:0 sc1 lds\n"
        "buffer_load_dwordx4 v1, s[40:43], s71 offen offset:1024 sc1 lds\n"
        "buffer_load_dwordx4 v1, s[40:43], s71 offen offset:2048 sc1 lds\n"
        "buffer_load_dwordx4 v1, s[40:43], s71 offen offset:3072 sc1 lds\n"
        "s_add_u32 m0, m0, 16384\n"
        "s_add_u32 s71, s71, 65536\n"
        "buffer_load_dwordx4 v1, s[40:43], s71 offen offset:0 sc1 lds\n"
        "buffer_load_dwordx4 v1, s[40:43], s71 offen offset:1024 sc1 lds\n"
        "buffer_load_dwordx4 v1, s[40:43], s71 offen offset:2048 sc1 lds\n"
        "buffer_load_dwordx4 v1, s[40:43], s71 offen offset:3072 sc1 lds\n"
        "s_add_u32 s69, s69, 1\n"
        "s_waitcnt vmcnt(8)\n"
        "v_mov_b32 v5, s74\n"
        "buffer_store_dword v5, v3, s[40:43], s73 offen sc1\n"
        "s_waitcnt vmcnt(5)\n"
        "s_branch .Lop_go0_%=\n"
        ".Lop_late0_%=:\n"
        "s_waitcnt vmcnt(0)\n"
        "v_mov_b32 v5, s74\n"
        "buffer_store_dword v5, v3, s[40:43], s73 offen sc1\n"
        "s_mov_b32 s64, 0\n"
        ".Lop_in0_top_%=:\n"
        "buffer_load_dword v4, v2, s[40:43], s86 offen sc1\n"
        "s_waitcnt vmcnt(0)\n"
        "v_cmp_gt_i32 vcc, s70, v4\n"
        "s_nop 0\n"
        "s_and_b64 vcc, vcc, 3\n"
        "s_cbranch_vccz .Lop_in0_ok_%=\n"
        "s_add_u32 s64, s64, 1\n"
        "s_and_b32 s65, s64, 255\n"
        "s_cbranch_scc1 .Lop_in0_go_%=\n"
        "s_mov_b32 s65, 49152\n"
        "s_cmp_lt_u32 s64, 1048576\n"
        "s_cbranch_scc1 .Lop_in0_chk_%=\n"
        "v_mov_b32 v4, 1\n"
        "buffer_store_dword v4, off, s[40:43], s65 sc1\n"
        "s_waitcnt vmcnt(0)\n"
        ".Lop_in0_chk_%=:\n"
        "buffer_load_dword v4, off, s[40:43], s65 sc1\n"
        "s_waitcnt vmcnt(0)\n"
        "v_readfirstlane_b32 s65, v4\n"
        "s_nop 0\n"
        "s_cmp_lg_u32 s65, 1\n"
        "s_cbranch_scc1 .Lop_in0_go_%=\n"
        "s_endpgm\n"
        ".Lop_in0_go_%=:\n"
        "s_branch .Lop_in0_top_%=\n"
        ".Lop_in0_ok_%=:\n"
        "buffer_load_dwordx4 v1, s[40:43], s71 offen offset:0 sc1 lds\n"
        "buffer_load_dwordx4 v1, s[40:43], s71 offen offset:1024 sc1 lds\n"
        "buffer_load_dwordx4 v1, s[40:43], s71 offen offset:2048 sc1 lds\n"
        "buffer_load_dwordx4 v1, s[40:43], s71 offen offset:3072 sc1 lds\n"
        "s_add_u32 m0, m0, 16384\n"
        "s_add_u32 s71, s71, 65536\n"
        "buffer_load_dwordx4 v1, s[40:43], s71 offen offset:0 sc1 lds\n"
        "buffer_load_dwordx4 v1, s[40:43], s71 offen offset:1024 sc1 lds\n"
        "buffer_load_dwordx4 v1, s[40:43], s71 offen offset:2048 sc1 lds\n"
        "buffer_load_dwordx4 v1, s[40:43], s71 offen offset:3072 sc1 lds\n"
        "s_add_u32 s69, s69, 1\n"
        "s_waitcnt vmcnt(4)\n"
        ".Lop_go0_%=:\n"
        "s_mov_b32 s72, 241172480\n"
        "s_mov_b32 s83, 241172480\n"
        "s_mov_b32 s73, 241172480\n"
        "v_add_u32 v0, s78, v6\n"
        "s_barrier\n"
        "ds_read_b128 v[8:11], v0 offset:0\n"
        "ds_read_b128 v[12:15], v0 offset:1024\n"
        "ds_read_b128 v[16:19], v0 offset:2048\n"
        "ds_read_b128 v[20:23], v0 offset:3072\n"
        "s_waitcnt lgkmcnt(2)\n"
        "s_branch .Lop_i1_%=\n"
        ".Lop_i1_%=:\n"
        "v_mfma_f32_16x16x32_bf16 v[36:39], a[0:3], v[8:11], 0\n"
        "buffer_load_dword v4, v2, s[40:43], s86 offen sc1\n"
        "v_mfma_f32_16x16x32_bf16 v[40:43], a[4:7], v[8:11], 0\n"
        "s_lshr_b32 s67, s69, 1\n"
        "s_bitcmp1_b64 s[52:53], s67\n"
        "s_cselect_b32 s65, 8192, 0\n"
        "v_mfma_f32_16x16x32_bf16 v[44:47], a[8:11], v[8:11], 0\n"
        "s_lshl_b32 s77, s67, 21\n"
        "ds_read_b128 v[8:11], v0 offset:4096\n"
        "s_add_u32 s77, s77, s65\n"
        "v_mfma_f32_16x16x32_bf16 v[36:39], a[12:15], v[12:15], v[36:39]\n"
        "s_add_u32 s71, s77, s58\n"
        "s_add_u32 s78, s78, 32768\n"
        "v_cvt_pk_bf16_f32 v24, v24, v25\n"
        "v_mfma_f32_16x16x32_bf16 v[40:43], a[16:19], v[12:15], v[40:43]\n"
        "s_cmp_eq_u32 s78, 98304\n"
        "s_cselect_b32 s78, 0, s78\n"
        "s_add_u32 m0, s78, s61\n"
        "v_mfma_f32_16x16x32_bf16 v[44:47], a[20:23], v[12:15], v[44:47]\n"
        "s_waitcnt lgkmcnt(1)\n"
        "ds_read_b128 v[12:15], v0 offset:5120\n"
        "s_lshr_b32 s70, s69, 0\n"
        "v_mfma_f32_16x16x32_bf16 v[36:39], a[24:27], v[16:19], v[36:39]\n"
        "v_cvt_pk_bf16_f32 v25, v26, v27\n"
        "v_cvt_pk_bf16_f32 v26, v28, v29\n"
        "v_mfma_f32_16x16x32_bf16 v[40:43], a[28:31], v[16:19], v[40:43]\n"
        "v_cvt_pk_bf16_f32 v27, v30, v31\n"
        "v_mfma_f32_16x16x32_bf16 v[44:47], a[32:35], v[16:19], v[44:47]\n"
        "ds_read_b128 v[16:19], v0 offset:6144\n"
        "v_mfma_f32_16x16x32_bf16 v[36:39], a[36:39], v[20:23], v[36:39]\n"
        "buffer_store_dwordx4 v[24:27], v48, s[40:43], s72 offen offset:0 sc1\n"
        "v_mfma_f32_16x16x32_bf16 v[40:43], a[40:43], v[20:23], v[40:43]\n"
        "v_cvt_pk_bf16_f32 v32, v32, v33\n"
        "v_cvt_pk_bf16_f32 v33, v34, v35\n"
        "v_mfma_f32_16x16x32_bf16 v[44:47], a[44:47], v[20:23], v[44:47]\n"
        "s_waitcnt lgkmcnt(1)\n"
        "ds_read_b128 v[20:23], v0 offset:7168\n"
        "s_nop 0\n"
        "v_mfma_f32_16x16x32_bf16 v[36:39], a[48:51], v[8:11], v[36:39]\n"
        "v_permlane16_swap_b32 v32, v34\n"
        "v_mfma_f32_16x16x32_bf16 v[40:43], a[52:55], v[8:11], v[40:43]\n"
        "v_permlane16_swap_b32 v33, v35\n"
        "v_mfma_f32_16x16x32_bf16 v[44:47], a[56:59], v[8:11], v[44:47]\n"
        "ds_read_b128 v[8:11], v0 offset:8192\n"
        "v_mfma_f32_16x16x32_bf16 v[36:39], a[60:63], v[12:15], v[36:39]\n"
        "v_mfma_f32_16x16x32_bf16 v[40:43], a[64:67], v[12:15], v[40:43]\n"
        "buffer_store_dwordx4 v[32:35], v7, s[40:43], s72 offen offset:0 sc1\n"
        "v_mfma_f32_16x16x32_bf16 v[44:47], a[68:71], v[12:15], v[44:47]\n"
        "s_waitcnt lgkmcnt(1)\n"
        "ds_read_b128 v[12:15], v0 offset:9216\n"
        "v_mfma_f32_16x16x32_bf16 v[36:39], a[72:75], v[16:19], v[36:39]\n"
        "v_mfma_f32_16x16x32_bf16 v[40:43], a[76:79], v[16:19], v[40:43]\n"
        "s_waitcnt vmcnt(2)\n"
        "v_cmp_gt_i32 vcc, s70, v4\n"
        "v_mfma_f32_16x16x32_bf16 v[44:47], a[80:83], v[16:19], v[44:47]\n"
        "ds_read_b128 v[16:19], v0 offset:10240\n"
        "v_mfma_f32_16x16x32_bf16 v[36:39], a[84:87], v[20:23], v[36:39]\n"
        "s_and_b64 vcc, vcc, 3\n"
        "v_mfma_f32_16x16x32_bf16 v[40:43], a[88:91], v[20:23], v[40:43]\n"
        "s_cbranch_vccnz .Lop_i10_%=\n"
        "v_mfma_f32_16x16x32_bf16 v[44:47], a[92:95], v[20:23], v[44:47]\n"
        "s_waitcnt lgkmcnt(1)\n"
        "ds_read_b128 v[20:23], v0 offset:11264\n"
        "v_mfma_f32_16x16x32_bf16 v[36:39], a[96:99], v[8:11], v[36:39]\n"
        "buffer_load_dwordx4 v1, s[40:43], s71 offen offset:0 sc1 lds\n"
        "v_mfma_f32_16x16x32_bf16 v[40:43], a[100:103], v[8:11], v[40:43]\n"
        "v_mfma_f32_16x16x32_bf16 v[44:47], a[104:107], v[8:11], v[44:47]\n"
        "ds_read_b128 v[8:11], v0 offset:12288\n"
        "v_mfma_f32_16x16x32_bf16 v[36:39], a[108:111], v[12:15], v[36:39]\n"
        "buffer_load_dwordx4 v1, s[40:43], s71 offen offset:1024 sc1 lds\n"
        "v_mfma_f32_16x16x32_bf16 v[40:43], a[112:115], v[12:15], v[40:43]\n"
        "v_mfma_f32_16x16x32_bf16 v[44:47], a[116:119], v[12:15], v[44:47]\n"
        "s_waitcnt lgkmcnt(1)\n"
        "ds_read_b128 v[12:15], v0 offset:13312\n"
        "v_mfma_f32_16x16x32_bf16 v[36:39], a[120:123], v[16:19], v[36:39]\n"
        "v_mfma_f32_16x16x32_bf16 v[40:43], a[124:127], v[16:19], v[40:43]\n"
        "buffer_load_dwordx4 v1, s[40:43], s71 offen offset:2048 sc1 lds\n"
        "v_mfma_f32_16x16x32_bf16 v[44:47], a[128:131], v[16:19], v[44:47]\n"
        "ds_read_b128 v[16:19], v0 offset:14336\n"
        "v_mfma_f32_16x16x32_bf16 v[36:39], a[132:135], v[20:23], v[36:39]\n"
        "v_mfma_f32_16x16x32_bf16 v[40:43], a[136:139], v[20:23], v[40:43]\n"
        "v_mfma_f32_16x16x32_bf16 v[44:47], a[140:143], v[20:23], v[44:47]\n"
        "s_waitcnt lgkmcnt(1)\n"
        "ds_read_b128 v[20:23], v0 offset:15360\n"
        "v_mfma_f32_16x16x32_bf16 v[36:39], a[144:147], v[8:11], v[36:39]\n"
        "buffer_load_dwordx4 v1, s[40:43], s71 offen offset:3072 sc1 lds\n"
        "v_mfma_f32_16x16x32_bf16 v[40:43], a[148:151], v[8:11], v[40:43]\n"
        "s_barrier\n"
        "s_add_u32 m0, m0, 16384\n"
        "v_mfma_f32_16x16x32_bf16 v[44:47], a[152:155], v[8:11], v[44:47]\n"
        "s_add_u32 s71, s71, 65536\n"
        "ds_read_b128 v[8:11], v0 offset:16384\n"
        "v_mfma_f32_16x16x32_bf16 v[36:39], a[156:159], v[12:15], v[36:39]\n"
        "buffer_load_dwordx4 v1, s[40:43], s71 offen offset:0 sc1 lds\n"
        "v_mfma_f32_16x16x32_bf16 v[40:43], a[160:163], v[12:15], v[40:43]\n"
        "buffer_store_dword v5, v3, s[40:43], s84 offen sc1\n"
        "v_mfma_f32_16x16x32_bf16 v[44:47], a[164:167], v[12:15], v[44:47]\n"
        "s_waitcnt lgkmcnt(1)\n"
        "ds_read_b128 v[12:15], v0 offset:17408\n"
        "v_mov_b32 v5, s74\n"
        "v_mfma_f32_16x16x32_bf16 v[36:39], a[168:171], v[16:19], v[36:39]\n"
        "s_getreg_b32 s88, hwreg(HW_REG_IB_STS, 0, 4)\n"
        "v_mfma_f32_16x16x32_bf16 v[40:43], a[172:175], v[16:19], v[40:43]\n"
        "buffer_load_dwordx4 v1, s[40:43], s71 offen offset:1024 sc1 lds\n"
        "v_mfma_f32_16x16x32_bf16 v[44:47], a[176:179], v[16:19], v[44:47]\n"
        "ds_read_b128 v[16:19], v0 offset:18432\n"
        "v_mfma_f32_16x16x32_bf16 v[36:39], a[180:183], v[20:23], v[36:39]\n"
        "s_cmp_le_u32 s88, 6\n"
        "s_cselect_b32 s88, s73, s89\n"
        "s_cselect_b32 s73, s89, s73\n"
        "v_mfma_f32_16x16x32_bf16 v[40:43], a[184:187], v[20:23], v[40:43]\n"
        "buffer_store_dword v5, v3, s[40:43], s88 offen sc1\n"
        "v_mfma_f32_16x16x32_bf16 v[44:47], a[188:191], v[20:23], v[44:47]\n"
        "s_waitcnt lgkmcnt(1)\n"
        "ds_read_b128 v[20:23], v0 offset:19456\n"
        "v_mfma_f32_16x16x32_bf16 v[36:39], a[192:195], v[8:11], v[36:39]\n"
        "s_getreg_b32 s88, hwreg(HW_REG_IB_STS, 0, 4)\n"
        "buffer_load_dwordx4 v1, s[40:43], s71 offen offset:2048 sc1 lds\n"
        "v_mfma_f32_16x16x32_bf16 v[40:43], a[196:199], v[8:11], v[40:43]\n"
        "s_cmp_le_u32 s88, 8\n"
        "s_cselect_b32 s88, s73, s89\n"
        "s_cselect_b32 s73, s89, s73\n"
        "v_mfma_f32_16x16x32_bf16 v[44:47], a[200:203], v[8:11], v[44:47]\n"
        "ds_read_b128 v[8:11], v0 offset:20480\n"
        "v_mfma_f32_16x16x32_bf16 v[36:39], a[204:207], v[12:15], v[36:39]\n"
        "buffer_load_dwordx4 v1, s[40:43], s71 offen offset:3072 sc1 lds\n"
        "v_mfma_f32_16x16x32_bf16 v[40:43], a[208:211], v[12:15], v[40:43]\n"
        "s_add_u32 s69, s69, 1\n"
        "buffer_store_dword v5, v3, s[40:43], s88 offen sc1\n"
        "v_mfma_f32_16x16x32_bf16 v[44:47], a[212:215], v[12:15], v[44:47]\n"
        "s_waitcnt lgkmcnt(1)\n"
        "ds_read_b128 v[12:15], v0 offset:21504\n"
        "v_mfma_f32_16x16x32_bf16 v[36:39], a[216:219], v[16:19], v[36:39]\n"
        "v_mfma_f32_16x16x32_bf16 v[40:43], a[220:223], v[16:19], v[40:43]\n"
        "v_mfma_f32_16x16x32_bf16 v[44:47], a[224:227], v[16:19], v[44:47]\n"
        "s_getreg_b32 s88, hwreg(HW_REG_IB_STS, 0, 4)\n"
        "ds_read_b128 v[16:19], v0 offset:22528\n"
        "v_mfma_f32_16x16x32_bf16 v[36:39], a[228:231], v[20:23], v[36:39]\n"
        "s_cmp_le_u32 s88, 11\n"
        "s_cselect_b32 s88, s73, s89\n"
        "s_cselect_b32 s73, s89, s73\n"
        "v_mfma_f32_16x16x32_bf16 v[40:43], a[232:235], v[20:23], v[40:43]\n"
        "buffer_store_dword v5, v3, s[40:43], s88 offen sc1\n"
        "v_mfma_f32_16x16x32_bf16 v[44:47], a[236:239], v[20:23], v[44:47]\n"
        "s_waitcnt lgkmcnt(1)\n"
        "ds_read_b128 v[20:23], v0 offset:23552\n"
        "v_mfma_f32_16x16x32_bf16 v[36:39], a[240:243], v[8:11], v[36:39]\n"
        "v_mfma_f32_16x16x32_bf16 v[40:43], a[244:247], v[8:11], v[40:43]\n"
        "v_mfma_f32_16x16x32_bf16 v[44:47], a[248:251], v[8:11], v[44:47]\n"
        "ds_read_b128 v[8:11], v0 offset:24576\n"
        "v_mfma_f32_16x16x32_bf16 v[36:39], a[252:255], v[12:15], v[36:39]\n"
        "v_mfma_f32_16x16x32_bf16 v[40:43], v[80:83], v[12:15], v[40:43]\n"
        "v_mfma_f32_16x16x32_bf16 v[44:47], v[84:87], v[12:15], v[44:47]\n"
        "s_waitcnt lgkmcnt(1)\n"
        "ds_read_b128 v[12:15], v0 offset:25600\n"
        "v_mfma_f32_16x16x32_bf16 v[36:39], v[88:91], v[16:19], v[36:39]\n"
        "v_mfma_f32_16x16x32_bf16 v[40:43], v[92:95], v[16:19], v[40:43]\n"
        "v_mfma_f32_16x16x32_bf16 v[44:47], v[96:99], v[16:19], v[44:47]\n"
        "ds_read_b128 v[16:19], v0 offset:26624\n"
        "v_mfma_f32_16x16x32_bf16 v[36:39], v[100:103], v[20:23], v[36:39]\n"
        "v_mfma_f32_16x16x32_bf16 v[40:43], v[104:107], v[20:23], v[40:43]\n"
        "s_waitcnt vmcnt(12)\n"
        "buffer_store_dword v5, v3, s[40:43], s73 offen sc1\n"
        "v_mfma_f32_16x16x32_bf16 v[44:47], v[108:111], v[20:23], v[44:47]\n"
        "s_waitcnt lgkmcnt(1)\n"
        "ds_read_b128 v[20:23], v0 offset:27648\n"
        "s_mov_b32 s84, s89\n"
        "v_mfma_f32_16x16x32_bf16 v[36:39], v[112:115], v[8:11], v[36:39]\n"
        "s_mov_b32 s73, s60\n"
        "s_add_u32 s74, s74, 1\n"
        "s_add_u32 s72, s75, s59\n"
        "v_mfma_f32_16x16x32_bf16 v[40:43], v[116:119], v[8:11], v[40:43]\n"
        "s_add_u32 s72, s72, 147456\n"
        "v_mfma_f32_16x16x32_bf16 v[44:47], v[120:123], v[8:11], v[44:47]\n"
        "ds_read_b128 v[8:11], v0 offset:28672\n"
        "v_mfma_f32_16x16x32_bf16 v[36:39], v[124:127], v[12:15], v[36:39]\n"
        "v_mfma_f32_16x16x32_bf16 v[40:43], v[128:131], v[12:15], v[40:43]\n"
        "v_mfma_f32_16x16x32_bf16 v[44:47], v[132:135], v[12:15], v[44:47]\n"
        "s_waitcnt lgkmcnt(1)\n"
        "ds_read_b128 v[12:15], v0 offset:29696\n"
        "v_mfma_f32_16x16x32_bf16 v[36:39], v[136:139], v[16:19], v[36:39]\n"
        "v_mfma_f32_16x16x32_bf16 v[40:43], v[140:143], v[16:19], v[40:43]\n"
        "v_mfma_f32_16x16x32_bf16 v[44:47], v[144:147], v[16:19], v[44:47]\n"
        "ds_read_b128 v[16:19], v0 offset:30720\n"
        "v_mfma_f32_16x16x32_bf16 v[36:39], v[148:151], v[20:23], v[36:39]\n"
        "v_mfma_f32_16x16x32_bf16 v[40:43], v[152:155], v[20:23], v[40:43]\n"
        "v_mfma_f32_16x16x32_bf16 v[44:47], v[156:159], v[20:23], v[44:47]\n"
        "s_waitcnt lgkmcnt(1)\n"
        "ds_read_b128 v[20:23], v0 offset:31744\n"
        "v_mfma_f32_16x16x32_bf16 v[36:39], v[160:163], v[8:11], v[36:39]\n"
        "v_mfma_f32_16x16x32_bf16 v[40:43], v[164:167], v[8:11], v[40:43]\n"
        "s_waitcnt vmcnt(9)\n"
        "s_barrier\n"
        "v_add_u32 v0, s78, v6\n"
        "v_mfma_f32_16x16x32_bf16 v[44:47], v[168:171], v[8:11], v[44:47]\n"
        "ds_read_b128 v[8:11], v0 offset:0\n"
        "v_mfma_f32_16x16x32_bf16 v[36:39], v[172:175], v[12:15], v[36:39]\n"
        "v_mfma_f32_16x16x32_bf16 v[40:43], v[176:179], v[12:15], v[40:43]\n"
        "v_mfma_f32_16x16x32_bf16 v[44:47], v[180:183], v[12:15], v[44:47]\n"
        "s_waitcnt lgkmcnt(1)\n"
        "ds_read_b128 v[12:15], v0 offset:1024\n"
        "v_mfma_f32_16x16x32_bf16 v[36:39], v[184:187], v[16:19], v[36:39]\n"
        "v_mfma_f32_16x16x32_bf16 v[40:43], v[188:191], v[16:19], v[40:43]\n"
        "v_mfma_f32_16x16x32_bf16 v[44:47], v[192:195], v[16:19], v[44:47]\n"
        "ds_read_b128 v[16:19], v0 offset:2048\n"
        "v_mfma_f32_16x16x32_bf16 v[36:39], v[196:199], v[20:23], v[36:39]\n"
        "v_mfma_f32_16x16x32_bf16 v[40:43], v[200:203], v[20:23], v[40:43]\n"
        "v_mfma_f32_16x16x32_bf16 v[44:47], v[204:207], v[20:23], v[44:47]\n"
        "s_waitcnt lgkmcnt(1)\n"
        "ds_read_b128 v[20:23], v0 offset:3072\n"
        "s_branch .Lop_i0_%=\n"
        ".Lop_i10_%=:\n"
        "v_mfma_f32_16x16x32_bf16 v[44:47], a[92:95], v[20:23], v[44:47]\n"
        "s_waitcnt lgkmcnt(1)\n"
        "ds_read_b128 v[20:23], v0 offset:11264\n"
        "v_mfma_f32_16x16x32_bf16 v[36:39], a[96:99], v[8:11], v[36:39]\n"
        "buffer_load_dword v4, v2, s[40:43], s86 offen sc1\n"
        "v_mfma_f32_16x16x32_bf16 v[40:43], a[100:103], v[8:11], v[40:43]\n"
        "v_mfma_f32_16x16x32_bf16 v[44:47], a[104:107], v[8:11], v[44:47]\n"
        "ds_read_b128 v[8:11], v0 offset:12288\n"
        "v_mfma_f32_16x16x32_bf16 v[36:39], a[108:111], v[12:15], v[36:39]\n"
        "v_mfma_f32_16x16x32_bf16 v[40:43], a[112:115], v[12:15], v[40:43]\n"
        "v_mfma_f32_16x16x32_bf16 v[44:47], a[116:119], v[12:15], v[44:47]\n"
        "s_waitcnt lgkmcnt(1)\n"
        "ds_read_b128 v[12:15], v0 offset:13312\n"
        "v_mfma_f32_16x16x32_bf16 v[36:39], a[120:123], v[16:19], v[36:39]\n"
        "v_mfma_f32_16x16x32_bf16 v[40:43], a[124:127], v[16:19], v[40:43]\n"
        "v_mfma_f32_16x16x32_bf16 v[44:47], a[128:131], v[16:19], v[44:47]\n"
        "ds_read_b128 v[16:19], v0 offset:14336\n"
        "v_mfma_f32_16x16x32_bf16 v[36:39], a[132:135], v[20:23], v[36:39]\n"
        "v_mfma_f32_16x16x32_bf16 v[40:43], a[136:139], v[20:23], v[40:43]\n"
        "v_mfma_f32_16x16x32_bf16 v[44:47], a[140:143], v[20:23], v[44:47]\n"
        "s_waitcnt lgkmcnt(1)\n"
        "ds_read_b128 v[20:23], v0 offset:15360\n"
        "v_mfma_f32_16x16x32_bf16 v[36:39], a[144:147], v[8:11], v[36:39]\n"
        "v_mfma_f32_16x16x32_bf16 v[40:43], a[148:151], v[8:11], v[40:43]\n"
        "s_barrier\n"
        "v_mfma_f32_16x16x32_bf16 v[44:47], a[152:155], v[8:11], v[44:47]\n"
        "ds_read_b128 v[8:11], v0 offset:16384\n"
        "v_mfma_f32_16x16x32_bf16 v[36:39], a[156:159], v[12:15], v[36:39]\n"
        "buffer_store_dword v5, v3, s[40:43], s84 offen sc1\n"
        "v_mfma_f32_16x16x32_bf16 v[40:43], a[160:163], v[12:15], v[40:43]\n"
        "v_mov_b32 v5, s74\n"
        "v_mfma_f32_16x16x32_bf16 v[44:47], a[164:167], v[12:15], v[44:47]\n"
        "s_waitcnt lgkmcnt(1)\n"
        "ds_read_b128 v[12:15], v0 offset:17408\n"
        "v_mfma_f32_16x16x32_bf16 v[36:39], a[168:171], v[16:19], v[36:39]\n"
        "s_getreg_b32 s88, hwreg(HW_REG_IB_STS, 0, 4)\n"
        "v_mfma_f32_16x16x32_bf16 v[40:43], a[172:175], v[16:19], v[40:43]\n"
        "s_cmp_le_u32 s88, 2\n"
        "s_cselect_b32 s88, s73, s89\n"
        "s_cselect_b32 s73, s89, s73\n"
        "v_mfma_f32_16x16x32_bf16 v[44:47], a[176:179], v[16:19], v[44:47]\n"
        "ds_read_b128 v[16:19], v0 offset:18432\n"
        "v_mfma_f32_16x16x32_bf16 v[36:39], a[180:183], v[20:23], v[36:39]\n"
        "buffer_store_dword v5, v3, s[40:43], s88 offen sc1\n"
        "v_mfma_f32_16x16x32_bf16 v[40:43], a[184:187], v[20:23], v[40:43]\n"
        "v_mfma_f32_16x16x32_bf16 v[44:47], a[188:191], v[20:23], v[44:47]\n"
        "s_waitcnt lgkmcnt(1)\n"
        "ds_read_b128 v[20:23], v0 offset:19456\n"
        "v_mfma_f32_16x16x32_bf16 v[36:39], a[192:195], v[8:11], v[36:39]\n"
        "s_getreg_b32 s88, hwreg(HW_REG_IB_STS, 0, 4)\n"
        "v_mfma_f32_16x16x32_bf16 v[40:43], a[196:199], v[8:11], v[40:43]\n"
        "s_waitcnt vmcnt(2)\n"
        "v_cmp_gt_i32 vcc, s70, v4\n"
        "v_mfma_f32_16x16x32_bf16 v[44:47], a[200:203], v[8:11], v[44:47]\n"
        "ds_read_b128 v[8:11], v0 offset:20480\n"
        "v_mfma_f32_16x16x32_bf16 v[36:39], a[204:207], v[12:15], v[36:39]\n"
        "s_and_b64 vcc, vcc, 3\n"
        "v_mfma_f32_16x16x32_bf16 v[40:43], a[208:211], v[12:15], v[40:43]\n"
        "s_cbranch_vccnz .Lop_i11_%=\n"
        "v_mfma_f32_16x16x32_bf16 v[44:47], a[212:215], v[12:15], v[44:47]\n"
        "s_waitcnt lgkmcnt(1)\n"
        "ds_read_b128 v[12:15], v0 offset:21504\n"
        "v_mfma_f32_16x16x32_bf16 v[36:39], a[216:219], v[16:19], v[36:39]\n"
        "buffer_load_dwordx4 v1, s[40:43], s71 offen offset:0 sc1 lds\n"
        "v_mfma_f32_16x16x32_bf16 v[40:43], a[220:223], v[16:19], v[40:43]\n"
        "s_cmp_le_u32 s88, 3\n"
        "s_cselect_b32 s88, s73, s89\n"
        "s_cselect_b32 s73, s89, s73\n"
        "v_mfma_f32_16x16x32_bf16 v[44:47], a[224:227], v[16:19], v[44:47]\n"
        "ds_read_b128 v[16:19], v0 offset:22528\n"
        "v_mfma_f32_16x16x32_bf16 v[36:39], a[228:231], v[20:23], v[36:39]\n"
        "buffer_load_dwordx4 v1, s[40:43], s71 offen offset:1024 sc1 lds\n"
        "v_mfma_f32_16x16x32_bf16 v[40:43], a[232:235], v[20:23], v[40:43]\n"
        "buffer_store_dword v5, v3, s[40:43], s88 offen sc1\n"
        "v_mfma_f32_16x16x32_bf16 v[44:47], a[236:239], v[20:23], v[44:47]\n"
        "s_waitcnt lgkmcnt(1)\n"
        "ds_read_b128 v[20:23], v0 offset:23552\n"
        "v_mfma_f32_16x16x32_bf16 v[36:39], a[240:243], v[8:11], v[36:39]\n"
        "s_getreg_b32 s88, hwreg(HW_REG_IB_STS, 0, 4)\n"
        "buffer_load_dwordx4 v1, s[40:43], s71 offen offset:2048 sc1 lds\n"
        "v_mfma_f32_16x16x32_bf16 v[40:43], a[244:247], v[8:11], v[40:43]\n"
        "s_cmp_le_u32 s88, 15\n"
        "s_cselect_b32 s88, s73, s89\n"
        "s_cselect_b32 s73, s89, s73\n"
        "v_mfma_f32_16x16x32_bf16 v[44:47], a[248:251], v[8:11], v[44:47]\n"
        "ds_read_b128 v[8:11], v0 offset:24576\n"
        "v_mfma_f32_16x16x32_bf16 v[36:39], a[252:255], v[12:15], v[36:39]\n"
        "buffer_load_dwordx4 v1, s[40:43], s71 offen offset:3072 sc1 lds\n"
        "v_mfma_f32_16x16x32_bf16 v[40:43], v[80:83], v[12:15], v[40:43]\n"
        "s_add_u32 m0, m0, 16384\n"
        "s_add_u32 s71, s71, 65536\n"
        "v_mfma_f32_16x16x32_bf16 v[44:47], v[84:87], v[12:15], v[44:47]\n"
        "s_waitcnt lgkmcnt(1)\n"
        "ds_read_b128 v[12:15], v0 offset:25600\n"
        "v_mfma_f32_16x16x32_bf16 v[36:39], v[88:91], v[16:19], v[36:39]\n"
        "buffer_load_dwordx4 v1, s[40:43], s71 offen offset:0 sc1 lds\n"
        "v_mfma_f32_16x16x32_bf16 v[40:43], v[92:95], v[16:19], v[40:43]\n"
        "buffer_store_dword v5, v3, s[40:43], s88 offen sc1\n"
        "v_mfma_f32_16x16x32_bf16 v[44:47], v[96:99], v[16:19], v[44:47]\n"
        "ds_read_b128 v[16:19], v0 offset:26624\n"
        "v_mfma_f32_16x16x32_bf16 v[36:39], v[100:103], v[20:23], v[36:39]\n"
        "v_mfma_f32_16x16x32_bf16 v[40:43], v[104:107], v[20:23], v[40:43]\n"
        "buffer_store_dword v5, v3, s[40:43], s73 offen sc1\n"
        "v_mfma_f32_16x16x32_bf16 v[44:47], v[108:111], v[20:23], v[44:47]\n"
        "s_waitcnt lgkmcnt(1)\n"
        "ds_read_b128 v[20:23], v0 offset:27648\n"
        "s_mov_b32 s84, s89\n"
        "v_mfma_f32_16x16x32_bf16 v[36:39], v[112:115], v[8:11], v[36:39]\n"
        "s_mov_b32 s73, s60\n"
        "s_add_u32 s74, s74, 1\n"
        "s_add_u32 s72, s75, s59\n"
        "v_mfma_f32_16x16x32_bf16 v[40:43], v[116:119], v[8:11], v[40:43]\n"
        "s_add_u32 s72, s72, 147456\n"
        "buffer_load_dwordx4 v1, s[40:43], s71 offen offset:1024 sc1 lds\n"
        "v_mfma_f32_16x16x32_bf16 v[44:47], v[120:123], v[8:11], v[44:47]\n"
        "ds_read_b128 v[8:11], v0 offset:28672\n"
        "v_mfma_f32_16x16x32_bf16 v[36:39], v[124:127], v[12:15], v[36:39]\n"
        "v_mfma_f32_16x16x32_bf16 v[40:43], v[128:131], v[12:15], v[40:43]\n"
        "v_mfma_f32_16x16x32_bf16 v[44:47], v[132:135], v[12:15], v[44:47]\n"
        "s_waitcnt lgkmcnt(1)\n"
        "ds_read_b128 v[12:15], v0 offset:29696\n"
        "v_mfma_f32_16x16x32_bf16 v[36:39], v[136:139], v[16:19], v[36:39]\n"
        "buffer_load_dwordx4 v1, s[40:43], s71 offen offset:2048 sc1 lds\n"
        "v_mfma_f32_16x16x32_bf16 v[40:43], v[140:143], v[16:19], v[40:43]\n"
        "v_mfma_f32_16x16x32_bf16 v[44:47], v[144:147], v[16:19], v[44:47]\n"
        "ds_read_b128 v[16:19], v0 offset:30720\n"
        "v_mfma_f32_16x16x32_bf16 v[36:39], v[148:151], v[20:23], v[36:39]\n"
        "buffer_load_dwordx4 v1, s[40:43], s71 offen offset:3072 sc1 lds\n"
        "v_mfma_f32_16x16x32_bf16 v[40:43], v[152:155], v[20:23], v[40:43]\n"
        "s_add_u32 s69, s69, 1\n"
        "v_mfma_f32_16x16x32_bf16 v[44:47], v[156:159], v[20:23], v[44:47]\n"
        "s_waitcnt lgkmcnt(1)\n"
        "ds_read_b128 v[20:23], v0 offset:31744\n"
        "v_mfma_f32_16x16x32_bf16 v[36:39], v[160:163], v[8:11], v[36:39]\n"
        "v_mfma_f32_16x16x32_bf16 v[40:43], v[164:167], v[8:11], v[40:43]\n"
        "s_waitcnt vmcnt(6)\n"
        "s_barrier\n"
        "v_add_u32 v0, s78, v6\n"
        "v_mfma_f32_16x16x32_bf16 v[44:47], v[168:171], v[8:11], v[44:47]\n"
        "ds_read_b128 v[8:11], v0 offset:0\n"
        "v_mfma_f32_16x16x32_bf16 v[36:39], v[172:175], v[12:15], v[36:39]\n"
        "v_mfma_f32_16x16x32_bf16 v[40:43], v[176:179], v[12:15], v[40:43]\n"
        "v_mfma_f32_16x16x32_bf16 v[44:47], v[180:183], v[12:15], v[44:47]\n"
        "s_waitcnt lgkmcnt(1)\n"
        "ds_read_b128 v[12:15], v0 offset:1024\n"
        "v_mfma_f32_16x16x32_bf16 v[36:39], v[184:187], v[16:19], v[36:39]\n"
        "v_mfma_f32_16x16x32_bf16 v[40:43], v[188:191], v[16:19], v[40:43]\n"
        "v_mfma_f32_16x16x32_bf16 v[44:47], v[192:195], v[16:19], v[44:47]\n"
        "ds_read_b128 v[16:19], v0 offset:2048\n"
        "v_mfma_f32_16x16x32_bf16 v[36:39], v[196:199], v[20:23], v[36:39]\n"
        "v_mfma_f32_16x16x32_bf16 v[40:43], v[200:203], v[20:23], v[40:43]\n"
        "v_mfma_f32_16x16x32_bf16 v[44:47], v[204:207], v[20:23], v[44:47]\n"
        "s_waitcnt lgkmcnt(1)\n"
        "ds_read_b128 v[20:23], v0 offset:3072\n"
        "s_branch .Lop_i0_%=\n"
        ".Lop_i11_%=:\n"
        "v_mfma_f32_16x16x32_bf16 v[44:47], a[212:215], v[12:15], v[44:47]\n"
        "s_waitcnt lgkmcnt(1)\n"
        "ds_read_b128 v[12:15], v0 offset:21504\n"
        "v_mfma_f32_16x16x32_bf16 v[36:39], a[216:219], v[16:19], v[36:39]\n"
        "buffer_load_dword v4, v2, s[40:43], s86 offen sc1\n"
        "v_mfma_f32_16x16x32_bf16 v[40:43], a[220:223], v[16:19], v[40:43]\n"
        "s_cmp_le_u32 s88, 3\n"
        "s_cselect_b32 s88, s73, s89\n"
        "s_cselect_b32 s73, s89, s73\n"
        "v_mfma_f32_16x16x32_bf16 v[44:47], a[224:227], v[16:19], v[44:47]\n"
        "ds_read_b128 v[16:19], v0 offset:22528\n"
        "v_mfma_f32_16x16x32_bf16 v[36:39], a[228:231], v[20:23], v[36:39]\n"
        "buffer_store_dword v5, v3, s[40:43], s88 offen sc1\n"
        "v_mfma_f32_16x16x32_bf16 v[40:43], a[232:235], v[20:23], v[40:43]\n"
        "s_getreg_b32 s88, hwreg(HW_REG_IB_STS, 0, 4)\n"
        "v_mfma_f32_16x16x32_bf16 v[44:47], a[236:239], v[20:23], v[44:47]\n"
        "s_waitcnt lgkmcnt(1)\n"
        "ds_read_b128 v[20:23], v0 offset:23552\n"
        "v_mfma_f32_16x16x32_bf16 v[36:39], a[240:243], v[8:11], v[36:39]\n"
        "s_cmp_le_u32 s88, 15\n"
        "s_cselect_b32 s88, s73, s89\n"
        "s_cselect_b32 s73, s89, s73\n"
        "v_mfma_f32_16x16x32_bf16 v[40:43], a[244:247], v[8:11], v[40:43]\n"
        "buffer_store_dword v5, v3, s[40:43], s88 offen sc1\n"
        "v_mfma_f32_16x16x32_bf16 v[44:47], a[248:251], v[8:11], v[44:47]\n"
        "ds_read_b128 v[8:11], v0 offset:24576\n"
        "v_mfma_f32_16x16x32_bf16 v[36:39], a[252:255], v[12:15], v[36:39]\n"
        "v_mfma_f32_16x16x32_bf16 v[40:43], v[80:83], v[12:15], v[40:43]\n"
        "v_mfma_f32_16x16x32_bf16 v[44:47], v[84:87], v[12:15], v[44:47]\n"
        "s_waitcnt lgkmcnt(1)\n"
        "ds_read_b128 v[12:15], v0 offset:25600\n"
        "v_mfma_f32_16x16x32_bf16 v[36:39], v[88:91], v[16:19], v[36:39]\n"
        "v_mfma_f32_16x16x32_bf16 v[40:43], v[92:95], v[16:19], v[40:43]\n"
        "v_mfma_f32_16x16x32_bf16 v[44:47], v[96:99], v[16:19], v[44:47]\n"
        "ds_read_b128 v[16:19], v0 offset:26624\n"
        "v_mfma_f32_16x16x32_bf16 v[36:39], v[100:103], v[20:23], v[36:39]\n"
        "v_mfma_f32_16x16x32_bf16 v[40:43], v[104:107], v[20:23], v[40:43]\n"
        "buffer_store_dword v5, v3, s[40:43], s73 offen sc1\n"
        "v_mfma_f32_16x16x32_bf16 v[44:47], v[108:111], v[20:23], v[44:47]\n"
        "s_waitcnt lgkmcnt(1)\n"
        "ds_read_b128 v[20:23], v0 offset:27648\n"
        "s_mov_b32 s84, s89\n"
        "v_mfma_f32_16x16x32_bf16 v[36:39], v[112:115], v[8:11], v[36:39]\n"
        "s_mov_b32 s73, s60\n"
        "s_add_u32 s74, s74, 1\n"
        "s_add_u32 s72, s75, s59\n"
        "v_mfma_f32_16x16x32_bf16 v[40:43], v[116:119], v[8:11], v[40:43]\n"
        "s_add_u32 s72, s72, 147456\n"
        "v_mfma_f32_16x16x32_bf16 v[44:47], v[120:123], v[8:11], v[44:47]\n"
        "ds_read_b128 v[8:11], v0 offset:28672\n"
        "v_mfma_f32_16x16x32_bf16 v[36:39], v[124:127], v[12:15], v[36:39]\n"
        "v_mfma_f32_16x16x32_bf16 v[40:43], v[128:131], v[12:15], v[40:43]\n"
        "v_mfma_f32_16x16x32_bf16 v[44:47], v[132:135], v[12:15], v[44:47]\n"
        "s_waitcnt lgkmcnt(1)\n"
        "ds_read_b128 v[12:15], v0 offset:29696\n"
        "v_mfma_f32_16x16x32_bf16 v[36:39], v[136:139], v[16:19], v[36:39]\n"
        "v_mfma_f32_16x16x32_bf16 v[40:43], v[140:143], v[16:19], v[40:43]\n"
        "v_mfma_f32_16x16x32_bf16 v[44:47], v[144:147], v[16:19], v[44:47]\n"
        "ds_read_b128 v[16:19], v0 offset:30720\n"
        "v_mfma_f32_16x16x32_bf16 v[36:39], v[148:151], v[20:23], v[36:39]\n"
        "s_waitcnt vmcnt(3)\n"
        "v_cmp_gt_i32 vcc, s70, v4\n"
        "v_mfma_f32_16x16x32_bf16 v[40:43], v[152:155], v[20:23], v[40:43]\n"
        "v_mfma_f32_16x16x32_bf16 v[44:47], v[156:159], v[20:23], v[44:47]\n"
        "s_waitcnt lgkmcnt(1)\n"
        "ds_read_b128 v[20:23], v0 offset:31744\n"
        "s_and_b64 vcc, vcc, 3\n"
        "v_mfma_f32_16x16x32_bf16 v[36:39], v[160:163], v[8:11], v[36:39]\n"
        "s_cbranch_vccnz .Lop_i12_%=\n"
        "v_mfma_f32_16x16x32_bf16 v[40:43], v[164:167], v[8:11], v[40:43]\n"
        "buffer_load_dwordx4 v1, s[40:43], s71 offen offset:0 sc1 lds\n"
        "v_mfma_f32_16x16x32_bf16 v[44:47], v[168:171], v[8:11], v[44:47]\n"
        "v_mfma_f32_16x16x32_bf16 v[36:39], v[172:175], v[12:15], v[36:39]\n"
        "buffer_load_dwordx4 v1, s[40:43], s71 offen offset:1024 sc1 lds\n"
        "v_mfma_f32_16x16x32_bf16 v[40:43], v[176:179], v[12:15], v[40:43]\n"
        "v_mfma_f32_16x16x32_bf16 v[44:47], v[180:183], v[12:15], v[44:47]\n"
        "s_waitcnt lgkmcnt(0)\n"
        "buffer_load_dwordx4 v1, s[40:43], s71 offen offset:2048 sc1 lds\n"
        "v_mfma_f32_16x16x32_bf16 v[36:39], v[184:187], v[16:19], v[36:39]\n"
        "v_mfma_f32_16x16x32_bf16 v[40:43], v[188:191], v[16:19], v[40:43]\n"
        "buffer_load_dwordx4 v1, s[40:43], s71 offen offset:3072 sc1 lds\n"
        "v_mfma_f32_16x16x32_bf16 v[44:47], v[192:195], v[16:19], v[44:47]\n"
        "s_add_u32 m0, m0, 16384\n"
        "s_add_u32 s71, s71, 65536\n"
        "v_mfma_f32_16x16x32_bf16 v[36:39], v[196:199], v[20:23], v[36:39]\n"
        "buffer_load_dwordx4 v1, s[40:43], s71 offen offset:0 sc1 lds\n"
        "v_mfma_f32_16x16x32_bf16 v[40:43], v[200:203], v[20:23], v[40:43]\n"
        "v_mfma_f32_16x16x32_bf16 v[44:47], v[204:207], v[20:23], v[44:47]\n"
        "buffer_load_dwordx4 v1, s[40:43], s71 offen offset:1024 sc1 lds\n"
        "buffer_load_dwordx4 v1, s[40:43], s71 offen offset:2048 sc1 lds\n"
        "buffer_load_dwordx4 v1, s[40:43], s71 offen offset:3072 sc1 lds\n"
        "s_add_u32 s69, s69, 1\n"
        "s_nop 2\n"
        "v_cvt_pk_bf16_f32 v36, v36, v37\n"
        "v_cvt_pk_bf16_f32 v37, v38, v39\n"
        "v_cvt_pk_bf16_f32 v38, v40, v41\n"
        "v_cvt_pk_bf16_f32 v39, v42, v43\n"
        "buffer_store_dwordx4 v[36:39], v48, s[40:43], s72 offen offset:0 sc1\n"
        "v_cvt_pk_bf16_f32 v44, v44, v45\n"
        "v_cvt_pk_bf16_f32 v45, v46, v47\n"
        "s_nop 0\n"
        "v_permlane16_swap_b32 v44, v46\n"
        "v_permlane16_swap_b32 v45, v47\n"
        "buffer_store_dwordx4 v[44:47], v7, s[40:43], s72 offen offset:0 sc1\n"
        "v_mov_b32 v5, s74\n"
        "s_mov_b32 s84, s60\n"
        "s_waitcnt vmcnt(6)\n"
        "s_branch .Lop_go1_%=\n"
        ".Lop_i12_%=:\n"
        "v_mfma_f32_16x16x32_bf16 v[40:43], v[164:167], v[8:11], v[40:43]\n"
        "buffer_load_dword v4, v2, s[40:43], s86 offen sc1\n"
        "v_mfma_f32_16x16x32_bf16 v[44:47], v[168:171], v[8:11], v[44:47]\n"
        "v_mfma_f32_16x16x32_bf16 v[36:39], v[172:175], v[12:15], v[36:39]\n"
        "v_mfma_f32_16x16x32_bf16 v[40:43], v[176:179], v[12:15], v[40:43]\n"
        "v_mfma_f32_16x16x32_bf16 v[44:47], v[180:183], v[12:15], v[44:47]\n"
        "s_waitcnt lgkmcnt(0)\n"
        "v_mfma_f32_16x16x32_bf16 v[36:39], v[184:187], v[16:19], v[36:39]\n"
        "v_mfma_f32_16x16x32_bf16 v[40:43], v[188:191], v[16:19], v[40:43]\n"
        "v_mfma_f32_16x16x32_bf16 v[44:47], v[192:195], v[16:19], v[44:47]\n"
        "v_mfma_f32_16x16x32_bf16 v[36:39], v[196:199], v[20:23], v[36:39]\n"
        "v_mfma_f32_16x16x32_bf16 v[40:43], v[200:203], v[20:23], v[40:43]\n"
        "v_mfma_f32_16x16x32_bf16 v[44:47], v[204:207], v[20:23], v[44:47]\n"
        "s_nop 2\n"
        "v_cvt_pk_bf16_f32 v36, v36, v37\n"
        "v_cvt_pk_bf16_f32 v37, v38, v39\n"
        "v_cvt_pk_bf16_f32 v38, v40, v41\n"
        "v_cvt_pk_bf16_f32 v39, v42, v43\n"
        "buffer_store_dwordx4 v[36:39], v48, s[40:43], s72 offen offset:0 sc1\n"
        "v_cvt_pk_bf16_f32 v44, v44, v45\n"
        "v_cvt_pk_bf16_f32 v45, v46, v47\n"
        "s_nop 0\n"
        "v_permlane16_swap_b32 v44, v46\n"
        "v_permlane16_swap_b32 v45, v47\n"
        "buffer_store_dwordx4 v[44:47], v7, s[40:43], s72 offen offset:0 sc1\n"
        "s_waitcnt vmcnt(2)\n"
        "v_cmp_gt_i32 vcc, s70, v4\n"
        "s_nop 0\n"
        "s_and_b64 vcc, vcc, 3\n"
        "s_cbranch_vccnz .Lop_late1_%=\n"
        "buffer_load_dwordx4 v1, s[40:43], s71 offen offset:0 sc1 lds\n"
        "buffer_load_dwordx4 v1, s[40:43], s71 offen offset:1024 sc1 lds\n"
        "buffer_load_dwordx4 v1, s[40:43], s71 offen offset:2048 sc1 lds\n"
        "buffer_load_dwordx4 v1, s[40:43], s71 offen offset:3072 sc1 lds\n"
        "s_add_u32 m0, m0, 16384\n"
        "s_add_u32 s71, s71, 65536\n"
        "buffer_load_dwordx4 v1, s[40:43], s71 offen offset:0 sc1 lds\n"
        "buffer_load_dwordx4 v1, s[40:43], s71 offen offset:1024 sc1 lds\n"
        "buffer_load_dwordx4 v1, s[40:43], s71 offen offset:2048 sc1 lds\n"
        "buffer_load_dwordx4 v1, s[40:43], s71 offen offset:3072 sc1 lds\n"
        "s_add_u32 s69, s69, 1\n"
        "s_waitcnt vmcnt(8)\n"
        "v_mov_b32 v5, s74\n"
        "buffer_store_dword v5, v3, s[40:43], s73 offen sc1\n"
        "s_waitcnt vmcnt(5)\n"
        "s_branch .Lop_go1_%=\n"
        ".Lop_late1_%=:\n"
        "s_waitcnt vmcnt(0)\n"
        "v_mov_b32 v5, s74\n"
        "buffer_store_dword v5, v3, s[40:43], s73 offen sc1\n"
        "s_cmp_eq_u32 s69, 128\n"
        "s_cbranch_scc0 .Lop_wait1_%=\n"
        "s_endpgm\n"
        ".Lop_wait1_%=:\n"
        "s_mov_b32 s64, 0\n"
        ".Lop_in1_top_%=:\n"
        "buffer_load_dword v4, v2, s[40:43], s86 offen sc1\n"
        "s_waitcnt vmcnt(0)\n"
        "v_cmp_gt_i32 vcc, s70, v4\n"
        "s_nop 0\n"
        "s_and_b64 vcc, vcc, 3\n"
        "s_cbranch_vccz .Lop_in1_ok_%=\n"
        "s_add_u32 s64, s64, 1\n"
        "s_and_b32 s65, s64, 255\n"
        "s_cbranch_scc1 .Lop_in1_go_%=\n"
        "s_mov_b32 s65, 49152\n"
        "s_cmp_lt_u32 s64, 1048576\n"
        "s_cbranch_scc1 .Lop_in1_chk_%=\n"
        "v_mov_b32 v4, 1\n"
        "buffer_store_dword v4, off, s[40:43], s65 sc1\n"
        "s_waitcnt vmcnt(0)\n"
        ".Lop_in1_chk_%=:\n"
        "buffer_load_dword v4, off, s[40:43], s65 sc1\n"
        "s_waitcnt vmcnt(0)\n"
        "v_readfirstlane_b32 s65, v4\n"
        "s_nop 0\n"
        "s_cmp_lg_u32 s65, 1\n"
        "s_cbranch_scc1 .Lop_in1_go_%=\n"
        "s_endpgm\n"
        ".Lop_in1_go_%=:\n"
        "s_branch .Lop_in1_top_%=\n"
        ".Lop_in1_ok_%=:\n"
        "buffer_load_dwordx4 v1, s[40:43], s71 offen offset:0 sc1 lds\n"
        "buffer_load_dwordx4 v1, s[40:43], s71 offen offset:1024 sc1 lds\n"
        "buffer_load_dwordx4 v1, s[40:43], s71 offen offset:2048 sc1 lds\n"
        "buffer_load_dwordx4 v1, s[40:43], s71 offen offset:3072 sc1 lds\n"
        "s_add_u32 m0, m0, 16384\n"
        "s_add_u32 s71, s71, 65536\n"
        "buffer_load_dwordx4 v1, s[40:43], s71 offen offset:0 sc1 lds\n"
        "buffer_load_dwordx4 v1, s[40:43], s71 offen offset:1024 sc1 lds\n"
        "buffer_load_dwordx4 v1, s[40:43], s71 offen offset:2048 sc1 lds\n"
        "buffer_load_dwordx4 v1, s[40:43], s71 offen offset:3072 sc1 lds\n"
        "s_add_u32 s69, s69, 1\n"
        "s_waitcnt vmcnt(4)\n"
        ".Lop_go1_%=:\n"
        "s_mov_b32 s72, 241172480\n"
        "s_mov_b32 s83, 241172480\n"
        "s_mov_b32 s73, 241172480\n"
        "v_add_u32 v0, s78, v6\n"
        "s_barrier\n"
        "ds_read_b128 v[8:11], v0 offset:0\n"
        "ds_read_b128 v[12:15], v0 offset:1024\n"
        "ds_read_b128 v[16:19], v0 offset:2048\n"
        "ds_read_b128 v[20:23], v0 offset:3072\n"
        "s_waitcnt lgkmcnt(2)\n"
        "s_branch .Lop_i0_%=\n"
        ".Lop_end_%=:\n"
            :: "s"(p.ws), "s"(p.Wo), "s"(p.Wo), "s"(p.Wo), "s"(pair(w, delay)), "s"(pair(x4, pr)),
               "s"(pair(PAGE + spread(A_Q) + (8 * kap + w) * 16384, 65 * PAGE + spread(B_P3))), "s"(pair(flag(F_OP + fl), w * 4096)), "s"(pair(0, 0)),
               "v"(pair(lane * 16, lane * 16)), "v"(pair(lane < 2 ? flag(F_AT + (8 * kap + w + 4 * lane) * 3 + nu) : OOB, fo4)), K_ARGS
            : "v0", "v1", "v2", "v3", "v4", "v5", "v6", "v7", "v8", "v9", "v10", "v11", "v12", "v13", "v14", "v15", "v16", "v17", "v18", "v19", "v20", "v21", "v22", "v23", "v24", "v25", "v26", "v27", "v28", "v29", "v30", "v31", "v32", "v33", "v34", "v35", "v36", "v37", "v38", "v39", "v40", "v41", "v42", "v43", "v44", "v45", "v46", "v47", "v48", "v49", "v50", "v51", "v52", "v53", "v54", "v55", "v56", "v57", "v58", "v59", "v60", "v61", "v62", "v63", "v64", "v65", "v66", "v67", "v68", "v69", "v70", "v71", "v72", "v73", "v74", "v75", "v76", "v77", "v78", "v79", "v255", "a255", "s26", "s27", "s28", "s29", "s30", "s31", "s32", "s33", "s34", "s35", "s36", "s37", "s38", "s39", "s40", "s41", "s42", "s43", "s44", "s45", "s46", "s47", "s48", "s49", "s50", "s51", "s52", "s53", "s54", "s55", "s56", "s57", "s58", "s59", "s60", "s61", "s62", "s63", "s64", "s65", "s66", "s67", "s68", "s69", "s70", "s71", "s72", "s73", "s74", "s75", "s76", "s77", "s78", "s79", "s80", "s81", "s82", "s83", "s84", "s85", "s86", "s87", "s88", "s89", "s90", "s91", "s92", "s93", "s94", "s95", "s96", "s97", "s98", "s99", "vcc", "scc", "memory");
    } else {
        const unsigned hb = 65 * PAGE + spread(B_H + kap * 73728);
        asm volatile(
        "s_nop 4\n"
        "s_mov_b64 s[40:41], %0\n"
        "s_and_b32 s41, s41, 0xffff\n"
        "s_mov_b32 s42, 236978176\n"
        "s_mov_b32 s43, 0x20000\n"
        "s_mov_b64 s[48:49], %4\n"
        "s_mov_b64 s[56:57], %5\n"
        "s_mov_b64 s[58:59], %6\n"
        "s_mov_b64 s[60:61], %7\n"
        "s_mov_b64 s[62:63], %8\n"
        "v_mov_b64 v[0:1], %9\n"
        "v_mov_b64 v[2:3], %10\n"
        "v_mov_b64 v[4:5], %11\n"
        "v_mov_b64 v[6:7], %12\n"
        "v_mov_b64 v[8:9], %13\n"
        "v_mov_b64 v[10:11], %14\n"
        "v_mov_b64 v[12:13], %15\n"
        "v_mov_b64 v[14:15], %16\n"
        "s_mov_b32 s47, 0x20000\n"
        "s_mul_i32 s79, s48, 40960\n"
        ".Lf2_start_%=:\n"
        "s_sub_u32 s49, s49, 1\n"
        "s_cbranch_scc1 .Lf2_start2_%=\n"
        "s_sleep 16\n"
        "s_branch .Lf2_start_%=\n"
        ".Lf2_start2_%=:\n"
        "s_mov_b64 s[44:45], %1\n"
        "s_and_b32 s45, s45, 0xffff\n"
        "s_mov_b32 s46, 5308416\n"
        "s_add_u32 m0, s79, 0\n"
        "s_mov_b32 s65, 0\n"
        "buffer_load_dwordx4 v8, s[44:47], s65 offen lds\n"
        "buffer_load_dwordx4 v9, s[44:47], s65 offen offset:1024 lds\n"
        "buffer_load_dwordx4 v10, s[44:47], s65 offen offset:2048 lds\n"
        "s_add_u32 m0, s79, 3072\n"
        "s_mov_b32 s65, 55296\n"
        "buffer_load_dwordx4 v8, s[44:47], s65 offen lds\n"
        "buffer_load_dwordx4 v9, s[44:47], s65 offen offset:1024 lds\n"
        "buffer_load_dwordx4 v10, s[44:47], s65 offen offset:2048 lds\n"
        "s_waitcnt vmcnt(3)\n"
        "ds_read_b64_tr_b16 a[0:1], v11 offset:0\n"
        "ds_read_b64_tr_b16 a[2:3], v11 offset:1536\n"
        "ds_read_b64_tr_b16 a[4:5], v11 offset:8\n"
        "ds_read_b64_tr_b16 a[6:7], v11 offset:1544\n"
        "ds_read_b64_tr_b16 a[8:9], v15 offset:0\n"
        "ds_read_b64_tr_b16 a[10:11], v15 offset:1536\n"
        "s_waitcnt vmcnt(0)\n"
        "ds_read_b64_tr_b16 a[12:13], v11 offset:3072\n"
        "ds_read_b64_tr_b16 a[14:15], v11 offset:4608\n"
        "ds_read_b64_tr_b16 a[16:17], v11 offset:3080\n"
        "ds_read_b64_tr_b16 a[18:19], v11 offset:4616\n"
        "ds_read_b64_tr_b16 a[20:21], v15 offset:3072\n"
        "ds_read_b64_tr_b16 a[22:23], v15 offset:4608\n"
        "s_waitcnt lgkmcnt(6)\n"
        "s_add_u32 m0, s79, 0\n"
        "s_mov_b32 s65, 110592\n"
        "buffer_load_dwordx4 v8, s[44:47], s65 offen lds\n"
        "buffer_load_dwordx4 v9, s[44:47], s65 offen offset:1024 lds\n"
        "buffer_load_dwordx4 v10, s[44:47], s65 offen offset:2048 lds\n"
        "s_waitcnt vmcnt(0)\n"
        "ds_read_b64_tr_b16 a[24:25], v11 offset:0\n"
        "ds_read_b64_tr_b16 a[26:27], v11 offset:1536\n"
        "ds_read_b64_tr_b16 a[28:29], v11 offset:8\n"
        "ds_read_b64_tr_b16 a[30:31], v11 offset:1544\n"
        "ds_read_b64_tr_b16 a[32:33], v15 offset:0\n"
        "ds_read_b64_tr_b16 a[34:35], v15 offset:1536\n"
        "s_waitcnt lgkmcnt(6)\n"
        "s_add_u32 m0, s79, 3072\n"
        "s_mov_b32 s65, 165888\n"
        "buffer_load_dwordx4 v8, s[44:47], s65 offen lds\n"
        "buffer_load_dwordx4 v9, s[44:47], s65 offen offset:1024 lds\n"
        "buffer_load_dwordx4 v10, s[44:47], s65 offen offset:2048 lds\n"
        "s_waitcnt vmcnt(0)\n"
        "ds_read_b64_tr_b16 a[36:37], v11 offset:3072\n"
        "ds_read_b64_tr_b16 a[38:39], v11 offset:4608\n"
        "ds_read_b64_tr_b16 a[40:41], v11 offset:3080\n"
        "ds_read_b64_tr_b16 a[42:43], v11 offset:4616\n"
        "ds_read_b64_tr_b16 a[44:45], v15 offset:3072\n"
        "ds_read_b64_tr_b16 a[46:47], v15 offset:4608\n"
        "s_waitcnt lgkmcnt(6)\n"
        "s_add_u32 m0, s79, 0\n"
        "s_mov_b32 s65, 221184\n"
        "buffer_load_dwordx4 v8, s[44:47], s65 offen lds\n"
        "buffer_load_dwordx4 v9, s[44:47], s65 offen offset:1024 lds\n"
        "buffer_load_dwordx4 v10, s[44:47], s65 offen offset:2048 lds\n"
        "s_waitcnt vmcnt(0)\n"
        "ds_read_b64_tr_b16 a[48:49], v11 offset:0\n"
        "ds_read_b64_tr_b16 a[50:51], v11 offset:1536\n"
        "ds_read_b64_tr_b16 a[52:53], v11 offset:8\n"
        "ds_read_b64_tr_b16 a[54:55], v11 offset:1544\n"
        "ds_read_b64_tr_b16 a[56:57], v15 offset:0\n"
        "ds_read_b64_tr_b16 a[58:59], v15 offset:1536\n"
        "s_waitcnt lgkmcnt(6)\n"
        "s_add_u32 m0, s79, 3072\n"
        "s_mov_b32 s65, 276480\n"
        "buffer_load_dwordx4 v8, s[44:47], s65 offen lds\n"
        "buffer_load_dwordx4 v9, s[44:47], s65 offen offset:1024 lds\n"
        "buffer_load_dwordx4 v10, s[44:47], s65 offen offset:2048 lds\n"
        "s_waitcnt vmcnt(0)\n"
        "ds_read_b64_tr_b16 a[60:61], v11 offset:3072\n"
        "ds_read_b64_tr_b16 a[62:63], v11 offset:4608\n"
        "ds_read_b64_tr_b16 a[64:65], v11 offset:3080\n"
        "ds_read_b64_tr_b16 a[66:67], v11 offset:4616\n"
        "ds_read_b64_tr_b16 a[68:69], v15 offset:3072\n"
        "ds_read_b64_tr_b16 a[70:71], v15 offset:4608\n"
        "s_waitcnt lgkmcnt(6)\n"
        "s_add_u32 m0, s79, 0\n"
        "s_mov_b32 s65, 331776\n"
        "buffer_load_dwordx4 v8, s[44:47], s65 offen lds\n"
        "buffer_load_dwordx4 v9, s[44:47], s65 offen offset:1024 lds\n"
        "buffer_load_dwordx4 v10, s[44:47], s65 offen offset:2048 lds\n"
        "s_waitcnt vmcnt(0)\n"
        "ds_read_b64_tr_b16 a[72:73], v11 offset:0\n"
        "ds_read_b64_tr_b16 a[74:75], v11 offset:1536\n"
        "ds_read_b64_tr_b16 a[76:77], v11 offset:8\n"
        "ds_read_b64_tr_b16 a[78:79], v11 offset:1544\n"
        "ds_read_b64_tr_b16 a[80:81], v15 offset:0\n"
        "ds_read_b64_tr_b16 a[82:83], v15 offset:1536\n"
        "s_waitcnt lgkmcnt(6)\n"
        "s_add_u32 m0, s79, 3072\n"
        "s_mov_b32 s65, 387072\n"
        "buffer_load_dwordx4 v8, s[44:47], s65 offen lds\n"
        "buffer_load_dwordx4 v9, s[44:47], s65 offen offset:1024 lds\n"
        "buffer_load_dwordx4 v10, s[44:47], s65 offen offset:2048 lds\n"
        "s_waitcnt vmcnt(0)\n"
        "ds_read_b64_tr_b16 a[84:85], v11 offset:3072\n"
        "ds_read_b64_tr_b16 a[86:87], v11 offset:4608\n"
        "ds_read_b64_tr_b16 a[88:89], v11 offset:3080\n"
        "ds_read_b64_tr_b16 a[90:91], v11 offset:4616\n"
        "ds_read_b64_tr_b16 a[92:93], v15 offset:3072\n"
        "ds_read_b64_tr_b16 a[94:95], v15 offset:4608\n"
        "s_waitcnt lgkmcnt(6)\n"
        "s_add_u32 m0, s79, 0\n"
        "s_mov_b32 s65, 0\n"
        "buffer_load_dwordx4 v12, s[44:47], s65 offen lds\n"
        "buffer_load_dwordx4 v13, s[44:47], s65 offen offset:1024 lds\n"
        "buffer_load_dwordx4 v14, s[44:47], s65 offen offset:2048 lds\n"
        "s_waitcnt vmcnt(0)\n"
        "ds_read_b64_tr_b16 a[96:97], v11 offset:0\n"
        "ds_read_b64_tr_b16 a[98:99], v11 offset:1536\n"
        "ds_read_b64_tr_b16 a[100:101], v11 offset:8\n"
        "ds_read_b64_tr_b16 a[102:103], v11 offset:1544\n"
        "ds_read_b64_tr_b16 a[104:105], v15 offset:0\n"
        "ds_read_b64_tr_b16 a[106:107], v15 offset:1536\n"
        "s_waitcnt lgkmcnt(6)\n"
        "s_add_u32 m0, s79, 3072\n"
        "s_mov_b32 s65, 110592\n"
        "buffer_load_dwordx4 v12, s[44:47], s65 offen lds\n"
        "buffer_load_dwordx4 v13, s[44:47], s65 offen offset:1024 lds\n"
        "buffer_load_dwordx4 v14, s[44:47], s65 offen offset:2048 lds\n"
        "s_waitcnt vmcnt(0)\n"
        "ds_read_b64_tr_b16 a[108:109], v11 offset:3072\n"
        "ds_read_b64_tr_b16 a[110:111], v11 offset:4608\n"
        "ds_read_b64_tr_b16 a[112:113], v11 offset:3080\n"
        "ds_read_b64_tr_b16 a[114:115], v11 offset:4616\n"
        "ds_read_b64_tr_b16 a[116:117], v15 offset:3072\n"
        "ds_read_b64_tr_b16 a[118:119], v15 offset:4608\n"
        "s_waitcnt lgkmcnt(6)\n"
        "s_add_u32 m0, s79, 0\n"
        "s_mov_b32 s65, 221184\n"
        "buffer_load_dwordx4 v12, s[44:47], s65 offen lds\n"
        "buffer_load_dwordx4 v13, s[44:47], s65 offen offset:1024 lds\n"
        "buffer_load_dwordx4 v14, s[44:47], s65 offen offset:2048 lds\n"
        "s_waitcnt vmcnt(0)\n"
        "ds_read_b64_tr_b16 a[120:121], v11 offset:0\n"
        "ds_read_b64_tr_b16 a[122:123], v11 offset:1536\n"
        "ds_read_b64_tr_b16 a[124:125], v11 offset:8\n"
        "ds_read_b64_tr_b16 a[126:127], v11 offset:1544\n"
        "ds_read_b64_tr_b16 a[128:129], v15 offset:0\n"
        "ds_read_b64_tr_b16 a[130:131], v15 offset:1536\n"
        "s_waitcnt lgkmcnt(6)\n"
        "s_add_u32 m0, s79, 3072\n"
        "s_mov_b32 s65, 331776\n"
        "buffer_load_dwordx4 v12, s[44:47], s65 offen lds\n"
        "buffer_load_dwordx4 v13, s[44:47], s65 offen offset:1024 lds\n"
        "buffer_load_dwordx4 v14, s[44:47], s65 offen offset:2048 lds\n"
        "s_waitcnt vmcnt(0)\n"
        "ds_read_b64_tr_b16 a[132:133], v11 offset:3072\n"
        "ds_read_b64_tr_b16 a[134:135], v11 offset:4608\n"
        "ds_read_b64_tr_b16 a[136:137], v11 offset:3080\n"
        "ds_read_b64_tr_b16 a[138:139], v11 offset:4616\n"
        "ds_read_b64_tr_b16 a[140:141], v15 offset:3072\n"
        "ds_read_b64_tr_b16 a[142:143], v15 offset:4608\n"
        "s_waitcnt lgkmcnt(6)\n"
        "s_add_u32 m0, s79, 0\n"
        "s_mov_b32 s65, 442368\n"
        "buffer_load_dwordx4 v8, s[44:47], s65 offen lds\n"
        "buffer_load_dwordx4 v9, s[44:47], s65 offen offset:1024 lds\n"
        "buffer_load_dwordx4 v10, s[44:47], s65 offen offset:2048 lds\n"
        "s_waitcnt vmcnt(0)\n"
        "ds_read_b64_tr_b16 a[144:145], v11 offset:0\n"
        "ds_read_b64_tr_b16 a[146:147], v11 offset:1536\n"
        "ds_read_b64_tr_b16 a[148:149], v11 offset:8\n"
        "ds_read_b64_tr_b16 a[150:151], v11 offset:1544\n"
        "ds_read_b64_tr_b16 a[152:153], v15 offset:0\n"
        "ds_read_b64_tr_b16 a[154:155], v15 offset:1536\n"
        "s_waitcnt lgkmcnt(6)\n"
        "s_add_u32 m0, s79, 3072\n"
        "s_mov_b32 s65, 497664\n"
        "buffer_load_dwordx4 v8, s[44:47], s65 offen lds\n"
        "buffer_load_dwordx4 v9, s[44:47], s65 offen offset:1024 lds\n"
        "buffer_load_dwordx4 v10, s[44:47], s65 offen offset:2048 lds\n"
        "s_waitcnt vmcnt(0)\n"
        "ds_read_b64_tr_b16 a[156:157], v11 offset:3072\n"
        "ds_read_b64_tr_b16 a[158:159], v11 offset:4608\n"
        "ds_read_b64_tr_b16 a[160:161], v11 offset:3080\n"
        "ds_read_b64_tr_b16 a[162:163], v11 offset:4616\n"
        "ds_read_b64_tr_b16 a[164:165], v15 offset:3072\n"
        "ds_read_b64_tr_b16 a[166:167], v15 offset:4608\n"
        "s_waitcnt lgkmcnt(6)\n"
        "s_add_u32 m0, s79, 0\n"
        "s_mov_b32 s65, 552960\n"
        "buffer_load_dwordx4 v8, s[44:47], s65 offen lds\n"
        "buffer_load_dwordx4 v9, s[44:47], s65 offen offset:1024 lds\n"
        "buffer_load_dwordx4 v10, s[44:47], s65 offen offset:2048 lds\n"
        "s_waitcnt vmcnt(0)\n"
        "ds_read_b64_tr_b16 a[168:169], v11 offset:0\n"
        "ds_read_b64_tr_b16 a[170:171], v11 offset:1536\n"
        "ds_read_b64_tr_b16 a[172:173], v11 offset:8\n"
        "ds_read_b64_tr_b16 a[174:175], v11 offset:1544\n"
        "ds_read_b64_tr_b16 a[176:177], v15 offset:0\n"
        "ds_read_b64_tr_b16 a[178:179], v15 offset:1536\n"
        "s_waitcnt lgkmcnt(6)\n"
        "s_add_u32 m0, s79, 3072\n"
        "s_mov_b32 s65, 608256\n"
        "buffer_load_dwordx4 v8, s[44:47], s65 offen lds\n"
        "buffer_load_dwordx4 v9, s[44:47], s65 offen offset:1024 lds\n"
        "buffer_load_dwordx4 v10, s[44:47], s65 offen offset:2048 lds\n"
        "s_waitcnt vmcnt(0)\n"
        "ds_read_b64_tr_b16 a[180:181], v11 offset:3072\n"
        "ds_read_b64_tr_b16 a[182:183], v11 offset:4608\n"
        "ds_read_b64_tr_b16 a[184:185], v11 offset:3080\n"
        "ds_read_b64_tr_b16 a[186:187], v11 offset:4616\n"
        "ds_read_b64_tr_b16 a[188:189], v15 offset:3072\n"
        "ds_read_b64_tr_b16 a[190:191], v15 offset:4608\n"
        "s_waitcnt lgkmcnt(6)\n"
        "s_add_u32 m0, s79, 0\n"
        "s_mov_b32 s65, 663552\n"
        "buffer_load_dwordx4 v8, s[44:47], s65 offen lds\n"
        "buffer_load_dwordx4 v9, s[44:47], s65 offen offset:1024 lds\n"
        "buffer_load_dwordx4 v10, s[44:47], s65 offen offset:2048 lds\n"
        "s_waitcnt vmcnt(0)\n"
        "ds_read_b64_tr_b16 a[192:193], v11 offset:0\n"
        "ds_read_b64_tr_b16 a[194:195], v11 offset:1536\n"
        "ds_read_b64_tr_b16 a[196:197], v11 offset:8\n"
        "ds_read_b64_tr_b16 a[198:199], v11 offset:1544\n"
        "ds_read_b64_tr_b16 a[200:201], v15 offset:0\n"
        "ds_read_b64_tr_b16 a[202:203], v15 offset:1536\n"
        "s_waitcnt lgkmcnt(6)\n"
        "s_add_u32 m0, s79, 3072\n"
        "s_mov_b32 s65, 718848\n"
        "buffer_load_dwordx4 v8, s[44:47], s65 offen lds\n"
        "buffer_load_dwordx4 v9, s[44:47], s65 offen offset:1024 lds\n"
        "buffer_load_dwordx4 v10, s[44:47], s65 offen offset:2048 lds\n"
        "s_waitcnt vmcnt(0)\n"
        "ds_read_b64_tr_b16 a[204:205], v11 offset:3072\n"
        "ds_read_b64_tr_b16 a[206:207], v11 offset:4608\n"
        "ds_read_b64_tr_b16 a[208:209], v11 offset:3080\n"
        "ds_read_b64_tr_b16 a[210:211], v11 offset:4616\n"
        "ds_read_b64_tr_b16 a[212:213], v15 offset:3072\n"
        "ds_read_b64_tr_b16 a[214:215], v15 offset:4608\n"
        "s_waitcnt lgkmcnt(6)\n"
        "s_add_u32 m0, s79, 0\n"
        "s_mov_b32 s65, 774144\n"
        "buffer_load_dwordx4 v8, s[44:47], s65 offen lds\n"
        "buffer_load_dwordx4 v9, s[44:47], s65 offen offset:1024 lds\n"
        "buffer_load_dwordx4 v10, s[44:47], s65 offen offset:2048 lds\n"
        "s_waitcnt vmcnt(0)\n"
        "ds_read_b64_tr_b16 a[216:217], v11 offset:0\n"
        "ds_read_b64_tr_b16 a[218:219], v11 offset:1536\n"
        "ds_read_b64_tr_b16 a[220:221], v11 offset:8\n"
        "ds_read_b64_tr_b16 a[222:223], v11 offset:1544\n"
        "ds_read_b64_tr_b16 a[224:225], v15 offset:0\n"
        "ds_read_b64_tr_b16 a[226:227], v15 offset:1536\n"
        "s_waitcnt lgkmcnt(6)\n"
        "s_add_u32 m0, s79, 3072\n"
        "s_mov_b32 s65, 829440\n"
        "buffer_load_dwordx4 v8, s[44:47], s65 offen lds\n"
        "buffer_load_dwordx4 v9, s[44:47], s65 offen offset:1024 lds\n"
        "buffer_load_dwordx4 v10, s[44:47], s65 offen offset:2048 lds\n"
        "s_waitcnt vmcnt(0)\n"
        "ds_read_b64_tr_b16 a[228:229], v11 offset:3072\n"
        "ds_read_b64_tr_b16 a[230:231], v11 offset:4608\n"
        "ds_read_b64_tr_b16 a[232:233], v11 offset:3080\n"
        "ds_read_b64_tr_b16 a[234:235], v11 offset:4616\n"
        "ds_read_b64_tr_b16 a[236:237], v15 offset:3072\n"
        "ds_read_b64_tr_b16 a[238:239], v15 offset:4608\n"
        "s_waitcnt lgkmcnt(6)\n"
        "s_add_u32 m0, s79, 0\n"
        "s_mov_b32 s65, 442368\n"
        "buffer_load_dwordx4 v12, s[44:47], s65 offen lds\n"
        "buffer_load_dwordx4 v13, s[44:47], s65 offen offset:1024 lds\n"
        "buffer_load_dwordx4 v14, s[44:47], s65 offen offset:2048 lds\n"
        "s_waitcnt vmcnt(0)\n"
        "ds_read_b64_tr_b16 a[240:241], v11 offset:0\n"
        "ds_read_b64_tr_b16 a[242:243], v11 offset:1536\n"
        "ds_read_b64_tr_b16 a[244:245], v11 offset:8\n"
        "ds_read_b64_tr_b16 a[246:247], v11 offset:1544\n"
        "ds_read_b64_tr_b16 a[248:249], v15 offset:0\n"
        "ds_read_b64_tr_b16 a[250:251], v15 offset:1536\n"
        "s_waitcnt lgkmcnt(6)\n"
        "s_add_u32 m0, s79, 3072\n"
        "s_mov_b32 s65, 552960\n"
        "buffer_load_dwordx4 v12, s[44:47], s65 offen lds\n"
        "buffer_load_dwordx4 v13, s[44:47], s65 offen offset:1024 lds\n"
        "buffer_load_dwordx4 v14, s[44:47], s65 offen offset:2048 lds\n"
        "s_waitcnt vmcnt(0)\n"
        "ds_read_b64_tr_b16 a[252:253], v11 offset:3072\n"
        "ds_read_b64_tr_b16 a[254:255], v11 offset:4608\n"
        "ds_read_b64_tr_b16 v[80:81], v11 offset:3080\n"
        "ds_read_b64_tr_b16 v[82:83], v11 offset:4616\n"
        "ds_read_b64_tr_b16 v[84:85], v15 offset:3072\n"
        "ds_read_b64_tr_b16 v[86:87], v15 offset:4608\n"
        "s_waitcnt lgkmcnt(6)\n"
        "s_add_u32 m0, s79, 0\n"
        "s_mov_b32 s65, 663552\n"
        "buffer_load_dwordx4 v12, s[44:47], s65 offen lds\n"
        "buffer_load_dwordx4 v13, s[44:47], s65 offen offset:1024 lds\n"
        "buffer_load_dwordx4 v14, s[44:47], s65 offen offset:2048 lds\n"
        "s_waitcnt vmcnt(0)\n"
        "ds_read_b64_tr_b16 v[88:89], v11 offset:0\n"
        "ds_read_b64_tr_b16 v[90:91], v11 offset:1536\n"
        "ds_read_b64_tr_b16 v[92:93], v11 offset:8\n"
        "ds_read_b64_tr_b16 v[94:95], v11 offset:1544\n"
        "ds_read_b64_tr_b16 v[96:97], v15 offset:0\n"
        "ds_read_b64_tr_b16 v[98:99], v15 offset:1536\n"
        "s_waitcnt lgkmcnt(6)\n"
        "s_add_u32 m0, s79, 3072\n"
        "s_mov_b32 s65, 774144\n"
        "buffer_load_dwordx4 v12, s[44:47], s65 offen lds\n"
        "buffer_load_dwordx4 v13, s[44:47], s65 offen offset:1024 lds\n"
        "buffer_load_dwordx4 v14, s[44:47], s65 offen offset:2048 lds\n"
        "s_waitcnt vmcnt(0)\n"
        "ds_read_b64_tr_b16 v[100:101], v11 offset:3072\n"
        "ds_read_b64_tr_b16 v[102:103], v11 offset:4608\n"
        "ds_read_b64_tr_b16 v[104:105], v11 offset:3080\n"
        "ds_read_b64_tr_b16 v[106:107], v11 offset:4616\n"
        "ds_read_b64_tr_b16 v[108:109], v15 offset:3072\n"
        "ds_read_b64_tr_b16 v[110:111], v15 offset:4608\n"
        "s_waitcnt lgkmcnt(6)\n"
        "s_add_u32 m0, s79, 0\n"
        "s_mov_b32 s65, 884736\n"
        "buffer_load_dwordx4 v8, s[44:47], s65 offen lds\n"
        "buffer_load_dwordx4 v9, s[44:47], s65 offen offset:1024 lds\n"
        "buffer_load_dwordx4 v10, s[44:47], s65 offen offset:2048 lds\n"
        "s_waitcnt vmcnt(0)\n"
        "ds_read_b64_tr_b16 v[112:113], v11 offset:0\n"
        "ds_read_b64_tr_b16 v[114:115], v11 offset:1536\n"
        "ds_read_b64_tr_b16 v[116:117], v11 offset:8\n"
        "ds_read_b64_tr_b16 v[118:119], v11 offset:1544\n"
        "ds_read_b64_tr_b16 v[120:121], v15 offset:0\n"
        "ds_read_b64_tr_b16 v[122:123], v15 offset:1536\n"
        "s_waitcnt lgkmcnt(6)\n"
        "s_add_u32 m0, s79, 3072\n"
        "s_mov_b32 s65, 940032\n"
        "buffer_load_dwordx4 v8, s[44:47], s65 offen lds\n"
        "buffer_load_dwordx4 v9, s[44:47], s65 offen offset:1024 lds\n"
        "buffer_load_dwordx4 v10, s[44:47], s65 offen offset:2048 lds\n"
        "s_waitcnt vmcnt(0)\n"
        "ds_read_b64_tr_b16 v[124:125], v11 offset:3072\n"
        "ds_read_b64_tr_b16 v[126:127], v11 offset:4608\n"
        "ds_read_b64_tr_b16 v[128:129], v11 offset:3080\n"
        "ds_read_b64_tr_b16 v[130:131], v11 offset:4616\n"
        "ds_read_b64_tr_b16 v[132:133], v15 offset:3072\n"
        "ds_read_b64_tr_b16 v[134:135], v15 offset:4608\n"
        "s_waitcnt lgkmcnt(6)\n"
        "s_add_u32 m0, s79, 0\n"
        "s_mov_b32 s65, 995328\n"
        "buffer_load_dwordx4 v8, s[44:47], s65 offen lds\n"
        "buffer_load_dwordx4 v9, s[44:47], s65 offen offset:1024 lds\n"
        "buffer_load_dwordx4 v10, s[44:47], s65 offen offset:2048 lds\n"
        "s_waitcnt vmcnt(0)\n"
        "ds_read_b64_tr_b16 v[136:137], v11 offset:0\n"
        "ds_read_b64_tr_b16 v[138:139], v11 offset:1536\n"
        "ds_read_b64_tr_b16 v[140:141], v11 offset:8\n"
        "ds_read_b64_tr_b16 v[142:143], v11 offset:1544\n"
        "ds_read_b64_tr_b16 v[144:145], v15 offset:0\n"
        "ds_read_b64_tr_b16 v[146:147], v15 offset:1536\n"
        "s_waitcnt lgkmcnt(6)\n"
        "s_add_u32 m0, s79, 3072\n"
        "s_mov_b32 s65, 1050624\n"
        "buffer_load_dwordx4 v8, s[44:47], s65 offen lds\n"
        "buffer_load_dwordx4 v9, s[44:47], s65 offen offset:1024 lds\n"
        "buffer_load_dwordx4 v10, s[44:47], s65 offen offset:2048 lds\n"
        "s_waitcnt vmcnt(0)\n"
        "ds_read_b64_tr_b16 v[148:149], v11 offset:3072\n"
        "ds_read_b64_tr_b16 v[150:151], v11 offset:4608\n"
        "ds_read_b64_tr_b16 v[152:153], v11 offset:3080\n"
        "ds_read_b64_tr_b16 v[154:155], v11 offset:4616\n"
        "ds_read_b64_tr_b16 v[156:157], v15 offset:3072\n"
        "ds_read_b64_tr_b16 v[158:159], v15 offset:4608\n"
        "s_waitcnt lgkmcnt(6)\n"
        "s_add_u32 m0, s79, 0\n"
        "s_mov_b32 s65, 1105920\n"
        "buffer_load_dwordx4 v8, s[44:47], s65 offen lds\n"
        "buffer_load_dwordx4 v9, s[44:47], s65 offen offset:1024 lds\n"
        "buffer_load_dwordx4 v10, s[44:47], s65 offen offset:2048 lds\n"
        "s_waitcnt vmcnt(0)\n"
        "ds_read_b64_tr_b16 v[160:161], v11 offset:0\n"
        "ds_read_b64_tr_b16 v[162:163], v11 offset:1536\n"
        "ds_read_b64_tr_b16 v[164:165], v11 offset:8\n"
        "ds_read_b64_tr_b16 v[166:167], v11 offset:1544\n"
        "ds_read_b64_tr_b16 v[168:169], v15 offset:0\n"
        "ds_read_b64_tr_b16 v[170:171], v15 offset:1536\n"
        "s_waitcnt lgkmcnt(6)\n"
        "s_add_u32 m0, s79, 3072\n"
        "s_mov_b32 s65, 1161216\n"
        "buffer_load_dwordx4 v8, s[44:47], s65 offen lds\n"
        "buffer_load_dwordx4 v9, s[44:47], s65 offen offset:1024 lds\n"
        "buffer_load_dwordx4 v10, s[44:47], s65 offen offset:2048 lds\n"
        "s_waitcnt vmcnt(0)\n"
        "ds_read_b64_tr_b16 v[172:173], v11 offset:3072\n"
        "ds_read_b64_tr_b16 v[174:175], v11 offset:4608\n"
        "ds_read_b64_tr_b16 v[176:177], v11 offset:3080\n"
        "ds_read_b64_tr_b16 v[178:179], v11 offset:4616\n"
        "ds_read_b64_tr_b16 v[180:181], v15 offset:3072\n"
        "ds_read_b64_tr_b16 v[182:183], v15 offset:4608\n"
        "s_waitcnt lgkmcnt(6)\n"
        "s_add_u32 m0, s79, 0\n"
        "s_mov_b32 s65, 1216512\n"
        "buffer_load_dwordx4 v8, s[44:47], s65 offen lds\n"
        "buffer_load_dwordx4 v9, s[44:47], s65 offen offset:1024 lds\n"
        "buffer_load_dwordx4 v10, s[44:47], s65 offen offset:2048 lds\n"
        "s_waitcnt vmcnt(0)\n"
        "ds_read_b64_tr_b16 v[184:185], v11 offset:0\n"
        "ds_read_b64_tr_b16 v[186:187], v11 offset:1536\n"
        "ds_read_b64_tr_b16 v[188:189], v11 offset:8\n"
        "ds_read_b64_tr_b16 v[190:191], v11 offset:1544\n"
        "ds_read_b64_tr_b16 v[192:193], v15 offset:0\n"
        "ds_read_b64_tr_b16 v[194:195], v15 offset:1536\n"
        "s_waitcnt lgkmcnt(6)\n"
        "s_add_u32 m0, s79, 3072\n"
        "s_mov_b32 s65, 1271808\n"
        "buffer_load_dwordx4 v8, s[44:47], s65 offen lds\n"
        "buffer_load_dwordx4 v9, s[44:47], s65 offen offset:1024 lds\n"
        "buffer_load_dwordx4 v10, s[44:47], s65 offen offset:2048 lds\n"
        "s_waitcnt vmcnt(0)\n"
        "ds_read_b64_tr_b16 v[196:197], v11 offset:3072\n"
        "ds_read_b64_tr_b16 v[198:199], v11 offset:4608\n"
        "ds_read_b64_tr_b16 v[200:201], v11 offset:3080\n"
        "ds_read_b64_tr_b16 v[202:203], v11 offset:4616\n"
        "ds_read_b64_tr_b16 v[204:205], v15 offset:3072\n"
        "ds_read_b64_tr_b16 v[206:207], v15 offset:4608\n"
        "s_waitcnt lgkmcnt(6)\n"
        "s_add_u32 m0, s79, 0\n"
        "s_mov_b32 s65, 884736\n"
        "buffer_load_dwordx4 v12, s[44:47], s65 offen lds\n"
        "buffer_load_dwordx4 v13, s[44:47], s65 offen offset:1024 lds\n"
        "buffer_load_dwordx4 v14, s[44:47], s65 offen offset:2048 lds\n"
        "s_waitcnt vmcnt(0)\n"
        "ds_read_b64_tr_b16 v[208:209], v11 offset:0\n"
        "ds_read_b64_tr_b16 v[210:211], v11 offset:1536\n"
        "ds_read_b64_tr_b16 v[212:213], v11 offset:8\n"
        "ds_read_b64_tr_b16 v[214:215], v11 offset:1544\n"
        "ds_read_b64_tr_b16 v[216:217], v15 offset:0\n"
        "ds_read_b64_tr_b16 v[218:219], v15 offset:1536\n"
        "s_waitcnt lgkmcnt(6)\n"
        "s_add_u32 m0, s79, 3072\n"
        "s_mov_b32 s65, 995328\n"
        "buffer_load_dwordx4 v12, s[44:47], s65 offen lds\n"
        "buffer_load_dwordx4 v13, s[44:47], s65 offen offset:1024 lds\n"
        "buffer_load_dwordx4 v14, s[44:47], s65 offen offset:2048 lds\n"
        "s_waitcnt vmcnt(0)\n"
        "ds_read_b64_tr_b16 v[220:221], v11 offset:3072\n"
        "ds_read_b64_tr_b16 v[222:223], v11 offset:4608\n"
        "ds_read_b64_tr_b16 v[224:225], v11 offset:3080\n"
        "ds_read_b64_tr_b16 v[226:227], v11 offset:4616\n"
        "ds_read_b64_tr_b16 v[228:229], v15 offset:3072\n"
        "ds_read_b64_tr_b16 v[230:231], v15 offset:4608\n"
        "s_waitcnt lgkmcnt(6)\n"
        "s_add_u32 m0, s79, 0\n"
        "s_mov_b32 s65, 1105920\n"
        "buffer_load_dwordx4 v12, s[44:47], s65 offen lds\n"
        "buffer_load_dwordx4 v13, s[44:47], s65 offen offset:1024 lds\n"
        "buffer_load_dwordx4 v14, s[44:47], s65 offen offset:2048 lds\n"
        "s_waitcnt vmcnt(0)\n"
        "ds_read_b64_tr_b16 v[232:233], v11 offset:0\n"
        "ds_read_b64_tr_b16 v[234:235], v11 offset:1536\n"
        "ds_read_b64_tr_b16 v[236:237], v11 offset:8\n"
        "ds_read_b64_tr_b16 v[238:239], v11 offset:1544\n"
        "ds_read_b64_tr_b16 v[240:241], v15 offset:0\n"
        "ds_read_b64_tr_b16 v[242:243], v15 offset:1536\n"
        "s_waitcnt lgkmcnt(6)\n"
        "s_add_u32 m0, s79, 3072\n"
        "s_mov_b32 s65, 1216512\n"
        "buffer_load_dwordx4 v12, s[44:47], s65 offen lds\n"
        "buffer_load_dwordx4 v13, s[44:47], s65 offen offset:1024 lds\n"
        "buffer_load_dwordx4 v14, s[44:47], s65 offen offset:2048 lds\n"
        "s_waitcnt vmcnt(0)\n"
        "ds_read_b64_tr_b16 v[244:245], v11 offset:3072\n"
        "ds_read_b64_tr_b16 v[246:247], v11 offset:4608\n"
        "ds_read_b64_tr_b16 v[248:249], v11 offset:3080\n"
        "ds_read_b64_tr_b16 v[250:251], v11 offset:4616\n"
        "ds_read_b64_tr_b16 v[252:253], v15 offset:3072\n"
        "ds_read_b64_tr_b16 v[254:255], v15 offset:4608\n"
        "s_waitcnt lgkmcnt(0)\n"
        "s_getpc_b64 s[44:45]\n"
        ".Lf2_pc_%=:\n"
        "s_add_u32 s44, s44, .Lf2_code_%=-.Lf2_pc_%=\n"
        "s_addc_u32 s45, s45, 0\n"
        "s_and_b32 s45, s45, 0xffff\n"
        "s_and_b32 s44, s44, -4\n"
        "s_mov_b32 s46, .Lf2_end_%=-.Lf2_code_%=\n"
        "s_mov_b32 s47, 0x20000\n"
        "s_lshl_b32 s65, s48, 10\n"
        "buffer_load_dwordx4 v[24:27], v1, s[44:47], s65 offen\n"
        "s_add_u32 s65, s65, 4096\n"
        "buffer_load_dwordx4 v[28:31], v1, s[44:47], s65 offen\n"
        "s_add_u32 s65, s65, 4096\n"
        "buffer_load_dwordx4 v[32:35], v1, s[44:47], s65 offen\n"
        "s_add_u32 s65, s65, 4096\n"
        "buffer_load_dwordx4 v[36:39], v1, s[44:47], s65 offen\n"
        "s_add_u32 s65, s65, 4096\n"
        "buffer_load_dwordx4 v[40:43], v1, s[44:47], s65 offen\n"
        "s_add_u32 s65, s65, 4096\n"
        "v_lshrrev_b32 v20, 4, v1\n"
        "s_mov_b32 s50, s56\n"
        "v_lshlrev_b32 v22, 7, v20\n"
        "s_sub_u32 s66, 0, s50\n"
        "s_mov_b32 s52, 32768\n"
        "s_mov_b32 s64, 0\n"
        ".Lf2_pg0_top_%=:\n"
        "buffer_load_dword v21, v22, s[40:43], s52 offen sc1\n"
        "s_waitcnt vmcnt(0)\n"
        "v_cmp_gt_i32 vcc, 0, v21\n"
        "s_cbranch_vccz .Lf2_pg0_ok_%=\n"
        "s_add_u32 s64, s64, 1\n"
        "s_and_b32 s65, s64, 255\n"
        "s_cbranch_scc1 .Lf2_pg0_go_%=\n"
        "s_mov_b32 s65, 49152\n"
        "s_cmp_lt_u32 s64, 1048576\n"
        "s_cbranch_scc1 .Lf2_pg0_chk_%=\n"
        "v_mov_b32 v21, 1\n"
        "buffer_store_dword v21, off, s[40:43], s65 sc1\n"
        "s_waitcnt vmcnt(0)\n"
        ".Lf2_pg0_chk_%=:\n"
        "buffer_load_dword v21, off, s[40:43], s65 sc1\n"
        "s_waitcnt vmcnt(0)\n"
        "v_readfirstlane_b32 s65, v21\n"
        "s_nop 0\n"
        "s_cmp_lg_u32 s65, 1\n"
        "s_cbranch_scc1 .Lf2_pg0_go_%=\n"
        "s_endpgm\n"
        ".Lf2_pg0_go_%=:\n"
        "s_sleep 2\n"
        "s_branch .Lf2_pg0_top_%=\n"
        ".Lf2_pg0_ok_%=:\n"
        "v_cmp_eq_u32 s[52:53], 1, v21\n"
        "s_nop 1\n"
        "s_mov_b32 s54, 40960\n"
        "s_mov_b32 s64, 0\n"
        ".Lf2_pg1_top_%=:\n"
        "buffer_load_dword v21, v22, s[40:43], s54 offen sc1\n"
        "s_waitcnt vmcnt(0)\n"
        "v_cmp_gt_i32 vcc, 0, v21\n"
        "s_cbranch_vccz .Lf2_pg1_ok_%=\n"
        "s_add_u32 s64, s64, 1\n"
        "s_and_b32 s65, s64, 255\n"
        "s_cbranch_scc1 .Lf2_pg1_go_%=\n"
        "s_mov_b32 s65, 49152\n"
        "s_cmp_lt_u32 s64, 1048576\n"
        "s_cbranch_scc1 .Lf2_pg1_chk_%=\n"
        "v_mov_b32 v21, 1\n"
        "buffer_store_dword v21, off, s[40:43], s65 sc1\n"
        "s_waitcnt vmcnt(0)\n"
        ".Lf2_pg1_chk_%=:\n"
        "buffer_load_dword v21, off, s[40:43], s65 sc1\n"
        "s_waitcnt vmcnt(0)\n"
        "v_readfirstlane_b32 s65, v21\n"
        "s_nop 0\n"
        "s_cmp_lg_u32 s65, 1\n"
        "s_cbranch_scc1 .Lf2_pg1_go_%=\n"
        "s_endpgm\n"
        ".Lf2_pg1_go_%=:\n"
        "s_sleep 2\n"
        "s_branch .Lf2_pg1_top_%=\n"
        ".Lf2_pg1_ok_%=:\n"
        "v_cmp_eq_u32 s[54:55], 1, v21\n"
        "s_nop 1\n"
        "s_mov_b32 vcc_lo, 0x55555555\n"
        "s_mov_b32 vcc_hi, 0x55555555\n"
        "s_xor_b64 vcc, vcc, s[52:53]\n"
        "s_bcnt1_i32_b64 s65, vcc\n"
        "s_sub_u32 s64, s65, 23\n"
        "s_cmp_lt_u32 s64, 19\n"
        "s_cbranch_scc1 .Lf2_rawmap_%=\n"
        "s_cmp_lt_u32 s65, 32\n"
        "s_cselect_b32 s65, 0, -1\n"
        "s_xor_b32 s52, s65, 0x55555555\n"
        "s_xor_b32 s53, s65, 0x55555555\n"
        "s_xor_b32 s54, s65, 0x55555555\n"
        "s_xor_b32 s55, s65, 0xaaaaaaaa\n"
        ".Lf2_rawmap_%=:\n"
        "s_xor_b32 s52, s52, s66\n"
        "s_xor_b32 s53, s53, s66\n"
        "s_xor_b32 s54, s54, s66\n"
        "s_xor_b32 s55, s55, s66\n"
        "s_and_b32 s76, s55, 1\n"
        "s_lshl_b32 s76, s76, 13\n"
        "s_add_u32 s60, s60, s76\n"
        "s_barrier\n"
        "v_mov_b32 v48, v6\n"
        "s_mov_b32 s68, -1\n"
        "s_mov_b32 s69, 0\n"
        "s_mov_b32 s78, 0\n"
        "s_mov_b32 s74, -1\n"
        "s_mov_b32 s86, s76\n"
        "s_lshr_b32 s85, s61, 16\n"
        "s_and_b32 s61, s61, 0xffff\n"
        "s_mov_b32 s89, 241172480\n"
        "s_mov_b32 s84, 241172480\n"
        "v_mov_b32 v6, v0\n"
        "s_mov_b32 s80, 0xbfb8aa3b\n"
        "s_lshr_b32 s67, s69, 1\n"
        "s_lshr_b32 s65, s67, 1\n"
        "s_bitcmp1_b32 s54, s65\n"
        "s_cselect_b32 s65, 8192, 0\n"
        "s_lshl_b32 s77, s67, 20\n"
        "s_add_u32 s77, s77, s65\n"
        "s_add_u32 s71, s77, s58\n"
        "s_add_u32 s81, s77, s62\n"
        "s_add_u32 s78, s78, 36864\n"
        "s_cmp_eq_u32 s78, 110592\n"
        "s_cselect_b32 s78, 0, s78\n"
        "s_add_u32 m0, s78, s61\n"
        "s_lshr_b32 s70, s69, 0\n"
        "s_branch .Lf2_wait1_%=\n"
        ".Lf2_code_%=:\n"
        ".Lf2_i0_%=:\n"
        "v_mfma_f32_16x16x32_bf16 v[24:27], a[0:3], v[8:11], 0\n"
        "v_mfma_f32_16x16x32_bf16 v[28:31], a[4:7], v[8:11], 0\n"
        "s_add_u32 s71, s77, s58\n"
        "buffer_load_dword v4, v2, s[40:43], s86 offen sc1\n"
        "v_mfma_f32_16x16x32_bf16 v[32:35], a[8:11], v[8:11], 0\n"
        "s_add_u32 s71, s71, 49152\n"
        "ds_read_b128 v[8:11], v0 offset:4096\n"
        "s_add_u32 s81, s77, s62\n"
        "v_mfma_f32_16x16x32_bf16 v[24:27], a[12:15], v[12:15], v[24:27]\n"
        "s_add_u32 s81, s81, 4096\n"
        "s_add_u32 s78, s78, 36864\n"
        "v_cvt_pk_bf16_f32 v36, v36, v37\n"
        "v_mfma_f32_16x16x32_bf16 v[28:31], a[16:19], v[12:15], v[28:31]\n"
        "s_cmp_eq_u32 s78, 110592\n"
        "s_cselect_b32 s78, 0, s78\n"
        "s_add_u32 m0, s78, s61\n"
        "v_mfma_f32_16x16x32_bf16 v[32:35], a[20:23], v[12:15], v[32:35]\n"
        "s_waitcnt lgkmcnt(1)\n"
        "ds_read_b128 v[12:15], v0 offset:5120\n"
        "s_lshr_b32 s70, s69, 0\n"
        "v_mfma_f32_16x16x32_bf16 v[24:27], a[24:27], v[16:19], v[24:27]\n"
        "v_cvt_pk_bf16_f32 v37, v38, v39\n"
        "v_cvt_pk_bf16_f32 v38, v40, v41\n"
        "v_mfma_f32_16x16x32_bf16 v[28:31], a[28:31], v[16:19], v[28:31]\n"
        "v_cvt_pk_bf16_f32 v39, v42, v43\n"
        "v_mfma_f32_16x16x32_bf16 v[32:35], a[32:35], v[16:19], v[32:35]\n"
        "ds_read_b128 v[16:19], v0 offset:6144\n"
        "v_mfma_f32_16x16x32_bf16 v[24:27], a[36:39], v[20:23], v[24:27]\n"
        "buffer_store_dwordx4 v[36:39], v48, s[40:43], s72 offen offset:0 sc1\n"
        "v_mfma_f32_16x16x32_bf16 v[28:31], a[40:43], v[20:23], v[28:31]\n"
        "v_cvt_pk_bf16_f32 v44, v44, v45\n"
        "v_cvt_pk_bf16_f32 v45, v46, v47\n"
        "v_mfma_f32_16x16x32_bf16 v[32:35], a[44:47], v[20:23], v[32:35]\n"
        "s_waitcnt lgkmcnt(1)\n"
        "ds_read_b128 v[20:23], v0 offset:7168\n"
        "s_nop 0\n"
        "v_mfma_f32_16x16x32_bf16 v[24:27], a[48:51], v[8:11], v[24:27]\n"
        "v_permlane16_swap_b32 v44, v46\n"
        "v_mfma_f32_16x16x32_bf16 v[28:31], a[52:55], v[8:11], v[28:31]\n"
        "v_permlane16_swap_b32 v45, v47\n"
        "v_mfma_f32_16x16x32_bf16 v[32:35], a[56:59], v[8:11], v[32:35]\n"
        "ds_read_b128 v[8:11], v0 offset:8192\n"
        "v_mfma_f32_16x16x32_bf16 v[24:27], a[60:63], v[12:15], v[24:27]\n"
        "v_mfma_f32_16x16x32_bf16 v[28:31], a[64:67], v[12:15], v[28:31]\n"
        "buffer_store_dwordx4 v[44:47], v7, s[40:43], s72 offen offset:0 sc1\n"
        "v_mfma_f32_16x16x32_bf16 v[32:35], a[68:71], v[12:15], v[32:35]\n"
        "s_waitcnt lgkmcnt(1)\n"
        "ds_read_b128 v[12:15], v0 offset:9216\n"
        "v_mfma_f32_16x16x32_bf16 v[24:27], a[72:75], v[16:19], v[24:27]\n"
        "v_mfma_f32_16x16x32_bf16 v[28:31], a[76:79], v[16:19], v[28:31]\n"
        "v_mfma_f32_16x16x32_bf16 v[32:35], a[80:83], v[16:19], v[32:35]\n"
        "ds_read_b128 v[16:19], v0 offset:10240\n"
        "v_mfma_f32_16x16x32_bf16 v[24:27], a[84:87], v[20:23], v[24:27]\n"
        "v_mfma_f32_16x16x32_bf16 v[28:31], a[88:91], v[20:23], v[28:31]\n"
        "v_mfma_f32_16x16x32_bf16 v[32:35], a[92:95], v[20:23], v[32:35]\n"
        "s_waitcnt lgkmcnt(1)\n"
        "ds_read_b128 v[20:23], v0 offset:11264\n"
        "v_mfma_f32_16x16x32_bf16 v[24:27], a[96:99], v[8:11], v[24:27]\n"
        "v_mfma_f32_16x16x32_bf16 v[28:31], a[100:103], v[8:11], v[28:31]\n"
        "s_waitcnt vmcnt(3)\n"
        "s_barrier\n"
        "v_mfma_f32_16x16x32_bf16 v[32:35], a[104:107], v[8:11], v[32:35]\n"
        "ds_read_b128 v[8:11], v0 offset:12288\n"
        "v_mfma_f32_16x16x32_bf16 v[24:27], a[108:111], v[12:15], v[24:27]\n"
        "buffer_store_dword v5, v3, s[40:43], s84 offen sc1\n"
        "v_mfma_f32_16x16x32_bf16 v[28:31], a[112:115], v[12:15], v[28:31]\n"
        "v_mov_b32 v5, s74\n"
        "v_mfma_f32_16x16x32_bf16 v[32:35], a[116:119], v[12:15], v[32:35]\n"
        "s_waitcnt lgkmcnt(1)\n"
        "ds_read_b128 v[12:15], v0 offset:13312\n"
        "v_mfma_f32_16x16x32_bf16 v[24:27], a[120:123], v[16:19], v[24:27]\n"
        "v_mfma_f32_16x16x32_bf16 v[28:31], a[124:127], v[16:19], v[28:31]\n"
        "s_waitcnt vmcnt(3)\n"
        "v_cmp_gt_i32 vcc, s70, v4\n"
        "v_mfma_f32_16x16x32_bf16 v[32:35], a[128:131], v[16:19], v[32:35]\n"
        "ds_read_b128 v[16:19], v0 offset:14336\n"
        "v_mfma_f32_16x16x32_bf16 v[24:27], a[132:135], v[20:23], v[24:27]\n"
        "s_and_b64 vcc, vcc, 63\n"
        "v_mfma_f32_16x16x32_bf16 v[28:31], a[136:139], v[20:23], v[28:31]\n"
        "s_cbranch_vccnz .Lf2_i00_%=\n"
        "v_mfma_f32_16x16x32_bf16 v[32:35], a[140:143], v[20:23], v[32:35]\n"
        "s_waitcnt lgkmcnt(1)\n"
        "ds_read_b128 v[20:23], v0 offset:15360\n"
        "v_mfma_f32_16x16x32_bf16 v[24:27], a[144:147], v[8:11], v[24:27]\n"
        "buffer_load_dwordx4 v1, s[40:43], s71 offen offset:0 sc1 lds\n"
        "v_mfma_f32_16x16x32_bf16 v[28:31], a[148:151], v[8:11], v[28:31]\n"
        "v_mfma_f32_16x16x32_bf16 v[32:35], a[152:155], v[8:11], v[32:35]\n"
        "ds_read_b128 v[8:11], v0 offset:16384\n"
        "v_mfma_f32_16x16x32_bf16 v[24:27], a[156:159], v[12:15], v[24:27]\n"
        "buffer_load_dwordx4 v1, s[40:43], s71 offen offset:1024 sc1 lds\n"
        "v_mfma_f32_16x16x32_bf16 v[28:31], a[160:163], v[12:15], v[28:31]\n"
        "s_add_u32 m0, m0, s85\n"
        "s_add_u32 s71, s71, 16384\n"
        "v_mfma_f32_16x16x32_bf16 v[32:35], a[164:167], v[12:15], v[32:35]\n"
        "s_waitcnt lgkmcnt(1)\n"
        "ds_read_b128 v[12:15], v0 offset:17408\n"
        "v_mfma_f32_16x16x32_bf16 v[24:27], a[168:171], v[16:19], v[24:27]\n"
        "s_getreg_b32 s88, hwreg(HW_REG_IB_STS, 0, 4)\n"
        "v_mfma_f32_16x16x32_bf16 v[28:31], a[172:175], v[16:19], v[28:31]\n"
        "buffer_load_dwordx4 v1, s[40:43], s81 offen offset:0 sc1 lds\n"
        "v_mfma_f32_16x16x32_bf16 v[32:35], a[176:179], v[16:19], v[32:35]\n"
        "s_sub_u32 m0, m0, s85\n"
        "ds_read_b128 v[16:19], v0 offset:18432\n"
        "s_add_u32 m0, m0, 12288\n"
        "v_mfma_f32_16x16x32_bf16 v[24:27], a[180:183], v[20:23], v[24:27]\n"
        "s_add_u32 s81, s81, 16384\n"
        "v_mfma_f32_16x16x32_bf16 v[28:31], a[184:187], v[20:23], v[28:31]\n"
        "s_cmp_le_u32 s88, 3\n"
        "s_cselect_b32 s88, s73, s89\n"
        "s_cselect_b32 s73, s89, s73\n"
        "v_mfma_f32_16x16x32_bf16 v[32:35], a[188:191], v[20:23], v[32:35]\n"
        "s_waitcnt lgkmcnt(1)\n"
        "ds_read_b128 v[20:23], v0 offset:19456\n"
        "v_mfma_f32_16x16x32_bf16 v[24:27], a[192:195], v[8:11], v[24:27]\n"
        "buffer_load_dwordx4 v1, s[40:43], s71 offen offset:0 sc1 lds\n"
        "v_mfma_f32_16x16x32_bf16 v[28:31], a[196:199], v[8:11], v[28:31]\n"
        "buffer_store_dword v5, v3, s[40:43], s88 offen sc1\n"
        "v_mfma_f32_16x16x32_bf16 v[32:35], a[200:203], v[8:11], v[32:35]\n"
        "s_getreg_b32 s88, hwreg(HW_REG_IB_STS, 0, 4)\n"
        "ds_read_b128 v[8:11], v0 offset:20480\n"
        "v_mfma_f32_16x16x32_bf16 v[24:27], a[204:207], v[12:15], v[24:27]\n"
        "buffer_load_dwordx4 v1, s[40:43], s71 offen offset:1024 sc1 lds\n"
        "v_mfma_f32_16x16x32_bf16 v[28:31], a[208:211], v[12:15], v[28:31]\n"
        "s_add_u32 m0, m0, s85\n"
        "s_add_u32 s71, s71, 16384\n"
        "v_mfma_f32_16x16x32_bf16 v[32:35], a[212:215], v[12:15], v[32:35]\n"
        "s_waitcnt lgkmcnt(1)\n"
        "ds_read_b128 v[12:15], v0 offset:21504\n"
        "v_mfma_f32_16x16x32_bf16 v[24:27], a[216:219], v[16:19], v[24:27]\n"
        "s_cmp_le_u32 s88, 6\n"
        "s_cselect_b32 s88, s73, s89\n"
        "s_cselect_b32 s73, s89, s73\n"
        "v_mfma_f32_16x16x32_bf16 v[28:31], a[220:223], v[16:19], v[28:31]\n"
        "buffer_load_dwordx4 v1, s[40:43], s81 offen offset:0 sc1 lds\n"
        "v_mfma_f32_16x16x32_bf16 v[32:35], a[224:227], v[16:19], v[32:35]\n"
        "s_sub_u32 m0, m0, s85\n"
        "ds_read_b128 v[16:19], v0 offset:22528\n"
        "s_add_u32 m0, m0, 12288\n"
        "v_mfma_f32_16x16x32_bf16 v[24:27], a[228:231], v[20:23], v[24:27]\n"
        "s_add_u32 s81, s81, 16384\n"
        "buffer_store_dword v5, v3, s[40:43], s88 offen sc1\n"
        "v_mfma_f32_16x16x32_bf16 v[28:31], a[232:235], v[20:23], v[28:31]\n"
        "s_getreg_b32 s88, hwreg(HW_REG_IB_STS, 0, 4)\n"
        "v_mfma_f32_16x16x32_bf16 v[32:35], a[236:239], v[20:23], v[32:35]\n"
        "s_waitcnt lgkmcnt(1)\n"
        "ds_read_b128 v[20:23], v0 offset:23552\n"
        "v_mfma_f32_16x16x32_bf16 v[24:27], a[240:243], v[8:11], v[24:27]\n"
        "buffer_load_dwordx4 v1, s[40:43], s71 offen offset:0 sc1 lds\n"
        "v_mfma_f32_16x16x32_bf16 v[28:31], a[244:247], v[8:11], v[28:31]\n"
        "s_cmp_le_u32 s88, 9\n"
        "s_cselect_b32 s88, s73, s89\n"
        "s_cselect_b32 s73, s89, s73\n"
        "v_mfma_f32_16x16x32_bf16 v[32:35], a[248:251], v[8:11], v[32:35]\n"
        "ds_read_b128 v[8:11], v0 offset:24576\n"
        "v_mfma_f32_16x16x32_bf16 v[24:27], a[252:255], v[12:15], v[24:27]\n"
        "buffer_load_dwordx4 v1, s[40:43], s71 offen offset:1024 sc1 lds\n"
        "v_mfma_f32_16x16x32_bf16 v[28:31], v[80:83], v[12:15], v[28:31]\n"
        "s_add_u32 m0, m0, s85\n"
        "s_add_u32 s71, s71, 16384\n"
        "v_mfma_f32_16x16x32_bf16 v[32:35], v[84:87], v[12:15], v[32:35]\n"
        "s_waitcnt lgkmcnt(1)\n"
        "ds_read_b128 v[12:15], v0 offset:25600\n"
        "v_mfma_f32_16x16x32_bf16 v[24:27], v[88:91], v[16:19], v[24:27]\n"
        "buffer_store_dword v5, v3, s[40:43], s88 offen sc1\n"
        "v_mfma_f32_16x16x32_bf16 v[28:31], v[92:95], v[16:19], v[28:31]\n"
        "buffer_load_dwordx4 v1, s[40:43], s81 offen offset:0 sc1 lds\n"
        "v_mfma_f32_16x16x32_bf16 v[32:35], v[96:99], v[16:19], v[32:35]\n"
        "s_add_u32 s69, s69, 1\n"
        "ds_read_b128 v[16:19], v0 offset:26624\n"
        "v_mfma_f32_16x16x32_bf16 v[24:27], v[100:103], v[20:23], v[24:27]\n"
        "v_mfma_f32_16x16x32_bf16 v[28:31], v[104:107], v[20:23], v[28:31]\n"
        "s_waitcnt vmcnt(13)\n"
        "buffer_store_dword v5, v3, s[40:43], s73 offen sc1\n"
        "v_mfma_f32_16x16x32_bf16 v[32:35], v[108:111], v[20:23], v[32:35]\n"
        "s_waitcnt lgkmcnt(1)\n"
        "ds_read_b128 v[20:23], v0 offset:27648\n"
        "s_mov_b32 s84, s89\n"
        "v_mfma_f32_16x16x32_bf16 v[24:27], v[112:115], v[8:11], v[24:27]\n"
        "s_mov_b32 s73, s60\n"
        "s_add_u32 s74, s74, 1\n"
        "s_add_u32 s68, s68, 1\n"
        "v_mfma_f32_16x16x32_bf16 v[28:31], v[116:119], v[8:11], v[28:31]\n"
        "s_lshr_b32 s66, s68, 2\n"
        "s_add_u32 s66, s66, 1\n"
        "v_mfma_f32_16x16x32_bf16 v[32:35], v[120:123], v[8:11], v[32:35]\n"
        "s_bitcmp1_b32 s55, s66\n"
        "s_cselect_b32 s66, 8192, 0\n"
        "ds_read_b128 v[8:11], v0 offset:28672\n"
        "v_mfma_f32_16x16x32_bf16 v[24:27], v[124:127], v[12:15], v[24:27]\n"
        "s_lshl_b32 s75, s68, 19\n"
        "s_add_u32 s75, s75, s66\n"
        "s_add_u32 s72, s75, s59\n"
        "v_mfma_f32_16x16x32_bf16 v[28:31], v[128:131], v[12:15], v[28:31]\n"
        "v_mfma_f32_16x16x32_bf16 v[32:35], v[132:135], v[12:15], v[32:35]\n"
        "s_waitcnt lgkmcnt(1)\n"
        "ds_read_b128 v[12:15], v0 offset:29696\n"
        "v_mfma_f32_16x16x32_bf16 v[24:27], v[136:139], v[16:19], v[24:27]\n"
        "v_mfma_f32_16x16x32_bf16 v[28:31], v[140:143], v[16:19], v[28:31]\n"
        "v_mfma_f32_16x16x32_bf16 v[32:35], v[144:147], v[16:19], v[32:35]\n"
        "ds_read_b128 v[16:19], v0 offset:30720\n"
        "v_mfma_f32_16x16x32_bf16 v[24:27], v[148:151], v[20:23], v[24:27]\n"
        "v_mfma_f32_16x16x32_bf16 v[28:31], v[152:155], v[20:23], v[28:31]\n"
        "v_mfma_f32_16x16x32_bf16 v[32:35], v[156:159], v[20:23], v[32:35]\n"
        "s_waitcnt lgkmcnt(1)\n"
        "ds_read_b128 v[20:23], v0 offset:31744\n"
        "v_mfma_f32_16x16x32_bf16 v[24:27], v[160:163], v[8:11], v[24:27]\n"
        "v_mfma_f32_16x16x32_bf16 v[28:31], v[164:167], v[8:11], v[28:31]\n"
        "v_mfma_f32_16x16x32_bf16 v[32:35], v[168:171], v[8:11], v[32:35]\n"
        "ds_read_b128 v[8:11], v0 offset:32768\n"
        "v_mfma_f32_16x16x32_bf16 v[24:27], v[172:175], v[12:15], v[24:27]\n"
        "v_mfma_f32_16x16x32_bf16 v[28:31], v[176:179], v[12:15], v[28:31]\n"
        "v_mfma_f32_16x16x32_bf16 v[32:35], v[180:183], v[12:15], v[32:35]\n"
        "s_waitcnt lgkmcnt(1)\n"
        "ds_read_b128 v[12:15], v0 offset:33792\n"
        "v_mfma_f32_16x16x32_bf16 v[24:27], v[184:187], v[16:19], v[24:27]\n"
        "v_mfma_f32_16x16x32_bf16 v[28:31], v[188:191], v[16:19], v[28:31]\n"
        "v_mfma_f32_16x16x32_bf16 v[32:35], v[192:195], v[16:19], v[32:35]\n"
        "ds_read_b128 v[16:19], v0 offset:34816\n"
        "v_mfma_f32_16x16x32_bf16 v[24:27], v[196:199], v[20:23], v[24:27]\n"
        "v_mfma_f32_16x16x32_bf16 v[28:31], v[200:203], v[20:23], v[28:31]\n"
        "v_mfma_f32_16x16x32_bf16 v[32:35], v[204:207], v[20:23], v[32:35]\n"
        "s_waitcnt lgkmcnt(1)\n"
        "ds_read_b128 v[20:23], v0 offset:35840\n"
        "v_mfma_f32_16x16x32_bf16 v[24:27], v[208:211], v[8:11], v[24:27]\n"
        "v_mfma_f32_16x16x32_bf16 v[28:31], v[212:215], v[8:11], v[28:31]\n"
        "s_waitcnt vmcnt(10)\n"
        "s_barrier\n"
        "v_add_u32 v0, s78, v6\n"
        "v_mfma_f32_16x16x32_bf16 v[32:35], v[216:219], v[8:11], v[32:35]\n"
        "ds_read_b128 v[8:11], v0 offset:0\n"
        "v_mfma_f32_16x16x32_bf16 v[24:27], v[220:223], v[12:15], v[24:27]\n"
        "v_mfma_f32_16x16x32_bf16 v[28:31], v[224:227], v[12:15], v[28:31]\n"
        "v_mfma_f32_16x16x32_bf16 v[32:35], v[228:231], v[12:15], v[32:35]\n"
        "s_waitcnt lgkmcnt(1)\n"
        "ds_read_b128 v[12:15], v0 offset:1024\n"
        "v_mfma_f32_16x16x32_bf16 v[24:27], v[232:235], v[16:19], v[24:27]\n"
        "v_mfma_f32_16x16x32_bf16 v[28:31], v[236:239], v[16:19], v[28:31]\n"
        "v_mfma_f32_16x16x32_bf16 v[32:35], v[240:243], v[16:19], v[32:35]\n"
        "ds_read_b128 v[16:19], v0 offset:2048\n"
        "v_mfma_f32_16x16x32_bf16 v[24:27], v[244:247], v[20:23], v[24:27]\n"
        "v_mfma_f32_16x16x32_bf16 v[28:31], v[248:251], v[20:23], v[28:31]\n"
        "v_mfma_f32_16x16x32_bf16 v[32:35], v[252:255], v[20:23], v[32:35]\n"
        "s_waitcnt lgkmcnt(1)\n"
        "ds_read_b128 v[20:23], v0 offset:3072\n"
        "s_branch .Lf2_i1_%=\n"
        ".Lf2_i00_%=:\n"
        "v_mfma_f32_16x16x32_bf16 v[32:35], a[140:143], v[20:23], v[32:35]\n"
        "s_waitcnt lgkmcnt(1)\n"
        "ds_read_b128 v[20:23], v0 offset:15360\n"
        "v_mfma_f32_16x16x32_bf16 v[24:27], a[144:147], v[8:11], v[24:27]\n"
        "buffer_load_dword v4, v2, s[40:43], s86 offen sc1\n"
        "v_mfma_f32_16x16x32_bf16 v[28:31], a[148:151], v[8:11], v[28:31]\n"
        "v_mfma_f32_16x16x32_bf16 v[32:35], a[152:155], v[8:11], v[32:35]\n"
        "ds_read_b128 v[8:11], v0 offset:16384\n"
        "v_mfma_f32_16x16x32_bf16 v[24:27], a[156:159], v[12:15], v[24:27]\n"
        "v_mfma_f32_16x16x32_bf16 v[28:31], a[160:163], v[12:15], v[28:31]\n"
        "s_getreg_b32 s88, hwreg(HW_REG_IB_STS, 0, 4)\n"
        "v_mfma_f32_16x16x32_bf16 v[32:35], a[164:167], v[12:15], v[32:35]\n"
        "s_waitcnt lgkmcnt(1)\n"
        "ds_read_b128 v[12:15], v0 offset:17408\n"
        "v_mfma_f32_16x16x32_bf16 v[24:27], a[168:171], v[16:19], v[24:27]\n"
        "s_cmp_le_u32 s88, 2\n"
        "s_cselect_b32 s88, s73, s89\n"
        "s_cselect_b32 s73, s89, s73\n"
        "v_mfma_f32_16x16x32_bf16 v[28:31], a[172:175], v[16:19], v[28:31]\n"
        "buffer_store_dword v5, v3, s[40:43], s88 offen sc1\n"
        "v_mfma_f32_16x16x32_bf16 v[32:35], a[176:179], v[16:19], v[32:35]\n"
        "ds_read_b128 v[16:19], v0 offset:18432\n"
        "v_mfma_f32_16x16x32_bf16 v[24:27], a[180:183], v[20:23], v[24:27]\n"
        "v_mfma_f32_16x16x32_bf16 v[28:31], a[184:187], v[20:23], v[28:31]\n"
        "v_mfma_f32_16x16x32_bf16 v[32:35], a[188:191], v[20:23], v[32:35]\n"
        "s_waitcnt lgkmcnt(1)\n"
        "ds_read_b128 v[20:23], v0 offset:19456\n"
        "v_mfma_f32_16x16x32_bf16 v[24:27], a[192:195], v[8:11], v[24:27]\n"
        "s_getreg_b32 s88, hwreg(HW_REG_IB_STS, 0, 4)\n"
        "v_mfma_f32_16x16x32_bf16 v[28:31], a[196:199], v[8:11], v[28:31]\n"
        "s_cmp_le_u32 s88, 3\n"
        "s_cselect_b32 s88, s73, s89\n"
        "s_cselect_b32 s73, s89, s73\n"
        "v_mfma_f32_16x16x32_bf16 v[32:35], a[200:203], v[8:11], v[32:35]\n"
        "ds_read_b128 v[8:11], v0 offset:20480\n"
        "v_mfma_f32_16x16x32_bf16 v[24:27], a[204:207], v[12:15], v[24:27]\n"
        "buffer_store_dword v5, v3, s[40:43], s88 offen sc1\n"
        "v_mfma_f32_16x16x32_bf16 v[28:31], a[208:211], v[12:15], v[28:31]\n"
        "v_mfma_f32_16x16x32_bf16 v[32:35], a[212:215], v[12:15], v[32:35]\n"
        "s_waitcnt lgkmcnt(1)\n"
        "ds_read_b128 v[12:15], v0 offset:21504\n"
        "v_mfma_f32_16x16x32_bf16 v[24:27], a[216:219], v[16:19], v[24:27]\n"
        "v_mfma_f32_16x16x32_bf16 v[28:31], a[220:223], v[16:19], v[28:31]\n"
        "v_mfma_f32_16x16x32_bf16 v[32:35], a[224:227], v[16:19], v[32:35]\n"
        "s_getreg_b32 s88, hwreg(HW_REG_IB_STS, 0, 4)\n"
        "ds_read_b128 v[16:19], v0 offset:22528\n"
        "v_mfma_f32_16x16x32_bf16 v[24:27], a[228:231], v[20:23], v[24:27]\n"
        "s_cmp_le_u32 s88, 4\n"
        "s_cselect_b32 s88, s73, s89\n"
        "s_cselect_b32 s73, s89, s73\n"
        "v_mfma_f32_16x16x32_bf16 v[28:31], a[232:235], v[20:23], v[28:31]\n"
        "buffer_store_dword v5, v3, s[40:43], s88 offen sc1\n"
        "v_mfma_f32_16x16x32_bf16 v[32:35], a[236:239], v[20:23], v[32:35]\n"
        "s_waitcnt lgkmcnt(1)\n"
        "ds_read_b128 v[20:23], v0 offset:23552\n"
        "v_mfma_f32_16x16x32_bf16 v[24:27], a[240:243], v[8:11], v[24:27]\n"
        "v_mfma_f32_16x16x32_bf16 v[28:31], a[244:247], v[8:11], v[28:31]\n"
        "s_waitcnt vmcnt(3)\n"
        "v_cmp_gt_i32 vcc, s70, v4\n"
        "v_mfma_f32_16x16x32_bf16 v[32:35], a[248:251], v[8:11], v[32:35]\n"
        "ds_read_b128 v[8:11], v0 offset:24576\n"
        "v_mfma_f32_16x16x32_bf16 v[24:27], a[252:255], v[12:15], v[24:27]\n"
        "s_and_b64 vcc, vcc, 63\n"
        "v_mfma_f32_16x16x32_bf16 v[28:31], v[80:83], v[12:15], v[28:31]\n"
        "s_cbranch_vccnz .Lf2_i01_%=\n"
        "v_mfma_f32_16x16x32_bf16 v[32:35], v[84:87], v[12:15], v[32:35]\n"
        "s_waitcnt lgkmcnt(1)\n"
        "ds_read_b128 v[12:15], v0 offset:25600\n"
        "v_mfma_f32_16x16x32_bf16 v[24:27], v[88:91], v[16:19], v[24:27]\n"
        "buffer_load_dwordx4 v1, s[40:43], s71 offen offset:0 sc1 lds\n"
        "v_mfma_f32_16x16x32_bf16 v[28:31], v[92:95], v[16:19], v[28:31]\n"
        "v_mfma_f32_16x16x32_bf16 v[32:35], v[96:99], v[16:19], v[32:35]\n"
        "ds_read_b128 v[16:19], v0 offset:26624\n"
        "v_mfma_f32_16x16x32_bf16 v[24:27], v[100:103], v[20:23], v[24:27]\n"
        "buffer_load_dwordx4 v1, s[40:43], s71 offen offset:1024 sc1 lds\n"
        "v_mfma_f32_16x16x32_bf16 v[28:31], v[104:107], v[20:23], v[28:31]\n"
        "s_add_u32 m0, m0, s85\n"
        "s_add_u32 s71, s71, 16384\n"
        "v_mfma_f32_16x16x32_bf16 v[32:35], v[108:111], v[20:23], v[32:35]\n"
        "s_waitcnt lgkmcnt(1)\n"
        "ds_read_b128 v[20:23], v0 offset:27648\n"
        "v_mfma_f32_16x16x32_bf16 v[24:27], v[112:115], v[8:11], v[24:27]\n"
        "buffer_load_dwordx4 v1, s[40:43], s81 offen offset:0 sc1 lds\n"
        "v_mfma_f32_16x16x32_bf16 v[28:31], v[116:119], v[8:11], v[28:31]\n"
        "s_sub_u32 m0, m0, s85\n"
        "s_add_u32 m0, m0, 12288\n"
        "s_add_u32 s81, s81, 16384\n"
        "v_mfma_f32_16x16x32_bf16 v[32:35], v[120:123], v[8:11], v[32:35]\n"
        "ds_read_b128 v[8:11], v0 offset:28672\n"
        "v_mfma_f32_16x16x32_bf16 v[24:27], v[124:127], v[12:15], v[24:27]\n"
        "buffer_load_dwordx4 v1, s[40:43], s71 offen offset:0 sc1 lds\n"
        "v_mfma_f32_16x16x32_bf16 v[28:31], v[128:131], v[12:15], v[28:31]\n"
        "buffer_store_dword v5, v3, s[40:43], s73 offen sc1\n"
        "v_mfma_f32_16x16x32_bf16 v[32:35], v[132:135], v[12:15], v[32:35]\n"
        "s_waitcnt lgkmcnt(1)\n"
        "ds_read_b128 v[12:15], v0 offset:29696\n"
        "s_mov_b32 s84, s89\n"
        "v_mfma_f32_16x16x32_bf16 v[24:27], v[136:139], v[16:19], v[24:27]\n"
        "s_mov_b32 s73, s60\n"
        "buffer_load_dwordx4 v1, s[40:43], s71 offen offset:1024 sc1 lds\n"
        "v_mfma_f32_16x16x32_bf16 v[28:31], v[140:143], v[16:19], v[28:31]\n"
        "s_add_u32 m0, m0, s85\n"
        "s_add_u32 s71, s71, 16384\n"
        "s_add_u32 s74, s74, 1\n"
        "v_mfma_f32_16x16x32_bf16 v[32:35], v[144:147], v[16:19], v[32:35]\n"
        "s_add_u32 s68, s68, 1\n"
        "ds_read_b128 v[16:19], v0 offset:30720\n"
        "s_lshr_b32 s66, s68, 2\n"
        "v_mfma_f32_16x16x32_bf16 v[24:27], v[148:151], v[20:23], v[24:27]\n"
        "s_add_u32 s66, s66, 1\n"
        "s_bitcmp1_b32 s55, s66\n"
        "s_cselect_b32 s66, 8192, 0\n"
        "v_mfma_f32_16x16x32_bf16 v[28:31], v[152:155], v[20:23], v[28:31]\n"
        "s_lshl_b32 s75, s68, 19\n"
        "s_add_u32 s75, s75, s66\n"
        "s_add_u32 s72, s75, s59\n"
        "v_mfma_f32_16x16x32_bf16 v[32:35], v[156:159], v[20:23], v[32:35]\n"
        "s_waitcnt lgkmcnt(1)\n"
        "ds_read_b128 v[20:23], v0 offset:31744\n"
        "v_mfma_f32_16x16x32_bf16 v[24:27], v[160:163], v[8:11], v[24:27]\n"
        "v_mfma_f32_16x16x32_bf16 v[28:31], v[164:167], v[8:11], v[28:31]\n"
        "buffer_load_dwordx4 v1, s[40:43], s81 offen offset:0 sc1 lds\n"
        "v_mfma_f32_16x16x32_bf16 v[32:35], v[168:171], v[8:11], v[32:35]\n"
        "s_sub_u32 m0, m0, s85\n"
        "ds_read_b128 v[8:11], v0 offset:32768\n"
        "s_add_u32 m0, m0, 12288\n"
        "v_mfma_f32_16x16x32_bf16 v[24:27], v[172:175], v[12:15], v[24:27]\n"
        "s_add_u32 s81, s81, 16384\n"
        "v_mfma_f32_16x16x32_bf16 v[28:31], v[176:179], v[12:15], v[28:31]\n"
        "v_mfma_f32_16x16x32_bf16 v[32:35], v[180:183], v[12:15], v[32:35]\n"
        "s_waitcnt lgkmcnt(1)\n"
        "ds_read_b128 v[12:15], v0 offset:33792\n"
        "v_mfma_f32_16x16x32_bf16 v[24:27], v[184:187], v[16:19], v[24:27]\n"
        "buffer_load_dwordx4 v1, s[40:43], s71 offen offset:0 sc1 lds\n"
        "v_mfma_f32_16x16x32_bf16 v[28:31], v[188:191], v[16:19], v[28:31]\n"
        "v_mfma_f32_16x16x32_bf16 v[32:35], v[192:195], v[16:19], v[32:35]\n"
        "ds_read_b128 v[16:19], v0 offset:34816\n"
        "v_mfma_f32_16x16x32_bf16 v[24:27], v[196:199], v[20:23], v[24:27]\n"
        "buffer_load_dwordx4 v1, s[40:43], s71 offen offset:1024 sc1 lds\n"
        "v_mfma_f32_16x16x32_bf16 v[28:31], v[200:203], v[20:23], v[28:31]\n"
        "s_add_u32 m0, m0, s85\n"
        "s_add_u32 s71, s71, 16384\n"
        "v_mfma_f32_16x16x32_bf16 v[32:35], v[204:207], v[20:23], v[32:35]\n"
        "s_waitcnt lgkmcnt(1)\n"
        "ds_read_b128 v[20:23], v0 offset:35840\n"
        "v_mfma_f32_16x16x32_bf16 v[24:27], v[208:211], v[8:11], v[24:27]\n"
        "v_mfma_f32_16x16x32_bf16 v[28:31], v[212:215], v[8:11], v[28:31]\n"
        "s_waitcnt vmcnt(6)\n"
        "s_barrier\n"
        "v_add_u32 v0, s78, v6\n"
        "v_mfma_f32_16x16x32_bf16 v[32:35], v[216:219], v[8:11], v[32:35]\n"
        "ds_read_b128 v[8:11], v0 offset:0\n"
        "v_mfma_f32_16x16x32_bf16 v[24:27], v[220:223], v[12:15], v[24:27]\n"
        "buffer_load_dwordx4 v1, s[40:43], s81 offen offset:0 sc1 lds\n"
        "v_mfma_f32_16x16x32_bf16 v[28:31], v[224:227], v[12:15], v[28:31]\n"
        "s_add_u32 s69, s69, 1\n"
        "v_mfma_f32_16x16x32_bf16 v[32:35], v[228:231], v[12:15], v[32:35]\n"
        "s_waitcnt lgkmcnt(1)\n"
        "ds_read_b128 v[12:15], v0 offset:1024\n"
        "v_mfma_f32_16x16x32_bf16 v[24:27], v[232:235], v[16:19], v[24:27]\n"
        "v_mfma_f32_16x16x32_bf16 v[28:31], v[236:239], v[16:19], v[28:31]\n"
        "v_mfma_f32_16x16x32_bf16 v[32:35], v[240:243], v[16:19], v[32:35]\n"
        "ds_read_b128 v[16:19], v0 offset:2048\n"
        "v_mfma_f32_16x16x32_bf16 v[24:27], v[244:247], v[20:23], v[24:27]\n"
        "v_mfma_f32_16x16x32_bf16 v[28:31], v[248:251], v[20:23], v[28:31]\n"
        "v_mfma_f32_16x16x32_bf16 v[32:35], v[252:255], v[20:23], v[32:35]\n"
        "s_waitcnt lgkmcnt(1)\n"
        "ds_read_b128 v[20:23], v0 offset:3072\n"
        "s_branch .Lf2_i1_%=\n"
        ".Lf2_i01_%=:\n"
        "v_mfma_f32_16x16x32_bf16 v[32:35], v[84:87], v[12:15], v[32:35]\n"
        "s_waitcnt lgkmcnt(1)\n"
        "ds_read_b128 v[12:15], v0 offset:25600\n"
        "v_mfma_f32_16x16x32_bf16 v[24:27], v[88:91], v[16:19], v[24:27]\n"
        "buffer_load_dword v4, v2, s[40:43], s86 offen sc1\n"
        "v_mfma_f32_16x16x32_bf16 v[28:31], v[92:95], v[16:19], v[28:31]\n"
        "v_mfma_f32_16x16x32_bf16 v[32:35], v[96:99], v[16:19], v[32:35]\n"
        "ds_read_b128 v[16:19], v0 offset:26624\n"
        "v_mfma_f32_16x16x32_bf16 v[24:27], v[100:103], v[20:23], v[24:27]\n"
        "v_mfma_f32_16x16x32_bf16 v[28:31], v[104:107], v[20:23], v[28:31]\n"
        "buffer_store_dword v5, v3, s[40:43], s73 offen sc1\n"
        "v_mfma_f32_16x16x32_bf16 v[32:35], v[108:111], v[20:23], v[32:35]\n"
        "s_waitcnt lgkmcnt(1)\n"
        "ds_read_b128 v[20:23], v0 offset:27648\n"
        "s_mov_b32 s84, s89\n"
        "v_mfma_f32_16x16x32_bf16 v[24:27], v[112:115], v[8:11], v[24:27]\n"
        "s_mov_b32 s73, s60\n"
        "s_add_u32 s74, s74, 1\n"
        "s_add_u32 s68, s68, 1\n"
        "v_mfma_f32_16x16x32_bf16 v[28:31], v[116:119], v[8:11], v[28:31]\n"
        "s_lshr_b32 s66, s68, 2\n"
        "s_add_u32 s66, s66, 1\n"
        "v_mfma_f32_16x16x32_bf16 v[32:35], v[120:123], v[8:11], v[32:35]\n"
        "s_bitcmp1_b32 s55, s66\n"
        "s_cselect_b32 s66, 8192, 0\n"
        "ds_read_b128 v[8:11], v0 offset:28672\n"
        "v_mfma_f32_16x16x32_bf16 v[24:27], v[124:127], v[12:15], v[24:27]\n"
        "s_lshl_b32 s75, s68, 19\n"
        "s_add_u32 s75, s75, s66\n"
        "s_add_u32 s72, s75, s59\n"
        "v_mfma_f32_16x16x32_bf16 v[28:31], v[128:131], v[12:15], v[28:31]\n"
        "v_mfma_f32_16x16x32_bf16 v[32:35], v[132:135], v[12:15], v[32:35]\n"
        "s_waitcnt lgkmcnt(1)\n"
        "ds_read_b128 v[12:15], v0 offset:29696\n"
        "v_mfma_f32_16x16x32_bf16 v[24:27], v[136:139], v[16:19], v[24:27]\n"
        "v_mfma_f32_16x16x32_bf16 v[28:31], v[140:143], v[16:19], v[28:31]\n"
        "v_mfma_f32_16x16x32_bf16 v[32:35], v[144:147], v[16:19], v[32:35]\n"
        "ds_read_b128 v[16:19], v0 offset:30720\n"
        "v_mfma_f32_16x16x32_bf16 v[24:27], v[148:151], v[20:23], v[24:27]\n"
        "v_mfma_f32_16x16x32_bf16 v[28:31], v[152:155], v[20:23], v[28:31]\n"
        "v_mfma_f32_16x16x32_bf16 v[32:35], v[156:159], v[20:23], v[32:35]\n"
        "s_waitcnt lgkmcnt(1)\n"
        "ds_read_b128 v[20:23], v0 offset:31744\n"
        "v_mfma_f32_16x16x32_bf16 v[24:27], v[160:163], v[8:11], v[24:27]\n"
        "v_mfma_f32_16x16x32_bf16 v[28:31], v[164:167], v[8:11], v[28:31]\n"
        "v_mfma_f32_16x16x32_bf16 v[32:35], v[168:171], v[8:11], v[32:35]\n"
        "ds_read_b128 v[8:11], v0 offset:32768\n"
        "v_mfma_f32_16x16x32_bf16 v[24:27], v[172:175], v[12:15], v[24:27]\n"
        "v_mfma_f32_16x16x32_bf16 v[28:31], v[176:179], v[12:15], v[28:31]\n"
        "v_mfma_f32_16x16x32_bf16 v[32:35], v[180:183], v[12:15], v[32:35]\n"
        "s_waitcnt lgkmcnt(1)\n"
        "ds_read_b128 v[12:15], v0 offset:33792\n"
        "v_mfma_f32_16x16x32_bf16 v[24:27], v[184:187], v[16:19], v[24:27]\n"
        "v_mfma_f32_16x16x32_bf16 v[28:31], v[188:191], v[16:19], v[28:31]\n"
        "v_mfma_f32_16x16x32_bf16 v[32:35], v[192:195], v[16:19], v[32:35]\n"
        "ds_read_b128 v[16:19], v0 offset:34816\n"
        "v_mfma_f32_16x16x32_bf16 v[24:27], v[196:199], v[20:23], v[24:27]\n"
        "s_waitcnt vmcnt(1)\n"
        "v_cmp_gt_i32 vcc, s70, v4\n"
        "v_mfma_f32_16x16x32_bf16 v[28:31], v[200:203], v[20:23], v[28:31]\n"
        "v_mfma_f32_16x16x32_bf16 v[32:35], v[204:207], v[20:23], v[32:35]\n"
        "s_waitcnt lgkmcnt(1)\n"
        "ds_read_b128 v[20:23], v0 offset:35840\n"
        "s_and_b64 vcc, vcc, 63\n"
        "v_mfma_f32_16x16x32_bf16 v[24:27], v[208:211], v[8:11], v[24:27]\n"
        "s_cbranch_vccnz .Lf2_i02_%=\n"
        "v_mfma_f32_16x16x32_bf16 v[28:31], v[212:215], v[8:11], v[28:31]\n"
        "buffer_load_dwordx4 v1, s[40:43], s71 offen offset:0 sc1 lds\n"
        "v_mfma_f32_16x16x32_bf16 v[32:35], v[216:219], v[8:11], v[32:35]\n"
        "v_mfma_f32_16x16x32_bf16 v[24:27], v[220:223], v[12:15], v[24:27]\n"
        "buffer_load_dwordx4 v1, s[40:43], s71 offen offset:1024 sc1 lds\n"
        "v_mfma_f32_16x16x32_bf16 v[28:31], v[224:227], v[12:15], v[28:31]\n"
        "s_add_u32 m0, m0, s85\n"
        "s_add_u32 s71, s71, 16384\n"
        "v_mfma_f32_16x16x32_bf16 v[32:35], v[228:231], v[12:15], v[32:35]\n"
        "s_waitcnt lgkmcnt(0)\n"
        "buffer_load_dwordx4 v1, s[40:43], s81 offen offset:0 sc1 lds\n"
        "v_mfma_f32_16x16x32_bf16 v[24:27], v[232:235], v[16:19], v[24:27]\n"
        "s_sub_u32 m0, m0, s85\n"
        "s_add_u32 m0, m0, 12288\n"
        "s_add_u32 s81, s81, 16384\n"
        "v_mfma_f32_16x16x32_bf16 v[28:31], v[236:239], v[16:19], v[28:31]\n"
        "buffer_load_dwordx4 v1, s[40:43], s71 offen offset:0 sc1 lds\n"
        "v_mfma_f32_16x16x32_bf16 v[32:35], v[240:243], v[16:19], v[32:35]\n"
        "v_mfma_f32_16x16x32_bf16 v[24:27], v[244:247], v[20:23], v[24:27]\n"
        "buffer_load_dwordx4 v1, s[40:43], s71 offen offset:1024 sc1 lds\n"
        "v_mfma_f32_16x16x32_bf16 v[28:31], v[248:251], v[20:23], v[28:31]\n"
        "s_add_u32 m0, m0, s85\n"
        "s_add_u32 s71, s71, 16384\n"
        "v_mfma_f32_16x16x32_bf16 v[32:35], v[252:255], v[20:23], v[32:35]\n"
        "buffer_load_dwordx4 v1, s[40:43], s81 offen offset:0 sc1 lds\n"
        "s_sub_u32 m0, m0, s85\n"
        "s_add_u32 m0, m0, 12288\n"
        "s_add_u32 s81, s81, 16384\n"
        "buffer_load_dwordx4 v1, s[40:43], s71 offen offset:0 sc1 lds\n"
        "buffer_load_dwordx4 v1, s[40:43], s71 offen offset:1024 sc1 lds\n"
        "s_add_u32 m0, m0, s85\n"
        "s_add_u32 s71, s71, 16384\n"
        "buffer_load_dwordx4 v1, s[40:43], s81 offen offset:0 sc1 lds\n"
        "s_add_u32 s69, s69, 1\n"
        "s_nop 2\n"
        "v_cvt_pk_bf16_f32 v24, v24, v25\n"
        "v_cvt_pk_bf16_f32 v25, v26, v27\n"
        "v_cvt_pk_bf16_f32 v26, v28, v29\n"
        "v_cvt_pk_bf16_f32 v27, v30, v31\n"
        "buffer_store_dwordx4 v[24:27], v48, s[40:43], s72 offen offset:0 sc1\n"
        "v_cvt_pk_bf16_f32 v32, v32, v33\n"
        "v_cvt_pk_bf16_f32 v33, v34, v35\n"
        "s_nop 0\n"
        "v_permlane16_swap_b32 v32, v34\n"
        "v_permlane16_swap_b32 v33, v35\n"
        "buffer_store_dwordx4 v[32:35], v7, s[40:43], s72 offen offset:0 sc1\n"
        "v_mov_b32 v5, s74\n"
        "s_mov_b32 s84, s60\n"
        "s_waitcnt vmcnt(8)\n"
        "s_branch .Lf2_go0_%=\n"
        ".Lf2_i02_%=:\n"
        "v_mfma_f32_16x16x32_bf16 v[28:31], v[212:215], v[8:11], v[28:31]\n"
        "buffer_load_dword v4, v2, s[40:43], s86 offen sc1\n"
        "v_mfma_f32_16x16x32_bf16 v[32:35], v[216:219], v[8:11], v[32:35]\n"
        "v_mfma_f32_16x16x32_bf16 v[24:27], v[220:223], v[12:15], v[24:27]\n"
        "v_mfma_f32_16x16x32_bf16 v[28:31], v[224:227], v[12:15], v[28:31]\n"
        "v_mfma_f32_16x16x32_bf16 v[32:35], v[228:231], v[12:15], v[32:35]\n"
        "s_waitcnt lgkmcnt(0)\n"
        "v_mfma_f32_16x16x32_bf16 v[24:27], v[232:235], v[16:19], v[24:27]\n"
        "v_mfma_f32_16x16x32_bf16 v[28:31], v[236:239], v[16:19], v[28:31]\n"
        "v_mfma_f32_16x16x32_bf16 v[32:35], v[240:243], v[16:19], v[32:35]\n"
        "v_mfma_f32_16x16x32_bf16 v[24:27], v[244:247], v[20:23], v[24:27]\n"
        "v_mfma_f32_16x16x32_bf16 v[28:31], v[248:251], v[20:23], v[28:31]\n"
        "v_mfma_f32_16x16x32_bf16 v[32:35], v[252:255], v[20:23], v[32:35]\n"
        "s_nop 2\n"
        "v_cvt_pk_bf16_f32 v24, v24, v25\n"
        "v_cvt_pk_bf16_f32 v25, v26, v27\n"
        "v_cvt_pk_bf16_f32 v26, v28, v29\n"
        "v_cvt_pk_bf16_f32 v27, v30, v31\n"
        "buffer_store_dwordx4 v[24:27], v48, s[40:43], s72 offen offset:0 sc1\n"
        "v_cvt_pk_bf16_f32 v32, v32, v33\n"
        "v_cvt_pk_bf16_f32 v33, v34, v35\n"
        "s_nop 0\n"
        "v_permlane16_swap_b32 v32, v34\n"
        "v_permlane16_swap_b32 v33, v35\n"
        "buffer_store_dwordx4 v[32:35], v7, s[40:43], s72 offen offset:0 sc1\n"
        "s_waitcnt vmcnt(2)\n"
        "v_cmp_gt_i32 vcc, s70, v4\n"
        "s_nop 0\n"
        "s_and_b64 vcc, vcc, 63\n"
        "s_cbranch_vccnz .Lf2_late0_%=\n"
        "buffer_load_dwordx4 v1, s[40:43], s71 offen offset:0 sc1 lds\n"
        "buffer_load_dwordx4 v1, s[40:43], s71 offen offset:1024 sc1 lds\n"
        "s_add_u32 m0, m0, s85\n"
        "s_add_u32 s71, s71, 16384\n"
        "buffer_load_dwordx4 v1, s[40:43], s81 offen offset:0 sc1 lds\n"
        "s_sub_u32 m0, m0, s85\n"
        "s_add_u32 m0, m0, 12288\n"
        "s_add_u32 s81, s81, 16384\n"
        "buffer_load_dwordx4 v1, s[40:43], s71 offen offset:0 sc1 lds\n"
        "buffer_load_dwordx4 v1, s[40:43], s71 offen offset:1024 sc1 lds\n"
        "s_add_u32 m0, m0, s85\n"
        "s_add_u32 s71, s71, 16384\n"
        "buffer_load_dwordx4 v1, s[40:43], s81 offen offset:0 sc1 lds\n"
        "s_sub_u32 m0, m0, s85\n"
        "s_add_u32 m0, m0, 12288\n"
        "s_add_u32 s81, s81, 16384\n"
        "buffer_load_dwordx4 v1, s[40:43], s71 offen offset:0 sc1 lds\n"
        "buffer_load_dwordx4 v1, s[40:43], s71 offen offset:1024 sc1 lds\n"
        "s_add_u32 m0, m0, s85\n"
        "s_add_u32 s71, s71, 16384\n"
        "buffer_load_dwordx4 v1, s[40:43], s81 offen offset:0 sc1 lds\n"
        "s_add_u32 s69, s69, 1\n"
        "s_waitcnt vmcnt(9)\n"
        "v_mov_b32 v5, s74\n"
        "buffer_store_dword v5, v3, s[40:43], s73 offen sc1\n"
        "s_waitcnt vmcnt(7)\n"
        "s_branch .Lf2_go0_%=\n"
        ".Lf2_late0_%=:\n"
        "s_waitcnt vmcnt(0)\n"
        "v_mov_b32 v5, s74\n"
        "buffer_store_dword v5, v3, s[40:43], s73 offen sc1\n"
        "s_mov_b32 s64, 0\n"
        ".Lf2_in0_top_%=:\n"
        "buffer_load_dword v4, v2, s[40:43], s86 offen sc1\n"
        "s_waitcnt vmcnt(0)\n"
        "v_cmp_gt_i32 vcc, s70, v4\n"
        "s_nop 0\n"
        "s_and_b64 vcc, vcc, 63\n"
        "s_cbranch_vccz .Lf2_in0_ok_%=\n"
        "s_add_u32 s64, s64, 1\n"
        "s_and_b32 s65, s64, 255\n"
        "s_cbranch_scc1 .Lf2_in0_go_%=\n"
        "s_mov_b32 s65, 49152\n"
        "s_cmp_lt_u32 s64, 1048576\n"
        "s_cbranch_scc1 .Lf2_in0_chk_%=\n"
        "v_mov_b32 v4, 1\n"
        "buffer_store_dword v4, off, s[40:43], s65 sc1\n"
        "s_waitcnt vmcnt(0)\n"
        ".Lf2_in0_chk_%=:\n"
        "buffer_load_dword v4, off, s[40:43], s65 sc1\n"
        "s_waitcnt vmcnt(0)\n"
        "v_readfirstlane_b32 s65, v4\n"
        "s_nop 0\n"
        "s_cmp_lg_u32 s65, 1\n"
        "s_cbranch_scc1 .Lf2_in0_go_%=\n"
        "s_endpgm\n"
        ".Lf2_in0_go_%=:\n"
        "s_branch .Lf2_in0_top_%=\n"
        ".Lf2_in0_ok_%=:\n"
        "buffer_load_dwordx4 v1, s[40:43], s71 offen offset:0 sc1 lds\n"
        "buffer_load_dwordx4 v1, s[40:43], s71 offen offset:1024 sc1 lds\n"
        "s_add_u32 m0, m0, s85\n"
        "s_add_u32 s71, s71, 16384\n"
        "buffer_load_dwordx4 v1, s[40:43], s81 offen offset:0 sc1 lds\n"
        "s_sub_u32 m0, m0, s85\n"
        "s_add_u32 m0, m0, 12288\n"
        "s_add_u32 s81, s81, 16384\n"
        "buffer_load_dwordx4 v1, s[40:43], s71 offen offset:0 sc1 lds\n"
        "buffer_load_dwordx4 v1, s[40:43], s71 offen offset:1024 sc1 lds\n"
        "s_add_u32 m0, m0, s85\n"
        "s_add_u32 s71, s71, 16384\n"
        "buffer_load_dwordx4 v1, s[40:43], s81 offen offset:0 sc1 lds\n"
        "s_sub_u32 m0, m0, s85\n"
        "s_add_u32 m0, m0, 12288\n"
        "s_add_u32 s81, s81, 16384\n"
        "buffer_load_dwordx4 v1, s[40:43], s71 offen offset:0 sc1 lds\n"
        "buffer_load_dwordx4 v1, s[40:43], s71 offen offset:1024 sc1 lds\n"
        "s_add_u32 m0, m0, s85\n"
        "s_add_u32 s71, s71, 16384\n"
        "buffer_load_dwordx4 v1, s[40:43], s81 offen offset:0 sc1 lds\n"
        "s_add_u32 s69, s69, 1\n"
        "s_waitcnt vmcnt(6)\n"
        ".Lf2_go0_%=:\n"
        "s_mov_b32 s72, 241172480\n"
        "s_mov_b32 s83, 241172480\n"
        "s_mov_b32 s73, 241172480\n"
        "v_add_u32 v0, s78, v6\n"
        "s_barrier\n"
        "ds_read_b128 v[8:11], v0 offset:0\n"
        "ds_read_b128 v[12:15], v0 offset:1024\n"
        "ds_read_b128 v[16:19], v0 offset:2048\n"
        "ds_read_b128 v[20:23], v0 offset:3072\n"
        "s_waitcnt lgkmcnt(2)\n"
        "s_branch .Lf2_i1_%=\n"
        ".Lf2_i1_%=:\n"
        "v_mfma_f32_16x16x32_bf16 v[36:39], a[0:3], v[8:11], 0\n"
        "v_mfma_f32_16x16x32_bf16 v[40:43], a[4:7], v[8:11], 0\n"
        "s_lshr_b32 s67, s69, 1\n"
        "buffer_load_dword v4, v2, s[40:43], s86 offen sc1\n"
        "v_mfma_f32_16x16x32_bf16 v[44:47], a[8:11], v[8:11], 0\n"
        "s_lshr_b32 s65, s67, 1\n"
        "ds_read_b128 v[8:11], v0 offset:4096\n"
        "v_cvt_pk_bf16_f32 v24, v24, v25\n"
        "v_mfma_f32_16x16x32_bf16 v[36:39], a[12:15], v[12:15], v[36:39]\n"
        "s_bitcmp1_b32 s54, s65\n"
        "s_cselect_b32 s65, 8192, 0\n"
        "s_lshl_b32 s77, s67, 20\n"
        "v_mfma_f32_16x16x32_bf16 v[40:43], a[16:19], v[12:15], v[40:43]\n"
        "s_add_u32 s77, s77, s65\n"
        "s_add_u32 s71, s77, s58\n"
        "s_add_u32 s81, s77, s62\n"
        "v_mfma_f32_16x16x32_bf16 v[44:47], a[20:23], v[12:15], v[44:47]\n"
        "s_waitcnt lgkmcnt(1)\n"
        "ds_read_b128 v[12:15], v0 offset:5120\n"
        "s_add_u32 s78, s78, 36864\n"
        "v_mfma_f32_16x16x32_bf16 v[36:39], a[24:27], v[16:19], v[36:39]\n"
        "s_cmp_eq_u32 s78, 110592\n"
        "s_cselect_b32 s78, 0, s78\n"
        "s_add_u32 m0, s78, s61\n"
        "v_mfma_f32_16x16x32_bf16 v[40:43], a[28:31], v[16:19], v[40:43]\n"
        "s_lshr_b32 s70, s69, 0\n"
        "v_cvt_pk_bf16_f32 v25, v26, v27\n"
        "v_cvt_pk_bf16_f32 v26, v28, v29\n"
        "v_mfma_f32_16x16x32_bf16 v[44:47], a[32:35], v[16:19], v[44:47]\n"
        "ds_read_b128 v[16:19], v0 offset:6144\n"
        "v_cvt_pk_bf16_f32 v27, v30, v31\n"
        "v_mfma_f32_16x16x32_bf16 v[36:39], a[36:39], v[20:23], v[36:39]\n"
        "buffer_store_dwordx4 v[24:27], v48, s[40:43], s72 offen offset:0 sc1\n"
        "v_mfma_f32_16x16x32_bf16 v[40:43], a[40:43], v[20:23], v[40:43]\n"
        "v_cvt_pk_bf16_f32 v32, v32, v33\n"
        "v_cvt_pk_bf16_f32 v33, v34, v35\n"
        "v_mfma_f32_16x16x32_bf16 v[44:47], a[44:47], v[20:23], v[44:47]\n"
        "s_waitcnt lgkmcnt(1)\n"
        "ds_read_b128 v[20:23], v0 offset:7168\n"
        "s_nop 0\n"
        "v_mfma_f32_16x16x32_bf16 v[36:39], a[48:51], v[8:11], v[36:39]\n"
        "v_permlane16_swap_b32 v32, v34\n"
        "v_mfma_f32_16x16x32_bf16 v[40:43], a[52:55], v[8:11], v[40:43]\n"
        "v_permlane16_swap_b32 v33, v35\n"
        "v_mfma_f32_16x16x32_bf16 v[44:47], a[56:59], v[8:11], v[44:47]\n"
        "ds_read_b128 v[8:11], v0 offset:8192\n"
        "v_mfma_f32_16x16x32_bf16 v[36:39], a[60:63], v[12:15], v[36:39]\n"
        "v_mfma_f32_16x16x32_bf16 v[40:43], a[64:67], v[12:15], v[40:43]\n"
        "buffer_store_dwordx4 v[32:35], v7, s[40:43], s72 offen offset:0 sc1\n"
        "v_mfma_f32_16x16x32_bf16 v[44:47], a[68:71], v[12:15], v[44:47]\n"
        "s_waitcnt lgkmcnt(1)\n"
        "ds_read_b128 v[12:15], v0 offset:9216\n"
        "v_mfma_f32_16x16x32_bf16 v[36:39], a[72:75], v[16:19], v[36:39]\n"
        "v_mfma_f32_16x16x32_bf16 v[40:43], a[76:79], v[16:19], v[40:43]\n"
        "v_mfma_f32_16x16x32_bf16 v[44:47], a[80:83], v[16:19], v[44:47]\n"
        "ds_read_b128 v[16:19], v0 offset:10240\n"
        "v_mfma_f32_16x16x32_bf16 v[36:39], a[84:87], v[20:23], v[36:39]\n"
        "v_mfma_f32_16x16x32_bf16 v[40:43], a[88:91], v[20:23], v[40:43]\n"
        "v_mfma_f32_16x16x32_bf16 v[44:47], a[92:95], v[20:23], v[44:47]\n"
        "s_waitcnt lgkmcnt(1)\n"
        "ds_read_b128 v[20:23], v0 offset:11264\n"
        "v_mfma_f32_16x16x32_bf16 v[36:39], a[96:99], v[8:11], v[36:39]\n"
        "v_mfma_f32_16x16x32_bf16 v[40:43], a[100:103], v[8:11], v[40:43]\n"
        "s_waitcnt vmcnt(3)\n"
        "s_barrier\n"
        "v_mfma_f32_16x16x32_bf16 v[44:47], a[104:107], v[8:11], v[44:47]\n"
        "ds_read_b128 v[8:11], v0 offset:12288\n"
        "v_mfma_f32_16x16x32_bf16 v[36:39], a[108:111], v[12:15], v[36:39]\n"
        "buffer_store_dword v5, v3, s[40:43], s84 offen sc1\n"
        "v_mfma_f32_16x16x32_bf16 v[40:43], a[112:115], v[12:15], v[40:43]\n"
        "v_mov_b32 v5, s74\n"
        "v_mfma_f32_16x16x32_bf16 v[44:47], a[116:119], v[12:15], v[44:47]\n"
        "s_waitcnt lgkmcnt(1)\n"
        "ds_read_b128 v[12:15], v0 offset:13312\n"
        "v_mfma_f32_16x16x32_bf16 v[36:39], a[120:123], v[16:19], v[36:39]\n"
        "v_mfma_f32_16x16x32_bf16 v[40:43], a[124:127], v[16:19], v[40:43]\n"
        "s_waitcnt vmcnt(3)\n"
        "v_cmp_gt_i32 vcc, s70, v4\n"
        "v_mfma_f32_16x16x32_bf16 v[44:47], a[128:131], v[16:19], v[44:47]\n"
        "ds_read_b128 v[16:19], v0 offset:14336\n"
        "v_mfma_f32_16x16x32_bf16 v[36:39], a[132:135], v[20:23], v[36:39]\n"
        "s_and_b64 vcc, vcc, 63\n"
        "v_mfma_f32_16x16x32_bf16 v[40:43], a[136:139], v[20:23], v[40:43]\n"
        "s_cbranch_vccnz .Lf2_i10_%=\n"
        "v_mfma_f32_16x16x32_bf16 v[44:47], a[140:143], v[20:23], v[44:47]\n"
        "s_waitcnt lgkmcnt(1)\n"
        "ds_read_b128 v[20:23], v0 offset:15360\n"
        "v_mfma_f32_16x16x32_bf16 v[36:39], a[144:147], v[8:11], v[36:39]\n"
        "buffer_load_dwordx4 v1, s[40:43], s71 offen offset:0 sc1 lds\n"
        "v_mfma_f32_16x16x32_bf16 v[40:43], a[148:151], v[8:11], v[40:43]\n"
        "v_mfma_f32_16x16x32_bf16 v[44:47], a[152:155], v[8:11], v[44:47]\n"
        "ds_read_b128 v[8:11], v0 offset:16384\n"
        "v_mfma_f32_16x16x32_bf16 v[36:39], a[156:159], v[12:15], v[36:39]\n"
        "buffer_load_dwordx4 v1, s[40:43], s71 offen offset:1024 sc1 lds\n"
        "v_mfma_f32_16x16x32_bf16 v[40:43], a[160:163], v[12:15], v[40:43]\n"
        "s_add_u32 m0, m0, s85\n"
        "s_add_u32 s71, s71, 16384\n"
        "v_mfma_f32_16x16x32_bf16 v[44:47], a[164:167], v[12:15], v[44:47]\n"
        "s_waitcnt lgkmcnt(1)\n"
        "ds_read_b128 v[12:15], v0 offset:17408\n"
        "v_mfma_f32_16x16x32_bf16 v[36:39], a[168:171], v[16:19], v[36:39]\n"
        "s_getreg_b32 s88, hwreg(HW_REG_IB_STS, 0, 4)\n"
        "v_mfma_f32_16x16x32_bf16 v[40:43], a[172:175], v[16:19], v[40:43]\n"
        "buffer_load_dwordx4 v1, s[40:43], s81 offen offset:0 sc1 lds\n"
        "v_mfma_f32_16x16x32_bf16 v[44:47], a[176:179], v[16:19], v[44:47]\n"
        "s_sub_u32 m0, m0, s85\n"
        "ds_read_b128 v[16:19], v0 offset:18432\n"
        "s_add_u32 m0, m0, 12288\n"
        "v_mfma_f32_16x16x32_bf16 v[36:39], a[180:183], v[20:23], v[36:39]\n"
        "s_add_u32 s81, s81, 16384\n"
        "v_mfma_f32_16x16x32_bf16 v[40:43], a[184:187], v[20:23], v[40:43]\n"
        "s_cmp_le_u32 s88, 3\n"
        "s_cselect_b32 s88, s73, s89\n"
        "s_cselect_b32 s73, s89, s73\n"
        "v_mfma_f32_16x16x32_bf16 v[44:47], a[188:191], v[20:23], v[44:47]\n"
        "s_waitcnt lgkmcnt(1)\n"
        "ds_read_b128 v[20:23], v0 offset:19456\n"
        "v_mfma_f32_16x16x32_bf16 v[36:39], a[192:195], v[8:11], v[36:39]\n"
        "buffer_load_dwordx4 v1, s[40:43], s71 offen offset:0 sc1 lds\n"
        "v_mfma_f32_16x16x32_bf16 v[40:43], a[196:199], v[8:11], v[40:43]\n"
        "buffer_store_dword v5, v3, s[40:43], s88 offen sc1\n"
        "v_mfma_f32_16x16x32_bf16 v[44:47], a[200:203], v[8:11], v[44:47]\n"
        "s_getreg_b32 s88, hwreg(HW_REG_IB_STS, 0, 4)\n"
        "ds_read_b128 v[8:11], v0 offset:20480\n"
        "v_mfma_f32_16x16x32_bf16 v[36:39], a[204:207], v[12:15], v[36:39]\n"
        "buffer_load_dwordx4 v1, s[40:43], s71 offen offset:1024 sc1 lds\n"
        "v_mfma_f32_16x16x32_bf16 v[40:43], a[208:211], v[12:15], v[40:43]\n"
        "s_add_u32 m0, m0, s85\n"
        "s_add_u32 s71, s71, 16384\n"
        "v_mfma_f32_16x16x32_bf16 v[44:47], a[212:215], v[12:15], v[44:47]\n"
        "s_waitcnt lgkmcnt(1)\n"
        "ds_read_b128 v[12:15], v0 offset:21504\n"
        "v_mfma_f32_16x16x32_bf16 v[36:39], a[216:219], v[16:19], v[36:39]\n"
        "s_cmp_le_u32 s88, 6\n"
        "s_cselect_b32 s88, s73, s89\n"
        "s_cselect_b32 s73, s89, s73\n"
        "v_mfma_f32_16x16x32_bf16 v[40:43], a[220:223], v[16:19], v[40:43]\n"
        "buffer_load_dwordx4 v1, s[40:43], s81 offen offset:0 sc1 lds\n"
        "v_mfma_f32_16x16x32_bf16 v[44:47], a[224:227], v[16:19], v[44:47]\n"
        "s_sub_u32 m0, m0, s85\n"
        "ds_read_b128 v[16:19], v0 offset:22528\n"
        "s_add_u32 m0, m0, 12288\n"
        "v_mfma_f32_16x16x32_bf16 v[36:39], a[228:231], v[20:23], v[36:39]\n"
        "s_add_u32 s81, s81, 16384\n"
        "buffer_store_dword v5, v3, s[40:43], s88 offen sc1\n"
        "v_mfma_f32_16x16x32_bf16 v[40:43], a[232:235], v[20:23], v[40:43]\n"
        "s_getreg_b32 s88, hwreg(HW_REG_IB_STS, 0, 4)\n"
        "v_mfma_f32_16x16x32_bf16 v[44:47], a[236:239], v[20:23], v[44:47]\n"
        "s_waitcnt lgkmcnt(1)\n"
        "ds_read_b128 v[20:23], v0 offset:23552\n"
        "v_mfma_f32_16x16x32_bf16 v[36:39], a[240:243], v[8:11], v[36:39]\n"
        "buffer_load_dwordx4 v1, s[40:43], s71 offen offset:0 sc1 lds\n"
        "v_mfma_f32_16x16x32_bf16 v[40:43], a[244:247], v[8:11], v[40:43]\n"
        "s_cmp_le_u32 s88, 9\n"
        "s_cselect_b32 s88, s73, s89\n"
        "s_cselect_b32 s73, s89, s73\n"
        "v_mfma_f32_16x16x32_bf16 v[44:47], a[248:251], v[8:11], v[44:47]\n"
        "ds_read_b128 v[8:11], v0 offset:24576\n"
        "v_mfma_f32_16x16x32_bf16 v[36:39], a[252:255], v[12:15], v[36:39]\n"
        "buffer_load_dwordx4 v1, s[40:43], s71 offen offset:1024 sc1 lds\n"
        "v_mfma_f32_16x16x32_bf16 v[40:43], v[80:83], v[12:15], v[40:43]\n"
        "s_add_u32 m0, m0, s85\n"
        "s_add_u32 s71, s71, 16384\n"
        "v_mfma_f32_16x16x32_bf16 v[44:47], v[84:87], v[12:15], v[44:47]\n"
        "s_waitcnt lgkmcnt(1)\n"
        "ds_read_b128 v[12:15], v0 offset:25600\n"
        "v_mfma_f32_16x16x32_bf16 v[36:39], v[88:91], v[16:19], v[36:39]\n"
        "buffer_store_dword v5, v3, s[40:43], s88 offen sc1\n"
        "v_mfma_f32_16x16x32_bf16 v[40:43], v[92:95], v[16:19], v[40:43]\n"
        "buffer_load_dwordx4 v1, s[40:43], s81 offen offset:0 sc1 lds\n"
        "v_mfma_f32_16x16x32_bf16 v[44:47], v[96:99], v[16:19], v[44:47]\n"
        "s_add_u32 s69, s69, 1\n"
        "ds_read_b128 v[16:19], v0 offset:26624\n"
        "v_mfma_f32_16x16x32_bf16 v[36:39], v[100:103], v[20:23], v[36:39]\n"
        "v_mfma_f32_16x16x32_bf16 v[40:43], v[104:107], v[20:23], v[40:43]\n"
        "s_waitcnt vmcnt(13)\n"
        "buffer_store_dword v5, v3, s[40:43], s73 offen sc1\n"
        "v_mfma_f32_16x16x32_bf16 v[44:47], v[108:111], v[20:23], v[44:47]\n"
        "s_waitcnt lgkmcnt(1)\n"
        "ds_read_b128 v[20:23], v0 offset:27648\n"
        "s_mov_b32 s84, s89\n"
        "v_mfma_f32_16x16x32_bf16 v[36:39], v[112:115], v[8:11], v[36:39]\n"
        "s_mov_b32 s73, s60\n"
        "s_add_u32 s74, s74, 1\n"
        "s_add_u32 s72, s75, s59\n"
        "v_mfma_f32_16x16x32_bf16 v[40:43], v[116:119], v[8:11], v[40:43]\n"
        "s_add_u32 s72, s72, 147456\n"
        "v_mfma_f32_16x16x32_bf16 v[44:47], v[120:123], v[8:11], v[44:47]\n"
        "ds_read_b128 v[8:11], v0 offset:28672\n"
        "v_mfma_f32_16x16x32_bf16 v[36:39], v[124:127], v[12:15], v[36:39]\n"
        "v_mfma_f32_16x16x32_bf16 v[40:43], v[128:131], v[12:15], v[40:43]\n"
        "v_mfma_f32_16x16x32_bf16 v[44:47], v[132:135], v[12:15], v[44:47]\n"
        "s_waitcnt lgkmcnt(1)\n"
        "ds_read_b128 v[12:15], v0 offset:29696\n"
        "v_mfma_f32_16x16x32_bf16 v[36:39], v[136:139], v[16:19], v[36:39]\n"
        "v_mfma_f32_16x16x32_bf16 v[40:43], v[140:143], v[16:19], v[40:43]\n"
        "v_mfma_f32_16x16x32_bf16 v[44:47], v[144:147], v[16:19], v[44:47]\n"
        "ds_read_b128 v[16:19], v0 offset:30720\n"
        "v_mfma_f32_16x16x32_bf16 v[36:39], v[148:151], v[20:23], v[36:39]\n"
        "v_mfma_f32_16x16x32_bf16 v[40:43], v[152:155], v[20:23], v[40:43]\n"
        "v_mfma_f32_16x16x32_bf16 v[44:47], v[156:159], v[20:23], v[44:47]\n"
        "s_waitcnt lgkmcnt(1)\n"
        "ds_read_b128 v[20:23], v0 offset:31744\n"
        "v_mfma_f32_16x16x32_bf16 v[36:39], v[160:163], v[8:11], v[36:39]\n"
        "v_mfma_f32_16x16x32_bf16 v[40:43], v[164:167], v[8:11], v[40:43]\n"
        "v_mfma_f32_16x16x32_bf16 v[44:47], v[168:171], v[8:11], v[44:47]\n"
        "ds_read_b128 v[8:11], v0 offset:32768\n"
        "v_mfma_f32_16x16x32_bf16 v[36:39], v[172:175], v[12:15], v[36:39]\n"
        "v_mfma_f32_16x16x32_bf16 v[40:43], v[176:179], v[12:15], v[40:43]\n"
        "v_mfma_f32_16x16x32_bf16 v[44:47], v[180:183], v[12:15], v[44:47]\n"
        "s_waitcnt lgkmcnt(1)\n"
        "ds_read_b128 v[12:15], v0 offset:33792\n"
        "v_mfma_f32_16x16x32_bf16 v[36:39], v[184:187], v[16:19], v[36:39]\n"
        "v_mfma_f32_16x16x32_bf16 v[40:43], v[188:191], v[16:19], v[40:43]\n"
        "v_mfma_f32_16x16x32_bf16 v[44:47], v[192:195], v[16:19], v[44:47]\n"
        "ds_read_b128 v[16:19], v0 offset:34816\n"
        "v_mfma_f32_16x16x32_bf16 v[36:39], v[196:199], v[20:23], v[36:39]\n"
        "v_mfma_f32_16x16x32_bf16 v[40:43], v[200:203], v[20:23], v[40:43]\n"
        "v_mfma_f32_16x16x32_bf16 v[44:47], v[204:207], v[20:23], v[44:47]\n"
        "s_waitcnt lgkmcnt(1)\n"
        "ds_read_b128 v[20:23], v0 offset:35840\n"
        "v_mfma_f32_16x16x32_bf16 v[36:39], v[208:211], v[8:11], v[36:39]\n"
        "v_mfma_f32_16x16x32_bf16 v[40:43], v[212:215], v[8:11], v[40:43]\n"
        "s_waitcnt vmcnt(10)\n"
        "s_barrier\n"
        "v_add_u32 v0, s78, v6\n"
        "v_mfma_f32_16x16x32_bf16 v[44:47], v[216:219], v[8:11], v[44:47]\n"
        "ds_read_b128 v[8:11], v0 offset:0\n"
        "v_mfma_f32_16x16x32_bf16 v[36:39], v[220:223], v[12:15], v[36:39]\n"
        "v_mfma_f32_16x16x32_bf16 v[40:43], v[224:227], v[12:15], v[40:43]\n"
        "v_mfma_f32_16x16x32_bf16 v[44:47], v[228:231], v[12:15], v[44:47]\n"
        "s_waitcnt lgkmcnt(1)\n"
        "ds_read_b128 v[12:15], v0 offset:1024\n"
        "v_mfma_f32_16x16x32_bf16 v[36:39], v[232:235], v[16:19], v[36:39]\n"
        "v_mfma_f32_16x16x32_bf16 v[40:43], v[236:239], v[16:19], v[40:43]\n"
        "v_mfma_f32_16x16x32_bf16 v[44:47], v[240:243], v[16:19], v[44:47]\n"
        "ds_read_b128 v[16:19], v0 offset:2048\n"
        "v_mfma_f32_16x16x32_bf16 v[36:39], v[244:247], v[20:23], v[36:39]\n"
        "v_mfma_f32_16x16x32_bf16 v[40:43], v[248:251], v[20:23], v[40:43]\n"
        "v_mfma_f32_16x16x32_bf16 v[44:47], v[252:255], v[20:23], v[44:47]\n"
        "s_waitcnt lgkmcnt(1)\n"
        "ds_read_b128 v[20:23], v0 offset:3072\n"
        "s_branch .Lf2_i0_%=\n"
        ".Lf2_i10_%=:\n"
        "v_mfma_f32_16x16x32_bf16 v[44:47], a[140:143], v[20:23], v[44:47]\n"
        "s_waitcnt lgkmcnt(1)\n"
        "ds_read_b128 v[20:23], v0 offset:15360\n"
        "v_mfma_f32_16x16x32_bf16 v[36:39], a[144:147], v[8:11], v[36:39]\n"
        "buffer_load_dword v4, v2, s[40:43], s86 offen sc1\n"
        "v_mfma_f32_16x16x32_bf16 v[40:43], a[148:151], v[8:11], v[40:43]\n"
        "v_mfma_f32_16x16x32_bf16 v[44:47], a[152:155], v[8:11], v[44:47]\n"
        "ds_read_b128 v[8:11], v0 offset:16384\n"
        "v_mfma_f32_16x16x32_bf16 v[36:39], a[156:159], v[12:15], v[36:39]\n"
        "v_mfma_f32_16x16x32_bf16 v[40:43], a[160:163], v[12:15], v[40:43]\n"
        "s_getreg_b32 s88, hwreg(HW_REG_IB_STS, 0, 4)\n"
        "v_mfma_f32_16x16x32_bf16 v[44:47], a[164:167], v[12:15], v[44:47]\n"
        "s_waitcnt lgkmcnt(1)\n"
        "ds_read_b128 v[12:15], v0 offset:17408\n"
        "v_mfma_f32_16x16x32_bf16 v[36:39], a[168:171], v[16:19], v[36:39]\n"
        "s_cmp_le_u32 s88, 2\n"
        "s_cselect_b32 s88, s73, s89\n"
        "s_cselect_b32 s73, s89, s73\n"
        "v_mfma_f32_16x16x32_bf16 v[40:43], a[172:175], v[16:19], v[40:43]\n"
        "buffer_store_dword v5, v3, s[40:43], s88 offen sc1\n"
        "v_mfma_f32_16x16x32_bf16 v[44:47], a[176:179], v[16:19], v[44:47]\n"
        "ds_read_b128 v[16:19], v0 offset:18432\n"
        "v_mfma_f32_16x16x32_bf16 v[36:39], a[180:183], v[20:23], v[36:39]\n"
        "v_mfma_f32_16x16x32_bf16 v[40:43], a[184:187], v[20:23], v[40:43]\n"
        "v_mfma_f32_16x16x32_bf16 v[44:47], a[188:191], v[20:23], v[44:47]\n"
        "s_waitcnt lgkmcnt(1)\n"
        "ds_read_b128 v[20:23], v0 offset:19456\n"
        "v_mfma_f32_16x16x32_bf16 v[36:39], a[192:195], v[8:11], v[36:39]\n"
        "s_getreg_b32 s88, hwreg(HW_REG_IB_STS, 0, 4)\n"
        "v_mfma_f32_16x16x32_bf16 v[40:43], a[196:199], v[8:11], v[40:43]\n"
        "s_cmp_le_u32 s88, 3\n"
        "s_cselect_b32 s88, s73, s89\n"
        "s_cselect_b32 s73, s89, s73\n"
        "v_mfma_f32_16x16x32_bf16 v[44:47], a[200:203], v[8:11], v[44:47]\n"
        "ds_read_b128 v[8:11], v0 offset:20480\n"
        "v_mfma_f32_16x16x32_bf16 v[36:39], a[204:207], v[12:15], v[36:39]\n"
        "buffer_store_dword v5, v3, s[40:43], s88 offen sc1\n"
        "v_mfma_f32_16x16x32_bf16 v[40:43], a[208:211], v[12:15], v[40:43]\n"
        "v_mfma_f32_16x16x32_bf16 v[44:47], a[212:215], v[12:15], v[44:47]\n"
        "s_waitcnt lgkmcnt(1)\n"
        "ds_read_b128 v[12:15], v0 offset:21504\n"
        "v_mfma_f32_16x16x32_bf16 v[36:39], a[216:219], v[16:19], v[36:39]\n"
        "v_mfma_f32_16x16x32_bf16 v[40:43], a[220:223], v[16:19], v[40:43]\n"
        "v_mfma_f32_16x16x32_bf16 v[44:47], a[224:227], v[16:19], v[44:47]\n"
        "s_getreg_b32 s88, hwreg(HW_REG_IB_STS, 0, 4)\n"
        "ds_read_b128 v[16:19], v0 offset:22528\n"
        "v_mfma_f32_16x16x32_bf16 v[36:39], a[228:231], v[20:23], v[36:39]\n"
        "s_cmp_le_u32 s88, 4\n"
        "s_cselect_b32 s88, s73, s89\n"
        "s_cselect_b32 s73, s89, s73\n"
        "v_mfma_f32_16x16x32_bf16 v[40:43], a[232:235], v[20:23], v[40:43]\n"
        "buffer_store_dword v5, v3, s[40:43], s88 offen sc1\n"
        "v_mfma_f32_16x16x32_bf16 v[44:47], a[236:239], v[20:23], v[44:47]\n"
        "s_waitcnt lgkmcnt(1)\n"
        "ds_read_b128 v[20:23], v0 offset:23552\n"
        "v_mfma_f32_16x16x32_bf16 v[36:39], a[240:243], v[8:11], v[36:39]\n"
        "v_mfma_f32_16x16x32_bf16 v[40:43], a[244:247], v[8:11], v[40:43]\n"
        "s_waitcnt vmcnt(3)\n"
        "v_cmp_gt_i32 vcc, s70, v4\n"
        "v_mfma_f32_16x16x32_bf16 v[44:47], a[248:251], v[8:11], v[44:47]\n"
        "ds_read_b128 v[8:11], v0 offset:24576\n"
        "v_mfma_f32_16x16x32_bf16 v[36:39], a[252:255], v[12:15], v[36:39]\n"
        "s_and_b64 vcc, vcc, 63\n"
        "v_mfma_f32_16x16x32_bf16 v[40:43], v[80:83], v[12:15], v[40:43]\n"
        "s_cbranch_vccnz .Lf2_i11_%=\n"
        "v_mfma_f32_16x16x32_bf16 v[44:47], v[84:87], v[12:15], v[44:47]\n"
        "s_waitcnt lgkmcnt(1)\n"
        "ds_read_b128 v[12:15], v0 offset:25600\n"
        "v_mfma_f32_16x16x32_bf16 v[36:39], v[88:91], v[16:19], v[36:39]\n"
        "buffer_load_dwordx4 v1, s[40:43], s71 offen offset:0 sc1 lds\n"
        "v_mfma_f32_16x16x32_bf16 v[40:43], v[92:95], v[16:19], v[40:43]\n"
        "v_mfma_f32_16x16x32_bf16 v[44:47], v[96:99], v[16:19], v[44:47]\n"
        "ds_read_b128 v[16:19], v0 offset:26624\n"
        "v_mfma_f32_16x16x32_bf16 v[36:39], v[100:103], v[20:23], v[36:39]\n"
        "buffer_load_dwordx4 v1, s[40:43], s71 offen offset:1024 sc1 lds\n"
        "v_mfma_f32_16x16x32_bf16 v[40:43], v[104:107], v[20:23], v[40:43]\n"
        "s_add_u32 m0, m0, s85\n"
        "s_add_u32 s71, s71, 16384\n"
        "v_mfma_f32_16x16x32_bf16 v[44:47], v[108:111], v[20:23], v[44:47]\n"
        "s_waitcnt lgkmcnt(1)\n"
        "ds_read_b128 v[20:23], v0 offset:27648\n"
        "v_mfma_f32_16x16x32_bf16 v[36:39], v[112:115], v[8:11], v[36:39]\n"
        "buffer_load_dwordx4 v1, s[40:43], s81 offen offset:0 sc1 lds\n"
        "v_mfma_f32_16x16x32_bf16 v[40:43], v[116:119], v[8:11], v[40:43]\n"
        "s_sub_u32 m0, m0, s85\n"
        "s_add_u32 m0, m0, 12288\n"
        "s_add_u32 s81, s81, 16384\n"
        "v_mfma_f32_16x16x32_bf16 v[44:47], v[120:123], v[8:11], v[44:47]\n"
        "ds_read_b128 v[8:11], v0 offset:28672\n"
        "v_mfma_f32_16x16x32_bf16 v[36:39], v[124:127], v[12:15], v[36:39]\n"
        "buffer_load_dwordx4 v1, s[40:43], s71 offen offset:0 sc1 lds\n"
        "v_mfma_f32_16x16x32_bf16 v[40:43], v[128:131], v[12:15], v[40:43]\n"
        "buffer_store_dword v5, v3, s[40:43], s73 offen sc1\n"
        "v_mfma_f32_16x16x32_bf16 v[44:47], v[132:135], v[12:15], v[44:47]\n"
        "s_waitcnt lgkmcnt(1)\n"
        "ds_read_b128 v[12:15], v0 offset:29696\n"
        "s_mov_b32 s84, s89\n"
        "v_mfma_f32_16x16x32_bf16 v[36:39], v[136:139], v[16:19], v[36:39]\n"
        "s_mov_b32 s73, s60\n"
        "buffer_load_dwordx4 v1, s[40:43], s71 offen offset:1024 sc1 lds\n"
        "v_mfma_f32_16x16x32_bf16 v[40:43], v[140:143], v[16:19], v[40:43]\n"
        "s_add_u32 m0, m0, s85\n"
        "s_add_u32 s71, s71, 16384\n"
        "s_add_u32 s74, s74, 1\n"
        "v_mfma_f32_16x16x32_bf16 v[44:47], v[144:147], v[16:19], v[44:47]\n"
        "s_add_u32 s72, s75, s59\n"
        "ds_read_b128 v[16:19], v0 offset:30720\n"
        "s_add_u32 s72, s72, 147456\n"
        "v_mfma_f32_16x16x32_bf16 v[36:39], v[148:151], v[20:23], v[36:39]\n"
        "v_mfma_f32_16x16x32_bf16 v[40:43], v[152:155], v[20:23], v[40:43]\n"
        "v_mfma_f32_16x16x32_bf16 v[44:47], v[156:159], v[20:23], v[44:47]\n"
        "s_waitcnt lgkmcnt(1)\n"
        "ds_read_b128 v[20:23], v0 offset:31744\n"
        "v_mfma_f32_16x16x32_bf16 v[36:39], v[160:163], v[8:11], v[36:39]\n"
        "v_mfma_f32_16x16x32_bf16 v[40:43], v[164:167], v[8:11], v[40:43]\n"
        "buffer_load_dwordx4 v1, s[40:43], s81 offen offset:0 sc1 lds\n"
        "v_mfma_f32_16x16x32_bf16 v[44:47], v[168:171], v[8:11], v[44:47]\n"
        "s_sub_u32 m0, m0, s85\n"
        "ds_read_b128 v[8:11], v0 offset:32768\n"
        "s_add_u32 m0, m0, 12288\n"
        "v_mfma_f32_16x16x32_bf16 v[36:39], v[172:175], v[12:15], v[36:39]\n"
        "s_add_u32 s81, s81, 16384\n"
        "v_mfma_f32_16x16x32_bf16 v[40:43], v[176:179], v[12:15], v[40:43]\n"
        "v_mfma_f32_16x16x32_bf16 v[44:47], v[180:183], v[12:15], v[44:47]\n"
        "s_waitcnt lgkmcnt(1)\n"
        "ds_read_b128 v[12:15], v0 offset:33792\n"
        "v_mfma_f32_16x16x32_bf16 v[36:39], v[184:187], v[16:19], v[36:39]\n"
        "buffer_load_dwordx4 v1, s[40:43], s71 offen offset:0 sc1 lds\n"
        "v_mfma_f32_16x16x32_bf16 v[40:43], v[188:191], v[16:19], v[40:43]\n"
        "v_mfma_f32_16x16x32_bf16 v[44:47], v[192:195], v[16:19], v[44:47]\n"
        "ds_read_b128 v[16:19], v0 offset:34816\n"
        "v_mfma_f32_16x16x32_bf16 v[36:39], v[196:199], v[20:23], v[36:39]\n"
        "buffer_load_dwordx4 v1, s[40:43], s71 offen offset:1024 sc1 lds\n"
        "v_mfma_f32_16x16x32_bf16 v[40:43], v[200:203], v[20:23], v[40:43]\n"
        "s_add_u32 m0, m0, s85\n"
        "s_add_u32 s71, s71, 16384\n"
        "v_mfma_f32_16x16x32_bf16 v[44:47], v[204:207], v[20:23], v[44:47]\n"
        "s_waitcnt lgkmcnt(1)\n"
        "ds_read_b128 v[20:23], v0 offset:35840\n"
        "v_mfma_f32_16x16x32_bf16 v[36:39], v[208:211], v[8:11], v[36:39]\n"
        "v_mfma_f32_16x16x32_bf16 v[40:43], v[212:215], v[8:11], v[40:43]\n"
        "s_waitcnt vmcnt(6)\n"
        "s_barrier\n"
        "v_add_u32 v0, s78, v6\n"
        "v_mfma_f32_16x16x32_bf16 v[44:47], v[216:219], v[8:11], v[44:47]\n"
        "ds_read_b128 v[8:11], v0 offset:0\n"
        "v_mfma_f32_16x16x32_bf16 v[36:39], v[220:223], v[12:15], v[36:39]\n"
        "buffer_load_dwordx4 v1, s[40:43], s81 offen offset:0 sc1 lds\n"
        "v_mfma_f32_16x16x32_bf16 v[40:43], v[224:227], v[12:15], v[40:43]\n"
        "s_add_u32 s69, s69, 1\n"
        "v_mfma_f32_16x16x32_bf16 v[44:47], v[228:231], v[12:15], v[44:47]\n"
        "s_waitcnt lgkmcnt(1)\n"
        "ds_read_b128 v[12:15], v0 offset:1024\n"
        "v_mfma_f32_16x16x32_bf16 v[36:39], v[232:235], v[16:19], v[36:39]\n"
        "v_mfma_f32_16x16x32_bf16 v[40:43], v[236:239], v[16:19], v[40:43]\n"
        "v_mfma_f32_16x16x32_bf16 v[44:47], v[240:243], v[16:19], v[44:47]\n"
        "ds_read_b128 v[16:19], v0 offset:2048\n"
        "v_mfma_f32_16x16x32_bf16 v[36:39], v[244:247], v[20:23], v[36:39]\n"
        "v_mfma_f32_16x16x32_bf16 v[40:43], v[248:251], v[20:23], v[40:43]\n"
        "v_mfma_f32_16x16x32_bf16 v[44:47], v[252:255], v[20:23], v[44:47]\n"
        "s_waitcnt lgkmcnt(1)\n"
        "ds_read_b128 v[20:23], v0 offset:3072\n"
        "s_branch .Lf2_i0_%=\n"
        ".Lf2_i11_%=:\n"
        "v_mfma_f32_16x16x32_bf16 v[44:47], v[84:87], v[12:15], v[44:47]\n"
        "s_waitcnt lgkmcnt(1)\n"
        "ds_read_b128 v[12:15], v0 offset:25600\n"
        "v_mfma_f32_16x16x32_bf16 v[36:39], v[88:91], v[16:19], v[36:39]\n"
        "buffer_load_dword v4, v2, s[40:43], s86 offen sc1\n"
        "v_mfma_f32_16x16x32_bf16 v[40:43], v[92:95], v[16:19], v[40:43]\n"
        "v_mfma_f32_16x16x32_bf16 v[44:47], v[96:99], v[16:19], v[44:47]\n"
        "ds_read_b128 v[16:19], v0 offset:26624\n"
        "v_mfma_f32_16x16x32_bf16 v[36:39], v[100:103], v[20:23], v[36:39]\n"
        "v_mfma_f32_16x16x32_bf16 v[40:43], v[104:107], v[20:23], v[40:43]\n"
        "buffer_store_dword v5, v3, s[40:43], s73 offen sc1\n"
        "v_mfma_f32_16x16x32_bf16 v[44:47], v[108:111], v[20:23], v[44:47]\n"
        "s_waitcnt lgkmcnt(1)\n"
        "ds_read_b128 v[20:23], v0 offset:27648\n"
        "s_mov_b32 s84, s89\n"
        "v_mfma_f32_16x16x32_bf16 v[36:39], v[112:115], v[8:11], v[36:39]\n"
        "s_mov_b32 s73, s60\n"
        "s_add_u32 s74, s74, 1\n"
        "s_add_u32 s72, s75, s59\n"
        "v_mfma_f32_16x16x32_bf16 v[40:43], v[116:119], v[8:11], v[40:43]\n"
        "s_add_u32 s72, s72, 147456\n"
        "v_mfma_f32_16x16x32_bf16 v[44:47], v[120:123], v[8:11], v[44:47]\n"
        "ds_read_b128 v[8:11], v0 offset:28672\n"
        "v_mfma_f32_16x16x32_bf16 v[36:39], v[124:127], v[12:15], v[36:39]\n"
        "v_mfma_f32_16x16x32_bf16 v[40:43], v[128:131], v[12:15], v[40:43]\n"
        "v_mfma_f32_16x16x32_bf16 v[44:47], v[132:135], v[12:15], v[44:47]\n"
        "s_waitcnt lgkmcnt(1)\n"
        "ds_read_b128 v[12:15], v0 offset:29696\n"
        "v_mfma_f32_16x16x32_bf16 v[36:39], v[136:139], v[16:19], v[36:39]\n"
        "v_mfma_f32_16x16x32_bf16 v[40:43], v[140:143], v[16:19], v[40:43]\n"
        "v_mfma_f32_16x16x32_bf16 v[44:47], v[144:147], v[16:19], v[44:47]\n"
        "ds_read_b128 v[16:19], v0 offset:30720\n"
        "v_mfma_f32_16x16x32_bf16 v[36:39], v[148:151], v[20:23], v[36:39]\n"
        "v_mfma_f32_16x16x32_bf16 v[40:43], v[152:155], v[20:23], v[40:43]\n"
        "v_mfma_f32_16x16x32_bf16 v[44:47], v[156:159], v[20:23], v[44:47]\n"
        "s_waitcnt lgkmcnt(1)\n"
        "ds_read_b128 v[20:23], v0 offset:31744\n"
        "v_mfma_f32_16x16x32_bf16 v[36:39], v[160:163], v[8:11], v[36:39]\n"
        "v_mfma_f32_16x16x32_bf16 v[40:43], v[164:167], v[8:11], v[40:43]\n"
        "v_mfma_f32_16x16x32_bf16 v[44:47], v[168:171], v[8:11], v[44:47]\n"
        "ds_read_b128 v[8:11], v0 offset:32768\n"
        "v_mfma_f32_16x16x32_bf16 v[36:39], v[172:175], v[12:15], v[36:39]\n"
        "v_mfma_f32_16x16x32_bf16 v[40:43], v[176:179], v[12:15], v[40:43]\n"
        "v_mfma_f32_16x16x32_bf16 v[44:47], v[180:183], v[12:15], v[44:47]\n"
        "s_waitcnt lgkmcnt(1)\n"
        "ds_read_b128 v[12:15], v0 offset:33792\n"
        "v_mfma_f32_16x16x32_bf16 v[36:39], v[184:187], v[16:19], v[36:39]\n"
        "v_mfma_f32_16x16x32_bf16 v[40:43], v[188:191], v[16:19], v[40:43]\n"
        "v_mfma_f32_16x16x32_bf16 v[44:47], v[192:195], v[16:19], v[44:47]\n"
        "ds_read_b128 v[16:19], v0 offset:34816\n"
        "v_mfma_f32_16x16x32_bf16 v[36:39], v[196:199], v[20:23], v[36:39]\n"
        "s_waitcnt vmcnt(1)\n"
        "v_cmp_gt_i32 vcc, s70, v4\n"
        "v_mfma_f32_16x16x32_bf16 v[40:43], v[200:203], v[20:23], v[40:43]\n"
        "v_mfma_f32_16x16x32_bf16 v[44:47], v[204:207], v[20:23], v[44:47]\n"
        "s_waitcnt lgkmcnt(1)\n"
        "ds_read_b128 v[20:23], v0 offset:35840\n"
        "s_and_b64 vcc, vcc, 63\n"
        "v_mfma_f32_16x16x32_bf16 v[36:39], v[208:211], v[8:11], v[36:39]\n"
        "s_cbranch_vccnz .Lf2_i12_%=\n"
        "v_mfma_f32_16x16x32_bf16 v[40:43], v[212:215], v[8:11], v[40:43]\n"
        "buffer_load_dwordx4 v1, s[40:43], s71 offen offset:0 sc1 lds\n"
        "v_mfma_f32_16x16x32_bf16 v[44:47], v[216:219], v[8:11], v[44:47]\n"
        "v_mfma_f32_16x16x32_bf16 v[36:39], v[220:223], v[12:15], v[36:39]\n"
        "buffer_load_dwordx4 v1, s[40:43], s71 offen offset:1024 sc1 lds\n"
        "v_mfma_f32_16x16x32_bf16 v[40:43], v[224:227], v[12:15], v[40:43]\n"
        "s_add_u32 m0, m0, s85\n"
        "s_add_u32 s71, s71, 16384\n"
        "v_mfma_f32_16x16x32_bf16 v[44:47], v[228:231], v[12:15], v[44:47]\n"
        "s_waitcnt lgkmcnt(0)\n"
        "buffer_load_dwordx4 v1, s[40:43], s81 offen offset:0 sc1 lds\n"
        "v_mfma_f32_16x16x32_bf16 v[36:39], v[232:235], v[16:19], v[36:39]\n"
        "s_sub_u32 m0, m0, s85\n"
        "s_add_u32 m0, m0, 12288\n"
        "s_add_u32 s81, s81, 16384\n"
        "v_mfma_f32_16x16x32_bf16 v[40:43], v[236:239], v[16:19], v[40:43]\n"
        "buffer_load_dwordx4 v1, s[40:43], s71 offen offset:0 sc1 lds\n"
        "v_mfma_f32_16x16x32_bf16 v[44:47], v[240:243], v[16:19], v[44:47]\n"
        "v_mfma_f32_16x16x32_bf16 v[36:39], v[244:247], v[20:23], v[36:39]\n"
        "buffer_load_dwordx4 v1, s[40:43], s71 offen offset:1024 sc1 lds\n"
        "v_mfma_f32_16x16x32_bf16 v[40:43], v[248:251], v[20:23], v[40:43]\n"
        "s_add_u32 m0, m0, s85\n"
        "s_add_u32 s71, s71, 16384\n"
        "v_mfma_f32_16x16x32_bf16 v[44:47], v[252:255], v[20:23], v[44:47]\n"
        "buffer_load_dwordx4 v1, s[40:43], s81 offen offset:0 sc1 lds\n"
        "s_sub_u32 m0, m0, s85\n"
        "s_add_u32 m0, m0, 12288\n"
        "s_add_u32 s81, s81, 16384\n"
        "buffer_load_dwordx4 v1, s[40:43], s71 offen offset:0 sc1 lds\n"
        "buffer_load_dwordx4 v1, s[40:43], s71 offen offset:1024 sc1 lds\n"
        "s_add_u32 m0, m0, s85\n"
        "s_add_u32 s71, s71, 16384\n"
        "buffer_load_dwordx4 v1, s[40:43], s81 offen offset:0 sc1 lds\n"
        "s_add_u32 s69, s69, 1\n"
        "s_nop 2\n"
        "v_cvt_pk_bf16_f32 v36, v36, v37\n"
        "v_cvt_pk_bf16_f32 v37, v38, v39\n"
        "v_cvt_pk_bf16_f32 v38, v40, v41\n"
        "v_cvt_pk_bf16_f32 v39, v42, v43\n"
        "buffer_store_dwordx4 v[36:39], v48, s[40:43], s72 offen offset:0 sc1\n"
        "v_cvt_pk_bf16_f32 v44, v44, v45\n"
        "v_cvt_pk_bf16_f32 v45, v46, v47\n"
        "s_nop 0\n"
        "v_permlane16_swap_b32 v44, v46\n"
        "v_permlane16_swap_b32 v45, v47\n"
        "buffer_store_dwordx4 v[44:47], v7, s[40:43], s72 offen offset:0 sc1\n"
        "v_mov_b32 v5, s74\n"
        "s_mov_b32 s84, s60\n"
        "s_waitcnt vmcnt(8)\n"
        "s_branch .Lf2_go1_%=\n"
        ".Lf2_i12_%=:\n"
        "v_mfma_f32_16x16x32_bf16 v[40:43], v[212:215], v[8:11], v[40:43]\n"
        "buffer_load_dword v4, v2, s[40:43], s86 offen sc1\n"
        "v_mfma_f32_16x16x32_bf16 v[44:47], v[216:219], v[8:11], v[44:47]\n"
        "v_mfma_f32_16x16x32_bf16 v[36:39], v[220:223], v[12:15], v[36:39]\n"
        "v_mfma_f32_16x16x32_bf16 v[40:43], v[224:227], v[12:15], v[40:43]\n"
        "v_mfma_f32_16x16x32_bf16 v[44:47], v[228:231], v[12:15], v[44:47]\n"
        "s_waitcnt lgkmcnt(0)\n"
        "v_mfma_f32_16x16x32_bf16 v[36:39], v[232:235], v[16:19], v[36:39]\n"
        "v_mfma_f32_16x16x32_bf16 v[40:43], v[236:239], v[16:19], v[40:43]\n"
        "v_mfma_f32_16x16x32_bf16 v[44:47], v[240:243], v[16:19], v[44:47]\n"
        "v_mfma_f32_16x16x32_bf16 v[36:39], v[244:247], v[20:23], v[36:39]\n"
        "v_mfma_f32_16x16x32_bf16 v[40:43], v[248:251], v[20:23], v[40:43]\n"
        "v_mfma_f32_16x16x32_bf16 v[44:47], v[252:255], v[20:23], v[44:47]\n"
        "s_nop 2\n"
        "v_cvt_pk_bf16_f32 v36, v36, v37\n"
        "v_cvt_pk_bf16_f32 v37, v38, v39\n"
        "v_cvt_pk_bf16_f32 v38, v40, v41\n"
        "v_cvt_pk_bf16_f32 v39, v42, v43\n"
        "buffer_store_dwordx4 v[36:39], v48, s[40:43], s72 offen offset:0 sc1\n"
        "v_cvt_pk_bf16_f32 v44, v44, v45\n"
        "v_cvt_pk_bf16_f32 v45, v46, v47\n"
        "s_nop 0\n"
        "v_permlane16_swap_b32 v44, v46\n"
        "v_permlane16_swap_b32 v45, v47\n"
        "buffer_store_dwordx4 v[44:47], v7, s[40:43], s72 offen offset:0 sc1\n"
        "s_waitcnt vmcnt(2)\n"
        "v_cmp_gt_i32 vcc, s70, v4\n"
        "s_nop 0\n"
        "s_and_b64 vcc, vcc, 63\n"
        "s_cbranch_vccnz .Lf2_late1_%=\n"
        "buffer_load_dwordx4 v1, s[40:43], s71 offen offset:0 sc1 lds\n"
        "buffer_load_dwordx4 v1, s[40:43], s71 offen offset:1024 sc1 lds\n"
        "s_add_u32 m0, m0, s85\n"
        "s_add_u32 s71, s71, 16384\n"
        "buffer_load_dwordx4 v1, s[40:43], s81 offen offset:0 sc1 lds\n"
        "s_sub_u32 m0, m0, s85\n"
        "s_add_u32 m0, m0, 12288\n"
        "s_add_u32 s81, s81, 16384\n"
        "buffer_load_dwordx4 v1, s[40:43], s71 offen offset:0 sc1 lds\n"
        "buffer_load_dwordx4 v1, s[40:43], s71 offen offset:1024 sc1 lds\n"
        "s_add_u32 m0, m0, s85\n"
        "s_add_u32 s71, s71, 16384\n"
        "buffer_load_dwordx4 v1, s[40:43], s81 offen offset:0 sc1 lds\n"
        "s_sub_u32 m0, m0, s85\n"
        "s_add_u32 m0, m0, 12288\n"
        "s_add_u32 s81, s81, 16384\n"
        "buffer_load_dwordx4 v1, s[40:43], s71 offen offset:0 sc1 lds\n"
        "buffer_load_dwordx4 v1, s[40:43], s71 offen offset:1024 sc1 lds\n"
        "s_add_u32 m0, m0, s85\n"
        "s_add_u32 s71, s71, 16384\n"
        "buffer_load_dwordx4 v1, s[40:43], s81 offen offset:0 sc1 lds\n"
        "s_add_u32 s69, s69, 1\n"
        "s_waitcnt vmcnt(9)\n"
        "v_mov_b32 v5, s74\n"
        "buffer_store_dword v5, v3, s[40:43], s73 offen sc1\n"
        "s_waitcnt vmcnt(7)\n"
        "s_branch .Lf2_go1_%=\n"
        ".Lf2_late1_%=:\n"
        "s_waitcnt vmcnt(0)\n"
        "v_mov_b32 v5, s74\n"
        "buffer_store_dword v5, v3, s[40:43], s73 offen sc1\n"
        "s_cmp_eq_u32 s69, 128\n"
        "s_cbranch_scc0 .Lf2_wait1_%=\n"
        "s_endpgm\n"
        ".Lf2_wait1_%=:\n"
        "s_mov_b32 s64, 0\n"
        ".Lf2_in1_top_%=:\n"
        "buffer_load_dword v4, v2, s[40:43], s86 offen sc1\n"
        "s_waitcnt vmcnt(0)\n"
        "v_cmp_gt_i32 vcc, s70, v4\n"
        "s_nop 0\n"
        "s_and_b64 vcc, vcc, 63\n"
        "s_cbranch_vccz .Lf2_in1_ok_%=\n"
        "s_add_u32 s64, s64, 1\n"
        "s_and_b32 s65, s64, 255\n"
        "s_cbranch_scc1 .Lf2_in1_go_%=\n"
        "s_mov_b32 s65, 49152\n"
        "s_cmp_lt_u32 s64, 1048576\n"
        "s_cbranch_scc1 .Lf2_in1_chk_%=\n"
        "v_mov_b32 v4, 1\n"
        "buffer_store_dword v4, off, s[40:43], s65 sc1\n"
        "s_waitcnt vmcnt(0)\n"
        ".Lf2_in1_chk_%=:\n"
        "buffer_load_dword v4, off, s[40:43], s65 sc1\n"
        "s_waitcnt vmcnt(0)\n"
        "v_readfirstlane_b32 s65, v4\n"
        "s_nop 0\n"
        "s_cmp_lg_u32 s65, 1\n"
        "s_cbranch_scc1 .Lf2_in1_go_%=\n"
        "s_endpgm\n"
        ".Lf2_in1_go_%=:\n"
        "s_branch .Lf2_in1_top_%=\n"
        ".Lf2_in1_ok_%=:\n"
        "buffer_load_dwordx4 v1, s[40:43], s71 offen offset:0 sc1 lds\n"
        "buffer_load_dwordx4 v1, s[40:43], s71 offen offset:1024 sc1 lds\n"
        "s_add_u32 m0, m0, s85\n"
        "s_add_u32 s71, s71, 16384\n"
        "buffer_load_dwordx4 v1, s[40:43], s81 offen offset:0 sc1 lds\n"
        "s_sub_u32 m0, m0, s85\n"
        "s_add_u32 m0, m0, 12288\n"
        "s_add_u32 s81, s81, 16384\n"
        "buffer_load_dwordx4 v1, s[40:43], s71 offen offset:0 sc1 lds\n"
        "buffer_load_dwordx4 v1, s[40:43], s71 offen offset:1024 sc1 lds\n"
        "s_add_u32 m0, m0, s85\n"
        "s_add_u32 s71, s71, 16384\n"
        "buffer_load_dwordx4 v1, s[40:43], s81 offen offset:0 sc1 lds\n"
        "s_sub_u32 m0, m0, s85\n"
        "s_add_u32 m0, m0, 12288\n"
        "s_add_u32 s81, s81, 16384\n"
        "buffer_load_dwordx4 v1, s[40:43], s71 offen offset:0 sc1 lds\n"
        "buffer_load_dwordx4 v1, s[40:43], s71 offen offset:1024 sc1 lds\n"
        "s_add_u32 m0, m0, s85\n"
        "s_add_u32 s71, s71, 16384\n"
        "buffer_load_dwordx4 v1, s[40:43], s81 offen offset:0 sc1 lds\n"
        "s_add_u32 s69, s69, 1\n"
        "s_waitcnt vmcnt(6)\n"
        ".Lf2_go1_%=:\n"
        "s_mov_b32 s72, 241172480\n"
        "s_mov_b32 s83, 241172480\n"
        "s_mov_b32 s73, 241172480\n"
        "v_add_u32 v0, s78, v6\n"
        "s_barrier\n"
        "ds_read_b128 v[8:11], v0 offset:0\n"
        "ds_read_b128 v[12:15], v0 offset:1024\n"
        "ds_read_b128 v[16:19], v0 offset:2048\n"
        "ds_read_b128 v[20:23], v0 offset:3072\n"
        "s_waitcnt lgkmcnt(2)\n"
        "s_branch .Lf2_i0_%=\n"
        ".Lf2_end_%=:\n"
            :: "s"(p.ws), "s"(p.w2), "s"(p.w2), "s"(p.w2), "s"(pair(w, delay)), "s"(pair(x4, pr)),
               "s"(pair(hb + w * 2048, 97 * PAGE + spread(C_P5))), "s"(pair(flag(F_F2 + fl), (w * 2048) | (8192 - w * 1024) << 16)), "s"(pair(hb + 6 * 16384 + w * 1024, 0)),
               "v"(pair(lane * 16, lane * 16)), "v"(pair(lane < 6 ? flag(F_F1 + 128 * nu + 24 * kap + 8 * (lane >> 1) + 2 * w + (lane & 1)) : OOB, fo4)), K_ARGS
            : "v0", "v1", "v2", "v3", "v4", "v5", "v6", "v7", "v8", "v9", "v10", "v11", "v12", "v13", "v14", "v15", "v16", "v17", "v18", "v19", "v20", "v21", "v22", "v23", "v24", "v25", "v26", "v27", "v28", "v29", "v30", "v31", "v32", "v33", "v34", "v35", "v36", "v37", "v38", "v39", "v40", "v41", "v42", "v43", "v44", "v45", "v46", "v47", "v48", "v49", "v50", "v51", "v52", "v53", "v54", "v55", "v56", "v57", "v58", "v59", "v60", "v61", "v62", "v63", "v64", "v65", "v66", "v67", "v68", "v69", "v70", "v71", "v72", "v73", "v74", "v75", "v76", "v77", "v78", "v79", "v255", "a255", "s26", "s27", "s28", "s29", "s30", "s31", "s32", "s33", "s34", "s35", "s36", "s37", "s38", "s39", "s40", "s41", "s42", "s43", "s44", "s45", "s46", "s47", "s48", "s49", "s50", "s51", "s52", "s53", "s54", "s55", "s56", "s57", "s58", "s59", "s60", "s61", "s62", "s63", "s64", "s65", "s66", "s67", "s68", "s69", "s70", "s71", "s72", "s73", "s74", "s75", "s76", "s77", "s78", "s79", "s80", "s81", "s82", "s83", "s84", "s85", "s86", "s87", "s88", "s89", "s90", "s91", "s92", "s93", "s94", "s95", "s96", "s97", "s98", "s99", "vcc", "scc", "memory");
    }
    __builtin_unreachable();
}

__device__ __forceinline__ void role_attn(const Params& p, int h0, int x4, int pr) {
    const unsigned lane = threadIdx.x & 63, i = lane & 15, g = lane >> 4;
    const int w = rfl(threadIdx.x >> 6), h = h0 + w;
    asm volatile(
        "s_nop 4\n"
        "s_mov_b64 s[40:41], %0\n"
        "s_and_b32 s41, s41, 0xffff\n"
        "s_mov_b32 s42, 236978176\n"
        "s_mov_b32 s43, 0x20000\n"
        "s_mov_b64 s[56:57], %1\n"
        "s_mov_b64 s[58:59], %2\n"
        "s_mov_b64 s[60:61], %3\n"
        "v_mov_b64 v[0:1], %4\n"
        "v_mov_b64 v[2:3], %5\n"
        "v_mov_b64 v[4:5], %6\n"
        "v_cmp_ge_i32 s[76:77], 0, v5\n"
        "v_cmp_ge_i32 s[78:79], -1, v5\n"
        "v_cmp_ge_i32 s[80:81], -2, v5\n"
        "v_cmp_ge_i32 s[82:83], -3, v5\n"
        "s_mov_b32 s84, 0\n"
        "s_mov_b32 s85, -1\n"
        "s_mov_b32 s75, 0x3e0293ee\n"
        "s_getpc_b64 s[44:45]\n"
        ".Lat_pc_%=:\n"
        "s_add_u32 s44, s44, .Lat_code_%=-.Lat_pc_%=\n"
        "s_addc_u32 s45, s45, 0\n"
        "s_and_b32 s45, s45, 0xffff\n"
        "s_and_b32 s44, s44, -4\n"
        "s_mov_b32 s46, .Lat_end_%=-.Lat_code_%=\n"
        "s_mov_b32 s47, 0x20000\n"
        "s_mov_b32 s65, 0\n"
        "buffer_load_dwordx4 v[24:27], v0, s[44:47], s65 offen\n"
        "s_add_u32 s65, s65, 1024\n"
        "buffer_load_dwordx4 v[28:31], v0, s[44:47], s65 offen\n"
        "s_add_u32 s65, s65, 1024\n"
        "buffer_load_dwordx4 v[32:35], v0, s[44:47], s65 offen\n"
        "s_add_u32 s65, s65, 1024\n"
        "buffer_load_dwordx4 v[36:39], v0, s[44:47], s65 offen\n"
        "s_add_u32 s65, s65, 1024\n"
        "buffer_load_dwordx4 v[40:43], v0, s[44:47], s65 offen\n"
        "s_add_u32 s65, s65, 1024\n"
        "v_lshrrev_b32 v12, 4, v0\n"
        "s_mov_b32 s50, s56\n"
        "v_lshlrev_b32 v14, 7, v12\n"
        "s_sub_u32 s66, 0, s50\n"
        "s_mov_b32 s52, 32768\n"
        "s_mov_b32 s64, 0\n"
        ".Lat_pg0_top_%=:\n"
        "buffer_load_dword v13, v14, s[40:43], s52 offen sc1\n"
        "s_waitcnt vmcnt(0)\n"
        "v_cmp_gt_i32 vcc, 0, v13\n"
        "s_cbranch_vccz .Lat_pg0_ok_%=\n"
        "s_add_u32 s64, s64, 1\n"
        "s_and_b32 s65, s64, 255\n"
        "s_cbranch_scc1 .Lat_pg0_go_%=\n"
        "s_mov_b32 s65, 49152\n"
        "s_cmp_lt_u32 s64, 1048576\n"
        "s_cbranch_scc1 .Lat_pg0_chk_%=\n"
        "v_mov_b32 v13, 1\n"
        "buffer_store_dword v13, off, s[40:43], s65 sc1\n"
        "s_waitcnt vmcnt(0)\n"
        ".Lat_pg0_chk_%=:\n"
        "buffer_load_dword v13, off, s[40:43], s65 sc1\n"
        "s_waitcnt vmcnt(0)\n"
        "v_readfirstlane_b32 s65, v13\n"
        "s_nop 0\n"
        "s_cmp_lg_u32 s65, 1\n"
        "s_cbranch_scc1 .Lat_pg0_go_%=\n"
        "s_endpgm\n"
        ".Lat_pg0_go_%=:\n"
        "s_sleep 2\n"
        "s_branch .Lat_pg0_top_%=\n"
        ".Lat_pg0_ok_%=:\n"
        "v_cmp_eq_u32 s[52:53], 1, v13\n"
        "s_nop 1\n"
        "s_mov_b32 s54, 40960\n"
        "s_mov_b32 s64, 0\n"
        ".Lat_pg1_top_%=:\n"
        "buffer_load_dword v13, v14, s[40:43], s54 offen sc1\n"
        "s_waitcnt vmcnt(0)\n"
        "v_cmp_gt_i32 vcc, 0, v13\n"
        "s_cbranch_vccz .Lat_pg1_ok_%=\n"
        "s_add_u32 s64, s64, 1\n"
        "s_and_b32 s65, s64, 255\n"
        "s_cbranch_scc1 .Lat_pg1_go_%=\n"
        "s_mov_b32 s65, 49152\n"
        "s_cmp_lt_u32 s64, 1048576\n"
        "s_cbranch_scc1 .Lat_pg1_chk_%=\n"
        "v_mov_b32 v13, 1\n"
        "buffer_store_dword v13, off, s[40:43], s65 sc1\n"
        "s_waitcnt vmcnt(0)\n"
        ".Lat_pg1_chk_%=:\n"
        "buffer_load_dword v13, off, s[40:43], s65 sc1\n"
        "s_waitcnt vmcnt(0)\n"
        "v_readfirstlane_b32 s65, v13\n"
        "s_nop 0\n"
        "s_cmp_lg_u32 s65, 1\n"
        "s_cbranch_scc1 .Lat_pg1_go_%=\n"
        "s_endpgm\n"
        ".Lat_pg1_go_%=:\n"
        "s_sleep 2\n"
        "s_branch .Lat_pg1_top_%=\n"
        ".Lat_pg1_ok_%=:\n"
        "v_cmp_eq_u32 s[54:55], 1, v13\n"
        "s_nop 1\n"
        "s_mov_b32 vcc_lo, 0x55555555\n"
        "s_mov_b32 vcc_hi, 0x55555555\n"
        "s_xor_b64 vcc, vcc, s[52:53]\n"
        "s_bcnt1_i32_b64 s65, vcc\n"
        "s_sub_u32 s64, s65, 23\n"
        "s_cmp_lt_u32 s64, 19\n"
        "s_cbranch_scc1 .Lat_rawmap_%=\n"
        "s_cmp_lt_u32 s65, 32\n"
        "s_cselect_b32 s65, 0, -1\n"
        "s_xor_b32 s52, s65, 0x55555555\n"
        "s_xor_b32 s53, s65, 0x55555555\n"
        "s_xor_b32 s54, s65, 0x55555555\n"
        "s_xor_b32 s55, s65, 0xaaaaaaaa\n"
        ".Lat_rawmap_%=:\n"
        "s_xor_b32 s52, s52, s66\n"
        "s_xor_b32 s53, s53, s66\n"
        "s_xor_b32 s54, s54, s66\n"
        "s_xor_b32 s55, s55, s66\n"
        "s_and_b32 s71, s55, 1\n"
        "s_lshl_b32 s71, s71, 13\n"
        "s_add_u32 s60, s60, s71\n"
        "v_mov_b64 v[166:167], 0\n"
        "v_mov_b64 v[172:173], 0\n"
        "v_mov_b64 v[176:177], 0\n"
        "v_mov_b64 v[178:179], 0\n"
        "v_mov_b64 v[182:183], 0\n"
        "v_mov_b32 v10, 0xff61b1e6\n"
        "s_mov_b32 s68, 0\n"
        "s_mov_b32 s72, 0\n"
        "s_bitcmp1_b64 s[52:53], s68\n"
        "s_cselect_b32 s65, 8192, 0\n"
        "s_lshl_b32 s70, s68, 21\n"
        "s_add_u32 s70, s70, s65\n"
        ".Lat_code_%=:\n"
        ".Lat_top_%=:\n"
        "s_mov_b32 s69, s70\n"
        "s_lshl_b32 s67, s68, 1\n"
        "s_mov_b32 s64, 0\n"
        "s_mov_b32 s86, s72\n"
        "s_cmp_eq_u32 s72, 7\n"
        "s_cbranch_scc1 .Lat_T_%=\n"
        ".Lat_more_%=:\n"
        "buffer_load_dword v5, v1, s[40:43], s71 offen sc1\n"
        "s_mov_b32 s86, s72\n"
        "s_waitcnt vmcnt(0)\n"
        "v_min_i32_dpp v5, v5, v5 quad_perm:[1,0,3,2] row_mask:0xf bank_mask:0xf\n"
        "s_nop 1\n"
        "v_min_i32_dpp v5, v5, v5 quad_perm:[2,3,0,1] row_mask:0xf bank_mask:0xf\n"
        "s_nop 0\n"
        "v_readlane_b32 s73, v5, 0\n"
        "v_readlane_b32 s74, v5, 4\n"
        "s_nop 0\n"
        "s_bitcmp1_b32 s72, 0\n"
        "s_cbranch_scc1 .Lat_hasK_%=\n"
        "s_add_u32 s65, s67, 1\n"
        "s_cmp_ge_i32 s73, s65\n"
        "s_cbranch_scc0 .Lat_hasK_%=\n"
        "s_add_u32 s65, s69, s58\n"
        "s_add_u32 s65, s65, 524288\n"
        "s_add_u32 s66, s65, 4096\n"
        "buffer_load_dwordx4 v[48:51], v0, s[40:43], s65 offen offset:0 sc1\n"
        "buffer_load_dwordx4 v[52:55], v0, s[40:43], s65 offen offset:1024 sc1\n"
        "buffer_load_dwordx4 v[56:59], v0, s[40:43], s65 offen offset:2048 sc1\n"
        "buffer_load_dwordx4 v[60:63], v0, s[40:43], s65 offen offset:3072 sc1\n"
        "buffer_load_dwordx4 v[64:67], v0, s[40:43], s66 offen offset:0 sc1\n"
        "buffer_load_dwordx4 v[68:71], v0, s[40:43], s66 offen offset:1024 sc1\n"
        "buffer_load_dwordx4 v[72:75], v0, s[40:43], s66 offen offset:2048 sc1\n"
        "buffer_load_dwordx4 v[76:79], v0, s[40:43], s66 offen offset:3072 sc1\n"
        "s_or_b32 s72, s72, 1\n"
        ".Lat_hasK_%=:\n"
        "s_bitcmp1_b32 s72, 1\n"
        "s_cbranch_scc1 .Lat_hasV_%=\n"
        "s_add_u32 s65, s67, 1\n"
        "s_cmp_ge_i32 s73, s65\n"
        "s_cbranch_scc0 .Lat_hasV_%=\n"
        "s_add_u32 s65, s69, s58\n"
        "s_add_u32 s65, s65, 1048576\n"
        "s_add_u32 s66, s65, 4096\n"
        "buffer_load_dwordx4 v[80:83], v0, s[40:43], s65 offen offset:0 sc1\n"
        "buffer_load_dwordx4 v[84:87], v0, s[40:43], s65 offen offset:1024 sc1\n"
        "buffer_load_dwordx4 v[88:91], v0, s[40:43], s65 offen offset:2048 sc1\n"
        "buffer_load_dwordx4 v[92:95], v0, s[40:43], s65 offen offset:3072 sc1\n"
        "buffer_load_dwordx4 v[96:99], v0, s[40:43], s66 offen offset:0 sc1\n"
        "buffer_load_dwordx4 v[100:103], v0, s[40:43], s66 offen offset:1024 sc1\n"
        "buffer_load_dwordx4 v[104:107], v0, s[40:43], s66 offen offset:2048 sc1\n"
        "buffer_load_dwordx4 v[108:111], v0, s[40:43], s66 offen offset:3072 sc1\n"
        "s_or_b32 s72, s72, 2\n"
        ".Lat_hasV_%=:\n"
        "s_bitcmp1_b32 s72, 3\n"
        "s_cbranch_scc1 .Lat_hasM_%=\n"
        "s_cmp_ge_i32 s74, s68\n"
        "s_cbranch_scc0 .Lat_hasM_%=\n"
        "s_add_u32 s65, s69, s59\n"
        "buffer_load_dwordx4 v[112:115], v3, s[40:43], s65 offen offset:0 sc1\n"
        "buffer_load_dwordx4 v[116:119], v3, s[40:43], s65 offen offset:512 sc1\n"
        "buffer_load_dwordx4 v[120:123], v3, s[40:43], s65 offen offset:1024 sc1\n"
        "buffer_load_dwordx4 v[124:127], v3, s[40:43], s65 offen offset:1536 sc1\n"
        "buffer_load_dwordx4 v[128:131], v4, s[40:43], s65 offen offset:2048 sc1\n"
        "buffer_load_dwordx4 v[132:135], v4, s[40:43], s65 offen offset:2560 sc1\n"
        "buffer_load_dwordx4 v[136:139], v4, s[40:43], s65 offen offset:3072 sc1\n"
        "buffer_load_dwordx4 v[140:143], v4, s[40:43], s65 offen offset:3584 sc1\n"
        "s_or_b32 s72, s72, 8\n"
        ".Lat_hasM_%=:\n"
        "s_bitcmp1_b32 s72, 2\n"
        "s_cbranch_scc1 .Lat_hasQ_%=\n"
        "s_add_u32 s65, s67, 1\n"
        "s_cmp_ge_i32 s73, s65\n"
        "s_cbranch_scc0 .Lat_hasQ_%=\n"
        "s_add_u32 s65, s69, s58\n"
        "s_add_u32 s65, s65, 0\n"
        "s_add_u32 s66, s65, 4096\n"
        "buffer_load_dwordx4 v[16:19], v0, s[40:43], s65 offen offset:0 sc1\n"
        "buffer_load_dwordx4 v[20:23], v0, s[40:43], s65 offen offset:1024 sc1\n"
        "buffer_load_dwordx4 v[24:27], v0, s[40:43], s65 offen offset:2048 sc1\n"
        "buffer_load_dwordx4 v[28:31], v0, s[40:43], s65 offen offset:3072 sc1\n"
        "buffer_load_dwordx4 v[32:35], v0, s[40:43], s66 offen offset:0 sc1\n"
        "buffer_load_dwordx4 v[36:39], v0, s[40:43], s66 offen offset:1024 sc1\n"
        "buffer_load_dwordx4 v[40:43], v0, s[40:43], s66 offen offset:2048 sc1\n"
        "buffer_load_dwordx4 v[44:47], v0, s[40:43], s66 offen offset:3072 sc1\n"
        "s_or_b32 s72, s72, 4\n"
        ".Lat_hasQ_%=:\n"
        "s_and_b32 s65, s72, 7\n"
        "s_cmp_eq_u32 s65, 7\n"
        "s_cbranch_scc1 .Lat_T_%=\n"
        "s_add_u32 s64, s64, 1\n"
        "s_and_b32 s65, s64, 255\n"
        "s_cbranch_scc1 .Lat_sp_go_%=\n"
        "s_mov_b32 s65, 49152\n"
        "s_cmp_lt_u32 s64, 1048576\n"
        "s_cbranch_scc1 .Lat_sp_chk_%=\n"
        "v_mov_b32 v6, 1\n"
        "buffer_store_dword v6, off, s[40:43], s65 sc1\n"
        "s_waitcnt vmcnt(0)\n"
        ".Lat_sp_chk_%=:\n"
        "buffer_load_dword v6, off, s[40:43], s65 sc1\n"
        "s_waitcnt vmcnt(0)\n"
        "v_readfirstlane_b32 s65, v6\n"
        "s_nop 0\n"
        "s_cmp_lg_u32 s65, 1\n"
        "s_cbranch_scc1 .Lat_sp_go_%=\n"
        "s_endpgm\n"
        ".Lat_sp_go_%=:\n"
        "s_branch .Lat_more_%=\n"
        ".Lat_T_%=:\n"
        "s_add_u32 s66, s68, 1\n"
        "s_bitcmp1_b64 s[52:53], s66\n"
        "s_cselect_b32 s65, 8192, 0\n"
        "s_lshl_b32 s70, s66, 21\n"
        "s_add_u32 s70, s70, s65\n"
        "s_add_u32 s67, s67, 2\n"
        "s_cmp_eq_u32 s86, 7\n"
        "s_cbranch_scc1 .Lat_T2_%=\n"
        "s_waitcnt vmcnt(0)\n"
        ".Lat_T2_%=:\n"
        "s_cmp_eq_u32 s72, 15\n"
        "s_cbranch_scc1 .Lat_T3_%=\n"
        "buffer_load_dword v5, v1, s[40:43], s71 offen sc1\n"
        ".Lat_T3_%=:\n"
        "s_waitcnt vmcnt(10)\n"
        "v_mfma_f32_16x16x32_bf16 v[144:147], v[48:51], v[16:19], 0\n"
        "v_mfma_f32_16x16x32_bf16 v[152:155], v[52:55], v[20:23], 0\n"
        "v_mfma_f32_16x16x32_bf16 v[148:151], v[48:51], v[20:23], 0\n"
        "v_mfma_f32_16x16x32_bf16 v[144:147], v[56:59], v[24:27], v[144:147]\n"
        "v_mfma_f32_16x16x32_bf16 v[152:155], v[60:63], v[28:31], v[152:155]\n"
        "v_mfma_f32_16x16x32_bf16 v[148:151], v[56:59], v[28:31], v[148:151]\n"
        "v_mfma_f32_16x16x32_bf16 v[144:147], v[64:67], v[32:35], v[144:147]\n"
        "v_mfma_f32_16x16x32_bf16 v[152:155], v[68:71], v[36:39], v[152:155]\n"
        "v_mfma_f32_16x16x32_bf16 v[148:151], v[64:67], v[36:39], v[148:151]\n"
        "v_mfma_f32_16x16x32_bf16 v[144:147], v[72:75], v[40:43], v[144:147]\n"
        "v_mfma_f32_16x16x32_bf16 v[152:155], v[76:79], v[44:47], v[152:155]\n"
        "v_mfma_f32_16x16x32_bf16 v[148:151], v[72:75], v[44:47], v[148:151]\n"
        "v_cndmask_b32 v144, v10, v144, s[76:77]\n"
        "v_cndmask_b32 v145, v10, v145, s[78:79]\n"
        "v_cndmask_b32 v146, v10, v146, s[80:81]\n"
        "v_cndmask_b32 v147, v10, v147, s[82:83]\n"
        "v_cndmask_b32 v152, v10, v152, s[76:77]\n"
        "v_cndmask_b32 v153, v10, v153, s[78:79]\n"
        "v_cndmask_b32 v154, v10, v154, s[80:81]\n"
        "v_cndmask_b32 v155, v10, v155, s[82:83]\n"
        "v_max3_f32 v8, v144, v145, v146\n"
        "v_max3_f32 v9, v152, v153, v154\n"
        "v_max_f32 v8, v8, v147\n"
        "v_max3_f32 v9, v9, v155, v148\n"
        "v_max3_f32 v9, v9, v149, v150\n"
        "v_max_f32 v9, v9, v151\n"
        "v_mul_f32 v144, s75, v144\n"
        "v_mul_f32 v145, s75, v145\n"
        "v_mul_f32 v146, s75, v146\n"
        "v_mul_f32 v147, s75, v147\n"
        "v_mul_f32 v148, s75, v148\n"
        "v_mul_f32 v149, s75, v149\n"
        "v_mul_f32 v150, s75, v150\n"
        "v_mul_f32 v151, s75, v151\n"
        "v_mul_f32 v152, s75, v152\n"
        "v_mul_f32 v153, s75, v153\n"
        "v_mul_f32 v154, s75, v154\n"
        "v_mul_f32 v155, s75, v155\n"
        "s_cmp_eq_u32 s72, 15\n"
        "s_cbranch_scc1 .Lat_hasm_%=\n"
        "s_mov_b32 s64, 0\n"
        ".Lat_wm_%=:\n"
        "s_waitcnt vmcnt(0)\n"
        "v_min_i32_dpp v5, v5, v5 quad_perm:[1,0,3,2] row_mask:0xf bank_mask:0xf\n"
        "s_nop 1\n"
        "v_min_i32_dpp v5, v5, v5 quad_perm:[2,3,0,1] row_mask:0xf bank_mask:0xf\n"
        "s_nop 0\n"
        "v_readlane_b32 s73, v5, 0\n"
        "v_readlane_b32 s74, v5, 4\n"
        "s_nop 0\n"
        "s_cmp_ge_i32 s74, s68\n"
        "s_cbranch_scc1 .Lat_gm_%=\n"
        "s_add_u32 s64, s64, 1\n"
        "s_and_b32 s65, s64, 255\n"
        "s_cbranch_scc1 .Lat_sq_go_%=\n"
        "s_mov_b32 s65, 49152\n"
        "s_cmp_lt_u32 s64, 1048576\n"
        "s_cbranch_scc1 .Lat_sq_chk_%=\n"
        "v_mov_b32 v6, 1\n"
        "buffer_store_dword v6, off, s[40:43], s65 sc1\n"
        "s_waitcnt vmcnt(0)\n"
        ".Lat_sq_chk_%=:\n"
        "buffer_load_dword v6, off, s[40:43], s65 sc1\n"
        "s_waitcnt vmcnt(0)\n"
        "v_readfirstlane_b32 s65, v6\n"
        "s_nop 0\n"
        "s_cmp_lg_u32 s65, 1\n"
        "s_cbranch_scc1 .Lat_sq_go_%=\n"
        "s_endpgm\n"
        ".Lat_sq_go_%=:\n"
        "buffer_load_dword v5, v1, s[40:43], s71 offen sc1\n"
        "s_branch .Lat_wm_%=\n"
        ".Lat_gm_%=:\n"
        "s_add_u32 s65, s69, s59\n"
        "buffer_load_dwordx4 v[112:115], v3, s[40:43], s65 offen offset:0 sc1\n"
        "buffer_load_dwordx4 v[116:119], v3, s[40:43], s65 offen offset:512 sc1\n"
        "buffer_load_dwordx4 v[120:123], v3, s[40:43], s65 offen offset:1024 sc1\n"
        "buffer_load_dwordx4 v[124:127], v3, s[40:43], s65 offen offset:1536 sc1\n"
        "buffer_load_dwordx4 v[128:131], v4, s[40:43], s65 offen offset:2048 sc1\n"
        "buffer_load_dwordx4 v[132:135], v4, s[40:43], s65 offen offset:2560 sc1\n"
        "buffer_load_dwordx4 v[136:139], v4, s[40:43], s65 offen offset:3072 sc1\n"
        "buffer_load_dwordx4 v[140:143], v4, s[40:43], s65 offen offset:3584 sc1\n"
        ".Lat_hasm_%=:\n"
        "s_mov_b32 s72, 0\n"
        "s_add_u32 s87, s70, s58\n"
        "s_add_u32 s87, s87, 524288\n"
        "s_add_u32 s88, s67, 1\n"
        "s_cmp_ge_i32 s73, s88\n"
        "s_cselect_b32 s87, s87, 241172480\n"
        "s_cselect_b32 s88, 1, 0\n"
        "s_or_b32 s72, s72, s88\n"
        "s_add_u32 s88, s87, 4096\n"
        "s_add_u32 s65, s69, s58\n"
        "s_add_u32 s66, s65, 4096\n"
        "s_lshl_b32 s89, s68, 1\n"
        "v_mov_b32 v6, s89\n"
        "s_waitcnt vmcnt(7)\n"
        "v_mfma_f32_16x16x32_bf16 v[156:159], v[112:115], v[16:19], 0\n"
        "s_waitcnt vmcnt(6)\n"
        "v_mfma_f32_16x16x32_bf16 v[156:159], v[116:119], v[24:27], v[156:159]\n"
        "s_waitcnt vmcnt(5)\n"
        "v_mfma_f32_16x16x32_bf16 v[156:159], v[120:123], v[32:35], v[156:159]\n"
        "s_waitcnt vmcnt(4)\n"
        "v_mfma_f32_16x16x32_bf16 v[156:159], v[124:127], v[40:43], v[156:159]\n"
        "v_mfma_f32_16x16x32_bf16 v[160:163], v[112:115], v[20:23], 0\n"
        "v_mfma_f32_16x16x32_bf16 v[160:163], v[116:119], v[28:31], v[160:163]\n"
        "v_mfma_f32_16x16x32_bf16 v[160:163], v[120:123], v[36:39], v[160:163]\n"
        "v_mfma_f32_16x16x32_bf16 v[160:163], v[124:127], v[44:47], v[160:163]\n"
        "v_max3_f32 v8, v8, v156, v157\n"
        "v_max3_f32 v216, v8, v158, v159\n"
        "v_max3_f32 v217, v8, v158, v159\n"
        "v_mul_f32 v156, s75, v156\n"
        "v_mul_f32 v157, s75, v157\n"
        "v_permlane16_swap_b32 v216, v217\n"
        "v_max_f32 v8, v216, v217\n"
        "v_max_f32 v216, v216, v217\n"
        "v_mul_f32 v158, s75, v158\n"
        "v_mul_f32 v159, s75, v159\n"
        "v_permlane32_swap_b32 v8, v216\n"
        "v_max_f32 v8, v8, v216\n"
        "v_mul_f32 v8, s75, v8\n"
        "v_cndmask_b32 v216, v8, -v10, s[84:85]\n"
        "v_sub_f32 v144, v144, v8\n"
        "v_exp_f32 v144, v144\n"
        "v_sub_f32 v145, v145, v8\n"
        "v_exp_f32 v145, v145\n"
        "v_sub_f32 v146, v146, v8\n"
        "v_exp_f32 v146, v146\n"
        "v_sub_f32 v147, v147, v8\n"
        "v_exp_f32 v147, v147\n"
        "v_sub_f32 v156, v156, v216\n"
        "v_exp_f32 v156, v156\n"
        "v_sub_f32 v157, v157, v216\n"
        "v_exp_f32 v157, v157\n"
        "v_sub_f32 v158, v158, v216\n"
        "v_exp_f32 v158, v158\n"
        "v_sub_f32 v159, v159, v216\n"
        "v_exp_f32 v159, v159\n"
        "v_add_f32 v216, v144, v145\n"
        "v_add_f32 v217, v146, v147\n"
        "v_add_f32 v216, v216, v156\n"
        "v_add_f32 v217, v217, v157\n"
        "v_add_f32 v216, v216, v158\n"
        "v_add_f32 v217, v217, v159\n"
        "v_add_f32 v8, v216, v217\n"
        "v_add_f32 v216, v216, v217\n"
        "s_nop 1\n"
        "v_permlane16_swap_b32 v8, v216\n"
        "v_add_f32 v217, v8, v216\n"
        "v_add_f32 v216, v8, v216\n"
        "s_nop 1\n"
        "v_permlane32_swap_b32 v216, v217\n"
        "v_add_f32 v8, v216, v217\n"
        "v_rcp_f32 v8, v8\n"
        "s_nop 0\n"
        "v_mul_f32 v156, v156, v8\n"
        "v_mul_f32 v157, v157, v8\n"
        "v_mul_f32 v158, v158, v8\n"
        "v_mul_f32 v159, v159, v8\n"
        "v_cvt_pk_bf16_f32 v174, v156, v157\n"
        "v_cvt_pk_bf16_f32 v175, v158, v159\n"
        "v_mul_f32 v144, v144, v8\n"
        "v_mul_f32 v145, v145, v8\n"
        "v_cvt_pk_bf16_f32 v164, v144, v145\n"
        "v_mul_f32 v146, v146, v8\n"
        "v_mul_f32 v147, v147, v8\n"
        "v_cvt_pk_bf16_f32 v165, v146, v147\n"
        "s_waitcnt vmcnt(0)\n"
        "s_nop 0\n"
        "v_mfma_f32_16x16x32_bf16 v[184:187], v[128:131], v[174:177], 0\n"
        "v_max3_f32 v9, v9, v160, v161\n"
        "v_max3_f32 v220, v9, v162, v163\n"
        "v_max3_f32 v221, v9, v162, v163\n"
        "v_mfma_f32_16x16x32_bf16 v[188:191], v[128:131], v[172:175], 0\n"
        "v_mul_f32 v160, s75, v160\n"
        "v_mul_f32 v161, s75, v161\n"
        "v_mfma_f32_16x16x32_bf16 v[184:187], v[80:83], v[164:167], v[184:187]\n"
        "v_permlane16_swap_b32 v220, v221\n"
        "v_max_f32 v9, v220, v221\n"
        "v_mfma_f32_16x16x32_bf16 v[188:191], v[84:87], v[164:167], v[188:191]\n"
        "v_max_f32 v220, v220, v221\n"
        "v_mul_f32 v162, s75, v162\n"
        "v_mul_f32 v163, s75, v163\n"
        "v_mfma_f32_16x16x32_bf16 v[192:195], v[132:135], v[174:177], 0\n"
        "v_permlane32_swap_b32 v9, v220\n"
        "v_max_f32 v9, v9, v220\n"
        "v_mfma_f32_16x16x32_bf16 v[196:199], v[132:135], v[172:175], 0\n"
        "v_cvt_pk_bf16_f32 v184, v184, v185\n"
        "v_cvt_pk_bf16_f32 v185, v186, v187\n"
        "v_cvt_pk_bf16_f32 v186, v188, v189\n"
        "v_mfma_f32_16x16x32_bf16 v[192:195], v[88:91], v[164:167], v[192:195]\n"
        "v_cvt_pk_bf16_f32 v187, v190, v191\n"
        "buffer_store_dwordx4 v[184:187], v0, s[40:43], s65 offen offset:0 sc1\n"
        "v_mfma_f32_16x16x32_bf16 v[196:199], v[92:95], v[164:167], v[196:199]\n"
        "v_mul_f32 v9, s75, v9\n"
        "v_cndmask_b32 v220, v9, -v10, s[84:85]\n"
        "v_sub_f32 v148, v148, v9\n"
        "v_mfma_f32_16x16x32_bf16 v[200:203], v[136:139], v[174:177], 0\n"
        "v_exp_f32 v148, v148\n"
        "v_sub_f32 v149, v149, v9\n"
        "v_mfma_f32_16x16x32_bf16 v[204:207], v[136:139], v[172:175], 0\n"
        "v_cvt_pk_bf16_f32 v192, v192, v193\n"
        "v_cvt_pk_bf16_f32 v193, v194, v195\n"
        "v_cvt_pk_bf16_f32 v194, v196, v197\n"
        "v_mfma_f32_16x16x32_bf16 v[200:203], v[96:99], v[164:167], v[200:203]\n"
        "v_cvt_pk_bf16_f32 v195, v198, v199\n"
        "buffer_store_dwordx4 v[192:195], v0, s[40:43], s65 offen offset:1024 sc1\n"
        "v_mfma_f32_16x16x32_bf16 v[204:207], v[100:103], v[164:167], v[204:207]\n"
        "v_exp_f32 v149, v149\n"
        "v_sub_f32 v150, v150, v9\n"
        "v_mfma_f32_16x16x32_bf16 v[208:211], v[140:143], v[174:177], 0\n"
        "v_exp_f32 v150, v150\n"
        "v_sub_f32 v151, v151, v9\n"
        "v_mfma_f32_16x16x32_bf16 v[212:215], v[140:143], v[172:175], 0\n"
        "v_cvt_pk_bf16_f32 v200, v200, v201\n"
        "v_cvt_pk_bf16_f32 v201, v202, v203\n"
        "v_cvt_pk_bf16_f32 v202, v204, v205\n"
        "v_mfma_f32_16x16x32_bf16 v[208:211], v[104:107], v[164:167], v[208:211]\n"
        "v_cvt_pk_bf16_f32 v203, v206, v207\n"
        "buffer_store_dwordx4 v[200:203], v0, s[40:43], s65 offen offset:2048 sc1\n"
        "v_mfma_f32_16x16x32_bf16 v[212:215], v[108:111], v[164:167], v[212:215]\n"
        "v_exp_f32 v151, v151\n"
        "v_sub_f32 v152, v152, v9\n"
        "v_exp_f32 v152, v152\n"
        "v_sub_f32 v153, v153, v9\n"
        "v_exp_f32 v153, v153\n"
        "v_sub_f32 v154, v154, v9\n"
        "v_exp_f32 v154, v154\n"
        "v_sub_f32 v155, v155, v9\n"
        "v_exp_f32 v155, v155\n"
        "v_sub_f32 v160, v160, v220\n"
        "v_cvt_pk_bf16_f32 v208, v208, v209\n"
        "v_cvt_pk_bf16_f32 v209, v210, v211\n"
        "v_cvt_pk_bf16_f32 v210, v212, v213\n"
        "v_cvt_pk_bf16_f32 v211, v214, v215\n"
        "buffer_store_dwordx4 v[208:211], v0, s[40:43], s65 offen offset:3072 sc1\n"
        "v_exp_f32 v160, v160\n"
        "v_sub_f32 v161, v161, v220\n"
        "v_exp_f32 v161, v161\n"
        "v_sub_f32 v162, v162, v220\n"
        "v_exp_f32 v162, v162\n"
        "v_sub_f32 v163, v163, v220\n"
        "buffer_load_dwordx4 v[48:51], v0, s[40:43], s87 offen offset:0 sc1\n"
        "v_exp_f32 v163, v163\n"
        "v_add_f32 v220, v148, v149\n"
        "v_add_f32 v221, v150, v151\n"
        "v_add_f32 v220, v220, v152\n"
        "v_add_f32 v221, v221, v153\n"
        "v_add_f32 v220, v220, v154\n"
        "buffer_load_dwordx4 v[52:55], v0, s[40:43], s87 offen offset:1024 sc1\n"
        "v_add_f32 v221, v221, v155\n"
        "v_add_f32 v220, v220, v160\n"
        "v_add_f32 v221, v221, v161\n"
        "v_add_f32 v220, v220, v162\n"
        "v_add_f32 v221, v221, v163\n"
        "v_add_f32 v9, v220, v221\n"
        "buffer_load_dwordx4 v[56:59], v0, s[40:43], s87 offen offset:2048 sc1\n"
        "v_add_f32 v220, v220, v221\n"
        "s_nop 1\n"
        "v_permlane16_swap_b32 v9, v220\n"
        "v_add_f32 v221, v9, v220\n"
        "v_add_f32 v220, v9, v220\n"
        "s_nop 1\n"
        "buffer_load_dwordx4 v[60:63], v0, s[40:43], s87 offen offset:3072 sc1\n"
        "v_permlane32_swap_b32 v220, v221\n"
        "v_add_f32 v9, v220, v221\n"
        "v_rcp_f32 v9, v9\n"
        "s_nop 0\n"
        "v_mul_f32 v160, v160, v9\n"
        "v_mul_f32 v161, v161, v9\n"
        "buffer_load_dwordx4 v[64:67], v0, s[40:43], s88 offen offset:0 sc1\n"
        "v_mul_f32 v162, v162, v9\n"
        "v_mul_f32 v163, v163, v9\n"
        "v_cvt_pk_bf16_f32 v180, v160, v161\n"
        "v_cvt_pk_bf16_f32 v181, v162, v163\n"
        "v_mul_f32 v148, v148, v9\n"
        "v_mul_f32 v149, v149, v9\n"
        "buffer_load_dwordx4 v[68:71], v0, s[40:43], s88 offen offset:1024 sc1\n"
        "v_cvt_pk_bf16_f32 v168, v148, v149\n"
        "v_mul_f32 v150, v150, v9\n"
        "v_mul_f32 v151, v151, v9\n"
        "v_cvt_pk_bf16_f32 v169, v150, v151\n"
        "v_mul_f32 v152, v152, v9\n"
        "v_mul_f32 v153, v153, v9\n"
        "buffer_load_dwordx4 v[72:75], v0, s[40:43], s88 offen offset:2048 sc1\n"
        "v_cvt_pk_bf16_f32 v170, v152, v153\n"
        "v_mul_f32 v154, v154, v9\n"
        "v_mul_f32 v155, v155, v9\n"
        "v_cvt_pk_bf16_f32 v171, v154, v155\n"
        "s_nop 1\n"
        "v_mfma_f32_16x16x32_bf16 v[184:187], v[128:131], v[180:183], 0\n"
        "buffer_load_dwordx4 v[76:79], v0, s[40:43], s88 offen offset:3072 sc1\n"
        "v_mfma_f32_16x16x32_bf16 v[188:191], v[128:131], v[178:181], 0\n"
        "buffer_load_dword v5, v1, s[40:43], s71 offen sc1\n"
        "v_mfma_f32_16x16x32_bf16 v[184:187], v[80:83], v[168:171], v[184:187]\n"
        "v_mfma_f32_16x16x32_bf16 v[188:191], v[84:87], v[168:171], v[188:191]\n"
        "v_mfma_f32_16x16x32_bf16 v[192:195], v[132:135], v[180:183], 0\n"
        "s_waitcnt vmcnt(9)\n"
        "buffer_store_dword v6, v2, s[40:43], s60 offen sc1\n"
        "v_mfma_f32_16x16x32_bf16 v[196:199], v[132:135], v[178:181], 0\n"
        "v_cvt_pk_bf16_f32 v184, v184, v185\n"
        "v_cvt_pk_bf16_f32 v185, v186, v187\n"
        "v_cvt_pk_bf16_f32 v186, v188, v189\n"
        "v_mfma_f32_16x16x32_bf16 v[192:195], v[88:91], v[168:171], v[192:195]\n"
        "v_cvt_pk_bf16_f32 v187, v190, v191\n"
        "buffer_store_dwordx4 v[184:187], v0, s[40:43], s66 offen offset:0 sc1\n"
        "v_mfma_f32_16x16x32_bf16 v[196:199], v[92:95], v[168:171], v[196:199]\n"
        "v_mfma_f32_16x16x32_bf16 v[200:203], v[136:139], v[180:183], 0\n"
        "v_mfma_f32_16x16x32_bf16 v[204:207], v[136:139], v[178:181], 0\n"
        "v_cvt_pk_bf16_f32 v192, v192, v193\n"
        "v_cvt_pk_bf16_f32 v193, v194, v195\n"
        "v_cvt_pk_bf16_f32 v194, v196, v197\n"
        "v_mfma_f32_16x16x32_bf16 v[200:203], v[96:99], v[168:171], v[200:203]\n"
        "v_cvt_pk_bf16_f32 v195, v198, v199\n"
        "buffer_store_dwordx4 v[192:195], v0, s[40:43], s66 offen offset:1024 sc1\n"
        "v_mfma_f32_16x16x32_bf16 v[204:207], v[100:103], v[168:171], v[204:207]\n"
        "v_mfma_f32_16x16x32_bf16 v[208:211], v[140:143], v[180:183], 0\n"
        "v_mfma_f32_16x16x32_bf16 v[212:215], v[140:143], v[178:181], 0\n"
        "v_cvt_pk_bf16_f32 v200, v200, v201\n"
        "v_cvt_pk_bf16_f32 v201, v202, v203\n"
        "v_cvt_pk_bf16_f32 v202, v204, v205\n"
        "v_mfma_f32_16x16x32_bf16 v[208:211], v[104:107], v[168:171], v[208:211]\n"
        "v_cvt_pk_bf16_f32 v203, v206, v207\n"
        "buffer_store_dwordx4 v[200:203], v0, s[40:43], s66 offen offset:2048 sc1\n"
        "v_mfma_f32_16x16x32_bf16 v[212:215], v[108:111], v[168:171], v[212:215]\n"
        "s_waitcnt vmcnt(4)\n"
        "v_min_i32_dpp v5, v5, v5 quad_perm:[1,0,3,2] row_mask:0xf bank_mask:0xf\n"
        "s_nop 1\n"
        "v_min_i32_dpp v5, v5, v5 quad_perm:[2,3,0,1] row_mask:0xf bank_mask:0xf\n"
        "s_nop 0\n"
        "v_readlane_b32 s73, v5, 0\n"
        "v_readlane_b32 s74, v5, 4\n"
        "s_nop 0\n"
        "s_nop 0\n"
        "v_cvt_pk_bf16_f32 v208, v208, v209\n"
        "v_cvt_pk_bf16_f32 v209, v210, v211\n"
        "v_cvt_pk_bf16_f32 v210, v212, v213\n"
        "v_cvt_pk_bf16_f32 v211, v214, v215\n"
        "buffer_store_dwordx4 v[208:211], v0, s[40:43], s66 offen offset:3072 sc1\n"
        "s_add_u32 s65, s70, s58\n"
        "s_add_u32 s65, s65, 0\n"
        "s_add_u32 s66, s67, 1\n"
        "s_cmp_ge_i32 s73, s66\n"
        "s_cselect_b32 s65, s65, 241172480\n"
        "s_cselect_b32 s66, 4, 0\n"
        "s_or_b32 s72, s72, s66\n"
        "s_add_u32 s66, s65, 4096\n"
        "buffer_load_dwordx4 v[16:19], v0, s[40:43], s65 offen offset:0 sc1\n"
        "buffer_load_dwordx4 v[20:23], v0, s[40:43], s65 offen offset:1024 sc1\n"
        "buffer_load_dwordx4 v[24:27], v0, s[40:43], s65 offen offset:2048 sc1\n"
        "buffer_load_dwordx4 v[28:31], v0, s[40:43], s65 offen offset:3072 sc1\n"
        "buffer_load_dwordx4 v[32:35], v0, s[40:43], s66 offen offset:0 sc1\n"
        "buffer_load_dwordx4 v[36:39], v0, s[40:43], s66 offen offset:1024 sc1\n"
        "buffer_load_dwordx4 v[40:43], v0, s[40:43], s66 offen offset:2048 sc1\n"
        "buffer_load_dwordx4 v[44:47], v0, s[40:43], s66 offen offset:3072 sc1\n"
        "s_add_u32 s65, s70, s58\n"
        "s_add_u32 s65, s65, 1048576\n"
        "s_add_u32 s66, s67, 1\n"
        "s_cmp_ge_i32 s73, s66\n"
        "s_cselect_b32 s65, s65, 241172480\n"
        "s_cselect_b32 s66, 2, 0\n"
        "s_or_b32 s72, s72, s66\n"
        "s_add_u32 s66, s65, 4096\n"
        "v_add_u32 v6, 1, v6\n"
        "s_waitcnt vmcnt(8)\n"
        "buffer_store_dword v6, v2, s[40:43], s60 offen sc1\n"
        "buffer_load_dwordx4 v[80:83], v0, s[40:43], s65 offen offset:0 sc1\n"
        "buffer_load_dwordx4 v[84:87], v0, s[40:43], s65 offen offset:1024 sc1\n"
        "buffer_load_dwordx4 v[88:91], v0, s[40:43], s65 offen offset:2048 sc1\n"
        "buffer_load_dwordx4 v[92:95], v0, s[40:43], s65 offen offset:3072 sc1\n"
        "buffer_load_dwordx4 v[96:99], v0, s[40:43], s66 offen offset:0 sc1\n"
        "buffer_load_dwordx4 v[100:103], v0, s[40:43], s66 offen offset:1024 sc1\n"
        "buffer_load_dwordx4 v[104:107], v0, s[40:43], s66 offen offset:2048 sc1\n"
        "buffer_load_dwordx4 v[108:111], v0, s[40:43], s66 offen offset:3072 sc1\n"
        "s_add_u32 s68, s68, 1\n"
        "s_cmp_lt_u32 s68, 64\n"
        "s_cbranch_scc1 .Lat_top_%=\n"
        "s_cmp_eq_u32 s61, 0\n"
        "s_cbranch_scc0 .Lat_bye_%=\n"
        "s_and_b32 s66, s57, 7\n"
        "s_lshl_b32 s66, s66, 1\n"
        "s_add_u32 s66, s66, s50\n"
        "s_lshl_b32 s66, s66, 7\n"
        "s_add_u32 s66, s66, 51456\n"
        ".Lat_bye_%=:\n"
        "s_endpgm\n"
        ".Lat_end_%=:\n"
        :: "s"(p.ws), "s"(pair(x4, pr)), "s"(pair(PAGE + h * 16384, PAGE + spread(A_KM) + (h >> 1) * 16384 + (h & 1) * 4096)), "s"(pair(0, w)),
           "v"(pair(lane * 16, lane < 8 ? flag(((lane & 4) ? F_CH : F_S1 + 128) + 4 * h + (lane & 3)) : OOB)), "v"(pair(lane < 3 ? flag(F_AT + 3 * h + lane) : OOB, (g * 8 + (i & 7)) * 16)),
           "v"(pair((g & 1) * 256 + i * 16, 4 * g - i))
        : "v0", "v1", "v2", "v3", "v4", "v5", "v6", "v7", "v8", "v9", "v10", "v11", "v12", "v13", "v14", "v15", "v16", "v17", "v18", "v19", "v20", "v21", "v22", "v23", "v24", "v25", "v26", "v27", "v28", "v29", "v30", "v31", "v32", "v33", "v34", "v35", "v36", "v37", "v38", "v39", "v40", "v41", "v42", "v43", "v44", "v45", "v46", "v47", "v48", "v49", "v50", "v51", "v52", "v53", "v54", "v55", "v56", "v57", "v58", "v59", "v60", "v61", "v62", "v63", "v64", "v65", "v66", "v67", "v68", "v69", "v70", "v71", "v72", "v73", "v74", "v75", "v76", "v77", "v78", "v79", "v255", "a255", "s26", "s27", "s28", "s29", "s30", "s31", "s32", "s33", "s34", "s35", "s36", "s37", "s38", "s39", "s40", "s41", "s42", "s43", "s44", "s45", "s46", "s47", "s48", "s49", "s50", "s51", "s52", "s53", "s54", "s55", "s56", "s57", "s58", "s59", "s60", "s61", "s62", "s63", "s64", "s65", "s66", "s67", "s68", "s69", "s70", "s71", "s72", "s73", "s74", "s75", "s76", "s77", "s78", "s79", "s80", "s81", "s82", "s83", "s84", "s85", "s86", "s87", "s88", "s89", "s90", "s91", "s92", "s93", "s94", "s95", "s96", "s97", "s98", "s99", "vcc", "scc", "memory");
    __builtin_unreachable();
}

template <bool R5>
__device__ __forceinline__ void role_red(const Params& p, int a, int x4, int pr, u64 epoch) {
    const unsigned lane = threadIdx.x & 63, i = lane & 15, g = lane >> 4;
    const int w = rfl(threadIdx.x >> 6);
    if (w == 3) return;
    unsigned to[3], po[3], one[4];
#pragma unroll
    for (int n = 0; n < 3; ++n) { const unsigned b = 3 * w + n; to[n] = b < 8 ? a * 16384 + b * 1024 : 65536 + a * 1024; po[n] = part_off(a, 0, 8 * b); }
#pragma unroll
    for (int e = 0; e < 4; ++e) one[e] = ((i & 7) == 2 * e) ? 0x3f80u : (((i & 7) == 2 * e + 1) ? 0x3f800000u : 0u);
    const u64 o0 = pair(one[0], one[1]), o1 = pair(one[2], one[3]);
    const bool lo = i < 8, r0 = g == 2 * (i >> 3), r1 = g == 1 + 2 * (i >> 3);
    const unsigned fin = lane < 16 ? flag((R5 ? F_F2 : F_OP) + a * 64 + w * 16 + lane) : OOB;
    const unsigned fout = (w == 0 && lane < (R5 ? 64 : 48)) ? flag((R5 ? F_XN : F_R3) + 8 * (lane >> 1) + 2 * a + (lane & 1)) : OOB;
#define RED_ARGS :: "s"(p.ws), "s"(p.x), "s"(p.out), "s"(pair(x4 | (a >> 1) << 8, pr)), "s"(pair(to[0], to[1])), "s"(pair(to[2], po[0])), "s"(pair(po[1], po[2])), \
        "v"(pair(lane * 16, fin)), "v"(pair(fout, g * 16384 + i * 16)), "v"(pair(g * (w == 2 ? 1024 : 16384) + i * 16, (8 * a + (lane & 7)) * 1152 + w * 384 + (lane >> 3) * 16)), \
        "v"(pair(w * 64 + (lane & 7) * 8, (lane & 7) * 8)), "v"(lo ? o0 : 0), "v"(lo ? o1 : 0), "v"(lo ? 0 : o0), "v"(lo ? 0 : o1), \
        "v"(r0 ? o0 : 0), "v"(r0 ? o1 : 0), "v"(r1 ? o0 : 0), "v"(r1 ? o1 : 0), "v"(epoch + 8), "v"(pair(a == 0 && threadIdx.x == 0 ? 0u : OOB, a == 3 && threadIdx.x < 18 ? (G_DONE + threadIdx.x) * 128 : OOB))
    if constexpr (R5) {
        asm volatile(
        "s_nop 4\n"
        "s_mov_b64 s[40:41], %0\n"
        "s_and_b32 s41, s41, 0xffff\n"
        "s_mov_b32 s42, 236978176\n"
        "s_mov_b32 s43, 0x20000\n"
        "s_mov_b64 s[44:45], %1\n"
        "s_and_b32 s45, s45, 0xffff\n"
        "s_mov_b32 s46, 2359296\n"
        "s_mov_b32 s47, 0x20000\n"
        "s_mov_b64 s[56:57], %3\n"
        "s_mov_b64 s[58:59], %4\n"
        "s_mov_b64 s[60:61], %5\n"
        "s_mov_b64 s[62:63], %6\n"
        "v_mov_b64 v[0:1], %7\n"
        "v_mov_b64 v[2:3], %8\n"
        "v_mov_b64 v[4:5], %9\n"
        "v_mov_b64 v[6:7], %10\n"
        "v_mov_b64 v[8:9], %11\n"
        "v_mov_b64 v[10:11], %12\n"
        "v_mov_b64 v[12:13], %13\n"
        "v_mov_b64 v[14:15], %14\n"
        "v_mov_b64 v[16:17], %15\n"
        "v_mov_b64 v[18:19], %16\n"
        "v_mov_b64 v[20:21], %17\n"
        "v_mov_b64 v[22:23], %18\n"
        "v_mov_b64 v[24:25], %19\n"
        "v_mov_b64 v[26:27], %20\n"
        "v_mov_b64 v[200:201], v[24:25]\n"
        "v_mov_b64 v[202:203], v[26:27]\n"
        "s_lshr_b32 s74, s56, 8\n"
        "s_and_b32 s56, s56, 1\n"
        "s_mov_b32 s68, 0\n"
        "s_mul_i32 s65, s68, 36864\n"
        "buffer_load_dwordx4 v[80:83], v5, s[44:47], s65 offen offset:0\n"
        "buffer_load_dwordx4 v[84:87], v5, s[44:47], s65 offen offset:128\n"
        "buffer_load_dwordx4 v[88:91], v5, s[44:47], s65 offen offset:256\n"
        "s_getpc_b64 s[76:77]\n"
        ".Lr5_pc_%=:\n"
        "s_add_u32 s76, s76, .Lr5_code_%=-.Lr5_pc_%=\n"
        "s_addc_u32 s77, s77, 0\n"
        "s_and_b32 s77, s77, 0xffff\n"
        "s_and_b32 s76, s76, -4\n"
        "s_mov_b32 s78, .Lr5_end_%=-.Lr5_code_%=\n"
        "s_mov_b32 s79, 0x20000\n"
        "s_mov_b32 s65, 0\n"
        "buffer_load_dwordx4 v[32:35], v0, s[76:79], s65 offen\n"
        "s_add_u32 s65, s65, 1024\n"
        "buffer_load_dwordx4 v[36:39], v0, s[76:79], s65 offen\n"
        "s_add_u32 s65, s65, 1024\n"
        "buffer_load_dwordx4 v[40:43], v0, s[76:79], s65 offen\n"
        "s_add_u32 s65, s65, 1024\n"
        "buffer_load_dwordx4 v[44:47], v0, s[76:79], s65 offen\n"
        "s_add_u32 s65, s65, 1024\n"
        "buffer_load_dwordx4 v[48:51], v0, s[76:79], s65 offen\n"
        "s_add_u32 s65, s65, 1024\n"
        "v_lshrrev_b32 v26, 4, v0\n"
        "s_mov_b32 s50, s56\n"
        "v_lshlrev_b32 v28, 7, v26\n"
        "s_sub_u32 s66, 0, s50\n"
        "s_mov_b32 s52, 32768\n"
        "s_mov_b32 s64, 0\n"
        ".Lr5_pg0_top_%=:\n"
        "buffer_load_dword v27, v28, s[40:43], s52 offen sc1\n"
        "s_waitcnt vmcnt(0)\n"
        "v_cmp_gt_i32 vcc, 0, v27\n"
        "s_cbranch_vccz .Lr5_pg0_ok_%=\n"
        "s_add_u32 s64, s64, 1\n"
        "s_and_b32 s65, s64, 255\n"
        "s_cbranch_scc1 .Lr5_pg0_go_%=\n"
        "s_mov_b32 s65, 49152\n"
        "s_cmp_lt_u32 s64, 1048576\n"
        "s_cbranch_scc1 .Lr5_pg0_chk_%=\n"
        "v_mov_b32 v27, 1\n"
        "buffer_store_dword v27, off, s[40:43], s65 sc1\n"
        "s_waitcnt vmcnt(0)\n"
        ".Lr5_pg0_chk_%=:\n"
        "buffer_load_dword v27, off, s[40:43], s65 sc1\n"
        "s_waitcnt vmcnt(0)\n"
        "v_readfirstlane_b32 s65, v27\n"
        "s_nop 0\n"
        "s_cmp_lg_u32 s65, 1\n"
        "s_cbranch_scc1 .Lr5_pg0_go_%=\n"
        "s_endpgm\n"
        ".Lr5_pg0_go_%=:\n"
        "s_sleep 2\n"
        "s_branch .Lr5_pg0_top_%=\n"
        ".Lr5_pg0_ok_%=:\n"
        "v_cmp_eq_u32 s[52:53], 1, v27\n"
        "s_nop 1\n"
        "s_mov_b32 s54, 40960\n"
        "s_mov_b32 s64, 0\n"
        ".Lr5_pg1_top_%=:\n"
        "buffer_load_dword v27, v28, s[40:43], s54 offen sc1\n"
        "s_waitcnt vmcnt(0)\n"
        "v_cmp_gt_i32 vcc, 0, v27\n"
        "s_cbranch_vccz .Lr5_pg1_ok_%=\n"
        "s_add_u32 s64, s64, 1\n"
        "s_and_b32 s65, s64, 255\n"
        "s_cbranch_scc1 .Lr5_pg1_go_%=\n"
        "s_mov_b32 s65, 49152\n"
        "s_cmp_lt_u32 s64, 1048576\n"
        "s_cbranch_scc1 .Lr5_pg1_chk_%=\n"
        "v_mov_b32 v27, 1\n"
        "buffer_store_dword v27, off, s[40:43], s65 sc1\n"
        "s_waitcnt vmcnt(0)\n"
        ".Lr5_pg1_chk_%=:\n"
        "buffer_load_dword v27, off, s[40:43], s65 sc1\n"
        "s_waitcnt vmcnt(0)\n"
        "v_readfirstlane_b32 s65, v27\n"
        "s_nop 0\n"
        "s_cmp_lg_u32 s65, 1\n"
        "s_cbranch_scc1 .Lr5_pg1_go_%=\n"
        "s_endpgm\n"
        ".Lr5_pg1_go_%=:\n"
        "s_sleep 2\n"
        "s_branch .Lr5_pg1_top_%=\n"
        ".Lr5_pg1_ok_%=:\n"
        "v_cmp_eq_u32 s[54:55], 1, v27\n"
        "s_nop 1\n"
        "s_mov_b32 vcc_lo, 0x55555555\n"
        "s_mov_b32 vcc_hi, 0x55555555\n"
        "s_xor_b64 vcc, vcc, s[52:53]\n"
        "s_bcnt1_i32_b64 s65, vcc\n"
        "s_sub_u32 s64, s65, 23\n"
        "s_cmp_lt_u32 s64, 19\n"
        "s_cbranch_scc1 .Lr5_rawmap_%=\n"
        "s_cmp_lt_u32 s65, 32\n"
        "s_cselect_b32 s65, 0, -1\n"
        "s_xor_b32 s52, s65, 0x55555555\n"
        "s_xor_b32 s53, s65, 0x55555555\n"
        "s_xor_b32 s54, s65, 0x55555555\n"
        "s_xor_b32 s55, s65, 0xaaaaaaaa\n"
        ".Lr5_rawmap_%=:\n"
        "s_xor_b32 s52, s52, s66\n"
        "s_xor_b32 s53, s53, s66\n"
        "s_xor_b32 s54, s54, s66\n"
        "s_xor_b32 s55, s55, s66\n"
        "s_and_b32 s69, s55, 1\n"
        "s_lshl_b32 s69, s69, 13\n"
        "s_xor_b32 s70, s69, 8192\n"
        "s_cmp_eq_u32 s50, 0\n"
        "s_cbranch_scc0 .Lr5_l1_%=\n"
        ".Lr5_code_%=:\n"
        ".Lr5_l0_%=:\n"
        "s_bitcmp1_b64 s[52:53], s68\n"
        "s_cselect_b32 s65, 8192, 0\n"
        "s_lshl_b32 s71, s68, 21\n"
        "s_add_u32 s71, s71, s65\n"
        "s_add_u32 s71, s71, 2097152\n"
        "s_lshr_b32 s65, s68, 2\n"
        "s_add_u32 s65, s65, 1\n"
        "s_bitcmp1_b32 s55, s65\n"
        "s_cselect_b32 s65, 8192, 0\n"
        "s_lshl_b32 s72, s68, 19\n"
        "s_add_u32 s72, s72, s65\n"
        "s_add_u32 s72, s72, 203423744\n"
        "s_add_u32 s76, s72, s61\n"
        "s_add_u32 s79, s71, s58\n"
        "s_add_u32 s79, s79, 1998848\n"
        "s_add_u32 s77, s72, s62\n"
        "s_add_u32 s80, s71, s59\n"
        "s_add_u32 s80, s80, 1998848\n"
        "s_add_u32 s78, s72, s63\n"
        "s_add_u32 s81, s71, s60\n"
        "s_add_u32 s81, s81, 1998848\n"
        "s_lshl_b32 s73, s68, 1\n"
        "s_add_u32 s73, s73, s74\n"
        "s_mov_b32 s64, 0\n"
        ".Lr5_l0w_top_%=:\n"
        "buffer_load_dword v24, v1, s[40:43], s69 offen sc1\n"
        "s_waitcnt vmcnt(0)\n"
        "v_cmp_gt_i32 vcc, s73, v24\n"
        "s_nop 0\n"
        "s_and_b64 vcc, vcc, 0xffff\n"
        "s_cbranch_vccz .Lr5_l0w_ok_%=\n"
        "s_add_u32 s64, s64, 1\n"
        "s_and_b32 s65, s64, 255\n"
        "s_cbranch_scc1 .Lr5_l0w_go_%=\n"
        "s_mov_b32 s65, 49152\n"
        "s_cmp_lt_u32 s64, 1048576\n"
        "s_cbranch_scc1 .Lr5_l0w_chk_%=\n"
        "v_mov_b32 v24, 1\n"
        "buffer_store_dword v24, off, s[40:43], s65 sc1\n"
        "s_waitcnt vmcnt(0)\n"
        ".Lr5_l0w_chk_%=:\n"
        "buffer_load_dword v24, off, s[40:43], s65 sc1\n"
        "s_waitcnt vmcnt(0)\n"
        "v_readfirstlane_b32 s65, v24\n"
        "s_nop 0\n"
        "s_cmp_lg_u32 s65, 1\n"
        "s_cbranch_scc1 .Lr5_l0w_go_%=\n"
        "s_endpgm\n"
        ".Lr5_l0w_go_%=:\n"
        "s_branch .Lr5_l0w_top_%=\n"
        ".Lr5_l0w_ok_%=:\n"
        "buffer_load_dwordx4 v[32:35], v3, s[40:43], s76 offen offset:0 sc1\n"
        "buffer_load_dwordx4 v[40:43], v3, s[40:43], s76 offen offset:512 sc1\n"
        "buffer_load_dwordx4 v[36:39], v3, s[40:43], s76 offen offset:256 sc1\n"
        "buffer_load_dwordx4 v[44:47], v3, s[40:43], s76 offen offset:768 sc1\n"
        "buffer_load_dwordx4 v[80:83], v0, s[40:43], s79 offen sc1\n"
        "buffer_load_dwordx4 v[48:51], v3, s[40:43], s77 offen offset:0 sc1\n"
        "buffer_load_dwordx4 v[56:59], v3, s[40:43], s77 offen offset:512 sc1\n"
        "buffer_load_dwordx4 v[52:55], v3, s[40:43], s77 offen offset:256 sc1\n"
        "buffer_load_dwordx4 v[60:63], v3, s[40:43], s77 offen offset:768 sc1\n"
        "buffer_load_dwordx4 v[84:87], v0, s[40:43], s80 offen sc1\n"
        "buffer_load_dwordx4 v[64:67], v4, s[40:43], s78 offen offset:0 sc1\n"
        "buffer_load_dwordx4 v[72:75], v4, s[40:43], s78 offen offset:512 sc1\n"
        "buffer_load_dwordx4 v[68:71], v4, s[40:43], s78 offen offset:256 sc1\n"
        "buffer_load_dwordx4 v[76:79], v4, s[40:43], s78 offen offset:768 sc1\n"
        "buffer_load_dwordx4 v[88:91], v0, s[40:43], s81 offen sc1\n"
        "s_xor_b32 s71, s71, 8192\n"
        "s_add_u32 s72, s71, 1835008\n"
        "s_add_u32 s71, s71, 1916928\n"
        "s_add_u32 s82, s72, s58\n"
        "s_add_u32 s85, s71, s58\n"
        "s_add_u32 s83, s72, s59\n"
        "s_add_u32 s86, s71, s59\n"
        "s_add_u32 s84, s72, s60\n"
        "s_add_u32 s87, s71, s60\n"
        "s_waitcnt vmcnt(14)\n"
        "v_mfma_f32_16x16x32_bf16 v[92:95], v[8:11], v[32:35], 0\n"
        "s_waitcnt vmcnt(13)\n"
        "v_mfma_f32_16x16x32_bf16 v[92:95], v[12:15], v[40:43], v[92:95]\n"
        "s_waitcnt vmcnt(12)\n"
        "v_mfma_f32_16x16x32_bf16 v[96:99], v[8:11], v[36:39], 0\n"
        "s_waitcnt vmcnt(11)\n"
        "v_mfma_f32_16x16x32_bf16 v[96:99], v[12:15], v[44:47], v[96:99]\n"
        "s_waitcnt vmcnt(10)\n"
        "v_mfma_f32_16x16x32_bf16 v[92:95], v[16:19], v[80:83], v[92:95]\n"
        "v_mfma_f32_16x16x32_bf16 v[96:99], v[20:23], v[80:83], v[96:99]\n"
        "s_waitcnt vmcnt(9)\n"
        "v_mfma_f32_16x16x32_bf16 v[100:103], v[8:11], v[48:51], 0\n"
        "s_waitcnt vmcnt(8)\n"
        "v_mfma_f32_16x16x32_bf16 v[100:103], v[12:15], v[56:59], v[100:103]\n"
        "s_waitcnt vmcnt(7)\n"
        "v_mfma_f32_16x16x32_bf16 v[104:107], v[8:11], v[52:55], 0\n"
        "s_waitcnt vmcnt(6)\n"
        "v_mfma_f32_16x16x32_bf16 v[104:107], v[12:15], v[60:63], v[104:107]\n"
        "s_waitcnt vmcnt(5)\n"
        "v_mfma_f32_16x16x32_bf16 v[100:103], v[16:19], v[84:87], v[100:103]\n"
        "v_mfma_f32_16x16x32_bf16 v[104:107], v[20:23], v[84:87], v[104:107]\n"
        "s_waitcnt vmcnt(4)\n"
        "v_mfma_f32_16x16x32_bf16 v[108:111], v[8:11], v[64:67], 0\n"
        "s_waitcnt vmcnt(3)\n"
        "v_mfma_f32_16x16x32_bf16 v[108:111], v[12:15], v[72:75], v[108:111]\n"
        "s_waitcnt vmcnt(2)\n"
        "v_mfma_f32_16x16x32_bf16 v[112:115], v[8:11], v[68:71], 0\n"
        "s_waitcnt vmcnt(1)\n"
        "v_mfma_f32_16x16x32_bf16 v[112:115], v[12:15], v[76:79], v[112:115]\n"
        "s_waitcnt vmcnt(0)\n"
        "v_mfma_f32_16x16x32_bf16 v[108:111], v[16:19], v[88:91], v[108:111]\n"
        "v_mfma_f32_16x16x32_bf16 v[112:115], v[20:23], v[88:91], v[112:115]\n"
        "s_add_u32 s68, s68, 1\n"
        "s_sub_u32 s68, s68, 1\n"
        "v_pk_add_f32 v[164:165], v[92:93], v[94:95]\n"
        "v_pk_add_f32 v[166:167], v[96:97], v[98:99]\n"
        "v_pk_add_f32 v[168:169], v[100:101], v[102:103]\n"
        "v_pk_add_f32 v[170:171], v[104:105], v[106:107]\n"
        "v_pk_add_f32 v[164:165], v[164:165], v[166:167]\n"
        "v_pk_add_f32 v[168:169], v[168:169], v[170:171]\n"
        "v_pk_add_f32 v[164:165], v[164:165], v[168:169]\n"
        "s_nop 0\n"
        "v_pk_add_f32 v[166:167], v[108:109], v[110:111]\n"
        "v_pk_add_f32 v[168:169], v[112:113], v[114:115]\n"
        "v_pk_add_f32 v[164:165], v[164:165], v[166:167]\n"
        "v_pk_add_f32 v[164:165], v[164:165], v[168:169]\n"
        "v_add_f32 v164, v164, v165\n"
        "v_cvt_pk_bf16_f32 v140, v92, v93\n"
        "v_cvt_pk_bf16_f32 v141, v94, v95\n"
        "v_add_f32_dpp v166, v164, v164 row_ror:8 row_mask:0xf bank_mask:0xf\n"
        "v_add_f32_dpp v167, v164, v164 row_ror:8 row_mask:0xf bank_mask:0xf\n"
        "v_cvt_pk_bf16_f32 v142, v96, v97\n"
        "v_cvt_pk_bf16_f32 v143, v98, v99\n"
        "v_permlane16_swap_b32 v166, v167\n"
        "v_add_f32 v164, v166, v167\n"
        "v_add_f32 v166, v166, v167\n"
        "v_cvt_pk_bf16_f32 v144, v100, v101\n"
        "v_cvt_pk_bf16_f32 v145, v102, v103\n"
        "v_permlane32_swap_b32 v164, v166\n"
        "v_add_f32 v164, v164, v166\n"
        "v_mul_f32 v164, 0x3baaaaab, v164\n"
        "v_pk_add_f32 v[116:117], v[92:93], v[164:165] op_sel_hi:[1,0] neg_lo:[0,1] neg_hi:[0,1]\n"
        "v_pk_add_f32 v[118:119], v[94:95], v[164:165] op_sel_hi:[1,0] neg_lo:[0,1] neg_hi:[0,1]\n"
        "v_pk_add_f32 v[120:121], v[96:97], v[164:165] op_sel_hi:[1,0] neg_lo:[0,1] neg_hi:[0,1]\n"
        "v_pk_add_f32 v[122:123], v[98:99], v[164:165] op_sel_hi:[1,0] neg_lo:[0,1] neg_hi:[0,1]\n"
        "v_pk_add_f32 v[124:125], v[100:101], v[164:165] op_sel_hi:[1,0] neg_lo:[0,1] neg_hi:[0,1]\n"
        "v_pk_add_f32 v[126:127], v[102:103], v[164:165] op_sel_hi:[1,0] neg_lo:[0,1] neg_hi:[0,1]\n"
        "v_pk_add_f32 v[128:129], v[104:105], v[164:165] op_sel_hi:[1,0] neg_lo:[0,1] neg_hi:[0,1]\n"
        "v_pk_add_f32 v[130:131], v[106:107], v[164:165] op_sel_hi:[1,0] neg_lo:[0,1] neg_hi:[0,1]\n"
        "v_pk_add_f32 v[132:133], v[108:109], v[164:165] op_sel_hi:[1,0] neg_lo:[0,1] neg_hi:[0,1]\n"
        "v_pk_add_f32 v[134:135], v[110:111], v[164:165] op_sel_hi:[1,0] neg_lo:[0,1] neg_hi:[0,1]\n"
        "v_pk_add_f32 v[136:137], v[112:113], v[164:165] op_sel_hi:[1,0] neg_lo:[0,1] neg_hi:[0,1]\n"
        "v_pk_add_f32 v[138:139], v[114:115], v[164:165] op_sel_hi:[1,0] neg_lo:[0,1] neg_hi:[0,1]\n"
        "v_pk_mul_f32 v[166:167], v[116:117], v[116:117]\n"
        "v_pk_mul_f32 v[168:169], v[118:119], v[118:119]\n"
        "v_pk_fma_f32 v[166:167], v[120:121], v[120:121], v[166:167]\n"
        "v_pk_fma_f32 v[168:169], v[122:123], v[122:123], v[168:169]\n"
        "v_pk_fma_f32 v[166:167], v[124:125], v[124:125], v[166:167]\n"
        "v_pk_fma_f32 v[168:169], v[126:127], v[126:127], v[168:169]\n"
        "v_pk_fma_f32 v[166:167], v[128:129], v[128:129], v[166:167]\n"
        "v_pk_fma_f32 v[168:169], v[130:131], v[130:131], v[168:169]\n"
        "v_pk_fma_f32 v[166:167], v[132:133], v[132:133], v[166:167]\n"
        "v_pk_fma_f32 v[168:169], v[134:135], v[134:135], v[168:169]\n"
        "v_pk_fma_f32 v[166:167], v[136:137], v[136:137], v[166:167]\n"
        "v_pk_fma_f32 v[168:169], v[138:139], v[138:139], v[168:169]\n"
        "v_pk_add_f32 v[166:167], v[166:167], v[168:169]\n"
        "v_add_f32 v165, v166, v167\n"
        "v_cvt_pk_bf16_f32 v146, v104, v105\n"
        "v_cvt_pk_bf16_f32 v147, v106, v107\n"
        "v_add_f32_dpp v166, v165, v165 row_ror:8 row_mask:0xf bank_mask:0xf\n"
        "v_add_f32_dpp v167, v165, v165 row_ror:8 row_mask:0xf bank_mask:0xf\n"
        "v_cvt_pk_bf16_f32 v148, v108, v109\n"
        "v_cvt_pk_bf16_f32 v149, v110, v111\n"
        "v_permlane16_swap_b32 v166, v167\n"
        "v_add_f32 v165, v166, v167\n"
        "v_add_f32 v166, v166, v167\n"
        "v_cvt_pk_bf16_f32 v150, v112, v113\n"
        "v_cvt_pk_bf16_f32 v151, v114, v115\n"
        "v_permlane32_swap_b32 v165, v166\n"
        "v_add_f32 v165, v165, v166\n"
        "ds_write_b64 v6, v[164:165]\n"
        "v_permlane16_swap_b32 v140, v142\n"
        "v_permlane16_swap_b32 v141, v143\n"
        "v_permlane16_swap_b32 v144, v146\n"
        "v_permlane16_swap_b32 v145, v147\n"
        "s_waitcnt lgkmcnt(0)\n"
        "s_barrier\n"
        "ds_read_b64 v[166:167], v7 offset:0\n"
        "ds_read_b64 v[168:169], v7 offset:64\n"
        "ds_read_b64 v[170:171], v7 offset:128\n"
        "v_permlane16_swap_b32 v148, v150\n"
        "v_permlane16_swap_b32 v149, v151\n"
        "buffer_store_dwordx4 v[140:143], v0, s[40:43], s85 offen sc1\n"
        "buffer_store_dwordx4 v[144:147], v0, s[40:43], s86 offen sc1\n"
        "buffer_store_dwordx4 v[148:151], v0, s[40:43], s87 offen sc1\n"
        "s_waitcnt lgkmcnt(0)\n"
        "v_add_f32 v172, v166, v168\n"
        "v_add_f32 v173, v167, v169\n"
        "v_add_f32 v172, v172, v170\n"
        "v_add_f32 v173, v173, v171\n"
        "v_mul_f32 v172, 0x3eaaaaab, v172\n"
        "v_sub_f32 v166, v166, v172\n"
        "v_sub_f32 v168, v168, v172\n"
        "v_sub_f32 v170, v170, v172\n"
        "v_mul_f32 v166, v166, v166\n"
        "v_fmac_f32 v166, v168, v168\n"
        "v_fmac_f32 v166, v170, v170\n"
        "v_fmamk_f32 v173, v166, 0x43400000, v173\n"
        "v_mul_f32 v173, 0x3ae38e39, v173\n"
        "v_cvt_pk_bf16_f32 v172, v172, v173\n"
        "v_and_b32 v173, 0xffff0000, v172\n"
        "v_lshlrev_b32 v172, 16, v172\n"
        "v_add_f32 v173, 0x3727c5ac, v173\n"
        "v_cvt_pk_bf16_f32 v173, v173, v173\n"
        "v_lshlrev_b32 v173, 16, v173\n"
        "v_sqrt_f32 v173, v173\n"
        "v_sub_f32 v172, v164, v172\n"
        "v_cvt_pk_bf16_f32 v173, v173, v173\n"
        "v_lshlrev_b32 v173, 16, v173\n"
        "v_rcp_f32 v173, v173\n"
        "v_mov_b32 v25, s68\n"
        "v_mul_f32 v172, v172, v173\n"
        "v_pk_fma_f32 v[116:117], v[116:117], v[172:173], v[172:173] op_sel:[0,1,0] op_sel_hi:[1,1,0]\n"
        "v_pk_fma_f32 v[118:119], v[118:119], v[172:173], v[172:173] op_sel:[0,1,0] op_sel_hi:[1,1,0]\n"
        "v_pk_fma_f32 v[120:121], v[120:121], v[172:173], v[172:173] op_sel:[0,1,0] op_sel_hi:[1,1,0]\n"
        "v_pk_fma_f32 v[122:123], v[122:123], v[172:173], v[172:173] op_sel:[0,1,0] op_sel_hi:[1,1,0]\n"
        "v_cvt_pk_bf16_f32 v152, v116, v117\n"
        "v_cvt_pk_bf16_f32 v153, v118, v119\n"
        "v_cvt_pk_bf16_f32 v154, v120, v121\n"
        "v_cvt_pk_bf16_f32 v155, v122, v123\n"
        "s_nop 0\n"
        "v_permlane16_swap_b32 v152, v154\n"
        "v_permlane16_swap_b32 v153, v155\n"
        "buffer_store_dwordx4 v[152:155], v0, s[40:43], s82 offen sc1\n"
        "v_pk_fma_f32 v[124:125], v[124:125], v[172:173], v[172:173] op_sel:[0,1,0] op_sel_hi:[1,1,0]\n"
        "v_pk_fma_f32 v[126:127], v[126:127], v[172:173], v[172:173] op_sel:[0,1,0] op_sel_hi:[1,1,0]\n"
        "v_pk_fma_f32 v[128:129], v[128:129], v[172:173], v[172:173] op_sel:[0,1,0] op_sel_hi:[1,1,0]\n"
        "v_pk_fma_f32 v[130:131], v[130:131], v[172:173], v[172:173] op_sel:[0,1,0] op_sel_hi:[1,1,0]\n"
        "v_cvt_pk_bf16_f32 v156, v124, v125\n"
        "v_cvt_pk_bf16_f32 v157, v126, v127\n"
        "v_cvt_pk_bf16_f32 v158, v128, v129\n"
        "v_cvt_pk_bf16_f32 v159, v130, v131\n"
        "s_nop 0\n"
        "v_permlane16_swap_b32 v156, v158\n"
        "v_permlane16_swap_b32 v157, v159\n"
        "buffer_store_dwordx4 v[156:159], v0, s[40:43], s83 offen sc1\n"
        "v_pk_fma_f32 v[132:133], v[132:133], v[172:173], v[172:173] op_sel:[0,1,0] op_sel_hi:[1,1,0]\n"
        "v_pk_fma_f32 v[134:135], v[134:135], v[172:173], v[172:173] op_sel:[0,1,0] op_sel_hi:[1,1,0]\n"
        "v_pk_fma_f32 v[136:137], v[136:137], v[172:173], v[172:173] op_sel:[0,1,0] op_sel_hi:[1,1,0]\n"
        "v_pk_fma_f32 v[138:139], v[138:139], v[172:173], v[172:173] op_sel:[0,1,0] op_sel_hi:[1,1,0]\n"
        "v_cvt_pk_bf16_f32 v160, v132, v133\n"
        "v_cvt_pk_bf16_f32 v161, v134, v135\n"
        "v_cvt_pk_bf16_f32 v162, v136, v137\n"
        "v_cvt_pk_bf16_f32 v163, v138, v139\n"
        "s_nop 0\n"
        "v_permlane16_swap_b32 v160, v162\n"
        "v_permlane16_swap_b32 v161, v163\n"
        "buffer_store_dwordx4 v[160:163], v0, s[40:43], s84 offen sc1\n"
        "s_waitcnt vmcnt(0)\n"
        "s_barrier\n"
        "buffer_store_dword v25, v2, s[40:43], s70 offen sc1\n"
        "s_add_u32 s68, s68, 1\n"
        "s_cmp_lt_u32 s68, 64\n"
        "s_cbranch_scc1 .Lr5_l0_%=\n"
        "s_endpgm\n"
        ".Lr5_l1_%=:\n"
        "s_bitcmp1_b64 s[52:53], s68\n"
        "s_cselect_b32 s65, 8192, 0\n"
        "s_lshl_b32 s71, s68, 21\n"
        "s_add_u32 s71, s71, s65\n"
        "s_add_u32 s71, s71, 2097152\n"
        "s_xor_b32 s71, s71, 8192\n"
        "s_add_u32 s72, s71, 1835008\n"
        "s_add_u32 s71, s71, 1916928\n"
        "s_add_u32 s82, s72, s58\n"
        "s_add_u32 s85, s71, s58\n"
        "s_add_u32 s83, s72, s59\n"
        "s_add_u32 s86, s71, s59\n"
        "s_add_u32 s84, s72, s60\n"
        "s_add_u32 s87, s71, s60\n"
        "s_waitcnt vmcnt(0)\n"
        "v_mfma_f32_16x16x32_bf16 v[92:95], v[16:19], v[80:83], 0\n"
        "v_mfma_f32_16x16x32_bf16 v[96:99], v[20:23], v[80:83], 0\n"
        "v_mfma_f32_16x16x32_bf16 v[100:103], v[16:19], v[84:87], 0\n"
        "v_mfma_f32_16x16x32_bf16 v[104:107], v[20:23], v[84:87], 0\n"
        "v_mfma_f32_16x16x32_bf16 v[108:111], v[16:19], v[88:91], 0\n"
        "v_mfma_f32_16x16x32_bf16 v[112:115], v[20:23], v[88:91], 0\n"
        "s_add_u32 s68, s68, 1\n"
        "s_mul_i32 s65, s68, 36864\n"
        "buffer_load_dwordx4 v[80:83], v5, s[44:47], s65 offen offset:0\n"
        "buffer_load_dwordx4 v[84:87], v5, s[44:47], s65 offen offset:128\n"
        "buffer_load_dwordx4 v[88:91], v5, s[44:47], s65 offen offset:256\n"
        "s_sub_u32 s68, s68, 1\n"
        "v_pk_add_f32 v[164:165], v[92:93], v[94:95]\n"
        "v_pk_add_f32 v[166:167], v[96:97], v[98:99]\n"
        "v_pk_add_f32 v[168:169], v[100:101], v[102:103]\n"
        "v_pk_add_f32 v[170:171], v[104:105], v[106:107]\n"
        "v_pk_add_f32 v[164:165], v[164:165], v[166:167]\n"
        "v_pk_add_f32 v[168:169], v[168:169], v[170:171]\n"
        "v_pk_add_f32 v[164:165], v[164:165], v[168:169]\n"
        "v_pk_add_f32 v[166:167], v[108:109], v[110:111]\n"
        "v_pk_add_f32 v[168:169], v[112:113], v[114:115]\n"
        "v_pk_add_f32 v[164:165], v[164:165], v[166:167]\n"
        "v_pk_add_f32 v[164:165], v[164:165], v[168:169]\n"
        "v_add_f32 v164, v164, v165\n"
        "s_nop 1\n"
        "v_add_f32_dpp v166, v164, v164 row_ror:8 row_mask:0xf bank_mask:0xf\n"
        "v_add_f32_dpp v167, v164, v164 row_ror:8 row_mask:0xf bank_mask:0xf\n"
        "s_nop 1\n"
        "v_permlane16_swap_b32 v166, v167\n"
        "v_add_f32 v164, v166, v167\n"
        "v_add_f32 v166, v166, v167\n"
        "s_nop 1\n"
        "v_permlane32_swap_b32 v164, v166\n"
        "v_add_f32 v164, v164, v166\n"
        "v_mul_f32 v164, 0x3baaaaab, v164\n"
        "v_pk_add_f32 v[116:117], v[92:93], v[164:165] op_sel_hi:[1,0] neg_lo:[0,1] neg_hi:[0,1]\n"
        "v_pk_add_f32 v[118:119], v[94:95], v[164:165] op_sel_hi:[1,0] neg_lo:[0,1] neg_hi:[0,1]\n"
        "v_pk_add_f32 v[120:121], v[96:97], v[164:165] op_sel_hi:[1,0] neg_lo:[0,1] neg_hi:[0,1]\n"
        "v_pk_add_f32 v[122:123], v[98:99], v[164:165] op_sel_hi:[1,0] neg_lo:[0,1] neg_hi:[0,1]\n"
        "v_pk_add_f32 v[124:125], v[100:101], v[164:165] op_sel_hi:[1,0] neg_lo:[0,1] neg_hi:[0,1]\n"
        "v_pk_add_f32 v[126:127], v[102:103], v[164:165] op_sel_hi:[1,0] neg_lo:[0,1] neg_hi:[0,1]\n"
        "v_pk_add_f32 v[128:129], v[104:105], v[164:165] op_sel_hi:[1,0] neg_lo:[0,1] neg_hi:[0,1]\n"
        "v_pk_add_f32 v[130:131], v[106:107], v[164:165] op_sel_hi:[1,0] neg_lo:[0,1] neg_hi:[0,1]\n"
        "v_pk_add_f32 v[132:133], v[108:109], v[164:165] op_sel_hi:[1,0] neg_lo:[0,1] neg_hi:[0,1]\n"
        "v_pk_add_f32 v[134:135], v[110:111], v[164:165] op_sel_hi:[1,0] neg_lo:[0,1] neg_hi:[0,1]\n"
        "v_pk_add_f32 v[136:137], v[112:113], v[164:165] op_sel_hi:[1,0] neg_lo:[0,1] neg_hi:[0,1]\n"
        "v_pk_add_f32 v[138:139], v[114:115], v[164:165] op_sel_hi:[1,0] neg_lo:[0,1] neg_hi:[0,1]\n"
        "v_pk_mul_f32 v[166:167], v[116:117], v[116:117]\n"
        "v_pk_mul_f32 v[168:169], v[118:119], v[118:119]\n"
        "v_pk_fma_f32 v[166:167], v[120:121], v[120:121], v[166:167]\n"
        "v_pk_fma_f32 v[168:169], v[122:123], v[122:123], v[168:169]\n"
        "v_pk_fma_f32 v[166:167], v[124:125], v[124:125], v[166:167]\n"
        "v_pk_fma_f32 v[168:169], v[126:127], v[126:127], v[168:169]\n"
        "v_pk_fma_f32 v[166:167], v[128:129], v[128:129], v[166:167]\n"
        "v_pk_fma_f32 v[168:169], v[130:131], v[130:131], v[168:169]\n"
        "v_pk_fma_f32 v[166:167], v[132:133], v[132:133], v[166:167]\n"
        "v_pk_fma_f32 v[168:169], v[134:135], v[134:135], v[168:169]\n"
        "v_pk_fma_f32 v[166:167], v[136:137], v[136:137], v[166:167]\n"
        "v_pk_fma_f32 v[168:169], v[138:139], v[138:139], v[168:169]\n"
        "v_pk_add_f32 v[166:167], v[166:167], v[168:169]\n"
        "v_add_f32 v165, v166, v167\n"
        "s_nop 1\n"
        "v_add_f32_dpp v166, v165, v165 row_ror:8 row_mask:0xf bank_mask:0xf\n"
        "v_add_f32_dpp v167, v165, v165 row_ror:8 row_mask:0xf bank_mask:0xf\n"
        "s_nop 1\n"
        "v_permlane16_swap_b32 v166, v167\n"
        "v_add_f32 v165, v166, v167\n"
        "v_add_f32 v166, v166, v167\n"
        "s_nop 1\n"
        "v_permlane32_swap_b32 v165, v166\n"
        "v_add_f32 v165, v165, v166\n"
        "ds_write_b64 v6, v[164:165]\n"
        "s_waitcnt lgkmcnt(0)\n"
        "s_barrier\n"
        "ds_read_b64 v[166:167], v7 offset:0\n"
        "ds_read_b64 v[168:169], v7 offset:64\n"
        "ds_read_b64 v[170:171], v7 offset:128\n"
        "s_waitcnt lgkmcnt(0)\n"
        "v_add_f32 v172, v166, v168\n"
        "v_add_f32 v173, v167, v169\n"
        "v_add_f32 v172, v172, v170\n"
        "v_add_f32 v173, v173, v171\n"
        "v_mul_f32 v172, 0x3eaaaaab, v172\n"
        "v_sub_f32 v166, v166, v172\n"
        "v_sub_f32 v168, v168, v172\n"
        "v_sub_f32 v170, v170, v172\n"
        "v_mul_f32 v166, v166, v166\n"
        "v_fmac_f32 v166, v168, v168\n"
        "v_fmac_f32 v166, v170, v170\n"
        "v_fmamk_f32 v173, v166, 0x43400000, v173\n"
        "v_mul_f32 v173, 0x3ae38e39, v173\n"
        "v_cvt_pk_bf16_f32 v172, v172, v173\n"
        "v_and_b32 v173, 0xffff0000, v172\n"
        "v_lshlrev_b32 v172, 16, v172\n"
        "v_add_f32 v173, 0x3727c5ac, v173\n"
        "v_cvt_pk_bf16_f32 v173, v173, v173\n"
        "v_lshlrev_b32 v173, 16, v173\n"
        "v_sqrt_f32 v173, v173\n"
        "v_sub_f32 v172, v164, v172\n"
        "v_cvt_pk_bf16_f32 v173, v173, v173\n"
        "v_lshlrev_b32 v173, 16, v173\n"
        "v_rcp_f32 v173, v173\n"
        "v_mov_b32 v25, s68\n"
        "v_mul_f32 v172, v172, v173\n"
        "v_pk_fma_f32 v[116:117], v[116:117], v[172:173], v[172:173] op_sel:[0,1,0] op_sel_hi:[1,1,0]\n"
        "v_pk_fma_f32 v[118:119], v[118:119], v[172:173], v[172:173] op_sel:[0,1,0] op_sel_hi:[1,1,0]\n"
        "v_pk_fma_f32 v[120:121], v[120:121], v[172:173], v[172:173] op_sel:[0,1,0] op_sel_hi:[1,1,0]\n"
        "v_pk_fma_f32 v[122:123], v[122:123], v[172:173], v[172:173] op_sel:[0,1,0] op_sel_hi:[1,1,0]\n"
        "v_cvt_pk_bf16_f32 v152, v116, v117\n"
        "v_cvt_pk_bf16_f32 v153, v118, v119\n"
        "v_cvt_pk_bf16_f32 v154, v120, v121\n"
        "v_cvt_pk_bf16_f32 v155, v122, v123\n"
        "s_nop 0\n"
        "v_permlane16_swap_b32 v152, v154\n"
        "v_permlane16_swap_b32 v153, v155\n"
        "buffer_store_dwordx4 v[152:155], v0, s[40:43], s82 offen sc1\n"
        "v_pk_fma_f32 v[124:125], v[124:125], v[172:173], v[172:173] op_sel:[0,1,0] op_sel_hi:[1,1,0]\n"
        "v_pk_fma_f32 v[126:127], v[126:127], v[172:173], v[172:173] op_sel:[0,1,0] op_sel_hi:[1,1,0]\n"
        "v_pk_fma_f32 v[128:129], v[128:129], v[172:173], v[172:173] op_sel:[0,1,0] op_sel_hi:[1,1,0]\n"
        "v_pk_fma_f32 v[130:131], v[130:131], v[172:173], v[172:173] op_sel:[0,1,0] op_sel_hi:[1,1,0]\n"
        "v_cvt_pk_bf16_f32 v156, v124, v125\n"
        "v_cvt_pk_bf16_f32 v157, v126, v127\n"
        "v_cvt_pk_bf16_f32 v158, v128, v129\n"
        "v_cvt_pk_bf16_f32 v159, v130, v131\n"
        "s_nop 0\n"
        "v_permlane16_swap_b32 v156, v158\n"
        "v_permlane16_swap_b32 v157, v159\n"
        "buffer_store_dwordx4 v[156:159], v0, s[40:43], s83 offen sc1\n"
        "v_pk_fma_f32 v[132:133], v[132:133], v[172:173], v[172:173] op_sel:[0,1,0] op_sel_hi:[1,1,0]\n"
        "v_pk_fma_f32 v[134:135], v[134:135], v[172:173], v[172:173] op_sel:[0,1,0] op_sel_hi:[1,1,0]\n"
        "v_pk_fma_f32 v[136:137], v[136:137], v[172:173], v[172:173] op_sel:[0,1,0] op_sel_hi:[1,1,0]\n"
        "v_pk_fma_f32 v[138:139], v[138:139], v[172:173], v[172:173] op_sel:[0,1,0] op_sel_hi:[1,1,0]\n"
        "v_cvt_pk_bf16_f32 v160, v132, v133\n"
        "v_cvt_pk_bf16_f32 v161, v134, v135\n"
        "v_cvt_pk_bf16_f32 v162, v136, v137\n"
        "v_cvt_pk_bf16_f32 v163, v138, v139\n"
        "s_nop 0\n"
        "v_permlane16_swap_b32 v160, v162\n"
        "v_permlane16_swap_b32 v161, v163\n"
        "buffer_store_dwordx4 v[160:163], v0, s[40:43], s84 offen sc1\n"
        "s_waitcnt vmcnt(0)\n"
        "s_barrier\n"
        "buffer_store_dword v25, v2, s[40:43], s70 offen sc1\n"
        "s_add_u32 s68, s68, 1\n"
        "s_cmp_lt_u32 s68, 64\n"
        "s_cbranch_scc1 .Lr5_l1_%=\n"
        "s_mov_b32 s68, 0\n"
        "s_mov_b64 s[44:45], %2\n"
        "s_and_b32 s45, s45, 0xffff\n"
        ".Lr5_fin_%=:\n"
        "s_bitcmp1_b64 s[52:53], s68\n"
        "s_cselect_b32 s65, 8192, 0\n"
        "s_lshl_b32 s71, s68, 21\n"
        "s_add_u32 s71, s71, s65\n"
        "s_add_u32 s71, s71, 2097152\n"
        "s_lshr_b32 s65, s68, 2\n"
        "s_add_u32 s65, s65, 1\n"
        "s_bitcmp1_b32 s55, s65\n"
        "s_cselect_b32 s65, 8192, 0\n"
        "s_lshl_b32 s72, s68, 19\n"
        "s_add_u32 s72, s72, s65\n"
        "s_add_u32 s72, s72, 203423744\n"
        "s_add_u32 s76, s72, s61\n"
        "s_add_u32 s79, s71, s58\n"
        "s_add_u32 s79, s79, 1998848\n"
        "s_add_u32 s77, s72, s62\n"
        "s_add_u32 s80, s71, s59\n"
        "s_add_u32 s80, s80, 1998848\n"
        "s_add_u32 s78, s72, s63\n"
        "s_add_u32 s81, s71, s60\n"
        "s_add_u32 s81, s81, 1998848\n"
        "s_lshl_b32 s73, s68, 1\n"
        "s_add_u32 s73, s73, s74\n"
        "s_mov_b32 s64, 0\n"
        ".Lr5_finw_top_%=:\n"
        "buffer_load_dword v24, v1, s[40:43], s69 offen sc1\n"
        "s_waitcnt vmcnt(0)\n"
        "v_cmp_gt_i32 vcc, s73, v24\n"
        "s_nop 0\n"
        "s_and_b64 vcc, vcc, 0xffff\n"
        "s_cbranch_vccz .Lr5_finw_ok_%=\n"
        "s_add_u32 s64, s64, 1\n"
        "s_and_b32 s65, s64, 255\n"
        "s_cbranch_scc1 .Lr5_finw_go_%=\n"
        "s_mov_b32 s65, 49152\n"
        "s_cmp_lt_u32 s64, 1048576\n"
        "s_cbranch_scc1 .Lr5_finw_chk_%=\n"
        "v_mov_b32 v24, 1\n"
        "buffer_store_dword v24, off, s[40:43], s65 sc1\n"
        "s_waitcnt vmcnt(0)\n"
        ".Lr5_finw_chk_%=:\n"
        "buffer_load_dword v24, off, s[40:43], s65 sc1\n"
        "s_waitcnt vmcnt(0)\n"
        "v_readfirstlane_b32 s65, v24\n"
        "s_nop 0\n"
        "s_cmp_lg_u32 s65, 1\n"
        "s_cbranch_scc1 .Lr5_finw_go_%=\n"
        "s_endpgm\n"
        ".Lr5_finw_go_%=:\n"
        "s_branch .Lr5_finw_top_%=\n"
        ".Lr5_finw_ok_%=:\n"
        "s_cmp_eq_u32 s68, 63\n"
        "s_cselect_b32 s65, 0, 241172480\n"
        "v_mov_b32 v25, 1\n"
        "buffer_store_dword v25, v203, s[40:43], s65 offen sc1\n"
        "buffer_load_dwordx4 v[32:35], v3, s[40:43], s76 offen offset:0 sc1\n"
        "buffer_load_dwordx4 v[40:43], v3, s[40:43], s76 offen offset:512 sc1\n"
        "buffer_load_dwordx4 v[36:39], v3, s[40:43], s76 offen offset:256 sc1\n"
        "buffer_load_dwordx4 v[44:47], v3, s[40:43], s76 offen offset:768 sc1\n"
        "buffer_load_dwordx4 v[80:83], v0, s[40:43], s79 offen sc1\n"
        "buffer_load_dwordx4 v[48:51], v3, s[40:43], s77 offen offset:0 sc1\n"
        "buffer_load_dwordx4 v[56:59], v3, s[40:43], s77 offen offset:512 sc1\n"
        "buffer_load_dwordx4 v[52:55], v3, s[40:43], s77 offen offset:256 sc1\n"
        "buffer_load_dwordx4 v[60:63], v3, s[40:43], s77 offen offset:768 sc1\n"
        "buffer_load_dwordx4 v[84:87], v0, s[40:43], s80 offen sc1\n"
        "buffer_load_dwordx4 v[64:67], v4, s[40:43], s78 offen offset:0 sc1\n"
        "buffer_load_dwordx4 v[72:75], v4, s[40:43], s78 offen offset:512 sc1\n"
        "buffer_load_dwordx4 v[68:71], v4, s[40:43], s78 offen offset:256 sc1\n"
        "buffer_load_dwordx4 v[76:79], v4, s[40:43], s78 offen offset:768 sc1\n"
        "buffer_load_dwordx4 v[88:91], v0, s[40:43], s81 offen sc1\n"
        "s_waitcnt vmcnt(14)\n"
        "v_mfma_f32_16x16x32_bf16 v[92:95], v[8:11], v[32:35], 0\n"
        "s_waitcnt vmcnt(13)\n"
        "v_mfma_f32_16x16x32_bf16 v[92:95], v[12:15], v[40:43], v[92:95]\n"
        "s_waitcnt vmcnt(12)\n"
        "v_mfma_f32_16x16x32_bf16 v[96:99], v[8:11], v[36:39], 0\n"
        "s_waitcnt vmcnt(11)\n"
        "v_mfma_f32_16x16x32_bf16 v[96:99], v[12:15], v[44:47], v[96:99]\n"
        "s_waitcnt vmcnt(10)\n"
        "v_mfma_f32_16x16x32_bf16 v[92:95], v[16:19], v[80:83], v[92:95]\n"
        "v_mfma_f32_16x16x32_bf16 v[96:99], v[20:23], v[80:83], v[96:99]\n"
        "s_waitcnt vmcnt(9)\n"
        "v_mfma_f32_16x16x32_bf16 v[100:103], v[8:11], v[48:51], 0\n"
        "s_waitcnt vmcnt(8)\n"
        "v_mfma_f32_16x16x32_bf16 v[100:103], v[12:15], v[56:59], v[100:103]\n"
        "s_waitcnt vmcnt(7)\n"
        "v_mfma_f32_16x16x32_bf16 v[104:107], v[8:11], v[52:55], 0\n"
        "s_waitcnt vmcnt(6)\n"
        "v_mfma_f32_16x16x32_bf16 v[104:107], v[12:15], v[60:63], v[104:107]\n"
        "s_waitcnt vmcnt(5)\n"
        "v_mfma_f32_16x16x32_bf16 v[100:103], v[16:19], v[84:87], v[100:103]\n"
        "v_mfma_f32_16x16x32_bf16 v[104:107], v[20:23], v[84:87], v[104:107]\n"
        "s_waitcnt vmcnt(4)\n"
        "v_mfma_f32_16x16x32_bf16 v[108:111], v[8:11], v[64:67], 0\n"
        "s_waitcnt vmcnt(3)\n"
        "v_mfma_f32_16x16x32_bf16 v[108:111], v[12:15], v[72:75], v[108:111]\n"
        "s_waitcnt vmcnt(2)\n"
        "v_mfma_f32_16x16x32_bf16 v[112:115], v[8:11], v[68:71], 0\n"
        "s_waitcnt vmcnt(1)\n"
        "v_mfma_f32_16x16x32_bf16 v[112:115], v[12:15], v[76:79], v[112:115]\n"
        "s_waitcnt vmcnt(0)\n"
        "v_mfma_f32_16x16x32_bf16 v[108:111], v[16:19], v[88:91], v[108:111]\n"
        "v_mfma_f32_16x16x32_bf16 v[112:115], v[20:23], v[88:91], v[112:115]\n"
        "s_mul_i32 s65, s68, 36864\n"
        "v_cvt_pk_bf16_f32 v140, v92, v93\n"
        "v_cvt_pk_bf16_f32 v141, v94, v95\n"
        "v_cvt_pk_bf16_f32 v142, v96, v97\n"
        "v_cvt_pk_bf16_f32 v143, v98, v99\n"
        "s_nop 0\n"
        "v_permlane16_swap_b32 v140, v142\n"
        "v_permlane16_swap_b32 v141, v143\n"
        "buffer_store_dwordx4 v[140:143], v5, s[44:47], s65 offen offset:0 sc1\n"
        "v_cvt_pk_bf16_f32 v144, v100, v101\n"
        "v_cvt_pk_bf16_f32 v145, v102, v103\n"
        "v_cvt_pk_bf16_f32 v146, v104, v105\n"
        "v_cvt_pk_bf16_f32 v147, v106, v107\n"
        "s_nop 0\n"
        "v_permlane16_swap_b32 v144, v146\n"
        "v_permlane16_swap_b32 v145, v147\n"
        "buffer_store_dwordx4 v[144:147], v5, s[44:47], s65 offen offset:128 sc1\n"
        "s_nop 0\n"
        "v_cvt_pk_bf16_f32 v148, v108, v109\n"
        "v_cvt_pk_bf16_f32 v149, v110, v111\n"
        "v_cvt_pk_bf16_f32 v150, v112, v113\n"
        "v_cvt_pk_bf16_f32 v151, v114, v115\n"
        "s_nop 0\n"
        "v_permlane16_swap_b32 v148, v150\n"
        "v_permlane16_swap_b32 v149, v151\n"
        "buffer_store_dwordx4 v[148:151], v5, s[44:47], s65 offen offset:256 sc1\n"
        "s_add_u32 s68, s68, 1\n"
        "s_cmp_lt_u32 s68, 64\n"
        "s_cbranch_scc1 .Lr5_fin_%=\n"
        "s_barrier\n"
        "s_mov_b32 s65, 49280\n"
        "buffer_store_dwordx2 v[200:201], v202, s[40:43], s65 offen sc1\n"
        "s_endpgm\n"
        ".Lr5_end_%=:\n"
            RED_ARGS : "v0", "v1", "v2", "v3", "v4", "v5", "v6", "v7", "v8", "v9", "v10", "v11", "v12", "v13", "v14", "v15", "v16", "v17", "v18", "v19", "v20", "v21", "v22", "v23", "v24", "v25", "v26", "v27", "v28", "v29", "v30", "v31", "v32", "v33", "v34", "v35", "v36", "v37", "v38", "v39", "v40", "v41", "v42", "v43", "v44", "v45", "v46", "v47", "v48", "v49", "v50", "v51", "v52", "v53", "v54", "v55", "v56", "v57", "v58", "v59", "v60", "v61", "v62", "v63", "v64", "v65", "v66", "v67", "v68", "v69", "v70", "v71", "v72", "v73", "v74", "v75", "v76", "v77", "v78", "v79", "v255", "a255", "s26", "s27", "s28", "s29", "s30", "s31", "s32", "s33", "s34", "s35", "s36", "s37", "s38", "s39", "s40", "s41", "s42", "s43", "s44", "s45", "s46", "s47", "s48", "s49", "s50", "s51", "s52", "s53", "s54", "s55", "s56", "s57", "s58", "s59", "s60", "s61", "s62", "s63", "s64", "s65", "s66", "s67", "s68", "s69", "s70", "s71", "s72", "s73", "s74", "s75", "s76", "s77", "s78", "s79", "s80", "s81", "s82", "s83", "s84", "s85", "s86", "s87", "s88", "s89", "s90", "s91", "s92", "s93", "s94", "s95", "s96", "s97", "s98", "s99", "vcc", "scc", "memory");
    } else {
        asm volatile(
        "s_nop 4\n"
        "s_mov_b64 s[40:41], %0\n"
        "s_and_b32 s41, s41, 0xffff\n"
        "s_mov_b32 s42, 236978176\n"
        "s_mov_b32 s43, 0x20000\n"
        "s_mov_b64 s[44:45], %1\n"
        "s_and_b32 s45, s45, 0xffff\n"
        "s_mov_b32 s46, 2359296\n"
        "s_mov_b32 s47, 0x20000\n"
        "s_mov_b64 s[56:57], %3\n"
        "s_mov_b64 s[58:59], %4\n"
        "s_mov_b64 s[60:61], %5\n"
        "s_mov_b64 s[62:63], %6\n"
        "v_mov_b64 v[0:1], %7\n"
        "v_mov_b64 v[2:3], %8\n"
        "v_mov_b64 v[4:5], %9\n"
        "v_mov_b64 v[6:7], %10\n"
        "v_mov_b64 v[8:9], %11\n"
        "v_mov_b64 v[10:11], %12\n"
        "v_mov_b64 v[12:13], %13\n"
        "v_mov_b64 v[14:15], %14\n"
        "v_mov_b64 v[16:17], %15\n"
        "v_mov_b64 v[18:19], %16\n"
        "v_mov_b64 v[20:21], %17\n"
        "v_mov_b64 v[22:23], %18\n"
        "v_mov_b64 v[24:25], %19\n"
        "v_mov_b64 v[26:27], %20\n"
        "v_mov_b64 v[200:201], v[24:25]\n"
        "v_mov_b64 v[202:203], v[26:27]\n"
        "s_lshr_b32 s74, s56, 8\n"
        "s_and_b32 s56, s56, 1\n"
        "s_mov_b32 s68, 0\n"
        "s_mul_i32 s65, s68, 36864\n"
        "buffer_load_dwordx4 v[80:83], v5, s[44:47], s65 offen offset:0\n"
        "buffer_load_dwordx4 v[84:87], v5, s[44:47], s65 offen offset:128\n"
        "buffer_load_dwordx4 v[88:91], v5, s[44:47], s65 offen offset:256\n"
        "s_getpc_b64 s[76:77]\n"
        ".Lr3_pc_%=:\n"
        "s_add_u32 s76, s76, .Lr3_code_%=-.Lr3_pc_%=\n"
        "s_addc_u32 s77, s77, 0\n"
        "s_and_b32 s77, s77, 0xffff\n"
        "s_and_b32 s76, s76, -4\n"
        "s_mov_b32 s78, .Lr3_end_%=-.Lr3_code_%=\n"
        "s_mov_b32 s79, 0x20000\n"
        "s_mov_b32 s65, 0\n"
        "buffer_load_dwordx4 v[32:35], v0, s[76:79], s65 offen\n"
        "s_add_u32 s65, s65, 1024\n"
        "buffer_load_dwordx4 v[36:39], v0, s[76:79], s65 offen\n"
        "s_add_u32 s65, s65, 1024\n"
        "buffer_load_dwordx4 v[40:43], v0, s[76:79], s65 offen\n"
        "s_add_u32 s65, s65, 1024\n"
        "buffer_load_dwordx4 v[44:47], v0, s[76:79], s65 offen\n"
        "s_add_u32 s65, s65, 1024\n"
        "buffer_load_dwordx4 v[48:51], v0, s[76:79], s65 offen\n"
        "s_add_u32 s65, s65, 1024\n"
        "v_lshrrev_b32 v26, 4, v0\n"
        "s_mov_b32 s50, s56\n"
        "v_lshlrev_b32 v28, 7, v26\n"
        "s_sub_u32 s66, 0, s50\n"
        "s_mov_b32 s52, 32768\n"
        "s_mov_b32 s64, 0\n"
        ".Lr3_pg0_top_%=:\n"
        "buffer_load_dword v27, v28, s[40:43], s52 offen sc1\n"
        "s_waitcnt vmcnt(0)\n"
        "v_cmp_gt_i32 vcc, 0, v27\n"
        "s_cbranch_vccz .Lr3_pg0_ok_%=\n"
        "s_add_u32 s64, s64, 1\n"
        "s_and_b32 s65, s64, 255\n"
        "s_cbranch_scc1 .Lr3_pg0_go_%=\n"
        "s_mov_b32 s65, 49152\n"
        "s_cmp_lt_u32 s64, 1048576\n"
        "s_cbranch_scc1 .Lr3_pg0_chk_%=\n"
        "v_mov_b32 v27, 1\n"
        "buffer_store_dword v27, off, s[40:43], s65 sc1\n"
        "s_waitcnt vmcnt(0)\n"
        ".Lr3_pg0_chk_%=:\n"
        "buffer_load_dword v27, off, s[40:43], s65 sc1\n"
        "s_waitcnt vmcnt(0)\n"
        "v_readfirstlane_b32 s65, v27\n"
        "s_nop 0\n"
        "s_cmp_lg_u32 s65, 1\n"
        "s_cbranch_scc1 .Lr3_pg0_go_%=\n"
        "s_endpgm\n"
        ".Lr3_pg0_go_%=:\n"
        "s_sleep 2\n"
        "s_branch .Lr3_pg0_top_%=\n"
        ".Lr3_pg0_ok_%=:\n"
        "v_cmp_eq_u32 s[52:53], 1, v27\n"
        "s_nop 1\n"
        "s_mov_b32 s54, 40960\n"
        "s_mov_b32 s64, 0\n"
        ".Lr3_pg1_top_%=:\n"
        "buffer_load_dword v27, v28, s[40:43], s54 offen sc1\n"
        "s_waitcnt vmcnt(0)\n"
        "v_cmp_gt_i32 vcc, 0, v27\n"
        "s_cbranch_vccz .Lr3_pg1_ok_%=\n"
        "s_add_u32 s64, s64, 1\n"
        "s_and_b32 s65, s64, 255\n"
        "s_cbranch_scc1 .Lr3_pg1_go_%=\n"
        "s_mov_b32 s65, 49152\n"
        "s_cmp_lt_u32 s64, 1048576\n"
        "s_cbranch_scc1 .Lr3_pg1_chk_%=\n"
        "v_mov_b32 v27, 1\n"
        "buffer_store_dword v27, off, s[40:43], s65 sc1\n"
        "s_waitcnt vmcnt(0)\n"
        ".Lr3_pg1_chk_%=:\n"
        "buffer_load_dword v27, off, s[40:43], s65 sc1\n"
        "s_waitcnt vmcnt(0)\n"
        "v_readfirstlane_b32 s65, v27\n"
        "s_nop 0\n"
        "s_cmp_lg_u32 s65, 1\n"
        "s_cbranch_scc1 .Lr3_pg1_go_%=\n"
        "s_endpgm\n"
        ".Lr3_pg1_go_%=:\n"
        "s_sleep 2\n"
        "s_branch .Lr3_pg1_top_%=\n"
        ".Lr3_pg1_ok_%=:\n"
        "v_cmp_eq_u32 s[54:55], 1, v27\n"
        "s_nop 1\n"
        "s_mov_b32 vcc_lo, 0x55555555\n"
        "s_mov_b32 vcc_hi, 0x55555555\n"
        "s_xor_b64 vcc, vcc, s[52:53]\n"
        "s_bcnt1_i32_b64 s65, vcc\n"
        "s_sub_u32 s64, s65, 23\n"
        "s_cmp_lt_u32 s64, 19\n"
        "s_cbranch_scc1 .Lr3_rawmap_%=\n"
        "s_cmp_lt_u32 s65, 32\n"
        "s_cselect_b32 s65, 0, -1\n"
        "s_xor_b32 s52, s65, 0x55555555\n"
        "s_xor_b32 s53, s65, 0x55555555\n"
        "s_xor_b32 s54, s65, 0x55555555\n"
        "s_xor_b32 s55, s65, 0xaaaaaaaa\n"
        ".Lr3_rawmap_%=:\n"
        "s_xor_b32 s52, s52, s66\n"
        "s_xor_b32 s53, s53, s66\n"
        "s_xor_b32 s54, s54, s66\n"
        "s_xor_b32 s55, s55, s66\n"
        "s_and_b32 s69, s55, 1\n"
        "s_lshl_b32 s69, s69, 13\n"
        "s_xor_b32 s70, s69, 0\n"
        "s_cmp_eq_u32 s50, 0\n"
        "s_cbranch_scc0 .Lr3_l1_%=\n"
        ".Lr3_code_%=:\n"
        ".Lr3_l0_%=:\n"
        "s_bitcmp1_b64 s[52:53], s68\n"
        "s_cselect_b32 s65, 8192, 0\n"
        "s_lshl_b32 s71, s68, 21\n"
        "s_add_u32 s71, s71, s65\n"
        "s_add_u32 s71, s71, 2097152\n"
        "s_lshr_b32 s65, s68, 1\n"
        "s_bitcmp1_b32 s54, s65\n"
        "s_cselect_b32 s65, 8192, 0\n"
        "s_lshl_b32 s72, s68, 20\n"
        "s_add_u32 s72, s72, s65\n"
        "s_add_u32 s72, s72, 136904704\n"
        "s_add_u32 s76, s72, s61\n"
        "s_add_u32 s79, s71, s58\n"
        "s_add_u32 s79, s79, 1916928\n"
        "s_add_u32 s77, s72, s62\n"
        "s_add_u32 s80, s71, s59\n"
        "s_add_u32 s80, s80, 1916928\n"
        "s_add_u32 s78, s72, s63\n"
        "s_add_u32 s81, s71, s60\n"
        "s_add_u32 s81, s81, 1916928\n"
        "s_lshl_b32 s73, s68, 1\n"
        "s_add_u32 s73, s73, s74\n"
        "s_mov_b32 s64, 0\n"
        ".Lr3_l0w_top_%=:\n"
        "buffer_load_dword v24, v1, s[40:43], s69 offen sc1\n"
        "s_waitcnt vmcnt(0)\n"
        "v_cmp_gt_i32 vcc, s73, v24\n"
        "s_nop 0\n"
        "s_and_b64 vcc, vcc, 0xffff\n"
        "s_cbranch_vccz .Lr3_l0w_ok_%=\n"
        "s_add_u32 s64, s64, 1\n"
        "s_and_b32 s65, s64, 255\n"
        "s_cbranch_scc1 .Lr3_l0w_go_%=\n"
        "s_mov_b32 s65, 49152\n"
        "s_cmp_lt_u32 s64, 1048576\n"
        "s_cbranch_scc1 .Lr3_l0w_chk_%=\n"
        "v_mov_b32 v24, 1\n"
        "buffer_store_dword v24, off, s[40:43], s65 sc1\n"
        "s_waitcnt vmcnt(0)\n"
        ".Lr3_l0w_chk_%=:\n"
        "buffer_load_dword v24, off, s[40:43], s65 sc1\n"
        "s_waitcnt vmcnt(0)\n"
        "v_readfirstlane_b32 s65, v24\n"
        "s_nop 0\n"
        "s_cmp_lg_u32 s65, 1\n"
        "s_cbranch_scc1 .Lr3_l0w_go_%=\n"
        "s_endpgm\n"
        ".Lr3_l0w_go_%=:\n"
        "s_branch .Lr3_l0w_top_%=\n"
        ".Lr3_l0w_ok_%=:\n"
        "buffer_load_dwordx4 v[32:35], v3, s[40:43], s76 offen offset:0 sc1\n"
        "buffer_load_dwordx4 v[40:43], v3, s[40:43], s76 offen offset:512 sc1\n"
        "buffer_load_dwordx4 v[36:39], v3, s[40:43], s76 offen offset:256 sc1\n"
        "buffer_load_dwordx4 v[44:47], v3, s[40:43], s76 offen offset:768 sc1\n"
        "buffer_load_dwordx4 v[48:51], v3, s[40:43], s77 offen offset:0 sc1\n"
        "buffer_load_dwordx4 v[56:59], v3, s[40:43], s77 offen offset:512 sc1\n"
        "buffer_load_dwordx4 v[52:55], v3, s[40:43], s77 offen offset:256 sc1\n"
        "buffer_load_dwordx4 v[60:63], v3, s[40:43], s77 offen offset:768 sc1\n"
        "buffer_load_dwordx4 v[64:67], v4, s[40:43], s78 offen offset:0 sc1\n"
        "buffer_load_dwordx4 v[72:75], v4, s[40:43], s78 offen offset:512 sc1\n"
        "buffer_load_dwordx4 v[68:71], v4, s[40:43], s78 offen offset:256 sc1\n"
        "buffer_load_dwordx4 v[76:79], v4, s[40:43], s78 offen offset:768 sc1\n"
        "s_add_u32 s72, s72, 294912\n"
        "s_add_u32 s71, s71, 1998848\n"
        "s_add_u32 s82, s72, s58\n"
        "s_add_u32 s85, s71, s58\n"
        "s_add_u32 s83, s72, s59\n"
        "s_add_u32 s86, s71, s59\n"
        "s_add_u32 s84, s72, s60\n"
        "s_add_u32 s87, s71, s60\n"
        "s_waitcnt vmcnt(11)\n"
        "v_mfma_f32_16x16x32_bf16 v[92:95], v[8:11], v[32:35], 0\n"
        "s_waitcnt vmcnt(10)\n"
        "v_mfma_f32_16x16x32_bf16 v[92:95], v[12:15], v[40:43], v[92:95]\n"
        "s_waitcnt vmcnt(9)\n"
        "v_mfma_f32_16x16x32_bf16 v[96:99], v[8:11], v[36:39], 0\n"
        "s_waitcnt vmcnt(8)\n"
        "v_mfma_f32_16x16x32_bf16 v[96:99], v[12:15], v[44:47], v[96:99]\n"
        "v_mfma_f32_16x16x32_bf16 v[92:95], v[16:19], v[80:83], v[92:95]\n"
        "v_mfma_f32_16x16x32_bf16 v[96:99], v[20:23], v[80:83], v[96:99]\n"
        "s_waitcnt vmcnt(7)\n"
        "v_mfma_f32_16x16x32_bf16 v[100:103], v[8:11], v[48:51], 0\n"
        "s_waitcnt vmcnt(6)\n"
        "v_mfma_f32_16x16x32_bf16 v[100:103], v[12:15], v[56:59], v[100:103]\n"
        "s_waitcnt vmcnt(5)\n"
        "v_mfma_f32_16x16x32_bf16 v[104:107], v[8:11], v[52:55], 0\n"
        "s_waitcnt vmcnt(4)\n"
        "v_mfma_f32_16x16x32_bf16 v[104:107], v[12:15], v[60:63], v[104:107]\n"
        "v_mfma_f32_16x16x32_bf16 v[100:103], v[16:19], v[84:87], v[100:103]\n"
        "v_mfma_f32_16x16x32_bf16 v[104:107], v[20:23], v[84:87], v[104:107]\n"
        "s_waitcnt vmcnt(3)\n"
        "v_mfma_f32_16x16x32_bf16 v[108:111], v[8:11], v[64:67], 0\n"
        "s_waitcnt vmcnt(2)\n"
        "v_mfma_f32_16x16x32_bf16 v[108:111], v[12:15], v[72:75], v[108:111]\n"
        "s_waitcnt vmcnt(1)\n"
        "v_mfma_f32_16x16x32_bf16 v[112:115], v[8:11], v[68:71], 0\n"
        "s_waitcnt vmcnt(0)\n"
        "v_mfma_f32_16x16x32_bf16 v[112:115], v[12:15], v[76:79], v[112:115]\n"
        "v_mfma_f32_16x16x32_bf16 v[108:111], v[16:19], v[88:91], v[108:111]\n"
        "v_mfma_f32_16x16x32_bf16 v[112:115], v[20:23], v[88:91], v[112:115]\n"
        "s_add_u32 s68, s68, 1\n"
        "s_mul_i32 s65, s68, 36864\n"
        "buffer_load_dwordx4 v[80:83], v5, s[44:47], s65 offen offset:0\n"
        "buffer_load_dwordx4 v[84:87], v5, s[44:47], s65 offen offset:128\n"
        "buffer_load_dwordx4 v[88:91], v5, s[44:47], s65 offen offset:256\n"
        "s_sub_u32 s68, s68, 1\n"
        "v_pk_add_f32 v[164:165], v[92:93], v[94:95]\n"
        "v_pk_add_f32 v[166:167], v[96:97], v[98:99]\n"
        "v_pk_add_f32 v[168:169], v[100:101], v[102:103]\n"
        "v_pk_add_f32 v[170:171], v[104:105], v[106:107]\n"
        "v_pk_add_f32 v[164:165], v[164:165], v[166:167]\n"
        "v_pk_add_f32 v[168:169], v[168:169], v[170:171]\n"
        "v_pk_add_f32 v[164:165], v[164:165], v[168:169]\n"
        "v_pk_add_f32 v[166:167], v[108:109], v[110:111]\n"
        "v_pk_add_f32 v[168:169], v[112:113], v[114:115]\n"
        "v_pk_add_f32 v[164:165], v[164:165], v[166:167]\n"
        "v_pk_add_f32 v[164:165], v[164:165], v[168:169]\n"
        "v_add_f32 v164, v164, v165\n"
        "v_cvt_pk_bf16_f32 v140, v92, v93\n"
        "v_cvt_pk_bf16_f32 v141, v94, v95\n"
        "v_add_f32_dpp v166, v164, v164 row_ror:8 row_mask:0xf bank_mask:0xf\n"
        "v_add_f32_dpp v167, v164, v164 row_ror:8 row_mask:0xf bank_mask:0xf\n"
        "v_cvt_pk_bf16_f32 v142, v96, v97\n"
        "v_cvt_pk_bf16_f32 v143, v98, v99\n"
        "v_permlane16_swap_b32 v166, v167\n"
        "v_add_f32 v164, v166, v167\n"
        "v_add_f32 v166, v166, v167\n"
        "v_cvt_pk_bf16_f32 v144, v100, v101\n"
        "v_cvt_pk_bf16_f32 v145, v102, v103\n"
        "v_permlane32_swap_b32 v164, v166\n"
        "v_add_f32 v164, v164, v166\n"
        "v_mul_f32 v164, 0x3baaaaab, v164\n"
        "v_pk_add_f32 v[116:117], v[92:93], v[164:165] op_sel_hi:[1,0] neg_lo:[0,1] neg_hi:[0,1]\n"
        "v_pk_add_f32 v[118:119], v[94:95], v[164:165] op_sel_hi:[1,0] neg_lo:[0,1] neg_hi:[0,1]\n"
        "v_pk_add_f32 v[120:121], v[96:97], v[164:165] op_sel_hi:[1,0] neg_lo:[0,1] neg_hi:[0,1]\n"
        "v_pk_add_f32 v[122:123], v[98:99], v[164:165] op_sel_hi:[1,0] neg_lo:[0,1] neg_hi:[0,1]\n"
        "v_pk_add_f32 v[124:125], v[100:101], v[164:165] op_sel_hi:[1,0] neg_lo:[0,1] neg_hi:[0,1]\n"
        "v_pk_add_f32 v[126:127], v[102:103], v[164:165] op_sel_hi:[1,0] neg_lo:[0,1] neg_hi:[0,1]\n"
        "v_pk_add_f32 v[128:129], v[104:105], v[164:165] op_sel_hi:[1,0] neg_lo:[0,1] neg_hi:[0,1]\n"
        "v_pk_add_f32 v[130:131], v[106:107], v[164:165] op_sel_hi:[1,0] neg_lo:[0,1] neg_hi:[0,1]\n"
        "v_pk_add_f32 v[132:133], v[108:109], v[164:165] op_sel_hi:[1,0] neg_lo:[0,1] neg_hi:[0,1]\n"
        "v_pk_add_f32 v[134:135], v[110:111], v[164:165] op_sel_hi:[1,0] neg_lo:[0,1] neg_hi:[0,1]\n"
        "v_pk_add_f32 v[136:137], v[112:113], v[164:165] op_sel_hi:[1,0] neg_lo:[0,1] neg_hi:[0,1]\n"
        "v_pk_add_f32 v[138:139], v[114:115], v[164:165] op_sel_hi:[1,0] neg_lo:[0,1] neg_hi:[0,1]\n"
        "v_pk_mul_f32 v[166:167], v[116:117], v[116:117]\n"
        "v_pk_mul_f32 v[168:169], v[118:119], v[118:119]\n"
        "v_pk_fma_f32 v[166:167], v[120:121], v[120:121], v[166:167]\n"
        "v_pk_fma_f32 v[168:169], v[122:123], v[122:123], v[168:169]\n"
        "v_pk_fma_f32 v[166:167], v[124:125], v[124:125], v[166:167]\n"
        "v_pk_fma_f32 v[168:169], v[126:127], v[126:127], v[168:169]\n"
        "v_pk_fma_f32 v[166:167], v[128:129], v[128:129], v[166:167]\n"
        "v_pk_fma_f32 v[168:169], v[130:131], v[130:131], v[168:169]\n"
        "v_pk_fma_f32 v[166:167], v[132:133], v[132:133], v[166:167]\n"
        "v_pk_fma_f32 v[168:169], v[134:135], v[134:135], v[168:169]\n"
        "v_pk_fma_f32 v[166:167], v[136:137], v[136:137], v[166:167]\n"
        "v_pk_fma_f32 v[168:169], v[138:139], v[138:139], v[168:169]\n"
        "v_pk_add_f32 v[166:167], v[166:167], v[168:169]\n"
        "v_add_f32 v165, v166, v167\n"
        "v_cvt_pk_bf16_f32 v146, v104, v105\n"
        "v_cvt_pk_bf16_f32 v147, v106, v107\n"
        "v_add_f32_dpp v166, v165, v165 row_ror:8 row_mask:0xf bank_mask:0xf\n"
        "v_add_f32_dpp v167, v165, v165 row_ror:8 row_mask:0xf bank_mask:0xf\n"
        "v_cvt_pk_bf16_f32 v148, v108, v109\n"
        "v_cvt_pk_bf16_f32 v149, v110, v111\n"
        "v_permlane16_swap_b32 v166, v167\n"
        "v_add_f32 v165, v166, v167\n"
        "v_add_f32 v166, v166, v167\n"
        "v_cvt_pk_bf16_f32 v150, v112, v113\n"
        "v_cvt_pk_bf16_f32 v151, v114, v115\n"
        "v_permlane32_swap_b32 v165, v166\n"
        "v_add_f32 v165, v165, v166\n"
        "ds_write_b64 v6, v[164:165]\n"
        "v_permlane16_swap_b32 v140, v142\n"
        "v_permlane16_swap_b32 v141, v143\n"
        "v_permlane16_swap_b32 v144, v146\n"
        "v_permlane16_swap_b32 v145, v147\n"
        "s_waitcnt lgkmcnt(0)\n"
        "s_barrier\n"
        "ds_read_b64 v[166:167], v7 offset:0\n"
        "ds_read_b64 v[168:169], v7 offset:64\n"
        "ds_read_b64 v[170:171], v7 offset:128\n"
        "v_permlane16_swap_b32 v148, v150\n"
        "v_permlane16_swap_b32 v149, v151\n"
        "buffer_store_dwordx4 v[140:143], v0, s[40:43], s85 offen sc1\n"
        "buffer_store_dwordx4 v[144:147], v0, s[40:43], s86 offen sc1\n"
        "buffer_store_dwordx4 v[148:151], v0, s[40:43], s87 offen sc1\n"
        "s_waitcnt lgkmcnt(0)\n"
        "v_add_f32 v172, v166, v168\n"
        "v_add_f32 v173, v167, v169\n"
        "v_add_f32 v172, v172, v170\n"
        "v_add_f32 v173, v173, v171\n"
        "v_mul_f32 v172, 0x3eaaaaab, v172\n"
        "v_sub_f32 v166, v166, v172\n"
        "v_sub_f32 v168, v168, v172\n"
        "v_sub_f32 v170, v170, v172\n"
        "v_mul_f32 v166, v166, v166\n"
        "v_fmac_f32 v166, v168, v168\n"
        "v_fmac_f32 v166, v170, v170\n"
        "v_fmamk_f32 v173, v166, 0x43400000, v173\n"
        "v_mul_f32 v173, 0x3ae38e39, v173\n"
        "v_cvt_pk_bf16_f32 v172, v172, v173\n"
        "v_and_b32 v173, 0xffff0000, v172\n"
        "v_lshlrev_b32 v172, 16, v172\n"
        "v_add_f32 v173, 0x3727c5ac, v173\n"
        "v_cvt_pk_bf16_f32 v173, v173, v173\n"
        "v_lshlrev_b32 v173, 16, v173\n"
        "v_sqrt_f32 v173, v173\n"
        "v_sub_f32 v172, v164, v172\n"
        "v_cvt_pk_bf16_f32 v173, v173, v173\n"
        "v_lshlrev_b32 v173, 16, v173\n"
        "v_rcp_f32 v173, v173\n"
        "v_mov_b32 v25, s68\n"
        "v_mul_f32 v172, v172, v173\n"
        "v_pk_fma_f32 v[116:117], v[116:117], v[172:173], v[172:173] op_sel:[0,1,0] op_sel_hi:[1,1,0]\n"
        "v_pk_fma_f32 v[118:119], v[118:119], v[172:173], v[172:173] op_sel:[0,1,0] op_sel_hi:[1,1,0]\n"
        "v_pk_fma_f32 v[120:121], v[120:121], v[172:173], v[172:173] op_sel:[0,1,0] op_sel_hi:[1,1,0]\n"
        "v_pk_fma_f32 v[122:123], v[122:123], v[172:173], v[172:173] op_sel:[0,1,0] op_sel_hi:[1,1,0]\n"
        "v_cvt_pk_bf16_f32 v152, v116, v117\n"
        "v_cvt_pk_bf16_f32 v153, v118, v119\n"
        "v_cvt_pk_bf16_f32 v154, v120, v121\n"
        "v_cvt_pk_bf16_f32 v155, v122, v123\n"
        "s_nop 0\n"
        "v_permlane16_swap_b32 v152, v154\n"
        "v_permlane16_swap_b32 v153, v155\n"
        "buffer_store_dwordx4 v[152:155], v0, s[40:43], s82 offen sc1\n"
        "v_pk_fma_f32 v[124:125], v[124:125], v[172:173], v[172:173] op_sel:[0,1,0] op_sel_hi:[1,1,0]\n"
        "v_pk_fma_f32 v[126:127], v[126:127], v[172:173], v[172:173] op_sel:[0,1,0] op_sel_hi:[1,1,0]\n"
        "v_pk_fma_f32 v[128:129], v[128:129], v[172:173], v[172:173] op_sel:[0,1,0] op_sel_hi:[1,1,0]\n"
        "v_pk_fma_f32 v[130:131], v[130:131], v[172:173], v[172:173] op_sel:[0,1,0] op_sel_hi:[1,1,0]\n"
        "v_cvt_pk_bf16_f32 v156, v124, v125\n"
        "v_cvt_pk_bf16_f32 v157, v126, v127\n"
        "v_cvt_pk_bf16_f32 v158, v128, v129\n"
        "v_cvt_pk_bf16_f32 v159, v130, v131\n"
        "s_nop 0\n"
        "v_permlane16_swap_b32 v156, v158\n"
        "v_permlane16_swap_b32 v157, v159\n"
        "buffer_store_dwordx4 v[156:159], v0, s[40:43], s83 offen sc1\n"
        "v_pk_fma_f32 v[132:133], v[132:133], v[172:173], v[172:173] op_sel:[0,1,0] op_sel_hi:[1,1,0]\n"
        "v_pk_fma_f32 v[134:135], v[134:135], v[172:173], v[172:173] op_sel:[0,1,0] op_sel_hi:[1,1,0]\n"
        "v_pk_fma_f32 v[136:137], v[136:137], v[172:173], v[172:173] op_sel:[0,1,0] op_sel_hi:[1,1,0]\n"
        "v_pk_fma_f32 v[138:139], v[138:139], v[172:173], v[172:173] op_sel:[0,1,0] op_sel_hi:[1,1,0]\n"
        "v_cvt_pk_bf16_f32 v160, v132, v133\n"
        "v_cvt_pk_bf16_f32 v161, v134, v135\n"
        "v_cvt_pk_bf16_f32 v162, v136, v137\n"
        "v_cvt_pk_bf16_f32 v163, v138, v139\n"
        "s_nop 0\n"
        "v_permlane16_swap_b32 v160, v162\n"
        "v_permlane16_swap_b32 v161, v163\n"
        "buffer_store_dwordx4 v[160:163], v0, s[40:43], s84 offen sc1\n"
        "s_waitcnt vmcnt(0)\n"
        "s_barrier\n"
        "buffer_store_dword v25, v2, s[40:43], s70 offen sc1\n"
        "s_add_u32 s68, s68, 1\n"
        "s_cmp_lt_u32 s68, 64\n"
        "s_cbranch_scc1 .Lr3_l0_%=\n"
        "s_endpgm\n"
        ".Lr3_l1_%=:\n"
        "s_bitcmp1_b64 s[52:53], s68\n"
        "s_cselect_b32 s65, 8192, 0\n"
        "s_lshl_b32 s71, s68, 21\n"
        "s_add_u32 s71, s71, s65\n"
        "s_add_u32 s71, s71, 2097152\n"
        "s_lshr_b32 s65, s68, 1\n"
        "s_bitcmp1_b32 s54, s65\n"
        "s_cselect_b32 s65, 8192, 0\n"
        "s_lshl_b32 s72, s68, 20\n"
        "s_add_u32 s72, s72, s65\n"
        "s_add_u32 s72, s72, 136904704\n"
        "s_add_u32 s76, s72, s61\n"
        "s_add_u32 s79, s71, s58\n"
        "s_add_u32 s79, s79, 1916928\n"
        "s_add_u32 s77, s72, s62\n"
        "s_add_u32 s80, s71, s59\n"
        "s_add_u32 s80, s80, 1916928\n"
        "s_add_u32 s78, s72, s63\n"
        "s_add_u32 s81, s71, s60\n"
        "s_add_u32 s81, s81, 1916928\n"
        "s_lshl_b32 s73, s68, 1\n"
        "s_add_u32 s73, s73, s74\n"
        "s_mov_b32 s64, 0\n"
        ".Lr3_l1w_top_%=:\n"
        "buffer_load_dword v24, v1, s[40:43], s69 offen sc1\n"
        "s_waitcnt vmcnt(0)\n"
        "v_cmp_gt_i32 vcc, s73, v24\n"
        "s_nop 0\n"
        "s_and_b64 vcc, vcc, 0xffff\n"
        "s_cbranch_vccz .Lr3_l1w_ok_%=\n"
        "s_add_u32 s64, s64, 1\n"
        "s_and_b32 s65, s64, 255\n"
        "s_cbranch_scc1 .Lr3_l1w_go_%=\n"
        "s_mov_b32 s65, 49152\n"
        "s_cmp_lt_u32 s64, 1048576\n"
        "s_cbranch_scc1 .Lr3_l1w_chk_%=\n"
        "v_mov_b32 v24, 1\n"
        "buffer_store_dword v24, off, s[40:43], s65 sc1\n"
        "s_waitcnt vmcnt(0)\n"
        ".Lr3_l1w_chk_%=:\n"
        "buffer_load_dword v24, off, s[40:43], s65 sc1\n"
        "s_waitcnt vmcnt(0)\n"
        "v_readfirstlane_b32 s65, v24\n"
        "s_nop 0\n"
        "s_cmp_lg_u32 s65, 1\n"
        "s_cbranch_scc1 .Lr3_l1w_go_%=\n"
        "s_endpgm\n"
        ".Lr3_l1w_go_%=:\n"
        "s_branch .Lr3_l1w_top_%=\n"
        ".Lr3_l1w_ok_%=:\n"
        "buffer_load_dwordx4 v[32:35], v3, s[40:43], s76 offen offset:0 sc1\n"
        "buffer_load_dwordx4 v[40:43], v3, s[40:43], s76 offen offset:512 sc1\n"
        "buffer_load_dwordx4 v[36:39], v3, s[40:43], s76 offen offset:256 sc1\n"
        "buffer_load_dwordx4 v[44:47], v3, s[40:43], s76 offen offset:768 sc1\n"
        "buffer_load_dwordx4 v[80:83], v0, s[40:43], s79 offen sc1\n"
        "buffer_load_dwordx4 v[48:51], v3, s[40:43], s77 offen offset:0 sc1\n"
        "buffer_load_dwordx4 v[56:59], v3, s[40:43], s77 offen offset:512 sc1\n"
        "buffer_load_dwordx4 v[52:55], v3, s[40:43], s77 offen offset:256 sc1\n"
        "buffer_load_dwordx4 v[60:63], v3, s[40:43], s77 offen offset:768 sc1\n"
        "buffer_load_dwordx4 v[84:87], v0, s[40:43], s80 offen sc1\n"
        "buffer_load_dwordx4 v[64:67], v4, s[40:43], s78 offen offset:0 sc1\n"
        "buffer_load_dwordx4 v[72:75], v4, s[40:43], s78 offen offset:512 sc1\n"
        "buffer_load_dwordx4 v[68:71], v4, s[40:43], s78 offen offset:256 sc1\n"
        "buffer_load_dwordx4 v[76:79], v4, s[40:43], s78 offen offset:768 sc1\n"
        "buffer_load_dwordx4 v[88:91], v0, s[40:43], s81 offen sc1\n"
        "s_add_u32 s72, s72, 294912\n"
        "s_add_u32 s71, s71, 1998848\n"
        "s_add_u32 s82, s72, s58\n"
        "s_add_u32 s85, s71, s58\n"
        "s_add_u32 s83, s72, s59\n"
        "s_add_u32 s86, s71, s59\n"
        "s_add_u32 s84, s72, s60\n"
        "s_add_u32 s87, s71, s60\n"
        "s_waitcnt vmcnt(14)\n"
        "v_mfma_f32_16x16x32_bf16 v[92:95], v[8:11], v[32:35], 0\n"
        "s_waitcnt vmcnt(13)\n"
        "v_mfma_f32_16x16x32_bf16 v[92:95], v[12:15], v[40:43], v[92:95]\n"
        "s_waitcnt vmcnt(12)\n"
        "v_mfma_f32_16x16x32_bf16 v[96:99], v[8:11], v[36:39], 0\n"
        "s_waitcnt vmcnt(11)\n"
        "v_mfma_f32_16x16x32_bf16 v[96:99], v[12:15], v[44:47], v[96:99]\n"
        "s_waitcnt vmcnt(10)\n"
        "v_mfma_f32_16x16x32_bf16 v[92:95], v[16:19], v[80:83], v[92:95]\n"
        "v_mfma_f32_16x16x32_bf16 v[96:99], v[20:23], v[80:83], v[96:99]\n"
        "s_waitcnt vmcnt(9)\n"
        "v_mfma_f32_16x16x32_bf16 v[100:103], v[8:11], v[48:51], 0\n"
        "s_waitcnt vmcnt(8)\n"
        "v_mfma_f32_16x16x32_bf16 v[100:103], v[12:15], v[56:59], v[100:103]\n"
        "s_waitcnt vmcnt(7)\n"
        "v_mfma_f32_16x16x32_bf16 v[104:107], v[8:11], v[52:55], 0\n"
        "s_waitcnt vmcnt(6)\n"
        "v_mfma_f32_16x16x32_bf16 v[104:107], v[12:15], v[60:63], v[104:107]\n"
        "s_waitcnt vmcnt(5)\n"
        "v_mfma_f32_16x16x32_bf16 v[100:103], v[16:19], v[84:87], v[100:103]\n"
        "v_mfma_f32_16x16x32_bf16 v[104:107], v[20:23], v[84:87], v[104:107]\n"
        "s_waitcnt vmcnt(4)\n"
        "v_mfma_f32_16x16x32_bf16 v[108:111], v[8:11], v[64:67], 0\n"
        "s_waitcnt vmcnt(3)\n"
        "v_mfma_f32_16x16x32_bf16 v[108:111], v[12:15], v[72:75], v[108:111]\n"
        "s_waitcnt vmcnt(2)\n"
        "v_mfma_f32_16x16x32_bf16 v[112:115], v[8:11], v[68:71], 0\n"
        "s_waitcnt vmcnt(1)\n"
        "v_mfma_f32_16x16x32_bf16 v[112:115], v[12:15], v[76:79], v[112:115]\n"
        "s_waitcnt vmcnt(0)\n"
        "v_mfma_f32_16x16x32_bf16 v[108:111], v[16:19], v[88:91], v[108:111]\n"
        "v_mfma_f32_16x16x32_bf16 v[112:115], v[20:23], v[88:91], v[112:115]\n"
        "s_add_u32 s68, s68, 1\n"
        "s_sub_u32 s68, s68, 1\n"
        "v_pk_add_f32 v[164:165], v[92:93], v[94:95]\n"
        "v_pk_add_f32 v[166:167], v[96:97], v[98:99]\n"
        "v_pk_add_f32 v[168:169], v[100:101], v[102:103]\n"
        "v_pk_add_f32 v[170:171], v[104:105], v[106:107]\n"
        "v_pk_add_f32 v[164:165], v[164:165], v[166:167]\n"
        "v_pk_add_f32 v[168:169], v[168:169], v[170:171]\n"
        "v_pk_add_f32 v[164:165], v[164:165], v[168:169]\n"
        "s_nop 0\n"
        "v_pk_add_f32 v[166:167], v[108:109], v[110:111]\n"
        "v_pk_add_f32 v[168:169], v[112:113], v[114:115]\n"
        "v_pk_add_f32 v[164:165], v[164:165], v[166:167]\n"
        "v_pk_add_f32 v[164:165], v[164:165], v[168:169]\n"
        "v_add_f32 v164, v164, v165\n"
        "v_cvt_pk_bf16_f32 v140, v92, v93\n"
        "v_cvt_pk_bf16_f32 v141, v94, v95\n"
        "v_add_f32_dpp v166, v164, v164 row_ror:8 row_mask:0xf bank_mask:0xf\n"
        "v_add_f32_dpp v167, v164, v164 row_ror:8 row_mask:0xf bank_mask:0xf\n"
        "v_cvt_pk_bf16_f32 v142, v96, v97\n"
        "v_cvt_pk_bf16_f32 v143, v98, v99\n"
        "v_permlane16_swap_b32 v166, v167\n"
        "v_add_f32 v164, v166, v167\n"
        "v_add_f32 v166, v166, v167\n"
        "v_cvt_pk_bf16_f32 v144, v100, v101\n"
        "v_cvt_pk_bf16_f32 v145, v102, v103\n"
        "v_permlane32_swap_b32 v164, v166\n"
        "v_add_f32 v164, v164, v166\n"
        "v_mul_f32 v164, 0x3baaaaab, v164\n"
        "v_pk_add_f32 v[116:117], v[92:93], v[164:165] op_sel_hi:[1,0] neg_lo:[0,1] neg_hi:[0,1]\n"
        "v_pk_add_f32 v[118:119], v[94:95], v[164:165] op_sel_hi:[1,0] neg_lo:[0,1] neg_hi:[0,1]\n"
        "v_pk_add_f32 v[120:121], v[96:97], v[164:165] op_sel_hi:[1,0] neg_lo:[0,1] neg_hi:[0,1]\n"
        "v_pk_add_f32 v[122:123], v[98:99], v[164:165] op_sel_hi:[1,0] neg_lo:[0,1] neg_hi:[0,1]\n"
        "v_pk_add_f32 v[124:125], v[100:101], v[164:165] op_sel_hi:[1,0] neg_lo:[0,1] neg_hi:[0,1]\n"
        "v_pk_add_f32 v[126:127], v[102:103], v[164:165] op_sel_hi:[1,0] neg_lo:[0,1] neg_hi:[0,1]\n"
        "v_pk_add_f32 v[128:129], v[104:105], v[164:165] op_sel_hi:[1,0] neg_lo:[0,1] neg_hi:[0,1]\n"
        "v_pk_add_f32 v[130:131], v[106:107], v[164:165] op_sel_hi:[1,0] neg_lo:[0,1] neg_hi:[0,1]\n"
        "v_pk_add_f32 v[132:133], v[108:109], v[164:165] op_sel_hi:[1,0] neg_lo:[0,1] neg_hi:[0,1]\n"
        "v_pk_add_f32 v[134:135], v[110:111], v[164:165] op_sel_hi:[1,0] neg_lo:[0,1] neg_hi:[0,1]\n"
        "v_pk_add_f32 v[136:137], v[112:113], v[164:165] op_sel_hi:[1,0] neg_lo:[0,1] neg_hi:[0,1]\n"
        "v_pk_add_f32 v[138:139], v[114:115], v[164:165] op_sel_hi:[1,0] neg_lo:[0,1] neg_hi:[0,1]\n"
        "v_pk_mul_f32 v[166:167], v[116:117], v[116:117]\n"
        "v_pk_mul_f32 v[168:169], v[118:119], v[118:119]\n"
        "v_pk_fma_f32 v[166:167], v[120:121], v[120:121], v[166:167]\n"
        "v_pk_fma_f32 v[168:169], v[122:123], v[122:123], v[168:169]\n"
        "v_pk_fma_f32 v[166:167], v[124:125], v[124:125], v[166:167]\n"
        "v_pk_fma_f32 v[168:169], v[126:127], v[126:127], v[168:169]\n"
        "v_pk_fma_f32 v[166:167], v[128:129], v[128:129], v[166:167]\n"
        "v_pk_fma_f32 v[168:169], v[130:131], v[130:131], v[168:169]\n"
        "v_pk_fma_f32 v[166:167], v[132:133], v[132:133], v[166:167]\n"
        "v_pk_fma_f32 v[168:169], v[134:135], v[134:135], v[168:169]\n"
        "v_pk_fma_f32 v[166:167], v[136:137], v[136:137], v[166:167]\n"
        "v_pk_fma_f32 v[168:169], v[138:139], v[138:139], v[168:169]\n"
        "v_pk_add_f32 v[166:167], v[166:167], v[168:169]\n"
        "v_add_f32 v165, v166, v167\n"
        "v_cvt_pk_bf16_f32 v146, v104, v105\n"
        "v_cvt_pk_bf16_f32 v147, v106, v107\n"
        "v_add_f32_dpp v166, v165, v165 row_ror:8 row_mask:0xf bank_mask:0xf\n"
        "v_add_f32_dpp v167, v165, v165 row_ror:8 row_mask:0xf bank_mask:0xf\n"
        "v_cvt_pk_bf16_f32 v148, v108, v109\n"
        "v_cvt_pk_bf16_f32 v149, v110, v111\n"
        "v_permlane16_swap_b32 v166, v167\n"
        "v_add_f32 v165, v166, v167\n"
        "v_add_f32 v166, v166, v167\n"
        "v_cvt_pk_bf16_f32 v150, v112, v113\n"
        "v_cvt_pk_bf16_f32 v151, v114, v115\n"
        "v_permlane32_swap_b32 v165, v166\n"
        "v_add_f32 v165, v165, v166\n"
        "ds_write_b64 v6, v[164:165]\n"
        "v_permlane16_swap_b32 v140, v142\n"
        "v_permlane16_swap_b32 v141, v143\n"
        "v_permlane16_swap_b32 v144, v146\n"
        "v_permlane16_swap_b32 v145, v147\n"
        "s_waitcnt lgkmcnt(0)\n"
        "s_barrier\n"
        "ds_read_b64 v[166:167], v7 offset:0\n"
        "ds_read_b64 v[168:169], v7 offset:64\n"
        "ds_read_b64 v[170:171], v7 offset:128\n"
        "v_permlane16_swap_b32 v148, v150\n"
        "v_permlane16_swap_b32 v149, v151\n"
        "buffer_store_dwordx4 v[140:143], v0, s[40:43], s85 offen sc1\n"
        "buffer_store_dwordx4 v[144:147], v0, s[40:43], s86 offen sc1\n"
        "buffer_store_dwordx4 v[148:151], v0, s[40:43], s87 offen sc1\n"
        "s_waitcnt lgkmcnt(0)\n"
        "v_add_f32 v172, v166, v168\n"
        "v_add_f32 v173, v167, v169\n"
        "v_add_f32 v172, v172, v170\n"
        "v_add_f32 v173, v173, v171\n"
        "v_mul_f32 v172, 0x3eaaaaab, v172\n"
        "v_sub_f32 v166, v166, v172\n"
        "v_sub_f32 v168, v168, v172\n"
        "v_sub_f32 v170, v170, v172\n"
        "v_mul_f32 v166, v166, v166\n"
        "v_fmac_f32 v166, v168, v168\n"
        "v_fmac_f32 v166, v170, v170\n"
        "v_fmamk_f32 v173, v166, 0x43400000, v173\n"
        "v_mul_f32 v173, 0x3ae38e39, v173\n"
        "v_cvt_pk_bf16_f32 v172, v172, v173\n"
        "v_and_b32 v173, 0xffff0000, v172\n"
        "v_lshlrev_b32 v172, 16, v172\n"
        "v_add_f32 v173, 0x3727c5ac, v173\n"
        "v_cvt_pk_bf16_f32 v173, v173, v173\n"
        "v_lshlrev_b32 v173, 16, v173\n"
        "v_sqrt_f32 v173, v173\n"
        "v_sub_f32 v172, v164, v172\n"
        "v_cvt_pk_bf16_f32 v173, v173, v173\n"
        "v_lshlrev_b32 v173, 16, v173\n"
        "v_rcp_f32 v173, v173\n"
        "v_mov_b32 v25, s68\n"
        "v_mul_f32 v172, v172, v173\n"
        "v_pk_fma_f32 v[116:117], v[116:117], v[172:173], v[172:173] op_sel:[0,1,0] op_sel_hi:[1,1,0]\n"
        "v_pk_fma_f32 v[118:119], v[118:119], v[172:173], v[172:173] op_sel:[0,1,0] op_sel_hi:[1,1,0]\n"
        "v_pk_fma_f32 v[120:121], v[120:121], v[172:173], v[172:173] op_sel:[0,1,0] op_sel_hi:[1,1,0]\n"
        "v_pk_fma_f32 v[122:123], v[122:123], v[172:173], v[172:173] op_sel:[0,1,0] op_sel_hi:[1,1,0]\n"
        "v_cvt_pk_bf16_f32 v152, v116, v117\n"
        "v_cvt_pk_bf16_f32 v153, v118, v119\n"
        "v_cvt_pk_bf16_f32 v154, v120, v121\n"
        "v_cvt_pk_bf16_f32 v155, v122, v123\n"
        "s_nop 0\n"
        "v_permlane16_swap_b32 v152, v154\n"
        "v_permlane16_swap_b32 v153, v155\n"
        "buffer_store_dwordx4 v[152:155], v0, s[40:43], s82 offen sc1\n"
        "v_pk_fma_f32 v[124:125], v[124:125], v[172:173], v[172:173] op_sel:[0,1,0] op_sel_hi:[1,1,0]\n"
        "v_pk_fma_f32 v[126:127], v[126:127], v[172:173], v[172:173] op_sel:[0,1,0] op_sel_hi:[1,1,0]\n"
        "v_pk_fma_f32 v[128:129], v[128:129], v[172:173], v[172:173] op_sel:[0,1,0] op_sel_hi:[1,1,0]\n"
        "v_pk_fma_f32 v[130:131], v[130:131], v[172:173], v[172:173] op_sel:[0,1,0] op_sel_hi:[1,1,0]\n"
        "v_cvt_pk_bf16_f32 v156, v124, v125\n"
        "v_cvt_pk_bf16_f32 v157, v126, v127\n"
        "v_cvt_pk_bf16_f32 v158, v128, v129\n"
        "v_cvt_pk_bf16_f32 v159, v130, v131\n"
        "s_nop 0\n"
        "v_permlane16_swap_b32 v156, v158\n"
        "v_permlane16_swap_b32 v157, v159\n"
        "buffer_store_dwordx4 v[156:159], v0, s[40:43], s83 offen sc1\n"
        "v_pk_fma_f32 v[132:133], v[132:133], v[172:173], v[172:173] op_sel:[0,1,0] op_sel_hi:[1,1,0]\n"
        "v_pk_fma_f32 v[134:135], v[134:135], v[172:173], v[172:173] op_sel:[0,1,0] op_sel_hi:[1,1,0]\n"
        "v_pk_fma_f32 v[136:137], v[136:137], v[172:173], v[172:173] op_sel:[0,1,0] op_sel_hi:[1,1,0]\n"
        "v_pk_fma_f32 v[138:139], v[138:139], v[172:173], v[172:173] op_sel:[0,1,0] op_sel_hi:[1,1,0]\n"
        "v_cvt_pk_bf16_f32 v160, v132, v133\n"
        "v_cvt_pk_bf16_f32 v161, v134, v135\n"
        "v_cvt_pk_bf16_f32 v162, v136, v137\n"
        "v_cvt_pk_bf16_f32 v163, v138, v139\n"
        "s_nop 0\n"
        "v_permlane16_swap_b32 v160, v162\n"
        "v_permlane16_swap_b32 v161, v163\n"
        "buffer_store_dwordx4 v[160:163], v0, s[40:43], s84 offen sc1\n"
        "s_waitcnt vmcnt(0)\n"
        "s_barrier\n"
        "buffer_store_dword v25, v2, s[40:43], s70 offen sc1\n"
        "s_add_u32 s68, s68, 1\n"
        "s_cmp_lt_u32 s68, 64\n"
        "s_cbranch_scc1 .Lr3_l1_%=\n"
        "s_endpgm\n"
        ".Lr3_end_%=:\n"
            RED_ARGS : "v0", "v1", "v2", "v3", "v4", "v5", "v6", "v7", "v8", "v9", "v10", "v11", "v12", "v13", "v14", "v15", "v16", "v17", "v18", "v19", "v20", "v21", "v22", "v23", "v24", "v25", "v26", "v27", "v28", "v29", "v30", "v31", "v32", "v33", "v34", "v35", "v36", "v37", "v38", "v39", "v40", "v41", "v42", "v43", "v44", "v45", "v46", "v47", "v48", "v49", "v50", "v51", "v52", "v53", "v54", "v55", "v56", "v57", "v58", "v59", "v60", "v61", "v62", "v63", "v64", "v65", "v66", "v67", "v68", "v69", "v70", "v71", "v72", "v73", "v74", "v75", "v76", "v77", "v78", "v79", "v255", "a255", "s26", "s27", "s28", "s29", "s30", "s31", "s32", "s33", "s34", "s35", "s36", "s37", "s38", "s39", "s40", "s41", "s42", "s43", "s44", "s45", "s46", "s47", "s48", "s49", "s50", "s51", "s52", "s53", "s54", "s55", "s56", "s57", "s58", "s59", "s60", "s61", "s62", "s63", "s64", "s65", "s66", "s67", "s68", "s69", "s70", "s71", "s72", "s73", "s74", "s75", "s76", "s77", "s78", "s79", "s80", "s81", "s82", "s83", "s84", "s85", "s86", "s87", "s88", "s89", "s90", "s91", "s92", "s93", "s94", "s95", "s96", "s97", "s98", "s99", "vcc", "scc", "memory");
    }
    __builtin_unreachable();
}

__global__ __launch_bounds__(256, 1) void mega_kernel(Params p) {
    extern __shared__ __attribute__((aligned(16))) char smem[];
    const rsrc_t rs = __builtin_amdgcn_make_buffer_rsrc(p.ws, 0, WS_USED, 0x00020000);
    const v2u ev = __builtin_amdgcn_raw_buffer_load_b64(rs, G_EPOCH * 128, 0, 16);
    const int roles = role_tab[(threadIdx.x & 3) * 32 + (blockIdx.x >> 3)];
    if (blockIdx.x == 0) st4(rs, threadIdx.x <= 128 || (threadIdx.x >= G_DONE - G_PG && threadIdx.x < G_DONE - G_PG + 18) ? (G_PG + threadIdx.x) * 128 : OOB, -1);
    const u64 epoch = pair(rfl(ev[0]), rfl(ev[1])), tag = (epoch + 0x9e3779b97f4a7c15ull) & ~7ull;
    unsigned* const srot = reinterpret_cast<unsigned*>(smem + CH_IMG);
    if (blockIdx.x == 0) {
        DRAIN();
        if (threadIdx.x == 0) srot[1] = (unsigned)__builtin_amdgcn_s_memrealtime() + TCAL;
        BAR();
        st16(rs, (G_ROT + threadIdx.x) * 128, 0, v4u{(unsigned)tag | my_xcc(), (unsigned)(tag >> 32), srot[1], 0u});
    }
    if (threadIdx.x == 0) {
        unsigned rot = 8, tc = 0;
#pragma unroll 1
        for (unsigned spins = 0; spins < SPIN_CAP; ++spins) {
            const v4u v = ld16(rs, (G_ROT + blockIdx.x) * 128, 0);
            if ((pair(v[0], v[1]) & ~7ull) == tag) { rot = v[0] & 7; tc = v[2]; break; }
            asm volatile("" ::: "memory");
        }
        srot[0] = rot; srot[2] = tc;
    }
    BAR();
    const unsigned rot = rfl(srot[0]), tcal = rfl(srot[2]);
    if (rot == 8) return;
    BAR();
    const unsigned xb = (blockIdx.x + rot) & 7;
    const int x4 = xb >> 2, half = x4, pr = __builtin_amdgcn_readlane(roles, xb & 3), k = pr >> 2, grp = pr & 3, x = x4 * 4 + grp;
    if (k >= (half ? 8 : 16) && k < 28 && !((p.tune2 >> 60) & 1)) {
        const unsigned w = rfl(threadIdx.x >> 6), first = ((half ? pr - 32 : pr + 16) * 4 + w) * 1024;
        warm(p.Wq, 4718592, first, 512); warm(p.x, 2359296, first, 512); warm(p.Wk, 4718592, first, 512); warm(p.Wo, 4718592, first, 512); warm(p.Wv, 4718592, first, 512);
        if (half == 1) { warm(p.w1, 5308416, first, 320); warm(p.w3, 5308416, first, 320); warm(p.w2, 5308416, first, 320); }
        DRAIN();
        BAR();
    }
    auto dl = [&](int r) __attribute__((always_inline)) -> int { return (int)(p.tune2 >> (6 * (r + 5 * half))) & 63; };
    if (k < 8) role_chain(p, grp * 8 + k, x4 | dl(0) << 8, tcal, smem);
    else if (k < 16) role_s1(p, grp * 8 + k - 8, x4, pr, dl(1));
    else if (k < 22) role_f1(p, grp, k - 16, x4, pr, dl(3));
    else if (k < 25) role_k<true>(p, grp, k - 22, x4, pr, dl(2));
    else if (k < 28) role_k<false>(p, grp, k - 25, x4, pr, dl(4));
    else if (k >= 28) {
        startup(rs, ((k - 28) * 8 + x) * 4 + rfl(threadIdx.x >> 6));
        if (k < 30) role_attn(p, grp * 8 + (k - 28) * 4, x4, pr);
        else if (k == 30) role_red<false>(p, grp, x4, pr, epoch);
        else role_red<true>(p, grp, x4, pr, epoch);
    }
}

}

extern "C" void kernel_launch(void* const* d_in, const int* in_sizes, int n_in, void* d_out, int out_size,
                              void* d_ws, size_t ws_size, hipStream_t stream) {
    if (ws_size < (size_t)WS_USED + 2 * PAGE) return;
    Params p;
    p.x = (const u16*)d_in[0]; p.mem0 = (const u16*)d_in[1];
    p.Wq = (const u16*)d_in[2]; p.Wk = (const u16*)d_in[3]; p.Wv = (const u16*)d_in[4]; p.Wo = (const u16*)d_in[5];
    p.w1 = (const u16*)d_in[8]; p.w3 = (const u16*)d_in[9]; p.w2 = (const u16*)d_in[10];
    p.out = (u16*)d_out;
    p.ws = (char*)d_ws + ((0 - (size_t)d_ws) & (PAGE - 1));
    p.tune0 = (u64)D1 | (u64)D2 << 32; p.tune1 = (u64)D3 | (u64)D4 << 32;
    p.tune2 = (u64)DL0 | (u64)DL1 << 30;
    (void)hipFuncSetAttribute((const void*)mega_kernel, hipFuncAttributeMaxDynamicSharedMemorySize, LDS_BYTES);
    mega_kernel<<<256, 256, LDS_BYTES, stream>>>(p);
}
```

```cpp
#include <hip/hip_runtime.h>
#include <stdint.h>

typedef unsigned short u16;
typedef unsigned long long u64;
typedef unsigned v4u __attribute__((ext_vector_type(4)));
typedef unsigned v2u __attribute__((ext_vector_type(2)));
typedef short bf16x8 __attribute__((ext_vector_type(8)));
typedef float f32x4 __attribute__((ext_vector_type(4)));
typedef __bf16 bf2_t __attribute__((ext_vector_type(2)));
typedef __amdgpu_buffer_rsrc_t rsrc_t;

#define XF2L 0
#define XF2S 0
#define XF2M 24
#define XFN 1
#define WARMD 4
#define TCAL 200
#define RSLEEP 1
namespace {

constexpr unsigned A_K = 262144u;
constexpr unsigned A_KM = 786432u;
constexpr unsigned A_PROBE = 1044480u;
constexpr unsigned A_Q = 0u;
constexpr unsigned A_ST = 1036288u;
constexpr unsigned A_V = 524288u;
constexpr unsigned A_X1 = 999424u;
constexpr unsigned A_XN = 917504u;
constexpr unsigned A_XR = 958464u;
constexpr unsigned B_H = 0u;
constexpr unsigned B_P3 = 294912u;
constexpr unsigned B_XN1 = 442368u;
constexpr unsigned C_P5 = 0u;
constexpr unsigned DBG = 236978176u;
constexpr unsigned DBG2 = 238026752u;
constexpr unsigned EXB = 294912u;
constexpr unsigned EXL = 270336u;
constexpr unsigned F_AT = 832u;
constexpr unsigned F_CH = 704u;
constexpr unsigned F_F1 = 960u;
constexpr unsigned F_F2 = 1600u;
constexpr unsigned F_LINES = 1856u;
constexpr unsigned F_OP = 1344u;
constexpr unsigned F_R3 = 256u;
constexpr unsigned F_S1 = 448u;
constexpr unsigned F_ST = 928u;
constexpr unsigned F_XN = 0u;
constexpr unsigned G_ABORT = 384u;
constexpr unsigned G_DONE = 400u;
constexpr unsigned G_EPOCH = 385u;
constexpr unsigned G_LINES = 512u;
constexpr unsigned G_PG = 256u;
constexpr unsigned G_ROT = 0u;
constexpr unsigned K1 = 1024u;
constexpr unsigned LF0 = 32768u;
constexpr unsigned MEMB = 40960u;
constexpr unsigned MEML = 565248u;
constexpr unsigned NPAGES = 113u;
constexpr unsigned OOB = 2147483648u;
constexpr unsigned PAGE = 2097152u;
constexpr unsigned SPIN_CAP = 1048576u;
constexpr unsigned WS_USED = 236978176u;
constexpr unsigned CH_WB0 = 0u;
constexpr unsigned CH_WB1 = 40960u;
constexpr unsigned CH_WB2 = 81920u;
constexpr unsigned CH_WB3 = 116736u;
constexpr unsigned CH_IMG = 151552u;
constexpr unsigned CH_SO = 149504u;
constexpr unsigned CH_SELT = 160768u;
constexpr unsigned NPROBE = 6u;
constexpr unsigned NCOPY = 4u;
constexpr unsigned DL0 = 407437568u;
constexpr unsigned DL1 = 851077383u;
constexpr unsigned D1 = 1530658922u;
constexpr unsigned D2 = 2726035456u;
constexpr unsigned D3 = 1007419392u;
constexpr unsigned D4 = 402472u;
constexpr int LDS_BYTES = 160 * 1024;
constexpr unsigned spread(unsigned o) { return (o >> 13) * 16384u + (o & 8191u); }

__device__ const unsigned char role_tab[128] = {0, 4, 8, 12, 16, 20, 24, 28, 1, 5, 9, 13, 17, 21, 25, 29, 2, 6, 10, 14, 18, 22, 26, 30, 3, 7, 11, 15, 19, 23, 27, 31, 32, 36, 40, 44, 48, 52, 56, 60, 33, 37, 41, 45, 49, 64, 68, 72, 76, 80, 84, 65, 69, 73, 77, 100, 104, 108, 101, 105, 109, 112, 116, 113, 53, 57, 61, 34, 38, 42, 46, 50, 54, 58, 62, 81, 85, 66, 70, 74, 78, 82, 86, 67, 71, 102, 106, 110, 103, 107, 111, 117, 114, 118, 115, 119, 35, 39, 43, 47, 51, 55, 59, 63, 75, 79, 83, 87, 88, 92, 96, 89, 93, 97, 90, 94, 98, 91, 95, 99, 120, 124, 121, 125, 122, 126, 123, 127};

struct Params {
    const u16* x; const u16* mem0; const u16* Wq; const u16* Wk; const u16* Wv; const u16* Wo;
    const u16* w1; const u16* w3; const u16* w2; u16* out; char* ws;
    u64 tune0, tune1, tune2;
};

__device__ __forceinline__ v4u ld16(rsrc_t rs, unsigned voff, unsigned soff) { return __builtin_amdgcn_raw_buffer_load_b128(rs, voff, soff, 16); }
__device__ __forceinline__ int ld4(rsrc_t rs, unsigned off) { return (int)__builtin_amdgcn_raw_buffer_load_b32(rs, off, 0, 16); }
__device__ __forceinline__ void st16(rsrc_t rs, unsigned voff, unsigned soff, v4u v) { __builtin_amdgcn_raw_buffer_store_b128(v, rs, voff, soff, 16); }
__device__ __forceinline__ void st4(rsrc_t rs, unsigned off, int v) { __builtin_amdgcn_raw_buffer_store_b32((unsigned)v, rs, off, 0, 16); }
#define DRAIN() asm volatile("s_waitcnt vmcnt(0)" ::: "memory")
#define BAR() do { asm volatile("s_waitcnt lgkmcnt(0)" ::: "memory"); __builtin_amdgcn_s_barrier(); asm volatile("" ::: "memory"); } while (0)
__device__ __forceinline__ int rfl(int v) { return __builtin_amdgcn_readfirstlane(v); }
__device__ __forceinline__ u64 pair(unsigned lo, unsigned hi) { return (u64)lo | (u64)hi << 32; }
__device__ __forceinline__ unsigned my_xcc() {
    unsigned id;
    asm volatile("s_getreg_b32 %0, hwreg(HW_REG_XCC_ID)" : "=s"(id));
    return id & 7;
}

__device__ __forceinline__ void startup(rsrc_t rs, int hw) {
    const int lane = threadIdx.x & 63;
    const v4u ones = {~0u, ~0u, ~0u, ~0u};
    const unsigned o = (unsigned)(hw < 96 ? hw + 1 : (hw == 96 ? 0 : hw)) * PAGE + spread(A_PROBE);
    unsigned best[2] = {~0u, ~0u};
    if (hw < NPAGES) {
#pragma unroll 1
        for (int r = 0; r < 2 * NPROBE; ++r) {
            const unsigned t0 = (unsigned)__builtin_amdgcn_s_memtime();
            st16(rs, lane < 8 ? lane * 16 : OOB, o + (r & 1) * 8192, ones);
            DRAIN();
            const unsigned t = (unsigned)__builtin_amdgcn_s_memtime() - t0;
            if (r & 1) best[1] = min(best[1], t); else best[0] = min(best[0], t);
        }
    }
#pragma unroll
    for (int q = 0; q < 11; ++q) st16(rs, lane * 16, 2 * LF0 + (hw * 11 + q) * 1024, ones);
    static_assert(2 * LF0 + 128 * 11 * 1024 >= 2 * (MEML + 4 * MEMB) && 2 * LF0 + 128 * 11 * 1024 <= 2 * A_PROBE, "flags and exchange area");
    DRAIN();
    if (lane == 0) st4(rs, (G_PG + hw) * 128, (best[1] < best[0] ? 1 : 0) ^ (my_xcc() >> 2));
}

__device__ __forceinline__ void warm(const u16* W, unsigned bytes, unsigned first, unsigned step) {
    const rsrc_t r = __builtin_amdgcn_make_buffer_rsrc(const_cast<u16*>(W), 0, bytes, 0x00020000);
    const unsigned vo = (threadIdx.x & 63) * 16;
#pragma unroll 1
    for (unsigned o = first; o < bytes; o += step * 1024)
        asm volatile("s_mov_b32 m0, 0\n\ts_nop 0\n\tbuffer_load_dwordx4 %0, %1, %2 offen lds\n\ts_waitcnt vmcnt(%3)" :: "v"(vo), "s"(r), "s"(o), "n"(WARMD) : "memory");
}

__device__ __forceinline__ void role_chain(const Params& p, int h, int x4, int pr, char* smem) {
    const unsigned tid = threadIdx.x, lane = tid & 63, c = lane & 15, g = lane >> 4;
    const int w = rfl(tid >> 6);
    const unsigned wb = w == 0 ? CH_WB0 : (w == 1 ? CH_WB1 : (w == 2 ? CH_WB2 : CH_WB3));
    const unsigned slot = (g * 8 + (c & 7)) * 16;
    const unsigned spr = CH_IMG + (c >> 3) * 512 + slot;
    const unsigned gam = lane >> 4, nh = (lane >> 3) & 1, ksl = (lane >> 2) & 1, tau = ((lane ^ gam) >> 1) & 1, rho = lane & 1;
    auto img = [&](unsigned r) __attribute__((always_inline)) -> unsigned { return (2 * (r & 3) + (g & 1)) * 128 + (((g ^ r) & 2) + (r >> 2)) * 16; };
    if (w == 3) {
    } else if (w == 2) {
        v4u sel;
#pragma unroll
        for (int e = 0; e < 4; ++e) sel[e] = ((c & 7) == 2 * e) ? 0x3f80u : (((c & 7) == 2 * e + 1) ? 0x3f800000u : 0u);
        const unsigned g3 = ksl * 512 + (2 * (gam & 1) + nh) * 128 + (4 * (gam >> 1) + 2 * tau + rho) * 16;
        *reinterpret_cast<v4u*>(smem + wb + 32768 + lane * 16) = sel;
        *reinterpret_cast<v4u*>(smem + wb + 32768 + 1024 + lane * 16) =
            v4u{lane < 8 * NCOPY ? (lane >> 3) * 1024 + (lane & 7) * 16 : OOB, g3, lane < 8 * NCOPY ? (lane >> 3) * 16384 + ((lane & 7) ^ ((h >> 1) & 2)) * 16 : OOB, 0u};
    } else if (w == 0) {
        const bool onq = g == (c & 3), upq = (c & 4) != 0, onk = g == (c >> 2), upk = (c & 2) != 0;
        *reinterpret_cast<v4u*>(smem + spr + CH_SELT - CH_IMG) = v4u{onq ? (upq ? 0x3f800000u : 0x3f80u) : 0u, onq ? (upq ? 0x3f80u : 0x3f800000u) : 0u,
                                                                     onk ? (upk ? 0x3f800000u : 0x3f80u) : 0u, onk ? (upk ? 0x3f80u : 0x3f800000u) : 0u};
    } else {
        const unsigned pub = (lane >> 4) * 16384 + ((lane >> 3) & 1) * 4096 + (lane & 7) * 16;
        *reinterpret_cast<v4u*>(smem + spr + CH_SELT - CH_IMG + 1024) = v4u{(c & 1) ? OOB : lane * 8, (g & 1) ? OOB : (g >> 1) * 256 + c * 16, pub, CH_SO + img(c & 7) + (c >> 3) * 8};
    }
    if (w == 0) *reinterpret_cast<unsigned*>(smem + spr + CH_SELT - CH_IMG + 2048) = ~0u;
    BAR();
    const unsigned idx = (lane & 2) ? 0xeeeeu : 0x4444u;
    const unsigned va = CH_IMG + img((c >> 2) + 4 * ((((c & 3) + 1) >> 1) & 1));
    const unsigned pp = lane >> 2, row = (pp & 9) | ((pp & 2) << 1) | ((pp & 4) >> 1);
    auto qo = [&](unsigned hb) __attribute__((always_inline)) -> unsigned { return (16 * w + g) * 8192 + 256 * h + (c ^ (2 * hb + 4 * (g & 1) + 8 * (w >> 1))) * 16; };
    const unsigned o2 = (128 * h + 16 * (lane >> 5) + ((lane >> 3) & 3)) * 1152 + w * 128 + ((lane & 7) ^ (4 * (lane >> 5))) * 16;
    const unsigned oo = (128 * h + 16 * ((row >> 2) & 1) + 4 * (row >> 3) + (row & 3)) * 1152 + w * 128 + (lane & 3) * 16;
    const unsigned q = c >> 2;
    const unsigned tr = wb + ((q & 1) + 2 * (g & 1) + 8 * (g >> 1)) * 64 + (lane & 3) * 8;
    auto to = [&](unsigned t) __attribute__((always_inline)) -> unsigned { return wb + (4 * g + (q & 1)) * 128 + ((t ^ (g & 1)) * 64) + (q >> 1) * 32 + (lane & 3) * 8; };
    auto tq = [&](unsigned t, unsigned a) __attribute__((always_inline)) -> unsigned {
        const unsigned part = 2 * (q >> 1) + a;
        return (part == 3 ? CH_WB3 : part * CH_WB1) + (4 * g + (q & 1)) * 256 + (((4 * w + 2 * t) ^ (2 * (g & 1) + 4 * (q & 1) + 8 * (q >> 1))) * 16) + (lane & 3) * 8;
    };
    const unsigned m0 = CH_IMG + (gam + 4 * rho) * 1152 + (4 * ksl + 2 * tau + nh) * 16 + (w == 3 ? 2 : w) * 384;
    asm volatile(
        "s_nop 4\n"
        "s_mov_b64 s[40:41], %0\n"
        "s_and_b32 s41, s41, 0xffff\n"
        "s_mov_b32 s42, 236978176\n"
        "s_mov_b32 s43, 0x20000\n"
        "s_mov_b64 s[48:49], %6\n"
        "s_mov_b64 s[56:57], %7\n"
        "s_mov_b64 s[60:61], %8\n"
        "s_mov_b64 s[62:63], %9\n"
        "v_mov_b64 v[0:1], %10\n"
        "v_mov_b64 v[2:3], %11\n"
        "v_mov_b64 v[4:5], %12\n"
        "v_mov_b64 v[6:7], %13\n"
        "v_mov_b64 v[8:9], %14\n"
        "v_mov_b64 v[10:11], %15\n"
        "v_mov_b64 v[12:13], %16\n"
        "v_mov_b64 v[14:15], %17\n"
        "v_mov_b64 v[16:17], %18\n"
        "s_mov_b32 s47, 0x20000\n"
        "v_readfirstlane_b32 s82, v2\n"
        "s_lshr_b32 s65, s56, 8\n"
        "s_and_b32 s56, s56, 1\n"
        "s_mov_b32 s64, s65\n"
        "s_cmp_eq_u32 s65, 0\n"
        "s_cbranch_scc1 .Lch_start2_%=\n"
        "s_memrealtime s[36:37]\n"
        "s_waitcnt lgkmcnt(0)\n"
        "s_sub_u32 s66, s36, s57\n"
        "s_cmp_gt_u32 s66, -2000\n"
        "s_cselect_b32 s67, s57, s36\n"
        "s_subb_u32 s67, s67, -1\n"
        "s_mov_b32 s35, 0\n"
        "s_mov_b32 s34, 0\n"
        ".Lch_cala_%=:\n"
        "s_memrealtime s[36:37]\n"
        "s_memtime s[90:91]\n"
        "s_waitcnt lgkmcnt(0)\n"
        "s_sub_u32 s66, s36, s67\n"
        "s_cmp_lt_i32 s66, 0\n"
        "s_cbranch_scc1 .Lch_cala_%=\n"
        "s_add_u32 s67, s36, 5\n"
        "s_sub_u32 s66, s36, s57\n"
        "s_mul_i32 s66, s66, 23\n"
        "s_sub_u32 s66, s90, s66\n"
        "s_cmp_eq_u32 s34, 0\n"
        "s_cselect_b32 s38, s66, s38\n"
        "s_sub_u32 s66, s66, s38\n"
        "s_add_u32 s35, s35, s66\n"
        "s_add_u32 s34, s34, 1\n"
        "s_cmp_lt_u32 s34, 8\n"
        "s_cbranch_scc1 .Lch_cala_%=\n"
        "s_ashr_i32 s35, s35, 3\n"
        "s_add_u32 s38, s38, s35\n"
        ".Lch_start_%=:\n"
        "s_sub_u32 s65, s65, 1\n"
        "s_cbranch_scc1 .Lch_start2_%=\n"
        "s_sleep 16\n"
        "s_branch .Lch_start_%=\n"
        ".Lch_start2_%=:\n"
        "s_mov_b64 s[44:45], %1\n"
        "s_and_b32 s45, s45, 0xffff\n"
        "s_mov_b32 s46, 4718592\n"
        "s_add_u32 m0, s82, 0\n"
        "s_mov_b32 s65, 0\n"
        "buffer_load_dwordx4 v6, s[44:47], s65 offen offset:0 lds\n"
        "s_mov_b32 s65, 31744\n"
        "buffer_load_dwordx4 v7, s[44:47], s65 offen offset:1024 lds\n"
        "s_mov_b32 s65, 63488\n"
        "buffer_load_dwordx4 v6, s[44:47], s65 offen offset:2048 lds\n"
        "s_mov_b32 s65, 95232\n"
        "buffer_load_dwordx4 v7, s[44:47], s65 offen offset:3072 lds\n"
        "s_add_u32 m0, s82, 4096\n"
        "s_mov_b32 s65, 524288\n"
        "buffer_load_dwordx4 v6, s[44:47], s65 offen offset:0 lds\n"
        "s_mov_b32 s65, 556032\n"
        "buffer_load_dwordx4 v7, s[44:47], s65 offen offset:1024 lds\n"
        "s_mov_b32 s65, 587776\n"
        "buffer_load_dwordx4 v6, s[44:47], s65 offen offset:2048 lds\n"
        "s_mov_b32 s65, 619520\n"
        "buffer_load_dwordx4 v7, s[44:47], s65 offen offset:3072 lds\n"
        "s_add_u32 m0, s82, 8192\n"
        "s_mov_b32 s65, 1048576\n"
        "buffer_load_dwordx4 v6, s[44:47], s65 offen offset:0 lds\n"
        "s_mov_b32 s65, 1080320\n"
        "buffer_load_dwordx4 v7, s[44:47], s65 offen offset:1024 lds\n"
        "s_mov_b32 s65, 1112064\n"
        "buffer_load_dwordx4 v6, s[44:47], s65 offen offset:2048 lds\n"
        "s_mov_b32 s65, 1143808\n"
        "buffer_load_dwordx4 v7, s[44:47], s65 offen offset:3072 lds\n"
        "s_cmp_eq_u32 s64, 0\n"
        "s_cbranch_scc0 .Lch_start3_%=\n"
        "s_memrealtime s[36:37]\n"
        "s_waitcnt lgkmcnt(0)\n"
        "s_sub_u32 s66, s36, s57\n"
        "s_cmp_gt_u32 s66, -2000\n"
        "s_cselect_b32 s67, s57, s36\n"
        "s_subb_u32 s67, s67, -1\n"
        "s_mov_b32 s35, 0\n"
        "s_mov_b32 s34, 0\n"
        ".Lch_calb_%=:\n"
        "s_memrealtime s[36:37]\n"
        "s_memtime s[90:91]\n"
        "s_waitcnt lgkmcnt(0)\n"
        "s_sub_u32 s66, s36, s67\n"
        "s_cmp_lt_i32 s66, 0\n"
        "s_cbranch_scc1 .Lch_calb_%=\n"
        "s_add_u32 s67, s36, 5\n"
        "s_sub_u32 s66, s36, s57\n"
        "s_mul_i32 s66, s66, 23\n"
        "s_sub_u32 s66, s90, s66\n"
        "s_cmp_eq_u32 s34, 0\n"
        "s_cselect_b32 s38, s66, s38\n"
        "s_sub_u32 s66, s66, s38\n"
        "s_add_u32 s35, s35, s66\n"
        "s_add_u32 s34, s34, 1\n"
        "s_cmp_lt_u32 s34, 8\n"
        "s_cbranch_scc1 .Lch_calb_%=\n"
        "s_ashr_i32 s35, s35, 3\n"
        "s_add_u32 s38, s38, s35\n"
        ".Lch_start3_%=:\n"
        "s_waitcnt vmcnt(8)\n"
        "s_waitcnt lgkmcnt(0)\n"
        "s_barrier\n"
        "ds_read_b64_tr_b16 a[0:1], v14 offset:0\n"
        "ds_read_b64_tr_b16 a[2:3], v14 offset:512\n"
        "ds_read_b64_tr_b16 a[4:5], v15 offset:0\n"
        "ds_read_b64_tr_b16 a[6:7], v15 offset:512\n"
        "ds_read_b64_tr_b16 a[8:9], v16 offset:0\n"
        "ds_read_b64_tr_b16 a[10:11], v16 offset:512\n"
        "ds_read_b64_tr_b16 a[12:13], v17 offset:0\n"
        "ds_read_b64_tr_b16 a[14:15], v17 offset:512\n"
        "s_waitcnt vmcnt(4)\n"
        "s_waitcnt lgkmcnt(0)\n"
        "s_barrier\n"
        "s_add_u32 m0, s82, 12288\n"
        "s_mov_b32 s65, 1572864\n"
        "buffer_load_dwordx4 v6, s[44:47], s65 offen offset:0 lds\n"
        "s_mov_b32 s65, 1604608\n"
        "buffer_load_dwordx4 v7, s[44:47], s65 offen offset:1024 lds\n"
        "s_mov_b32 s65, 1636352\n"
        "buffer_load_dwordx4 v6, s[44:47], s65 offen offset:2048 lds\n"
        "s_mov_b32 s65, 1668096\n"
        "buffer_load_dwordx4 v7, s[44:47], s65 offen offset:3072 lds\n"
        "ds_read_b64_tr_b16 a[16:17], v14 offset:4096\n"
        "ds_read_b64_tr_b16 a[18:19], v14 offset:4608\n"
        "ds_read_b64_tr_b16 a[20:21], v15 offset:4096\n"
        "ds_read_b64_tr_b16 a[22:23], v15 offset:4608\n"
        "ds_read_b64_tr_b16 a[24:25], v16 offset:4096\n"
        "ds_read_b64_tr_b16 a[26:27], v16 offset:4608\n"
        "ds_read_b64_tr_b16 a[28:29], v17 offset:4096\n"
        "ds_read_b64_tr_b16 a[30:31], v17 offset:4608\n"
        "s_waitcnt vmcnt(4)\n"
        "s_waitcnt lgkmcnt(0)\n"
        "s_barrier\n"
        "s_add_u32 m0, s82, 16384\n"
        "s_mov_b32 s65, 2097152\n"
        "buffer_load_dwordx4 v6, s[44:47], s65 offen offset:0 lds\n"
        "s_mov_b32 s65, 2128896\n"
        "buffer_load_dwordx4 v7, s[44:47], s65 offen offset:1024 lds\n"
        "s_mov_b32 s65, 2160640\n"
        "buffer_load_dwordx4 v6, s[44:47], s65 offen offset:2048 lds\n"
        "s_mov_b32 s65, 2192384\n"
        "buffer_load_dwordx4 v7, s[44:47], s65 offen offset:3072 lds\n"
        "s_mov_b64 s[44:45], %5\n"
        "s_and_b32 s45, s45, 0xffff\n"
        "s_mov_b32 s46, 9216\n"
        "s_mul_i32 s65, s48, 3072\n"
        "s_add_u32 m0, s65, 151552\n"
        "s_cmp_eq_u32 s48, 3\n"
        "s_cbranch_scc1 .Lch_nom_%=\n"
        "buffer_load_dwordx4 v3, s[44:47], s65 offen offset:0 lds\n"
        "buffer_load_dwordx4 v3, s[44:47], s65 offen offset:1024 lds\n"
        "buffer_load_dwordx4 v3, s[44:47], s65 offen offset:2048 lds\n"
        ".Lch_nom_%=:\n"
        "ds_read_b64_tr_b16 a[32:33], v14 offset:8192\n"
        "ds_read_b64_tr_b16 a[34:35], v14 offset:8704\n"
        "ds_read_b64_tr_b16 a[36:37], v15 offset:8192\n"
        "ds_read_b64_tr_b16 a[38:39], v15 offset:8704\n"
        "ds_read_b64_tr_b16 a[40:41], v16 offset:8192\n"
        "ds_read_b64_tr_b16 a[42:43], v16 offset:8704\n"
        "ds_read_b64_tr_b16 a[44:45], v17 offset:8192\n"
        "ds_read_b64_tr_b16 a[46:47], v17 offset:8704\n"
        "s_waitcnt vmcnt(4)\n"
        "s_waitcnt lgkmcnt(0)\n"
        "s_barrier\n"
        "s_mov_b64 s[44:45], %1\n"
        "s_and_b32 s45, s45, 0xffff\n"
        "s_mov_b32 s46, 4718592\n"
        "s_add_u32 m0, s82, 20480\n"
        "s_mov_b32 s65, 2621440\n"
        "buffer_load_dwordx4 v6, s[44:47], s65 offen offset:0 lds\n"
        "s_mov_b32 s65, 2653184\n"
        "buffer_load_dwordx4 v7, s[44:47], s65 offen offset:1024 lds\n"
        "s_mov_b32 s65, 2684928\n"
        "buffer_load_dwordx4 v6, s[44:47], s65 offen offset:2048 lds\n"
        "s_mov_b32 s65, 2716672\n"
        "buffer_load_dwordx4 v7, s[44:47], s65 offen offset:3072 lds\n"
        "ds_read_b64_tr_b16 a[48:49], v14 offset:12288\n"
        "ds_read_b64_tr_b16 a[50:51], v14 offset:12800\n"
        "ds_read_b64_tr_b16 a[52:53], v15 offset:12288\n"
        "ds_read_b64_tr_b16 a[54:55], v15 offset:12800\n"
        "ds_read_b64_tr_b16 a[56:57], v16 offset:12288\n"
        "ds_read_b64_tr_b16 a[58:59], v16 offset:12800\n"
        "ds_read_b64_tr_b16 a[60:61], v17 offset:12288\n"
        "ds_read_b64_tr_b16 a[62:63], v17 offset:12800\n"
        "s_waitcnt vmcnt(4)\n"
        "s_waitcnt lgkmcnt(0)\n"
        "s_barrier\n"
        "s_add_u32 m0, s82, 24576\n"
        "s_mov_b32 s65, 3145728\n"
        "buffer_load_dwordx4 v6, s[44:47], s65 offen offset:0 lds\n"
        "s_mov_b32 s65, 3177472\n"
        "buffer_load_dwordx4 v7, s[44:47], s65 offen offset:1024 lds\n"
        "s_mov_b32 s65, 3209216\n"
        "buffer_load_dwordx4 v6, s[44:47], s65 offen offset:2048 lds\n"
        "s_mov_b32 s65, 3240960\n"
        "buffer_load_dwordx4 v7, s[44:47], s65 offen offset:3072 lds\n"
        "s_getpc_b64 s[44:45]\n"
        ".Lch_pc_%=:\n"
        "s_add_u32 s44, s44, .Lch_code_%=-.Lch_pc_%=\n"
        "s_addc_u32 s45, s45, 0\n"
        "s_and_b32 s45, s45, 0xffff\n"
        "s_and_b32 s44, s44, -4\n"
        "s_mov_b32 s46, .Lch_end_%=-.Lch_code_%=\n"
        "s_mov_b32 s47, 0x20000\n"
        "s_lshl_b32 s65, s48, 10\n"
        "buffer_load_dwordx4 v[64:67], v3, s[44:47], s65 offen\n"
        "s_add_u32 s65, s65, 4096\n"
        "buffer_load_dwordx4 v[68:71], v3, s[44:47], s65 offen\n"
        "s_add_u32 s65, s65, 4096\n"
        "ds_read_b64_tr_b16 a[64:65], v14 offset:16384\n"
        "ds_read_b64_tr_b16 a[66:67], v14 offset:16896\n"
        "ds_read_b64_tr_b16 a[68:69], v15 offset:16384\n"
        "ds_read_b64_tr_b16 a[70:71], v15 offset:16896\n"
        "ds_read_b64_tr_b16 a[72:73], v16 offset:16384\n"
        "ds_read_b64_tr_b16 a[74:75], v16 offset:16896\n"
        "ds_read_b64_tr_b16 a[76:77], v17 offset:16384\n"
        "ds_read_b64_tr_b16 a[78:79], v17 offset:16896\n"
        "s_waitcnt vmcnt(6)\n"
        "s_waitcnt lgkmcnt(0)\n"
        "s_barrier\n"
        "s_mov_b64 s[44:45], %1\n"
        "s_and_b32 s45, s45, 0xffff\n"
        "s_mov_b32 s46, 4718592\n"
        "s_add_u32 m0, s82, 28672\n"
        "s_mov_b32 s65, 3670016\n"
        "buffer_load_dwordx4 v6, s[44:47], s65 offen offset:0 lds\n"
        "s_mov_b32 s65, 3701760\n"
        "buffer_load_dwordx4 v7, s[44:47], s65 offen offset:1024 lds\n"
        "s_mov_b32 s65, 3733504\n"
        "buffer_load_dwordx4 v6, s[44:47], s65 offen offset:2048 lds\n"
        "s_mov_b32 s65, 3765248\n"
        "buffer_load_dwordx4 v7, s[44:47], s65 offen offset:3072 lds\n"
        "ds_read_b64_tr_b16 a[80:81], v14 offset:20480\n"
        "ds_read_b64_tr_b16 a[82:83], v14 offset:20992\n"
        "ds_read_b64_tr_b16 a[84:85], v15 offset:20480\n"
        "ds_read_b64_tr_b16 a[86:87], v15 offset:20992\n"
        "ds_read_b64_tr_b16 a[88:89], v16 offset:20480\n"
        "ds_read_b64_tr_b16 a[90:91], v16 offset:20992\n"
        "ds_read_b64_tr_b16 a[92:93], v17 offset:20480\n"
        "ds_read_b64_tr_b16 a[94:95], v17 offset:20992\n"
        "s_waitcnt vmcnt(6)\n"
        "s_waitcnt lgkmcnt(0)\n"
        "s_barrier\n"
        "s_add_u32 m0, s82, 0\n"
        "s_mov_b32 s65, 4194304\n"
        "buffer_load_dwordx4 v6, s[44:47], s65 offen offset:0 lds\n"
        "s_mov_b32 s65, 4226048\n"
        "buffer_load_dwordx4 v7, s[44:47], s65 offen offset:1024 lds\n"
        "s_mov_b32 s65, 4257792\n"
        "buffer_load_dwordx4 v6, s[44:47], s65 offen offset:2048 lds\n"
        "s_mov_b32 s65, 4289536\n"
        "buffer_load_dwordx4 v7, s[44:47], s65 offen offset:3072 lds\n"
        "ds_read_b64_tr_b16 a[96:97], v14 offset:24576\n"
        "ds_read_b64_tr_b16 a[98:99], v14 offset:25088\n"
        "ds_read_b64_tr_b16 a[100:101], v15 offset:24576\n"
        "ds_read_b64_tr_b16 a[102:103], v15 offset:25088\n"
        "ds_read_b64_tr_b16 a[104:105], v16 offset:24576\n"
        "ds_read_b64_tr_b16 a[106:107], v16 offset:25088\n"
        "ds_read_b64_tr_b16 a[108:109], v17 offset:24576\n"
        "ds_read_b64_tr_b16 a[110:111], v17 offset:25088\n"
        "s_waitcnt vmcnt(4)\n"
        "s_waitcnt lgkmcnt(0)\n"
        "s_barrier\n"
        "s_mov_b64 s[44:45], %2\n"
        "s_and_b32 s45, s45, 0xffff\n"
        "s_mov_b32 s46, 4718592\n"
        "s_add_u32 m0, s82, 4096\n"
        "s_mov_b32 s65, 0\n"
        "buffer_load_dwordx4 v6, s[44:47], s65 offen offset:0 lds\n"
        "s_mov_b32 s65, 31744\n"
        "buffer_load_dwordx4 v7, s[44:47], s65 offen offset:1024 lds\n"
        "s_mov_b32 s65, 63488\n"
        "buffer_load_dwordx4 v6, s[44:47], s65 offen offset:2048 lds\n"
        "s_mov_b32 s65, 95232\n"
        "buffer_load_dwordx4 v7, s[44:47], s65 offen offset:3072 lds\n"
        "ds_read_b64_tr_b16 a[112:113], v14 offset:28672\n"
        "ds_read_b64_tr_b16 a[114:115], v14 offset:29184\n"
        "ds_read_b64_tr_b16 a[116:117], v15 offset:28672\n"
        "ds_read_b64_tr_b16 a[118:119], v15 offset:29184\n"
        "ds_read_b64_tr_b16 a[120:121], v16 offset:28672\n"
        "ds_read_b64_tr_b16 a[122:123], v16 offset:29184\n"
        "ds_read_b64_tr_b16 a[124:125], v17 offset:28672\n"
        "ds_read_b64_tr_b16 a[126:127], v17 offset:29184\n"
        "s_waitcnt vmcnt(4)\n"
        "s_waitcnt lgkmcnt(0)\n"
        "s_barrier\n"
        "s_add_u32 m0, s82, 8192\n"
        "s_mov_b32 s65, 524288\n"
        "buffer_load_dwordx4 v6, s[44:47], s65 offen offset:0 lds\n"
        "s_mov_b32 s65, 556032\n"
        "buffer_load_dwordx4 v7, s[44:47], s65 offen offset:1024 lds\n"
        "s_mov_b32 s65, 587776\n"
        "buffer_load_dwordx4 v6, s[44:47], s65 offen offset:2048 lds\n"
        "s_mov_b32 s65, 619520\n"
        "buffer_load_dwordx4 v7, s[44:47], s65 offen offset:3072 lds\n"
        "ds_read_b64_tr_b16 a[128:129], v14 offset:0\n"
        "ds_read_b64_tr_b16 a[130:131], v14 offset:512\n"
        "ds_read_b64_tr_b16 a[132:133], v15 offset:0\n"
        "ds_read_b64_tr_b16 a[134:135], v15 offset:512\n"
        "ds_read_b64_tr_b16 a[136:137], v16 offset:0\n"
        "ds_read_b64_tr_b16 a[138:139], v16 offset:512\n"
        "ds_read_b64_tr_b16 a[140:141], v17 offset:0\n"
        "ds_read_b64_tr_b16 a[142:143], v17 offset:512\n"
        "s_waitcnt vmcnt(4)\n"
        "s_waitcnt lgkmcnt(0)\n"
        "s_barrier\n"
        "s_add_u32 m0, s82, 12288\n"
        "s_mov_b32 s65, 1048576\n"
        "buffer_load_dwordx4 v6, s[44:47], s65 offen offset:0 lds\n"
        "s_mov_b32 s65, 1080320\n"
        "buffer_load_dwordx4 v7, s[44:47], s65 offen offset:1024 lds\n"
        "s_mov_b32 s65, 1112064\n"
        "buffer_load_dwordx4 v6, s[44:47], s65 offen offset:2048 lds\n"
        "s_mov_b32 s65, 1143808\n"
        "buffer_load_dwordx4 v7, s[44:47], s65 offen offset:3072 lds\n"
        "ds_read_b64_tr_b16 a[144:145], v14 offset:4096\n"
        "ds_read_b64_tr_b16 a[146:147], v14 offset:4608\n"
        "ds_read_b64_tr_b16 a[148:149], v15 offset:4096\n"
        "ds_read_b64_tr_b16 a[150:151], v15 offset:4608\n"
        "ds_read_b64_tr_b16 a[152:153], v16 offset:4096\n"
        "ds_read_b64_tr_b16 a[154:155], v16 offset:4608\n"
        "ds_read_b64_tr_b16 a[156:157], v17 offset:4096\n"
        "ds_read_b64_tr_b16 a[158:159], v17 offset:4608\n"
        "s_waitcnt vmcnt(4)\n"
        "s_waitcnt lgkmcnt(0)\n"
        "s_barrier\n"
        "s_add_u32 m0, s82, 16384\n"
        "s_mov_b32 s65, 1572864\n"
        "buffer_load_dwordx4 v6, s[44:47], s65 offen offset:0 lds\n"
        "s_mov_b32 s65, 1604608\n"
        "buffer_load_dwordx4 v7, s[44:47], s65 offen offset:1024 lds\n"
        "s_mov_b32 s65, 1636352\n"
        "buffer_load_dwordx4 v6, s[44:47], s65 offen offset:2048 lds\n"
        "s_mov_b32 s65, 1668096\n"
        "buffer_load_dwordx4 v7, s[44:47], s65 offen offset:3072 lds\n"
        "ds_read_b64_tr_b16 a[160:161], v14 offset:8192\n"
        "ds_read_b64_tr_b16 a[162:163], v14 offset:8704\n"
        "ds_read_b64_tr_b16 a[164:165], v15 offset:8192\n"
        "ds_read_b64_tr_b16 a[166:167], v15 offset:8704\n"
        "ds_read_b64_tr_b16 a[168:169], v16 offset:8192\n"
        "ds_read_b64_tr_b16 a[170:171], v16 offset:8704\n"
        "ds_read_b64_tr_b16 a[172:173], v17 offset:8192\n"
        "ds_read_b64_tr_b16 a[174:175], v17 offset:8704\n"
        "s_waitcnt vmcnt(4)\n"
        "s_waitcnt lgkmcnt(0)\n"
        "s_barrier\n"
        "s_add_u32 m0, s82, 20480\n"
        "s_mov_b32 s65, 2097152\n"
        "buffer_load_dwordx4 v6, s[44:47], s65 offen offset:0 lds\n"
        "s_mov_b32 s65, 2128896\n"
        "buffer_load_dwordx4 v7, s[44:47], s65 offen offset:1024 lds\n"
        "s_mov_b32 s65, 2160640\n"
        "buffer_load_dwordx4 v6, s[44:47], s65 offen offset:2048 lds\n"
        "s_mov_b32 s65, 2192384\n"
        "buffer_load_dwordx4 v7, s[44:47], s65 offen offset:3072 lds\n"
        "ds_read_b64_tr_b16 a[176:177], v14 offset:12288\n"
        "ds_read_b64_tr_b16 a[178:179], v14 offset:12800\n"
        "ds_read_b64_tr_b16 a[180:181], v15 offset:12288\n"
        "ds_read_b64_tr_b16 a[182:183], v15 offset:12800\n"
        "ds_read_b64_tr_b16 a[184:185], v16 offset:12288\n"
        "ds_read_b64_tr_b16 a[186:187], v16 offset:12800\n"
        "ds_read_b64_tr_b16 a[188:189], v17 offset:12288\n"
        "ds_read_b64_tr_b16 a[190:191], v17 offset:12800\n"
        "s_waitcnt vmcnt(4)\n"
        "s_waitcnt lgkmcnt(0)\n"
        "s_barrier\n"
        "s_add_u32 m0, s82, 24576\n"
        "s_mov_b32 s65, 2621440\n"
        "buffer_load_dwordx4 v6, s[44:47], s65 offen offset:0 lds\n"
        "s_mov_b32 s65, 2653184\n"
        "buffer_load_dwordx4 v7, s[44:47], s65 offen offset:1024 lds\n"
        "s_mov_b32 s65, 2684928\n"
        "buffer_load_dwordx4 v6, s[44:47], s65 offen offset:2048 lds\n"
        "s_mov_b32 s65, 2716672\n"
        "buffer_load_dwordx4 v7, s[44:47], s65 offen offset:3072 lds\n"
        "ds_read_b128 v[24:27], v11 offset:0\n"
        "ds_read_b128 v[28:31], v11 offset:128\n"
        "ds_read_b128 v[32:35], v11 offset:256\n"
        "s_min_u32 s65, s48, 2\n"
        "s_mul_i32 s65, s65, 3072\n"
        "s_add_u32 s65, s65, 151552\n"
        "v_add_u32 v36, s65, v3\n"
        "ds_read_b64_tr_b16 a[192:193], v14 offset:16384\n"
        "ds_read_b64_tr_b16 a[194:195], v14 offset:16896\n"
        "ds_read_b64_tr_b16 a[196:197], v15 offset:16384\n"
        "ds_read_b64_tr_b16 a[198:199], v15 offset:16896\n"
        "ds_read_b64_tr_b16 a[200:201], v16 offset:16384\n"
        "ds_read_b64_tr_b16 a[202:203], v16 offset:16896\n"
        "ds_read_b64_tr_b16 a[204:205], v17 offset:16384\n"
        "ds_read_b64_tr_b16 a[206:207], v17 offset:16896\n"
        "s_waitcnt vmcnt(4)\n"
        "s_waitcnt lgkmcnt(0)\n"
        "s_barrier\n"
        "s_add_u32 m0, s82, 28672\n"
        "s_mov_b32 s65, 3145728\n"
        "buffer_load_dwordx4 v6, s[44:47], s65 offen offset:0 lds\n"
        "s_mov_b32 s65, 3177472\n"
        "buffer_load_dwordx4 v7, s[44:47], s65 offen offset:1024 lds\n"
        "s_mov_b32 s65, 3209216\n"
        "buffer_load_dwordx4 v6, s[44:47], s65 offen offset:2048 lds\n"
        "s_mov_b32 s65, 3240960\n"
        "buffer_load_dwordx4 v7, s[44:47], s65 offen offset:3072 lds\n"
        "ds_write_b128 v36, v[24:27] offset:0\n"
        "ds_write_b128 v36, v[28:31] offset:1024\n"
        "ds_write_b128 v36, v[32:35] offset:2048\n"
        "ds_read_b64_tr_b16 a[208:209], v14 offset:20480\n"
        "ds_read_b64_tr_b16 a[210:211], v14 offset:20992\n"
        "ds_read_b64_tr_b16 a[212:213], v15 offset:20480\n"
        "ds_read_b64_tr_b16 a[214:215], v15 offset:20992\n"
        "ds_read_b64_tr_b16 a[216:217], v16 offset:20480\n"
        "ds_read_b64_tr_b16 a[218:219], v16 offset:20992\n"
        "ds_read_b64_tr_b16 a[220:221], v17 offset:20480\n"
        "ds_read_b64_tr_b16 a[222:223], v17 offset:20992\n"
        "s_waitcnt vmcnt(4)\n"
        "s_waitcnt lgkmcnt(0)\n"
        "s_barrier\n"
        "s_add_u32 m0, s82, 0\n"
        "s_mov_b32 s65, 3670016\n"
        "buffer_load_dwordx4 v6, s[44:47], s65 offen offset:0 lds\n"
        "s_mov_b32 s65, 3701760\n"
        "buffer_load_dwordx4 v7, s[44:47], s65 offen offset:1024 lds\n"
        "s_mov_b32 s65, 3733504\n"
        "buffer_load_dwordx4 v6, s[44:47], s65 offen offset:2048 lds\n"
        "s_mov_b32 s65, 3765248\n"
        "buffer_load_dwordx4 v7, s[44:47], s65 offen offset:3072 lds\n"
        "ds_read_b64_tr_b16 a[224:225], v14 offset:24576\n"
        "ds_read_b64_tr_b16 a[226:227], v14 offset:25088\n"
        "ds_read_b64_tr_b16 a[228:229], v15 offset:24576\n"
        "ds_read_b64_tr_b16 a[230:231], v15 offset:25088\n"
        "ds_read_b64_tr_b16 a[232:233], v16 offset:24576\n"
        "ds_read_b64_tr_b16 a[234:235], v16 offset:25088\n"
        "ds_read_b64_tr_b16 a[236:237], v17 offset:24576\n"
        "ds_read_b64_tr_b16 a[238:239], v17 offset:25088\n"
        "s_waitcnt vmcnt(4)\n"
        "s_waitcnt lgkmcnt(0)\n"
        "s_barrier\n"
        "s_add_u32 m0, s82, 4096\n"
        "s_mov_b32 s65, 4194304\n"
        "buffer_load_dwordx4 v6, s[44:47], s65 offen offset:0 lds\n"
        "s_mov_b32 s65, 4226048\n"
        "buffer_load_dwordx4 v7, s[44:47], s65 offen offset:1024 lds\n"
        "s_mov_b32 s65, 4257792\n"
        "buffer_load_dwordx4 v6, s[44:47], s65 offen offset:2048 lds\n"
        "s_mov_b32 s65, 4289536\n"
        "buffer_load_dwordx4 v7, s[44:47], s65 offen offset:3072 lds\n"
        "ds_read_b64_tr_b16 a[240:241], v14 offset:28672\n"
        "ds_read_b64_tr_b16 a[242:243], v14 offset:29184\n"
        "ds_read_b64_tr_b16 a[244:245], v15 offset:28672\n"
        "ds_read_b64_tr_b16 a[246:247], v15 offset:29184\n"
        "ds_read_b64_tr_b16 a[248:249], v16 offset:28672\n"
        "ds_read_b64_tr_b16 a[250:251], v16 offset:29184\n"
        "ds_read_b64_tr_b16 a[252:253], v17 offset:28672\n"
        "ds_read_b64_tr_b16 a[254:255], v17 offset:29184\n"
        "s_waitcnt vmcnt(4)\n"
        "s_waitcnt lgkmcnt(0)\n"
        "s_barrier\n"
        "s_mov_b64 s[44:45], %4\n"
        "s_and_b32 s45, s45, 0xffff\n"
        "s_mov_b32 s46, 4718592\n"
        "s_add_u32 m0, s82, 8192\n"
        "s_mov_b32 s65, 0\n"
        "buffer_load_dwordx4 v9, s[44:47], s65 offen offset:0 lds\n"
        "s_mov_b32 s65, 3584\n"
        "buffer_load_dwordx4 v9, s[44:47], s65 offen offset:1024 lds\n"
        "s_mov_b32 s65, 7168\n"
        "buffer_load_dwordx4 v9, s[44:47], s65 offen offset:2048 lds\n"
        "s_mov_b32 s65, 10752\n"
        "buffer_load_dwordx4 v9, s[44:47], s65 offen offset:3072 lds\n"
        "ds_read_b64_tr_b16 v[80:81], v14 offset:0\n"
        "ds_read_b64_tr_b16 v[82:83], v14 offset:512\n"
        "ds_read_b64_tr_b16 v[84:85], v15 offset:0\n"
        "ds_read_b64_tr_b16 v[86:87], v15 offset:512\n"
        "ds_read_b64_tr_b16 v[88:89], v16 offset:0\n"
        "ds_read_b64_tr_b16 v[90:91], v16 offset:512\n"
        "ds_read_b64_tr_b16 v[92:93], v17 offset:0\n"
        "ds_read_b64_tr_b16 v[94:95], v17 offset:512\n"
        "s_waitcnt vmcnt(4)\n"
        "s_waitcnt lgkmcnt(0)\n"
        "s_barrier\n"
        "s_add_u32 m0, s82, 12288\n"
        "s_mov_b32 s65, 36864\n"
        "buffer_load_dwordx4 v9, s[44:47], s65 offen offset:0 lds\n"
        "s_mov_b32 s65, 40448\n"
        "buffer_load_dwordx4 v9, s[44:47], s65 offen offset:1024 lds\n"
        "s_mov_b32 s65, 44032\n"
        "buffer_load_dwordx4 v9, s[44:47], s65 offen offset:2048 lds\n"
        "s_mov_b32 s65, 47616\n"
        "buffer_load_dwordx4 v9, s[44:47], s65 offen offset:3072 lds\n"
        "ds_read_b64_tr_b16 v[96:97], v14 offset:4096\n"
        "ds_read_b64_tr_b16 v[98:99], v14 offset:4608\n"
        "ds_read_b64_tr_b16 v[100:101], v15 offset:4096\n"
        "ds_read_b64_tr_b16 v[102:103], v15 offset:4608\n"
        "ds_read_b64_tr_b16 v[104:105], v16 offset:4096\n"
        "ds_read_b64_tr_b16 v[106:107], v16 offset:4608\n"
        "ds_read_b64_tr_b16 v[108:109], v17 offset:4096\n"
        "ds_read_b64_tr_b16 v[110:111], v17 offset:4608\n"
        "s_cmp_lt_u32 s48, 2\n"
        "s_cbranch_scc0 .Lch_noex_%=\n"
        "s_lshl_b32 s66, s48, 6\n"
        "s_add_u32 m0, s82, 32768\n"
        "s_sub_u32 s65, 1024, s66\n"
        "buffer_load_dwordx4 v8, s[44:47], s65 offen lds\n"
        "s_sub_u32 s65, 9216, s66\n"
        "buffer_load_dwordx4 v8, s[44:47], s65 offen offset:1024 lds\n"
        "s_add_u32 m0, s82, 34816\n"
        "s_sub_u32 s65, 37888, s66\n"
        "buffer_load_dwordx4 v8, s[44:47], s65 offen lds\n"
        "s_sub_u32 s65, 46080, s66\n"
        "buffer_load_dwordx4 v8, s[44:47], s65 offen offset:1024 lds\n"
        "s_add_u32 m0, s82, 36864\n"
        "s_sub_u32 s65, 74752, s66\n"
        "buffer_load_dwordx4 v8, s[44:47], s65 offen lds\n"
        "s_sub_u32 s65, 82944, s66\n"
        "buffer_load_dwordx4 v8, s[44:47], s65 offen offset:1024 lds\n"
        "s_add_u32 m0, s82, 38912\n"
        "s_sub_u32 s65, 111616, s66\n"
        "buffer_load_dwordx4 v8, s[44:47], s65 offen lds\n"
        "s_sub_u32 s65, 119808, s66\n"
        "buffer_load_dwordx4 v8, s[44:47], s65 offen offset:1024 lds\n"
        ".Lch_noex_%=:\n"
        "s_waitcnt vmcnt(4)\n"
        "s_waitcnt lgkmcnt(0)\n"
        "s_barrier\n"
        "s_add_u32 m0, s82, 16384\n"
        "s_mov_b32 s65, 73728\n"
        "buffer_load_dwordx4 v9, s[44:47], s65 offen offset:0 lds\n"
        "s_mov_b32 s65, 77312\n"
        "buffer_load_dwordx4 v9, s[44:47], s65 offen offset:1024 lds\n"
        "s_mov_b32 s65, 80896\n"
        "buffer_load_dwordx4 v9, s[44:47], s65 offen offset:2048 lds\n"
        "s_mov_b32 s65, 84480\n"
        "buffer_load_dwordx4 v9, s[44:47], s65 offen offset:3072 lds\n"
        "ds_read_b64_tr_b16 v[112:113], v12 offset:8192\n"
        "ds_read_b64_tr_b16 v[114:115], v12 offset:8448\n"
        "ds_read_b64_tr_b16 v[116:117], v12 offset:10240\n"
        "ds_read_b64_tr_b16 v[118:119], v12 offset:10496\n"
        "ds_read_b64_tr_b16 v[144:145], v13 offset:8192\n"
        "ds_read_b64_tr_b16 v[146:147], v13 offset:8448\n"
        "ds_read_b64_tr_b16 v[148:149], v13 offset:10240\n"
        "ds_read_b64_tr_b16 v[150:151], v13 offset:10496\n"
        "s_waitcnt vmcnt(4)\n"
        "s_waitcnt lgkmcnt(0)\n"
        "s_barrier\n"
        "s_add_u32 m0, s82, 20480\n"
        "s_mov_b32 s65, 110592\n"
        "buffer_load_dwordx4 v9, s[44:47], s65 offen offset:0 lds\n"
        "s_mov_b32 s65, 114176\n"
        "buffer_load_dwordx4 v9, s[44:47], s65 offen offset:1024 lds\n"
        "s_mov_b32 s65, 117760\n"
        "buffer_load_dwordx4 v9, s[44:47], s65 offen offset:2048 lds\n"
        "s_mov_b32 s65, 121344\n"
        "buffer_load_dwordx4 v9, s[44:47], s65 offen offset:3072 lds\n"
        "ds_read_b64_tr_b16 v[120:121], v12 offset:12288\n"
        "ds_read_b64_tr_b16 v[122:123], v12 offset:12544\n"
        "ds_read_b64_tr_b16 v[124:125], v12 offset:14336\n"
        "ds_read_b64_tr_b16 v[126:127], v12 offset:14592\n"
        "ds_read_b64_tr_b16 v[152:153], v13 offset:12288\n"
        "ds_read_b64_tr_b16 v[154:155], v13 offset:12544\n"
        "ds_read_b64_tr_b16 v[156:157], v13 offset:14336\n"
        "ds_read_b64_tr_b16 v[158:159], v13 offset:14592\n"
        "s_waitcnt vmcnt(4)\n"
        "s_waitcnt lgkmcnt(0)\n"
        "s_barrier\n"
        "s_add_u32 m0, s82, 24576\n"
        "s_mov_b32 s65, 512\n"
        "buffer_load_dwordx4 v9, s[44:47], s65 offen offset:0 lds\n"
        "s_mov_b32 s65, 4096\n"
        "buffer_load_dwordx4 v9, s[44:47], s65 offen offset:1024 lds\n"
        "s_mov_b32 s65, 7680\n"
        "buffer_load_dwordx4 v9, s[44:47], s65 offen offset:2048 lds\n"
        "s_mov_b32 s65, 11264\n"
        "buffer_load_dwordx4 v9, s[44:47], s65 offen offset:3072 lds\n"
        "ds_read_b64_tr_b16 v[128:129], v12 offset:16384\n"
        "ds_read_b64_tr_b16 v[130:131], v12 offset:16640\n"
        "ds_read_b64_tr_b16 v[132:133], v12 offset:18432\n"
        "ds_read_b64_tr_b16 v[134:135], v12 offset:18688\n"
        "ds_read_b64_tr_b16 v[160:161], v13 offset:16384\n"
        "ds_read_b64_tr_b16 v[162:163], v13 offset:16640\n"
        "ds_read_b64_tr_b16 v[164:165], v13 offset:18432\n"
        "ds_read_b64_tr_b16 v[166:167], v13 offset:18688\n"
        "s_waitcnt vmcnt(4)\n"
        "s_waitcnt lgkmcnt(0)\n"
        "s_barrier\n"
        "s_add_u32 m0, s82, 28672\n"
        "s_mov_b32 s65, 37376\n"
        "buffer_load_dwordx4 v9, s[44:47], s65 offen offset:0 lds\n"
        "s_mov_b32 s65, 40960\n"
        "buffer_load_dwordx4 v9, s[44:47], s65 offen offset:1024 lds\n"
        "s_mov_b32 s65, 44544\n"
        "buffer_load_dwordx4 v9, s[44:47], s65 offen offset:2048 lds\n"
        "s_mov_b32 s65, 48128\n"
        "buffer_load_dwordx4 v9, s[44:47], s65 offen offset:3072 lds\n"
        "ds_read_b64_tr_b16 v[136:137], v12 offset:20480\n"
        "ds_read_b64_tr_b16 v[138:139], v12 offset:20736\n"
        "ds_read_b64_tr_b16 v[140:141], v12 offset:22528\n"
        "ds_read_b64_tr_b16 v[142:143], v12 offset:22784\n"
        "ds_read_b64_tr_b16 v[168:169], v13 offset:20480\n"
        "ds_read_b64_tr_b16 v[170:171], v13 offset:20736\n"
        "ds_read_b64_tr_b16 v[172:173], v13 offset:22528\n"
        "ds_read_b64_tr_b16 v[174:175], v13 offset:22784\n"
        "s_waitcnt vmcnt(4)\n"
        "s_waitcnt lgkmcnt(0)\n"
        "s_barrier\n"
        "s_add_u32 m0, s82, 0\n"
        "s_mov_b32 s65, 74240\n"
        "buffer_load_dwordx4 v9, s[44:47], s65 offen offset:0 lds\n"
        "s_mov_b32 s65, 77824\n"
        "buffer_load_dwordx4 v9, s[44:47], s65 offen offset:1024 lds\n"
        "s_mov_b32 s65, 81408\n"
        "buffer_load_dwordx4 v9, s[44:47], s65 offen offset:2048 lds\n"
        "s_mov_b32 s65, 84992\n"
        "buffer_load_dwordx4 v9, s[44:47], s65 offen offset:3072 lds\n"
        "ds_read_b64_tr_b16 v[176:177], v12 offset:24576\n"
        "ds_read_b64_tr_b16 v[178:179], v12 offset:24832\n"
        "ds_read_b64_tr_b16 v[180:181], v12 offset:26624\n"
        "ds_read_b64_tr_b16 v[182:183], v12 offset:26880\n"
        "ds_read_b64_tr_b16 v[208:209], v13 offset:24576\n"
        "ds_read_b64_tr_b16 v[210:211], v13 offset:24832\n"
        "ds_read_b64_tr_b16 v[212:213], v13 offset:26624\n"
        "ds_read_b64_tr_b16 v[214:215], v13 offset:26880\n"
        "s_waitcnt vmcnt(4)\n"
        "s_waitcnt lgkmcnt(0)\n"
        "s_barrier\n"
        "s_add_u32 m0, s82, 4096\n"
        "s_mov_b32 s65, 111104\n"
        "buffer_load_dwordx4 v9, s[44:47], s65 offen offset:0 lds\n"
        "s_mov_b32 s65, 114688\n"
        "buffer_load_dwordx4 v9, s[44:47], s65 offen offset:1024 lds\n"
        "s_mov_b32 s65, 118272\n"
        "buffer_load_dwordx4 v9, s[44:47], s65 offen offset:2048 lds\n"
        "s_mov_b32 s65, 121856\n"
        "buffer_load_dwordx4 v9, s[44:47], s65 offen offset:3072 lds\n"
        "ds_read_b64_tr_b16 v[184:185], v12 offset:28672\n"
        "ds_read_b64_tr_b16 v[186:187], v12 offset:28928\n"
        "ds_read_b64_tr_b16 v[188:189], v12 offset:30720\n"
        "ds_read_b64_tr_b16 v[190:191], v12 offset:30976\n"
        "ds_read_b64_tr_b16 v[216:217], v13 offset:28672\n"
        "ds_read_b64_tr_b16 v[218:219], v13 offset:28928\n"
        "ds_read_b64_tr_b16 v[220:221], v13 offset:30720\n"
        "ds_read_b64_tr_b16 v[222:223], v13 offset:30976\n"
        "s_waitcnt vmcnt(4)\n"
        "s_waitcnt lgkmcnt(0)\n"
        "s_barrier\n"
        "s_mov_b64 s[44:45], %3\n"
        "s_and_b32 s45, s45, 0xffff\n"
        "s_mov_b32 s46, 4718592\n"
        "s_add_u32 m0, s82, 8192\n"
        "s_mov_b32 s65, 4194304\n"
        "buffer_load_dwordx4 v6, s[44:47], s65 offen offset:0 lds\n"
        "s_mov_b32 s65, 4226048\n"
        "buffer_load_dwordx4 v7, s[44:47], s65 offen offset:1024 lds\n"
        "s_mov_b32 s65, 4257792\n"
        "buffer_load_dwordx4 v6, s[44:47], s65 offen offset:2048 lds\n"
        "s_mov_b32 s65, 4289536\n"
        "buffer_load_dwordx4 v7, s[44:47], s65 offen offset:3072 lds\n"
        "ds_read_b64_tr_b16 v[192:193], v12 offset:0\n"
        "ds_read_b64_tr_b16 v[194:195], v12 offset:256\n"
        "ds_read_b64_tr_b16 v[196:197], v12 offset:2048\n"
        "ds_read_b64_tr_b16 v[198:199], v12 offset:2304\n"
        "ds_read_b64_tr_b16 v[224:225], v13 offset:0\n"
        "ds_read_b64_tr_b16 v[226:227], v13 offset:256\n"
        "ds_read_b64_tr_b16 v[228:229], v13 offset:2048\n"
        "ds_read_b64_tr_b16 v[230:231], v13 offset:2304\n"
        "s_cmp_lt_u32 s48, 2\n"
        "s_cbranch_scc0 .Lch_noex2_%=\n"
        "ds_read_b64_tr_b16 v[24:25], v10 offset:32768\n"
        "ds_read_b64_tr_b16 v[26:27], v10 offset:33024\n"
        "ds_read_b64_tr_b16 v[28:29], v10 offset:33792\n"
        "ds_read_b64_tr_b16 v[30:31], v10 offset:34048\n"
        "ds_read_b64_tr_b16 v[32:33], v10 offset:34816\n"
        "ds_read_b64_tr_b16 v[34:35], v10 offset:35072\n"
        "ds_read_b64_tr_b16 v[36:37], v10 offset:35840\n"
        "ds_read_b64_tr_b16 v[38:39], v10 offset:36096\n"
        "ds_read_b64_tr_b16 v[40:41], v10 offset:36864\n"
        "ds_read_b64_tr_b16 v[42:43], v10 offset:37120\n"
        "ds_read_b64_tr_b16 v[44:45], v10 offset:37888\n"
        "ds_read_b64_tr_b16 v[46:47], v10 offset:38144\n"
        "ds_read_b64_tr_b16 v[48:49], v10 offset:38912\n"
        "ds_read_b64_tr_b16 v[50:51], v10 offset:39168\n"
        "ds_read_b64_tr_b16 v[52:53], v10 offset:39936\n"
        "ds_read_b64_tr_b16 v[54:55], v10 offset:40192\n"
        "s_waitcnt lgkmcnt(0)\n"
        "ds_write_b128 v2, v[24:27] offset:32768\n"
        "ds_write_b128 v2, v[28:31] offset:33792\n"
        "ds_write_b128 v2, v[32:35] offset:34816\n"
        "ds_write_b128 v2, v[36:39] offset:35840\n"
        "ds_write_b128 v2, v[40:43] offset:36864\n"
        "ds_write_b128 v2, v[44:47] offset:37888\n"
        "ds_write_b128 v2, v[48:51] offset:38912\n"
        "ds_write_b128 v2, v[52:55] offset:39936\n"
        ".Lch_noex2_%=:\n"
        "s_waitcnt vmcnt(4)\n"
        "s_waitcnt lgkmcnt(0)\n"
        "s_barrier\n"
        "s_add_u32 m0, s82, 12288\n"
        "s_mov_b32 s65, 1572864\n"
        "buffer_load_dwordx4 v6, s[44:47], s65 offen offset:0 lds\n"
        "s_mov_b32 s65, 1604608\n"
        "buffer_load_dwordx4 v7, s[44:47], s65 offen offset:1024 lds\n"
        "s_mov_b32 s65, 1636352\n"
        "buffer_load_dwordx4 v6, s[44:47], s65 offen offset:2048 lds\n"
        "s_mov_b32 s65, 1668096\n"
        "buffer_load_dwordx4 v7, s[44:47], s65 offen offset:3072 lds\n"
        "ds_read_b64_tr_b16 v[200:201], v12 offset:4096\n"
        "ds_read_b64_tr_b16 v[202:203], v12 offset:4352\n"
        "ds_read_b64_tr_b16 v[204:205], v12 offset:6144\n"
        "ds_read_b64_tr_b16 v[206:207], v12 offset:6400\n"
        "ds_read_b64_tr_b16 v[232:233], v13 offset:4096\n"
        "ds_read_b64_tr_b16 v[234:235], v13 offset:4352\n"
        "ds_read_b64_tr_b16 v[236:237], v13 offset:6144\n"
        "ds_read_b64_tr_b16 v[238:239], v13 offset:6400\n"
        "s_waitcnt vmcnt(4)\n"
        "s_waitcnt lgkmcnt(0)\n"
        "s_barrier\n"
        "s_add_u32 m0, s82, 16384\n"
        "s_mov_b32 s65, 2097152\n"
        "buffer_load_dwordx4 v6, s[44:47], s65 offen offset:0 lds\n"
        "s_mov_b32 s65, 2128896\n"
        "buffer_load_dwordx4 v7, s[44:47], s65 offen offset:1024 lds\n"
        "s_mov_b32 s65, 2160640\n"
        "buffer_load_dwordx4 v6, s[44:47], s65 offen offset:2048 lds\n"
        "s_mov_b32 s65, 2192384\n"
        "buffer_load_dwordx4 v7, s[44:47], s65 offen offset:3072 lds\n"
        "ds_read_b64_tr_b16 v[240:241], v14 offset:8192\n"
        "ds_read_b64_tr_b16 v[242:243], v14 offset:8704\n"
        "ds_read_b64_tr_b16 v[244:245], v15 offset:8192\n"
        "ds_read_b64_tr_b16 v[246:247], v15 offset:8704\n"
        "ds_read_b64_tr_b16 v[248:249], v16 offset:8192\n"
        "ds_read_b64_tr_b16 v[250:251], v16 offset:8704\n"
        "ds_read_b64_tr_b16 v[252:253], v17 offset:8192\n"
        "ds_read_b64_tr_b16 v[254:255], v17 offset:8704\n"
        "s_waitcnt vmcnt(4)\n"
        "s_waitcnt lgkmcnt(0)\n"
        "s_barrier\n"
        "s_add_u32 m0, s82, 20480\n"
        "s_mov_b32 s65, 2621440\n"
        "buffer_load_dwordx4 v6, s[44:47], s65 offen offset:0 lds\n"
        "s_mov_b32 s65, 2653184\n"
        "buffer_load_dwordx4 v7, s[44:47], s65 offen offset:1024 lds\n"
        "s_mov_b32 s65, 2684928\n"
        "buffer_load_dwordx4 v6, s[44:47], s65 offen offset:2048 lds\n"
        "s_mov_b32 s65, 2716672\n"
        "buffer_load_dwordx4 v7, s[44:47], s65 offen offset:3072 lds\n"
        "ds_read_b64_tr_b16 v[40:41], v14 offset:12288\n"
        "ds_read_b64_tr_b16 v[42:43], v14 offset:12800\n"
        "ds_read_b64_tr_b16 v[44:45], v15 offset:12288\n"
        "ds_read_b64_tr_b16 v[46:47], v15 offset:12800\n"
        "ds_read_b64_tr_b16 v[48:49], v16 offset:12288\n"
        "ds_read_b64_tr_b16 v[50:51], v16 offset:12800\n"
        "ds_read_b64_tr_b16 v[52:53], v17 offset:12288\n"
        "ds_read_b64_tr_b16 v[54:55], v17 offset:12800\n"
        "s_waitcnt vmcnt(4)\n"
        "s_waitcnt lgkmcnt(0)\n"
        "s_barrier\n"
        "ds_write_b128 v2, v[40:43] offset:12288\n"
        "ds_write_b128 v2, v[44:47] offset:13312\n"
        "ds_write_b128 v2, v[48:51] offset:14336\n"
        "ds_write_b128 v2, v[52:55] offset:15360\n"
        "s_add_u32 m0, s82, 24576\n"
        "s_mov_b32 s65, 3145728\n"
        "buffer_load_dwordx4 v6, s[44:47], s65 offen offset:0 lds\n"
        "s_mov_b32 s65, 3177472\n"
        "buffer_load_dwordx4 v7, s[44:47], s65 offen offset:1024 lds\n"
        "s_mov_b32 s65, 3209216\n"
        "buffer_load_dwordx4 v6, s[44:47], s65 offen offset:2048 lds\n"
        "s_mov_b32 s65, 3240960\n"
        "buffer_load_dwordx4 v7, s[44:47], s65 offen offset:3072 lds\n"
        "ds_read_b64_tr_b16 v[24:25], v14 offset:16384\n"
        "ds_read_b64_tr_b16 v[26:27], v14 offset:16896\n"
        "ds_read_b64_tr_b16 v[28:29], v15 offset:16384\n"
        "ds_read_b64_tr_b16 v[30:31], v15 offset:16896\n"
        "ds_read_b64_tr_b16 v[32:33], v16 offset:16384\n"
        "ds_read_b64_tr_b16 v[34:35], v16 offset:16896\n"
        "ds_read_b64_tr_b16 v[36:37], v17 offset:16384\n"
        "ds_read_b64_tr_b16 v[38:39], v17 offset:16896\n"
        "s_waitcnt vmcnt(4)\n"
        "s_waitcnt lgkmcnt(0)\n"
        "s_barrier\n"
        "ds_write_b128 v2, v[24:27] offset:16384\n"
        "ds_write_b128 v2, v[28:31] offset:17408\n"
        "ds_write_b128 v2, v[32:35] offset:18432\n"
        "ds_write_b128 v2, v[36:39] offset:19456\n"
        "s_add_u32 m0, s82, 28672\n"
        "s_mov_b32 s65, 3670016\n"
        "buffer_load_dwordx4 v6, s[44:47], s65 offen offset:0 lds\n"
        "s_mov_b32 s65, 3701760\n"
        "buffer_load_dwordx4 v7, s[44:47], s65 offen offset:1024 lds\n"
        "s_mov_b32 s65, 3733504\n"
        "buffer_load_dwordx4 v6, s[44:47], s65 offen offset:2048 lds\n"
        "s_mov_b32 s65, 3765248\n"
        "buffer_load_dwordx4 v7, s[44:47], s65 offen offset:3072 lds\n"
        "ds_read_b64_tr_b16 v[40:41], v14 offset:20480\n"
        "ds_read_b64_tr_b16 v[42:43], v14 offset:20992\n"
        "ds_read_b64_tr_b16 v[44:45], v15 offset:20480\n"
        "ds_read_b64_tr_b16 v[46:47], v15 offset:20992\n"
        "ds_read_b64_tr_b16 v[48:49], v16 offset:20480\n"
        "ds_read_b64_tr_b16 v[50:51], v16 offset:20992\n"
        "ds_read_b64_tr_b16 v[52:53], v17 offset:20480\n"
        "ds_read_b64_tr_b16 v[54:55], v17 offset:20992\n"
        "s_waitcnt vmcnt(4)\n"
        "s_waitcnt lgkmcnt(0)\n"
        "s_barrier\n"
        "ds_write_b128 v2, v[40:43] offset:20480\n"
        "ds_write_b128 v2, v[44:47] offset:21504\n"
        "ds_write_b128 v2, v[48:51] offset:22528\n"
        "ds_write_b128 v2, v[52:55] offset:23552\n"
        "s_add_u32 m0, s82, 0\n"
        "s_mov_b32 s65, 0\n"
        "buffer_load_dwordx4 v6, s[44:47], s65 offen offset:0 lds\n"
        "s_mov_b32 s65, 31744\n"
        "buffer_load_dwordx4 v7, s[44:47], s65 offen offset:1024 lds\n"
        "s_mov_b32 s65, 63488\n"
        "buffer_load_dwordx4 v6, s[44:47], s65 offen offset:2048 lds\n"
        "s_mov_b32 s65, 95232\n"
        "buffer_load_dwordx4 v7, s[44:47], s65 offen offset:3072 lds\n"
        "ds_read_b64_tr_b16 v[24:25], v14 offset:24576\n"
        "ds_read_b64_tr_b16 v[26:27], v14 offset:25088\n"
        "ds_read_b64_tr_b16 v[28:29], v15 offset:24576\n"
        "ds_read_b64_tr_b16 v[30:31], v15 offset:25088\n"
        "ds_read_b64_tr_b16 v[32:33], v16 offset:24576\n"
        "ds_read_b64_tr_b16 v[34:35], v16 offset:25088\n"
        "ds_read_b64_tr_b16 v[36:37], v17 offset:24576\n"
        "ds_read_b64_tr_b16 v[38:39], v17 offset:25088\n"
        "s_waitcnt vmcnt(4)\n"
        "s_waitcnt lgkmcnt(0)\n"
        "s_barrier\n"
        "ds_write_b128 v2, v[24:27] offset:24576\n"
        "ds_write_b128 v2, v[28:31] offset:25600\n"
        "ds_write_b128 v2, v[32:35] offset:26624\n"
        "ds_write_b128 v2, v[36:39] offset:27648\n"
        "s_add_u32 m0, s82, 4096\n"
        "s_mov_b32 s65, 524288\n"
        "buffer_load_dwordx4 v6, s[44:47], s65 offen offset:0 lds\n"
        "s_mov_b32 s65, 556032\n"
        "buffer_load_dwordx4 v7, s[44:47], s65 offen offset:1024 lds\n"
        "s_mov_b32 s65, 587776\n"
        "buffer_load_dwordx4 v6, s[44:47], s65 offen offset:2048 lds\n"
        "s_mov_b32 s65, 619520\n"
        "buffer_load_dwordx4 v7, s[44:47], s65 offen offset:3072 lds\n"
        "ds_read_b64_tr_b16 v[40:41], v14 offset:28672\n"
        "ds_read_b64_tr_b16 v[42:43], v14 offset:29184\n"
        "ds_read_b64_tr_b16 v[44:45], v15 offset:28672\n"
        "ds_read_b64_tr_b16 v[46:47], v15 offset:29184\n"
        "ds_read_b64_tr_b16 v[48:49], v16 offset:28672\n"
        "ds_read_b64_tr_b16 v[50:51], v16 offset:29184\n"
        "ds_read_b64_tr_b16 v[52:53], v17 offset:28672\n"
        "ds_read_b64_tr_b16 v[54:55], v17 offset:29184\n"
        "s_waitcnt vmcnt(4)\n"
        "s_waitcnt lgkmcnt(0)\n"
        "s_barrier\n"
        "ds_write_b128 v2, v[40:43] offset:28672\n"
        "ds_write_b128 v2, v[44:47] offset:29696\n"
        "ds_write_b128 v2, v[48:51] offset:30720\n"
        "ds_write_b128 v2, v[52:55] offset:31744\n"
        "s_add_u32 m0, s82, 8192\n"
        "s_mov_b32 s65, 1048576\n"
        "buffer_load_dwordx4 v6, s[44:47], s65 offen offset:0 lds\n"
        "s_mov_b32 s65, 1080320\n"
        "buffer_load_dwordx4 v7, s[44:47], s65 offen offset:1024 lds\n"
        "s_mov_b32 s65, 1112064\n"
        "buffer_load_dwordx4 v6, s[44:47], s65 offen offset:2048 lds\n"
        "s_mov_b32 s65, 1143808\n"
        "buffer_load_dwordx4 v7, s[44:47], s65 offen offset:3072 lds\n"
        "ds_read_b64_tr_b16 v[24:25], v14 offset:0\n"
        "ds_read_b64_tr_b16 v[26:27], v14 offset:512\n"
        "ds_read_b64_tr_b16 v[28:29], v15 offset:0\n"
        "ds_read_b64_tr_b16 v[30:31], v15 offset:512\n"
        "ds_read_b64_tr_b16 v[32:33], v16 offset:0\n"
        "ds_read_b64_tr_b16 v[34:35], v16 offset:512\n"
        "ds_read_b64_tr_b16 v[36:37], v17 offset:0\n"
        "ds_read_b64_tr_b16 v[38:39], v17 offset:512\n"
        "s_waitcnt vmcnt(4)\n"
        "s_waitcnt lgkmcnt(0)\n"
        "s_barrier\n"
        "ds_write_b128 v2, v[24:27] offset:0\n"
        "ds_write_b128 v2, v[28:31] offset:1024\n"
        "ds_write_b128 v2, v[32:35] offset:2048\n"
        "ds_write_b128 v2, v[36:39] offset:3072\n"
        "v_lshlrev_b32 v60, 3, v3\n"
        "s_mov_b32 s65, 32768\n"
        "buffer_load_dword v61, v60, s[40:43], s65 offen sc1\n"
        "s_mov_b32 s65, 40960\n"
        "buffer_load_dword v62, v60, s[40:43], s65 offen sc1\n"
        "ds_read_b64_tr_b16 v[40:41], v14 offset:4096\n"
        "ds_read_b64_tr_b16 v[42:43], v14 offset:4608\n"
        "ds_read_b64_tr_b16 v[44:45], v15 offset:4096\n"
        "ds_read_b64_tr_b16 v[46:47], v15 offset:4608\n"
        "ds_read_b64_tr_b16 v[48:49], v16 offset:4096\n"
        "ds_read_b64_tr_b16 v[50:51], v16 offset:4608\n"
        "ds_read_b64_tr_b16 v[52:53], v17 offset:4096\n"
        "ds_read_b64_tr_b16 v[54:55], v17 offset:4608\n"
        "s_waitcnt vmcnt(2)\n"
        "s_waitcnt lgkmcnt(0)\n"
        "s_barrier\n"
        "ds_write_b128 v2, v[40:43] offset:4096\n"
        "ds_write_b128 v2, v[44:47] offset:5120\n"
        "ds_write_b128 v2, v[48:51] offset:6144\n"
        "ds_write_b128 v2, v[52:55] offset:7168\n"
        "ds_read_b64_tr_b16 v[24:25], v14 offset:8192\n"
        "ds_read_b64_tr_b16 v[26:27], v14 offset:8704\n"
        "ds_read_b64_tr_b16 v[28:29], v15 offset:8192\n"
        "ds_read_b64_tr_b16 v[30:31], v15 offset:8704\n"
        "ds_read_b64_tr_b16 v[32:33], v16 offset:8192\n"
        "ds_read_b64_tr_b16 v[34:35], v16 offset:8704\n"
        "ds_read_b64_tr_b16 v[36:37], v17 offset:8192\n"
        "ds_read_b64_tr_b16 v[38:39], v17 offset:8704\n"
        "s_waitcnt lgkmcnt(0)\n"
        "s_barrier\n"
        "ds_write_b128 v2, v[24:27] offset:8192\n"
        "ds_write_b128 v2, v[28:31] offset:9216\n"
        "ds_write_b128 v2, v[32:35] offset:10240\n"
        "ds_write_b128 v2, v[36:39] offset:11264\n"
        "s_waitcnt vmcnt(0) lgkmcnt(0)\n"
        "v_lshrrev_b32 v12, 4, v3\n"
        "s_mov_b32 s50, s56\n"
        "v_lshlrev_b32 v14, 7, v12\n"
        "s_sub_u32 s66, 0, s50\n"
        "s_mov_b32 s52, 32768\n"
        "v_cmp_gt_i32 vcc, 0, v61\n"
        "s_cbranch_vccz .Lch_pg0_ok_%=\n"
        "s_mov_b32 s64, 0\n"
        ".Lch_pg0_top_%=:\n"
        "buffer_load_dword v61, v14, s[40:43], s52 offen sc1\n"
        "s_waitcnt vmcnt(0)\n"
        "v_cmp_gt_i32 vcc, 0, v61\n"
        "s_cbranch_vccz .Lch_pg0_ok_%=\n"
        "s_add_u32 s64, s64, 1\n"
        "s_and_b32 s65, s64, 255\n"
        "s_cbranch_scc1 .Lch_pg0_go_%=\n"
        "s_mov_b32 s65, 49152\n"
        "s_cmp_lt_u32 s64, 1048576\n"
        "s_cbranch_scc1 .Lch_pg0_chk_%=\n"
        "v_mov_b32 v61, 1\n"
        "buffer_store_dword v61, off, s[40:43], s65 sc1\n"
        "s_waitcnt vmcnt(0)\n"
        ".Lch_pg0_chk_%=:\n"
        "buffer_load_dword v61, off, s[40:43], s65 sc1\n"
        "s_waitcnt vmcnt(0)\n"
        "v_readfirstlane_b32 s65, v61\n"
        "s_nop 0\n"
        "s_cmp_lg_u32 s65, 1\n"
        "s_cbranch_scc1 .Lch_pg0_go_%=\n"
        "s_endpgm\n"
        ".Lch_pg0_go_%=:\n"
        "s_sleep 2\n"
        "s_branch .Lch_pg0_top_%=\n"
        ".Lch_pg0_ok_%=:\n"
        "v_cmp_eq_u32 s[52:53], 1, v61\n"
        "s_nop 1\n"
        "s_mov_b32 s54, 40960\n"
        "v_cmp_gt_i32 vcc, 0, v62\n"
        "s_cbranch_vccz .Lch_pg1_ok_%=\n"
        "s_mov_b32 s64, 0\n"
        ".Lch_pg1_top_%=:\n"
        "buffer_load_dword v62, v14, s[40:43], s54 offen sc1\n"
        "s_waitcnt vmcnt(0)\n"
        "v_cmp_gt_i32 vcc, 0, v62\n"
        "s_cbranch_vccz .Lch_pg1_ok_%=\n"
        "s_add_u32 s64, s64, 1\n"
        "s_and_b32 s65, s64, 255\n"
        "s_cbranch_scc1 .Lch_pg1_go_%=\n"
        "s_mov_b32 s65, 49152\n"
        "s_cmp_lt_u32 s64, 1048576\n"
        "s_cbranch_scc1 .Lch_pg1_chk_%=\n"
        "v_mov_b32 v62, 1\n"
        "buffer_store_dword v62, off, s[40:43], s65 sc1\n"
        "s_waitcnt vmcnt(0)\n"
        ".Lch_pg1_chk_%=:\n"
        "buffer_load_dword v62, off, s[40:43], s65 sc1\n"
        "s_waitcnt vmcnt(0)\n"
        "v_readfirstlane_b32 s65, v62\n"
        "s_nop 0\n"
        "s_cmp_lg_u32 s65, 1\n"
        "s_cbranch_scc1 .Lch_pg1_go_%=\n"
        "s_endpgm\n"
        ".Lch_pg1_go_%=:\n"
        "s_sleep 2\n"
        "s_branch .Lch_pg1_top_%=\n"
        ".Lch_pg1_ok_%=:\n"
        "v_cmp_eq_u32 s[54:55], 1, v62\n"
        "s_nop 1\n"
        "s_mov_b32 vcc_lo, 0x55555555\n"
        "s_mov_b32 vcc_hi, 0x55555555\n"
        "s_xor_b64 vcc, vcc, s[52:53]\n"
        "s_bcnt1_i32_b64 s65, vcc\n"
        "s_sub_u32 s64, s65, 23\n"
        "s_cmp_lt_u32 s64, 19\n"
        "s_cbranch_scc1 .Lch_rawmap_%=\n"
        "s_cmp_lt_u32 s65, 32\n"
        "s_cselect_b32 s65, 0, -1\n"
        "s_xor_b32 s52, s65, 0x55555555\n"
        "s_xor_b32 s53, s65, 0x55555555\n"
        "s_xor_b32 s54, s65, 0x55555555\n"
        "s_xor_b32 s55, s65, 0xaaaaaaaa\n"
        ".Lch_rawmap_%=:\n"
        "s_xor_b32 s52, s52, s66\n"
        "s_xor_b32 s53, s53, s66\n"
        "s_xor_b32 s54, s54, s66\n"
        "s_xor_b32 s55, s55, s66\n"
        "s_mov_b32 s58, 0xff00ff00\n"
        "s_mov_b32 s59, 0xff00ff00\n"
        "s_and_b32 s67, s55, 1\n"
        "s_lshl_b32 s67, s67, 13\n"
        "s_lshl_b32 s65, s49, 2\n"
        "s_add_u32 s65, s65, s48\n"
        "s_lshl_b32 s65, s65, 7\n"
        "s_lshr_b32 s66, s65, 13\n"
        "s_lshl_b32 s66, s66, 13\n"
        "s_add_u32 s66, s66, s65\n"
        "s_add_u32 s66, s66, s67\n"
        "s_add_u32 s78, s66, 180224\n"
        "s_add_u32 s79, s66, 245760\n"
        "s_lshl_b32 s83, s49, 14\n"
        "s_lshl_b32 s65, s48, 11\n"
        "s_add_u32 s83, s83, s65\n"
        "s_add_u32 s83, s83, 524288\n"
        "s_lshl_b32 s65, s49, 12\n"
        "s_lshr_b32 s84, s65, 13\n"
        "s_lshl_b32 s84, s84, 13\n"
        "s_add_u32 s84, s84, s65\n"
        "s_lshl_b32 s65, s48, 9\n"
        "s_add_u32 s84, s84, s65\n"
        "s_add_u32 s84, s84, 1572864\n"
        "s_lshr_b32 s65, s49, 2\n"
        "s_add_u32 s65, s65, 64\n"
        "s_cmp_eq_u32 s48, 2\n"
        "s_cselect_b32 s65, s49, s65\n"
        "s_add_u32 s66, s49, 32\n"
        "s_cmp_eq_u32 s48, 3\n"
        "s_cselect_b32 s65, s66, s65\n"
        "s_lshl_b32 s85, s65, 7\n"
        "s_cmp_eq_u32 s48, 0\n"
        "s_cselect_b32 s66, 57344, 0\n"
        "s_add_u32 s85, s85, s66\n"
        "s_add_u32 s85, s85, s67\n"
        "s_add_u32 s85, s85, 1130496\n"
        "s_lshr_b32 s66, s49, 3\n"
        "s_lshl_b32 s54, s66, 14\n"
        "s_lshl_b32 s55, s66, 10\n"
        "s_and_b32 s66, s48, 1\n"
        "s_lshl_b32 s66, s66, 12\n"
        "s_add_u32 s54, s54, s66\n"
        "s_add_u32 s66, s67, 1130496\n"
        "s_add_u32 s54, s54, s66\n"
        "s_add_u32 s55, s55, s66\n"
        "s_add_u32 s55, s55, 65536\n"
        "s_add_u32 s72, s85, 245760\n"
        "s_lshl_b32 s66, s65, 12\n"
        "s_lshr_b32 s87, s66, 13\n"
        "s_lshl_b32 s87, s87, 13\n"
        "s_add_u32 s87, s87, s66\n"
        "s_and_b32 s65, s49, 3\n"
        "s_or_b32 s88, s65, s48\n"
        "s_lshl_b32 s86, s48, 16\n"
        "s_lshl_b32 s65, s49, 7\n"
        "s_add_u32 s86, s86, s65\n"
        "s_add_u32 s67, s67, 540672\n"
        "s_add_u32 s86, s86, s67\n"
        "s_add_u32 s87, s87, s67\n"
        "s_mov_b32 s73, s86\n"
        "s_add_u32 s74, s73, 262144\n"
        "s_mov_b32 s76, s87\n"
        "s_lshl_b32 s65, s48, 15\n"
        "s_sub_u32 s75, s73, s65\n"
        "s_add_u32 s75, s75, 524288\n"
        "s_cmp_lt_u32 s48, 2\n"
        "s_cselect_b32 s65, s88, 0\n"
        "s_cmp_eq_u32 s65, 0\n"
        "s_cselect_b32 s86, 0, 2147483648\n"
        "s_lshl_b32 s65, s48, 3\n"
        "s_lshr_b32 s60, s60, s65\n"
        "s_and_b32 s60, s60, 255\n"
        "s_lshl_b32 s60, s60, 3\n"
        "s_add_u32 s60, s60, 1280\n"
        "s_add_u32 s60, s60, s38\n"
        "s_lshr_b32 s34, s61, s65\n"
        "s_and_b32 s34, s34, 255\n"
        "s_lshl_b32 s34, s34, 3\n"
        "s_add_u32 s34, s34, 2048\n"
        "s_add_u32 s34, s34, s38\n"
        "s_mov_b32 s87, s60\n"
        "s_bfe_u32 s35, s62, 0x80010\n"
        "s_lshr_b32 s61, s62, 24\n"
        "s_memrealtime s[66:67]\n"
        "s_memtime s[90:91]\n"
        "s_waitcnt lgkmcnt(0)\n"
        "s_mov_b32 s29, s90\n"
        "s_mov_b32 s28, s66\n"
        "s_mov_b32 s27, 0\n"
        "s_mov_b32 s26, 0\n"
        "s_lshl_b32 s33, s48, 10\n"
        "s_lshl_b32 s32, s48, 8\n"
        "s_add_u32 s32, s32, 155648\n"
        "s_lshr_b32 s65, s48, 1\n"
        "s_lshl_b32 s31, s65, 10\n"
        "s_and_b32 s65, s48, 1\n"
        "s_lshl_b32 s65, s65, 6\n"
        "s_add_u32 s31, s31, s65\n"
        "s_mov_b32 s51, -1\n"
        "s_mov_b32 s80, -1\n"
        "s_lshl_b32 s62, s49, 7\n"
        "s_cmp_eq_u32 s48, 1\n"
        "s_cselect_b32 s62, s62, 2147483648\n"
        "s_and_b32 s47, s79, 8192\n"
        "s_add_u32 s47, s47, 299008\n"
        "s_add_u32 s46, s47, s62\n"
        "s_or_b32 s47, s47, s86\n"
        "s_bfe_u32 s56, s63, 0x80008\n"
        "s_lshl_b32 s56, s56, 4\n"
        "s_lshr_b32 s57, s63, 16\n"
        "s_lshl_b32 s57, s57, 4\n"
        "s_and_b32 s63, s63, 255\n"
        "s_mov_b32 s44, 0x3f803f80\n"
        "s_mov_b32 s45, 0x3f803f80\n"
        "s_branch .Lch_idle2_%=\n"
        ".p2align 6\n"
        ".p2align 5\n"
        ".Lch_code_%=:\n"
        ".Lch_A2_%=:\n"
        "s_waitcnt vmcnt(0)\n"
        ".Lch_A_%=:\n"
        "s_waitcnt lgkmcnt(0)\n"
        "s_barrier\n"
        "ds_read_b128 v[32:35], v1 offset:0\n"
        "ds_read_b32 v7, v5 offset:0\n"
        "ds_read_b128 v[36:39], v1 offset:1024\n"
        "s_waitcnt lgkmcnt(2)\n"
        "v_smfmac_f32_16x16x64_bf16 v[8:11], v[32:35], a[0:7], v0\n"
        "v_max3_u32 v6, v6, v32, v33\n"
        "v_smfmac_f32_16x16x64_bf16 v[16:19], v[32:35], a[144:151], v0\n"
        "s_waitcnt lgkmcnt(1)\n"
        "v_cmp_eq_u32 vcc, -1, v7\n"
        "v_smfmac_f32_16x16x64_bf16 v[24:27], v[32:35], v[40:47], v0\n"
        "ds_read_b128 v[40:43], v2 offset:6144\n"
        "v_max3_u32 v6, v6, v34, v35\n"
        "v_smfmac_f32_16x16x64_bf16 v[12:15], v[32:35], a[8:15], v0\n"
        "s_cbranch_vccnz .Lch_rt0_%=\n"
        "v_smfmac_f32_16x16x64_bf16 v[20:23], v[32:35], a[152:159], v0\n"
        "ds_read_b128 v[72:75], v1 offset:2048\n"
        "ds_read_b128 v[44:47], v2 offset:7168\n"
        "v_smfmac_f32_16x16x64_bf16 v[28:31], v[32:35], v[48:55], v0\n"
        "s_waitcnt lgkmcnt(3)\n"
        "ds_read_b128 v[48:51], v2 offset:8192\n"
        "v_smfmac_f32_16x16x64_bf16 v[8:11], v[36:39], a[16:23], v0\n"
        "ds_read_b128 v[52:55], v2 offset:9216\n"
        "v_max3_u32 v6, v6, v36, v37\n"
        "v_smfmac_f32_16x16x64_bf16 v[16:19], v[36:39], a[160:167], v0\n"
        "buffer_load_dwordx4 v[64:67], v3, s[40:43], s30 offen offset:0 sc1\n"
        "v_smfmac_f32_16x16x64_bf16 v[24:27], v[36:39], v[56:63], v0\n"
        "ds_read_b128 v[56:59], v2 offset:10240\n"
        "v_max3_u32 v6, v6, v38, v39\n"
        "v_smfmac_f32_16x16x64_bf16 v[12:15], v[36:39], a[24:31], v0\n"
        "ds_read_b128 v[60:63], v2 offset:11264\n"
        "v_smfmac_f32_16x16x64_bf16 v[20:23], v[36:39], a[168:175], v0\n"
        "s_waitcnt lgkmcnt(4)\n"
        "ds_read_b128 v[32:35], v1 offset:3072\n"
        "v_smfmac_f32_16x16x64_bf16 v[28:31], v[36:39], v[40:47], v0\n"
        "ds_read_b128 v[40:43], v2 offset:12288\n"
        "v_smfmac_f32_16x16x64_bf16 v[8:11], v[72:75], a[32:39], v0\n"
        "ds_read_b128 v[44:47], v2 offset:13312\n"
        "v_max3_u32 v6, v6, v72, v73\n"
        "v_smfmac_f32_16x16x64_bf16 v[16:19], v[72:75], a[176:183], v0\n"
        "s_waitcnt lgkmcnt(5)\n"
        "buffer_load_dwordx4 v[68:71], v3, s[40:43], s30 offen offset:1024 sc1\n"
        "v_smfmac_f32_16x16x64_bf16 v[24:27], v[72:75], v[48:55], v0\n"
        "ds_read_b128 v[48:51], v2 offset:14336\n"
        "v_max3_u32 v6, v6, v74, v75\n"
        "v_smfmac_f32_16x16x64_bf16 v[12:15], v[72:75], a[40:47], v0\n"
        "ds_read_b128 v[52:55], v2 offset:15360\n"
        "v_smfmac_f32_16x16x64_bf16 v[20:23], v[72:75], a[184:191], v0\n"
        "s_waitcnt lgkmcnt(5)\n"
        "s_waitcnt vmcnt(3)\n"
        "s_barrier\n"
        "v_smfmac_f32_16x16x64_bf16 v[28:31], v[72:75], v[56:63], v0\n"
        "s_waitcnt lgkmcnt(4)\n"
        "ds_read_b32 v7, v5 offset:4096\n"
        "ds_read_b128 v[36:39], v1 offset:4096\n"
        "v_smfmac_f32_16x16x64_bf16 v[8:11], v[32:35], a[48:55], v0\n"
        "ds_read_b128 v[56:59], v2 offset:16384\n"
        "v_max3_u32 v6, v6, v32, v33\n"
        "v_smfmac_f32_16x16x64_bf16 v[16:19], v[32:35], a[192:199], v0\n"
        "s_waitcnt lgkmcnt(5)\n"
        "ds_read_b128 v[60:63], v2 offset:17408\n"
        "v_smfmac_f32_16x16x64_bf16 v[24:27], v[32:35], v[40:47], v0\n"
        "ds_read_b128 v[40:43], v2 offset:18432\n"
        "v_max3_u32 v6, v6, v34, v35\n"
        "v_smfmac_f32_16x16x64_bf16 v[12:15], v[32:35], a[56:63], v0\n"
        "ds_read_b128 v[44:47], v2 offset:19456\n"
        "v_smfmac_f32_16x16x64_bf16 v[20:23], v[32:35], a[200:207], v0\n"
        "s_waitcnt lgkmcnt(5)\n"
        "ds_read_b128 v[72:75], v1 offset:5120\n"
        "v_cmp_eq_u32 vcc, -1, v7\n"
        "v_smfmac_f32_16x16x64_bf16 v[28:31], v[32:35], v[48:55], v0\n"
        "s_cbranch_vccnz .Lch_rt1_%=\n"
        ".Lch_ok1_%=:\n"
        "s_waitcnt lgkmcnt(5)\n"
        "v_smfmac_f32_16x16x64_bf16 v[8:11], v[36:39], a[64:71], v0\n"
        "ds_read_b128 v[48:51], v2 offset:20480\n"
        "v_max3_u32 v6, v6, v36, v37\n"
        "v_smfmac_f32_16x16x64_bf16 v[16:19], v[36:39], a[208:215], v0\n"
        "s_waitcnt lgkmcnt(4)\n"
        "ds_read_b128 v[52:55], v2 offset:21504\n"
        "v_smfmac_f32_16x16x64_bf16 v[24:27], v[36:39], v[56:63], v0\n"
        "ds_read_b128 v[56:59], v2 offset:22528\n"
        "v_max3_u32 v6, v6, v38, v39\n"
        "v_smfmac_f32_16x16x64_bf16 v[12:15], v[36:39], a[72:79], v0\n"
        "ds_read_b128 v[60:63], v2 offset:23552\n"
        "v_smfmac_f32_16x16x64_bf16 v[20:23], v[36:39], a[216:223], v0\n"
        "s_waitcnt lgkmcnt(5)\n"
        "ds_read_b128 v[32:35], v1 offset:6144\n"
        "v_smfmac_f32_16x16x64_bf16 v[28:31], v[36:39], v[40:47], v0\n"
        "s_waitcnt lgkmcnt(5)\n"
        "ds_read_b128 v[40:43], v2 offset:24576\n"
        "v_smfmac_f32_16x16x64_bf16 v[8:11], v[72:75], a[80:87], v0\n"
        "ds_read_b128 v[44:47], v2 offset:25600\n"
        "v_max3_u32 v6, v6, v72, v73\n"
        "v_smfmac_f32_16x16x64_bf16 v[16:19], v[72:75], a[224:231], v0\n"
        "s_waitcnt lgkmcnt(5)\n"
        "v_smfmac_f32_16x16x64_bf16 v[24:27], v[72:75], v[48:55], v0\n"
        "ds_read_b128 v[48:51], v2 offset:26624\n"
        "v_max3_u32 v6, v6, v74, v75\n"
        "v_smfmac_f32_16x16x64_bf16 v[12:15], v[72:75], a[88:95], v0\n"
        "ds_read_b128 v[52:55], v2 offset:27648\n"
        "v_smfmac_f32_16x16x64_bf16 v[20:23], v[72:75], a[232:239], v0\n"
        "s_waitcnt lgkmcnt(5)\n"
        "ds_read_b128 v[36:39], v1 offset:7168\n"
        "v_smfmac_f32_16x16x64_bf16 v[28:31], v[72:75], v[56:63], v0\n"
        "s_waitcnt lgkmcnt(5)\n"
        "ds_read_b128 v[56:59], v2 offset:28672\n"
        "v_smfmac_f32_16x16x64_bf16 v[8:11], v[32:35], a[96:103], v0\n"
        "ds_read_b128 v[60:63], v2 offset:29696\n"
        "v_max3_u32 v6, v6, v32, v33\n"
        "v_smfmac_f32_16x16x64_bf16 v[16:19], v[32:35], a[240:247], v0\n"
        "s_waitcnt lgkmcnt(5)\n"
        "s_waitcnt vmcnt(2)\n"
        "s_barrier\n"
        "v_smfmac_f32_16x16x64_bf16 v[24:27], v[32:35], v[40:47], v0\n"
        "s_memtime s[36:37]\n"
        "ds_read_b32 v7, v5 offset:8192\n"
        "v_max3_u32 v6, v6, v34, v35\n"
        "v_smfmac_f32_16x16x64_bf16 v[12:15], v[32:35], a[104:111], v0\n"
        "ds_read_b128 v[72:75], v1 offset:8192\n"
        "v_smfmac_f32_16x16x64_bf16 v[20:23], v[32:35], a[248:255], v0\n"
        "s_waitcnt lgkmcnt(6)\n"
        "v_smfmac_f32_16x16x64_bf16 v[28:31], v[32:35], v[48:55], v0\n"
        "s_waitcnt lgkmcnt(5)\n"
        "buffer_load_dwordx4 v[48:51], v3, s[40:43], s89 offen offset:0 sc1\n"
        "v_smfmac_f32_16x16x64_bf16 v[8:11], v[36:39], a[112:119], v0\n"
        "buffer_load_dwordx4 v[52:55], v3, s[40:43], s77 offen offset:1024 sc1\n"
        "v_max3_u32 v6, v6, v36, v37\n"
        "v_smfmac_f32_16x16x64_bf16 v[12:15], v[36:39], a[120:127], v0\n"
        "s_waitcnt lgkmcnt(1)\n"
        "v_cmp_eq_u32 vcc, -1, v7\n"
        "v_smfmac_f32_16x16x64_bf16 v[16:19], v[36:39], v[80:87], v0\n"
        "s_waitcnt lgkmcnt(0)\n"
        "s_and_b64 vcc, vcc, 0xffff\n"
        "v_readlane_b32 s81, v7, 48\n"
        "v_smfmac_f32_16x16x64_bf16 v[20:23], v[36:39], v[88:95], v0\n"
        "s_cbranch_vccnz .Lch_rt2_%=\n"
        ".Lch_ok2_%=:\n"
        "v_smfmac_f32_16x16x64_bf16 v[8:11], v[72:75], a[128:135], v0\n"
        "s_sub_u32 s37, s36, s38\n"
        "ds_read_b128 v[40:43], v2 offset:30720\n"
        "v_mov_b32 v7, s51\n"
        "v_smfmac_f32_16x16x64_bf16 v[12:15], v[72:75], a[136:143], v0\n"
        "s_cmp_eq_u32 s81, s51\n"
        "s_cselect_b32 s65, 2147483648, s46\n"
        "v_lshl_or_b32 v7, s37, 6, v7\n"
        "v_smfmac_f32_16x16x64_bf16 v[16:19], v[72:75], v[96:103], v0\n"
        "s_mov_b64 exec, 1\n"
        "buffer_store_dword v7, off, s[40:43], s65 sc1\n"
        "s_mov_b64 exec, -1\n"
        "v_smfmac_f32_16x16x64_bf16 v[20:23], v[72:75], v[104:111], v0\n"
        "ds_read_b128 v[44:47], v2 offset:31744\n"
        "v_cvt_pk_bf16_f32 v8, v8, v9\n"
        "v_cvt_pk_bf16_f32 v9, v10, v11\n"
        "v_cvt_pk_bf16_f32 v12, v12, v13\n"
        "v_cvt_pk_bf16_f32 v13, v14, v15\n"
        "v_mfma_f32_16x16x16_bf16 v[32:35], v[8:9], v[76:77], 0\n"
        "v_cvt_pk_bf16_f32 v16, v16, v17\n"
        "v_cvt_pk_bf16_f32 v17, v18, v19\n"
        "v_mfma_f32_16x16x16_bf16 v[8:11], v[12:13], v[76:77], 0\n"
        "v_cvt_pk_bf16_f32 v20, v20, v21\n"
        "v_cvt_pk_bf16_f32 v21, v22, v23\n"
        "v_mfma_f32_16x16x16_bf16 v[12:15], v[16:17], v[78:79], 0\n"
        "v_cvt_pk_bf16_f32 v32, v32, v33\n"
        "v_cvt_pk_bf16_f32 v33, v34, v35\n"
        "v_mfma_f32_16x16x16_bf16 v[16:19], v[20:21], v[78:79], 0\n"
        "v_cvt_pk_bf16_f32 v34, v8, v9\n"
        "v_cvt_pk_bf16_f32 v35, v10, v11\n"
        "s_cbranch_scc1 .Lch_slow_%=\n"
        ".Lch_back_%=:\n"
        "s_waitcnt vmcnt(3)\n"
        "v_mfma_f32_16x16x32_bf16 v[8:11], v[64:67], v[32:35], 0\n"
        "v_cvt_pk_bf16_f32 v12, v12, v13\n"
        "v_cvt_pk_bf16_f32 v13, v14, v15\n"
        "v_mfma_f32_16x16x32_bf16 v[20:23], v[68:71], v[32:35], 0\n"
        "s_waitcnt lgkmcnt(0)\n"
        "v_cvt_pk_bf16_f32 v14, v16, v17\n"
        "v_cvt_pk_bf16_f32 v15, v18, v19\n"
        "v_smfmac_f32_16x16x64_bf16 v[24:27], v[36:39], v[56:63], v0\n"
        "ds_read_b128 v[68:71], v4 offset:10240\n"
        "v_add_u32 v76, s33, v4\n"
        "v_mfma_f32_16x16x32_bf16 v[16:19], v[12:15], v[32:35], 0\n"
        "v_mov_b32_dpp v8, v20 quad_perm:[0,1,2,3] row_mask:0xf bank_mask:0xc\n"
        "v_mov_b32_dpp v9, v21 quad_perm:[0,1,2,3] row_mask:0xf bank_mask:0xc\n"
        "v_mov_b32_dpp v10, v22 quad_perm:[0,1,2,3] row_mask:0xf bank_mask:0xc\n"
        "v_mov_b32_dpp v11, v23 quad_perm:[0,1,2,3] row_mask:0xf bank_mask:0xc\n"
        "ds_write_b128 v76, v[8:11]\n"
        "v_lshrrev_b32 v78, 2, v3\n"
        "v_add_u32 v77, s32, v78\n"
        "v_add_u32 v78, 155648, v78\n"
        "v_mov_b32_dpp v16, v18 quad_perm:[0,1,2,3] row_mask:0xf bank_mask:0xc\n"
        "ds_write_b32 v77, v16\n"
        "v_smfmac_f32_16x16x64_bf16 v[28:31], v[36:39], v[40:47], v0\n"
        "v_xor_b32 v64, 0x400, v76\n"
        "v_smfmac_f32_16x16x64_bf16 v[24:27], v[72:75], v[240:247], v0\n"
        "v_xor_b32 v65, 0x800, v76\n"
        "v_smfmac_f32_16x16x64_bf16 v[28:31], v[72:75], v[248:255], v0\n"
        "v_xor_b32 v66, 0xc00, v76\n"
        "s_waitcnt lgkmcnt(0)\n"
        "s_barrier\n"
        "ds_read_b128 v[32:35], v64\n"
        "ds_read_b128 v[20:23], v65\n"
        "ds_read_b128 v[40:43], v66\n"
        "ds_read2st64_b32 v[44:45], v78 offset1:1\n"
        "ds_read2st64_b32 v[46:47], v78 offset0:2 offset1:3\n"
        "buffer_store_dwordx4 v[12:15], v68, s[40:43], s70 offen sc1\n"
        "v_max3_u32 v6, v6, v38, v39\n"
        "v_max3_u32 v6, v6, v72, v73\n"
        "v_max3_u32 v6, v6, v74, v75\n"
        "v_add_f32 v24, v24, v27\n"
        "v_add_f32 v25, v25, v26\n"
        "v_add_f32 v28, v28, v31\n"
        "v_add_f32 v29, v29, v30\n"
        "s_waitcnt lgkmcnt(4)\n"
        "v_pk_add_f32 v[32:33], v[32:33], v[8:9]\n"
        "v_pk_add_f32 v[34:35], v[34:35], v[10:11]\n"
        "s_waitcnt lgkmcnt(2)\n"
        "v_pk_add_f32 v[36:37], v[20:21], v[40:41]\n"
        "v_pk_add_f32 v[38:39], v[22:23], v[42:43]\n"
        "v_pk_add_f32 v[32:33], v[32:33], v[36:37]\n"
        "v_pk_add_f32 v[34:35], v[34:35], v[38:39]\n"
        "s_waitcnt lgkmcnt(0)\n"
        "v_pk_add_f32 v[72:73], v[44:45], v[46:47]\n"
        "v_max3_f32 v36, v32, v33, v34\n"
        "v_add_f32 v72, v72, v73\n"
        "v_max3_f32 v36, v36, v35, v72\n"
        "v_cvt_pk_bf16_f32 v30, v24, v25\n"
        "v_cvt_pk_bf16_f32 v31, v29, v28\n"
        "v_max_f32_dpp v37, v36, v36 row_ror:8 row_mask:0xf bank_mask:0xf\n"
        "v_max_f32_dpp v38, v36, v36 row_ror:8 row_mask:0xf bank_mask:0xf\n"
        "v_mov_b64 v[76:77], s[44:45]\n"
        "v_mov_b64 v[78:79], s[44:45]\n"
        "v_permlane16_swap_b32 v37, v38\n"
        "v_max_f32 v36, v37, v38\n"
        "v_max_f32 v39, v37, v38\n"
        "v_cmp_eq_u32 vcc, -1, v6\n"
        "v_lshlrev_b32 v6, 3, v3\n"
        "v_permlane32_swap_b32 v36, v39\n"
        "v_max_f32 v36, v36, v39\n"
        "v_mul_f32 v36, 0xbe0293ee, v36\n"
        "v_fmamk_f32 v72, v72, 0x3e0293ee, v36\n"
        "v_exp_f32 v72, v72\n"
        "v_fmamk_f32 v32, v32, 0x3e0293ee, v36\n"
        "v_fmamk_f32 v33, v33, 0x3e0293ee, v36\n"
        "v_mov_b32_dpp v73, v72 row_ror:8 row_mask:0xf bank_mask:0xf\n"
        "v_exp_f32 v32, v32\n"
        "v_cvt_pk_bf16_f32 v73, v72, v73\n"
        "v_exp_f32 v33, v33\n"
        "v_cndmask_b32 v44, v73, 0, s[58:59]\n"
        "v_cndmask_b32 v45, 0, v73, s[58:59]\n"
        "v_fmamk_f32 v34, v34, 0x3e0293ee, v36\n"
        "v_fmamk_f32 v35, v35, 0x3e0293ee, v36\n"
        "v_mfma_f32_16x16x16_bf16 v[56:59], v[76:77], v[44:45], 0\n"
        "v_exp_f32 v34, v34\n"
        "v_exp_f32 v35, v35\n"
        "v_cvt_pk_bf16_f32 v40, v32, v33\n"
        "v_mfma_f32_16x16x16_bf16 v[60:63], v[30:31], v[44:45], 0\n"
        "v_cvt_pk_bf16_f32 v41, v34, v35\n"
        "v_mov_b32_dpp v42, v40 row_ror:8 row_mask:0xf bank_mask:0xf\n"
        "s_waitcnt vmcnt(2)\n"
        "v_mov_b32_dpp v43, v41 row_ror:8 row_mask:0xf bank_mask:0xf\n"
        "v_swap_b32 v52, v54\n"
        "v_swap_b32 v53, v55\n"
        "v_mfma_f32_16x16x32_bf16 v[56:59], v[76:79], v[40:43], v[56:59]\n"
        "v_mov_b64 v[10:11], 0\n"
        "v_mov_b64 v[12:13], 0\n"
        "v_mfma_f32_16x16x32_bf16 v[32:35], v[48:51], v[40:43], v[60:63]\n"
        "s_mov_b32 exec_hi, 0\n"
        "buffer_load_dword v6, v6, s[40:43], s47 offen sc1\n"
        "s_mov_b32 exec_hi, -1\n"
        "v_mfma_f32_16x16x32_bf16 v[72:75], v[52:55], v[40:43], v[60:63]\n"
        "v_rcp_f32 v36, v56\n"
        "v_add_u32 v71, s31, v71\n"
        "v_mov_b64 v[8:9], 0\n"
        "v_mov_b64 v[14:15], 0\n"
        "v_mul_f32 v32, v32, v36\n"
        "v_mul_f32 v33, v33, v36\n"
        "v_mul_f32_e64 v34, v34, v36\n"
        "v_mul_f32_e64 v35, v35, v36\n"
        "v_mul_f32_dpp v32, v72, v36 quad_perm:[0,1,2,3] row_mask:0xf bank_mask:0xc\n"
        "v_mul_f32_dpp v33, v73, v36 quad_perm:[0,1,2,3] row_mask:0xf bank_mask:0xc\n"
        "v_mul_f32_dpp v34, v74, v36 quad_perm:[0,1,2,3] row_mask:0xf bank_mask:0xc\n"
        "v_mul_f32_dpp v35, v75, v36 quad_perm:[0,1,2,3] row_mask:0xf bank_mask:0xc\n"
        "v_cvt_pk_bf16_f32 v44, v32, v33\n"
        "v_cvt_pk_bf16_f32 v45, v34, v35\n"
        "ds_write_b64 v71, v[44:45]\n"
        "v_and_b32 v7, 0xffffffbf, v1\n"
        "v_subrev_u32 v7, 2048, v7\n"
        "v_cvt_pk_bf16_f32 v24, v24, v25\n"
        "v_cvt_pk_bf16_f32 v26, v28, v29\n"
        "s_cbranch_vccnz .Lch_redo_%=\n"
        "v_permlane16_swap_b32 v24, v25\n"
        "s_waitcnt lgkmcnt(0)\n"
        "s_barrier\n"
        "ds_read_b128 v[28:31], v7 offset:0\n"
        "ds_read_b128 v[32:35], v7 offset:64\n"
        "ds_read_b128 v[36:39], v7 offset:1024\n"
        "ds_read_b128 v[40:43], v7 offset:1088\n"
        "v_permlane16_swap_b32 v26, v27\n"
        "buffer_store_dwordx4 v[24:27], v69, s[40:43], s70 offen offset:2048 sc1\n"
        "s_waitcnt lgkmcnt(3)\n"
        "v_smfmac_f32_16x16x64_bf16 v[8:11], v[28:31], v[112:119], v0\n"
        "s_waitcnt lgkmcnt(2)\n"
        "v_mov_b64 v[16:17], 0\n"
        "v_smfmac_f32_16x16x64_bf16 v[8:11], v[32:35], v[120:127], v0\n"
        "s_waitcnt lgkmcnt(1)\n"
        "v_mov_b64 v[18:19], 0\n"
        "v_smfmac_f32_16x16x64_bf16 v[8:11], v[36:39], v[128:135], v0\n"
        "s_waitcnt lgkmcnt(0)\n"
        "v_mov_b64 v[20:21], 0\n"
        "v_smfmac_f32_16x16x64_bf16 v[8:11], v[40:43], v[136:143], v0\n"
        "v_mov_b64 v[22:23], 0\n"
        "v_smfmac_f32_16x16x64_bf16 v[12:15], v[28:31], v[144:151], v0\n"
        "ds_read_b128 v[44:47], v2 offset:32768\n"
        "v_mov_b64 v[24:25], 0\n"
        "v_smfmac_f32_16x16x64_bf16 v[12:15], v[32:35], v[152:159], v0\n"
        "ds_read_b128 v[48:51], v2 offset:33792\n"
        "v_cvt_pk_bf16_f32 v8, v8, v9\n"
        "v_smfmac_f32_16x16x64_bf16 v[12:15], v[36:39], v[160:167], v0\n"
        "ds_read_b128 v[52:55], v2 offset:34816\n"
        "v_cvt_pk_bf16_f32 v9, v11, v10\n"
        "v_smfmac_f32_16x16x64_bf16 v[12:15], v[40:43], v[168:175], v0\n"
        "ds_read_b128 v[56:59], v2 offset:35840\n"
        "v_bfi_b32 v8, -2, v8, s51\n"
        "v_smfmac_f32_16x16x64_bf16 v[16:19], v[28:31], v[176:183], v0\n"
        "ds_read_b128 v[60:63], v2 offset:36864\n"
        "v_mov_b64 v[26:27], 0\n"
        "v_smfmac_f32_16x16x64_bf16 v[16:19], v[32:35], v[184:191], v0\n"
        "v_cvt_pk_bf16_f32 v10, v12, v13\n"
        "v_cvt_pk_bf16_f32 v11, v15, v14\n"
        "v_bfi_b32 v10, -2, v10, s51\n"
        "buffer_store_dwordx4 v[8:11], v70, s[40:43], s73 offen sc1\n"
        "v_smfmac_f32_16x16x64_bf16 v[16:19], v[36:39], v[192:199], v0\n"
        "ds_read_b128 v[64:67], v2 offset:37888\n"
        "v_smfmac_f32_16x16x64_bf16 v[16:19], v[40:43], v[200:207], v0\n"
        "ds_read_b128 v[72:75], v2 offset:38912\n"
        "v_smfmac_f32_16x16x64_bf16 v[20:23], v[28:31], v[208:215], v0\n"
        "ds_read_b128 v[76:79], v2 offset:39936\n"
        "v_smfmac_f32_16x16x64_bf16 v[20:23], v[32:35], v[216:223], v0\n"
        "s_nop 0\n"
        "v_cvt_pk_bf16_f32 v16, v16, v17\n"
        "v_smfmac_f32_16x16x64_bf16 v[20:23], v[36:39], v[224:231], v0\n"
        "s_cmp_lt_u32 s48, 2\n"
        "v_cvt_pk_bf16_f32 v17, v19, v18\n"
        "v_smfmac_f32_16x16x64_bf16 v[20:23], v[40:43], v[232:239], v0\n"
        "s_nop 0\n"
        "v_bfi_b32 v16, -2, v16, s51\n"
        "s_cbranch_scc0 .Lch_R23_%=\n"
        "s_waitcnt lgkmcnt(0)\n"
        "v_smfmac_f32_16x16x64_bf16 v[24:27], v[28:31], v[44:51], v0\n"
        "v_smfmac_f32_16x16x64_bf16 v[24:27], v[32:35], v[52:59], v0\n"
        "v_cvt_pk_bf16_f32 v18, v20, v21\n"
        "v_cvt_pk_bf16_f32 v19, v23, v22\n"
        "v_bfi_b32 v18, -2, v18, s51\n"
        "buffer_store_dwordx4 v[16:19], v70, s[40:43], s74 offen sc1\n"
        "v_smfmac_f32_16x16x64_bf16 v[24:27], v[36:39], v[60:67], v0\n"
        "v_smfmac_f32_16x16x64_bf16 v[24:27], v[40:43], v[72:79], v0\n"
        "v_add_u32 v12, 114688, v3\n"
        "ds_read_b128 v[44:47], v12\n"
        "ds_read_b32 v50, v12 offset:1024\n"
        "s_nop 4\n"
        "v_cvt_pk_bf16_f32 v24, v24, v25\n"
        "v_bfi_b32 v24, -2, v24, s51\n"
        "v_cvt_pk_bf16_f32 v25, v27, v26\n"
        "s_nop 0\n"
        "v_permlane32_swap_b32 v24, v26\n"
        "v_permlane32_swap_b32 v25, v27\n"
        "s_mov_b32 exec_hi, 0\n"
        "buffer_store_dwordx4 v[24:27], v70, s[40:43], s75 offen sc1\n"
        "s_memtime s[90:91]\n"
        "s_waitcnt vmcnt(4)\n"
        "s_cmp_eq_u32 s88, 0\n"
        "s_cbranch_scc0 .Lch_idle_%=\n"
        "s_mov_b64 exec, 1\n"
        "buffer_load_dword v7, off, s[40:43], s78 sc1\n"
        "s_mov_b32 exec_lo, -1\n"
        "s_waitcnt vmcnt(4)\n"
        "s_branch .Lch_R0_%=\n"
        ".p2align 5\n"
        ".Lch_idle_%=:\n"
        "s_mov_b32 exec_hi, -1\n"
        "s_waitcnt lgkmcnt(0)\n"
        "s_waitcnt vmcnt(3)\n"
        "v_mov_b32 v12, s51\n"
        "s_mov_b64 exec, 1\n"
        "buffer_store_dword v12, off, s[40:43], s79 sc1\n"
        "s_mov_b64 exec, -1\n"
        "s_mov_b32 s65, s63\n"
        ".Lch_d4_%=:\n"
        "s_sub_u32 s65, s65, 1\n"
        "s_cbranch_scc0 .Lch_d4_%=\n"
        ".Lch_idle2_%=:\n"
        "s_mov_b64 exec, 1\n"
        "buffer_load_dword v7, off, s[40:43], s78 sc1\n"
        "s_mov_b64 exec, -1\n"
        "s_branch .Lch_nog_%=\n"
        ".p2align 5\n"
        ".Lch_R23_%=:\n"
        "v_add_u32 v12, 114688, v3\n"
        "s_waitcnt lgkmcnt(0)\n"
        "ds_read_b128 v[44:47], v12\n"
        "ds_read_b128 v[48:51], v12 offset:1024\n"
        "s_nop 0\n"
        "s_nop 0\n"
        "v_cvt_pk_bf16_f32 v18, v20, v21\n"
        "v_cvt_pk_bf16_f32 v19, v23, v22\n"
        "v_bfi_b32 v18, -2, v18, s51\n"
        "buffer_store_dwordx4 v[16:19], v70, s[40:43], s74 offen sc1\n"
        "s_memtime s[90:91]\n"
        "s_mov_b32 exec_hi, 0\n"
        "s_waitcnt vmcnt(3)\n"
        "s_cmp_eq_u32 s48, 2\n"
        "s_nop 0\n"
        "s_nop 0\n"
        "s_cbranch_scc1 .Lch_R_%=\n"
        "s_waitcnt vmcnt(2)\n"
        ".Lch_R0_%=:\n"
        "v_mov_b32_e64 v10, s51\n"
        "s_mov_b32 exec_lo, 1\n"
        "buffer_store_dword v10, off, s[40:43], s79 sc1\n"
        "s_mov_b32 exec_lo, -1\n"
        ".Lch_R_%=:\n"
        ".Lch_st1_%=:\n"
        "v_and_b32 v12, 63, v6\n"
        "v_cmp_ne_u32 vcc, s51, v12\n"
        "s_lshl_b32 s65, s37, 6\n"
        "v_subrev_u32_e64 v6, s65, v6\n"
        "v_ashrrev_i32_e64 v6, 6, v6\n"
        "s_add_u32 s66, s37, s60\n"
        "s_cbranch_vccnz .Lch_stwait_%=\n"
        ".Lch_st2_%=:\n"
        "s_waitcnt lgkmcnt(0)\n"
        "s_sub_u32 s65, s90, s66\n"
        "s_memtime s[90:91]\n"
        "v_sub_u32 v12, s65, v6\n"
        "v_cmp_lt_u32 vcc, 0xffffb1e0, v12\n"
        "s_cbranch_vccnz .Lch_st2_%=\n"
        "s_mov_b32 exec_hi, -1\n"
        ".Lch_rl_%=:\n"
        "buffer_load_dwordx4 v[28:31], v3, s[40:43], s76 offen offset:0 sc1\n"
        "buffer_load_dwordx4 v[32:35], v3, s[40:43], s76 offen offset:1024 sc1\n"
        "buffer_load_dwordx4 v[36:39], v3, s[40:43], s76 offen offset:2048 sc1\n"
        "buffer_load_dwordx4 v[40:43], v3, s[40:43], s76 offen offset:3072 sc1\n"
        "v_mov_b32 v10, s51\n"
        "s_waitcnt vmcnt(6)\n"
        "s_cmp_eq_u32 s48, 2\n"
        "s_cselect_b32 s65, s79, 2147483648\n"
        "s_mov_b64 exec, 1\n"
        "buffer_store_dword v10, off, s[40:43], s65 sc1\n"
        "s_mov_b64 exec, -1\n"
        "v_mov_b32 v8, v6\n"
        "v_mov_b32 v9, v6\n"
        "s_nop 0\n"
        "v_max_i32_dpp v8, v8, v8 row_ror:8 row_mask:0xf bank_mask:0xf\n"
        "v_min_i32_dpp v9, v9, v9 row_ror:8 row_mask:0xf bank_mask:0xf\n"
        "s_nop 0\n"
        "v_max_i32_dpp v8, v8, v8 row_ror:4 row_mask:0xf bank_mask:0xf\n"
        "v_min_i32_dpp v9, v9, v9 row_ror:4 row_mask:0xf bank_mask:0xf\n"
        "s_nop 0\n"
        "v_max_i32_dpp v8, v8, v8 row_ror:2 row_mask:0xf bank_mask:0xf\n"
        "v_min_i32_dpp v9, v9, v9 row_ror:2 row_mask:0xf bank_mask:0xf\n"
        "s_nop 0\n"
        "v_max_i32_dpp v8, v8, v8 row_ror:1 row_mask:0xf bank_mask:0xf\n"
        "v_min_i32_dpp v9, v9, v9 row_ror:1 row_mask:0xf bank_mask:0xf\n"
        "s_waitcnt lgkmcnt(0)\n"
        "s_memrealtime s[66:67]\n"
        "s_memtime s[90:91]\n"
        "s_waitcnt lgkmcnt(0)\n"
        "s_sub_u32 s91, s90, s29\n"
        "s_mov_b32 s29, s90\n"
        "s_sub_u32 s67, s66, s28\n"
        "s_mov_b32 s28, s66\n"
        "v_cvt_f32_u32 v10, s91\n"
        "v_cvt_f32_u32 v11, s67\n"
        "v_fma_f32 v10, s27, 0.5, v10\n"
        "v_fma_f32 v11, s26, 0.5, v11\n"
        "s_nop 0\n"
        "v_readfirstlane_b32 s27, v10\n"
        "v_readfirstlane_b32 s26, v11\n"
        "v_rcp_f32 v11, v11\n"
        "s_nop 0\n"
        "v_mul_f32 v10, v10, v11\n"
        "v_mul_f32 v10, 0x41800000, v10\n"
        "v_cvt_u32_f32 v10, v10\n"
        "s_nop 0\n"
        "v_readfirstlane_b32 s66, v10\n"
        "s_nop 0\n"
        "s_min_u32 s66, s66, 400\n"
        "s_max_u32 s66, s66, 256\n"
        "s_sub_u32 s66, s66, 336\n"
        "s_mul_i32 s67, s66, s35\n"
        "s_ashr_i32 s67, s67, 4\n"
        "s_add_u32 s60, s87, s67\n"
        "s_mul_i32 s67, s66, s61\n"
        "s_ashr_i32 s67, s67, 4\n"
        "s_add_u32 s67, s67, s34\n"
        "v_readlane_b32 s65, v8, 0\n"
        "v_readlane_b32 s66, v8, 16\n"
        "v_readlane_b32 s90, v9, 0\n"
        "v_readlane_b32 s91, v9, 16\n"
        "s_max_i32 s65, s65, s66\n"
        "s_min_i32 s90, s90, s91\n"
        "s_sub_u32 s90, s65, s90\n"
        "s_add_u32 s65, s65, s37\n"
        "s_add_u32 s65, s65, s67\n"
        "s_cmp_gt_u32 s90, s56\n"
        "s_cselect_b32 s90, s57, 0\n"
        "s_add_u32 s82, s65, s90\n"
        "s_and_b32 s71, s51, 3\n"
        "s_mul_i32 s71, s71, 81920\n"
        "s_add_u32 s39, s71, s85\n"
        "v_mov_b64 v[64:65], -1\n"
        "v_mov_b64 v[66:67], -1\n"
        "s_waitcnt vmcnt(4)\n"
        "v_mfma_f32_16x16x32_bf16 v[12:15], v[28:31], v[44:47], 0\n"
        "v_bitop3_b32 v8, v28, v30, s51 bitop3:0x7e\n"
        "s_waitcnt vmcnt(3)\n"
        "v_mfma_f32_16x16x32_bf16 v[12:15], v[32:35], v[44:47], v[12:15]\n"
        "v_bitop3_b32 v10, v32, v34, s51 bitop3:0x7e\n"
        "v_or_b32 v8, v8, v10\n"
        "s_waitcnt vmcnt(2)\n"
        "v_mfma_f32_16x16x32_bf16 v[12:15], v[36:39], v[44:47], v[12:15]\n"
        "v_bitop3_b32 v10, v36, v38, s51 bitop3:0x7e\n"
        "v_or_b32 v8, v8, v10\n"
        "s_waitcnt vmcnt(1)\n"
        "v_mfma_f32_16x16x32_bf16 v[12:15], v[40:43], v[44:47], v[12:15]\n"
        "v_bitop3_b32 v10, v40, v42, s51 bitop3:0x7e\n"
        "v_or_b32 v8, v8, v10\n"
        "v_and_b32 v8, 1, v8\n"
        "v_cmp_ne_u32 vcc, 0, v8\n"
        "s_nop 2\n"
        "s_cbranch_vccnz .Lch_rretry_%=\n"
        "v_permlane32_swap_b32 v12, v14\n"
        "v_permlane32_swap_b32 v13, v15\n"
        "v_add_f32 v12, v12, v14\n"
        "v_add_f32_e64 v13, v13, v15\n"
        "v_cvt_pk_bf16_f32 v16, v12, v13\n"
        "v_cvt_pk_bf16_f32 v18, v12, v13\n"
        "s_nop 0\n"
        "s_nop 0\n"
        "v_permlane16_swap_b32 v16, v18\n"
        "s_nop 0\n"
        "s_nop 0\n"
        "v_permlane32_swap_b32 v16, v17\n"
        "v_permlane32_swap_b32 v18, v19\n"
        "buffer_store_dwordx4 v[16:19], v50, s[40:43], s39 offen sc1\n"
        "buffer_store_dwordx4 v[64:67], v50, s[40:43], s72 offen sc1\n"
        "s_mov_b32 s72, s39\n"
        "s_cmp_eq_u32 s48, 0\n"
        "s_cbranch_scc0 .Lch_w23_%=\n"
        "s_add_u32 s51, s51, 1\n"
        "s_add_u32 s80, s80, 2\n"
        "s_bitcmp1_b64 s[52:53], s51\n"
        "s_cselect_b32 s65, 8192, 0\n"
        "s_lshl_b32 s68, s51, 21\n"
        "s_add_u32 s68, s68, s65\n"
        "s_add_u32 s68, s68, 2097152\n"
        "s_add_u32 s70, s68, s84\n"
        "s_add_u32 s69, s68, s83\n"
        "ds_read_b128 v[40:43], v2 offset:0\n"
        "ds_read_b128 v[44:47], v2 offset:1024\n"
        "ds_read_b128 v[48:51], v2 offset:2048\n"
        "ds_read_b128 v[52:55], v2 offset:3072\n"
        "ds_read_b128 v[56:59], v2 offset:4096\n"
        "ds_read_b128 v[60:63], v2 offset:5120\n"
        "ds_read_b128 v[76:79], v4 offset:9216\n"
        "v_mov_b64 v[8:9], 0\n"
        "v_mov_b64 v[10:11], 0\n"
        "v_mov_b64 v[12:13], 0\n"
        "v_mov_b64 v[14:15], 0\n"
        "v_mov_b64 v[16:17], 0\n"
        "v_mov_b64 v[18:19], 0\n"
        "v_mov_b64 v[20:21], 0\n"
        "v_mov_b64 v[22:23], 0\n"
        "v_mov_b64 v[24:25], 0\n"
        "v_mov_b64 v[26:27], 0\n"
        "v_mov_b64 v[28:29], 0\n"
        "v_mov_b64 v[30:31], 0\n"
        "v_mov_b32 v6, 0\n"
        "s_add_u32 s89, s69, 524288\n"
        "s_mov_b32 s30, s69\n"
        "s_mov_b32 s77, s89\n"
        "s_cmp_eq_u32 s51, 63\n"
        "s_cselect_b32 s67, 2147483647, s80\n"
        "v_readfirstlane_b32 s65, v7\n"
        "s_nop 0\n"
        "s_cmp_ge_i32 s65, s67\n"
        "s_cbranch_scc1 .Lch_A_%=\n"
        "s_mov_b32 s30, 2147483648\n"
        "s_mov_b32 s89, 2147483648\n"
        "s_sub_u32 s77, s78, 1024\n"
        "v_mov_b32 v7, s51\n"
        "ds_write_b32 v4, v7 offset:11264\n"
        "s_branch .Lch_A_%=\n"
        ".p2align 5\n"
        ".Lch_w23_%=:\n"
        "s_mov_b64 exec, 1\n"
        "buffer_load_dword v7, off, s[40:43], s78 sc1\n"
        "s_mov_b64 exec, -1\n"
        "v_mov_b32_e64 v32, v49\n"
        "s_cmp_eq_u32 s48, 2\n"
        "s_nop 0\n"
        "s_cbranch_scc0 .Lch_nog_%=\n"
        "s_and_b32 s66, s33, 1024\n"
        "s_lshl_b32 s66, s66, 2\n"
        "s_add_u32 m0, s66, 151552\n"
        "s_add_u32 s66, s54, s71\n"
        "s_memtime s[90:91]\n"
        "s_waitcnt lgkmcnt(0)\n"
        "s_sub_u32 s65, s82, s90\n"
        "s_cmp_lt_u32 s65, 20000\n"
        "s_cselect_b32 s65, s65, 0\n"
        "s_nop 0\n"
        "s_nop 0\n"
        ".Lch_gwa_%=:\n"
        "s_sub_u32 s65, s65, 28\n"
        "s_cbranch_scc0 .Lch_gwa_%=\n"
        "buffer_load_dwordx4 v3, s[40:43], s66 offen offset:0 sc1 lds\n"
        "buffer_load_dwordx4 v3, s[40:43], s66 offen offset:1024 sc1 lds\n"
        "buffer_load_dwordx4 v3, s[40:43], s66 offen offset:2048 sc1 lds\n"
        "buffer_load_dwordx4 v3, s[40:43], s66 offen offset:3072 sc1 lds\n"
        "s_add_u32 s51, s51, 1\n"
        "s_add_u32 s80, s80, 2\n"
        "s_bitcmp1_b64 s[52:53], s51\n"
        "s_cselect_b32 s65, 8192, 0\n"
        "s_lshl_b32 s68, s51, 21\n"
        "s_add_u32 s68, s68, s65\n"
        "s_add_u32 s68, s68, 2097152\n"
        "s_add_u32 s70, s68, s84\n"
        "s_add_u32 s69, s68, s83\n"
        "ds_read_b128 v[40:43], v2 offset:0\n"
        "ds_read_b128 v[44:47], v2 offset:1024\n"
        "ds_read_b128 v[48:51], v2 offset:2048\n"
        "ds_read_b128 v[52:55], v2 offset:3072\n"
        "ds_read_b128 v[56:59], v2 offset:4096\n"
        "ds_read_b128 v[60:63], v2 offset:5120\n"
        "ds_read_b128 v[76:79], v4 offset:9216\n"
        "v_mov_b64 v[8:9], 0\n"
        "v_mov_b64 v[10:11], 0\n"
        "v_mov_b64 v[12:13], 0\n"
        "v_mov_b64 v[14:15], 0\n"
        "v_mov_b64 v[16:17], 0\n"
        "v_mov_b64 v[18:19], 0\n"
        "v_mov_b64 v[20:21], 0\n"
        "v_mov_b64 v[22:23], 0\n"
        "v_mov_b64 v[24:25], 0\n"
        "v_mov_b64 v[26:27], 0\n"
        "v_mov_b64 v[28:29], 0\n"
        "v_mov_b64 v[30:31], 0\n"
        "v_mov_b32 v6, 0\n"
        "s_add_u32 s89, s69, 524288\n"
        "s_mov_b32 s30, s69\n"
        "s_mov_b32 s77, s89\n"
        "s_cmp_eq_u32 s51, 63\n"
        "s_cselect_b32 s67, 2147483647, s80\n"
        "s_waitcnt vmcnt(4)\n"
        "v_readfirstlane_b32 s65, v7\n"
        "s_nop 0\n"
        "s_cmp_ge_i32 s65, s67\n"
        "s_cbranch_scc1 .Lch_A2_%=\n"
        "s_mov_b32 s30, 2147483648\n"
        "s_mov_b32 s89, 2147483648\n"
        "s_sub_u32 s77, s78, 1024\n"
        "v_mov_b32 v7, s51\n"
        "ds_write_b32 v4, v7 offset:11264\n"
        "s_branch .Lch_A2_%=\n"
        ".p2align 5\n"
        ".Lch_nog_%=:\n"
        "s_add_u32 s51, s51, 1\n"
        "s_add_u32 s80, s80, 2\n"
        "s_bitcmp1_b64 s[52:53], s51\n"
        "s_cselect_b32 s65, 8192, 0\n"
        "s_lshl_b32 s68, s51, 21\n"
        "s_add_u32 s68, s68, s65\n"
        "s_add_u32 s68, s68, 2097152\n"
        "s_add_u32 s70, s68, s84\n"
        "s_add_u32 s69, s68, s83\n"
        "ds_read_b128 v[40:43], v2 offset:0\n"
        "ds_read_b128 v[44:47], v2 offset:1024\n"
        "ds_read_b128 v[48:51], v2 offset:2048\n"
        "ds_read_b128 v[52:55], v2 offset:3072\n"
        "ds_read_b128 v[56:59], v2 offset:4096\n"
        "ds_read_b128 v[60:63], v2 offset:5120\n"
        "ds_read_b128 v[76:79], v4 offset:9216\n"
        "v_mov_b64 v[8:9], 0\n"
        "v_mov_b64 v[10:11], 0\n"
        "v_mov_b64 v[12:13], 0\n"
        "v_mov_b64 v[14:15], 0\n"
        "v_mov_b64 v[16:17], 0\n"
        "v_mov_b64 v[18:19], 0\n"
        "v_mov_b64 v[20:21], 0\n"
        "v_mov_b64 v[22:23], 0\n"
        "v_mov_b64 v[24:25], 0\n"
        "v_mov_b64 v[26:27], 0\n"
        "v_mov_b64 v[28:29], 0\n"
        "v_mov_b64 v[30:31], 0\n"
        "v_mov_b32 v6, 0\n"
        "s_add_u32 s89, s69, 524288\n"
        "s_mov_b32 s30, s69\n"
        "s_mov_b32 s77, s89\n"
        "s_cmp_eq_u32 s51, 63\n"
        "s_cselect_b32 s67, 2147483647, s80\n"
        "s_cmp_eq_u32 s48, 3\n"
        "s_cselect_b32 s65, s51, 0\n"
        "s_cmp_eq_u32 s65, 0\n"
        "s_cbranch_scc0 .Lch_g3_%=\n"
        "s_waitcnt vmcnt(0)\n"
        "v_readfirstlane_b32 s65, v7\n"
        "s_nop 0\n"
        "s_cmp_ge_i32 s65, s67\n"
        "s_nop 0\n"
        "s_nop 0\n"
        "s_nop 0\n"
        "s_cbranch_scc1 .Lch_A_%=\n"
        "s_mov_b32 s30, 2147483648\n"
        "s_mov_b32 s89, 2147483648\n"
        "s_sub_u32 s77, s78, 1024\n"
        "v_mov_b32 v7, s51\n"
        "ds_write_b32 v4, v7 offset:11264\n"
        "s_branch .Lch_A_%=\n"
        ".p2align 5\n"
        ".Lch_g3_%=:\n"
        "s_and_b32 s66, s33, 1024\n"
        "s_lshl_b32 s66, s66, 2\n"
        "s_add_u32 m0, s66, 151552\n"
        "s_add_u32 s66, s54, s71\n"
        "s_memtime s[90:91]\n"
        "s_waitcnt lgkmcnt(0)\n"
        "s_sub_u32 s65, s82, s90\n"
        "s_cmp_lt_u32 s65, 20000\n"
        "s_cselect_b32 s65, s65, 0\n"
        "s_nop 0\n"
        "s_nop 0\n"
        "s_nop 0\n"
        ".Lch_gwb_%=:\n"
        "s_sub_u32 s65, s65, 28\n"
        "s_cbranch_scc0 .Lch_gwb_%=\n"
        "buffer_load_dwordx4 v3, s[40:43], s66 offen offset:0 sc1 lds\n"
        "buffer_load_dwordx4 v3, s[40:43], s66 offen offset:1024 sc1 lds\n"
        "buffer_load_dwordx4 v3, s[40:43], s66 offen offset:2048 sc1 lds\n"
        "buffer_load_dwordx4 v3, s[40:43], s66 offen offset:3072 sc1 lds\n"
        "s_mov_b32 m0, 159744\n"
        "s_add_u32 s66, s71, s55\n"
        "buffer_load_dwordx4 v32, s[40:43], s66 offen sc1 lds\n"
        "s_waitcnt vmcnt(5)\n"
        "v_readfirstlane_b32 s65, v7\n"
        "s_nop 0\n"
        "s_cmp_ge_i32 s65, s67\n"
        "s_cbranch_scc1 .Lch_A_%=\n"
        "s_mov_b32 s30, 2147483648\n"
        "s_mov_b32 s89, 2147483648\n"
        "s_sub_u32 s77, s78, 1024\n"
        "v_mov_b32 v7, s51\n"
        "ds_write_b32 v4, v7 offset:11264\n"
        "s_branch .Lch_A_%=\n"
        ".p2align 5\n"
        ".Lch_rretry_%=:\n"
        "s_waitcnt vmcnt(0)\n"
        "s_add_u32 s64, s64, 1\n"
        "s_and_b32 s65, s64, 255\n"
        "s_cbranch_scc1 .Lch_rr_go_%=\n"
        "s_mov_b32 s65, 49152\n"
        "s_cmp_lt_u32 s64, 1048576\n"
        "s_cbranch_scc1 .Lch_rr_chk_%=\n"
        "v_mov_b32 v8, 1\n"
        "buffer_store_dword v8, off, s[40:43], s65 sc1\n"
        "s_waitcnt vmcnt(0)\n"
        ".Lch_rr_chk_%=:\n"
        "buffer_load_dword v8, off, s[40:43], s65 sc1\n"
        "s_waitcnt vmcnt(0)\n"
        "v_readfirstlane_b32 s65, v8\n"
        "s_nop 0\n"
        "s_cmp_lg_u32 s65, 1\n"
        "s_cbranch_scc1 .Lch_rr_go_%=\n"
        "s_endpgm\n"
        ".p2align 5\n"
        ".Lch_rr_go_%=:\n"
        "s_branch .Lch_rl_%=\n"
        ".p2align 5\n"
        ".Lch_stwait_%=:\n"
        "s_add_u32 s64, s64, 1\n"
        "s_and_b32 s65, s64, 255\n"
        "s_cbranch_scc1 .Lch_sw_go_%=\n"
        "s_mov_b32 s65, 49152\n"
        "s_cmp_lt_u32 s64, 1048576\n"
        "s_cbranch_scc1 .Lch_sw_chk_%=\n"
        "v_mov_b32 v12, 1\n"
        "buffer_store_dword v12, off, s[40:43], s65 sc1\n"
        "s_waitcnt vmcnt(0)\n"
        ".Lch_sw_chk_%=:\n"
        "buffer_load_dword v12, off, s[40:43], s65 sc1\n"
        "s_waitcnt vmcnt(0)\n"
        "v_readfirstlane_b32 s65, v12\n"
        "s_nop 0\n"
        "s_cmp_lg_u32 s65, 1\n"
        "s_cbranch_scc1 .Lch_sw_go_%=\n"
        "s_endpgm\n"
        ".p2align 5\n"
        ".Lch_sw_go_%=:\n"
        "s_sleep 1\n"
        "v_lshlrev_b32 v6, 3, v3\n"
        "buffer_load_dword v6, v6, s[40:43], s47 offen sc1\n"
        "s_waitcnt vmcnt(0)\n"
        "s_branch .Lch_st1_%=\n"
        ".p2align 5\n"
        ".Lch_rt0_%=:\n"
        "s_waitcnt lgkmcnt(0)\n"
        "s_barrier\n"
        "s_add_u32 s64, s64, 1\n"
        "s_and_b32 s65, s64, 255\n"
        "s_nop 0\n"
        "s_nop 0\n"
        "s_nop 0\n"
        "s_cbranch_scc1 .Lch_rt0_go_%=\n"
        "s_mov_b32 s65, 49152\n"
        "s_cmp_lt_u32 s64, 1048576\n"
        "s_nop 0\n"
        "s_nop 0\n"
        "s_nop 0\n"
        "s_cbranch_scc1 .Lch_rt0_chk_%=\n"
        "v_mov_b32 v7, 1\n"
        "buffer_store_dword v7, off, s[40:43], s65 sc1\n"
        "s_waitcnt vmcnt(0)\n"
        ".Lch_rt0_chk_%=:\n"
        "buffer_load_dword v7, off, s[40:43], s65 sc1\n"
        "s_waitcnt vmcnt(0)\n"
        "v_readfirstlane_b32 s65, v7\n"
        "s_nop 0\n"
        "s_cmp_lg_u32 s65, 1\n"
        "s_cbranch_scc1 .Lch_rt0_go_%=\n"
        "s_endpgm\n"
        ".p2align 5\n"
        ".Lch_rt0_go_%=:\n"
        "s_cmp_eq_u32 s48, 2\n"
        "s_cbranch_scc0 .Lch_rw0_%=\n"
        "s_and_b32 s66, s33, 1024\n"
        "s_lshl_b32 s66, s66, 2\n"
        "s_add_u32 m0, s66, 151552\n"
        "s_add_u32 s66, s54, s71\n"
        "buffer_load_dwordx4 v3, s[40:43], s66 offen offset:0 sc1 lds\n"
        "buffer_load_dwordx4 v3, s[40:43], s66 offen offset:1024 sc1 lds\n"
        "buffer_load_dwordx4 v3, s[40:43], s66 offen offset:2048 sc1 lds\n"
        "buffer_load_dwordx4 v3, s[40:43], s66 offen offset:3072 sc1 lds\n"
        "s_waitcnt vmcnt(0)\n"
        ".Lch_rw0_%=:\n"
        "s_waitcnt vmcnt(0)\n"
        "ds_read_b128 v[40:43], v2 offset:0\n"
        "ds_read_b128 v[44:47], v2 offset:1024\n"
        "ds_read_b128 v[48:51], v2 offset:2048\n"
        "ds_read_b128 v[52:55], v2 offset:3072\n"
        "ds_read_b128 v[56:59], v2 offset:4096\n"
        "ds_read_b128 v[60:63], v2 offset:5120\n"
        "ds_read_b128 v[76:79], v4 offset:9216\n"
        "v_mov_b64 v[8:9], 0\n"
        "v_mov_b64 v[10:11], 0\n"
        "v_mov_b64 v[12:13], 0\n"
        "v_mov_b64 v[14:15], 0\n"
        "v_mov_b64 v[16:17], 0\n"
        "v_mov_b64 v[18:19], 0\n"
        "v_mov_b64 v[20:21], 0\n"
        "v_mov_b64 v[22:23], 0\n"
        "v_mov_b64 v[24:25], 0\n"
        "v_mov_b64 v[26:27], 0\n"
        "v_mov_b64 v[28:29], 0\n"
        "v_mov_b64 v[30:31], 0\n"
        "v_mov_b32 v6, 0\n"
        "s_branch .Lch_A_%=\n"
        ".p2align 5\n"
        ".Lch_rt1_%=:\n"
        "s_waitcnt lgkmcnt(0)\n"
        "s_barrier\n"
        "s_add_u32 s64, s64, 1\n"
        "s_and_b32 s65, s64, 255\n"
        "s_nop 0\n"
        "s_nop 0\n"
        "s_nop 0\n"
        "s_cbranch_scc1 .Lch_rt1_go_%=\n"
        "s_mov_b32 s65, 49152\n"
        "s_cmp_lt_u32 s64, 1048576\n"
        "s_nop 0\n"
        "s_nop 0\n"
        "s_nop 0\n"
        "s_cbranch_scc1 .Lch_rt1_chk_%=\n"
        "v_mov_b32 v7, 1\n"
        "buffer_store_dword v7, off, s[40:43], s65 sc1\n"
        "s_waitcnt vmcnt(0)\n"
        ".Lch_rt1_chk_%=:\n"
        "buffer_load_dword v7, off, s[40:43], s65 sc1\n"
        "s_waitcnt vmcnt(0)\n"
        "v_readfirstlane_b32 s65, v7\n"
        "s_nop 0\n"
        "s_cmp_lg_u32 s65, 1\n"
        "s_cbranch_scc1 .Lch_rt1_go_%=\n"
        "s_endpgm\n"
        ".p2align 5\n"
        ".Lch_rt1_go_%=:\n"
        "s_cmp_eq_u32 s48, 3\n"
        "s_cbranch_scc0 .Lch_rw1_%=\n"
        "s_and_b32 s66, s33, 1024\n"
        "s_lshl_b32 s66, s66, 2\n"
        "s_add_u32 m0, s66, 151552\n"
        "s_add_u32 s66, s54, s71\n"
        "buffer_load_dwordx4 v3, s[40:43], s66 offen offset:0 sc1 lds\n"
        "buffer_load_dwordx4 v3, s[40:43], s66 offen offset:1024 sc1 lds\n"
        "buffer_load_dwordx4 v3, s[40:43], s66 offen offset:2048 sc1 lds\n"
        "buffer_load_dwordx4 v3, s[40:43], s66 offen offset:3072 sc1 lds\n"
        "s_waitcnt vmcnt(0)\n"
        ".Lch_rw1_%=:\n"
        "s_barrier\n"
        "ds_read_b32 v7, v5 offset:4096\n"
        "ds_read_b128 v[36:39], v1 offset:4096\n"
        "ds_read_b128 v[72:75], v1 offset:5120\n"
        "s_waitcnt lgkmcnt(0)\n"
        "v_cmp_eq_u32 vcc, -1, v7\n"
        "s_cbranch_vccnz .Lch_rt1_%=\n"
        "s_branch .Lch_ok1_%=\n"
        ".p2align 5\n"
        ".Lch_rt2_%=:\n"
        "s_waitcnt lgkmcnt(0)\n"
        "s_barrier\n"
        "s_add_u32 s64, s64, 1\n"
        "s_and_b32 s65, s64, 255\n"
        "s_nop 0\n"
        "s_nop 0\n"
        "s_nop 0\n"
        "s_cbranch_scc1 .Lch_rt2_go_%=\n"
        "s_mov_b32 s65, 49152\n"
        "s_cmp_lt_u32 s64, 1048576\n"
        "s_nop 0\n"
        "s_nop 0\n"
        "s_nop 0\n"
        "s_cbranch_scc1 .Lch_rt2_chk_%=\n"
        "v_mov_b32 v7, 1\n"
        "buffer_store_dword v7, off, s[40:43], s65 sc1\n"
        "s_waitcnt vmcnt(0)\n"
        ".Lch_rt2_chk_%=:\n"
        "buffer_load_dword v7, off, s[40:43], s65 sc1\n"
        "s_waitcnt vmcnt(0)\n"
        "v_readfirstlane_b32 s65, v7\n"
        "s_nop 0\n"
        "s_cmp_lg_u32 s65, 1\n"
        "s_cbranch_scc1 .Lch_rt2_go_%=\n"
        "s_endpgm\n"
        ".p2align 5\n"
        ".Lch_rt2_go_%=:\n"
        "s_cmp_eq_u32 s48, 3\n"
        "s_cbranch_scc0 .Lch_rw2_%=\n"
        "v_add_u32 v7, 114688, v3\n"
        "ds_read_b32 v7, v7 offset:1028\n"
        "s_waitcnt lgkmcnt(0)\n"
        "s_mov_b32 m0, 159744\n"
        "s_add_u32 s66, s71, s55\n"
        "buffer_load_dwordx4 v7, s[40:43], s66 offen sc1 lds\n"
        "s_waitcnt vmcnt(0)\n"
        ".Lch_rw2_%=:\n"
        "s_barrier\n"
        "ds_read_b32 v7, v5 offset:8192\n"
        "ds_read_b128 v[72:75], v1 offset:8192\n"
        "s_memtime s[36:37]\n"
        "s_waitcnt lgkmcnt(0)\n"
        "v_cmp_eq_u32 vcc, -1, v7\n"
        "s_nop 0\n"
        "s_and_b64 vcc, vcc, 0xffff\n"
        "s_cbranch_vccnz .Lch_rt2_%=\n"
        "s_branch .Lch_ok2_%=\n"
        ".p2align 5\n"
        ".Lch_redo_%=:\n"
        "s_waitcnt vmcnt(0) lgkmcnt(0)\n"
        "s_barrier\n"
        "s_add_u32 s64, s64, 1\n"
        "s_and_b32 s65, s64, 255\n"
        "s_nop 0\n"
        "s_nop 0\n"
        "s_nop 0\n"
        "s_cbranch_scc1 .Lch_rd_go_%=\n"
        "s_mov_b32 s65, 49152\n"
        "s_cmp_lt_u32 s64, 1048576\n"
        "s_nop 0\n"
        "s_nop 0\n"
        "s_nop 0\n"
        "s_cbranch_scc1 .Lch_rd_chk_%=\n"
        "v_mov_b32 v7, 1\n"
        "buffer_store_dword v7, off, s[40:43], s65 sc1\n"
        "s_waitcnt vmcnt(0)\n"
        ".Lch_rd_chk_%=:\n"
        "buffer_load_dword v7, off, s[40:43], s65 sc1\n"
        "s_waitcnt vmcnt(0)\n"
        "v_readfirstlane_b32 s65, v7\n"
        "s_nop 0\n"
        "s_cmp_lg_u32 s65, 1\n"
        "s_cbranch_scc1 .Lch_rd_go_%=\n"
        "s_endpgm\n"
        ".p2align 5\n"
        ".Lch_rd_go_%=:\n"
        "s_cmp_lt_u32 s48, 2\n"
        "s_cbranch_scc1 .Lch_rd2_%=\n"
        "s_and_b32 s66, s33, 1024\n"
        "s_lshl_b32 s66, s66, 2\n"
        "s_add_u32 m0, s66, 151552\n"
        "s_add_u32 s66, s54, s71\n"
        "buffer_load_dwordx4 v3, s[40:43], s66 offen offset:0 sc1 lds\n"
        "buffer_load_dwordx4 v3, s[40:43], s66 offen offset:1024 sc1 lds\n"
        "buffer_load_dwordx4 v3, s[40:43], s66 offen offset:2048 sc1 lds\n"
        "buffer_load_dwordx4 v3, s[40:43], s66 offen offset:3072 sc1 lds\n"
        "v_add_u32 v7, 114688, v3\n"
        "ds_read_b32 v7, v7 offset:1028\n"
        "s_waitcnt lgkmcnt(0)\n"
        "s_mov_b32 m0, 159744\n"
        "s_add_u32 s66, s71, s55\n"
        "buffer_load_dwordx4 v7, s[40:43], s66 offen sc1 lds\n"
        ".Lch_rd2_%=:\n"
        "s_nop 7\n"
        "ds_read_b128 v[40:43], v2 offset:0\n"
        "ds_read_b128 v[44:47], v2 offset:1024\n"
        "ds_read_b128 v[48:51], v2 offset:2048\n"
        "ds_read_b128 v[52:55], v2 offset:3072\n"
        "ds_read_b128 v[56:59], v2 offset:4096\n"
        "ds_read_b128 v[60:63], v2 offset:5120\n"
        "ds_read_b128 v[76:79], v4 offset:9216\n"
        "v_mov_b64 v[8:9], 0\n"
        "v_mov_b64 v[10:11], 0\n"
        "v_mov_b64 v[12:13], 0\n"
        "v_mov_b64 v[14:15], 0\n"
        "v_mov_b64 v[16:17], 0\n"
        "v_mov_b64 v[18:19], 0\n"
        "v_mov_b64 v[20:21], 0\n"
        "v_mov_b64 v[22:23], 0\n"
        "v_mov_b64 v[24:25], 0\n"
        "v_mov_b64 v[26:27], 0\n"
        "v_mov_b64 v[28:29], 0\n"
        "v_mov_b64 v[30:31], 0\n"
        "v_mov_b32 v6, 0\n"
        "s_waitcnt vmcnt(0)\n"
        "s_branch .Lch_A_%=\n"
        ".p2align 5\n"
        ".Lch_slow_%=:\n"
        "v_max3_u32 v6, v6, v38, v39\n"
        "v_max3_u32 v6, v6, v72, v73\n"
        "v_max3_u32 v6, v6, v74, v75\n"
        "v_cmp_eq_u32_e64 vcc, -1, v6\n"
        "s_cbranch_vccnz .Lch_redo_%=\n"
        "s_waitcnt vmcnt(0) lgkmcnt(0)\n"
        "s_cmp_eq_u32 s30, s69\n"
        "s_cbranch_scc1 .Lch_slow3_%=\n"
        "v_readfirstlane_b32 s65, v52\n"
        "s_cmp_eq_u32 s51, 63\n"
        "s_cselect_b32 s65, -2, s65\n"
        "s_cmp_ge_i32 s65, s80\n"
        "s_cbranch_scc1 .Lch_kvw_ok_%=\n"
        "ds_read_b128 v[68:71], v4 offset:10240\n"
        "v_mov_b64 v[48:49], v[24:25]\n"
        "v_mov_b64 v[50:51], v[26:27]\n"
        "v_mov_b64 v[52:53], v[28:29]\n"
        "v_mov_b64 v[54:55], v[30:31]\n"
        "v_cvt_pk_bf16_f32 v64, v12, v13\n"
        "v_cvt_pk_bf16_f32 v65, v14, v15\n"
        "v_cvt_pk_bf16_f32 v66, v16, v17\n"
        "v_cvt_pk_bf16_f32 v67, v18, v19\n"
        "v_smfmac_f32_16x16x64_bf16 v[48:51], v[36:39], v[56:63], v0\n"
        "v_smfmac_f32_16x16x64_bf16 v[52:55], v[36:39], v[40:47], v0\n"
        "v_smfmac_f32_16x16x64_bf16 v[48:51], v[72:75], v[240:247], v0\n"
        "v_smfmac_f32_16x16x64_bf16 v[52:55], v[72:75], v[248:255], v0\n"
        "s_waitcnt lgkmcnt(0)\n"
        "buffer_store_dwordx4 v[64:67], v68, s[40:43], s70 offen sc1\n"
        "s_nop 5\n"
        "v_add_f32 v48, v48, v51\n"
        "v_add_f32 v49, v49, v50\n"
        "v_add_f32 v52, v52, v55\n"
        "v_add_f32 v53, v53, v54\n"
        "v_cvt_pk_bf16_f32 v48, v48, v49\n"
        "v_cvt_pk_bf16_f32 v50, v52, v53\n"
        "s_nop 1\n"
        "v_permlane16_swap_b32 v48, v49\n"
        "v_permlane16_swap_b32 v50, v51\n"
        "buffer_store_dwordx4 v[48:51], v69, s[40:43], s70 offen offset:2048 sc1\n"
        "s_mov_b64 exec, 1\n"
        "buffer_load_dword v64, off, s[40:43], s78 sc1\n"
        "s_mov_b64 exec, -1\n"
        "s_waitcnt vmcnt(0)\n"
        "v_mov_b32 v7, s51\n"
        "s_mov_b64 exec, 1\n"
        "buffer_store_dword v7, off, s[40:43], s79 sc1\n"
        "s_mov_b64 exec, -1\n"
        "s_cmp_eq_u32 s51, 63\n"
        "s_cbranch_scc0 .Lch_slow2_%=\n"
        "s_or_b32 s65, s49, s48\n"
        "s_nop 0\n"
        "s_nop 0\n"
        "s_nop 0\n"
        "s_cbranch_scc1 .Lch_bye_%=\n"
        "s_lshl_b32 s66, s50, 7\n"
        "s_add_u32 s66, s66, 51200\n"
        ".Lch_bye_%=:\n"
        "s_endpgm\n"
        ".p2align 5\n"
        ".Lch_slow2_%=:\n"
        "s_mov_b32 s70, 241172480\n"
        "v_readfirstlane_b32 s65, v64\n"
        "s_nop 0\n"
        "s_cmp_ge_i32 s65, s80\n"
        "s_nop 0\n"
        "s_nop 0\n"
        "s_nop 0\n"
        "s_cbranch_scc1 .Lch_kvw_ok_%=\n"
        "s_mov_b32 s64, 0\n"
        ".Lch_kvw_top_%=:\n"
        "s_mov_b64 exec, 1\n"
        "buffer_load_dword v7, off, s[40:43], s78 sc1\n"
        "s_mov_b64 exec, -1\n"
        "s_waitcnt vmcnt(0)\n"
        "v_cmp_gt_i32 vcc, s80, v7\n"
        "s_nop 0\n"
        "s_and_b64 vcc, vcc, 1\n"
        "s_cbranch_vccz .Lch_kvw_ok_%=\n"
        "s_add_u32 s64, s64, 1\n"
        "s_and_b32 s65, s64, 255\n"
        "s_nop 0\n"
        "s_nop 0\n"
        "s_cbranch_scc1 .Lch_kvw_go_%=\n"
        "s_mov_b32 s65, 49152\n"
        "s_cmp_lt_u32 s64, 1048576\n"
        "s_nop 0\n"
        "s_nop 0\n"
        "s_nop 0\n"
        "s_cbranch_scc1 .Lch_kvw_chk_%=\n"
        "v_mov_b32 v7, 1\n"
        "buffer_store_dword v7, off, s[40:43], s65 sc1\n"
        "s_waitcnt vmcnt(0)\n"
        ".Lch_kvw_chk_%=:\n"
        "buffer_load_dword v7, off, s[40:43], s65 sc1\n"
        "s_waitcnt vmcnt(0)\n"
        "v_readfirstlane_b32 s65, v7\n"
        "s_nop 0\n"
        "s_cmp_lg_u32 s65, 1\n"
        "s_cbranch_scc1 .Lch_kvw_go_%=\n"
        "s_endpgm\n"
        ".p2align 5\n"
        ".Lch_kvw_go_%=:\n"
        "s_branch .Lch_kvw_top_%=\n"
        ".p2align 5\n"
        ".Lch_kvw_ok_%=:\n"
        "s_add_u32 s89, s69, 524288\n"
        "buffer_load_dwordx4 v[68:71], v3, s[40:43], s69 offen offset:1024 sc1\n"
        "buffer_load_dwordx4 v[64:67], v3, s[40:43], s69 offen sc1\n"
        "buffer_load_dwordx4 v[48:51], v3, s[40:43], s89 offen sc1\n"
        "buffer_load_dwordx4 v[52:55], v3, s[40:43], s89 offen offset:1024 sc1\n"
        "s_mov_b32 s77, s89\n"
        ".Lch_slow3_%=:\n"
        "s_barrier\n"
        "s_memtime s[36:37]\n"
        "s_waitcnt lgkmcnt(0)\n"
        "s_add_u32 s36, s36, 256\n"
        "s_sub_u32 s37, s36, s38\n"
        "v_mov_b32 v7, s51\n"
        "v_lshl_or_b32 v7, s37, 6, v7\n"
        "s_mov_b64 exec, 1\n"
        "buffer_store_dword v7, off, s[40:43], s46 sc1\n"
        "s_mov_b64 exec, -1\n"
        "s_mov_b32 s30, s69\n"
        "s_branch .Lch_back_%=\n"
        ".Lch_end_%=:\n"
        :: "s"(p.ws), "s"(p.Wq), "s"(p.Wk), "s"(p.Wv), "s"(p.Wo), "s"(p.mem0), "s"(pair(w, h)), "s"(pair(x4, pr)), "s"(p.tune0), "s"(p.tune1),
           "v"(pair(idx, va + (lane & 2) * 32)), "v"(pair(wb + lane * 16, lane * 16)), "v"(pair(spr, CH_IMG + lane * 64 + c * 4)),
           "v"(pair(qo(0), qo(1))), "v"(pair(oo, o2)), "v"(pair(tr + (q >> 1) * 32, m0)), "v"(pair(to(0), to(1))), "v"(pair(tq(0, 0), tq(0, 1))), "v"(pair(tq(1, 0), tq(1, 1)))
        : "v0", "v1", "v2", "v3", "v4", "v5", "v6", "v7", "v8", "v9", "v10", "v11", "v12", "v13", "v14", "v15", "v16", "v17", "v18", "v19", "v20", "v21", "v22", "v23", "v24", "v25", "v26", "v27", "v28", "v29", "v30", "v31", "v32", "v33", "v34", "v35", "v36", "v37", "v38", "v39", "v40", "v41", "v42", "v43", "v44", "v45", "v46", "v47", "v48", "v49", "v50", "v51", "v52", "v53", "v54", "v55", "v56", "v57", "v58", "v59", "v60", "v61", "v62", "v63", "v64", "v65", "v66", "v67", "v68", "v69", "v70", "v71", "v72", "v73", "v74", "v75", "v76", "v77", "v78", "v79", "v255", "a255", "s26", "s27", "s28", "s29", "s30", "s31", "s32", "s33", "s34", "s35", "s36", "s37", "s38", "s39", "s40", "s41", "s42", "s43", "s44", "s45", "s46", "s47", "s48", "s49", "s50", "s51", "s52", "s53", "s54", "s55", "s56", "s57", "s58", "s59", "s60", "s61", "s62", "s63", "s64", "s65", "s66", "s67", "s68", "s69", "s70", "s71", "s72", "s73", "s74", "s75", "s76", "s77", "s78", "s79", "s80", "s81", "s82", "s83", "s84", "s85", "s86", "s87", "s88", "s89", "s90", "s91", "s92", "s93", "s94", "s95", "s96", "s97", "s98", "s99", "vcc", "scc", "memory");
    __builtin_unreachable();
}

constexpr unsigned flag(unsigned i) { return spread(LF0 + i * 128); }
#define GEMM_HEAD const unsigned lane = threadIdx.x & 63, i = lane & 15, g = lane >> 4, q = i >> 2, pp = lane & 3; const int w = rfl(threadIdx.x >> 6)
#define TILE_ARGS(base) "s"(pair(base + (w >> 1) * 16384 + (w & 1) * 4096, out1)), "s"(pair(fout, ((w >> 1) * 9216 + (w & 1) * 4096) | ((w >> 1) * 9216 + 8192) << 16)), \
        "s"(pair(base + 65536 + (w >> 1) * 1024, out2)), "v"(pair((i >> 3) * 9216 + g * 128 + (i & 7) * 16, lane * 16))
__device__ __forceinline__ void role_s1(const Params& p, int h, int x4, int pr, int delay) {
    GEMM_HEAD;
    const unsigned vo = (16 * w + g) * 8192 + 256 * h, tr = (g >> 1) * 40960 + (8 * (g & 1) + q) * 256 + pp * 8, sx = 2 * q + 8 * (g & 1);
    const unsigned t0 = tr + ((4 * w) ^ sx) * 16, t1 = tr + ((4 * w + 2) ^ sx) * 16;
    const unsigned out1 = PAGE + h * 16384 + w * 2048, out2 = 0, fout = flag(F_S1 + 4 * h + w);
    asm volatile(
        "s_nop 4\n"
        "s_mov_b64 s[40:41], %0\n"
        "s_and_b32 s41, s41, 0xffff\n"
        "s_mov_b32 s42, 236978176\n"
        "s_mov_b32 s43, 0x20000\n"
        "s_mov_b64 s[48:49], %4\n"
        "s_mov_b64 s[56:57], %5\n"
        "s_mov_b64 s[58:59], %6\n"
        "s_mov_b64 s[60:61], %7\n"
        "s_mov_b64 s[62:63], %8\n"
        "v_mov_b64 v[0:1], %9\n"
        "v_mov_b64 v[2:3], %10\n"
        "v_mov_b64 v[4:5], %11\n"
        "v_mov_b64 v[6:7], %12\n"
        "v_mov_b64 v[8:9], %13\n"
        "v_mov_b64 v[10:11], %14\n"
        "v_mov_b64 v[12:13], %15\n"
        "s_mov_b32 s47, 0x20000\n"
        "s_mul_i32 s79, s48, 40960\n"
        ".Ls1_start_%=:\n"
        "s_sub_u32 s49, s49, 1\n"
        "s_cbranch_scc1 .Ls1_start2_%=\n"
        "s_sleep 16\n"
        "s_branch .Ls1_start_%=\n"
        ".Ls1_start2_%=:\n"
        "s_mov_b64 s[44:45], %1\n"
        "s_and_b32 s45, s45, 0xffff\n"
        "s_mov_b32 s46, 4718592\n"
        "s_add_u32 m0, s79, 0\n"
        "s_mov_b32 s65, 0\n"
        "buffer_load_dwordx4 v8, s[44:47], s65 offen lds\n"
        "s_mov_b32 s65, 31744\n"
        "buffer_load_dwordx4 v8, s[44:47], s65 offen offset:1024 lds\n"
        "s_mov_b32 s65, 63488\n"
        "buffer_load_dwordx4 v9, s[44:47], s65 offen offset:2048 lds\n"
        "s_mov_b32 s65, 95232\n"
        "buffer_load_dwordx4 v9, s[44:47], s65 offen offset:3072 lds\n"
        "s_add_u32 m0, s79, 4096\n"
        "s_mov_b32 s65, 524288\n"
        "buffer_load_dwordx4 v8, s[44:47], s65 offen lds\n"
        "s_mov_b32 s65, 556032\n"
        "buffer_load_dwordx4 v8, s[44:47], s65 offen offset:1024 lds\n"
        "s_mov_b32 s65, 587776\n"
        "buffer_load_dwordx4 v9, s[44:47], s65 offen offset:2048 lds\n"
        "s_mov_b32 s65, 619520\n"
        "buffer_load_dwordx4 v9, s[44:47], s65 offen offset:3072 lds\n"
        "s_add_u32 m0, s79, 8192\n"
        "s_mov_b32 s65, 1048576\n"
        "buffer_load_dwordx4 v8, s[44:47], s65 offen lds\n"
        "s_mov_b32 s65, 1080320\n"
        "buffer_load_dwordx4 v8, s[44:47], s65 offen offset:1024 lds\n"
        "s_mov_b32 s65, 1112064\n"
        "buffer_load_dwordx4 v9, s[44:47], s65 offen offset:2048 lds\n"
        "s_mov_b32 s65, 1143808\n"
        "buffer_load_dwordx4 v9, s[44:47], s65 offen offset:3072 lds\n"
        "s_add_u32 m0, s79, 12288\n"
        "s_mov_b32 s65, 1572864\n"
        "buffer_load_dwordx4 v8, s[44:47], s65 offen lds\n"
        "s_mov_b32 s65, 1604608\n"
        "buffer_load_dwordx4 v8, s[44:47], s65 offen offset:1024 lds\n"
        "s_mov_b32 s65, 1636352\n"
        "buffer_load_dwordx4 v9, s[44:47], s65 offen offset:2048 lds\n"
        "s_mov_b32 s65, 1668096\n"
        "buffer_load_dwordx4 v9, s[44:47], s65 offen offset:3072 lds\n"
        "s_waitcnt vmcnt(12)\n"
        "s_waitcnt lgkmcnt(0)\n"
        "s_barrier\n"
        "ds_read_b64_tr_b16 a[0:1], v10 offset:0\n"
        "ds_read_b64_tr_b16 a[2:3], v10 offset:1024\n"
        "ds_read_b64_tr_b16 a[4:5], v11 offset:0\n"
        "ds_read_b64_tr_b16 a[6:7], v11 offset:1024\n"
        "ds_read_b64_tr_b16 a[24:25], v12 offset:0\n"
        "ds_read_b64_tr_b16 a[26:27], v12 offset:1024\n"
        "ds_read_b64_tr_b16 a[28:29], v13 offset:0\n"
        "ds_read_b64_tr_b16 a[30:31], v13 offset:1024\n"
        "s_waitcnt vmcnt(8)\n"
        "s_waitcnt lgkmcnt(0)\n"
        "s_barrier\n"
        "s_add_u32 m0, s79, 0\n"
        "s_mov_b32 s65, 2097152\n"
        "buffer_load_dwordx4 v8, s[44:47], s65 offen lds\n"
        "s_mov_b32 s65, 2128896\n"
        "buffer_load_dwordx4 v8, s[44:47], s65 offen offset:1024 lds\n"
        "s_mov_b32 s65, 2160640\n"
        "buffer_load_dwordx4 v9, s[44:47], s65 offen offset:2048 lds\n"
        "s_mov_b32 s65, 2192384\n"
        "buffer_load_dwordx4 v9, s[44:47], s65 offen offset:3072 lds\n"
        "ds_read_b64_tr_b16 a[48:49], v10 offset:4096\n"
        "ds_read_b64_tr_b16 a[50:51], v10 offset:5120\n"
        "ds_read_b64_tr_b16 a[52:53], v11 offset:4096\n"
        "ds_read_b64_tr_b16 a[54:55], v11 offset:5120\n"
        "ds_read_b64_tr_b16 a[72:73], v12 offset:4096\n"
        "ds_read_b64_tr_b16 a[74:75], v12 offset:5120\n"
        "ds_read_b64_tr_b16 a[76:77], v13 offset:4096\n"
        "ds_read_b64_tr_b16 a[78:79], v13 offset:5120\n"
        "s_waitcnt vmcnt(8)\n"
        "s_waitcnt lgkmcnt(0)\n"
        "s_barrier\n"
        "s_add_u32 m0, s79, 4096\n"
        "s_mov_b32 s65, 2621440\n"
        "buffer_load_dwordx4 v8, s[44:47], s65 offen lds\n"
        "s_mov_b32 s65, 2653184\n"
        "buffer_load_dwordx4 v8, s[44:47], s65 offen offset:1024 lds\n"
        "s_mov_b32 s65, 2684928\n"
        "buffer_load_dwordx4 v9, s[44:47], s65 offen offset:2048 lds\n"
        "s_mov_b32 s65, 2716672\n"
        "buffer_load_dwordx4 v9, s[44:47], s65 offen offset:3072 lds\n"
        "ds_read_b64_tr_b16 a[96:97], v10 offset:8192\n"
        "ds_read_b64_tr_b16 a[98:99], v10 offset:9216\n"
        "ds_read_b64_tr_b16 a[100:101], v11 offset:8192\n"
        "ds_read_b64_tr_b16 a[102:103], v11 offset:9216\n"
        "ds_read_b64_tr_b16 a[120:121], v12 offset:8192\n"
        "ds_read_b64_tr_b16 a[122:123], v12 offset:9216\n"
        "ds_read_b64_tr_b16 a[124:125], v13 offset:8192\n"
        "ds_read_b64_tr_b16 a[126:127], v13 offset:9216\n"
        "s_waitcnt vmcnt(8)\n"
        "s_waitcnt lgkmcnt(0)\n"
        "s_barrier\n"
        "s_add_u32 m0, s79, 8192\n"
        "s_mov_b32 s65, 3145728\n"
        "buffer_load_dwordx4 v8, s[44:47], s65 offen lds\n"
        "s_mov_b32 s65, 3177472\n"
        "buffer_load_dwordx4 v8, s[44:47], s65 offen offset:1024 lds\n"
        "s_mov_b32 s65, 3209216\n"
        "buffer_load_dwordx4 v9, s[44:47], s65 offen offset:2048 lds\n"
        "s_mov_b32 s65, 3240960\n"
        "buffer_load_dwordx4 v9, s[44:47], s65 offen offset:3072 lds\n"
        "ds_read_b64_tr_b16 a[144:145], v10 offset:12288\n"
        "ds_read_b64_tr_b16 a[146:147], v10 offset:13312\n"
        "ds_read_b64_tr_b16 a[148:149], v11 offset:12288\n"
        "ds_read_b64_tr_b16 a[150:151], v11 offset:13312\n"
        "ds_read_b64_tr_b16 a[168:169], v12 offset:12288\n"
        "ds_read_b64_tr_b16 a[170:171], v12 offset:13312\n"
        "ds_read_b64_tr_b16 a[172:173], v13 offset:12288\n"
        "ds_read_b64_tr_b16 a[174:175], v13 offset:13312\n"
        "s_waitcnt vmcnt(8)\n"
        "s_waitcnt lgkmcnt(0)\n"
        "s_barrier\n"
        "s_add_u32 m0, s79, 12288\n"
        "s_mov_b32 s65, 3670016\n"
        "buffer_load_dwordx4 v8, s[44:47], s65 offen lds\n"
        "s_mov_b32 s65, 3701760\n"
        "buffer_load_dwordx4 v8, s[44:47], s65 offen offset:1024 lds\n"
        "s_mov_b32 s65, 3733504\n"
        "buffer_load_dwordx4 v9, s[44:47], s65 offen offset:2048 lds\n"
        "s_mov_b32 s65, 3765248\n"
        "buffer_load_dwordx4 v9, s[44:47], s65 offen offset:3072 lds\n"
        "ds_read_b64_tr_b16 a[192:193], v10 offset:0\n"
        "ds_read_b64_tr_b16 a[194:195], v10 offset:1024\n"
        "ds_read_b64_tr_b16 a[196:197], v11 offset:0\n"
        "ds_read_b64_tr_b16 a[198:199], v11 offset:1024\n"
        "ds_read_b64_tr_b16 a[216:217], v12 offset:0\n"
        "ds_read_b64_tr_b16 a[218:219], v12 offset:1024\n"
        "ds_read_b64_tr_b16 a[220:221], v13 offset:0\n"
        "ds_read_b64_tr_b16 a[222:223], v13 offset:1024\n"
        "s_waitcnt vmcnt(8)\n"
        "s_waitcnt lgkmcnt(0)\n"
        "s_barrier\n"
        "s_add_u32 m0, s79, 0\n"
        "s_mov_b32 s65, 4194304\n"
        "buffer_load_dwordx4 v8, s[44:47], s65 offen lds\n"
        "s_mov_b32 s65, 4226048\n"
        "buffer_load_dwordx4 v8, s[44:47], s65 offen offset:1024 lds\n"
        "s_mov_b32 s65, 4257792\n"
        "buffer_load_dwordx4 v9, s[44:47], s65 offen offset:2048 lds\n"
        "s_mov_b32 s65, 4289536\n"
        "buffer_load_dwordx4 v9, s[44:47], s65 offen offset:3072 lds\n"
        "ds_read_b64_tr_b16 a[240:241], v10 offset:4096\n"
        "ds_read_b64_tr_b16 a[242:243], v10 offset:5120\n"
        "ds_read_b64_tr_b16 a[244:245], v11 offset:4096\n"
        "ds_read_b64_tr_b16 a[246:247], v11 offset:5120\n"
        "ds_read_b64_tr_b16 v[88:89], v12 offset:4096\n"
        "ds_read_b64_tr_b16 v[90:91], v12 offset:5120\n"
        "ds_read_b64_tr_b16 v[92:93], v13 offset:4096\n"
        "ds_read_b64_tr_b16 v[94:95], v13 offset:5120\n"
        "s_waitcnt vmcnt(8)\n"
        "s_waitcnt lgkmcnt(0)\n"
        "s_barrier\n"
        "s_mov_b64 s[44:45], %2\n"
        "s_and_b32 s45, s45, 0xffff\n"
        "s_mov_b32 s46, 4718592\n"
        "s_add_u32 m0, s79, 4096\n"
        "s_mov_b32 s65, 0\n"
        "buffer_load_dwordx4 v8, s[44:47], s65 offen lds\n"
        "s_mov_b32 s65, 31744\n"
        "buffer_load_dwordx4 v8, s[44:47], s65 offen offset:1024 lds\n"
        "s_mov_b32 s65, 63488\n"
        "buffer_load_dwordx4 v9, s[44:47], s65 offen offset:2048 lds\n"
        "s_mov_b32 s65, 95232\n"
        "buffer_load_dwordx4 v9, s[44:47], s65 offen offset:3072 lds\n"
        "ds_read_b64_tr_b16 v[112:113], v10 offset:8192\n"
        "ds_read_b64_tr_b16 v[114:115], v10 offset:9216\n"
        "ds_read_b64_tr_b16 v[116:117], v11 offset:8192\n"
        "ds_read_b64_tr_b16 v[118:119], v11 offset:9216\n"
        "ds_read_b64_tr_b16 v[136:137], v12 offset:8192\n"
        "ds_read_b64_tr_b16 v[138:139], v12 offset:9216\n"
        "ds_read_b64_tr_b16 v[140:141], v13 offset:8192\n"
        "ds_read_b64_tr_b16 v[142:143], v13 offset:9216\n"
        "s_waitcnt vmcnt(8)\n"
        "s_waitcnt lgkmcnt(0)\n"
        "s_barrier\n"
        "s_add_u32 m0, s79, 8192\n"
        "s_mov_b32 s65, 524288\n"
        "buffer_load_dwordx4 v8, s[44:47], s65 offen lds\n"
        "s_mov_b32 s65, 556032\n"
        "buffer_load_dwordx4 v8, s[44:47], s65 offen offset:1024 lds\n"
        "s_mov_b32 s65, 587776\n"
        "buffer_load_dwordx4 v9, s[44:47], s65 offen offset:2048 lds\n"
        "s_mov_b32 s65, 619520\n"
        "buffer_load_dwordx4 v9, s[44:47], s65 offen offset:3072 lds\n"
        "ds_read_b64_tr_b16 v[160:161], v10 offset:12288\n"
        "ds_read_b64_tr_b16 v[162:163], v10 offset:13312\n"
        "ds_read_b64_tr_b16 v[164:165], v11 offset:12288\n"
        "ds_read_b64_tr_b16 v[166:167], v11 offset:13312\n"
        "ds_read_b64_tr_b16 v[184:185], v12 offset:12288\n"
        "ds_read_b64_tr_b16 v[186:187], v12 offset:13312\n"
        "ds_read_b64_tr_b16 v[188:189], v13 offset:12288\n"
        "ds_read_b64_tr_b16 v[190:191], v13 offset:13312\n"
        "s_waitcnt vmcnt(8)\n"
        "s_waitcnt lgkmcnt(0)\n"
        "s_barrier\n"
        "s_add_u32 m0, s79, 12288\n"
        "s_mov_b32 s65, 1048576\n"
        "buffer_load_dwordx4 v8, s[44:47], s65 offen lds\n"
        "s_mov_b32 s65, 1080320\n"
        "buffer_load_dwordx4 v8, s[44:47], s65 offen offset:1024 lds\n"
        "s_mov_b32 s65, 1112064\n"
        "buffer_load_dwordx4 v9, s[44:47], s65 offen offset:2048 lds\n"
        "s_mov_b32 s65, 1143808\n"
        "buffer_load_dwordx4 v9, s[44:47], s65 offen offset:3072 lds\n"
        "ds_read_b64_tr_b16 v[208:209], v10 offset:0\n"
        "ds_read_b64_tr_b16 v[210:211], v10 offset:1024\n"
        "ds_read_b64_tr_b16 v[212:213], v11 offset:0\n"
        "ds_read_b64_tr_b16 v[214:215], v11 offset:1024\n"
        "ds_read_b64_tr_b16 v[232:233], v12 offset:0\n"
        "ds_read_b64_tr_b16 v[234:235], v12 offset:1024\n"
        "ds_read_b64_tr_b16 v[236:237], v13 offset:0\n"
        "ds_read_b64_tr_b16 v[238:239], v13 offset:1024\n"
        "s_waitcnt vmcnt(8)\n"
        "s_waitcnt lgkmcnt(0)\n"
        "s_barrier\n"
        "s_add_u32 m0, s79, 0\n"
        "s_mov_b32 s65, 1572864\n"
        "buffer_load_dwordx4 v8, s[44:47], s65 offen lds\n"
        "s_mov_b32 s65, 1604608\n"
        "buffer_load_dwordx4 v8, s[44:47], s65 offen offset:1024 lds\n"
        "s_mov_b32 s65, 1636352\n"
        "buffer_load_dwordx4 v9, s[44:47], s65 offen offset:2048 lds\n"
        "s_mov_b32 s65, 1668096\n"
        "buffer_load_dwordx4 v9, s[44:47], s65 offen offset:3072 lds\n"
        "ds_read_b64_tr_b16 a[8:9], v10 offset:4096\n"
        "ds_read_b64_tr_b16 a[10:11], v10 offset:5120\n"
        "ds_read_b64_tr_b16 a[12:13], v11 offset:4096\n"
        "ds_read_b64_tr_b16 a[14:15], v11 offset:5120\n"
        "ds_read_b64_tr_b16 a[32:33], v12 offset:4096\n"
        "ds_read_b64_tr_b16 a[34:35], v12 offset:5120\n"
        "ds_read_b64_tr_b16 a[36:37], v13 offset:4096\n"
        "ds_read_b64_tr_b16 a[38:39], v13 offset:5120\n"
        "s_waitcnt vmcnt(8)\n"
        "s_waitcnt lgkmcnt(0)\n"
        "s_barrier\n"
        "s_add_u32 m0, s79, 4096\n"
        "s_mov_b32 s65, 2097152\n"
        "buffer_load_dwordx4 v8, s[44:47], s65 offen lds\n"
        "s_mov_b32 s65, 2128896\n"
        "buffer_load_dwordx4 v8, s[44:47], s65 offen offset:1024 lds\n"
        "s_mov_b32 s65, 2160640\n"
        "buffer_load_dwordx4 v9, s[44:47], s65 offen offset:2048 lds\n"
        "s_mov_b32 s65, 2192384\n"
        "buffer_load_dwordx4 v9, s[44:47], s65 offen offset:3072 lds\n"
        "ds_read_b64_tr_b16 a[56:57], v10 offset:8192\n"
        "ds_read_b64_tr_b16 a[58:59], v10 offset:9216\n"
        "ds_read_b64_tr_b16 a[60:61], v11 offset:8192\n"
        "ds_read_b64_tr_b16 a[62:63], v11 offset:9216\n"
        "ds_read_b64_tr_b16 a[80:81], v12 offset:8192\n"
        "ds_read_b64_tr_b16 a[82:83], v12 offset:9216\n"
        "ds_read_b64_tr_b16 a[84:85], v13 offset:8192\n"
        "ds_read_b64_tr_b16 a[86:87], v13 offset:9216\n"
        "s_waitcnt vmcnt(8)\n"
        "s_waitcnt lgkmcnt(0)\n"
        "s_barrier\n"
        "s_add_u32 m0, s79, 8192\n"
        "s_mov_b32 s65, 2621440\n"
        "buffer_load_dwordx4 v8, s[44:47], s65 offen lds\n"
        "s_mov_b32 s65, 2653184\n"
        "buffer_load_dwordx4 v8, s[44:47], s65 offen offset:1024 lds\n"
        "s_mov_b32 s65, 2684928\n"
        "buffer_load_dwordx4 v9, s[44:47], s65 offen offset:2048 lds\n"
        "s_mov_b32 s65, 2716672\n"
        "buffer_load_dwordx4 v9, s[44:47], s65 offen offset:3072 lds\n"
        "ds_read_b64_tr_b16 a[104:105], v10 offset:12288\n"
        "ds_read_b64_tr_b16 a[106:107], v10 offset:13312\n"
        "ds_read_b64_tr_b16 a[108:109], v11 offset:12288\n"
        "ds_read_b64_tr_b16 a[110:111], v11 offset:13312\n"
        "ds_read_b64_tr_b16 a[128:129], v12 offset:12288\n"
        "ds_read_b64_tr_b16 a[130:131], v12 offset:13312\n"
        "ds_read_b64_tr_b16 a[132:133], v13 offset:12288\n"
        "ds_read_b64_tr_b16 a[134:135], v13 offset:13312\n"
        "s_waitcnt vmcnt(8)\n"
        "s_waitcnt lgkmcnt(0)\n"
        "s_barrier\n"
        "s_add_u32 m0, s79, 12288\n"
        "s_mov_b32 s65, 3145728\n"
        "buffer_load_dwordx4 v8, s[44:47], s65 offen lds\n"
        "s_mov_b32 s65, 3177472\n"
        "buffer_load_dwordx4 v8, s[44:47], s65 offen offset:1024 lds\n"
        "s_mov_b32 s65, 3209216\n"
        "buffer_load_dwordx4 v9, s[44:47], s65 offen offset:2048 lds\n"
        "s_mov_b32 s65, 3240960\n"
        "buffer_load_dwordx4 v9, s[44:47], s65 offen offset:3072 lds\n"
        "ds_read_b64_tr_b16 a[152:153], v10 offset:0\n"
        "ds_read_b64_tr_b16 a[154:155], v10 offset:1024\n"
        "ds_read_b64_tr_b16 a[156:157], v11 offset:0\n"
        "ds_read_b64_tr_b16 a[158:159], v11 offset:1024\n"
        "ds_read_b64_tr_b16 a[176:177], v12 offset:0\n"
        "ds_read_b64_tr_b16 a[178:179], v12 offset:1024\n"
        "ds_read_b64_tr_b16 a[180:181], v13 offset:0\n"
        "ds_read_b64_tr_b16 a[182:183], v13 offset:1024\n"
        "s_waitcnt vmcnt(8)\n"
        "s_waitcnt lgkmcnt(0)\n"
        "s_barrier\n"
        "s_add_u32 m0, s79, 0\n"
        "s_mov_b32 s65, 3670016\n"
        "buffer_load_dwordx4 v8, s[44:47], s65 offen lds\n"
        "s_mov_b32 s65, 3701760\n"
        "buffer_load_dwordx4 v8, s[44:47], s65 offen offset:1024 lds\n"
        "s_mov_b32 s65, 3733504\n"
        "buffer_load_dwordx4 v9, s[44:47], s65 offen offset:2048 lds\n"
        "s_mov_b32 s65, 3765248\n"
        "buffer_load_dwordx4 v9, s[44:47], s65 offen offset:3072 lds\n"
        "ds_read_b64_tr_b16 a[200:201], v10 offset:4096\n"
        "ds_read_b64_tr_b16 a[202:203], v10 offset:5120\n"
        "ds_read_b64_tr_b16 a[204:205], v11 offset:4096\n"
        "ds_read_b64_tr_b16 a[206:207], v11 offset:5120\n"
        "ds_read_b64_tr_b16 a[224:225], v12 offset:4096\n"
        "ds_read_b64_tr_b16 a[226:227], v12 offset:5120\n"
        "ds_read_b64_tr_b16 a[228:229], v13 offset:4096\n"
        "ds_read_b64_tr_b16 a[230:231], v13 offset:5120\n"
        "s_waitcnt vmcnt(8)\n"
        "s_waitcnt lgkmcnt(0)\n"
        "s_barrier\n"
        "s_add_u32 m0, s79, 4096\n"
        "s_mov_b32 s65, 4194304\n"
        "buffer_load_dwordx4 v8, s[44:47], s65 offen lds\n"
        "s_mov_b32 s65, 4226048\n"
        "buffer_load_dwordx4 v8, s[44:47], s65 offen offset:1024 lds\n"
        "s_mov_b32 s65, 4257792\n"
        "buffer_load_dwordx4 v9, s[44:47], s65 offen offset:2048 lds\n"
        "s_mov_b32 s65, 4289536\n"
        "buffer_load_dwordx4 v9, s[44:47], s65 offen offset:3072 lds\n"
        "ds_read_b64_tr_b16 a[248:249], v10 offset:8192\n"
        "ds_read_b64_tr_b16 a[250:251], v10 offset:9216\n"
        "ds_read_b64_tr_b16 a[252:253], v11 offset:8192\n"
        "ds_read_b64_tr_b16 a[254:255], v11 offset:9216\n"
        "ds_read_b64_tr_b16 v[96:97], v12 offset:8192\n"
        "ds_read_b64_tr_b16 v[98:99], v12 offset:9216\n"
        "ds_read_b64_tr_b16 v[100:101], v13 offset:8192\n"
        "ds_read_b64_tr_b16 v[102:103], v13 offset:9216\n"
        "s_waitcnt vmcnt(8)\n"
        "s_waitcnt lgkmcnt(0)\n"
        "s_barrier\n"
        "s_mov_b64 s[44:45], %3\n"
        "s_and_b32 s45, s45, 0xffff\n"
        "s_mov_b32 s46, 4718592\n"
        "s_add_u32 m0, s79, 8192\n"
        "s_mov_b32 s65, 0\n"
        "buffer_load_dwordx4 v8, s[44:47], s65 offen lds\n"
        "s_mov_b32 s65, 31744\n"
        "buffer_load_dwordx4 v8, s[44:47], s65 offen offset:1024 lds\n"
        "s_mov_b32 s65, 63488\n"
        "buffer_load_dwordx4 v9, s[44:47], s65 offen offset:2048 lds\n"
        "s_mov_b32 s65, 95232\n"
        "buffer_load_dwordx4 v9, s[44:47], s65 offen offset:3072 lds\n"
        "ds_read_b64_tr_b16 v[120:121], v10 offset:12288\n"
        "ds_read_b64_tr_b16 v[122:123], v10 offset:13312\n"
        "ds_read_b64_tr_b16 v[124:125], v11 offset:12288\n"
        "ds_read_b64_tr_b16 v[126:127], v11 offset:13312\n"
        "ds_read_b64_tr_b16 v[144:145], v12 offset:12288\n"
        "ds_read_b64_tr_b16 v[146:147], v12 offset:13312\n"
        "ds_read_b64_tr_b16 v[148:149], v13 offset:12288\n"
        "ds_read_b64_tr_b16 v[150:151], v13 offset:13312\n"
        "s_waitcnt vmcnt(8)\n"
        "s_waitcnt lgkmcnt(0)\n"
        "s_barrier\n"
        "s_add_u32 m0, s79, 12288\n"
        "s_mov_b32 s65, 524288\n"
        "buffer_load_dwordx4 v8, s[44:47], s65 offen lds\n"
        "s_mov_b32 s65, 556032\n"
        "buffer_load_dwordx4 v8, s[44:47], s65 offen offset:1024 lds\n"
        "s_mov_b32 s65, 587776\n"
        "buffer_load_dwordx4 v9, s[44:47], s65 offen offset:2048 lds\n"
        "s_mov_b32 s65, 619520\n"
        "buffer_load_dwordx4 v9, s[44:47], s65 offen offset:3072 lds\n"
        "ds_read_b64_tr_b16 v[168:169], v10 offset:0\n"
        "ds_read_b64_tr_b16 v[170:171], v10 offset:1024\n"
        "ds_read_b64_tr_b16 v[172:173], v11 offset:0\n"
        "ds_read_b64_tr_b16 v[174:175], v11 offset:1024\n"
        "ds_read_b64_tr_b16 v[192:193], v12 offset:0\n"
        "ds_read_b64_tr_b16 v[194:195], v12 offset:1024\n"
        "ds_read_b64_tr_b16 v[196:197], v13 offset:0\n"
        "ds_read_b64_tr_b16 v[198:199], v13 offset:1024\n"
        "s_waitcnt vmcnt(8)\n"
        "s_waitcnt lgkmcnt(0)\n"
        "s_barrier\n"
        "s_add_u32 m0, s79, 0\n"
        "s_mov_b32 s65, 1048576\n"
        "buffer_load_dwordx4 v8, s[44:47], s65 offen lds\n"
        "s_mov_b32 s65, 1080320\n"
        "buffer_load_dwordx4 v8, s[44:47], s65 offen offset:1024 lds\n"
        "s_mov_b32 s65, 1112064\n"
        "buffer_load_dwordx4 v9, s[44:47], s65 offen offset:2048 lds\n"
        "s_mov_b32 s65, 1143808\n"
        "buffer_load_dwordx4 v9, s[44:47], s65 offen offset:3072 lds\n"
        "ds_read_b64_tr_b16 v[216:217], v10 offset:4096\n"
        "ds_read_b64_tr_b16 v[218:219], v10 offset:5120\n"
        "ds_read_b64_tr_b16 v[220:221], v11 offset:4096\n"
        "ds_read_b64_tr_b16 v[222:223], v11 offset:5120\n"
        "ds_read_b64_tr_b16 v[240:241], v12 offset:4096\n"
        "ds_read_b64_tr_b16 v[242:243], v12 offset:5120\n"
        "ds_read_b64_tr_b16 v[244:245], v13 offset:4096\n"
        "ds_read_b64_tr_b16 v[246:247], v13 offset:5120\n"
        "s_waitcnt vmcnt(8)\n"
        "s_waitcnt lgkmcnt(0)\n"
        "s_barrier\n"
        "s_add_u32 m0, s79, 4096\n"
        "s_mov_b32 s65, 1572864\n"
        "buffer_load_dwordx4 v8, s[44:47], s65 offen lds\n"
        "s_mov_b32 s65, 1604608\n"
        "buffer_load_dwordx4 v8, s[44:47], s65 offen offset:1024 lds\n"
        "s_mov_b32 s65, 1636352\n"
        "buffer_load_dwordx4 v9, s[44:47], s65 offen offset:2048 lds\n"
        "s_mov_b32 s65, 1668096\n"
        "buffer_load_dwordx4 v9, s[44:47], s65 offen offset:3072 lds\n"
        "ds_read_b64_tr_b16 a[16:17], v10 offset:8192\n"
        "ds_read_b64_tr_b16 a[18:19], v10 offset:9216\n"
        "ds_read_b64_tr_b16 a[20:21], v11 offset:8192\n"
        "ds_read_b64_tr_b16 a[22:23], v11 offset:9216\n"
        "ds_read_b64_tr_b16 a[40:41], v12 offset:8192\n"
        "ds_read_b64_tr_b16 a[42:43], v12 offset:9216\n"
        "ds_read_b64_tr_b16 a[44:45], v13 offset:8192\n"
        "ds_read_b64_tr_b16 a[46:47], v13 offset:9216\n"
        "s_waitcnt vmcnt(8)\n"
        "s_waitcnt lgkmcnt(0)\n"
        "s_barrier\n"
        "s_add_u32 m0, s79, 8192\n"
        "s_mov_b32 s65, 2097152\n"
        "buffer_load_dwordx4 v8, s[44:47], s65 offen lds\n"
        "s_mov_b32 s65, 2128896\n"
        "buffer_load_dwordx4 v8, s[44:47], s65 offen offset:1024 lds\n"
        "s_mov_b32 s65, 2160640\n"
        "buffer_load_dwordx4 v9, s[44:47], s65 offen offset:2048 lds\n"
        "s_mov_b32 s65, 2192384\n"
        "buffer_load_dwordx4 v9, s[44:47], s65 offen offset:3072 lds\n"
        "ds_read_b64_tr_b16 a[64:65], v10 offset:12288\n"
        "ds_read_b64_tr_b16 a[66:67], v10 offset:13312\n"
        "ds_read_b64_tr_b16 a[68:69], v11 offset:12288\n"
        "ds_read_b64_tr_b16 a[70:71], v11 offset:13312\n"
        "ds_read_b64_tr_b16 a[88:89], v12 offset:12288\n"
        "ds_read_b64_tr_b16 a[90:91], v12 offset:13312\n"
        "ds_read_b64_tr_b16 a[92:93], v13 offset:12288\n"
        "ds_read_b64_tr_b16 a[94:95], v13 offset:13312\n"
        "s_waitcnt vmcnt(8)\n"
        "s_waitcnt lgkmcnt(0)\n"
        "s_barrier\n"
        "s_add_u32 m0, s79, 12288\n"
        "s_mov_b32 s65, 2621440\n"
        "buffer_load_dwordx4 v8, s[44:47], s65 offen lds\n"
        "s_mov_b32 s65, 2653184\n"
        "buffer_load_dwordx4 v8, s[44:47], s65 offen offset:1024 lds\n"
        "s_mov_b32 s65, 2684928\n"
        "buffer_load_dwordx4 v9, s[44:47], s65 offen offset:2048 lds\n"
        "s_mov_b32 s65, 2716672\n"
        "buffer_load_dwordx4 v9, s[44:47], s65 offen offset:3072 lds\n"
        "ds_read_b64_tr_b16 a[112:113], v10 offset:0\n"
        "ds_read_b64_tr_b16 a[114:115], v10 offset:1024\n"
        "ds_read_b64_tr_b16 a[116:117], v11 offset:0\n"
        "ds_read_b64_tr_b16 a[118:119], v11 offset:1024\n"
        "ds_read_b64_tr_b16 a[136:137], v12 offset:0\n"
        "ds_read_b64_tr_b16 a[138:139], v12 offset:1024\n"
        "ds_read_b64_tr_b16 a[140:141], v13 offset:0\n"
        "ds_read_b64_tr_b16 a[142:143], v13 offset:1024\n"
        "s_waitcnt vmcnt(8)\n"
        "s_waitcnt lgkmcnt(0)\n"
        "s_barrier\n"
        "s_add_u32 m0, s79, 0\n"
        "s_mov_b32 s65, 3145728\n"
        "buffer_load_dwordx4 v8, s[44:47], s65 offen lds\n"
        "s_mov_b32 s65, 3177472\n"
        "buffer_load_dwordx4 v8, s[44:47], s65 offen offset:1024 lds\n"
        "s_mov_b32 s65, 3209216\n"
        "buffer_load_dwordx4 v9, s[44:47], s65 offen offset:2048 lds\n"
        "s_mov_b32 s65, 3240960\n"
        "buffer_load_dwordx4 v9, s[44:47], s65 offen offset:3072 lds\n"
        "ds_read_b64_tr_b16 a[160:161], v10 offset:4096\n"
        "ds_read_b64_tr_b16 a[162:163], v10 offset:5120\n"
        "ds_read_b64_tr_b16 a[164:165], v11 offset:4096\n"
        "ds_read_b64_tr_b16 a[166:167], v11 offset:5120\n"
        "ds_read_b64_tr_b16 a[184:185], v12 offset:4096\n"
        "ds_read_b64_tr_b16 a[186:187], v12 offset:5120\n"
        "ds_read_b64_tr_b16 a[188:189], v13 offset:4096\n"
        "ds_read_b64_tr_b16 a[190:191], v13 offset:5120\n"
        "s_waitcnt vmcnt(8)\n"
        "s_waitcnt lgkmcnt(0)\n"
        "s_barrier\n"
        "s_add_u32 m0, s79, 4096\n"
        "s_mov_b32 s65, 3670016\n"
        "buffer_load_dwordx4 v8, s[44:47], s65 offen lds\n"
        "s_mov_b32 s65, 3701760\n"
        "buffer_load_dwordx4 v8, s[44:47], s65 offen offset:1024 lds\n"
        "s_mov_b32 s65, 3733504\n"
        "buffer_load_dwordx4 v9, s[44:47], s65 offen offset:2048 lds\n"
        "s_mov_b32 s65, 3765248\n"
        "buffer_load_dwordx4 v9, s[44:47], s65 offen offset:3072 lds\n"
        "ds_read_b64_tr_b16 a[208:209], v10 offset:8192\n"
        "ds_read_b64_tr_b16 a[210:211], v10 offset:9216\n"
        "ds_read_b64_tr_b16 a[212:213], v11 offset:8192\n"
        "ds_read_b64_tr_b16 a[214:215], v11 offset:9216\n"
        "ds_read_b64_tr_b16 a[232:233], v12 offset:8192\n"
        "ds_read_b64_tr_b16 a[234:235], v12 offset:9216\n"
        "ds_read_b64_tr_b16 a[236:237], v13 offset:8192\n"
        "ds_read_b64_tr_b16 a[238:239], v13 offset:9216\n"
        "s_waitcnt vmcnt(8)\n"
        "s_waitcnt lgkmcnt(0)\n"
        "s_barrier\n"
        "s_add_u32 m0, s79, 8192\n"
        "s_mov_b32 s65, 4194304\n"
        "buffer_load_dwordx4 v8, s[44:47], s65 offen lds\n"
        "s_mov_b32 s65, 4226048\n"
        "buffer_load_dwordx4 v8, s[44:47], s65 offen offset:1024 lds\n"
        "s_mov_b32 s65, 4257792\n"
        "buffer_load_dwordx4 v9, s[44:47], s65 offen offset:2048 lds\n"
        "s_mov_b32 s65, 4289536\n"
        "buffer_load_dwordx4 v9, s[44:47], s65 offen offset:3072 lds\n"
        "ds_read_b64_tr_b16 v[80:81], v10 offset:12288\n"
        "ds_read_b64_tr_b16 v[82:83], v10 offset:13312\n"
        "ds_read_b64_tr_b16 v[84:85], v11 offset:12288\n"
        "ds_read_b64_tr_b16 v[86:87], v11 offset:13312\n"
        "ds_read_b64_tr_b16 v[104:105], v12 offset:12288\n"
        "ds_read_b64_tr_b16 v[106:107], v12 offset:13312\n"
        "ds_read_b64_tr_b16 v[108:109], v13 offset:12288\n"
        "ds_read_b64_tr_b16 v[110:111], v13 offset:13312\n"
        "s_waitcnt vmcnt(8)\n"
        "s_waitcnt lgkmcnt(0)\n"
        "s_barrier\n"
        "ds_read_b64_tr_b16 v[128:129], v10 offset:0\n"
        "ds_read_b64_tr_b16 v[130:131], v10 offset:1024\n"
        "ds_read_b64_tr_b16 v[132:133], v11 offset:0\n"
        "ds_read_b64_tr_b16 v[134:135], v11 offset:1024\n"
        "ds_read_b64_tr_b16 v[152:153], v12 offset:0\n"
        "ds_read_b64_tr_b16 v[154:155], v12 offset:1024\n"
        "ds_read_b64_tr_b16 v[156:157], v13 offset:0\n"
        "ds_read_b64_tr_b16 v[158:159], v13 offset:1024\n"
        "s_waitcnt vmcnt(4)\n"
        "s_waitcnt lgkmcnt(0)\n"
        "s_barrier\n"
        "ds_read_b64_tr_b16 v[176:177], v10 offset:4096\n"
        "ds_read_b64_tr_b16 v[178:179], v10 offset:5120\n"
        "ds_read_b64_tr_b16 v[180:181], v11 offset:4096\n"
        "ds_read_b64_tr_b16 v[182:183], v11 offset:5120\n"
        "ds_read_b64_tr_b16 v[200:201], v12 offset:4096\n"
        "ds_read_b64_tr_b16 v[202:203], v12 offset:5120\n"
        "ds_read_b64_tr_b16 v[204:205], v13 offset:4096\n"
        "ds_read_b64_tr_b16 v[206:207], v13 offset:5120\n"
        "s_waitcnt vmcnt(0)\n"
        "s_waitcnt lgkmcnt(0)\n"
        "s_barrier\n"
        "ds_read_b64_tr_b16 v[224:225], v10 offset:8192\n"
        "ds_read_b64_tr_b16 v[226:227], v10 offset:9216\n"
        "ds_read_b64_tr_b16 v[228:229], v11 offset:8192\n"
        "ds_read_b64_tr_b16 v[230:231], v11 offset:9216\n"
        "ds_read_b64_tr_b16 v[248:249], v12 offset:8192\n"
        "ds_read_b64_tr_b16 v[250:251], v12 offset:9216\n"
        "ds_read_b64_tr_b16 v[252:253], v13 offset:8192\n"
        "ds_read_b64_tr_b16 v[254:255], v13 offset:9216\n"
        "s_waitcnt lgkmcnt(0)\n"
        "s_getpc_b64 s[44:45]\n"
        ".Ls1_pc_%=:\n"
        "s_add_u32 s44, s44, .Ls1_code_%=-.Ls1_pc_%=\n"
        "s_addc_u32 s45, s45, 0\n"
        "s_and_b32 s45, s45, 0xffff\n"
        "s_and_b32 s44, s44, -4\n"
        "s_mov_b32 s46, .Ls1_end_%=-.Ls1_code_%=\n"
        "s_mov_b32 s47, 0x20000\n"
        "s_lshl_b32 s65, s48, 10\n"
        "buffer_load_dwordx4 v[24:27], v1, s[44:47], s65 offen\n"
        "s_add_u32 s65, s65, 4096\n"
        "buffer_load_dwordx4 v[28:31], v1, s[44:47], s65 offen\n"
        "s_add_u32 s65, s65, 4096\n"
        "buffer_load_dwordx4 v[32:35], v1, s[44:47], s65 offen\n"
        "s_add_u32 s65, s65, 4096\n"
        "buffer_load_dwordx4 v[36:39], v1, s[44:47], s65 offen\n"
        "s_add_u32 s65, s65, 4096\n"
        "buffer_load_dwordx4 v[40:43], v1, s[44:47], s65 offen\n"
        "s_add_u32 s65, s65, 4096\n"
        "v_lshrrev_b32 v20, 4, v1\n"
        "s_mov_b32 s50, s56\n"
        "v_lshlrev_b32 v22, 7, v20\n"
        "s_sub_u32 s66, 0, s50\n"
        "s_mov_b32 s52, 32768\n"
        "s_mov_b32 s64, 0\n"
        ".Ls1_pg0_top_%=:\n"
        "buffer_load_dword v21, v22, s[40:43], s52 offen sc1\n"
        "s_waitcnt vmcnt(0)\n"
        "v_cmp_gt_i32 vcc, 0, v21\n"
        "s_cbranch_vccz .Ls1_pg0_ok_%=\n"
        "s_add_u32 s64, s64, 1\n"
        "s_and_b32 s65, s64, 255\n"
        "s_cbranch_scc1 .Ls1_pg0_go_%=\n"
        "s_mov_b32 s65, 49152\n"
        "s_cmp_lt_u32 s64, 1048576\n"
        "s_cbranch_scc1 .Ls1_pg0_chk_%=\n"
        "v_mov_b32 v21, 1\n"
        "buffer_store_dword v21, off, s[40:43], s65 sc1\n"
        "s_waitcnt vmcnt(0)\n"
        ".Ls1_pg0_chk_%=:\n"
        "buffer_load_dword v21, off, s[40:43], s65 sc1\n"
        "s_waitcnt vmcnt(0)\n"
        "v_readfirstlane_b32 s65, v21\n"
        "s_nop 0\n"
        "s_cmp_lg_u32 s65, 1\n"
        "s_cbranch_scc1 .Ls1_pg0_go_%=\n"
        "s_endpgm\n"
        ".Ls1_pg0_go_%=:\n"
        "s_sleep 2\n"
        "s_branch .Ls1_pg0_top_%=\n"
        ".Ls1_pg0_ok_%=:\n"
        "v_cmp_eq_u32 s[52:53], 1, v21\n"
        "s_nop 1\n"
        "s_mov_b32 s54, 40960\n"
        "s_mov_b32 s64, 0\n"
        ".Ls1_pg1_top_%=:\n"
        "buffer_load_dword v21, v22, s[40:43], s54 offen sc1\n"
        "s_waitcnt vmcnt(0)\n"
        "v_cmp_gt_i32 vcc, 0, v21\n"
        "s_cbranch_vccz .Ls1_pg1_ok_%=\n"
        "s_add_u32 s64, s64, 1\n"
        "s_and_b32 s65, s64, 255\n"
        "s_cbranch_scc1 .Ls1_pg1_go_%=\n"
        "s_mov_b32 s65, 49152\n"
        "s_cmp_lt_u32 s64, 1048576\n"
        "s_cbranch_scc1 .Ls1_pg1_chk_%=\n"
        "v_mov_b32 v21, 1\n"
        "buffer_store_dword v21, off, s[40:43], s65 sc1\n"
        "s_waitcnt vmcnt(0)\n"
        ".Ls1_pg1_chk_%=:\n"
        "buffer_load_dword v21, off, s[40:43], s65 sc1\n"
        "s_waitcnt vmcnt(0)\n"
        "v_readfirstlane_b32 s65, v21\n"
        "s_nop 0\n"
        "s_cmp_lg_u32 s65, 1\n"
        "s_cbranch_scc1 .Ls1_pg1_go_%=\n"
        "s_endpgm\n"
        ".Ls1_pg1_go_%=:\n"
        "s_sleep 2\n"
        "s_branch .Ls1_pg1_top_%=\n"
        ".Ls1_pg1_ok_%=:\n"
        "v_cmp_eq_u32 s[54:55], 1, v21\n"
        "s_nop 1\n"
        "s_mov_b32 vcc_lo, 0x55555555\n"
        "s_mov_b32 vcc_hi, 0x55555555\n"
        "s_xor_b64 vcc, vcc, s[52:53]\n"
        "s_bcnt1_i32_b64 s65, vcc\n"
        "s_sub_u32 s64, s65, 23\n"
        "s_cmp_lt_u32 s64, 19\n"
        "s_cbranch_scc1 .Ls1_rawmap_%=\n"
        "s_cmp_lt_u32 s65, 32\n"
        "s_cselect_b32 s65, 0, -1\n"
        "s_xor_b32 s52, s65, 0x55555555\n"
        "s_xor_b32 s53, s65, 0x55555555\n"
        "s_xor_b32 s54, s65, 0x55555555\n"
        "s_xor_b32 s55, s65, 0xaaaaaaaa\n"
        ".Ls1_rawmap_%=:\n"
        "s_xor_b32 s52, s52, s66\n"
        "s_xor_b32 s53, s53, s66\n"
        "s_xor_b32 s54, s54, s66\n"
        "s_xor_b32 s55, s55, s66\n"
        "s_and_b32 s76, s55, 1\n"
        "s_lshl_b32 s76, s76, 13\n"
        "s_add_u32 s60, s60, s76\n"
        "s_barrier\n"
        "s_mov_b32 s68, -1\n"
        "s_mov_b32 s69, 0\n"
        "s_mov_b32 s78, 0\n"
        "s_mov_b32 s74, -1\n"
        "s_mov_b32 s86, s76\n"
        "s_lshr_b32 s85, s61, 16\n"
        "s_and_b32 s61, s61, 0xffff\n"
        "s_mov_b32 s89, 241172480\n"
        "s_mov_b32 s84, 241172480\n"
        "v_mov_b32 v6, v0\n"
        "s_mov_b32 s80, 0xbfb8aa3b\n"
        "s_lshr_b32 s67, s69, 1\n"
        "s_bitcmp1_b64 s[52:53], s67\n"
        "s_cselect_b32 s65, 8192, 0\n"
        "s_lshl_b32 s77, s67, 21\n"
        "s_add_u32 s77, s77, s65\n"
        "s_add_u32 s71, s77, s58\n"
        "s_add_u32 s81, s77, s62\n"
        "s_add_u32 s78, s78, 18432\n"
        "s_cmp_eq_u32 s78, 55296\n"
        "s_cselect_b32 s78, 0, s78\n"
        "s_add_u32 m0, s78, s61\n"
        "s_lshr_b32 s70, s69, 1\n"
        "s_branch .Ls1_wait1_%=\n"
        ".p2align 6\n"
        ".p2align 5\n"
        ".Ls1_code_%=:\n"
        ".Ls1_i0_%=:\n"
        "v_mfma_f32_16x16x32_bf16 v[24:27], a[0:3], v[8:11], 0\n"
        "s_add_u32 s86, s76, 512\n"
        "buffer_load_dword v4, v2, s[40:43], s86 offen sc1\n"
        "v_mfma_f32_16x16x32_bf16 v[28:31], a[4:7], v[8:11], 0\n"
        "s_add_u32 s71, s77, s58\n"
        "v_cvt_pk_bf16_f32 v48, v48, v49\n"
        "v_cvt_pk_bf16_f32 v49, v50, v51\n"
        "v_mfma_f32_16x16x32_bf16 v[32:35], v[8:11], a[8:11], 0\n"
        "s_add_u32 s71, s71, 32768\n"
        "v_cvt_pk_bf16_f32 v50, v52, v53\n"
        "v_cvt_pk_bf16_f32 v51, v54, v55\n"
        "v_mfma_f32_16x16x32_bf16 v[36:39], v[8:11], a[12:15], 0\n"
        "s_add_u32 s81, s77, s62\n"
        "s_add_u32 s66, s72, 524288\n"
        "v_mfma_f32_16x16x32_bf16 v[40:43], a[16:19], v[8:11], 0\n"
        "s_add_u32 s81, s81, 2048\n"
        "buffer_store_dwordx4 v[48:51], v1, s[40:43], s66 offen offset:1024 sc1\n"
        "v_mfma_f32_16x16x32_bf16 v[44:47], a[20:23], v[8:11], 0\n"
        "s_waitcnt lgkmcnt(1)\n"
        "ds_read_b128 v[8:11], v0 offset:1536\n"
        "v_cvt_pk_bf16_f32 v74, v56, v57\n"
        "v_mfma_f32_16x16x32_bf16 v[24:27], a[24:27], v[12:15], v[24:27]\n"
        "s_add_u32 s78, s78, 18432\n"
        "v_cvt_pk_bf16_f32 v75, v58, v59\n"
        "s_add_u32 s66, s72, 1048576\n"
        "v_mfma_f32_16x16x32_bf16 v[28:31], a[28:31], v[12:15], v[28:31]\n"
        "s_cmp_eq_u32 s78, 55296\n"
        "s_cselect_b32 s78, 0, s78\n"
        "v_mfma_f32_16x16x32_bf16 v[32:35], v[12:15], a[32:35], v[32:35]\n"
        "s_add_u32 m0, s78, s61\n"
        "v_mfma_f32_16x16x32_bf16 v[36:39], v[12:15], a[36:39], v[36:39]\n"
        "s_lshr_b32 s70, s69, 1\n"
        "v_mfma_f32_16x16x32_bf16 v[40:43], a[40:43], v[12:15], v[40:43]\n"
        "v_mfma_f32_16x16x32_bf16 v[44:47], a[44:47], v[12:15], v[44:47]\n"
        "s_waitcnt lgkmcnt(1)\n"
        "ds_read_b128 v[12:15], v0 offset:2048\n"
        "v_mfma_f32_16x16x32_bf16 v[24:27], a[48:51], v[16:19], v[24:27]\n"
        "buffer_store_dwordx4 v[72:75], v1, s[40:43], s66 offen offset:0 sc1\n"
        "v_mfma_f32_16x16x32_bf16 v[28:31], a[52:55], v[16:19], v[28:31]\n"
        "v_cvt_pk_bf16_f32 v78, v60, v61\n"
        "v_cvt_pk_bf16_f32 v79, v62, v63\n"
        "v_mfma_f32_16x16x32_bf16 v[32:35], v[16:19], a[56:59], v[32:35]\n"
        "s_waitcnt vmcnt(3)\n"
        "v_mfma_f32_16x16x32_bf16 v[36:39], v[16:19], a[60:63], v[36:39]\n"
        "buffer_store_dword v5, v3, s[40:43], s84 offen sc1\n"
        "v_mfma_f32_16x16x32_bf16 v[40:43], a[64:67], v[16:19], v[40:43]\n"
        "v_mfma_f32_16x16x32_bf16 v[44:47], a[68:71], v[16:19], v[44:47]\n"
        "s_waitcnt lgkmcnt(1)\n"
        "ds_read_b128 v[16:19], v0 offset:2560\n"
        "v_mfma_f32_16x16x32_bf16 v[24:27], a[72:75], v[8:11], v[24:27]\n"
        "v_mfma_f32_16x16x32_bf16 v[28:31], a[76:79], v[8:11], v[28:31]\n"
        "buffer_store_dwordx4 v[76:79], v1, s[40:43], s66 offen offset:1024 sc1\n"
        "v_mfma_f32_16x16x32_bf16 v[32:35], v[8:11], a[80:83], v[32:35]\n"
        "v_cvt_pk_bf16_f32 v64, v64, v65\n"
        "v_cvt_pk_bf16_f32 v65, v66, v67\n"
        "v_mfma_f32_16x16x32_bf16 v[36:39], v[8:11], a[84:87], v[36:39]\n"
        "v_cvt_pk_bf16_f32 v66, v68, v69\n"
        "v_cvt_pk_bf16_f32 v67, v70, v71\n"
        "v_mfma_f32_16x16x32_bf16 v[40:43], a[88:91], v[8:11], v[40:43]\n"
        "v_mfma_f32_16x16x32_bf16 v[44:47], a[92:95], v[8:11], v[44:47]\n"
        "s_waitcnt lgkmcnt(1)\n"
        "ds_read_b128 v[8:11], v0 offset:3072\n"
        "v_mfma_f32_16x16x32_bf16 v[24:27], a[96:99], v[12:15], v[24:27]\n"
        "v_mfma_f32_16x16x32_bf16 v[28:31], a[100:103], v[12:15], v[28:31]\n"
        "v_mfma_f32_16x16x32_bf16 v[32:35], v[12:15], a[104:107], v[32:35]\n"
        "buffer_store_dwordx4 v[64:67], v1, s[40:43], s72 offen offset:1024 sc1\n"
        "v_mfma_f32_16x16x32_bf16 v[36:39], v[12:15], a[108:111], v[36:39]\n"
        "v_mov_b32 v5, s74\n"
        "v_mfma_f32_16x16x32_bf16 v[40:43], a[112:115], v[12:15], v[40:43]\n"
        "s_waitcnt vmcnt(5)\n"
        "v_cmp_gt_i32 vcc, s70, v4\n"
        "v_mfma_f32_16x16x32_bf16 v[44:47], a[116:119], v[12:15], v[44:47]\n"
        "s_waitcnt lgkmcnt(1)\n"
        "ds_read_b128 v[12:15], v0 offset:3584\n"
        "v_mfma_f32_16x16x32_bf16 v[24:27], a[120:123], v[16:19], v[24:27]\n"
        "s_and_b64 vcc, vcc, 1\n"
        "v_mfma_f32_16x16x32_bf16 v[28:31], a[124:127], v[16:19], v[28:31]\n"
        "s_cbranch_vccnz .Ls1_i00_%=\n"
        "v_mfma_f32_16x16x32_bf16 v[32:35], v[16:19], a[128:131], v[32:35]\n"
        "buffer_load_dwordx4 v1, s[40:43], s71 offen offset:0 sc1 lds\n"
        "v_mfma_f32_16x16x32_bf16 v[36:39], v[16:19], a[132:135], v[36:39]\n"
        "v_mfma_f32_16x16x32_bf16 v[40:43], a[136:139], v[16:19], v[40:43]\n"
        "v_mfma_f32_16x16x32_bf16 v[44:47], a[140:143], v[16:19], v[44:47]\n"
        "s_waitcnt lgkmcnt(1)\n"
        "ds_read_b128 v[16:19], v0 offset:4096\n"
        "v_mfma_f32_16x16x32_bf16 v[24:27], a[144:147], v[8:11], v[24:27]\n"
        "buffer_load_dwordx4 v1, s[40:43], s71 offen offset:1024 sc1 lds\n"
        "v_mfma_f32_16x16x32_bf16 v[28:31], a[148:151], v[8:11], v[28:31]\n"
        "v_mfma_f32_16x16x32_bf16 v[32:35], v[8:11], a[152:155], v[32:35]\n"
        "v_mfma_f32_16x16x32_bf16 v[36:39], v[8:11], a[156:159], v[36:39]\n"
        "v_mfma_f32_16x16x32_bf16 v[40:43], a[160:163], v[8:11], v[40:43]\n"
        "buffer_load_dwordx4 v1, s[40:43], s71 offen offset:2048 sc1 lds\n"
        "v_mfma_f32_16x16x32_bf16 v[44:47], a[164:167], v[8:11], v[44:47]\n"
        "s_waitcnt lgkmcnt(1)\n"
        "ds_read_b128 v[8:11], v0 offset:4608\n"
        "v_mfma_f32_16x16x32_bf16 v[24:27], a[168:171], v[12:15], v[24:27]\n"
        "v_mfma_f32_16x16x32_bf16 v[28:31], a[172:175], v[12:15], v[28:31]\n"
        "v_mfma_f32_16x16x32_bf16 v[32:35], v[12:15], a[176:179], v[32:35]\n"
        "buffer_load_dwordx4 v1, s[40:43], s71 offen offset:3072 sc1 lds\n"
        "v_mfma_f32_16x16x32_bf16 v[36:39], v[12:15], a[180:183], v[36:39]\n"
        "s_add_u32 m0, s78, s85\n"
        "s_add_u32 s69, s69, 1\n"
        "v_mfma_f32_16x16x32_bf16 v[40:43], a[184:187], v[12:15], v[40:43]\n"
        "v_mfma_f32_16x16x32_bf16 v[44:47], a[188:191], v[12:15], v[44:47]\n"
        "s_waitcnt lgkmcnt(1)\n"
        "ds_read_b128 v[12:15], v0 offset:5120\n"
        "v_mfma_f32_16x16x32_bf16 v[24:27], a[192:195], v[16:19], v[24:27]\n"
        "buffer_load_dwordx4 v1, s[40:43], s81 offen offset:0 sc1 lds\n"
        "v_mfma_f32_16x16x32_bf16 v[28:31], a[196:199], v[16:19], v[28:31]\n"
        "v_mfma_f32_16x16x32_bf16 v[32:35], v[16:19], a[200:203], v[32:35]\n"
        "s_getreg_b32 s88, hwreg(HW_REG_IB_STS, 0, 4)\n"
        "v_mfma_f32_16x16x32_bf16 v[36:39], v[16:19], a[204:207], v[36:39]\n"
        "s_cmp_le_u32 s88, 5\n"
        "s_cselect_b32 s88, s73, s89\n"
        "s_cselect_b32 s73, s89, s73\n"
        "v_mfma_f32_16x16x32_bf16 v[40:43], a[208:211], v[16:19], v[40:43]\n"
        "buffer_store_dword v5, v3, s[40:43], s88 offen sc1\n"
        "v_mfma_f32_16x16x32_bf16 v[44:47], a[212:215], v[16:19], v[44:47]\n"
        "s_waitcnt lgkmcnt(1)\n"
        "ds_read_b128 v[16:19], v0 offset:5632\n"
        "v_mfma_f32_16x16x32_bf16 v[24:27], a[216:219], v[8:11], v[24:27]\n"
        "v_mfma_f32_16x16x32_bf16 v[28:31], a[220:223], v[8:11], v[28:31]\n"
        "v_mfma_f32_16x16x32_bf16 v[32:35], v[8:11], a[224:227], v[32:35]\n"
        "v_mfma_f32_16x16x32_bf16 v[36:39], v[8:11], a[228:231], v[36:39]\n"
        "v_mfma_f32_16x16x32_bf16 v[40:43], a[232:235], v[8:11], v[40:43]\n"
        "s_getreg_b32 s88, hwreg(HW_REG_IB_STS, 0, 4)\n"
        "v_mfma_f32_16x16x32_bf16 v[44:47], a[236:239], v[8:11], v[44:47]\n"
        "s_waitcnt lgkmcnt(1)\n"
        "ds_read_b128 v[8:11], v0 offset:6144\n"
        "v_mfma_f32_16x16x32_bf16 v[24:27], a[240:243], v[12:15], v[24:27]\n"
        "s_cmp_le_u32 s88, 6\n"
        "s_cselect_b32 s88, s73, s89\n"
        "s_cselect_b32 s73, s89, s73\n"
        "v_mfma_f32_16x16x32_bf16 v[28:31], a[244:247], v[12:15], v[28:31]\n"
        "buffer_store_dword v5, v3, s[40:43], s88 offen sc1\n"
        "v_mfma_f32_16x16x32_bf16 v[32:35], v[12:15], a[248:251], v[32:35]\n"
        "v_mfma_f32_16x16x32_bf16 v[36:39], v[12:15], a[252:255], v[36:39]\n"
        "v_mfma_f32_16x16x32_bf16 v[40:43], v[80:83], v[12:15], v[40:43]\n"
        "v_mfma_f32_16x16x32_bf16 v[44:47], v[84:87], v[12:15], v[44:47]\n"
        "s_waitcnt lgkmcnt(1)\n"
        "ds_read_b128 v[12:15], v0 offset:6656\n"
        "v_mfma_f32_16x16x32_bf16 v[24:27], v[88:91], v[16:19], v[24:27]\n"
        "s_getreg_b32 s88, hwreg(HW_REG_IB_STS, 0, 4)\n"
        "v_mfma_f32_16x16x32_bf16 v[28:31], v[92:95], v[16:19], v[28:31]\n"
        "s_cmp_le_u32 s88, 7\n"
        "s_cselect_b32 s88, s73, s89\n"
        "s_cselect_b32 s73, s89, s73\n"
        "v_mfma_f32_16x16x32_bf16 v[32:35], v[16:19], v[96:99], v[32:35]\n"
        "buffer_store_dword v5, v3, s[40:43], s88 offen sc1\n"
        "v_mfma_f32_16x16x32_bf16 v[36:39], v[16:19], v[100:103], v[36:39]\n"
        "v_mfma_f32_16x16x32_bf16 v[40:43], v[104:107], v[16:19], v[40:43]\n"
        "v_mfma_f32_16x16x32_bf16 v[44:47], v[108:111], v[16:19], v[44:47]\n"
        "s_waitcnt lgkmcnt(1)\n"
        "ds_read_b128 v[16:19], v0 offset:7168\n"
        "v_mfma_f32_16x16x32_bf16 v[24:27], v[112:115], v[8:11], v[24:27]\n"
        "v_mfma_f32_16x16x32_bf16 v[28:31], v[116:119], v[8:11], v[28:31]\n"
        "v_mfma_f32_16x16x32_bf16 v[32:35], v[8:11], v[120:123], v[32:35]\n"
        "v_mfma_f32_16x16x32_bf16 v[36:39], v[8:11], v[124:127], v[36:39]\n"
        "v_mfma_f32_16x16x32_bf16 v[40:43], v[128:131], v[8:11], v[40:43]\n"
        "v_mfma_f32_16x16x32_bf16 v[44:47], v[132:135], v[8:11], v[44:47]\n"
        "s_waitcnt lgkmcnt(1)\n"
        "ds_read_b128 v[8:11], v0 offset:7680\n"
        "v_mfma_f32_16x16x32_bf16 v[24:27], v[136:139], v[12:15], v[24:27]\n"
        "v_mfma_f32_16x16x32_bf16 v[28:31], v[140:143], v[12:15], v[28:31]\n"
        "v_mfma_f32_16x16x32_bf16 v[32:35], v[12:15], v[144:147], v[32:35]\n"
        "s_waitcnt vmcnt(8)\n"
        "buffer_store_dword v5, v3, s[40:43], s73 offen sc1\n"
        "v_mfma_f32_16x16x32_bf16 v[36:39], v[12:15], v[148:151], v[36:39]\n"
        "s_mov_b32 s84, s89\n"
        "s_mov_b32 s73, s60\n"
        "s_add_u32 s74, s74, 1\n"
        "v_mfma_f32_16x16x32_bf16 v[40:43], v[152:155], v[12:15], v[40:43]\n"
        "s_add_u32 s68, s68, 1\n"
        "s_bitcmp1_b64 s[52:53], s68\n"
        "s_cselect_b32 s66, 8192, 0\n"
        "v_mfma_f32_16x16x32_bf16 v[44:47], v[156:159], v[12:15], v[44:47]\n"
        "s_waitcnt lgkmcnt(1)\n"
        "ds_read_b128 v[12:15], v0 offset:8192\n"
        "s_lshl_b32 s75, s68, 21\n"
        "v_mfma_f32_16x16x32_bf16 v[24:27], v[160:163], v[16:19], v[24:27]\n"
        "s_add_u32 s75, s75, s66\n"
        "s_add_u32 s72, s75, s59\n"
        "v_mfma_f32_16x16x32_bf16 v[28:31], v[164:167], v[16:19], v[28:31]\n"
        "v_mfma_f32_16x16x32_bf16 v[32:35], v[16:19], v[168:171], v[32:35]\n"
        "v_mfma_f32_16x16x32_bf16 v[36:39], v[16:19], v[172:175], v[36:39]\n"
        "v_mfma_f32_16x16x32_bf16 v[40:43], v[176:179], v[16:19], v[40:43]\n"
        "v_mfma_f32_16x16x32_bf16 v[44:47], v[180:183], v[16:19], v[44:47]\n"
        "s_waitcnt lgkmcnt(1)\n"
        "ds_read_b128 v[16:19], v0 offset:8704\n"
        "v_mfma_f32_16x16x32_bf16 v[24:27], v[184:187], v[8:11], v[24:27]\n"
        "v_mfma_f32_16x16x32_bf16 v[28:31], v[188:191], v[8:11], v[28:31]\n"
        "v_mfma_f32_16x16x32_bf16 v[32:35], v[8:11], v[192:195], v[32:35]\n"
        "v_mfma_f32_16x16x32_bf16 v[36:39], v[8:11], v[196:199], v[36:39]\n"
        "v_mfma_f32_16x16x32_bf16 v[40:43], v[200:203], v[8:11], v[40:43]\n"
        "s_waitcnt vmcnt(4)\n"
        "s_barrier\n"
        "v_add_u32 v0, s78, v6\n"
        "v_mfma_f32_16x16x32_bf16 v[44:47], v[204:207], v[8:11], v[44:47]\n"
        "s_waitcnt lgkmcnt(1)\n"
        "ds_read_b128 v[8:11], v0 offset:0\n"
        "v_mfma_f32_16x16x32_bf16 v[24:27], v[208:211], v[12:15], v[24:27]\n"
        "v_mfma_f32_16x16x32_bf16 v[28:31], v[212:215], v[12:15], v[28:31]\n"
        "v_mfma_f32_16x16x32_bf16 v[32:35], v[12:15], v[216:219], v[32:35]\n"
        "v_mfma_f32_16x16x32_bf16 v[36:39], v[12:15], v[220:223], v[36:39]\n"
        "v_mfma_f32_16x16x32_bf16 v[40:43], v[224:227], v[12:15], v[40:43]\n"
        "v_mfma_f32_16x16x32_bf16 v[44:47], v[228:231], v[12:15], v[44:47]\n"
        "s_waitcnt lgkmcnt(1)\n"
        "ds_read_b128 v[12:15], v0 offset:512\n"
        "v_mfma_f32_16x16x32_bf16 v[24:27], v[232:235], v[16:19], v[24:27]\n"
        "v_mfma_f32_16x16x32_bf16 v[28:31], v[236:239], v[16:19], v[28:31]\n"
        "v_mfma_f32_16x16x32_bf16 v[32:35], v[16:19], v[240:243], v[32:35]\n"
        "v_mfma_f32_16x16x32_bf16 v[36:39], v[16:19], v[244:247], v[36:39]\n"
        "v_mfma_f32_16x16x32_bf16 v[40:43], v[248:251], v[16:19], v[40:43]\n"
        "v_mfma_f32_16x16x32_bf16 v[44:47], v[252:255], v[16:19], v[44:47]\n"
        "s_waitcnt lgkmcnt(1)\n"
        "ds_read_b128 v[16:19], v0 offset:1024\n"
        "s_branch .Ls1_i1_%=\n"
        ".p2align 5\n"
        ".Ls1_i00_%=:\n"
        "v_mfma_f32_16x16x32_bf16 v[32:35], v[16:19], a[128:131], v[32:35]\n"
        "buffer_load_dword v4, v2, s[40:43], s86 offen sc1\n"
        "v_mfma_f32_16x16x32_bf16 v[36:39], v[16:19], a[132:135], v[36:39]\n"
        "v_mfma_f32_16x16x32_bf16 v[40:43], a[136:139], v[16:19], v[40:43]\n"
        "v_mfma_f32_16x16x32_bf16 v[44:47], a[140:143], v[16:19], v[44:47]\n"
        "s_waitcnt lgkmcnt(1)\n"
        "ds_read_b128 v[16:19], v0 offset:4096\n"
        "v_mfma_f32_16x16x32_bf16 v[24:27], a[144:147], v[8:11], v[24:27]\n"
        "v_mfma_f32_16x16x32_bf16 v[28:31], a[148:151], v[8:11], v[28:31]\n"
        "v_mfma_f32_16x16x32_bf16 v[32:35], v[8:11], a[152:155], v[32:35]\n"
        "v_mfma_f32_16x16x32_bf16 v[36:39], v[8:11], a[156:159], v[36:39]\n"
        "v_mfma_f32_16x16x32_bf16 v[40:43], a[160:163], v[8:11], v[40:43]\n"
        "v_mfma_f32_16x16x32_bf16 v[44:47], a[164:167], v[8:11], v[44:47]\n"
        "s_waitcnt lgkmcnt(1)\n"
        "ds_read_b128 v[8:11], v0 offset:4608\n"
        "v_mfma_f32_16x16x32_bf16 v[24:27], a[168:171], v[12:15], v[24:27]\n"
        "v_mfma_f32_16x16x32_bf16 v[28:31], a[172:175], v[12:15], v[28:31]\n"
        "v_mfma_f32_16x16x32_bf16 v[32:35], v[12:15], a[176:179], v[32:35]\n"
        "v_mfma_f32_16x16x32_bf16 v[36:39], v[12:15], a[180:183], v[36:39]\n"
        "v_mfma_f32_16x16x32_bf16 v[40:43], a[184:187], v[12:15], v[40:43]\n"
        "v_mfma_f32_16x16x32_bf16 v[44:47], a[188:191], v[12:15], v[44:47]\n"
        "s_waitcnt lgkmcnt(1)\n"
        "ds_read_b128 v[12:15], v0 offset:5120\n"
        "v_mfma_f32_16x16x32_bf16 v[24:27], a[192:195], v[16:19], v[24:27]\n"
        "v_mfma_f32_16x16x32_bf16 v[28:31], a[196:199], v[16:19], v[28:31]\n"
        "v_mfma_f32_16x16x32_bf16 v[32:35], v[16:19], a[200:203], v[32:35]\n"
        "s_getreg_b32 s88, hwreg(HW_REG_IB_STS, 0, 4)\n"
        "v_mfma_f32_16x16x32_bf16 v[36:39], v[16:19], a[204:207], v[36:39]\n"
        "s_cmp_le_u32 s88, 1\n"
        "s_cselect_b32 s88, s73, s89\n"
        "s_cselect_b32 s73, s89, s73\n"
        "v_mfma_f32_16x16x32_bf16 v[40:43], a[208:211], v[16:19], v[40:43]\n"
        "buffer_store_dword v5, v3, s[40:43], s88 offen sc1\n"
        "v_mfma_f32_16x16x32_bf16 v[44:47], a[212:215], v[16:19], v[44:47]\n"
        "s_waitcnt lgkmcnt(1)\n"
        "ds_read_b128 v[16:19], v0 offset:5632\n"
        "v_mfma_f32_16x16x32_bf16 v[24:27], a[216:219], v[8:11], v[24:27]\n"
        "v_mfma_f32_16x16x32_bf16 v[28:31], a[220:223], v[8:11], v[28:31]\n"
        "v_mfma_f32_16x16x32_bf16 v[32:35], v[8:11], a[224:227], v[32:35]\n"
        "v_mfma_f32_16x16x32_bf16 v[36:39], v[8:11], a[228:231], v[36:39]\n"
        "v_mfma_f32_16x16x32_bf16 v[40:43], a[232:235], v[8:11], v[40:43]\n"
        "s_getreg_b32 s88, hwreg(HW_REG_IB_STS, 0, 4)\n"
        "v_mfma_f32_16x16x32_bf16 v[44:47], a[236:239], v[8:11], v[44:47]\n"
        "s_waitcnt lgkmcnt(1)\n"
        "ds_read_b128 v[8:11], v0 offset:6144\n"
        "s_waitcnt vmcnt(1)\n"
        "v_cmp_gt_i32 vcc, s70, v4\n"
        "v_mfma_f32_16x16x32_bf16 v[24:27], a[240:243], v[12:15], v[24:27]\n"
        "s_cmp_le_u32 s88, 2\n"
        "s_cselect_b32 s88, s73, s89\n"
        "s_cselect_b32 s73, s89, s73\n"
        "v_mfma_f32_16x16x32_bf16 v[28:31], a[244:247], v[12:15], v[28:31]\n"
        "s_and_b64 vcc, vcc, 1\n"
        "v_mfma_f32_16x16x32_bf16 v[32:35], v[12:15], a[248:251], v[32:35]\n"
        "s_cbranch_vccnz .Ls1_i01_%=\n"
        "v_mfma_f32_16x16x32_bf16 v[36:39], v[12:15], a[252:255], v[36:39]\n"
        "buffer_load_dwordx4 v1, s[40:43], s71 offen offset:0 sc1 lds\n"
        "v_mfma_f32_16x16x32_bf16 v[40:43], v[80:83], v[12:15], v[40:43]\n"
        "buffer_store_dword v5, v3, s[40:43], s88 offen sc1\n"
        "v_mfma_f32_16x16x32_bf16 v[44:47], v[84:87], v[12:15], v[44:47]\n"
        "s_waitcnt lgkmcnt(1)\n"
        "ds_read_b128 v[12:15], v0 offset:6656\n"
        "v_mfma_f32_16x16x32_bf16 v[24:27], v[88:91], v[16:19], v[24:27]\n"
        "s_getreg_b32 s88, hwreg(HW_REG_IB_STS, 0, 4)\n"
        "buffer_load_dwordx4 v1, s[40:43], s71 offen offset:1024 sc1 lds\n"
        "v_mfma_f32_16x16x32_bf16 v[28:31], v[92:95], v[16:19], v[28:31]\n"
        "s_cmp_le_u32 s88, 15\n"
        "s_cselect_b32 s88, s73, s89\n"
        "s_cselect_b32 s73, s89, s73\n"
        "v_mfma_f32_16x16x32_bf16 v[32:35], v[16:19], v[96:99], v[32:35]\n"
        "buffer_store_dword v5, v3, s[40:43], s88 offen sc1\n"
        "v_mfma_f32_16x16x32_bf16 v[36:39], v[16:19], v[100:103], v[36:39]\n"
        "buffer_load_dwordx4 v1, s[40:43], s71 offen offset:2048 sc1 lds\n"
        "v_mfma_f32_16x16x32_bf16 v[40:43], v[104:107], v[16:19], v[40:43]\n"
        "v_mfma_f32_16x16x32_bf16 v[44:47], v[108:111], v[16:19], v[44:47]\n"
        "s_waitcnt lgkmcnt(1)\n"
        "ds_read_b128 v[16:19], v0 offset:7168\n"
        "v_mfma_f32_16x16x32_bf16 v[24:27], v[112:115], v[8:11], v[24:27]\n"
        "buffer_load_dwordx4 v1, s[40:43], s71 offen offset:3072 sc1 lds\n"
        "v_mfma_f32_16x16x32_bf16 v[28:31], v[116:119], v[8:11], v[28:31]\n"
        "s_add_u32 m0, s78, s85\n"
        "s_add_u32 s69, s69, 1\n"
        "v_mfma_f32_16x16x32_bf16 v[32:35], v[8:11], v[120:123], v[32:35]\n"
        "v_mfma_f32_16x16x32_bf16 v[36:39], v[8:11], v[124:127], v[36:39]\n"
        "buffer_load_dwordx4 v1, s[40:43], s81 offen offset:0 sc1 lds\n"
        "v_mfma_f32_16x16x32_bf16 v[40:43], v[128:131], v[8:11], v[40:43]\n"
        "v_mfma_f32_16x16x32_bf16 v[44:47], v[132:135], v[8:11], v[44:47]\n"
        "s_waitcnt lgkmcnt(1)\n"
        "ds_read_b128 v[8:11], v0 offset:7680\n"
        "v_mfma_f32_16x16x32_bf16 v[24:27], v[136:139], v[12:15], v[24:27]\n"
        "v_mfma_f32_16x16x32_bf16 v[28:31], v[140:143], v[12:15], v[28:31]\n"
        "v_mfma_f32_16x16x32_bf16 v[32:35], v[12:15], v[144:147], v[32:35]\n"
        "buffer_store_dword v5, v3, s[40:43], s73 offen sc1\n"
        "v_mfma_f32_16x16x32_bf16 v[36:39], v[12:15], v[148:151], v[36:39]\n"
        "s_mov_b32 s84, s89\n"
        "s_mov_b32 s73, s60\n"
        "s_add_u32 s74, s74, 1\n"
        "v_mfma_f32_16x16x32_bf16 v[40:43], v[152:155], v[12:15], v[40:43]\n"
        "s_add_u32 s68, s68, 1\n"
        "s_bitcmp1_b64 s[52:53], s68\n"
        "s_cselect_b32 s66, 8192, 0\n"
        "v_mfma_f32_16x16x32_bf16 v[44:47], v[156:159], v[12:15], v[44:47]\n"
        "s_waitcnt lgkmcnt(1)\n"
        "ds_read_b128 v[12:15], v0 offset:8192\n"
        "s_lshl_b32 s75, s68, 21\n"
        "v_mfma_f32_16x16x32_bf16 v[24:27], v[160:163], v[16:19], v[24:27]\n"
        "s_add_u32 s75, s75, s66\n"
        "s_add_u32 s72, s75, s59\n"
        "v_mfma_f32_16x16x32_bf16 v[28:31], v[164:167], v[16:19], v[28:31]\n"
        "v_mfma_f32_16x16x32_bf16 v[32:35], v[16:19], v[168:171], v[32:35]\n"
        "v_mfma_f32_16x16x32_bf16 v[36:39], v[16:19], v[172:175], v[36:39]\n"
        "v_mfma_f32_16x16x32_bf16 v[40:43], v[176:179], v[16:19], v[40:43]\n"
        "v_mfma_f32_16x16x32_bf16 v[44:47], v[180:183], v[16:19], v[44:47]\n"
        "s_waitcnt lgkmcnt(1)\n"
        "ds_read_b128 v[16:19], v0 offset:8704\n"
        "v_mfma_f32_16x16x32_bf16 v[24:27], v[184:187], v[8:11], v[24:27]\n"
        "v_mfma_f32_16x16x32_bf16 v[28:31], v[188:191], v[8:11], v[28:31]\n"
        "v_mfma_f32_16x16x32_bf16 v[32:35], v[8:11], v[192:195], v[32:35]\n"
        "v_mfma_f32_16x16x32_bf16 v[36:39], v[8:11], v[196:199], v[36:39]\n"
        "v_mfma_f32_16x16x32_bf16 v[40:43], v[200:203], v[8:11], v[40:43]\n"
        "s_waitcnt vmcnt(1)\n"
        "s_barrier\n"
        "v_add_u32 v0, s78, v6\n"
        "v_mfma_f32_16x16x32_bf16 v[44:47], v[204:207], v[8:11], v[44:47]\n"
        "s_waitcnt lgkmcnt(1)\n"
        "ds_read_b128 v[8:11], v0 offset:0\n"
        "v_mfma_f32_16x16x32_bf16 v[24:27], v[208:211], v[12:15], v[24:27]\n"
        "v_mfma_f32_16x16x32_bf16 v[28:31], v[212:215], v[12:15], v[28:31]\n"
        "v_mfma_f32_16x16x32_bf16 v[32:35], v[12:15], v[216:219], v[32:35]\n"
        "v_mfma_f32_16x16x32_bf16 v[36:39], v[12:15], v[220:223], v[36:39]\n"
        "v_mfma_f32_16x16x32_bf16 v[40:43], v[224:227], v[12:15], v[40:43]\n"
        "v_mfma_f32_16x16x32_bf16 v[44:47], v[228:231], v[12:15], v[44:47]\n"
        "s_waitcnt lgkmcnt(1)\n"
        "ds_read_b128 v[12:15], v0 offset:512\n"
        "v_mfma_f32_16x16x32_bf16 v[24:27], v[232:235], v[16:19], v[24:27]\n"
        "v_mfma_f32_16x16x32_bf16 v[28:31], v[236:239], v[16:19], v[28:31]\n"
        "v_mfma_f32_16x16x32_bf16 v[32:35], v[16:19], v[240:243], v[32:35]\n"
        "v_mfma_f32_16x16x32_bf16 v[36:39], v[16:19], v[244:247], v[36:39]\n"
        "v_mfma_f32_16x16x32_bf16 v[40:43], v[248:251], v[16:19], v[40:43]\n"
        "v_mfma_f32_16x16x32_bf16 v[44:47], v[252:255], v[16:19], v[44:47]\n"
        "s_waitcnt lgkmcnt(1)\n"
        "ds_read_b128 v[16:19], v0 offset:1024\n"
        "s_branch .Ls1_i1_%=\n"
        ".p2align 5\n"
        ".Ls1_i01_%=:\n"
        "v_mfma_f32_16x16x32_bf16 v[36:39], v[12:15], a[252:255], v[36:39]\n"
        "buffer_load_dword v4, v2, s[40:43], s86 offen sc1\n"
        "v_mfma_f32_16x16x32_bf16 v[40:43], v[80:83], v[12:15], v[40:43]\n"
        "buffer_store_dword v5, v3, s[40:43], s88 offen sc1\n"
        "v_mfma_f32_16x16x32_bf16 v[44:47], v[84:87], v[12:15], v[44:47]\n"
        "s_waitcnt lgkmcnt(1)\n"
        "ds_read_b128 v[12:15], v0 offset:6656\n"
        "v_mfma_f32_16x16x32_bf16 v[24:27], v[88:91], v[16:19], v[24:27]\n"
        "s_getreg_b32 s88, hwreg(HW_REG_IB_STS, 0, 4)\n"
        "v_mfma_f32_16x16x32_bf16 v[28:31], v[92:95], v[16:19], v[28:31]\n"
        "s_cmp_le_u32 s88, 15\n"
        "s_cselect_b32 s88, s73, s89\n"
        "s_cselect_b32 s73, s89, s73\n"
        "v_mfma_f32_16x16x32_bf16 v[32:35], v[16:19], v[96:99], v[32:35]\n"
        "buffer_store_dword v5, v3, s[40:43], s88 offen sc1\n"
        "v_mfma_f32_16x16x32_bf16 v[36:39], v[16:19], v[100:103], v[36:39]\n"
        "v_mfma_f32_16x16x32_bf16 v[40:43], v[104:107], v[16:19], v[40:43]\n"
        "v_mfma_f32_16x16x32_bf16 v[44:47], v[108:111], v[16:19], v[44:47]\n"
        "s_waitcnt lgkmcnt(1)\n"
        "ds_read_b128 v[16:19], v0 offset:7168\n"
        "v_mfma_f32_16x16x32_bf16 v[24:27], v[112:115], v[8:11], v[24:27]\n"
        "v_mfma_f32_16x16x32_bf16 v[28:31], v[116:119], v[8:11], v[28:31]\n"
        "v_mfma_f32_16x16x32_bf16 v[32:35], v[8:11], v[120:123], v[32:35]\n"
        "v_mfma_f32_16x16x32_bf16 v[36:39], v[8:11], v[124:127], v[36:39]\n"
        "v_mfma_f32_16x16x32_bf16 v[40:43], v[128:131], v[8:11], v[40:43]\n"
        "v_mfma_f32_16x16x32_bf16 v[44:47], v[132:135], v[8:11], v[44:47]\n"
        "s_waitcnt lgkmcnt(1)\n"
        "ds_read_b128 v[8:11], v0 offset:7680\n"
        "v_mfma_f32_16x16x32_bf16 v[24:27], v[136:139], v[12:15], v[24:27]\n"
        "v_mfma_f32_16x16x32_bf16 v[28:31], v[140:143], v[12:15], v[28:31]\n"
        "v_mfma_f32_16x16x32_bf16 v[32:35], v[12:15], v[144:147], v[32:35]\n"
        "buffer_store_dword v5, v3, s[40:43], s73 offen sc1\n"
        "v_mfma_f32_16x16x32_bf16 v[36:39], v[12:15], v[148:151], v[36:39]\n"
        "s_mov_b32 s84, s89\n"
        "s_mov_b32 s73, s60\n"
        "s_add_u32 s74, s74, 1\n"
        "v_mfma_f32_16x16x32_bf16 v[40:43], v[152:155], v[12:15], v[40:43]\n"
        "s_add_u32 s68, s68, 1\n"
        "s_bitcmp1_b64 s[52:53], s68\n"
        "s_cselect_b32 s66, 8192, 0\n"
        "v_mfma_f32_16x16x32_bf16 v[44:47], v[156:159], v[12:15], v[44:47]\n"
        "s_waitcnt lgkmcnt(1)\n"
        "ds_read_b128 v[12:15], v0 offset:8192\n"
        "s_lshl_b32 s75, s68, 21\n"
        "v_mfma_f32_16x16x32_bf16 v[24:27], v[160:163], v[16:19], v[24:27]\n"
        "s_add_u32 s75, s75, s66\n"
        "s_add_u32 s72, s75, s59\n"
        "v_mfma_f32_16x16x32_bf16 v[28:31], v[164:167], v[16:19], v[28:31]\n"
        "v_mfma_f32_16x16x32_bf16 v[32:35], v[16:19], v[168:171], v[32:35]\n"
        "v_mfma_f32_16x16x32_bf16 v[36:39], v[16:19], v[172:175], v[36:39]\n"
        "v_mfma_f32_16x16x32_bf16 v[40:43], v[176:179], v[16:19], v[40:43]\n"
        "v_mfma_f32_16x16x32_bf16 v[44:47], v[180:183], v[16:19], v[44:47]\n"
        "s_waitcnt lgkmcnt(1)\n"
        "ds_read_b128 v[16:19], v0 offset:8704\n"
        "v_mfma_f32_16x16x32_bf16 v[24:27], v[184:187], v[8:11], v[24:27]\n"
        "s_waitcnt vmcnt(3)\n"
        "v_cmp_gt_i32_e64 vcc, s70, v4\n"
        "v_mfma_f32_16x16x32_bf16 v[28:31], v[188:191], v[8:11], v[28:31]\n"
        "s_nop 0\n"
        "v_mfma_f32_16x16x32_bf16 v[32:35], v[8:11], v[192:195], v[32:35]\n"
        "s_and_b64 vcc, vcc, 1\n"
        "v_mfma_f32_16x16x32_bf16 v[36:39], v[8:11], v[196:199], v[36:39]\n"
        "s_cbranch_vccnz .Ls1_i02_%=\n"
        "v_mfma_f32_16x16x32_bf16 v[40:43], v[200:203], v[8:11], v[40:43]\n"
        "buffer_load_dwordx4 v1, s[40:43], s71 offen offset:0 sc1 lds\n"
        "v_mfma_f32_16x16x32_bf16 v[44:47], v[204:207], v[8:11], v[44:47]\n"
        "s_waitcnt lgkmcnt(1)\n"
        "v_mfma_f32_16x16x32_bf16 v[24:27], v[208:211], v[12:15], v[24:27]\n"
        "buffer_load_dwordx4 v1, s[40:43], s71 offen offset:1024 sc1 lds\n"
        "v_mfma_f32_16x16x32_bf16 v[28:31], v[212:215], v[12:15], v[28:31]\n"
        "v_mfma_f32_16x16x32_bf16 v[32:35], v[12:15], v[216:219], v[32:35]\n"
        "buffer_load_dwordx4 v1, s[40:43], s71 offen offset:2048 sc1 lds\n"
        "v_mfma_f32_16x16x32_bf16 v[36:39], v[12:15], v[220:223], v[36:39]\n"
        "v_mfma_f32_16x16x32_bf16 v[40:43], v[224:227], v[12:15], v[40:43]\n"
        "buffer_load_dwordx4 v1, s[40:43], s71 offen offset:3072 sc1 lds\n"
        "v_mfma_f32_16x16x32_bf16 v[44:47], v[228:231], v[12:15], v[44:47]\n"
        "s_waitcnt lgkmcnt(0)\n"
        "s_add_u32 m0, s78, s85\n"
        "s_add_u32 s69, s69, 1\n"
        "v_mfma_f32_16x16x32_bf16 v[24:27], v[232:235], v[16:19], v[24:27]\n"
        "buffer_load_dwordx4 v1, s[40:43], s81 offen offset:0 sc1 lds\n"
        "v_mfma_f32_16x16x32_bf16 v[28:31], v[236:239], v[16:19], v[28:31]\n"
        "v_mfma_f32_16x16x32_bf16 v[32:35], v[16:19], v[240:243], v[32:35]\n"
        "v_mfma_f32_16x16x32_bf16 v[36:39], v[16:19], v[244:247], v[36:39]\n"
        "v_mfma_f32_16x16x32_bf16 v[40:43], v[248:251], v[16:19], v[40:43]\n"
        "v_mfma_f32_16x16x32_bf16 v[44:47], v[252:255], v[16:19], v[44:47]\n"
        "v_cvt_pk_bf16_f32 v24, v24, v25\n"
        "v_cvt_pk_bf16_f32 v25, v26, v27\n"
        "v_cvt_pk_bf16_f32 v26, v28, v29\n"
        "v_cvt_pk_bf16_f32 v27, v30, v31\n"
        "s_add_u32 s66, s72, 524288\n"
        "buffer_store_dwordx4 v[24:27], v1, s[40:43], s66 offen offset:0 sc1\n"
        "v_cvt_pk_bf16_f32 v72, v32, v33\n"
        "v_cvt_pk_bf16_f32 v73, v34, v35\n"
        "v_cvt_pk_bf16_f32 v76, v36, v37\n"
        "v_cvt_pk_bf16_f32 v77, v38, v39\n"
        "v_cvt_pk_bf16_f32 v40, v40, v41\n"
        "v_cvt_pk_bf16_f32 v41, v42, v43\n"
        "v_cvt_pk_bf16_f32 v42, v44, v45\n"
        "v_cvt_pk_bf16_f32 v43, v46, v47\n"
        "buffer_store_dwordx4 v[40:43], v1, s[40:43], s72 offen offset:0 sc1\n"
        "v_mov_b32 v5, s74\n"
        "s_mov_b32 s84, s60\n"
        "s_waitcnt vmcnt(2)\n"
        "s_branch .Ls1_go0_%=\n"
        ".p2align 5\n"
        ".Ls1_i02_%=:\n"
        "v_mfma_f32_16x16x32_bf16 v[40:43], v[200:203], v[8:11], v[40:43]\n"
        "buffer_load_dword v4, v2, s[40:43], s86 offen sc1\n"
        "v_mfma_f32_16x16x32_bf16 v[44:47], v[204:207], v[8:11], v[44:47]\n"
        "s_waitcnt lgkmcnt(1)\n"
        "v_mfma_f32_16x16x32_bf16 v[24:27], v[208:211], v[12:15], v[24:27]\n"
        "v_mfma_f32_16x16x32_bf16 v[28:31], v[212:215], v[12:15], v[28:31]\n"
        "v_mfma_f32_16x16x32_bf16 v[32:35], v[12:15], v[216:219], v[32:35]\n"
        "v_mfma_f32_16x16x32_bf16 v[36:39], v[12:15], v[220:223], v[36:39]\n"
        "v_mfma_f32_16x16x32_bf16 v[40:43], v[224:227], v[12:15], v[40:43]\n"
        "v_mfma_f32_16x16x32_bf16 v[44:47], v[228:231], v[12:15], v[44:47]\n"
        "s_waitcnt lgkmcnt(0)\n"
        "v_mfma_f32_16x16x32_bf16 v[24:27], v[232:235], v[16:19], v[24:27]\n"
        "v_mfma_f32_16x16x32_bf16 v[28:31], v[236:239], v[16:19], v[28:31]\n"
        "v_mfma_f32_16x16x32_bf16 v[32:35], v[16:19], v[240:243], v[32:35]\n"
        "v_mfma_f32_16x16x32_bf16 v[36:39], v[16:19], v[244:247], v[36:39]\n"
        "v_mfma_f32_16x16x32_bf16 v[40:43], v[248:251], v[16:19], v[40:43]\n"
        "v_mfma_f32_16x16x32_bf16 v[44:47], v[252:255], v[16:19], v[44:47]\n"
        "v_cvt_pk_bf16_f32 v24, v24, v25\n"
        "v_cvt_pk_bf16_f32 v25, v26, v27\n"
        "v_cvt_pk_bf16_f32 v26, v28, v29\n"
        "v_cvt_pk_bf16_f32 v27, v30, v31\n"
        "s_add_u32 s66, s72, 524288\n"
        "buffer_store_dwordx4 v[24:27], v1, s[40:43], s66 offen offset:0 sc1\n"
        "v_cvt_pk_bf16_f32 v72, v32, v33\n"
        "v_cvt_pk_bf16_f32 v73, v34, v35\n"
        "v_cvt_pk_bf16_f32 v76, v36, v37\n"
        "v_cvt_pk_bf16_f32 v77, v38, v39\n"
        "v_cvt_pk_bf16_f32 v40, v40, v41\n"
        "v_cvt_pk_bf16_f32 v41, v42, v43\n"
        "v_cvt_pk_bf16_f32 v42, v44, v45\n"
        "v_cvt_pk_bf16_f32 v43, v46, v47\n"
        "buffer_store_dwordx4 v[40:43], v1, s[40:43], s72 offen offset:0 sc1\n"
        "s_waitcnt vmcnt(2)\n"
        "v_cmp_gt_i32 vcc, s70, v4\n"
        "s_nop 0\n"
        "s_and_b64 vcc, vcc, 1\n"
        "s_cbranch_vccnz .Ls1_late0_%=\n"
        "buffer_load_dwordx4 v1, s[40:43], s71 offen offset:0 sc1 lds\n"
        "buffer_load_dwordx4 v1, s[40:43], s71 offen offset:1024 sc1 lds\n"
        "buffer_load_dwordx4 v1, s[40:43], s71 offen offset:2048 sc1 lds\n"
        "buffer_load_dwordx4 v1, s[40:43], s71 offen offset:3072 sc1 lds\n"
        "s_add_u32 m0, s78, s85\n"
        "s_add_u32 s69, s69, 1\n"
        "buffer_load_dwordx4 v1, s[40:43], s81 offen offset:0 sc1 lds\n"
        "s_waitcnt vmcnt(5)\n"
        "v_mov_b32 v5, s74\n"
        "buffer_store_dword v5, v3, s[40:43], s73 offen sc1\n"
        "s_waitcnt vmcnt(1)\n"
        "s_branch .Ls1_go0_%=\n"
        ".p2align 5\n"
        ".Ls1_late0_%=:\n"
        "s_waitcnt vmcnt(0)\n"
        "v_mov_b32 v5, s74\n"
        "buffer_store_dword v5, v3, s[40:43], s73 offen sc1\n"
        "s_mov_b32 s64, 0\n"
        ".Ls1_in0_top_%=:\n"
        "buffer_load_dword v4, v2, s[40:43], s86 offen sc1\n"
        "s_waitcnt vmcnt(0)\n"
        "v_cmp_gt_i32 vcc, s70, v4\n"
        "s_nop 0\n"
        "s_and_b64 vcc, vcc, 1\n"
        "s_cbranch_vccz .Ls1_in0_ok_%=\n"
        "s_add_u32 s64, s64, 1\n"
        "s_and_b32 s65, s64, 255\n"
        "s_nop 0\n"
        "s_cbranch_scc1 .Ls1_in0_go_%=\n"
        "s_mov_b32 s65, 49152\n"
        "s_cmp_lt_u32 s64, 1048576\n"
        "s_nop 0\n"
        "s_nop 0\n"
        "s_nop 0\n"
        "s_cbranch_scc1 .Ls1_in0_chk_%=\n"
        "v_mov_b32 v4, 1\n"
        "buffer_store_dword v4, off, s[40:43], s65 sc1\n"
        "s_waitcnt vmcnt(0)\n"
        ".Ls1_in0_chk_%=:\n"
        "buffer_load_dword v4, off, s[40:43], s65 sc1\n"
        "s_waitcnt vmcnt(0)\n"
        "v_readfirstlane_b32 s65, v4\n"
        "s_nop 0\n"
        "s_cmp_lg_u32 s65, 1\n"
        "s_cbranch_scc1 .Ls1_in0_go_%=\n"
        "s_endpgm\n"
        ".p2align 5\n"
        ".Ls1_in0_go_%=:\n"
        "s_branch .Ls1_in0_top_%=\n"
        ".p2align 5\n"
        ".Ls1_in0_ok_%=:\n"
        "buffer_load_dwordx4 v1, s[40:43], s71 offen offset:0 sc1 lds\n"
        "buffer_load_dwordx4 v1, s[40:43], s71 offen offset:1024 sc1 lds\n"
        "buffer_load_dwordx4 v1, s[40:43], s71 offen offset:2048 sc1 lds\n"
        "buffer_load_dwordx4 v1, s[40:43], s71 offen offset:3072 sc1 lds\n"
        "s_add_u32 m0, s78, s85\n"
        "s_add_u32 s69, s69, 1\n"
        "buffer_load_dwordx4 v1, s[40:43], s81 offen offset:0 sc1 lds\n"
        "s_waitcnt vmcnt(0)\n"
        ".Ls1_go0_%=:\n"
        "s_mov_b32 s72, 241172480\n"
        "s_mov_b32 s83, 241172480\n"
        "s_mov_b32 s73, 241172480\n"
        "v_add_u32 v0, s78, v6\n"
        "s_barrier\n"
        "ds_read_b128 v[8:11], v0 offset:0\n"
        "ds_read_b128 v[12:15], v0 offset:512\n"
        "ds_read_b128 v[16:19], v0 offset:1024\n"
        "s_waitcnt lgkmcnt(2)\n"
        "s_branch .Ls1_i1_%=\n"
        ".p2align 5\n"
        ".Ls1_i1_%=:\n"
        "v_mfma_f32_16x16x32_bf16 v[48:51], a[0:3], v[8:11], 0\n"
        "s_add_u32 s86, s76, 0\n"
        "buffer_load_dword v4, v2, s[40:43], s86 offen sc1\n"
        "v_mfma_f32_16x16x32_bf16 v[52:55], a[4:7], v[8:11], 0\n"
        "s_lshr_b32 s67, s69, 1\n"
        "v_cvt_pk_bf16_f32 v24, v24, v25\n"
        "v_cvt_pk_bf16_f32 v25, v26, v27\n"
        "v_mfma_f32_16x16x32_bf16 v[56:59], v[8:11], a[8:11], 0\n"
        "s_bitcmp1_b64 s[52:53], s67\n"
        "s_cselect_b32 s65, 8192, 0\n"
        "v_cvt_pk_bf16_f32 v26, v28, v29\n"
        "v_mfma_f32_16x16x32_bf16 v[60:63], v[8:11], a[12:15], 0\n"
        "s_lshl_b32 s77, s67, 21\n"
        "v_cvt_pk_bf16_f32 v27, v30, v31\n"
        "s_add_u32 s66, s72, 524288\n"
        "v_mfma_f32_16x16x32_bf16 v[64:67], a[16:19], v[8:11], 0\n"
        "s_add_u32 s77, s77, s65\n"
        "buffer_store_dwordx4 v[24:27], v1, s[40:43], s66 offen offset:0 sc1\n"
        "v_mfma_f32_16x16x32_bf16 v[68:71], a[20:23], v[8:11], 0\n"
        "s_waitcnt lgkmcnt(1)\n"
        "ds_read_b128 v[8:11], v0 offset:1536\n"
        "v_cvt_pk_bf16_f32 v72, v32, v33\n"
        "v_mfma_f32_16x16x32_bf16 v[48:51], a[24:27], v[12:15], v[48:51]\n"
        "s_add_u32 s71, s77, s58\n"
        "v_cvt_pk_bf16_f32 v73, v34, v35\n"
        "v_cvt_pk_bf16_f32 v76, v36, v37\n"
        "v_mfma_f32_16x16x32_bf16 v[52:55], a[28:31], v[12:15], v[52:55]\n"
        "s_add_u32 s81, s77, s62\n"
        "v_cvt_pk_bf16_f32 v77, v38, v39\n"
        "v_cvt_pk_bf16_f32 v40, v40, v41\n"
        "v_mfma_f32_16x16x32_bf16 v[56:59], v[12:15], a[32:35], v[56:59]\n"
        "s_add_u32 s78, s78, 18432\n"
        "v_cvt_pk_bf16_f32 v41, v42, v43\n"
        "v_cvt_pk_bf16_f32 v42, v44, v45\n"
        "v_mfma_f32_16x16x32_bf16 v[60:63], v[12:15], a[36:39], v[60:63]\n"
        "s_cmp_eq_u32 s78, 55296\n"
        "s_cselect_b32 s78, 0, s78\n"
        "v_cvt_pk_bf16_f32 v43, v46, v47\n"
        "v_mfma_f32_16x16x32_bf16 v[64:67], a[40:43], v[12:15], v[64:67]\n"
        "s_add_u32 m0, s78, s61\n"
        "v_mfma_f32_16x16x32_bf16 v[68:71], a[44:47], v[12:15], v[68:71]\n"
        "s_waitcnt lgkmcnt(1)\n"
        "ds_read_b128 v[12:15], v0 offset:2048\n"
        "v_mfma_f32_16x16x32_bf16 v[48:51], a[48:51], v[16:19], v[48:51]\n"
        "s_lshr_b32 s70, s69, 1\n"
        "buffer_store_dwordx4 v[40:43], v1, s[40:43], s72 offen offset:0 sc1\n"
        "v_mfma_f32_16x16x32_bf16 v[52:55], a[52:55], v[16:19], v[52:55]\n"
        "v_mfma_f32_16x16x32_bf16 v[56:59], v[16:19], a[56:59], v[56:59]\n"
        "s_waitcnt vmcnt(3)\n"
        "v_mfma_f32_16x16x32_bf16 v[60:63], v[16:19], a[60:63], v[60:63]\n"
        "buffer_store_dword v5, v3, s[40:43], s84 offen sc1\n"
        "v_mfma_f32_16x16x32_bf16 v[64:67], a[64:67], v[16:19], v[64:67]\n"
        "v_mov_b32 v5, s74\n"
        "v_mfma_f32_16x16x32_bf16 v[68:71], a[68:71], v[16:19], v[68:71]\n"
        "s_waitcnt lgkmcnt(1)\n"
        "ds_read_b128 v[16:19], v0 offset:2560\n"
        "v_mfma_f32_16x16x32_bf16 v[48:51], a[72:75], v[8:11], v[48:51]\n"
        "v_mfma_f32_16x16x32_bf16 v[52:55], a[76:79], v[8:11], v[52:55]\n"
        "v_mfma_f32_16x16x32_bf16 v[56:59], v[8:11], a[80:83], v[56:59]\n"
        "v_mfma_f32_16x16x32_bf16 v[60:63], v[8:11], a[84:87], v[60:63]\n"
        "v_mfma_f32_16x16x32_bf16 v[64:67], a[88:91], v[8:11], v[64:67]\n"
        "v_mfma_f32_16x16x32_bf16 v[68:71], a[92:95], v[8:11], v[68:71]\n"
        "s_waitcnt lgkmcnt(1)\n"
        "ds_read_b128 v[8:11], v0 offset:3072\n"
        "v_mfma_f32_16x16x32_bf16 v[48:51], a[96:99], v[12:15], v[48:51]\n"
        "v_mfma_f32_16x16x32_bf16 v[52:55], a[100:103], v[12:15], v[52:55]\n"
        "v_mfma_f32_16x16x32_bf16 v[56:59], v[12:15], a[104:107], v[56:59]\n"
        "v_mfma_f32_16x16x32_bf16 v[60:63], v[12:15], a[108:111], v[60:63]\n"
        "s_nop 0\n"
        "v_mfma_f32_16x16x32_bf16 v[64:67], a[112:115], v[12:15], v[64:67]\n"
        "s_waitcnt vmcnt(3)\n"
        "v_cmp_gt_i32_e64 vcc, s70, v4\n"
        "v_mfma_f32_16x16x32_bf16 v[68:71], a[116:119], v[12:15], v[68:71]\n"
        "s_waitcnt lgkmcnt(1)\n"
        "ds_read_b128 v[12:15], v0 offset:3584\n"
        "v_mfma_f32_16x16x32_bf16 v[48:51], a[120:123], v[16:19], v[48:51]\n"
        "s_and_b64 vcc, vcc, 1\n"
        "v_mfma_f32_16x16x32_bf16 v[52:55], a[124:127], v[16:19], v[52:55]\n"
        "s_cbranch_vccnz .Ls1_i10_%=\n"
        "v_mfma_f32_16x16x32_bf16 v[56:59], v[16:19], a[128:131], v[56:59]\n"
        "buffer_load_dwordx4 v1, s[40:43], s71 offen offset:0 sc1 lds\n"
        "v_mfma_f32_16x16x32_bf16 v[60:63], v[16:19], a[132:135], v[60:63]\n"
        "v_mfma_f32_16x16x32_bf16 v[64:67], a[136:139], v[16:19], v[64:67]\n"
        "v_mfma_f32_16x16x32_bf16 v[68:71], a[140:143], v[16:19], v[68:71]\n"
        "s_waitcnt lgkmcnt(1)\n"
        "ds_read_b128 v[16:19], v0 offset:4096\n"
        "v_mfma_f32_16x16x32_bf16 v[48:51], a[144:147], v[8:11], v[48:51]\n"
        "s_getreg_b32 s88, hwreg(HW_REG_IB_STS, 0, 4)\n"
        "buffer_load_dwordx4 v1, s[40:43], s71 offen offset:1024 sc1 lds\n"
        "v_mfma_f32_16x16x32_bf16 v[52:55], a[148:151], v[8:11], v[52:55]\n"
        "s_cmp_le_u32 s88, 2\n"
        "s_cselect_b32 s88, s73, s89\n"
        "s_cselect_b32 s73, s89, s73\n"
        "v_mfma_f32_16x16x32_bf16 v[56:59], v[8:11], a[152:155], v[56:59]\n"
        "buffer_store_dword v5, v3, s[40:43], s88 offen sc1\n"
        "v_mfma_f32_16x16x32_bf16 v[60:63], v[8:11], a[156:159], v[60:63]\n"
        "v_mfma_f32_16x16x32_bf16 v[64:67], a[160:163], v[8:11], v[64:67]\n"
        "buffer_load_dwordx4 v1, s[40:43], s71 offen offset:2048 sc1 lds\n"
        "v_mfma_f32_16x16x32_bf16 v[68:71], a[164:167], v[8:11], v[68:71]\n"
        "s_waitcnt lgkmcnt(1)\n"
        "ds_read_b128 v[8:11], v0 offset:4608\n"
        "v_mfma_f32_16x16x32_bf16 v[48:51], a[168:171], v[12:15], v[48:51]\n"
        "v_mfma_f32_16x16x32_bf16 v[52:55], a[172:175], v[12:15], v[52:55]\n"
        "v_mfma_f32_16x16x32_bf16 v[56:59], v[12:15], a[176:179], v[56:59]\n"
        "s_getreg_b32 s88, hwreg(HW_REG_IB_STS, 0, 4)\n"
        "buffer_load_dwordx4 v1, s[40:43], s71 offen offset:3072 sc1 lds\n"
        "v_mfma_f32_16x16x32_bf16 v[60:63], v[12:15], a[180:183], v[60:63]\n"
        "s_add_u32 m0, s78, s85\n"
        "s_add_u32 s69, s69, 1\n"
        "v_mfma_f32_16x16x32_bf16 v[64:67], a[184:187], v[12:15], v[64:67]\n"
        "s_cmp_le_u32 s88, 5\n"
        "s_cselect_b32 s88, s73, s89\n"
        "s_cselect_b32 s73, s89, s73\n"
        "v_mfma_f32_16x16x32_bf16 v[68:71], a[188:191], v[12:15], v[68:71]\n"
        "s_waitcnt lgkmcnt(1)\n"
        "ds_read_b128 v[12:15], v0 offset:5120\n"
        "v_mfma_f32_16x16x32_bf16 v[48:51], a[192:195], v[16:19], v[48:51]\n"
        "buffer_load_dwordx4 v1, s[40:43], s81 offen offset:0 sc1 lds\n"
        "v_mfma_f32_16x16x32_bf16 v[52:55], a[196:199], v[16:19], v[52:55]\n"
        "buffer_store_dword v5, v3, s[40:43], s88 offen sc1\n"
        "v_mfma_f32_16x16x32_bf16 v[56:59], v[16:19], a[200:203], v[56:59]\n"
        "v_mfma_f32_16x16x32_bf16 v[60:63], v[16:19], a[204:207], v[60:63]\n"
        "v_mfma_f32_16x16x32_bf16 v[64:67], a[208:211], v[16:19], v[64:67]\n"
        "s_getreg_b32 s88, hwreg(HW_REG_IB_STS, 0, 4)\n"
        "v_mfma_f32_16x16x32_bf16 v[68:71], a[212:215], v[16:19], v[68:71]\n"
        "s_waitcnt lgkmcnt(1)\n"
        "ds_read_b128 v[16:19], v0 offset:5632\n"
        "v_mfma_f32_16x16x32_bf16 v[48:51], a[216:219], v[8:11], v[48:51]\n"
        "s_cmp_le_u32 s88, 8\n"
        "s_cselect_b32 s88, s73, s89\n"
        "s_cselect_b32 s73, s89, s73\n"
        "v_mfma_f32_16x16x32_bf16 v[52:55], a[220:223], v[8:11], v[52:55]\n"
        "buffer_store_dword v5, v3, s[40:43], s88 offen sc1\n"
        "v_mfma_f32_16x16x32_bf16 v[56:59], v[8:11], a[224:227], v[56:59]\n"
        "v_mfma_f32_16x16x32_bf16 v[60:63], v[8:11], a[228:231], v[60:63]\n"
        "v_mfma_f32_16x16x32_bf16 v[64:67], a[232:235], v[8:11], v[64:67]\n"
        "v_mfma_f32_16x16x32_bf16 v[68:71], a[236:239], v[8:11], v[68:71]\n"
        "s_waitcnt lgkmcnt(1)\n"
        "ds_read_b128 v[8:11], v0 offset:6144\n"
        "v_mfma_f32_16x16x32_bf16 v[48:51], a[240:243], v[12:15], v[48:51]\n"
        "v_mfma_f32_16x16x32_bf16 v[52:55], a[244:247], v[12:15], v[52:55]\n"
        "v_mfma_f32_16x16x32_bf16 v[56:59], v[12:15], a[248:251], v[56:59]\n"
        "v_mfma_f32_16x16x32_bf16 v[60:63], v[12:15], a[252:255], v[60:63]\n"
        "v_mfma_f32_16x16x32_bf16 v[64:67], v[80:83], v[12:15], v[64:67]\n"
        "v_mfma_f32_16x16x32_bf16 v[68:71], v[84:87], v[12:15], v[68:71]\n"
        "s_waitcnt lgkmcnt(1)\n"
        "ds_read_b128 v[12:15], v0 offset:6656\n"
        "v_mfma_f32_16x16x32_bf16 v[48:51], v[88:91], v[16:19], v[48:51]\n"
        "s_waitcnt vmcnt(9)\n"
        "buffer_store_dword v5, v3, s[40:43], s73 offen sc1\n"
        "v_mfma_f32_16x16x32_bf16 v[52:55], v[92:95], v[16:19], v[52:55]\n"
        "s_mov_b32 s84, s89\n"
        "s_mov_b32 s73, s60\n"
        "s_add_u32 s74, s74, 1\n"
        "v_mfma_f32_16x16x32_bf16 v[56:59], v[16:19], v[96:99], v[56:59]\n"
        "s_add_u32 s72, s75, s59\n"
        "v_mfma_f32_16x16x32_bf16 v[60:63], v[16:19], v[100:103], v[60:63]\n"
        "v_mfma_f32_16x16x32_bf16 v[64:67], v[104:107], v[16:19], v[64:67]\n"
        "v_mfma_f32_16x16x32_bf16 v[68:71], v[108:111], v[16:19], v[68:71]\n"
        "s_waitcnt lgkmcnt(1)\n"
        "ds_read_b128 v[16:19], v0 offset:7168\n"
        "v_mfma_f32_16x16x32_bf16 v[48:51], v[112:115], v[8:11], v[48:51]\n"
        "v_mfma_f32_16x16x32_bf16 v[52:55], v[116:119], v[8:11], v[52:55]\n"
        "v_mfma_f32_16x16x32_bf16 v[56:59], v[8:11], v[120:123], v[56:59]\n"
        "v_mfma_f32_16x16x32_bf16 v[60:63], v[8:11], v[124:127], v[60:63]\n"
        "v_mfma_f32_16x16x32_bf16 v[64:67], v[128:131], v[8:11], v[64:67]\n"
        "v_mfma_f32_16x16x32_bf16 v[68:71], v[132:135], v[8:11], v[68:71]\n"
        "s_waitcnt lgkmcnt(1)\n"
        "ds_read_b128 v[8:11], v0 offset:7680\n"
        "v_mfma_f32_16x16x32_bf16 v[48:51], v[136:139], v[12:15], v[48:51]\n"
        "v_mfma_f32_16x16x32_bf16 v[52:55], v[140:143], v[12:15], v[52:55]\n"
        "v_mfma_f32_16x16x32_bf16 v[56:59], v[12:15], v[144:147], v[56:59]\n"
        "v_mfma_f32_16x16x32_bf16 v[60:63], v[12:15], v[148:151], v[60:63]\n"
        "v_mfma_f32_16x16x32_bf16 v[64:67], v[152:155], v[12:15], v[64:67]\n"
        "v_mfma_f32_16x16x32_bf16 v[68:71], v[156:159], v[12:15], v[68:71]\n"
        "s_waitcnt lgkmcnt(1)\n"
        "ds_read_b128 v[12:15], v0 offset:8192\n"
        "v_mfma_f32_16x16x32_bf16 v[48:51], v[160:163], v[16:19], v[48:51]\n"
        "v_mfma_f32_16x16x32_bf16 v[52:55], v[164:167], v[16:19], v[52:55]\n"
        "v_mfma_f32_16x16x32_bf16 v[56:59], v[16:19], v[168:171], v[56:59]\n"
        "v_mfma_f32_16x16x32_bf16 v[60:63], v[16:19], v[172:175], v[60:63]\n"
        "v_mfma_f32_16x16x32_bf16 v[64:67], v[176:179], v[16:19], v[64:67]\n"
        "v_mfma_f32_16x16x32_bf16 v[68:71], v[180:183], v[16:19], v[68:71]\n"
        "s_waitcnt lgkmcnt(1)\n"
        "ds_read_b128 v[16:19], v0 offset:8704\n"
        "v_mfma_f32_16x16x32_bf16 v[48:51], v[184:187], v[8:11], v[48:51]\n"
        "v_mfma_f32_16x16x32_bf16 v[52:55], v[188:191], v[8:11], v[52:55]\n"
        "v_mfma_f32_16x16x32_bf16 v[56:59], v[8:11], v[192:195], v[56:59]\n"
        "v_mfma_f32_16x16x32_bf16 v[60:63], v[8:11], v[196:199], v[60:63]\n"
        "v_mfma_f32_16x16x32_bf16 v[64:67], v[200:203], v[8:11], v[64:67]\n"
        "s_waitcnt vmcnt(3)\n"
        "s_barrier\n"
        "v_add_u32 v0, s78, v6\n"
        "v_mfma_f32_16x16x32_bf16 v[68:71], v[204:207], v[8:11], v[68:71]\n"
        "s_waitcnt lgkmcnt(1)\n"
        "ds_read_b128 v[8:11], v0 offset:0\n"
        "v_mfma_f32_16x16x32_bf16 v[48:51], v[208:211], v[12:15], v[48:51]\n"
        "v_mfma_f32_16x16x32_bf16 v[52:55], v[212:215], v[12:15], v[52:55]\n"
        "v_mfma_f32_16x16x32_bf16 v[56:59], v[12:15], v[216:219], v[56:59]\n"
        "v_mfma_f32_16x16x32_bf16 v[60:63], v[12:15], v[220:223], v[60:63]\n"
        "v_mfma_f32_16x16x32_bf16 v[64:67], v[224:227], v[12:15], v[64:67]\n"
        "v_mfma_f32_16x16x32_bf16 v[68:71], v[228:231], v[12:15], v[68:71]\n"
        "s_waitcnt lgkmcnt(1)\n"
        "ds_read_b128 v[12:15], v0 offset:512\n"
        "v_mfma_f32_16x16x32_bf16 v[48:51], v[232:235], v[16:19], v[48:51]\n"
        "v_mfma_f32_16x16x32_bf16 v[52:55], v[236:239], v[16:19], v[52:55]\n"
        "v_mfma_f32_16x16x32_bf16 v[56:59], v[16:19], v[240:243], v[56:59]\n"
        "v_mfma_f32_16x16x32_bf16 v[60:63], v[16:19], v[244:247], v[60:63]\n"
        "v_mfma_f32_16x16x32_bf16 v[64:67], v[248:251], v[16:19], v[64:67]\n"
        "v_mfma_f32_16x16x32_bf16 v[68:71], v[252:255], v[16:19], v[68:71]\n"
        "s_waitcnt lgkmcnt(1)\n"
        "ds_read_b128 v[16:19], v0 offset:1024\n"
        "s_branch .Ls1_i0_%=\n"
        ".p2align 5\n"
        ".Ls1_i10_%=:\n"
        "v_mfma_f32_16x16x32_bf16 v[56:59], v[16:19], a[128:131], v[56:59]\n"
        "buffer_load_dword v4, v2, s[40:43], s86 offen sc1\n"
        "v_mfma_f32_16x16x32_bf16 v[60:63], v[16:19], a[132:135], v[60:63]\n"
        "v_mfma_f32_16x16x32_bf16 v[64:67], a[136:139], v[16:19], v[64:67]\n"
        "v_mfma_f32_16x16x32_bf16 v[68:71], a[140:143], v[16:19], v[68:71]\n"
        "s_waitcnt lgkmcnt(1)\n"
        "ds_read_b128 v[16:19], v0 offset:4096\n"
        "v_mfma_f32_16x16x32_bf16 v[48:51], a[144:147], v[8:11], v[48:51]\n"
        "s_getreg_b32 s88, hwreg(HW_REG_IB_STS, 0, 4)\n"
        "v_mfma_f32_16x16x32_bf16 v[52:55], a[148:151], v[8:11], v[52:55]\n"
        "s_cmp_le_u32 s88, 2\n"
        "s_cselect_b32 s88, s73, s89\n"
        "s_cselect_b32 s73, s89, s73\n"
        "v_mfma_f32_16x16x32_bf16 v[56:59], v[8:11], a[152:155], v[56:59]\n"
        "buffer_store_dword v5, v3, s[40:43], s88 offen sc1\n"
        "v_mfma_f32_16x16x32_bf16 v[60:63], v[8:11], a[156:159], v[60:63]\n"
        "v_mfma_f32_16x16x32_bf16 v[64:67], a[160:163], v[8:11], v[64:67]\n"
        "v_mfma_f32_16x16x32_bf16 v[68:71], a[164:167], v[8:11], v[68:71]\n"
        "s_waitcnt lgkmcnt(1)\n"
        "ds_read_b128 v[8:11], v0 offset:4608\n"
        "v_mfma_f32_16x16x32_bf16 v[48:51], a[168:171], v[12:15], v[48:51]\n"
        "v_mfma_f32_16x16x32_bf16 v[52:55], a[172:175], v[12:15], v[52:55]\n"
        "v_mfma_f32_16x16x32_bf16 v[56:59], v[12:15], a[176:179], v[56:59]\n"
        "s_getreg_b32 s88, hwreg(HW_REG_IB_STS, 0, 4)\n"
        "v_mfma_f32_16x16x32_bf16 v[60:63], v[12:15], a[180:183], v[60:63]\n"
        "s_cmp_le_u32 s88, 3\n"
        "s_cselect_b32 s88, s73, s89\n"
        "s_cselect_b32 s73, s89, s73\n"
        "v_mfma_f32_16x16x32_bf16 v[64:67], a[184:187], v[12:15], v[64:67]\n"
        "buffer_store_dword v5, v3, s[40:43], s88 offen sc1\n"
        "v_mfma_f32_16x16x32_bf16 v[68:71], a[188:191], v[12:15], v[68:71]\n"
        "s_waitcnt lgkmcnt(1)\n"
        "ds_read_b128 v[12:15], v0 offset:5120\n"
        "v_mfma_f32_16x16x32_bf16 v[48:51], a[192:195], v[16:19], v[48:51]\n"
        "v_mfma_f32_16x16x32_bf16 v[52:55], a[196:199], v[16:19], v[52:55]\n"
        "v_mfma_f32_16x16x32_bf16 v[56:59], v[16:19], a[200:203], v[56:59]\n"
        "v_mfma_f32_16x16x32_bf16 v[60:63], v[16:19], a[204:207], v[60:63]\n"
        "v_mfma_f32_16x16x32_bf16 v[64:67], a[208:211], v[16:19], v[64:67]\n"
        "s_getreg_b32 s88, hwreg(HW_REG_IB_STS, 0, 4)\n"
        "v_mfma_f32_16x16x32_bf16 v[68:71], a[212:215], v[16:19], v[68:71]\n"
        "s_waitcnt lgkmcnt(1)\n"
        "ds_read_b128 v[16:19], v0 offset:5632\n"
        "v_mfma_f32_16x16x32_bf16 v[48:51], a[216:219], v[8:11], v[48:51]\n"
        "s_cmp_le_u32 s88, 4\n"
        "s_cselect_b32 s88, s73, s89\n"
        "s_cselect_b32 s73, s89, s73\n"
        "v_mfma_f32_16x16x32_bf16 v[52:55], a[220:223], v[8:11], v[52:55]\n"
        "buffer_store_dword v5, v3, s[40:43], s88 offen sc1\n"
        "v_mfma_f32_16x16x32_bf16 v[56:59], v[8:11], a[224:227], v[56:59]\n"
        "v_mfma_f32_16x16x32_bf16 v[60:63], v[8:11], a[228:231], v[60:63]\n"
        "v_mfma_f32_16x16x32_bf16 v[64:67], a[232:235], v[8:11], v[64:67]\n"
        "v_mfma_f32_16x16x32_bf16 v[68:71], a[236:239], v[8:11], v[68:71]\n"
        "s_waitcnt lgkmcnt(1)\n"
        "ds_read_b128 v[8:11], v0 offset:6144\n"
        "s_waitcnt vmcnt(3)\n"
        "v_cmp_gt_i32 vcc, s70, v4\n"
        "v_mfma_f32_16x16x32_bf16 v[48:51], a[240:243], v[12:15], v[48:51]\n"
        "v_mfma_f32_16x16x32_bf16 v[52:55], a[244:247], v[12:15], v[52:55]\n"
        "s_and_b64 vcc, vcc, 1\n"
        "v_mfma_f32_16x16x32_bf16 v[56:59], v[12:15], a[248:251], v[56:59]\n"
        "s_cbranch_vccnz .Ls1_i11_%=\n"
        "v_mfma_f32_16x16x32_bf16 v[60:63], v[12:15], a[252:255], v[60:63]\n"
        "buffer_load_dwordx4 v1, s[40:43], s71 offen offset:0 sc1 lds\n"
        "v_mfma_f32_16x16x32_bf16 v[64:67], v[80:83], v[12:15], v[64:67]\n"
        "v_mfma_f32_16x16x32_bf16 v[68:71], v[84:87], v[12:15], v[68:71]\n"
        "s_waitcnt lgkmcnt(1)\n"
        "ds_read_b128 v[12:15], v0 offset:6656\n"
        "v_mfma_f32_16x16x32_bf16 v[48:51], v[88:91], v[16:19], v[48:51]\n"
        "buffer_load_dwordx4 v1, s[40:43], s71 offen offset:1024 sc1 lds\n"
        "v_mfma_f32_16x16x32_bf16 v[52:55], v[92:95], v[16:19], v[52:55]\n"
        "buffer_store_dword v5, v3, s[40:43], s73 offen sc1\n"
        "v_mfma_f32_16x16x32_bf16 v[56:59], v[16:19], v[96:99], v[56:59]\n"
        "s_mov_b32 s84, s89\n"
        "s_mov_b32 s73, s60\n"
        "s_add_u32 s74, s74, 1\n"
        "v_mfma_f32_16x16x32_bf16 v[60:63], v[16:19], v[100:103], v[60:63]\n"
        "s_add_u32 s72, s75, s59\n"
        "buffer_load_dwordx4 v1, s[40:43], s71 offen offset:2048 sc1 lds\n"
        "v_mfma_f32_16x16x32_bf16 v[64:67], v[104:107], v[16:19], v[64:67]\n"
        "v_mfma_f32_16x16x32_bf16 v[68:71], v[108:111], v[16:19], v[68:71]\n"
        "s_waitcnt lgkmcnt(1)\n"
        "ds_read_b128 v[16:19], v0 offset:7168\n"
        "v_mfma_f32_16x16x32_bf16 v[48:51], v[112:115], v[8:11], v[48:51]\n"
        "buffer_load_dwordx4 v1, s[40:43], s71 offen offset:3072 sc1 lds\n"
        "v_mfma_f32_16x16x32_bf16 v[52:55], v[116:119], v[8:11], v[52:55]\n"
        "s_add_u32 m0, s78, s85\n"
        "s_add_u32 s69, s69, 1\n"
        "v_mfma_f32_16x16x32_bf16 v[56:59], v[8:11], v[120:123], v[56:59]\n"
        "v_mfma_f32_16x16x32_bf16 v[60:63], v[8:11], v[124:127], v[60:63]\n"
        "buffer_load_dwordx4 v1, s[40:43], s81 offen offset:0 sc1 lds\n"
        "v_mfma_f32_16x16x32_bf16 v[64:67], v[128:131], v[8:11], v[64:67]\n"
        "v_mfma_f32_16x16x32_bf16 v[68:71], v[132:135], v[8:11], v[68:71]\n"
        "s_waitcnt lgkmcnt(1)\n"
        "ds_read_b128 v[8:11], v0 offset:7680\n"
        "v_mfma_f32_16x16x32_bf16 v[48:51], v[136:139], v[12:15], v[48:51]\n"
        "v_mfma_f32_16x16x32_bf16 v[52:55], v[140:143], v[12:15], v[52:55]\n"
        "v_mfma_f32_16x16x32_bf16 v[56:59], v[12:15], v[144:147], v[56:59]\n"
        "v_mfma_f32_16x16x32_bf16 v[60:63], v[12:15], v[148:151], v[60:63]\n"
        "v_mfma_f32_16x16x32_bf16 v[64:67], v[152:155], v[12:15], v[64:67]\n"
        "v_mfma_f32_16x16x32_bf16 v[68:71], v[156:159], v[12:15], v[68:71]\n"
        "s_waitcnt lgkmcnt(1)\n"
        "ds_read_b128 v[12:15], v0 offset:8192\n"
        "v_mfma_f32_16x16x32_bf16 v[48:51], v[160:163], v[16:19], v[48:51]\n"
        "v_mfma_f32_16x16x32_bf16 v[52:55], v[164:167], v[16:19], v[52:55]\n"
        "v_mfma_f32_16x16x32_bf16 v[56:59], v[16:19], v[168:171], v[56:59]\n"
        "v_mfma_f32_16x16x32_bf16 v[60:63], v[16:19], v[172:175], v[60:63]\n"
        "v_mfma_f32_16x16x32_bf16 v[64:67], v[176:179], v[16:19], v[64:67]\n"
        "v_mfma_f32_16x16x32_bf16 v[68:71], v[180:183], v[16:19], v[68:71]\n"
        "s_waitcnt lgkmcnt(1)\n"
        "ds_read_b128 v[16:19], v0 offset:8704\n"
        "v_mfma_f32_16x16x32_bf16 v[48:51], v[184:187], v[8:11], v[48:51]\n"
        "v_mfma_f32_16x16x32_bf16 v[52:55], v[188:191], v[8:11], v[52:55]\n"
        "v_mfma_f32_16x16x32_bf16 v[56:59], v[8:11], v[192:195], v[56:59]\n"
        "v_mfma_f32_16x16x32_bf16 v[60:63], v[8:11], v[196:199], v[60:63]\n"
        "v_mfma_f32_16x16x32_bf16 v[64:67], v[200:203], v[8:11], v[64:67]\n"
        "s_waitcnt vmcnt(0)\n"
        "s_barrier\n"
        "v_add_u32 v0, s78, v6\n"
        "v_mfma_f32_16x16x32_bf16 v[68:71], v[204:207], v[8:11], v[68:71]\n"
        "s_waitcnt lgkmcnt(1)\n"
        "ds_read_b128 v[8:11], v0 offset:0\n"
        "v_mfma_f32_16x16x32_bf16 v[48:51], v[208:211], v[12:15], v[48:51]\n"
        "v_mfma_f32_16x16x32_bf16 v[52:55], v[212:215], v[12:15], v[52:55]\n"
        "v_mfma_f32_16x16x32_bf16 v[56:59], v[12:15], v[216:219], v[56:59]\n"
        "v_mfma_f32_16x16x32_bf16 v[60:63], v[12:15], v[220:223], v[60:63]\n"
        "v_mfma_f32_16x16x32_bf16 v[64:67], v[224:227], v[12:15], v[64:67]\n"
        "v_mfma_f32_16x16x32_bf16 v[68:71], v[228:231], v[12:15], v[68:71]\n"
        "s_waitcnt lgkmcnt(1)\n"
        "ds_read_b128 v[12:15], v0 offset:512\n"
        "v_mfma_f32_16x16x32_bf16 v[48:51], v[232:235], v[16:19], v[48:51]\n"
        "v_mfma_f32_16x16x32_bf16 v[52:55], v[236:239], v[16:19], v[52:55]\n"
        "v_mfma_f32_16x16x32_bf16 v[56:59], v[16:19], v[240:243], v[56:59]\n"
        "v_mfma_f32_16x16x32_bf16 v[60:63], v[16:19], v[244:247], v[60:63]\n"
        "v_mfma_f32_16x16x32_bf16 v[64:67], v[248:251], v[16:19], v[64:67]\n"
        "v_mfma_f32_16x16x32_bf16 v[68:71], v[252:255], v[16:19], v[68:71]\n"
        "s_waitcnt lgkmcnt(1)\n"
        "ds_read_b128 v[16:19], v0 offset:1024\n"
        "s_branch .Ls1_i0_%=\n"
        ".p2align 5\n"
        ".Ls1_i11_%=:\n"
        "v_mfma_f32_16x16x32_bf16 v[60:63], v[12:15], a[252:255], v[60:63]\n"
        "buffer_load_dword v4, v2, s[40:43], s86 offen sc1\n"
        "v_mfma_f32_16x16x32_bf16 v[64:67], v[80:83], v[12:15], v[64:67]\n"
        "v_mfma_f32_16x16x32_bf16 v[68:71], v[84:87], v[12:15], v[68:71]\n"
        "s_waitcnt lgkmcnt(1)\n"
        "ds_read_b128 v[12:15], v0 offset:6656\n"
        "v_mfma_f32_16x16x32_bf16 v[48:51], v[88:91], v[16:19], v[48:51]\n"
        "buffer_store_dword v5, v3, s[40:43], s73 offen sc1\n"
        "v_mfma_f32_16x16x32_bf16 v[52:55], v[92:95], v[16:19], v[52:55]\n"
        "s_mov_b32 s84, s89\n"
        "s_mov_b32 s73, s60\n"
        "s_add_u32 s74, s74, 1\n"
        "v_mfma_f32_16x16x32_bf16 v[56:59], v[16:19], v[96:99], v[56:59]\n"
        "s_add_u32 s72, s75, s59\n"
        "v_mfma_f32_16x16x32_bf16 v[60:63], v[16:19], v[100:103], v[60:63]\n"
        "v_mfma_f32_16x16x32_bf16 v[64:67], v[104:107], v[16:19], v[64:67]\n"
        "v_mfma_f32_16x16x32_bf16 v[68:71], v[108:111], v[16:19], v[68:71]\n"
        "s_waitcnt lgkmcnt(1)\n"
        "ds_read_b128 v[16:19], v0 offset:7168\n"
        "v_mfma_f32_16x16x32_bf16 v[48:51], v[112:115], v[8:11], v[48:51]\n"
        "v_mfma_f32_16x16x32_bf16 v[52:55], v[116:119], v[8:11], v[52:55]\n"
        "v_mfma_f32_16x16x32_bf16 v[56:59], v[8:11], v[120:123], v[56:59]\n"
        "v_mfma_f32_16x16x32_bf16 v[60:63], v[8:11], v[124:127], v[60:63]\n"
        "v_mfma_f32_16x16x32_bf16 v[64:67], v[128:131], v[8:11], v[64:67]\n"
        "v_mfma_f32_16x16x32_bf16 v[68:71], v[132:135], v[8:11], v[68:71]\n"
        "s_waitcnt lgkmcnt(1)\n"
        "ds_read_b128 v[8:11], v0 offset:7680\n"
        "v_mfma_f32_16x16x32_bf16 v[48:51], v[136:139], v[12:15], v[48:51]\n"
        "v_mfma_f32_16x16x32_bf16 v[52:55], v[140:143], v[12:15], v[52:55]\n"
        "v_mfma_f32_16x16x32_bf16 v[56:59], v[12:15], v[144:147], v[56:59]\n"
        "v_mfma_f32_16x16x32_bf16 v[60:63], v[12:15], v[148:151], v[60:63]\n"
        "v_mfma_f32_16x16x32_bf16 v[64:67], v[152:155], v[12:15], v[64:67]\n"
        "v_mfma_f32_16x16x32_bf16 v[68:71], v[156:159], v[12:15], v[68:71]\n"
        "s_waitcnt lgkmcnt(1)\n"
        "ds_read_b128 v[12:15], v0 offset:8192\n"
        "v_mfma_f32_16x16x32_bf16 v[48:51], v[160:163], v[16:19], v[48:51]\n"
        "v_mfma_f32_16x16x32_bf16 v[52:55], v[164:167], v[16:19], v[52:55]\n"
        "v_mfma_f32_16x16x32_bf16 v[56:59], v[16:19], v[168:171], v[56:59]\n"
        "v_mfma_f32_16x16x32_bf16 v[60:63], v[16:19], v[172:175], v[60:63]\n"
        "v_mfma_f32_16x16x32_bf16 v[64:67], v[176:179], v[16:19], v[64:67]\n"
        "v_mfma_f32_16x16x32_bf16 v[68:71], v[180:183], v[16:19], v[68:71]\n"
        "s_waitcnt lgkmcnt(1)\n"
        "ds_read_b128 v[16:19], v0 offset:8704\n"
        "v_mfma_f32_16x16x32_bf16 v[48:51], v[184:187], v[8:11], v[48:51]\n"
        "s_waitcnt vmcnt(1)\n"
        "v_cmp_gt_i32 vcc, s70, v4\n"
        "v_mfma_f32_16x16x32_bf16 v[52:55], v[188:191], v[8:11], v[52:55]\n"
        "v_mfma_f32_16x16x32_bf16 v[56:59], v[8:11], v[192:195], v[56:59]\n"
        "s_and_b64 vcc, vcc, 1\n"
        "v_mfma_f32_16x16x32_bf16 v[60:63], v[8:11], v[196:199], v[60:63]\n"
        "s_cbranch_vccnz .Ls1_i12_%=\n"
        "v_mfma_f32_16x16x32_bf16 v[64:67], v[200:203], v[8:11], v[64:67]\n"
        "buffer_load_dwordx4 v1, s[40:43], s71 offen offset:0 sc1 lds\n"
        "v_mfma_f32_16x16x32_bf16 v[68:71], v[204:207], v[8:11], v[68:71]\n"
        "s_waitcnt lgkmcnt(1)\n"
        "v_mfma_f32_16x16x32_bf16 v[48:51], v[208:211], v[12:15], v[48:51]\n"
        "buffer_load_dwordx4 v1, s[40:43], s71 offen offset:1024 sc1 lds\n"
        "v_mfma_f32_16x16x32_bf16 v[52:55], v[212:215], v[12:15], v[52:55]\n"
        "v_mfma_f32_16x16x32_bf16 v[56:59], v[12:15], v[216:219], v[56:59]\n"
        "buffer_load_dwordx4 v1, s[40:43], s71 offen offset:2048 sc1 lds\n"
        "v_mfma_f32_16x16x32_bf16 v[60:63], v[12:15], v[220:223], v[60:63]\n"
        "v_mfma_f32_16x16x32_bf16 v[64:67], v[224:227], v[12:15], v[64:67]\n"
        "buffer_load_dwordx4 v1, s[40:43], s71 offen offset:3072 sc1 lds\n"
        "v_mfma_f32_16x16x32_bf16 v[68:71], v[228:231], v[12:15], v[68:71]\n"
        "s_waitcnt lgkmcnt(0)\n"
        "s_add_u32 m0, s78, s85\n"
        "s_add_u32 s69, s69, 1\n"
        "v_mfma_f32_16x16x32_bf16 v[48:51], v[232:235], v[16:19], v[48:51]\n"
        "buffer_load_dwordx4 v1, s[40:43], s81 offen offset:0 sc1 lds\n"
        "v_mfma_f32_16x16x32_bf16 v[52:55], v[236:239], v[16:19], v[52:55]\n"
        "v_mfma_f32_16x16x32_bf16 v[56:59], v[16:19], v[240:243], v[56:59]\n"
        "v_mfma_f32_16x16x32_bf16 v[60:63], v[16:19], v[244:247], v[60:63]\n"
        "v_mfma_f32_16x16x32_bf16 v[64:67], v[248:251], v[16:19], v[64:67]\n"
        "v_mfma_f32_16x16x32_bf16 v[68:71], v[252:255], v[16:19], v[68:71]\n"
        "v_cvt_pk_bf16_f32 v48, v48, v49\n"
        "v_cvt_pk_bf16_f32 v49, v50, v51\n"
        "v_cvt_pk_bf16_f32 v50, v52, v53\n"
        "v_cvt_pk_bf16_f32 v51, v54, v55\n"
        "s_add_u32 s66, s72, 524288\n"
        "buffer_store_dwordx4 v[48:51], v1, s[40:43], s66 offen offset:1024 sc1\n"
        "v_cvt_pk_bf16_f32 v74, v56, v57\n"
        "v_cvt_pk_bf16_f32 v75, v58, v59\n"
        "s_add_u32 s66, s72, 1048576\n"
        "buffer_store_dwordx4 v[72:75], v1, s[40:43], s66 offen offset:0 sc1\n"
        "v_cvt_pk_bf16_f32 v78, v60, v61\n"
        "v_cvt_pk_bf16_f32 v79, v62, v63\n"
        "buffer_store_dwordx4 v[76:79], v1, s[40:43], s66 offen offset:1024 sc1\n"
        "v_cvt_pk_bf16_f32 v64, v64, v65\n"
        "v_cvt_pk_bf16_f32 v65, v66, v67\n"
        "v_cvt_pk_bf16_f32 v66, v68, v69\n"
        "v_cvt_pk_bf16_f32 v67, v70, v71\n"
        "buffer_store_dwordx4 v[64:67], v1, s[40:43], s72 offen offset:1024 sc1\n"
        "v_mov_b32 v5, s74\n"
        "s_mov_b32 s84, s60\n"
        "s_waitcnt vmcnt(4)\n"
        "s_branch .Ls1_go1_%=\n"
        ".p2align 5\n"
        ".Ls1_i12_%=:\n"
        "v_mfma_f32_16x16x32_bf16 v[64:67], v[200:203], v[8:11], v[64:67]\n"
        "buffer_load_dword v4, v2, s[40:43], s86 offen sc1\n"
        "v_mfma_f32_16x16x32_bf16 v[68:71], v[204:207], v[8:11], v[68:71]\n"
        "s_waitcnt lgkmcnt(1)\n"
        "v_mfma_f32_16x16x32_bf16 v[48:51], v[208:211], v[12:15], v[48:51]\n"
        "v_mfma_f32_16x16x32_bf16 v[52:55], v[212:215], v[12:15], v[52:55]\n"
        "v_mfma_f32_16x16x32_bf16 v[56:59], v[12:15], v[216:219], v[56:59]\n"
        "v_mfma_f32_16x16x32_bf16 v[60:63], v[12:15], v[220:223], v[60:63]\n"
        "v_mfma_f32_16x16x32_bf16 v[64:67], v[224:227], v[12:15], v[64:67]\n"
        "v_mfma_f32_16x16x32_bf16 v[68:71], v[228:231], v[12:15], v[68:71]\n"
        "s_waitcnt lgkmcnt(0)\n"
        "v_mfma_f32_16x16x32_bf16 v[48:51], v[232:235], v[16:19], v[48:51]\n"
        "v_mfma_f32_16x16x32_bf16 v[52:55], v[236:239], v[16:19], v[52:55]\n"
        "v_mfma_f32_16x16x32_bf16 v[56:59], v[16:19], v[240:243], v[56:59]\n"
        "v_mfma_f32_16x16x32_bf16 v[60:63], v[16:19], v[244:247], v[60:63]\n"
        "v_mfma_f32_16x16x32_bf16 v[64:67], v[248:251], v[16:19], v[64:67]\n"
        "v_mfma_f32_16x16x32_bf16 v[68:71], v[252:255], v[16:19], v[68:71]\n"
        "v_cvt_pk_bf16_f32 v48, v48, v49\n"
        "v_cvt_pk_bf16_f32 v49, v50, v51\n"
        "v_cvt_pk_bf16_f32 v50, v52, v53\n"
        "v_cvt_pk_bf16_f32 v51, v54, v55\n"
        "s_add_u32 s66, s72, 524288\n"
        "buffer_store_dwordx4 v[48:51], v1, s[40:43], s66 offen offset:1024 sc1\n"
        "v_cvt_pk_bf16_f32 v74, v56, v57\n"
        "v_cvt_pk_bf16_f32 v75, v58, v59\n"
        "s_add_u32 s66, s72, 1048576\n"
        "buffer_store_dwordx4 v[72:75], v1, s[40:43], s66 offen offset:0 sc1\n"
        "v_cvt_pk_bf16_f32 v78, v60, v61\n"
        "v_cvt_pk_bf16_f32 v79, v62, v63\n"
        "buffer_store_dwordx4 v[76:79], v1, s[40:43], s66 offen offset:1024 sc1\n"
        "v_cvt_pk_bf16_f32 v64, v64, v65\n"
        "v_cvt_pk_bf16_f32 v65, v66, v67\n"
        "v_cvt_pk_bf16_f32 v66, v68, v69\n"
        "v_cvt_pk_bf16_f32 v67, v70, v71\n"
        "buffer_store_dwordx4 v[64:67], v1, s[40:43], s72 offen offset:1024 sc1\n"
        "s_waitcnt vmcnt(4)\n"
        "v_cmp_gt_i32 vcc, s70, v4\n"
        "s_nop 0\n"
        "s_and_b64 vcc, vcc, 1\n"
        "s_cbranch_vccnz .Ls1_late1_%=\n"
        "buffer_load_dwordx4 v1, s[40:43], s71 offen offset:0 sc1 lds\n"
        "buffer_load_dwordx4 v1, s[40:43], s71 offen offset:1024 sc1 lds\n"
        "buffer_load_dwordx4 v1, s[40:43], s71 offen offset:2048 sc1 lds\n"
        "buffer_load_dwordx4 v1, s[40:43], s71 offen offset:3072 sc1 lds\n"
        "s_add_u32 m0, s78, s85\n"
        "s_add_u32 s69, s69, 1\n"
        "buffer_load_dwordx4 v1, s[40:43], s81 offen offset:0 sc1 lds\n"
        "s_waitcnt vmcnt(5)\n"
        "v_mov_b32 v5, s74\n"
        "buffer_store_dword v5, v3, s[40:43], s73 offen sc1\n"
        "s_waitcnt vmcnt(1)\n"
        "s_branch .Ls1_go1_%=\n"
        ".p2align 5\n"
        ".Ls1_late1_%=:\n"
        "s_waitcnt vmcnt(0)\n"
        "v_mov_b32_e64 v5, s74\n"
        "buffer_store_dword v5, v3, s[40:43], s73 offen sc1\n"
        "s_cmp_eq_u32 s69, 128\n"
        "s_nop 0\n"
        "s_cbranch_scc0 .Ls1_wait1_%=\n"
        "s_endpgm\n"
        ".p2align 5\n"
        ".Ls1_wait1_%=:\n"
        "s_mov_b32 s64, 0\n"
        ".Ls1_in1_top_%=:\n"
        "buffer_load_dword v4, v2, s[40:43], s86 offen sc1\n"
        "s_waitcnt vmcnt(0)\n"
        "v_cmp_gt_i32_e64 vcc, s70, v4\n"
        "s_nop 0\n"
        "s_and_b64 vcc, vcc, 1\n"
        "s_cbranch_vccz .Ls1_in1_ok_%=\n"
        "s_add_u32 s64, s64, 1\n"
        "s_and_b32 s65, s64, 255\n"
        "s_cbranch_scc1 .Ls1_in1_go_%=\n"
        "s_mov_b32 s65, 49152\n"
        "s_cmp_lt_u32 s64, 1048576\n"
        "s_cbranch_scc1 .Ls1_in1_chk_%=\n"
        "v_mov_b32 v4, 1\n"
        "buffer_store_dword v4, off, s[40:43], s65 sc1\n"
        "s_waitcnt vmcnt(0)\n"
        ".Ls1_in1_chk_%=:\n"
        "buffer_load_dword v4, off, s[40:43], s65 sc1\n"
        "s_waitcnt vmcnt(0)\n"
        "v_readfirstlane_b32 s65, v4\n"
        "s_nop 0\n"
        "s_cmp_lg_u32 s65, 1\n"
        "s_cbranch_scc1 .Ls1_in1_go_%=\n"
        "s_endpgm\n"
        ".p2align 5\n"
        ".Ls1_in1_go_%=:\n"
        "s_branch .Ls1_in1_top_%=\n"
        ".p2align 5\n"
        ".Ls1_in1_ok_%=:\n"
        "buffer_load_dwordx4 v1, s[40:43], s71 offen offset:0 sc1 lds\n"
        "buffer_load_dwordx4 v1, s[40:43], s71 offen offset:1024 sc1 lds\n"
        "buffer_load_dwordx4 v1, s[40:43], s71 offen offset:2048 sc1 lds\n"
        "buffer_load_dwordx4 v1, s[40:43], s71 offen offset:3072 sc1 lds\n"
        "s_add_u32 m0, s78, s85\n"
        "s_add_u32 s69, s69, 1\n"
        "buffer_load_dwordx4 v1, s[40:43], s81 offen offset:0 sc1 lds\n"
        "s_waitcnt vmcnt(0)\n"
        ".Ls1_go1_%=:\n"
        "s_mov_b32 s72, 241172480\n"
        "s_mov_b32 s83, 241172480\n"
        "s_mov_b32 s73, 241172480\n"
        "v_add_u32 v0, s78, v6\n"
        "s_barrier\n"
        "ds_read_b128 v[8:11], v0 offset:0\n"
        "ds_read_b128 v[12:15], v0 offset:512\n"
        "ds_read_b128 v[16:19], v0 offset:1024\n"
        "s_waitcnt lgkmcnt(2)\n"
        "s_branch .Ls1_i0_%=\n"
        ".Ls1_end_%=:\n"
        :: "s"(p.ws), "s"(p.Wk), "s"(p.Wv), "s"(p.Wq), "s"(pair(w, delay)), "s"(pair(x4, pr)), TILE_ARGS(PAGE + spread(A_XN)),
           "v"(pair(lane == 0 ? flag(F_XN + 8 * h + w) : OOB, lane < 2 ? lane * 32768 : OOB)), "v"(pair(0, 0)), "v"(pair(0, 0)),
           "v"(pair(vo + (i ^ (2 * g)) * 16, vo + (i ^ (2 * g + 8)) * 16)), "v"(pair(t0, t1)), "v"(pair(t0 + 81920, t1 + 81920))
        : "v0", "v1", "v2", "v3", "v4", "v5", "v6", "v7", "v8", "v9", "v10", "v11", "v12", "v13", "v14", "v15", "v16", "v17", "v18", "v19", "v20", "v21", "v22", "v23", "v24", "v25", "v26", "v27", "v28", "v29", "v30", "v31", "v32", "v33", "v34", "v35", "v36", "v37", "v38", "v39", "v40", "v41", "v42", "v43", "v44", "v45", "v46", "v47", "v48", "v49", "v50", "v51", "v52", "v53", "v54", "v55", "v56", "v57", "v58", "v59", "v60", "v61", "v62", "v63", "v64", "v65", "v66", "v67", "v68", "v69", "v70", "v71", "v72", "v73", "v74", "v75", "v76", "v77", "v78", "v79", "v255", "a255", "s26", "s27", "s28", "s29", "s30", "s31", "s32", "s33", "s34", "s35", "s36", "s37", "s38", "s39", "s40", "s41", "s42", "s43", "s44", "s45", "s46", "s47", "s48", "s49", "s50", "s51", "s52", "s53", "s54", "s55", "s56", "s57", "s58", "s59", "s60", "s61", "s62", "s63", "s64", "s65", "s66", "s67", "s68", "s69", "s70", "s71", "s72", "s73", "s74", "s75", "s76", "s77", "s78", "s79", "s80", "s81", "s82", "s83", "s84", "s85", "s86", "s87", "s88", "s89", "s90", "s91", "s92", "s93", "s94", "s95", "s96", "s97", "s98", "s99", "vcc", "scc", "memory");
    __builtin_unreachable();
}
__device__ __forceinline__ void role_f1(const Params& p, int kap, int jj, int x4, int pr, int delay) {
    GEMM_HEAD;
    const unsigned u = jj * 4 + w;
    unsigned vo[3];
#pragma unroll
    for (int n = 0; n < 3; ++n) {
        const unsigned ps = (64 * n + lane) / 6;
        vo[n] = (8 * ((ps >> 2) & 3) + 4 * (ps >> 4) + (ps & 3)) * 9216 + (1152 * kap + 48 * u) * 2 + ((64 * n + lane) % 6) * 16 - 1024 * n;
    }
    const unsigned hb = 65 * PAGE + spread(B_H + kap * 73728);
    const unsigned out1 = hb + spread(u * 1024), out2 = hb + (6 + (u >> 3)) * 16384 + (u & 7) * 512, fout = flag(F_F1 + 24 * kap + u);
    asm volatile(
        "s_nop 4\n"
        "s_mov_b64 s[40:41], %0\n"
        "s_and_b32 s41, s41, 0xffff\n"
        "s_mov_b32 s42, 236978176\n"
        "s_mov_b32 s43, 0x20000\n"
        "s_mov_b64 s[48:49], %4\n"
        "s_mov_b64 s[56:57], %5\n"
        "s_mov_b64 s[58:59], %6\n"
        "s_mov_b64 s[60:61], %7\n"
        "s_mov_b64 s[62:63], %8\n"
        "v_mov_b64 v[0:1], %9\n"
        "v_mov_b64 v[2:3], %10\n"
        "v_mov_b64 v[4:5], %11\n"
        "v_mov_b64 v[6:7], %12\n"
        "v_mov_b64 v[8:9], %13\n"
        "v_mov_b64 v[10:11], %14\n"
        "s_mov_b32 s47, 0x20000\n"
        "s_mul_i32 s79, s48, 40960\n"
        ".Lf1_start_%=:\n"
        "s_sub_u32 s49, s49, 1\n"
        "s_cbranch_scc1 .Lf1_start2_%=\n"
        "s_sleep 16\n"
        "s_branch .Lf1_start_%=\n"
        ".Lf1_start2_%=:\n"
        "s_mov_b64 s[44:45], %1\n"
        "s_and_b32 s45, s45, 0xffff\n"
        "s_mov_b32 s46, 5308416\n"
        "s_add_u32 m0, s79, 0\n"
        "s_mov_b32 s65, 0\n"
        "buffer_load_dwordx4 v8, s[44:47], s65 offen lds\n"
        "buffer_load_dwordx4 v9, s[44:47], s65 offen offset:1024 lds\n"
        "buffer_load_dwordx4 v10, s[44:47], s65 offen offset:2048 lds\n"
        "s_add_u32 m0, s79, 3072\n"
        "s_mov_b32 s65, 294912\n"
        "buffer_load_dwordx4 v8, s[44:47], s65 offen lds\n"
        "buffer_load_dwordx4 v9, s[44:47], s65 offen offset:1024 lds\n"
        "buffer_load_dwordx4 v10, s[44:47], s65 offen offset:2048 lds\n"
        "s_waitcnt vmcnt(3)\n"
        "ds_read_b64_tr_b16 a[0:1], v11 offset:0\n"
        "ds_read_b64_tr_b16 a[2:3], v11 offset:1536\n"
        "ds_read_b64_tr_b16 a[8:9], v11 offset:32\n"
        "ds_read_b64_tr_b16 a[10:11], v11 offset:1568\n"
        "ds_read_b64_tr_b16 a[16:17], v11 offset:64\n"
        "ds_read_b64_tr_b16 a[18:19], v11 offset:1600\n"
        "s_waitcnt vmcnt(0)\n"
        "ds_read_b64_tr_b16 a[24:25], v11 offset:3072\n"
        "ds_read_b64_tr_b16 a[26:27], v11 offset:4608\n"
        "ds_read_b64_tr_b16 a[32:33], v11 offset:3104\n"
        "ds_read_b64_tr_b16 a[34:35], v11 offset:4640\n"
        "ds_read_b64_tr_b16 a[40:41], v11 offset:3136\n"
        "ds_read_b64_tr_b16 a[42:43], v11 offset:4672\n"
        "s_waitcnt lgkmcnt(6)\n"
        "s_add_u32 m0, s79, 0\n"
        "s_mov_b32 s65, 589824\n"
        "buffer_load_dwordx4 v8, s[44:47], s65 offen lds\n"
        "buffer_load_dwordx4 v9, s[44:47], s65 offen offset:1024 lds\n"
        "buffer_load_dwordx4 v10, s[44:47], s65 offen offset:2048 lds\n"
        "s_waitcnt vmcnt(0)\n"
        "ds_read_b64_tr_b16 a[48:49], v11 offset:0\n"
        "ds_read_b64_tr_b16 a[50:51], v11 offset:1536\n"
        "ds_read_b64_tr_b16 a[56:57], v11 offset:32\n"
        "ds_read_b64_tr_b16 a[58:59], v11 offset:1568\n"
        "ds_read_b64_tr_b16 a[64:65], v11 offset:64\n"
        "ds_read_b64_tr_b16 a[66:67], v11 offset:1600\n"
        "s_waitcnt lgkmcnt(6)\n"
        "s_add_u32 m0, s79, 3072\n"
        "s_mov_b32 s65, 884736\n"
        "buffer_load_dwordx4 v8, s[44:47], s65 offen lds\n"
        "buffer_load_dwordx4 v9, s[44:47], s65 offen offset:1024 lds\n"
        "buffer_load_dwordx4 v10, s[44:47], s65 offen offset:2048 lds\n"
        "s_waitcnt vmcnt(0)\n"
        "ds_read_b64_tr_b16 a[72:73], v11 offset:3072\n"
        "ds_read_b64_tr_b16 a[74:75], v11 offset:4608\n"
        "ds_read_b64_tr_b16 a[80:81], v11 offset:3104\n"
        "ds_read_b64_tr_b16 a[82:83], v11 offset:4640\n"
        "ds_read_b64_tr_b16 a[88:89], v11 offset:3136\n"
        "ds_read_b64_tr_b16 a[90:91], v11 offset:4672\n"
        "s_waitcnt lgkmcnt(6)\n"
        "s_add_u32 m0, s79, 0\n"
        "s_mov_b32 s65, 1179648\n"
        "buffer_load_dwordx4 v8, s[44:47], s65 offen lds\n"
        "buffer_load_dwordx4 v9, s[44:47], s65 offen offset:1024 lds\n"
        "buffer_load_dwordx4 v10, s[44:47], s65 offen offset:2048 lds\n"
        "s_waitcnt vmcnt(0)\n"
        "ds_read_b64_tr_b16 a[96:97], v11 offset:0\n"
        "ds_read_b64_tr_b16 a[98:99], v11 offset:1536\n"
        "ds_read_b64_tr_b16 a[104:105], v11 offset:32\n"
        "ds_read_b64_tr_b16 a[106:107], v11 offset:1568\n"
        "ds_read_b64_tr_b16 a[112:113], v11 offset:64\n"
        "ds_read_b64_tr_b16 a[114:115], v11 offset:1600\n"
        "s_waitcnt lgkmcnt(6)\n"
        "s_add_u32 m0, s79, 3072\n"
        "s_mov_b32 s65, 1474560\n"
        "buffer_load_dwordx4 v8, s[44:47], s65 offen lds\n"
        "buffer_load_dwordx4 v9, s[44:47], s65 offen offset:1024 lds\n"
        "buffer_load_dwordx4 v10, s[44:47], s65 offen offset:2048 lds\n"
        "s_waitcnt vmcnt(0)\n"
        "ds_read_b64_tr_b16 a[120:121], v11 offset:3072\n"
        "ds_read_b64_tr_b16 a[122:123], v11 offset:4608\n"
        "ds_read_b64_tr_b16 a[128:129], v11 offset:3104\n"
        "ds_read_b64_tr_b16 a[130:131], v11 offset:4640\n"
        "ds_read_b64_tr_b16 a[136:137], v11 offset:3136\n"
        "ds_read_b64_tr_b16 a[138:139], v11 offset:4672\n"
        "s_waitcnt lgkmcnt(6)\n"
        "s_add_u32 m0, s79, 0\n"
        "s_mov_b32 s65, 1769472\n"
        "buffer_load_dwordx4 v8, s[44:47], s65 offen lds\n"
        "buffer_load_dwordx4 v9, s[44:47], s65 offen offset:1024 lds\n"
        "buffer_load_dwordx4 v10, s[44:47], s65 offen offset:2048 lds\n"
        "s_waitcnt vmcnt(0)\n"
        "ds_read_b64_tr_b16 a[144:145], v11 offset:0\n"
        "ds_read_b64_tr_b16 a[146:147], v11 offset:1536\n"
        "ds_read_b64_tr_b16 a[152:153], v11 offset:32\n"
        "ds_read_b64_tr_b16 a[154:155], v11 offset:1568\n"
        "ds_read_b64_tr_b16 a[160:161], v11 offset:64\n"
        "ds_read_b64_tr_b16 a[162:163], v11 offset:1600\n"
        "s_waitcnt lgkmcnt(6)\n"
        "s_add_u32 m0, s79, 3072\n"
        "s_mov_b32 s65, 2064384\n"
        "buffer_load_dwordx4 v8, s[44:47], s65 offen lds\n"
        "buffer_load_dwordx4 v9, s[44:47], s65 offen offset:1024 lds\n"
        "buffer_load_dwordx4 v10, s[44:47], s65 offen offset:2048 lds\n"
        "s_waitcnt vmcnt(0)\n"
        "ds_read_b64_tr_b16 a[168:169], v11 offset:3072\n"
        "ds_read_b64_tr_b16 a[170:171], v11 offset:4608\n"
        "ds_read_b64_tr_b16 a[176:177], v11 offset:3104\n"
        "ds_read_b64_tr_b16 a[178:179], v11 offset:4640\n"
        "ds_read_b64_tr_b16 a[184:185], v11 offset:3136\n"
        "ds_read_b64_tr_b16 a[186:187], v11 offset:4672\n"
        "s_waitcnt lgkmcnt(6)\n"
        "s_add_u32 m0, s79, 0\n"
        "s_mov_b32 s65, 2359296\n"
        "buffer_load_dwordx4 v8, s[44:47], s65 offen lds\n"
        "buffer_load_dwordx4 v9, s[44:47], s65 offen offset:1024 lds\n"
        "buffer_load_dwordx4 v10, s[44:47], s65 offen offset:2048 lds\n"
        "s_waitcnt vmcnt(0)\n"
        "ds_read_b64_tr_b16 a[192:193], v11 offset:0\n"
        "ds_read_b64_tr_b16 a[194:195], v11 offset:1536\n"
        "ds_read_b64_tr_b16 a[200:201], v11 offset:32\n"
        "ds_read_b64_tr_b16 a[202:203], v11 offset:1568\n"
        "ds_read_b64_tr_b16 a[208:209], v11 offset:64\n"
        "ds_read_b64_tr_b16 a[210:211], v11 offset:1600\n"
        "s_waitcnt lgkmcnt(6)\n"
        "s_add_u32 m0, s79, 3072\n"
        "s_mov_b32 s65, 2654208\n"
        "buffer_load_dwordx4 v8, s[44:47], s65 offen lds\n"
        "buffer_load_dwordx4 v9, s[44:47], s65 offen offset:1024 lds\n"
        "buffer_load_dwordx4 v10, s[44:47], s65 offen offset:2048 lds\n"
        "s_waitcnt vmcnt(0)\n"
        "ds_read_b64_tr_b16 a[216:217], v11 offset:3072\n"
        "ds_read_b64_tr_b16 a[218:219], v11 offset:4608\n"
        "ds_read_b64_tr_b16 a[224:225], v11 offset:3104\n"
        "ds_read_b64_tr_b16 a[226:227], v11 offset:4640\n"
        "ds_read_b64_tr_b16 a[232:233], v11 offset:3136\n"
        "ds_read_b64_tr_b16 a[234:235], v11 offset:4672\n"
        "s_waitcnt lgkmcnt(6)\n"
        "s_add_u32 m0, s79, 0\n"
        "s_mov_b32 s65, 2949120\n"
        "buffer_load_dwordx4 v8, s[44:47], s65 offen lds\n"
        "buffer_load_dwordx4 v9, s[44:47], s65 offen offset:1024 lds\n"
        "buffer_load_dwordx4 v10, s[44:47], s65 offen offset:2048 lds\n"
        "s_waitcnt vmcnt(0)\n"
        "ds_read_b64_tr_b16 a[240:241], v11 offset:0\n"
        "ds_read_b64_tr_b16 a[242:243], v11 offset:1536\n"
        "ds_read_b64_tr_b16 a[248:249], v11 offset:32\n"
        "ds_read_b64_tr_b16 a[250:251], v11 offset:1568\n"
        "ds_read_b64_tr_b16 v[80:81], v11 offset:64\n"
        "ds_read_b64_tr_b16 v[82:83], v11 offset:1600\n"
        "s_waitcnt lgkmcnt(6)\n"
        "s_add_u32 m0, s79, 3072\n"
        "s_mov_b32 s65, 3244032\n"
        "buffer_load_dwordx4 v8, s[44:47], s65 offen lds\n"
        "buffer_load_dwordx4 v9, s[44:47], s65 offen offset:1024 lds\n"
        "buffer_load_dwordx4 v10, s[44:47], s65 offen offset:2048 lds\n"
        "s_waitcnt vmcnt(0)\n"
        "ds_read_b64_tr_b16 v[88:89], v11 offset:3072\n"
        "ds_read_b64_tr_b16 v[90:91], v11 offset:4608\n"
        "ds_read_b64_tr_b16 v[96:97], v11 offset:3104\n"
        "ds_read_b64_tr_b16 v[98:99], v11 offset:4640\n"
        "ds_read_b64_tr_b16 v[104:105], v11 offset:3136\n"
        "ds_read_b64_tr_b16 v[106:107], v11 offset:4672\n"
        "s_waitcnt lgkmcnt(6)\n"
        "s_add_u32 m0, s79, 0\n"
        "s_mov_b32 s65, 3538944\n"
        "buffer_load_dwordx4 v8, s[44:47], s65 offen lds\n"
        "buffer_load_dwordx4 v9, s[44:47], s65 offen offset:1024 lds\n"
        "buffer_load_dwordx4 v10, s[44:47], s65 offen offset:2048 lds\n"
        "s_waitcnt vmcnt(0)\n"
        "ds_read_b64_tr_b16 v[112:113], v11 offset:0\n"
        "ds_read_b64_tr_b16 v[114:115], v11 offset:1536\n"
        "ds_read_b64_tr_b16 v[120:121], v11 offset:32\n"
        "ds_read_b64_tr_b16 v[122:123], v11 offset:1568\n"
        "ds_read_b64_tr_b16 v[128:129], v11 offset:64\n"
        "ds_read_b64_tr_b16 v[130:131], v11 offset:1600\n"
        "s_waitcnt lgkmcnt(6)\n"
        "s_add_u32 m0, s79, 3072\n"
        "s_mov_b32 s65, 3833856\n"
        "buffer_load_dwordx4 v8, s[44:47], s65 offen lds\n"
        "buffer_load_dwordx4 v9, s[44:47], s65 offen offset:1024 lds\n"
        "buffer_load_dwordx4 v10, s[44:47], s65 offen offset:2048 lds\n"
        "s_waitcnt vmcnt(0)\n"
        "ds_read_b64_tr_b16 v[136:137], v11 offset:3072\n"
        "ds_read_b64_tr_b16 v[138:139], v11 offset:4608\n"
        "ds_read_b64_tr_b16 v[144:145], v11 offset:3104\n"
        "ds_read_b64_tr_b16 v[146:147], v11 offset:4640\n"
        "ds_read_b64_tr_b16 v[152:153], v11 offset:3136\n"
        "ds_read_b64_tr_b16 v[154:155], v11 offset:4672\n"
        "s_waitcnt lgkmcnt(6)\n"
        "s_add_u32 m0, s79, 0\n"
        "s_mov_b32 s65, 4128768\n"
        "buffer_load_dwordx4 v8, s[44:47], s65 offen lds\n"
        "buffer_load_dwordx4 v9, s[44:47], s65 offen offset:1024 lds\n"
        "buffer_load_dwordx4 v10, s[44:47], s65 offen offset:2048 lds\n"
        "s_waitcnt vmcnt(0)\n"
        "ds_read_b64_tr_b16 v[160:161], v11 offset:0\n"
        "ds_read_b64_tr_b16 v[162:163], v11 offset:1536\n"
        "ds_read_b64_tr_b16 v[168:169], v11 offset:32\n"
        "ds_read_b64_tr_b16 v[170:171], v11 offset:1568\n"
        "ds_read_b64_tr_b16 v[176:177], v11 offset:64\n"
        "ds_read_b64_tr_b16 v[178:179], v11 offset:1600\n"
        "s_waitcnt lgkmcnt(6)\n"
        "s_add_u32 m0, s79, 3072\n"
        "s_mov_b32 s65, 4423680\n"
        "buffer_load_dwordx4 v8, s[44:47], s65 offen lds\n"
        "buffer_load_dwordx4 v9, s[44:47], s65 offen offset:1024 lds\n"
        "buffer_load_dwordx4 v10, s[44:47], s65 offen offset:2048 lds\n"
        "s_waitcnt vmcnt(0)\n"
        "ds_read_b64_tr_b16 v[184:185], v11 offset:3072\n"
        "ds_read_b64_tr_b16 v[186:187], v11 offset:4608\n"
        "ds_read_b64_tr_b16 v[192:193], v11 offset:3104\n"
        "ds_read_b64_tr_b16 v[194:195], v11 offset:4640\n"
        "ds_read_b64_tr_b16 v[200:201], v11 offset:3136\n"
        "ds_read_b64_tr_b16 v[202:203], v11 offset:4672\n"
        "s_waitcnt lgkmcnt(6)\n"
        "s_add_u32 m0, s79, 0\n"
        "s_mov_b32 s65, 4718592\n"
        "buffer_load_dwordx4 v8, s[44:47], s65 offen lds\n"
        "buffer_load_dwordx4 v9, s[44:47], s65 offen offset:1024 lds\n"
        "buffer_load_dwordx4 v10, s[44:47], s65 offen offset:2048 lds\n"
        "s_waitcnt vmcnt(0)\n"
        "ds_read_b64_tr_b16 v[208:209], v11 offset:0\n"
        "ds_read_b64_tr_b16 v[210:211], v11 offset:1536\n"
        "ds_read_b64_tr_b16 v[216:217], v11 offset:32\n"
        "ds_read_b64_tr_b16 v[218:219], v11 offset:1568\n"
        "ds_read_b64_tr_b16 v[224:225], v11 offset:64\n"
        "ds_read_b64_tr_b16 v[226:227], v11 offset:1600\n"
        "s_waitcnt lgkmcnt(6)\n"
        "s_add_u32 m0, s79, 3072\n"
        "s_mov_b32 s65, 5013504\n"
        "buffer_load_dwordx4 v8, s[44:47], s65 offen lds\n"
        "buffer_load_dwordx4 v9, s[44:47], s65 offen offset:1024 lds\n"
        "buffer_load_dwordx4 v10, s[44:47], s65 offen offset:2048 lds\n"
        "s_waitcnt vmcnt(0)\n"
        "ds_read_b64_tr_b16 v[232:233], v11 offset:3072\n"
        "ds_read_b64_tr_b16 v[234:235], v11 offset:4608\n"
        "ds_read_b64_tr_b16 v[240:241], v11 offset:3104\n"
        "ds_read_b64_tr_b16 v[242:243], v11 offset:4640\n"
        "ds_read_b64_tr_b16 v[248:249], v11 offset:3136\n"
        "ds_read_b64_tr_b16 v[250:251], v11 offset:4672\n"
        "s_waitcnt lgkmcnt(6)\n"
        "s_mov_b64 s[44:45], %2\n"
        "s_and_b32 s45, s45, 0xffff\n"
        "s_mov_b32 s46, 5308416\n"
        "s_add_u32 m0, s79, 0\n"
        "s_mov_b32 s65, 0\n"
        "buffer_load_dwordx4 v8, s[44:47], s65 offen lds\n"
        "buffer_load_dwordx4 v9, s[44:47], s65 offen offset:1024 lds\n"
        "buffer_load_dwordx4 v10, s[44:47], s65 offen offset:2048 lds\n"
        "s_waitcnt vmcnt(0)\n"
        "ds_read_b64_tr_b16 a[4:5], v11 offset:0\n"
        "ds_read_b64_tr_b16 a[6:7], v11 offset:1536\n"
        "ds_read_b64_tr_b16 a[12:13], v11 offset:32\n"
        "ds_read_b64_tr_b16 a[14:15], v11 offset:1568\n"
        "ds_read_b64_tr_b16 a[20:21], v11 offset:64\n"
        "ds_read_b64_tr_b16 a[22:23], v11 offset:1600\n"
        "s_waitcnt lgkmcnt(6)\n"
        "s_add_u32 m0, s79, 3072\n"
        "s_mov_b32 s65, 294912\n"
        "buffer_load_dwordx4 v8, s[44:47], s65 offen lds\n"
        "buffer_load_dwordx4 v9, s[44:47], s65 offen offset:1024 lds\n"
        "buffer_load_dwordx4 v10, s[44:47], s65 offen offset:2048 lds\n"
        "s_waitcnt vmcnt(0)\n"
        "ds_read_b64_tr_b16 a[28:29], v11 offset:3072\n"
        "ds_read_b64_tr_b16 a[30:31], v11 offset:4608\n"
        "ds_read_b64_tr_b16 a[36:37], v11 offset:3104\n"
        "ds_read_b64_tr_b16 a[38:39], v11 offset:4640\n"
        "ds_read_b64_tr_b16 a[44:45], v11 offset:3136\n"
        "ds_read_b64_tr_b16 a[46:47], v11 offset:4672\n"
        "s_waitcnt lgkmcnt(6)\n"
        "s_add_u32 m0, s79, 0\n"
        "s_mov_b32 s65, 589824\n"
        "buffer_load_dwordx4 v8, s[44:47], s65 offen lds\n"
        "buffer_load_dwordx4 v9, s[44:47], s65 offen offset:1024 lds\n"
        "buffer_load_dwordx4 v10, s[44:47], s65 offen offset:2048 lds\n"
        "s_waitcnt vmcnt(0)\n"
        "ds_read_b64_tr_b16 a[52:53], v11 offset:0\n"
        "ds_read_b64_tr_b16 a[54:55], v11 offset:1536\n"
        "ds_read_b64_tr_b16 a[60:61], v11 offset:32\n"
        "ds_read_b64_tr_b16 a[62:63], v11 offset:1568\n"
        "ds_read_b64_tr_b16 a[68:69], v11 offset:64\n"
        "ds_read_b64_tr_b16 a[70:71], v11 offset:1600\n"
        "s_waitcnt lgkmcnt(6)\n"
        "s_add_u32 m0, s79, 3072\n"
        "s_mov_b32 s65, 884736\n"
        "buffer_load_dwordx4 v8, s[44:47], s65 offen lds\n"
        "buffer_load_dwordx4 v9, s[44:47], s65 offen offset:1024 lds\n"
        "buffer_load_dwordx4 v10, s[44:47], s65 offen offset:2048 lds\n"
        "s_waitcnt vmcnt(0)\n"
        "ds_read_b64_tr_b16 a[76:77], v11 offset:3072\n"
        "ds_read_b64_tr_b16 a[78:79], v11 offset:4608\n"
        "ds_read_b64_tr_b16 a[84:85], v11 offset:3104\n"
        "ds_read_b64_tr_b16 a[86:87], v11 offset:4640\n"
        "ds_read_b64_tr_b16 a[92:93], v11 offset:3136\n"
        "ds_read_b64_tr_b16 a[94:95], v11 offset:4672\n"
        "s_waitcnt lgkmcnt(6)\n"
        "s_add_u32 m0, s79, 0\n"
        "s_mov_b32 s65, 1179648\n"
        "buffer_load_dwordx4 v8, s[44:47], s65 offen lds\n"
        "buffer_load_dwordx4 v9, s[44:47], s65 offen offset:1024 lds\n"
        "buffer_load_dwordx4 v10, s[44:47], s65 offen offset:2048 lds\n"
        "s_waitcnt vmcnt(0)\n"
        "ds_read_b64_tr_b16 a[100:101], v11 offset:0\n"
        "ds_read_b64_tr_b16 a[102:103], v11 offset:1536\n"
        "ds_read_b64_tr_b16 a[108:109], v11 offset:32\n"
        "ds_read_b64_tr_b16 a[110:111], v11 offset:1568\n"
        "ds_read_b64_tr_b16 a[116:117], v11 offset:64\n"
        "ds_read_b64_tr_b16 a[118:119], v11 offset:1600\n"
        "s_waitcnt lgkmcnt(6)\n"
        "s_add_u32 m0, s79, 3072\n"
        "s_mov_b32 s65, 1474560\n"
        "buffer_load_dwordx4 v8, s[44:47], s65 offen lds\n"
        "buffer_load_dwordx4 v9, s[44:47], s65 offen offset:1024 lds\n"
        "buffer_load_dwordx4 v10, s[44:47], s65 offen offset:2048 lds\n"
        "s_waitcnt vmcnt(0)\n"
        "ds_read_b64_tr_b16 a[124:125], v11 offset:3072\n"
        "ds_read_b64_tr_b16 a[126:127], v11 offset:4608\n"
        "ds_read_b64_tr_b16 a[132:133], v11 offset:3104\n"
        "ds_read_b64_tr_b16 a[134:135], v11 offset:4640\n"
        "ds_read_b64_tr_b16 a[140:141], v11 offset:3136\n"
        "ds_read_b64_tr_b16 a[142:143], v11 offset:4672\n"
        "s_waitcnt lgkmcnt(6)\n"
        "s_add_u32 m0, s79, 0\n"
        "s_mov_b32 s65, 1769472\n"
        "buffer_load_dwordx4 v8, s[44:47], s65 offen lds\n"
        "buffer_load_dwordx4 v9, s[44:47], s65 offen offset:1024 lds\n"
        "buffer_load_dwordx4 v10, s[44:47], s65 offen offset:2048 lds\n"
        "s_waitcnt vmcnt(0)\n"
        "ds_read_b64_tr_b16 a[148:149], v11 offset:0\n"
        "ds_read_b64_tr_b16 a[150:151], v11 offset:1536\n"
        "ds_read_b64_tr_b16 a[156:157], v11 offset:32\n"
        "ds_read_b64_tr_b16 a[158:159], v11 offset:1568\n"
        "ds_read_b64_tr_b16 a[164:165], v11 offset:64\n"
        "ds_read_b64_tr_b16 a[166:167], v11 offset:1600\n"
        "s_waitcnt lgkmcnt(6)\n"
        "s_add_u32 m0, s79, 3072\n"
        "s_mov_b32 s65, 2064384\n"
        "buffer_load_dwordx4 v8, s[44:47], s65 offen lds\n"
        "buffer_load_dwordx4 v9, s[44:47], s65 offen offset:1024 lds\n"
        "buffer_load_dwordx4 v10, s[44:47], s65 offen offset:2048 lds\n"
        "s_waitcnt vmcnt(0)\n"
        "ds_read_b64_tr_b16 a[172:173], v11 offset:3072\n"
        "ds_read_b64_tr_b16 a[174:175], v11 offset:4608\n"
        "ds_read_b64_tr_b16 a[180:181], v11 offset:3104\n"
        "ds_read_b64_tr_b16 a[182:183], v11 offset:4640\n"
        "ds_read_b64_tr_b16 a[188:189], v11 offset:3136\n"
        "ds_read_b64_tr_b16 a[190:191], v11 offset:4672\n"
        "s_waitcnt lgkmcnt(6)\n"
        "s_add_u32 m0, s79, 0\n"
        "s_mov_b32 s65, 2359296\n"
        "buffer_load_dwordx4 v8, s[44:47], s65 offen lds\n"
        "buffer_load_dwordx4 v9, s[44:47], s65 offen offset:1024 lds\n"
        "buffer_load_dwordx4 v10, s[44:47], s65 offen offset:2048 lds\n"
        "s_waitcnt vmcnt(0)\n"
        "ds_read_b64_tr_b16 a[196:197], v11 offset:0\n"
        "ds_read_b64_tr_b16 a[198:199], v11 offset:1536\n"
        "ds_read_b64_tr_b16 a[204:205], v11 offset:32\n"
        "ds_read_b64_tr_b16 a[206:207], v11 offset:1568\n"
        "ds_read_b64_tr_b16 a[212:213], v11 offset:64\n"
        "ds_read_b64_tr_b16 a[214:215], v11 offset:1600\n"
        "s_waitcnt lgkmcnt(6)\n"
        "s_add_u32 m0, s79, 3072\n"
        "s_mov_b32 s65, 2654208\n"
        "buffer_load_dwordx4 v8, s[44:47], s65 offen lds\n"
        "buffer_load_dwordx4 v9, s[44:47], s65 offen offset:1024 lds\n"
        "buffer_load_dwordx4 v10, s[44:47], s65 offen offset:2048 lds\n"
        "s_waitcnt vmcnt(0)\n"
        "ds_read_b64_tr_b16 a[220:221], v11 offset:3072\n"
        "ds_read_b64_tr_b16 a[222:223], v11 offset:4608\n"
        "ds_read_b64_tr_b16 a[228:229], v11 offset:3104\n"
        "ds_read_b64_tr_b16 a[230:231], v11 offset:4640\n"
        "ds_read_b64_tr_b16 a[236:237], v11 offset:3136\n"
        "ds_read_b64_tr_b16 a[238:239], v11 offset:4672\n"
        "s_waitcnt lgkmcnt(6)\n"
        "s_add_u32 m0, s79, 0\n"
        "s_mov_b32 s65, 2949120\n"
        "buffer_load_dwordx4 v8, s[44:47], s65 offen lds\n"
        "buffer_load_dwordx4 v9, s[44:47], s65 offen offset:1024 lds\n"
        "buffer_load_dwordx4 v10, s[44:47], s65 offen offset:2048 lds\n"
        "s_waitcnt vmcnt(0)\n"
        "ds_read_b64_tr_b16 a[244:245], v11 offset:0\n"
        "ds_read_b64_tr_b16 a[246:247], v11 offset:1536\n"
        "ds_read_b64_tr_b16 a[252:253], v11 offset:32\n"
        "ds_read_b64_tr_b16 a[254:255], v11 offset:1568\n"
        "ds_read_b64_tr_b16 v[84:85], v11 offset:64\n"
        "ds_read_b64_tr_b16 v[86:87], v11 offset:1600\n"
        "s_waitcnt lgkmcnt(6)\n"
        "s_add_u32 m0, s79, 3072\n"
        "s_mov_b32 s65, 3244032\n"
        "buffer_load_dwordx4 v8, s[44:47], s65 offen lds\n"
        "buffer_load_dwordx4 v9, s[44:47], s65 offen offset:1024 lds\n"
        "buffer_load_dwordx4 v10, s[44:47], s65 offen offset:2048 lds\n"
        "s_waitcnt vmcnt(0)\n"
        "ds_read_b64_tr_b16 v[92:93], v11 offset:3072\n"
        "ds_read_b64_tr_b16 v[94:95], v11 offset:4608\n"
        "ds_read_b64_tr_b16 v[100:101], v11 offset:3104\n"
        "ds_read_b64_tr_b16 v[102:103], v11 offset:4640\n"
        "ds_read_b64_tr_b16 v[108:109], v11 offset:3136\n"
        "ds_read_b64_tr_b16 v[110:111], v11 offset:4672\n"
        "s_waitcnt lgkmcnt(6)\n"
        "s_add_u32 m0, s79, 0\n"
        "s_mov_b32 s65, 3538944\n"
        "buffer_load_dwordx4 v8, s[44:47], s65 offen lds\n"
        "buffer_load_dwordx4 v9, s[44:47], s65 offen offset:1024 lds\n"
        "buffer_load_dwordx4 v10, s[44:47], s65 offen offset:2048 lds\n"
        "s_waitcnt vmcnt(0)\n"
        "ds_read_b64_tr_b16 v[116:117], v11 offset:0\n"
        "ds_read_b64_tr_b16 v[118:119], v11 offset:1536\n"
        "ds_read_b64_tr_b16 v[124:125], v11 offset:32\n"
        "ds_read_b64_tr_b16 v[126:127], v11 offset:1568\n"
        "ds_read_b64_tr_b16 v[132:133], v11 offset:64\n"
        "ds_read_b64_tr_b16 v[134:135], v11 offset:1600\n"
        "s_waitcnt lgkmcnt(6)\n"
        "s_add_u32 m0, s79, 3072\n"
        "s_mov_b32 s65, 3833856\n"
        "buffer_load_dwordx4 v8, s[44:47], s65 offen lds\n"
        "buffer_load_dwordx4 v9, s[44:47], s65 offen offset:1024 lds\n"
        "buffer_load_dwordx4 v10, s[44:47], s65 offen offset:2048 lds\n"
        "s_waitcnt vmcnt(0)\n"
        "ds_read_b64_tr_b16 v[140:141], v11 offset:3072\n"
        "ds_read_b64_tr_b16 v[142:143], v11 offset:4608\n"
        "ds_read_b64_tr_b16 v[148:149], v11 offset:3104\n"
        "ds_read_b64_tr_b16 v[150:151], v11 offset:4640\n"
        "ds_read_b64_tr_b16 v[156:157], v11 offset:3136\n"
        "ds_read_b64_tr_b16 v[158:159], v11 offset:4672\n"
        "s_waitcnt lgkmcnt(6)\n"
        "s_add_u32 m0, s79, 0\n"
        "s_mov_b32 s65, 4128768\n"
        "buffer_load_dwordx4 v8, s[44:47], s65 offen lds\n"
        "buffer_load_dwordx4 v9, s[44:47], s65 offen offset:1024 lds\n"
        "buffer_load_dwordx4 v10, s[44:47], s65 offen offset:2048 lds\n"
        "s_waitcnt vmcnt(0)\n"
        "ds_read_b64_tr_b16 v[164:165], v11 offset:0\n"
        "ds_read_b64_tr_b16 v[166:167], v11 offset:1536\n"
        "ds_read_b64_tr_b16 v[172:173], v11 offset:32\n"
        "ds_read_b64_tr_b16 v[174:175], v11 offset:1568\n"
        "ds_read_b64_tr_b16 v[180:181], v11 offset:64\n"
        "ds_read_b64_tr_b16 v[182:183], v11 offset:1600\n"
        "s_waitcnt lgkmcnt(6)\n"
        "s_add_u32 m0, s79, 3072\n"
        "s_mov_b32 s65, 4423680\n"
        "buffer_load_dwordx4 v8, s[44:47], s65 offen lds\n"
        "buffer_load_dwordx4 v9, s[44:47], s65 offen offset:1024 lds\n"
        "buffer_load_dwordx4 v10, s[44:47], s65 offen offset:2048 lds\n"
        "s_waitcnt vmcnt(0)\n"
        "ds_read_b64_tr_b16 v[188:189], v11 offset:3072\n"
        "ds_read_b64_tr_b16 v[190:191], v11 offset:4608\n"
        "ds_read_b64_tr_b16 v[196:197], v11 offset:3104\n"
        "ds_read_b64_tr_b16 v[198:199], v11 offset:4640\n"
        "ds_read_b64_tr_b16 v[204:205], v11 offset:3136\n"
        "ds_read_b64_tr_b16 v[206:207], v11 offset:4672\n"
        "s_waitcnt lgkmcnt(6)\n"
        "s_add_u32 m0, s79, 0\n"
        "s_mov_b32 s65, 4718592\n"
        "buffer_load_dwordx4 v8, s[44:47], s65 offen lds\n"
        "buffer_load_dwordx4 v9, s[44:47], s65 offen offset:1024 lds\n"
        "buffer_load_dwordx4 v10, s[44:47], s65 offen offset:2048 lds\n"
        "s_waitcnt vmcnt(0)\n"
        "ds_read_b64_tr_b16 v[212:213], v11 offset:0\n"
        "ds_read_b64_tr_b16 v[214:215], v11 offset:1536\n"
        "ds_read_b64_tr_b16 v[220:221], v11 offset:32\n"
        "ds_read_b64_tr_b16 v[222:223], v11 offset:1568\n"
        "ds_read_b64_tr_b16 v[228:229], v11 offset:64\n"
        "ds_read_b64_tr_b16 v[230:231], v11 offset:1600\n"
        "s_waitcnt lgkmcnt(6)\n"
        "s_add_u32 m0, s79, 3072\n"
        "s_mov_b32 s65, 5013504\n"
        "buffer_load_dwordx4 v8, s[44:47], s65 offen lds\n"
        "buffer_load_dwordx4 v9, s[44:47], s65 offen offset:1024 lds\n"
        "buffer_load_dwordx4 v10, s[44:47], s65 offen offset:2048 lds\n"
        "s_waitcnt vmcnt(0)\n"
        "ds_read_b64_tr_b16 v[236:237], v11 offset:3072\n"
        "ds_read_b64_tr_b16 v[238:239], v11 offset:4608\n"
        "ds_read_b64_tr_b16 v[244:245], v11 offset:3104\n"
        "ds_read_b64_tr_b16 v[246:247], v11 offset:4640\n"
        "ds_read_b64_tr_b16 v[252:253], v11 offset:3136\n"
        "ds_read_b64_tr_b16 v[254:255], v11 offset:4672\n"
        "s_waitcnt lgkmcnt(0)\n"
        "s_getpc_b64 s[44:45]\n"
        ".Lf1_pc_%=:\n"
        "s_add_u32 s44, s44, .Lf1_code_%=-.Lf1_pc_%=\n"
        "s_addc_u32 s45, s45, 0\n"
        "s_and_b32 s45, s45, 0xffff\n"
        "s_and_b32 s44, s44, -4\n"
        "s_mov_b32 s46, .Lf1_end_%=-.Lf1_code_%=\n"
        "s_mov_b32 s47, 0x20000\n"
        "s_lshl_b32 s65, s48, 10\n"
        "buffer_load_dwordx4 v[24:27], v1, s[44:47], s65 offen\n"
        "s_add_u32 s65, s65, 4096\n"
        "buffer_load_dwordx4 v[28:31], v1, s[44:47], s65 offen\n"
        "s_add_u32 s65, s65, 4096\n"
        "buffer_load_dwordx4 v[32:35], v1, s[44:47], s65 offen\n"
        "s_add_u32 s65, s65, 4096\n"
        "buffer_load_dwordx4 v[36:39], v1, s[44:47], s65 offen\n"
        "s_add_u32 s65, s65, 4096\n"
        "buffer_load_dwordx4 v[40:43], v1, s[44:47], s65 offen\n"
        "s_add_u32 s65, s65, 4096\n"
        "v_lshrrev_b32 v20, 4, v1\n"
        "s_mov_b32 s50, s56\n"
        "v_lshlrev_b32 v22, 7, v20\n"
        "s_sub_u32 s66, 0, s50\n"
        "s_mov_b32 s52, 32768\n"
        "s_mov_b32 s64, 0\n"
        ".Lf1_pg0_top_%=:\n"
        "buffer_load_dword v21, v22, s[40:43], s52 offen sc1\n"
        "s_waitcnt vmcnt(0)\n"
        "v_cmp_gt_i32 vcc, 0, v21\n"
        "s_cbranch_vccz .Lf1_pg0_ok_%=\n"
        "s_add_u32 s64, s64, 1\n"
        "s_and_b32 s65, s64, 255\n"
        "s_cbranch_scc1 .Lf1_pg0_go_%=\n"
        "s_mov_b32 s65, 49152\n"
        "s_cmp_lt_u32 s64, 1048576\n"
        "s_cbranch_scc1 .Lf1_pg0_chk_%=\n"
        "v_mov_b32 v21, 1\n"
        "buffer_store_dword v21, off, s[40:43], s65 sc1\n"
        "s_waitcnt vmcnt(0)\n"
        ".Lf1_pg0_chk_%=:\n"
        "buffer_load_dword v21, off, s[40:43], s65 sc1\n"
        "s_waitcnt vmcnt(0)\n"
        "v_readfirstlane_b32 s65, v21\n"
        "s_nop 0\n"
        "s_cmp_lg_u32 s65, 1\n"
        "s_cbranch_scc1 .Lf1_pg0_go_%=\n"
        "s_endpgm\n"
        ".Lf1_pg0_go_%=:\n"
        "s_sleep 2\n"
        "s_branch .Lf1_pg0_top_%=\n"
        ".Lf1_pg0_ok_%=:\n"
        "v_cmp_eq_u32 s[52:53], 1, v21\n"
        "s_nop 1\n"
        "s_mov_b32 s54, 40960\n"
        "s_mov_b32 s64, 0\n"
        ".Lf1_pg1_top_%=:\n"
        "buffer_load_dword v21, v22, s[40:43], s54 offen sc1\n"
        "s_waitcnt vmcnt(0)\n"
        "v_cmp_gt_i32 vcc, 0, v21\n"
        "s_cbranch_vccz .Lf1_pg1_ok_%=\n"
        "s_add_u32 s64, s64, 1\n"
        "s_and_b32 s65, s64, 255\n"
        "s_cbranch_scc1 .Lf1_pg1_go_%=\n"
        "s_mov_b32 s65, 49152\n"
        "s_cmp_lt_u32 s64, 1048576\n"
        "s_cbranch_scc1 .Lf1_pg1_chk_%=\n"
        "v_mov_b32 v21, 1\n"
        "buffer_store_dword v21, off, s[40:43], s65 sc1\n"
        "s_waitcnt vmcnt(0)\n"
        ".Lf1_pg1_chk_%=:\n"
        "buffer_load_dword v21, off, s[40:43], s65 sc1\n"
        "s_waitcnt vmcnt(0)\n"
        "v_readfirstlane_b32 s65, v21\n"
        "s_nop 0\n"
        "s_cmp_lg_u32 s65, 1\n"
        "s_cbranch_scc1 .Lf1_pg1_go_%=\n"
        "s_endpgm\n"
        ".Lf1_pg1_go_%=:\n"
        "s_sleep 2\n"
        "s_branch .Lf1_pg1_top_%=\n"
        ".Lf1_pg1_ok_%=:\n"
        "v_cmp_eq_u32 s[54:55], 1, v21\n"
        "s_nop 1\n"
        "s_mov_b32 vcc_lo, 0x55555555\n"
        "s_mov_b32 vcc_hi, 0x55555555\n"
        "s_xor_b64 vcc, vcc, s[52:53]\n"
        "s_bcnt1_i32_b64 s65, vcc\n"
        "s_sub_u32 s64, s65, 23\n"
        "s_cmp_lt_u32 s64, 19\n"
        "s_cbranch_scc1 .Lf1_rawmap_%=\n"
        "s_cmp_lt_u32 s65, 32\n"
        "s_cselect_b32 s65, 0, -1\n"
        "s_xor_b32 s52, s65, 0x55555555\n"
        "s_xor_b32 s53, s65, 0x55555555\n"
        "s_xor_b32 s54, s65, 0x55555555\n"
        "s_xor_b32 s55, s65, 0xaaaaaaaa\n"
        ".Lf1_rawmap_%=:\n"
        "s_xor_b32 s52, s52, s66\n"
        "s_xor_b32 s53, s53, s66\n"
        "s_xor_b32 s54, s54, s66\n"
        "s_xor_b32 s55, s55, s66\n"
        "s_and_b32 s76, s55, 1\n"
        "s_lshl_b32 s76, s76, 13\n"
        "s_add_u32 s60, s60, s76\n"
        "s_barrier\n"
        "s_mov_b32 s68, -1\n"
        "s_mov_b32 s69, 0\n"
        "s_mov_b32 s78, 0\n"
        "s_mov_b32 s74, -1\n"
        "s_mov_b32 s86, s76\n"
        "s_lshr_b32 s85, s61, 16\n"
        "s_and_b32 s61, s61, 0xffff\n"
        "s_mov_b32 s89, 241172480\n"
        "s_mov_b32 s84, 241172480\n"
        "v_mov_b32 v6, v0\n"
        "s_mov_b32 s80, 0xbfb8aa3b\n"
        "s_lshr_b32 s67, s69, 1\n"
        "s_lshr_b32 s65, s67, 1\n"
        "s_bitcmp1_b32 s54, s65\n"
        "s_cselect_b32 s65, 8192, 0\n"
        "s_lshl_b32 s77, s67, 20\n"
        "s_add_u32 s77, s77, s65\n"
        "s_add_u32 s71, s77, s58\n"
        "s_add_u32 s81, s77, s62\n"
        "s_add_u32 s78, s78, 18432\n"
        "s_cmp_eq_u32 s78, 55296\n"
        "s_cselect_b32 s78, 0, s78\n"
        "s_add_u32 m0, s78, s61\n"
        "s_lshr_b32 s70, s69, 1\n"
        "s_branch .Lf1_wait1_%=\n"
        ".p2align 6\n"
        ".p2align 5\n"
        ".Lf1_code_%=:\n"
        ".Lf1_i0_%=:\n"
        "v_mfma_f32_16x16x32_bf16 v[24:27], a[0:3], v[8:11], 0\n"
        "s_add_u32 s86, s76, 512\n"
        "buffer_load_dword v4, v2, s[40:43], s86 offen sc1\n"
        "v_mfma_f32_16x16x32_bf16 v[28:31], a[4:7], v[8:11], 0\n"
        "s_add_u32 s71, s77, s58\n"
        "v_mul_f32 v72, s80, v48\n"
        "v_mul_f32 v73, s80, v49\n"
        "v_mfma_f32_16x16x32_bf16 v[32:35], a[8:11], v[8:11], 0\n"
        "s_add_u32 s71, s71, 32768\n"
        "v_mul_f32 v74, s80, v50\n"
        "v_mul_f32 v75, s80, v51\n"
        "v_mfma_f32_16x16x32_bf16 v[36:39], a[12:15], v[8:11], 0\n"
        "s_add_u32 s81, s77, s62\n"
        "v_exp_f32 v72, v72\n"
        "v_mfma_f32_16x16x32_bf16 v[40:43], a[16:19], v[8:11], 0\n"
        "s_add_u32 s81, s81, 2048\n"
        "v_exp_f32 v73, v73\n"
        "v_mfma_f32_16x16x32_bf16 v[44:47], a[20:23], v[8:11], 0\n"
        "s_waitcnt lgkmcnt(1)\n"
        "ds_read_b128 v[8:11], v0 offset:1536\n"
        "v_mul_f32 v76, s80, v56\n"
        "v_mfma_f32_16x16x32_bf16 v[24:27], a[24:27], v[12:15], v[24:27]\n"
        "s_add_u32 s78, s78, 18432\n"
        "v_exp_f32 v74, v74\n"
        "v_mfma_f32_16x16x32_bf16 v[28:31], a[28:31], v[12:15], v[28:31]\n"
        "s_cmp_eq_u32 s78, 55296\n"
        "s_cselect_b32 s78, 0, s78\n"
        "v_mul_f32 v77, s80, v57\n"
        "v_mfma_f32_16x16x32_bf16 v[32:35], a[32:35], v[12:15], v[32:35]\n"
        "s_add_u32 m0, s78, s61\n"
        "v_exp_f32 v75, v75\n"
        "v_mfma_f32_16x16x32_bf16 v[36:39], a[36:39], v[12:15], v[36:39]\n"
        "s_lshr_b32 s70, s69, 1\n"
        "v_mul_f32 v48, v48, v52\n"
        "v_mul_f32 v49, v49, v53\n"
        "v_mfma_f32_16x16x32_bf16 v[40:43], a[40:43], v[12:15], v[40:43]\n"
        "v_mul_f32 v50, v50, v54\n"
        "v_mul_f32 v51, v51, v55\n"
        "v_mfma_f32_16x16x32_bf16 v[44:47], a[44:47], v[12:15], v[44:47]\n"
        "s_waitcnt lgkmcnt(1)\n"
        "ds_read_b128 v[12:15], v0 offset:2048\n"
        "v_add_f32 v72, 1.0, v72\n"
        "v_mfma_f32_16x16x32_bf16 v[24:27], a[48:51], v[16:19], v[24:27]\n"
        "v_add_f32 v73, 1.0, v73\n"
        "v_add_f32 v74, 1.0, v74\n"
        "v_mfma_f32_16x16x32_bf16 v[28:31], a[52:55], v[16:19], v[28:31]\n"
        "v_add_f32 v75, 1.0, v75\n"
        "v_mul_f32 v78, s80, v58\n"
        "v_mfma_f32_16x16x32_bf16 v[32:35], a[56:59], v[16:19], v[32:35]\n"
        "s_waitcnt vmcnt(3)\n"
        "v_rcp_f32 v72, v72\n"
        "v_mfma_f32_16x16x32_bf16 v[36:39], a[60:63], v[16:19], v[36:39]\n"
        "buffer_store_dword v5, v3, s[40:43], s84 offen sc1\n"
        "v_mfma_f32_16x16x32_bf16 v[40:43], a[64:67], v[16:19], v[40:43]\n"
        "v_rcp_f32 v73, v73\n"
        "v_mfma_f32_16x16x32_bf16 v[44:47], a[68:71], v[16:19], v[44:47]\n"
        "s_waitcnt lgkmcnt(1)\n"
        "ds_read_b128 v[16:19], v0 offset:2560\n"
        "v_mul_f32 v79, s80, v59\n"
        "v_mfma_f32_16x16x32_bf16 v[24:27], a[72:75], v[8:11], v[24:27]\n"
        "v_rcp_f32 v74, v74\n"
        "v_mfma_f32_16x16x32_bf16 v[28:31], a[76:79], v[8:11], v[28:31]\n"
        "v_rcp_f32 v75, v75\n"
        "v_mfma_f32_16x16x32_bf16 v[32:35], a[80:83], v[8:11], v[32:35]\n"
        "v_mul_f32 v48, v48, v72\n"
        "v_mul_f32 v49, v49, v73\n"
        "v_mfma_f32_16x16x32_bf16 v[36:39], a[84:87], v[8:11], v[36:39]\n"
        "v_mul_f32 v50, v50, v74\n"
        "v_mul_f32 v51, v51, v75\n"
        "v_mfma_f32_16x16x32_bf16 v[40:43], a[88:91], v[8:11], v[40:43]\n"
        "v_cvt_pk_bf16_f32 v52, v48, v49\n"
        "v_cvt_pk_bf16_f32 v53, v50, v51\n"
        "v_mfma_f32_16x16x32_bf16 v[44:47], a[92:95], v[8:11], v[44:47]\n"
        "s_waitcnt lgkmcnt(1)\n"
        "ds_read_b128 v[8:11], v0 offset:3072\n"
        "v_mul_f32 v72, s80, v64\n"
        "v_mfma_f32_16x16x32_bf16 v[24:27], a[96:99], v[12:15], v[24:27]\n"
        "v_exp_f32 v76, v76\n"
        "v_mfma_f32_16x16x32_bf16 v[28:31], a[100:103], v[12:15], v[28:31]\n"
        "v_exp_f32 v77, v77\n"
        "v_mfma_f32_16x16x32_bf16 v[32:35], a[104:107], v[12:15], v[32:35]\n"
        "v_exp_f32 v78, v78\n"
        "v_mfma_f32_16x16x32_bf16 v[36:39], a[108:111], v[12:15], v[36:39]\n"
        "v_exp_f32 v79, v79\n"
        "v_mfma_f32_16x16x32_bf16 v[40:43], a[112:115], v[12:15], v[40:43]\n"
        "s_waitcnt vmcnt(1)\n"
        "v_cmp_gt_i32 vcc, s70, v4\n"
        "v_mul_f32 v56, v56, v60\n"
        "v_mfma_f32_16x16x32_bf16 v[44:47], a[116:119], v[12:15], v[44:47]\n"
        "s_waitcnt lgkmcnt(1)\n"
        "ds_read_b128 v[12:15], v0 offset:3584\n"
        "v_mul_f32_e64 v57, v57, v61\n"
        "v_mfma_f32_16x16x32_bf16 v[24:27], a[120:123], v[16:19], v[24:27]\n"
        "s_and_b64 vcc, vcc, 1\n"
        "v_mul_f32_e64 v58, v58, v62\n"
        "v_mfma_f32_16x16x32_bf16 v[28:31], a[124:127], v[16:19], v[28:31]\n"
        "s_cbranch_vccnz .Lf1_i00_%=\n"
        "v_mfma_f32_16x16x32_bf16 v[32:35], a[128:131], v[16:19], v[32:35]\n"
        "buffer_load_dwordx4 v1, s[40:43], s71 offen offset:0 sc1 lds\n"
        "v_mfma_f32_16x16x32_bf16 v[36:39], a[132:135], v[16:19], v[36:39]\n"
        "v_mul_f32 v59, v59, v63\n"
        "v_add_f32 v76, 1.0, v76\n"
        "v_mfma_f32_16x16x32_bf16 v[40:43], a[136:139], v[16:19], v[40:43]\n"
        "v_add_f32 v77, 1.0, v77\n"
        "v_add_f32 v78, 1.0, v78\n"
        "v_mfma_f32_16x16x32_bf16 v[44:47], a[140:143], v[16:19], v[44:47]\n"
        "s_waitcnt lgkmcnt(1)\n"
        "ds_read_b128 v[16:19], v0 offset:4096\n"
        "v_add_f32 v79, 1.0, v79\n"
        "v_mfma_f32_16x16x32_bf16 v[24:27], a[144:147], v[8:11], v[24:27]\n"
        "buffer_load_dwordx4 v1, s[40:43], s71 offen offset:1024 sc1 lds\n"
        "v_mfma_f32_16x16x32_bf16 v[28:31], a[148:151], v[8:11], v[28:31]\n"
        "v_rcp_f32 v76, v76\n"
        "v_mfma_f32_16x16x32_bf16 v[32:35], a[152:155], v[8:11], v[32:35]\n"
        "v_rcp_f32 v77, v77\n"
        "v_mfma_f32_16x16x32_bf16 v[36:39], a[156:159], v[8:11], v[36:39]\n"
        "v_rcp_f32 v78, v78\n"
        "v_mfma_f32_16x16x32_bf16 v[40:43], a[160:163], v[8:11], v[40:43]\n"
        "buffer_load_dwordx4 v1, s[40:43], s71 offen offset:2048 sc1 lds\n"
        "v_mfma_f32_16x16x32_bf16 v[44:47], a[164:167], v[8:11], v[44:47]\n"
        "s_waitcnt lgkmcnt(1)\n"
        "ds_read_b128 v[8:11], v0 offset:4608\n"
        "v_mul_f32 v73, s80, v65\n"
        "v_mfma_f32_16x16x32_bf16 v[24:27], a[168:171], v[12:15], v[24:27]\n"
        "v_rcp_f32 v79, v79\n"
        "v_mfma_f32_16x16x32_bf16 v[28:31], a[172:175], v[12:15], v[28:31]\n"
        "v_mul_f32 v56, v56, v76\n"
        "v_mul_f32 v57, v57, v77\n"
        "v_mfma_f32_16x16x32_bf16 v[32:35], a[176:179], v[12:15], v[32:35]\n"
        "buffer_load_dwordx4 v1, s[40:43], s71 offen offset:3072 sc1 lds\n"
        "v_mfma_f32_16x16x32_bf16 v[36:39], a[180:183], v[12:15], v[36:39]\n"
        "s_add_u32 m0, s78, s85\n"
        "s_add_u32 s69, s69, 1\n"
        "v_mul_f32 v58, v58, v78\n"
        "v_mfma_f32_16x16x32_bf16 v[40:43], a[184:187], v[12:15], v[40:43]\n"
        "v_mul_f32 v59, v59, v79\n"
        "v_cvt_pk_bf16_f32 v54, v56, v57\n"
        "v_mfma_f32_16x16x32_bf16 v[44:47], a[188:191], v[12:15], v[44:47]\n"
        "s_waitcnt lgkmcnt(1)\n"
        "ds_read_b128 v[12:15], v0 offset:5120\n"
        "v_cvt_pk_bf16_f32 v55, v58, v59\n"
        "v_mfma_f32_16x16x32_bf16 v[24:27], a[192:195], v[16:19], v[24:27]\n"
        "buffer_load_dwordx4 v1, s[40:43], s81 offen offset:0 sc1 lds\n"
        "v_mfma_f32_16x16x32_bf16 v[28:31], a[196:199], v[16:19], v[28:31]\n"
        "buffer_store_dwordx4 v[52:55], v1, s[40:43], s72 offen offset:0 sc1\n"
        "v_mfma_f32_16x16x32_bf16 v[32:35], a[200:203], v[16:19], v[32:35]\n"
        "v_mul_f32 v74, s80, v66\n"
        "v_mul_f32 v75, s80, v67\n"
        "v_mfma_f32_16x16x32_bf16 v[36:39], a[204:207], v[16:19], v[36:39]\n"
        "v_exp_f32 v72, v72\n"
        "v_mfma_f32_16x16x32_bf16 v[40:43], a[208:211], v[16:19], v[40:43]\n"
        "v_exp_f32 v73, v73\n"
        "v_mfma_f32_16x16x32_bf16 v[44:47], a[212:215], v[16:19], v[44:47]\n"
        "s_waitcnt lgkmcnt(1)\n"
        "ds_read_b128 v[16:19], v0 offset:5632\n"
        "v_mfma_f32_16x16x32_bf16 v[24:27], a[216:219], v[8:11], v[24:27]\n"
        "v_exp_f32 v74, v74\n"
        "v_mfma_f32_16x16x32_bf16 v[28:31], a[220:223], v[8:11], v[28:31]\n"
        "v_exp_f32 v75, v75\n"
        "v_mfma_f32_16x16x32_bf16 v[32:35], a[224:227], v[8:11], v[32:35]\n"
        "v_mul_f32 v64, v64, v68\n"
        "v_mul_f32 v65, v65, v69\n"
        "v_mfma_f32_16x16x32_bf16 v[36:39], a[228:231], v[8:11], v[36:39]\n"
        "v_mul_f32 v66, v66, v70\n"
        "v_mul_f32 v67, v67, v71\n"
        "v_mfma_f32_16x16x32_bf16 v[40:43], a[232:235], v[8:11], v[40:43]\n"
        "v_add_f32 v72, 1.0, v72\n"
        "v_add_f32 v73, 1.0, v73\n"
        "v_mfma_f32_16x16x32_bf16 v[44:47], a[236:239], v[8:11], v[44:47]\n"
        "s_waitcnt lgkmcnt(1)\n"
        "ds_read_b128 v[8:11], v0 offset:6144\n"
        "v_add_f32 v74, 1.0, v74\n"
        "v_mfma_f32_16x16x32_bf16 v[24:27], a[240:243], v[12:15], v[24:27]\n"
        "v_add_f32 v75, 1.0, v75\n"
        "v_mfma_f32_16x16x32_bf16 v[28:31], a[244:247], v[12:15], v[28:31]\n"
        "v_rcp_f32 v72, v72\n"
        "v_mfma_f32_16x16x32_bf16 v[32:35], a[248:251], v[12:15], v[32:35]\n"
        "v_rcp_f32 v73, v73\n"
        "v_mfma_f32_16x16x32_bf16 v[36:39], a[252:255], v[12:15], v[36:39]\n"
        "v_rcp_f32 v74, v74\n"
        "v_mfma_f32_16x16x32_bf16 v[40:43], v[80:83], v[12:15], v[40:43]\n"
        "v_rcp_f32 v75, v75\n"
        "v_mfma_f32_16x16x32_bf16 v[44:47], v[84:87], v[12:15], v[44:47]\n"
        "s_waitcnt lgkmcnt(1)\n"
        "ds_read_b128 v[12:15], v0 offset:6656\n"
        "v_mul_f32 v64, v64, v72\n"
        "v_mfma_f32_16x16x32_bf16 v[24:27], v[88:91], v[16:19], v[24:27]\n"
        "v_mul_f32 v65, v65, v73\n"
        "v_mul_f32 v66, v66, v74\n"
        "v_mfma_f32_16x16x32_bf16 v[28:31], v[92:95], v[16:19], v[28:31]\n"
        "v_mul_f32 v67, v67, v75\n"
        "v_cvt_pk_bf16_f32 v68, v64, v65\n"
        "v_mfma_f32_16x16x32_bf16 v[32:35], v[96:99], v[16:19], v[32:35]\n"
        "v_cvt_pk_bf16_f32 v69, v66, v67\n"
        "s_nop 0\n"
        "v_mfma_f32_16x16x32_bf16 v[36:39], v[100:103], v[16:19], v[36:39]\n"
        "v_permlane16_swap_b32 v68, v70\n"
        "v_mfma_f32_16x16x32_bf16 v[40:43], v[104:107], v[16:19], v[40:43]\n"
        "v_permlane16_swap_b32 v69, v71\n"
        "v_mfma_f32_16x16x32_bf16 v[44:47], v[108:111], v[16:19], v[44:47]\n"
        "s_waitcnt lgkmcnt(1)\n"
        "ds_read_b128 v[16:19], v0 offset:7168\n"
        "v_mfma_f32_16x16x32_bf16 v[24:27], v[112:115], v[8:11], v[24:27]\n"
        "buffer_store_dwordx4 v[68:71], v7, s[40:43], s83 offen offset:0 sc1\n"
        "v_mfma_f32_16x16x32_bf16 v[28:31], v[116:119], v[8:11], v[28:31]\n"
        "v_mov_b32 v5, s74\n"
        "v_mfma_f32_16x16x32_bf16 v[32:35], v[120:123], v[8:11], v[32:35]\n"
        "s_add_u32 s74, s74, 1\n"
        "v_mfma_f32_16x16x32_bf16 v[36:39], v[124:127], v[8:11], v[36:39]\n"
        "s_add_u32 s68, s68, 1\n"
        "v_mfma_f32_16x16x32_bf16 v[40:43], v[128:131], v[8:11], v[40:43]\n"
        "s_lshr_b32 s66, s68, 1\n"
        "v_mfma_f32_16x16x32_bf16 v[44:47], v[132:135], v[8:11], v[44:47]\n"
        "s_waitcnt lgkmcnt(1)\n"
        "ds_read_b128 v[8:11], v0 offset:7680\n"
        "v_mfma_f32_16x16x32_bf16 v[24:27], v[136:139], v[12:15], v[24:27]\n"
        "s_bitcmp1_b32 s54, s66\n"
        "s_cselect_b32 s66, 8192, 0\n"
        "v_mfma_f32_16x16x32_bf16 v[28:31], v[140:143], v[12:15], v[28:31]\n"
        "s_lshl_b32 s75, s68, 20\n"
        "v_mfma_f32_16x16x32_bf16 v[32:35], v[144:147], v[12:15], v[32:35]\n"
        "s_add_u32 s75, s75, s66\n"
        "v_mfma_f32_16x16x32_bf16 v[36:39], v[148:151], v[12:15], v[36:39]\n"
        "s_add_u32 s72, s75, s59\n"
        "v_mfma_f32_16x16x32_bf16 v[40:43], v[152:155], v[12:15], v[40:43]\n"
        "s_add_u32 s83, s75, s63\n"
        "v_mfma_f32_16x16x32_bf16 v[44:47], v[156:159], v[12:15], v[44:47]\n"
        "s_waitcnt lgkmcnt(1)\n"
        "ds_read_b128 v[12:15], v0 offset:8192\n"
        "v_mfma_f32_16x16x32_bf16 v[24:27], v[160:163], v[16:19], v[24:27]\n"
        "v_mfma_f32_16x16x32_bf16 v[28:31], v[164:167], v[16:19], v[28:31]\n"
        "v_mfma_f32_16x16x32_bf16 v[32:35], v[168:171], v[16:19], v[32:35]\n"
        "v_mfma_f32_16x16x32_bf16 v[36:39], v[172:175], v[16:19], v[36:39]\n"
        "v_mfma_f32_16x16x32_bf16 v[40:43], v[176:179], v[16:19], v[40:43]\n"
        "v_mfma_f32_16x16x32_bf16 v[44:47], v[180:183], v[16:19], v[44:47]\n"
        "s_waitcnt lgkmcnt(1)\n"
        "ds_read_b128 v[16:19], v0 offset:8704\n"
        "v_mfma_f32_16x16x32_bf16 v[24:27], v[184:187], v[8:11], v[24:27]\n"
        "v_mfma_f32_16x16x32_bf16 v[28:31], v[188:191], v[8:11], v[28:31]\n"
        "v_mfma_f32_16x16x32_bf16 v[32:35], v[192:195], v[8:11], v[32:35]\n"
        "v_mfma_f32_16x16x32_bf16 v[36:39], v[196:199], v[8:11], v[36:39]\n"
        "v_mfma_f32_16x16x32_bf16 v[40:43], v[200:203], v[8:11], v[40:43]\n"
        "s_waitcnt vmcnt(2)\n"
        "s_barrier\n"
        "v_add_u32 v0, s78, v6\n"
        "v_mfma_f32_16x16x32_bf16 v[44:47], v[204:207], v[8:11], v[44:47]\n"
        "s_waitcnt lgkmcnt(1)\n"
        "ds_read_b128 v[8:11], v0 offset:0\n"
        "v_mfma_f32_16x16x32_bf16 v[24:27], v[208:211], v[12:15], v[24:27]\n"
        "s_getreg_b32 s88, hwreg(HW_REG_IB_STS, 0, 4)\n"
        "v_mfma_f32_16x16x32_bf16 v[28:31], v[212:215], v[12:15], v[28:31]\n"
        "s_cmp_le_u32 s88, 0\n"
        "s_cselect_b32 s88, s73, s89\n"
        "s_cselect_b32 s73, s89, s73\n"
        "v_mfma_f32_16x16x32_bf16 v[32:35], v[216:219], v[12:15], v[32:35]\n"
        "buffer_store_dword v5, v3, s[40:43], s88 offen sc1\n"
        "v_mfma_f32_16x16x32_bf16 v[36:39], v[220:223], v[12:15], v[36:39]\n"
        "v_mfma_f32_16x16x32_bf16 v[40:43], v[224:227], v[12:15], v[40:43]\n"
        "v_mfma_f32_16x16x32_bf16 v[44:47], v[228:231], v[12:15], v[44:47]\n"
        "s_waitcnt lgkmcnt(1)\n"
        "ds_read_b128 v[12:15], v0 offset:512\n"
        "v_mfma_f32_16x16x32_bf16 v[24:27], v[232:235], v[16:19], v[24:27]\n"
        "v_mfma_f32_16x16x32_bf16 v[28:31], v[236:239], v[16:19], v[28:31]\n"
        "v_mfma_f32_16x16x32_bf16 v[32:35], v[240:243], v[16:19], v[32:35]\n"
        "s_getreg_b32 s88, hwreg(HW_REG_IB_STS, 0, 4)\n"
        "v_mfma_f32_16x16x32_bf16 v[36:39], v[244:247], v[16:19], v[36:39]\n"
        "s_cmp_le_u32 s88, 1\n"
        "s_cselect_b32 s88, s73, s89\n"
        "s_cselect_b32 s73, s89, s73\n"
        "v_mfma_f32_16x16x32_bf16 v[40:43], v[248:251], v[16:19], v[40:43]\n"
        "buffer_store_dword v5, v3, s[40:43], s88 offen sc1\n"
        "v_mfma_f32_16x16x32_bf16 v[44:47], v[252:255], v[16:19], v[44:47]\n"
        "s_waitcnt lgkmcnt(1)\n"
        "ds_read_b128 v[16:19], v0 offset:1024\n"
        "s_mov_b32 s84, s73\n"
        "s_mov_b32 s73, s60\n"
        "s_branch .Lf1_i1_%=\n"
        ".p2align 5\n"
        ".Lf1_i00_%=:\n"
        "v_mfma_f32_16x16x32_bf16 v[32:35], a[128:131], v[16:19], v[32:35]\n"
        "buffer_load_dword v4, v2, s[40:43], s86 offen sc1\n"
        "v_mfma_f32_16x16x32_bf16 v[36:39], a[132:135], v[16:19], v[36:39]\n"
        "v_mul_f32 v59, v59, v63\n"
        "v_add_f32 v76, 1.0, v76\n"
        "v_mfma_f32_16x16x32_bf16 v[40:43], a[136:139], v[16:19], v[40:43]\n"
        "v_add_f32 v77, 1.0, v77\n"
        "v_add_f32 v78, 1.0, v78\n"
        "v_mfma_f32_16x16x32_bf16 v[44:47], a[140:143], v[16:19], v[44:47]\n"
        "s_waitcnt lgkmcnt(1)\n"
        "ds_read_b128 v[16:19], v0 offset:4096\n"
        "v_add_f32 v79, 1.0, v79\n"
        "v_mfma_f32_16x16x32_bf16 v[24:27], a[144:147], v[8:11], v[24:27]\n"
        "v_rcp_f32 v76, v76\n"
        "v_mfma_f32_16x16x32_bf16 v[28:31], a[148:151], v[8:11], v[28:31]\n"
        "v_rcp_f32 v77, v77\n"
        "v_mfma_f32_16x16x32_bf16 v[32:35], a[152:155], v[8:11], v[32:35]\n"
        "v_rcp_f32 v78, v78\n"
        "v_mfma_f32_16x16x32_bf16 v[36:39], a[156:159], v[8:11], v[36:39]\n"
        "v_rcp_f32 v79, v79\n"
        "v_mfma_f32_16x16x32_bf16 v[40:43], a[160:163], v[8:11], v[40:43]\n"
        "v_mul_f32 v56, v56, v76\n"
        "v_mul_f32 v57, v57, v77\n"
        "v_mfma_f32_16x16x32_bf16 v[44:47], a[164:167], v[8:11], v[44:47]\n"
        "s_waitcnt lgkmcnt(1)\n"
        "ds_read_b128 v[8:11], v0 offset:4608\n"
        "v_mul_f32 v58, v58, v78\n"
        "v_mfma_f32_16x16x32_bf16 v[24:27], a[168:171], v[12:15], v[24:27]\n"
        "v_mul_f32 v59, v59, v79\n"
        "v_cvt_pk_bf16_f32 v54, v56, v57\n"
        "v_mfma_f32_16x16x32_bf16 v[28:31], a[172:175], v[12:15], v[28:31]\n"
        "v_cvt_pk_bf16_f32 v55, v58, v59\n"
        "v_mul_f32 v73, s80, v65\n"
        "v_mfma_f32_16x16x32_bf16 v[32:35], a[176:179], v[12:15], v[32:35]\n"
        "buffer_store_dwordx4 v[52:55], v1, s[40:43], s72 offen offset:0 sc1\n"
        "v_mfma_f32_16x16x32_bf16 v[36:39], a[180:183], v[12:15], v[36:39]\n"
        "v_mul_f32 v74, s80, v66\n"
        "v_mul_f32 v75, s80, v67\n"
        "v_mfma_f32_16x16x32_bf16 v[40:43], a[184:187], v[12:15], v[40:43]\n"
        "v_exp_f32 v72, v72\n"
        "v_mfma_f32_16x16x32_bf16 v[44:47], a[188:191], v[12:15], v[44:47]\n"
        "s_waitcnt lgkmcnt(1)\n"
        "ds_read_b128 v[12:15], v0 offset:5120\n"
        "v_mfma_f32_16x16x32_bf16 v[24:27], a[192:195], v[16:19], v[24:27]\n"
        "v_exp_f32 v73, v73\n"
        "v_mfma_f32_16x16x32_bf16 v[28:31], a[196:199], v[16:19], v[28:31]\n"
        "v_exp_f32 v74, v74\n"
        "v_mfma_f32_16x16x32_bf16 v[32:35], a[200:203], v[16:19], v[32:35]\n"
        "v_exp_f32 v75, v75\n"
        "v_mfma_f32_16x16x32_bf16 v[36:39], a[204:207], v[16:19], v[36:39]\n"
        "v_mul_f32 v64, v64, v68\n"
        "v_mul_f32 v65, v65, v69\n"
        "v_mfma_f32_16x16x32_bf16 v[40:43], a[208:211], v[16:19], v[40:43]\n"
        "v_mul_f32 v66, v66, v70\n"
        "v_mul_f32 v67, v67, v71\n"
        "v_mfma_f32_16x16x32_bf16 v[44:47], a[212:215], v[16:19], v[44:47]\n"
        "s_waitcnt lgkmcnt(1)\n"
        "ds_read_b128 v[16:19], v0 offset:5632\n"
        "v_add_f32 v72, 1.0, v72\n"
        "v_mfma_f32_16x16x32_bf16 v[24:27], a[216:219], v[8:11], v[24:27]\n"
        "v_add_f32 v73, 1.0, v73\n"
        "v_add_f32 v74, 1.0, v74\n"
        "v_mfma_f32_16x16x32_bf16 v[28:31], a[220:223], v[8:11], v[28:31]\n"
        "v_add_f32 v75, 1.0, v75\n"
        "v_mfma_f32_16x16x32_bf16 v[32:35], a[224:227], v[8:11], v[32:35]\n"
        "v_rcp_f32 v72, v72\n"
        "v_mfma_f32_16x16x32_bf16 v[36:39], a[228:231], v[8:11], v[36:39]\n"
        "v_rcp_f32 v73, v73\n"
        "v_mfma_f32_16x16x32_bf16 v[40:43], a[232:235], v[8:11], v[40:43]\n"
        "v_rcp_f32 v74, v74\n"
        "v_mfma_f32_16x16x32_bf16 v[44:47], a[236:239], v[8:11], v[44:47]\n"
        "s_waitcnt lgkmcnt(1)\n"
        "ds_read_b128 v[8:11], v0 offset:6144\n"
        "s_waitcnt vmcnt(1)\n"
        "v_cmp_gt_i32 vcc, s70, v4\n"
        "v_mfma_f32_16x16x32_bf16 v[24:27], a[240:243], v[12:15], v[24:27]\n"
        "v_rcp_f32 v75, v75\n"
        "v_mfma_f32_16x16x32_bf16 v[28:31], a[244:247], v[12:15], v[28:31]\n"
        "s_and_b64 vcc, vcc, 1\n"
        "v_mul_f32 v64, v64, v72\n"
        "v_mfma_f32_16x16x32_bf16 v[32:35], a[248:251], v[12:15], v[32:35]\n"
        "s_cbranch_vccnz .Lf1_i01_%=\n"
        "v_mfma_f32_16x16x32_bf16 v[36:39], a[252:255], v[12:15], v[36:39]\n"
        "buffer_load_dwordx4 v1, s[40:43], s71 offen offset:0 sc1 lds\n"
        "v_mfma_f32_16x16x32_bf16 v[40:43], v[80:83], v[12:15], v[40:43]\n"
        "v_mul_f32 v65, v65, v73\n"
        "v_mul_f32 v66, v66, v74\n"
        "v_mfma_f32_16x16x32_bf16 v[44:47], v[84:87], v[12:15], v[44:47]\n"
        "s_waitcnt lgkmcnt(1)\n"
        "ds_read_b128 v[12:15], v0 offset:6656\n"
        "v_mul_f32 v67, v67, v75\n"
        "v_mfma_f32_16x16x32_bf16 v[24:27], v[88:91], v[16:19], v[24:27]\n"
        "buffer_load_dwordx4 v1, s[40:43], s71 offen offset:1024 sc1 lds\n"
        "v_mfma_f32_16x16x32_bf16 v[28:31], v[92:95], v[16:19], v[28:31]\n"
        "v_cvt_pk_bf16_f32 v68, v64, v65\n"
        "v_cvt_pk_bf16_f32 v69, v66, v67\n"
        "v_mfma_f32_16x16x32_bf16 v[32:35], v[96:99], v[16:19], v[32:35]\n"
        "s_nop 0\n"
        "v_permlane16_swap_b32 v68, v70\n"
        "v_mfma_f32_16x16x32_bf16 v[36:39], v[100:103], v[16:19], v[36:39]\n"
        "buffer_load_dwordx4 v1, s[40:43], s71 offen offset:2048 sc1 lds\n"
        "v_mfma_f32_16x16x32_bf16 v[40:43], v[104:107], v[16:19], v[40:43]\n"
        "v_permlane16_swap_b32 v69, v71\n"
        "v_mfma_f32_16x16x32_bf16 v[44:47], v[108:111], v[16:19], v[44:47]\n"
        "s_waitcnt lgkmcnt(1)\n"
        "ds_read_b128 v[16:19], v0 offset:7168\n"
        "v_mfma_f32_16x16x32_bf16 v[24:27], v[112:115], v[8:11], v[24:27]\n"
        "buffer_load_dwordx4 v1, s[40:43], s71 offen offset:3072 sc1 lds\n"
        "v_mfma_f32_16x16x32_bf16 v[28:31], v[116:119], v[8:11], v[28:31]\n"
        "s_add_u32 m0, s78, s85\n"
        "s_add_u32 s69, s69, 1\n"
        "v_mfma_f32_16x16x32_bf16 v[32:35], v[120:123], v[8:11], v[32:35]\n"
        "buffer_store_dwordx4 v[68:71], v7, s[40:43], s83 offen offset:0 sc1\n"
        "v_mfma_f32_16x16x32_bf16 v[36:39], v[124:127], v[8:11], v[36:39]\n"
        "buffer_load_dwordx4 v1, s[40:43], s81 offen offset:0 sc1 lds\n"
        "v_mfma_f32_16x16x32_bf16 v[40:43], v[128:131], v[8:11], v[40:43]\n"
        "v_mov_b32 v5, s74\n"
        "v_mfma_f32_16x16x32_bf16 v[44:47], v[132:135], v[8:11], v[44:47]\n"
        "s_waitcnt lgkmcnt(1)\n"
        "ds_read_b128 v[8:11], v0 offset:7680\n"
        "v_mfma_f32_16x16x32_bf16 v[24:27], v[136:139], v[12:15], v[24:27]\n"
        "s_add_u32 s74, s74, 1\n"
        "v_mfma_f32_16x16x32_bf16 v[28:31], v[140:143], v[12:15], v[28:31]\n"
        "s_add_u32 s68, s68, 1\n"
        "v_mfma_f32_16x16x32_bf16 v[32:35], v[144:147], v[12:15], v[32:35]\n"
        "s_lshr_b32 s66, s68, 1\n"
        "v_mfma_f32_16x16x32_bf16 v[36:39], v[148:151], v[12:15], v[36:39]\n"
        "s_bitcmp1_b32 s54, s66\n"
        "s_cselect_b32 s66, 8192, 0\n"
        "v_mfma_f32_16x16x32_bf16 v[40:43], v[152:155], v[12:15], v[40:43]\n"
        "s_lshl_b32 s75, s68, 20\n"
        "v_mfma_f32_16x16x32_bf16 v[44:47], v[156:159], v[12:15], v[44:47]\n"
        "s_waitcnt lgkmcnt(1)\n"
        "ds_read_b128 v[12:15], v0 offset:8192\n"
        "v_mfma_f32_16x16x32_bf16 v[24:27], v[160:163], v[16:19], v[24:27]\n"
        "s_add_u32 s75, s75, s66\n"
        "v_mfma_f32_16x16x32_bf16 v[28:31], v[164:167], v[16:19], v[28:31]\n"
        "s_add_u32 s72, s75, s59\n"
        "v_mfma_f32_16x16x32_bf16 v[32:35], v[168:171], v[16:19], v[32:35]\n"
        "s_add_u32 s83, s75, s63\n"
        "v_mfma_f32_16x16x32_bf16 v[36:39], v[172:175], v[16:19], v[36:39]\n"
        "v_mfma_f32_16x16x32_bf16 v[40:43], v[176:179], v[16:19], v[40:43]\n"
        "v_mfma_f32_16x16x32_bf16 v[44:47], v[180:183], v[16:19], v[44:47]\n"
        "s_waitcnt lgkmcnt(1)\n"
        "ds_read_b128 v[16:19], v0 offset:8704\n"
        "v_mfma_f32_16x16x32_bf16 v[24:27], v[184:187], v[8:11], v[24:27]\n"
        "v_mfma_f32_16x16x32_bf16 v[28:31], v[188:191], v[8:11], v[28:31]\n"
        "v_mfma_f32_16x16x32_bf16 v[32:35], v[192:195], v[8:11], v[32:35]\n"
        "v_mfma_f32_16x16x32_bf16 v[36:39], v[196:199], v[8:11], v[36:39]\n"
        "v_mfma_f32_16x16x32_bf16 v[40:43], v[200:203], v[8:11], v[40:43]\n"
        "s_waitcnt vmcnt(0)\n"
        "s_barrier\n"
        "v_add_u32 v0, s78, v6\n"
        "v_mfma_f32_16x16x32_bf16 v[44:47], v[204:207], v[8:11], v[44:47]\n"
        "s_waitcnt lgkmcnt(1)\n"
        "ds_read_b128 v[8:11], v0 offset:0\n"
        "v_mfma_f32_16x16x32_bf16 v[24:27], v[208:211], v[12:15], v[24:27]\n"
        "v_mfma_f32_16x16x32_bf16 v[28:31], v[212:215], v[12:15], v[28:31]\n"
        "v_mfma_f32_16x16x32_bf16 v[32:35], v[216:219], v[12:15], v[32:35]\n"
        "s_getreg_b32 s88, hwreg(HW_REG_IB_STS, 0, 4)\n"
        "v_mfma_f32_16x16x32_bf16 v[36:39], v[220:223], v[12:15], v[36:39]\n"
        "s_cmp_le_u32 s88, 15\n"
        "s_cselect_b32 s88, s73, s89\n"
        "s_cselect_b32 s73, s89, s73\n"
        "v_mfma_f32_16x16x32_bf16 v[40:43], v[224:227], v[12:15], v[40:43]\n"
        "buffer_store_dword v5, v3, s[40:43], s88 offen sc1\n"
        "v_mfma_f32_16x16x32_bf16 v[44:47], v[228:231], v[12:15], v[44:47]\n"
        "s_waitcnt lgkmcnt(1)\n"
        "ds_read_b128 v[12:15], v0 offset:512\n"
        "v_mfma_f32_16x16x32_bf16 v[24:27], v[232:235], v[16:19], v[24:27]\n"
        "v_mfma_f32_16x16x32_bf16 v[28:31], v[236:239], v[16:19], v[28:31]\n"
        "v_mfma_f32_16x16x32_bf16 v[32:35], v[240:243], v[16:19], v[32:35]\n"
        "s_getreg_b32 s88, hwreg(HW_REG_IB_STS, 0, 4)\n"
        "v_mfma_f32_16x16x32_bf16 v[36:39], v[244:247], v[16:19], v[36:39]\n"
        "s_cmp_le_u32 s88, 15\n"
        "s_cselect_b32 s88, s73, s89\n"
        "s_cselect_b32 s73, s89, s73\n"
        "v_mfma_f32_16x16x32_bf16 v[40:43], v[248:251], v[16:19], v[40:43]\n"
        "buffer_store_dword v5, v3, s[40:43], s88 offen sc1\n"
        "v_mfma_f32_16x16x32_bf16 v[44:47], v[252:255], v[16:19], v[44:47]\n"
        "s_waitcnt lgkmcnt(1)\n"
        "ds_read_b128 v[16:19], v0 offset:1024\n"
        "s_mov_b32 s84, s73\n"
        "s_mov_b32 s73, s60\n"
        "s_branch .Lf1_i1_%=\n"
        ".p2align 5\n"
        ".Lf1_i01_%=:\n"
        "v_mfma_f32_16x16x32_bf16 v[36:39], a[252:255], v[12:15], v[36:39]\n"
        "buffer_load_dword v4, v2, s[40:43], s86 offen sc1\n"
        "v_mfma_f32_16x16x32_bf16 v[40:43], v[80:83], v[12:15], v[40:43]\n"
        "v_mul_f32 v65, v65, v73\n"
        "v_mul_f32 v66, v66, v74\n"
        "v_mfma_f32_16x16x32_bf16 v[44:47], v[84:87], v[12:15], v[44:47]\n"
        "s_waitcnt lgkmcnt(1)\n"
        "ds_read_b128 v[12:15], v0 offset:6656\n"
        "v_mul_f32 v67, v67, v75\n"
        "v_mfma_f32_16x16x32_bf16 v[24:27], v[88:91], v[16:19], v[24:27]\n"
        "v_cvt_pk_bf16_f32 v68, v64, v65\n"
        "v_cvt_pk_bf16_f32 v69, v66, v67\n"
        "v_mfma_f32_16x16x32_bf16 v[28:31], v[92:95], v[16:19], v[28:31]\n"
        "s_nop 0\n"
        "v_permlane16_swap_b32 v68, v70\n"
        "v_mfma_f32_16x16x32_bf16 v[32:35], v[96:99], v[16:19], v[32:35]\n"
        "v_permlane16_swap_b32 v69, v71\n"
        "v_mfma_f32_16x16x32_bf16 v[36:39], v[100:103], v[16:19], v[36:39]\n"
        "buffer_store_dwordx4 v[68:71], v7, s[40:43], s83 offen offset:0 sc1\n"
        "v_mfma_f32_16x16x32_bf16 v[40:43], v[104:107], v[16:19], v[40:43]\n"
        "v_mov_b32 v5, s74\n"
        "v_mfma_f32_16x16x32_bf16 v[44:47], v[108:111], v[16:19], v[44:47]\n"
        "s_waitcnt lgkmcnt(1)\n"
        "ds_read_b128 v[16:19], v0 offset:7168\n"
        "v_mfma_f32_16x16x32_bf16 v[24:27], v[112:115], v[8:11], v[24:27]\n"
        "s_add_u32 s74, s74, 1\n"
        "v_mfma_f32_16x16x32_bf16 v[28:31], v[116:119], v[8:11], v[28:31]\n"
        "s_add_u32 s68, s68, 1\n"
        "v_mfma_f32_16x16x32_bf16 v[32:35], v[120:123], v[8:11], v[32:35]\n"
        "s_lshr_b32 s66, s68, 1\n"
        "v_mfma_f32_16x16x32_bf16 v[36:39], v[124:127], v[8:11], v[36:39]\n"
        "s_bitcmp1_b32 s54, s66\n"
        "s_cselect_b32 s66, 8192, 0\n"
        "v_mfma_f32_16x16x32_bf16 v[40:43], v[128:131], v[8:11], v[40:43]\n"
        "s_lshl_b32 s75, s68, 20\n"
        "v_mfma_f32_16x16x32_bf16 v[44:47], v[132:135], v[8:11], v[44:47]\n"
        "s_waitcnt lgkmcnt(1)\n"
        "ds_read_b128 v[8:11], v0 offset:7680\n"
        "v_mfma_f32_16x16x32_bf16 v[24:27], v[136:139], v[12:15], v[24:27]\n"
        "s_add_u32 s75, s75, s66\n"
        "v_mfma_f32_16x16x32_bf16 v[28:31], v[140:143], v[12:15], v[28:31]\n"
        "s_add_u32 s72, s75, s59\n"
        "v_mfma_f32_16x16x32_bf16 v[32:35], v[144:147], v[12:15], v[32:35]\n"
        "s_add_u32 s83, s75, s63\n"
        "v_mfma_f32_16x16x32_bf16 v[36:39], v[148:151], v[12:15], v[36:39]\n"
        "v_mfma_f32_16x16x32_bf16 v[40:43], v[152:155], v[12:15], v[40:43]\n"
        "v_mfma_f32_16x16x32_bf16 v[44:47], v[156:159], v[12:15], v[44:47]\n"
        "s_waitcnt lgkmcnt(1)\n"
        "ds_read_b128 v[12:15], v0 offset:8192\n"
        "v_mfma_f32_16x16x32_bf16 v[24:27], v[160:163], v[16:19], v[24:27]\n"
        "v_mfma_f32_16x16x32_bf16 v[28:31], v[164:167], v[16:19], v[28:31]\n"
        "v_mfma_f32_16x16x32_bf16 v[32:35], v[168:171], v[16:19], v[32:35]\n"
        "v_mfma_f32_16x16x32_bf16 v[36:39], v[172:175], v[16:19], v[36:39]\n"
        "v_mfma_f32_16x16x32_bf16 v[40:43], v[176:179], v[16:19], v[40:43]\n"
        "v_mfma_f32_16x16x32_bf16 v[44:47], v[180:183], v[16:19], v[44:47]\n"
        "s_waitcnt lgkmcnt(1)\n"
        "ds_read_b128 v[16:19], v0 offset:8704\n"
        "v_mfma_f32_16x16x32_bf16 v[24:27], v[184:187], v[8:11], v[24:27]\n"
        "s_waitcnt vmcnt(1)\n"
        "v_cmp_gt_i32 vcc, s70, v4\n"
        "v_mfma_f32_16x16x32_bf16 v[28:31], v[188:191], v[8:11], v[28:31]\n"
        "v_mfma_f32_16x16x32_bf16 v[32:35], v[192:195], v[8:11], v[32:35]\n"
        "s_and_b64 vcc, vcc, 1\n"
        "v_mfma_f32_16x16x32_bf16 v[36:39], v[196:199], v[8:11], v[36:39]\n"
        "s_cbranch_vccnz .Lf1_i02_%=\n"
        "v_mfma_f32_16x16x32_bf16 v[40:43], v[200:203], v[8:11], v[40:43]\n"
        "s_getreg_b32 s88, hwreg(HW_REG_IB_STS, 0, 4)\n"
        "buffer_load_dwordx4 v1, s[40:43], s71 offen offset:0 sc1 lds\n"
        "v_mfma_f32_16x16x32_bf16 v[44:47], v[204:207], v[8:11], v[44:47]\n"
        "s_waitcnt lgkmcnt(1)\n"
        "v_mfma_f32_16x16x32_bf16 v[24:27], v[208:211], v[12:15], v[24:27]\n"
        "buffer_load_dwordx4 v1, s[40:43], s71 offen offset:1024 sc1 lds\n"
        "v_mfma_f32_16x16x32_bf16 v[28:31], v[212:215], v[12:15], v[28:31]\n"
        "s_cmp_le_u32 s88, 0\n"
        "s_cselect_b32 s88, s73, s89\n"
        "s_cselect_b32 s73, s89, s73\n"
        "v_mfma_f32_16x16x32_bf16 v[32:35], v[216:219], v[12:15], v[32:35]\n"
        "buffer_load_dwordx4 v1, s[40:43], s71 offen offset:2048 sc1 lds\n"
        "v_mfma_f32_16x16x32_bf16 v[36:39], v[220:223], v[12:15], v[36:39]\n"
        "buffer_store_dword v5, v3, s[40:43], s88 offen sc1\n"
        "v_mfma_f32_16x16x32_bf16 v[40:43], v[224:227], v[12:15], v[40:43]\n"
        "buffer_load_dwordx4 v1, s[40:43], s71 offen offset:3072 sc1 lds\n"
        "v_mfma_f32_16x16x32_bf16 v[44:47], v[228:231], v[12:15], v[44:47]\n"
        "s_waitcnt lgkmcnt(0)\n"
        "s_add_u32 m0, s78, s85\n"
        "s_add_u32 s69, s69, 1\n"
        "v_mfma_f32_16x16x32_bf16 v[24:27], v[232:235], v[16:19], v[24:27]\n"
        "s_getreg_b32 s88, hwreg(HW_REG_IB_STS, 0, 4)\n"
        "buffer_load_dwordx4 v1, s[40:43], s81 offen offset:0 sc1 lds\n"
        "v_mfma_f32_16x16x32_bf16 v[28:31], v[236:239], v[16:19], v[28:31]\n"
        "s_cmp_le_u32 s88, 5\n"
        "s_cselect_b32 s88, s73, s89\n"
        "s_cselect_b32 s73, s89, s73\n"
        "v_mfma_f32_16x16x32_bf16 v[32:35], v[240:243], v[16:19], v[32:35]\n"
        "buffer_store_dword v5, v3, s[40:43], s88 offen sc1\n"
        "v_mfma_f32_16x16x32_bf16 v[36:39], v[244:247], v[16:19], v[36:39]\n"
        "s_mov_b32 s84, s73\n"
        "s_mov_b32 s73, s60\n"
        "v_mfma_f32_16x16x32_bf16 v[40:43], v[248:251], v[16:19], v[40:43]\n"
        "v_mfma_f32_16x16x32_bf16 v[44:47], v[252:255], v[16:19], v[44:47]\n"
        "v_mul_f32 v72, s80, v24\n"
        "v_mul_f32 v73, s80, v25\n"
        "v_mul_f32 v74, s80, v26\n"
        "v_mul_f32 v75, s80, v27\n"
        "v_exp_f32 v72, v72\n"
        "v_exp_f32 v73, v73\n"
        "v_exp_f32 v74, v74\n"
        "v_exp_f32 v75, v75\n"
        "v_mul_f32 v24, v24, v28\n"
        "v_mul_f32 v25, v25, v29\n"
        "v_mul_f32 v26, v26, v30\n"
        "v_mul_f32 v27, v27, v31\n"
        "v_add_f32 v72, 1.0, v72\n"
        "v_add_f32 v73, 1.0, v73\n"
        "v_add_f32 v74, 1.0, v74\n"
        "v_add_f32 v75, 1.0, v75\n"
        "v_rcp_f32 v72, v72\n"
        "v_rcp_f32 v73, v73\n"
        "v_rcp_f32 v74, v74\n"
        "v_rcp_f32 v75, v75\n"
        "v_mul_f32 v24, v24, v72\n"
        "v_mul_f32 v25, v25, v73\n"
        "v_mul_f32 v26, v26, v74\n"
        "v_mul_f32 v27, v27, v75\n"
        "v_cvt_pk_bf16_f32 v28, v24, v25\n"
        "v_cvt_pk_bf16_f32 v29, v26, v27\n"
        "s_waitcnt vmcnt(7)\n"
        "buffer_store_dword v5, v3, s[40:43], s84 offen sc1\n"
        "s_mov_b32 s84, s89\n"
        "v_mul_f32 v76, s80, v32\n"
        "v_mul_f32 v77, s80, v33\n"
        "v_mul_f32 v78, s80, v34\n"
        "v_mul_f32 v79, s80, v35\n"
        "v_exp_f32 v76, v76\n"
        "v_exp_f32 v77, v77\n"
        "v_exp_f32 v78, v78\n"
        "v_exp_f32 v79, v79\n"
        "v_mul_f32 v32, v32, v36\n"
        "v_mul_f32 v33, v33, v37\n"
        "v_mul_f32 v34, v34, v38\n"
        "v_mul_f32 v35, v35, v39\n"
        "v_add_f32 v76, 1.0, v76\n"
        "v_add_f32 v77, 1.0, v77\n"
        "v_add_f32 v78, 1.0, v78\n"
        "v_add_f32 v79, 1.0, v79\n"
        "v_rcp_f32 v76, v76\n"
        "v_rcp_f32 v77, v77\n"
        "v_rcp_f32 v78, v78\n"
        "v_rcp_f32 v79, v79\n"
        "v_mul_f32 v32, v32, v76\n"
        "v_mul_f32 v33, v33, v77\n"
        "v_mul_f32 v34, v34, v78\n"
        "v_mul_f32 v35, v35, v79\n"
        "v_cvt_pk_bf16_f32 v30, v32, v33\n"
        "v_cvt_pk_bf16_f32 v31, v34, v35\n"
        "buffer_store_dwordx4 v[28:31], v1, s[40:43], s72 offen offset:0 sc1\n"
        "v_mul_f32 v72, s80, v40\n"
        "v_mul_f32 v73, s80, v41\n"
        "v_mul_f32 v74, s80, v42\n"
        "v_mul_f32 v75, s80, v43\n"
        "v_exp_f32 v72, v72\n"
        "v_exp_f32 v73, v73\n"
        "v_exp_f32 v74, v74\n"
        "v_exp_f32 v75, v75\n"
        "v_mul_f32 v40, v40, v44\n"
        "v_mul_f32 v41, v41, v45\n"
        "v_mul_f32 v42, v42, v46\n"
        "v_mul_f32 v43, v43, v47\n"
        "v_add_f32 v72, 1.0, v72\n"
        "v_add_f32 v73, 1.0, v73\n"
        "v_add_f32 v74, 1.0, v74\n"
        "v_add_f32 v75, 1.0, v75\n"
        "v_rcp_f32 v72, v72\n"
        "v_rcp_f32 v73, v73\n"
        "v_rcp_f32 v74, v74\n"
        "v_rcp_f32 v75, v75\n"
        "v_mul_f32 v40, v40, v72\n"
        "v_mul_f32 v41, v41, v73\n"
        "v_mul_f32 v42, v42, v74\n"
        "v_mul_f32 v43, v43, v75\n"
        "v_cvt_pk_bf16_f32 v44, v40, v41\n"
        "v_cvt_pk_bf16_f32 v45, v42, v43\n"
        "s_nop 0\n"
        "v_permlane16_swap_b32 v44, v46\n"
        "v_permlane16_swap_b32 v45, v47\n"
        "buffer_store_dwordx4 v[44:47], v7, s[40:43], s83 offen offset:0 sc1\n"
        "v_mov_b32 v5, s74\n"
        "s_mov_b32 s84, s60\n"
        "buffer_store_dword v5, v3, s[40:43], s89 offen sc1\n"
        "buffer_store_dword v5, v3, s[40:43], s89 offen sc1\n"
        "s_waitcnt vmcnt(6)\n"
        "s_branch .Lf1_go0_%=\n"
        ".p2align 5\n"
        ".Lf1_i02_%=:\n"
        "v_mfma_f32_16x16x32_bf16 v[40:43], v[200:203], v[8:11], v[40:43]\n"
        "s_getreg_b32 s88, hwreg(HW_REG_IB_STS, 0, 4)\n"
        "buffer_load_dword v4, v2, s[40:43], s86 offen sc1\n"
        "v_mfma_f32_16x16x32_bf16 v[44:47], v[204:207], v[8:11], v[44:47]\n"
        "s_waitcnt lgkmcnt(1)\n"
        "v_mfma_f32_16x16x32_bf16 v[24:27], v[208:211], v[12:15], v[24:27]\n"
        "s_cmp_le_u32 s88, 0\n"
        "s_cselect_b32 s88, s73, s89\n"
        "s_cselect_b32 s73, s89, s73\n"
        "v_mfma_f32_16x16x32_bf16 v[28:31], v[212:215], v[12:15], v[28:31]\n"
        "buffer_store_dword v5, v3, s[40:43], s88 offen sc1\n"
        "v_mfma_f32_16x16x32_bf16 v[32:35], v[216:219], v[12:15], v[32:35]\n"
        "v_mfma_f32_16x16x32_bf16 v[36:39], v[220:223], v[12:15], v[36:39]\n"
        "v_mfma_f32_16x16x32_bf16 v[40:43], v[224:227], v[12:15], v[40:43]\n"
        "v_mfma_f32_16x16x32_bf16 v[44:47], v[228:231], v[12:15], v[44:47]\n"
        "s_waitcnt lgkmcnt(0)\n"
        "v_mfma_f32_16x16x32_bf16 v[24:27], v[232:235], v[16:19], v[24:27]\n"
        "s_getreg_b32 s88, hwreg(HW_REG_IB_STS, 0, 4)\n"
        "v_mfma_f32_16x16x32_bf16 v[28:31], v[236:239], v[16:19], v[28:31]\n"
        "s_cmp_le_u32 s88, 2\n"
        "s_cselect_b32 s88, s73, s89\n"
        "s_cselect_b32 s73, s89, s73\n"
        "v_mfma_f32_16x16x32_bf16 v[32:35], v[240:243], v[16:19], v[32:35]\n"
        "buffer_store_dword v5, v3, s[40:43], s88 offen sc1\n"
        "v_mfma_f32_16x16x32_bf16 v[36:39], v[244:247], v[16:19], v[36:39]\n"
        "s_mov_b32 s84, s73\n"
        "s_mov_b32 s73, s60\n"
        "v_mfma_f32_16x16x32_bf16 v[40:43], v[248:251], v[16:19], v[40:43]\n"
        "v_mfma_f32_16x16x32_bf16 v[44:47], v[252:255], v[16:19], v[44:47]\n"
        "v_mul_f32 v72, s80, v24\n"
        "v_mul_f32 v73, s80, v25\n"
        "v_mul_f32 v74, s80, v26\n"
        "v_mul_f32 v75, s80, v27\n"
        "v_exp_f32 v72, v72\n"
        "v_exp_f32 v73, v73\n"
        "v_exp_f32 v74, v74\n"
        "v_exp_f32 v75, v75\n"
        "v_mul_f32 v24, v24, v28\n"
        "v_mul_f32 v25, v25, v29\n"
        "v_mul_f32 v26, v26, v30\n"
        "v_mul_f32 v27, v27, v31\n"
        "v_add_f32 v72, 1.0, v72\n"
        "v_add_f32 v73, 1.0, v73\n"
        "v_add_f32 v74, 1.0, v74\n"
        "v_add_f32 v75, 1.0, v75\n"
        "v_rcp_f32 v72, v72\n"
        "v_rcp_f32 v73, v73\n"
        "v_rcp_f32 v74, v74\n"
        "v_rcp_f32 v75, v75\n"
        "v_mul_f32 v24, v24, v72\n"
        "v_mul_f32 v25, v25, v73\n"
        "v_mul_f32 v26, v26, v74\n"
        "v_mul_f32 v27, v27, v75\n"
        "v_cvt_pk_bf16_f32 v28, v24, v25\n"
        "v_cvt_pk_bf16_f32 v29, v26, v27\n"
        "s_waitcnt vmcnt(3)\n"
        "buffer_store_dword v5, v3, s[40:43], s84 offen sc1\n"
        "s_mov_b32 s84, s89\n"
        "v_mul_f32 v76, s80, v32\n"
        "v_mul_f32 v77, s80, v33\n"
        "v_mul_f32 v78, s80, v34\n"
        "v_mul_f32 v79, s80, v35\n"
        "v_exp_f32 v76, v76\n"
        "v_exp_f32 v77, v77\n"
        "v_exp_f32 v78, v78\n"
        "v_exp_f32 v79, v79\n"
        "v_mul_f32 v32, v32, v36\n"
        "v_mul_f32 v33, v33, v37\n"
        "v_mul_f32 v34, v34, v38\n"
        "v_mul_f32 v35, v35, v39\n"
        "v_add_f32 v76, 1.0, v76\n"
        "v_add_f32 v77, 1.0, v77\n"
        "v_add_f32 v78, 1.0, v78\n"
        "v_add_f32 v79, 1.0, v79\n"
        "v_rcp_f32 v76, v76\n"
        "v_rcp_f32 v77, v77\n"
        "v_rcp_f32 v78, v78\n"
        "v_rcp_f32 v79, v79\n"
        "v_mul_f32 v32, v32, v76\n"
        "v_mul_f32 v33, v33, v77\n"
        "v_mul_f32 v34, v34, v78\n"
        "v_mul_f32 v35, v35, v79\n"
        "v_cvt_pk_bf16_f32 v30, v32, v33\n"
        "v_cvt_pk_bf16_f32 v31, v34, v35\n"
        "buffer_store_dwordx4 v[28:31], v1, s[40:43], s72 offen offset:0 sc1\n"
        "v_mul_f32 v72, s80, v40\n"
        "v_mul_f32 v73, s80, v41\n"
        "v_mul_f32 v74, s80, v42\n"
        "v_mul_f32 v75, s80, v43\n"
        "v_exp_f32 v72, v72\n"
        "v_exp_f32 v73, v73\n"
        "v_exp_f32 v74, v74\n"
        "v_exp_f32 v75, v75\n"
        "v_mul_f32 v40, v40, v44\n"
        "v_mul_f32 v41, v41, v45\n"
        "v_mul_f32 v42, v42, v46\n"
        "v_mul_f32 v43, v43, v47\n"
        "v_add_f32 v72, 1.0, v72\n"
        "v_add_f32 v73, 1.0, v73\n"
        "v_add_f32 v74, 1.0, v74\n"
        "v_add_f32 v75, 1.0, v75\n"
        "v_rcp_f32 v72, v72\n"
        "v_rcp_f32 v73, v73\n"
        "v_rcp_f32 v74, v74\n"
        "v_rcp_f32 v75, v75\n"
        "v_mul_f32 v40, v40, v72\n"
        "v_mul_f32 v41, v41, v73\n"
        "v_mul_f32 v42, v42, v74\n"
        "v_mul_f32 v43, v43, v75\n"
        "v_cvt_pk_bf16_f32 v44, v40, v41\n"
        "v_cvt_pk_bf16_f32 v45, v42, v43\n"
        "s_nop 0\n"
        "v_permlane16_swap_b32 v44, v46\n"
        "v_permlane16_swap_b32 v45, v47\n"
        "buffer_store_dwordx4 v[44:47], v7, s[40:43], s83 offen offset:0 sc1\n"
        "s_waitcnt vmcnt(5)\n"
        "v_cmp_gt_i32 vcc, s70, v4\n"
        "s_nop 0\n"
        "s_and_b64 vcc, vcc, 1\n"
        "s_cbranch_vccnz .Lf1_late0_%=\n"
        "buffer_load_dwordx4 v1, s[40:43], s71 offen offset:0 sc1 lds\n"
        "buffer_load_dwordx4 v1, s[40:43], s71 offen offset:1024 sc1 lds\n"
        "buffer_load_dwordx4 v1, s[40:43], s71 offen offset:2048 sc1 lds\n"
        "buffer_load_dwordx4 v1, s[40:43], s71 offen offset:3072 sc1 lds\n"
        "s_add_u32 m0, s78, s85\n"
        "s_add_u32 s69, s69, 1\n"
        "buffer_load_dwordx4 v1, s[40:43], s81 offen offset:0 sc1 lds\n"
        "s_waitcnt vmcnt(5)\n"
        "v_mov_b32 v5, s74\n"
        "buffer_store_dword v5, v3, s[40:43], s73 offen sc1\n"
        "s_waitcnt vmcnt(1)\n"
        "s_branch .Lf1_go0_%=\n"
        ".p2align 5\n"
        ".Lf1_late0_%=:\n"
        "s_waitcnt vmcnt(0)\n"
        "v_mov_b32 v5, s74\n"
        "buffer_store_dword v5, v3, s[40:43], s73 offen sc1\n"
        "s_mov_b32 s64, 0\n"
        ".Lf1_in0_top_%=:\n"
        "buffer_load_dword v4, v2, s[40:43], s86 offen sc1\n"
        "s_waitcnt vmcnt(0)\n"
        "v_cmp_gt_i32 vcc, s70, v4\n"
        "s_nop 0\n"
        "s_and_b64 vcc, vcc, 1\n"
        "s_cbranch_vccz .Lf1_in0_ok_%=\n"
        "s_add_u32 s64, s64, 1\n"
        "s_and_b32 s65, s64, 255\n"
        "s_nop 0\n"
        "s_cbranch_scc1 .Lf1_in0_go_%=\n"
        "s_mov_b32 s65, 49152\n"
        "s_cmp_lt_u32 s64, 1048576\n"
        "s_nop 0\n"
        "s_nop 0\n"
        "s_nop 0\n"
        "s_cbranch_scc1 .Lf1_in0_chk_%=\n"
        "v_mov_b32 v4, 1\n"
        "buffer_store_dword v4, off, s[40:43], s65 sc1\n"
        "s_waitcnt vmcnt(0)\n"
        ".Lf1_in0_chk_%=:\n"
        "buffer_load_dword v4, off, s[40:43], s65 sc1\n"
        "s_waitcnt vmcnt(0)\n"
        "v_readfirstlane_b32 s65, v4\n"
        "s_nop 0\n"
        "s_cmp_lg_u32 s65, 1\n"
        "s_cbranch_scc1 .Lf1_in0_go_%=\n"
        "s_endpgm\n"
        ".p2align 5\n"
        ".Lf1_in0_go_%=:\n"
        "s_branch .Lf1_in0_top_%=\n"
        ".p2align 5\n"
        ".Lf1_in0_ok_%=:\n"
        "buffer_load_dwordx4 v1, s[40:43], s71 offen offset:0 sc1 lds\n"
        "buffer_load_dwordx4 v1, s[40:43], s71 offen offset:1024 sc1 lds\n"
        "buffer_load_dwordx4 v1, s[40:43], s71 offen offset:2048 sc1 lds\n"
        "buffer_load_dwordx4 v1, s[40:43], s71 offen offset:3072 sc1 lds\n"
        "s_add_u32 m0, s78, s85\n"
        "s_add_u32 s69, s69, 1\n"
        "buffer_load_dwordx4 v1, s[40:43], s81 offen offset:0 sc1 lds\n"
        "s_waitcnt vmcnt(0)\n"
        ".Lf1_go0_%=:\n"
        "s_mov_b32 s72, 241172480\n"
        "s_mov_b32 s83, 241172480\n"
        "s_mov_b32 s73, 241172480\n"
        "v_add_u32 v0, s78, v6\n"
        "s_barrier\n"
        "ds_read_b128 v[8:11], v0 offset:0\n"
        "ds_read_b128 v[12:15], v0 offset:512\n"
        "ds_read_b128 v[16:19], v0 offset:1024\n"
        "s_waitcnt lgkmcnt(2)\n"
        "s_branch .Lf1_i1_%=\n"
        ".p2align 5\n"
        ".Lf1_i1_%=:\n"
        "v_mfma_f32_16x16x32_bf16 v[48:51], a[0:3], v[8:11], 0\n"
        "s_add_u32 s86, s76, 0\n"
        "buffer_load_dword v4, v2, s[40:43], s86 offen sc1\n"
        "v_mfma_f32_16x16x32_bf16 v[52:55], a[4:7], v[8:11], 0\n"
        "s_lshr_b32 s67, s69, 1\n"
        "v_mul_f32 v72, s80, v24\n"
        "v_mul_f32 v73, s80, v25\n"
        "v_mfma_f32_16x16x32_bf16 v[56:59], a[8:11], v[8:11], 0\n"
        "s_lshr_b32 s65, s67, 1\n"
        "v_mul_f32 v74, s80, v26\n"
        "v_mul_f32 v75, s80, v27\n"
        "v_mfma_f32_16x16x32_bf16 v[60:63], a[12:15], v[8:11], 0\n"
        "s_bitcmp1_b32 s54, s65\n"
        "s_cselect_b32 s65, 8192, 0\n"
        "v_mul_f32 v76, s80, v32\n"
        "v_mfma_f32_16x16x32_bf16 v[64:67], a[16:19], v[8:11], 0\n"
        "s_lshl_b32 s77, s67, 20\n"
        "v_exp_f32 v72, v72\n"
        "v_mfma_f32_16x16x32_bf16 v[68:71], a[20:23], v[8:11], 0\n"
        "s_waitcnt lgkmcnt(1)\n"
        "ds_read_b128 v[8:11], v0 offset:1536\n"
        "v_mul_f32 v77, s80, v33\n"
        "v_mfma_f32_16x16x32_bf16 v[48:51], a[24:27], v[12:15], v[48:51]\n"
        "s_add_u32 s77, s77, s65\n"
        "v_exp_f32 v73, v73\n"
        "v_mfma_f32_16x16x32_bf16 v[52:55], a[28:31], v[12:15], v[52:55]\n"
        "s_add_u32 s71, s77, s58\n"
        "v_exp_f32 v74, v74\n"
        "v_mfma_f32_16x16x32_bf16 v[56:59], a[32:35], v[12:15], v[56:59]\n"
        "s_add_u32 s81, s77, s62\n"
        "v_exp_f32 v75, v75\n"
        "v_mfma_f32_16x16x32_bf16 v[60:63], a[36:39], v[12:15], v[60:63]\n"
        "s_add_u32 s78, s78, 18432\n"
        "v_mul_f32 v24, v24, v28\n"
        "v_mul_f32 v25, v25, v29\n"
        "v_mfma_f32_16x16x32_bf16 v[64:67], a[40:43], v[12:15], v[64:67]\n"
        "s_cmp_eq_u32 s78, 55296\n"
        "s_cselect_b32 s78, 0, s78\n"
        "v_mul_f32 v26, v26, v30\n"
        "v_mfma_f32_16x16x32_bf16 v[68:71], a[44:47], v[12:15], v[68:71]\n"
        "s_waitcnt lgkmcnt(1)\n"
        "ds_read_b128 v[12:15], v0 offset:2048\n"
        "v_mul_f32 v27, v27, v31\n"
        "v_mfma_f32_16x16x32_bf16 v[48:51], a[48:51], v[16:19], v[48:51]\n"
        "s_add_u32 m0, s78, s61\n"
        "v_add_f32 v72, 1.0, v72\n"
        "v_add_f32 v73, 1.0, v73\n"
        "v_mfma_f32_16x16x32_bf16 v[52:55], a[52:55], v[16:19], v[52:55]\n"
        "s_lshr_b32 s70, s69, 1\n"
        "v_add_f32 v74, 1.0, v74\n"
        "v_add_f32 v75, 1.0, v75\n"
        "v_mfma_f32_16x16x32_bf16 v[56:59], a[56:59], v[16:19], v[56:59]\n"
        "s_waitcnt vmcnt(3)\n"
        "v_rcp_f32 v72, v72\n"
        "v_mfma_f32_16x16x32_bf16 v[60:63], a[60:63], v[16:19], v[60:63]\n"
        "buffer_store_dword v5, v3, s[40:43], s84 offen sc1\n"
        "v_mfma_f32_16x16x32_bf16 v[64:67], a[64:67], v[16:19], v[64:67]\n"
        "v_rcp_f32 v73, v73\n"
        "v_mfma_f32_16x16x32_bf16 v[68:71], a[68:71], v[16:19], v[68:71]\n"
        "s_waitcnt lgkmcnt(1)\n"
        "ds_read_b128 v[16:19], v0 offset:2560\n"
        "v_mul_f32 v78, s80, v34\n"
        "v_mfma_f32_16x16x32_bf16 v[48:51], a[72:75], v[8:11], v[48:51]\n"
        "v_rcp_f32 v74, v74\n"
        "v_mfma_f32_16x16x32_bf16 v[52:55], a[76:79], v[8:11], v[52:55]\n"
        "v_rcp_f32 v75, v75\n"
        "v_mfma_f32_16x16x32_bf16 v[56:59], a[80:83], v[8:11], v[56:59]\n"
        "v_mul_f32 v24, v24, v72\n"
        "v_mul_f32 v25, v25, v73\n"
        "v_mfma_f32_16x16x32_bf16 v[60:63], a[84:87], v[8:11], v[60:63]\n"
        "v_mul_f32 v26, v26, v74\n"
        "v_mul_f32 v27, v27, v75\n"
        "v_mfma_f32_16x16x32_bf16 v[64:67], a[88:91], v[8:11], v[64:67]\n"
        "v_cvt_pk_bf16_f32 v28, v24, v25\n"
        "v_cvt_pk_bf16_f32 v29, v26, v27\n"
        "v_mfma_f32_16x16x32_bf16 v[68:71], a[92:95], v[8:11], v[68:71]\n"
        "s_waitcnt lgkmcnt(1)\n"
        "ds_read_b128 v[8:11], v0 offset:3072\n"
        "v_mul_f32 v79, s80, v35\n"
        "v_mfma_f32_16x16x32_bf16 v[48:51], a[96:99], v[12:15], v[48:51]\n"
        "v_exp_f32 v76, v76\n"
        "v_mfma_f32_16x16x32_bf16 v[52:55], a[100:103], v[12:15], v[52:55]\n"
        "v_exp_f32 v77, v77\n"
        "v_mfma_f32_16x16x32_bf16 v[56:59], a[104:107], v[12:15], v[56:59]\n"
        "v_exp_f32 v78, v78\n"
        "v_mfma_f32_16x16x32_bf16 v[60:63], a[108:111], v[12:15], v[60:63]\n"
        "v_exp_f32 v79, v79\n"
        "v_mfma_f32_16x16x32_bf16 v[64:67], a[112:115], v[12:15], v[64:67]\n"
        "s_waitcnt vmcnt(1)\n"
        "v_cmp_gt_i32 vcc, s70, v4\n"
        "v_mul_f32 v32, v32, v36\n"
        "v_mfma_f32_16x16x32_bf16 v[68:71], a[116:119], v[12:15], v[68:71]\n"
        "s_waitcnt lgkmcnt(1)\n"
        "ds_read_b128 v[12:15], v0 offset:3584\n"
        "v_mul_f32 v33, v33, v37\n"
        "v_mfma_f32_16x16x32_bf16 v[48:51], a[120:123], v[16:19], v[48:51]\n"
        "s_and_b64 vcc, vcc, 1\n"
        "v_mul_f32_e64 v34, v34, v38\n"
        "v_mfma_f32_16x16x32_bf16 v[52:55], a[124:127], v[16:19], v[52:55]\n"
        "s_cbranch_vccnz .Lf1_i10_%=\n"
        "v_mfma_f32_16x16x32_bf16 v[56:59], a[128:131], v[16:19], v[56:59]\n"
        "buffer_load_dwordx4 v1, s[40:43], s71 offen offset:0 sc1 lds\n"
        "v_mfma_f32_16x16x32_bf16 v[60:63], a[132:135], v[16:19], v[60:63]\n"
        "v_mul_f32 v35, v35, v39\n"
        "v_add_f32 v76, 1.0, v76\n"
        "v_mfma_f32_16x16x32_bf16 v[64:67], a[136:139], v[16:19], v[64:67]\n"
        "v_add_f32 v77, 1.0, v77\n"
        "v_add_f32 v78, 1.0, v78\n"
        "v_mfma_f32_16x16x32_bf16 v[68:71], a[140:143], v[16:19], v[68:71]\n"
        "s_waitcnt lgkmcnt(1)\n"
        "ds_read_b128 v[16:19], v0 offset:4096\n"
        "v_add_f32 v79, 1.0, v79\n"
        "v_mfma_f32_16x16x32_bf16 v[48:51], a[144:147], v[8:11], v[48:51]\n"
        "buffer_load_dwordx4 v1, s[40:43], s71 offen offset:1024 sc1 lds\n"
        "v_mfma_f32_16x16x32_bf16 v[52:55], a[148:151], v[8:11], v[52:55]\n"
        "v_rcp_f32 v76, v76\n"
        "v_mfma_f32_16x16x32_bf16 v[56:59], a[152:155], v[8:11], v[56:59]\n"
        "v_rcp_f32 v77, v77\n"
        "v_mfma_f32_16x16x32_bf16 v[60:63], a[156:159], v[8:11], v[60:63]\n"
        "v_rcp_f32 v78, v78\n"
        "v_mfma_f32_16x16x32_bf16 v[64:67], a[160:163], v[8:11], v[64:67]\n"
        "buffer_load_dwordx4 v1, s[40:43], s71 offen offset:2048 sc1 lds\n"
        "v_mfma_f32_16x16x32_bf16 v[68:71], a[164:167], v[8:11], v[68:71]\n"
        "s_waitcnt lgkmcnt(1)\n"
        "ds_read_b128 v[8:11], v0 offset:4608\n"
        "v_mul_f32 v72, s80, v40\n"
        "v_mfma_f32_16x16x32_bf16 v[48:51], a[168:171], v[12:15], v[48:51]\n"
        "v_rcp_f32 v79, v79\n"
        "v_mfma_f32_16x16x32_bf16 v[52:55], a[172:175], v[12:15], v[52:55]\n"
        "v_mul_f32 v32, v32, v76\n"
        "v_mul_f32 v33, v33, v77\n"
        "v_mfma_f32_16x16x32_bf16 v[56:59], a[176:179], v[12:15], v[56:59]\n"
        "buffer_load_dwordx4 v1, s[40:43], s71 offen offset:3072 sc1 lds\n"
        "v_mfma_f32_16x16x32_bf16 v[60:63], a[180:183], v[12:15], v[60:63]\n"
        "s_add_u32 m0, s78, s85\n"
        "s_add_u32 s69, s69, 1\n"
        "v_mul_f32 v34, v34, v78\n"
        "v_mfma_f32_16x16x32_bf16 v[64:67], a[184:187], v[12:15], v[64:67]\n"
        "v_mul_f32 v35, v35, v79\n"
        "v_cvt_pk_bf16_f32 v30, v32, v33\n"
        "v_mfma_f32_16x16x32_bf16 v[68:71], a[188:191], v[12:15], v[68:71]\n"
        "s_waitcnt lgkmcnt(1)\n"
        "ds_read_b128 v[12:15], v0 offset:5120\n"
        "v_cvt_pk_bf16_f32 v31, v34, v35\n"
        "v_mfma_f32_16x16x32_bf16 v[48:51], a[192:195], v[16:19], v[48:51]\n"
        "buffer_load_dwordx4 v1, s[40:43], s81 offen offset:0 sc1 lds\n"
        "v_mfma_f32_16x16x32_bf16 v[52:55], a[196:199], v[16:19], v[52:55]\n"
        "buffer_store_dwordx4 v[28:31], v1, s[40:43], s72 offen offset:0 sc1\n"
        "v_mfma_f32_16x16x32_bf16 v[56:59], a[200:203], v[16:19], v[56:59]\n"
        "v_mul_f32 v73, s80, v41\n"
        "v_mul_f32 v74, s80, v42\n"
        "v_mfma_f32_16x16x32_bf16 v[60:63], a[204:207], v[16:19], v[60:63]\n"
        "v_mul_f32 v75, s80, v43\n"
        "v_mfma_f32_16x16x32_bf16 v[64:67], a[208:211], v[16:19], v[64:67]\n"
        "v_exp_f32 v72, v72\n"
        "v_mfma_f32_16x16x32_bf16 v[68:71], a[212:215], v[16:19], v[68:71]\n"
        "s_waitcnt lgkmcnt(1)\n"
        "ds_read_b128 v[16:19], v0 offset:5632\n"
        "v_mfma_f32_16x16x32_bf16 v[48:51], a[216:219], v[8:11], v[48:51]\n"
        "v_exp_f32 v73, v73\n"
        "v_mfma_f32_16x16x32_bf16 v[52:55], a[220:223], v[8:11], v[52:55]\n"
        "v_exp_f32 v74, v74\n"
        "v_mfma_f32_16x16x32_bf16 v[56:59], a[224:227], v[8:11], v[56:59]\n"
        "v_exp_f32 v75, v75\n"
        "v_mfma_f32_16x16x32_bf16 v[60:63], a[228:231], v[8:11], v[60:63]\n"
        "v_mul_f32 v40, v40, v44\n"
        "v_mul_f32 v41, v41, v45\n"
        "v_mfma_f32_16x16x32_bf16 v[64:67], a[232:235], v[8:11], v[64:67]\n"
        "v_mul_f32 v42, v42, v46\n"
        "v_mul_f32 v43, v43, v47\n"
        "v_mfma_f32_16x16x32_bf16 v[68:71], a[236:239], v[8:11], v[68:71]\n"
        "s_waitcnt lgkmcnt(1)\n"
        "ds_read_b128 v[8:11], v0 offset:6144\n"
        "v_add_f32 v72, 1.0, v72\n"
        "v_mfma_f32_16x16x32_bf16 v[48:51], a[240:243], v[12:15], v[48:51]\n"
        "v_add_f32 v73, 1.0, v73\n"
        "v_add_f32 v74, 1.0, v74\n"
        "v_mfma_f32_16x16x32_bf16 v[52:55], a[244:247], v[12:15], v[52:55]\n"
        "v_add_f32 v75, 1.0, v75\n"
        "v_mfma_f32_16x16x32_bf16 v[56:59], a[248:251], v[12:15], v[56:59]\n"
        "v_rcp_f32 v72, v72\n"
        "v_mfma_f32_16x16x32_bf16 v[60:63], a[252:255], v[12:15], v[60:63]\n"
        "v_rcp_f32 v73, v73\n"
        "v_mfma_f32_16x16x32_bf16 v[64:67], v[80:83], v[12:15], v[64:67]\n"
        "v_rcp_f32 v74, v74\n"
        "v_mfma_f32_16x16x32_bf16 v[68:71], v[84:87], v[12:15], v[68:71]\n"
        "s_waitcnt lgkmcnt(1)\n"
        "ds_read_b128 v[12:15], v0 offset:6656\n"
        "v_mfma_f32_16x16x32_bf16 v[48:51], v[88:91], v[16:19], v[48:51]\n"
        "v_rcp_f32 v75, v75\n"
        "v_mfma_f32_16x16x32_bf16 v[52:55], v[92:95], v[16:19], v[52:55]\n"
        "v_mul_f32 v40, v40, v72\n"
        "v_mul_f32 v41, v41, v73\n"
        "v_mfma_f32_16x16x32_bf16 v[56:59], v[96:99], v[16:19], v[56:59]\n"
        "v_mul_f32 v42, v42, v74\n"
        "v_mul_f32 v43, v43, v75\n"
        "v_mfma_f32_16x16x32_bf16 v[60:63], v[100:103], v[16:19], v[60:63]\n"
        "v_cvt_pk_bf16_f32 v44, v40, v41\n"
        "v_cvt_pk_bf16_f32 v45, v42, v43\n"
        "v_mfma_f32_16x16x32_bf16 v[64:67], v[104:107], v[16:19], v[64:67]\n"
        "s_nop 0\n"
        "v_permlane16_swap_b32 v44, v46\n"
        "v_mfma_f32_16x16x32_bf16 v[68:71], v[108:111], v[16:19], v[68:71]\n"
        "s_waitcnt lgkmcnt(1)\n"
        "ds_read_b128 v[16:19], v0 offset:7168\n"
        "v_mfma_f32_16x16x32_bf16 v[48:51], v[112:115], v[8:11], v[48:51]\n"
        "v_permlane16_swap_b32 v45, v47\n"
        "v_mfma_f32_16x16x32_bf16 v[52:55], v[116:119], v[8:11], v[52:55]\n"
        "buffer_store_dwordx4 v[44:47], v7, s[40:43], s83 offen offset:0 sc1\n"
        "v_mfma_f32_16x16x32_bf16 v[56:59], v[120:123], v[8:11], v[56:59]\n"
        "v_mov_b32 v5, s74\n"
        "v_mfma_f32_16x16x32_bf16 v[60:63], v[124:127], v[8:11], v[60:63]\n"
        "s_add_u32 s74, s74, 1\n"
        "v_mfma_f32_16x16x32_bf16 v[64:67], v[128:131], v[8:11], v[64:67]\n"
        "s_add_u32 s72, s75, s59\n"
        "v_mfma_f32_16x16x32_bf16 v[68:71], v[132:135], v[8:11], v[68:71]\n"
        "s_waitcnt lgkmcnt(1)\n"
        "ds_read_b128 v[8:11], v0 offset:7680\n"
        "v_mfma_f32_16x16x32_bf16 v[48:51], v[136:139], v[12:15], v[48:51]\n"
        "s_add_u32 s83, s75, s63\n"
        "v_mfma_f32_16x16x32_bf16 v[52:55], v[140:143], v[12:15], v[52:55]\n"
        "s_add_u32 s72, s72, 49152\n"
        "v_mfma_f32_16x16x32_bf16 v[56:59], v[144:147], v[12:15], v[56:59]\n"
        "s_add_u32 s83, s83, 4096\n"
        "v_mfma_f32_16x16x32_bf16 v[60:63], v[148:151], v[12:15], v[60:63]\n"
        "v_mfma_f32_16x16x32_bf16 v[64:67], v[152:155], v[12:15], v[64:67]\n"
        "v_mfma_f32_16x16x32_bf16 v[68:71], v[156:159], v[12:15], v[68:71]\n"
        "s_waitcnt lgkmcnt(1)\n"
        "ds_read_b128 v[12:15], v0 offset:8192\n"
        "v_mfma_f32_16x16x32_bf16 v[48:51], v[160:163], v[16:19], v[48:51]\n"
        "v_mfma_f32_16x16x32_bf16 v[52:55], v[164:167], v[16:19], v[52:55]\n"
        "v_mfma_f32_16x16x32_bf16 v[56:59], v[168:171], v[16:19], v[56:59]\n"
        "v_mfma_f32_16x16x32_bf16 v[60:63], v[172:175], v[16:19], v[60:63]\n"
        "v_mfma_f32_16x16x32_bf16 v[64:67], v[176:179], v[16:19], v[64:67]\n"
        "v_mfma_f32_16x16x32_bf16 v[68:71], v[180:183], v[16:19], v[68:71]\n"
        "s_waitcnt lgkmcnt(1)\n"
        "ds_read_b128 v[16:19], v0 offset:8704\n"
        "v_mfma_f32_16x16x32_bf16 v[48:51], v[184:187], v[8:11], v[48:51]\n"
        "v_mfma_f32_16x16x32_bf16 v[52:55], v[188:191], v[8:11], v[52:55]\n"
        "v_mfma_f32_16x16x32_bf16 v[56:59], v[192:195], v[8:11], v[56:59]\n"
        "v_mfma_f32_16x16x32_bf16 v[60:63], v[196:199], v[8:11], v[60:63]\n"
        "v_mfma_f32_16x16x32_bf16 v[64:67], v[200:203], v[8:11], v[64:67]\n"
        "s_waitcnt vmcnt(2)\n"
        "s_barrier\n"
        "v_add_u32 v0, s78, v6\n"
        "v_mfma_f32_16x16x32_bf16 v[68:71], v[204:207], v[8:11], v[68:71]\n"
        "s_waitcnt lgkmcnt(1)\n"
        "ds_read_b128 v[8:11], v0 offset:0\n"
        "v_mfma_f32_16x16x32_bf16 v[48:51], v[208:211], v[12:15], v[48:51]\n"
        "v_mfma_f32_16x16x32_bf16 v[52:55], v[212:215], v[12:15], v[52:55]\n"
        "s_getreg_b32 s88, hwreg(HW_REG_IB_STS, 0, 4)\n"
        "v_mfma_f32_16x16x32_bf16 v[56:59], v[216:219], v[12:15], v[56:59]\n"
        "s_cmp_le_u32 s88, 0\n"
        "s_cselect_b32 s88, s73, s89\n"
        "s_cselect_b32 s73, s89, s73\n"
        "v_mfma_f32_16x16x32_bf16 v[60:63], v[220:223], v[12:15], v[60:63]\n"
        "buffer_store_dword v5, v3, s[40:43], s88 offen sc1\n"
        "v_mfma_f32_16x16x32_bf16 v[64:67], v[224:227], v[12:15], v[64:67]\n"
        "v_mfma_f32_16x16x32_bf16 v[68:71], v[228:231], v[12:15], v[68:71]\n"
        "s_waitcnt lgkmcnt(1)\n"
        "ds_read_b128 v[12:15], v0 offset:512\n"
        "v_mfma_f32_16x16x32_bf16 v[48:51], v[232:235], v[16:19], v[48:51]\n"
        "v_mfma_f32_16x16x32_bf16 v[52:55], v[236:239], v[16:19], v[52:55]\n"
        "v_mfma_f32_16x16x32_bf16 v[56:59], v[240:243], v[16:19], v[56:59]\n"
        "s_getreg_b32 s88, hwreg(HW_REG_IB_STS, 0, 4)\n"
        "v_mfma_f32_16x16x32_bf16 v[60:63], v[244:247], v[16:19], v[60:63]\n"
        "s_cmp_le_u32 s88, 1\n"
        "s_cselect_b32 s88, s73, s89\n"
        "s_cselect_b32 s73, s89, s73\n"
        "v_mfma_f32_16x16x32_bf16 v[64:67], v[248:251], v[16:19], v[64:67]\n"
        "buffer_store_dword v5, v3, s[40:43], s88 offen sc1\n"
        "v_mfma_f32_16x16x32_bf16 v[68:71], v[252:255], v[16:19], v[68:71]\n"
        "s_waitcnt lgkmcnt(1)\n"
        "ds_read_b128 v[16:19], v0 offset:1024\n"
        "s_mov_b32 s84, s73\n"
        "s_mov_b32 s73, s60\n"
        "s_branch .Lf1_i0_%=\n"
        ".p2align 5\n"
        ".Lf1_i10_%=:\n"
        "v_mfma_f32_16x16x32_bf16 v[56:59], a[128:131], v[16:19], v[56:59]\n"
        "buffer_load_dword v4, v2, s[40:43], s86 offen sc1\n"
        "v_mfma_f32_16x16x32_bf16 v[60:63], a[132:135], v[16:19], v[60:63]\n"
        "v_mul_f32 v35, v35, v39\n"
        "v_add_f32 v76, 1.0, v76\n"
        "v_mfma_f32_16x16x32_bf16 v[64:67], a[136:139], v[16:19], v[64:67]\n"
        "v_add_f32 v77, 1.0, v77\n"
        "v_add_f32 v78, 1.0, v78\n"
        "v_mfma_f32_16x16x32_bf16 v[68:71], a[140:143], v[16:19], v[68:71]\n"
        "s_waitcnt lgkmcnt(1)\n"
        "ds_read_b128 v[16:19], v0 offset:4096\n"
        "v_add_f32 v79, 1.0, v79\n"
        "v_mfma_f32_16x16x32_bf16 v[48:51], a[144:147], v[8:11], v[48:51]\n"
        "v_rcp_f32 v76, v76\n"
        "v_mfma_f32_16x16x32_bf16 v[52:55], a[148:151], v[8:11], v[52:55]\n"
        "v_rcp_f32 v77, v77\n"
        "v_mfma_f32_16x16x32_bf16 v[56:59], a[152:155], v[8:11], v[56:59]\n"
        "v_rcp_f32 v78, v78\n"
        "v_mfma_f32_16x16x32_bf16 v[60:63], a[156:159], v[8:11], v[60:63]\n"
        "v_rcp_f32 v79, v79\n"
        "v_mfma_f32_16x16x32_bf16 v[64:67], a[160:163], v[8:11], v[64:67]\n"
        "v_mul_f32 v32, v32, v76\n"
        "v_mul_f32 v33, v33, v77\n"
        "v_mfma_f32_16x16x32_bf16 v[68:71], a[164:167], v[8:11], v[68:71]\n"
        "s_waitcnt lgkmcnt(1)\n"
        "ds_read_b128 v[8:11], v0 offset:4608\n"
        "v_mul_f32 v34, v34, v78\n"
        "v_mfma_f32_16x16x32_bf16 v[48:51], a[168:171], v[12:15], v[48:51]\n"
        "v_mul_f32 v35, v35, v79\n"
        "v_cvt_pk_bf16_f32 v30, v32, v33\n"
        "v_mfma_f32_16x16x32_bf16 v[52:55], a[172:175], v[12:15], v[52:55]\n"
        "v_cvt_pk_bf16_f32 v31, v34, v35\n"
        "v_mul_f32 v72, s80, v40\n"
        "v_mfma_f32_16x16x32_bf16 v[56:59], a[176:179], v[12:15], v[56:59]\n"
        "buffer_store_dwordx4 v[28:31], v1, s[40:43], s72 offen offset:0 sc1\n"
        "v_mfma_f32_16x16x32_bf16 v[60:63], a[180:183], v[12:15], v[60:63]\n"
        "v_mul_f32 v73, s80, v41\n"
        "v_mul_f32 v74, s80, v42\n"
        "v_mfma_f32_16x16x32_bf16 v[64:67], a[184:187], v[12:15], v[64:67]\n"
        "v_mul_f32 v75, s80, v43\n"
        "v_mfma_f32_16x16x32_bf16 v[68:71], a[188:191], v[12:15], v[68:71]\n"
        "s_waitcnt lgkmcnt(1)\n"
        "ds_read_b128 v[12:15], v0 offset:5120\n"
        "v_mfma_f32_16x16x32_bf16 v[48:51], a[192:195], v[16:19], v[48:51]\n"
        "v_exp_f32 v72, v72\n"
        "v_mfma_f32_16x16x32_bf16 v[52:55], a[196:199], v[16:19], v[52:55]\n"
        "v_exp_f32 v73, v73\n"
        "v_mfma_f32_16x16x32_bf16 v[56:59], a[200:203], v[16:19], v[56:59]\n"
        "v_exp_f32 v74, v74\n"
        "v_mfma_f32_16x16x32_bf16 v[60:63], a[204:207], v[16:19], v[60:63]\n"
        "v_exp_f32 v75, v75\n"
        "v_mfma_f32_16x16x32_bf16 v[64:67], a[208:211], v[16:19], v[64:67]\n"
        "v_mul_f32 v40, v40, v44\n"
        "v_mul_f32 v41, v41, v45\n"
        "v_mfma_f32_16x16x32_bf16 v[68:71], a[212:215], v[16:19], v[68:71]\n"
        "s_waitcnt lgkmcnt(1)\n"
        "ds_read_b128 v[16:19], v0 offset:5632\n"
        "v_mul_f32 v42, v42, v46\n"
        "v_mfma_f32_16x16x32_bf16 v[48:51], a[216:219], v[8:11], v[48:51]\n"
        "v_mul_f32 v43, v43, v47\n"
        "v_add_f32 v72, 1.0, v72\n"
        "v_mfma_f32_16x16x32_bf16 v[52:55], a[220:223], v[8:11], v[52:55]\n"
        "v_add_f32 v73, 1.0, v73\n"
        "v_add_f32 v74, 1.0, v74\n"
        "v_mfma_f32_16x16x32_bf16 v[56:59], a[224:227], v[8:11], v[56:59]\n"
        "v_add_f32 v75, 1.0, v75\n"
        "v_mfma_f32_16x16x32_bf16 v[60:63], a[228:231], v[8:11], v[60:63]\n"
        "v_rcp_f32 v72, v72\n"
        "v_mfma_f32_16x16x32_bf16 v[64:67], a[232:235], v[8:11], v[64:67]\n"
        "v_rcp_f32 v73, v73\n"
        "v_mfma_f32_16x16x32_bf16 v[68:71], a[236:239], v[8:11], v[68:71]\n"
        "s_waitcnt lgkmcnt(1)\n"
        "ds_read_b128 v[8:11], v0 offset:6144\n"
        "s_waitcnt vmcnt(1)\n"
        "v_cmp_gt_i32 vcc, s70, v4\n"
        "v_mfma_f32_16x16x32_bf16 v[48:51], a[240:243], v[12:15], v[48:51]\n"
        "v_rcp_f32 v74, v74\n"
        "v_mfma_f32_16x16x32_bf16 v[52:55], a[244:247], v[12:15], v[52:55]\n"
        "s_and_b64 vcc, vcc, 1\n"
        "v_mfma_f32_16x16x32_bf16 v[56:59], a[248:251], v[12:15], v[56:59]\n"
        "s_cbranch_vccnz .Lf1_i11_%=\n"
        "v_mfma_f32_16x16x32_bf16 v[60:63], a[252:255], v[12:15], v[60:63]\n"
        "buffer_load_dwordx4 v1, s[40:43], s71 offen offset:0 sc1 lds\n"
        "v_mfma_f32_16x16x32_bf16 v[64:67], v[80:83], v[12:15], v[64:67]\n"
        "v_rcp_f32 v75, v75\n"
        "v_mfma_f32_16x16x32_bf16 v[68:71], v[84:87], v[12:15], v[68:71]\n"
        "s_waitcnt lgkmcnt(1)\n"
        "ds_read_b128 v[12:15], v0 offset:6656\n"
        "v_mul_f32 v40, v40, v72\n"
        "v_mfma_f32_16x16x32_bf16 v[48:51], v[88:91], v[16:19], v[48:51]\n"
        "buffer_load_dwordx4 v1, s[40:43], s71 offen offset:1024 sc1 lds\n"
        "v_mfma_f32_16x16x32_bf16 v[52:55], v[92:95], v[16:19], v[52:55]\n"
        "v_mul_f32 v41, v41, v73\n"
        "v_mul_f32 v42, v42, v74\n"
        "v_mfma_f32_16x16x32_bf16 v[56:59], v[96:99], v[16:19], v[56:59]\n"
        "v_mul_f32 v43, v43, v75\n"
        "v_cvt_pk_bf16_f32 v44, v40, v41\n"
        "v_mfma_f32_16x16x32_bf16 v[60:63], v[100:103], v[16:19], v[60:63]\n"
        "buffer_load_dwordx4 v1, s[40:43], s71 offen offset:2048 sc1 lds\n"
        "v_mfma_f32_16x16x32_bf16 v[64:67], v[104:107], v[16:19], v[64:67]\n"
        "v_cvt_pk_bf16_f32 v45, v42, v43\n"
        "s_nop 0\n"
        "v_mfma_f32_16x16x32_bf16 v[68:71], v[108:111], v[16:19], v[68:71]\n"
        "s_waitcnt lgkmcnt(1)\n"
        "ds_read_b128 v[16:19], v0 offset:7168\n"
        "v_mfma_f32_16x16x32_bf16 v[48:51], v[112:115], v[8:11], v[48:51]\n"
        "buffer_load_dwordx4 v1, s[40:43], s71 offen offset:3072 sc1 lds\n"
        "v_mfma_f32_16x16x32_bf16 v[52:55], v[116:119], v[8:11], v[52:55]\n"
        "s_add_u32 m0, s78, s85\n"
        "s_add_u32 s69, s69, 1\n"
        "v_mfma_f32_16x16x32_bf16 v[56:59], v[120:123], v[8:11], v[56:59]\n"
        "v_permlane16_swap_b32 v44, v46\n"
        "v_mfma_f32_16x16x32_bf16 v[60:63], v[124:127], v[8:11], v[60:63]\n"
        "buffer_load_dwordx4 v1, s[40:43], s81 offen offset:0 sc1 lds\n"
        "v_mfma_f32_16x16x32_bf16 v[64:67], v[128:131], v[8:11], v[64:67]\n"
        "v_permlane16_swap_b32 v45, v47\n"
        "v_mfma_f32_16x16x32_bf16 v[68:71], v[132:135], v[8:11], v[68:71]\n"
        "s_waitcnt lgkmcnt(1)\n"
        "ds_read_b128 v[8:11], v0 offset:7680\n"
        "v_mfma_f32_16x16x32_bf16 v[48:51], v[136:139], v[12:15], v[48:51]\n"
        "buffer_store_dwordx4 v[44:47], v7, s[40:43], s83 offen offset:0 sc1\n"
        "v_mfma_f32_16x16x32_bf16 v[52:55], v[140:143], v[12:15], v[52:55]\n"
        "v_mov_b32 v5, s74\n"
        "v_mfma_f32_16x16x32_bf16 v[56:59], v[144:147], v[12:15], v[56:59]\n"
        "s_add_u32 s74, s74, 1\n"
        "v_mfma_f32_16x16x32_bf16 v[60:63], v[148:151], v[12:15], v[60:63]\n"
        "s_add_u32 s72, s75, s59\n"
        "v_mfma_f32_16x16x32_bf16 v[64:67], v[152:155], v[12:15], v[64:67]\n"
        "s_add_u32 s83, s75, s63\n"
        "v_mfma_f32_16x16x32_bf16 v[68:71], v[156:159], v[12:15], v[68:71]\n"
        "s_waitcnt lgkmcnt(1)\n"
        "ds_read_b128 v[12:15], v0 offset:8192\n"
        "v_mfma_f32_16x16x32_bf16 v[48:51], v[160:163], v[16:19], v[48:51]\n"
        "s_add_u32 s72, s72, 49152\n"
        "v_mfma_f32_16x16x32_bf16 v[52:55], v[164:167], v[16:19], v[52:55]\n"
        "s_add_u32 s83, s83, 4096\n"
        "v_mfma_f32_16x16x32_bf16 v[56:59], v[168:171], v[16:19], v[56:59]\n"
        "v_mfma_f32_16x16x32_bf16 v[60:63], v[172:175], v[16:19], v[60:63]\n"
        "v_mfma_f32_16x16x32_bf16 v[64:67], v[176:179], v[16:19], v[64:67]\n"
        "v_mfma_f32_16x16x32_bf16 v[68:71], v[180:183], v[16:19], v[68:71]\n"
        "s_waitcnt lgkmcnt(1)\n"
        "ds_read_b128 v[16:19], v0 offset:8704\n"
        "v_mfma_f32_16x16x32_bf16 v[48:51], v[184:187], v[8:11], v[48:51]\n"
        "v_mfma_f32_16x16x32_bf16 v[52:55], v[188:191], v[8:11], v[52:55]\n"
        "v_mfma_f32_16x16x32_bf16 v[56:59], v[192:195], v[8:11], v[56:59]\n"
        "v_mfma_f32_16x16x32_bf16 v[60:63], v[196:199], v[8:11], v[60:63]\n"
        "v_mfma_f32_16x16x32_bf16 v[64:67], v[200:203], v[8:11], v[64:67]\n"
        "s_waitcnt vmcnt(1)\n"
        "s_barrier\n"
        "v_add_u32 v0, s78, v6\n"
        "v_mfma_f32_16x16x32_bf16 v[68:71], v[204:207], v[8:11], v[68:71]\n"
        "s_waitcnt lgkmcnt(1)\n"
        "ds_read_b128 v[8:11], v0 offset:0\n"
        "v_mfma_f32_16x16x32_bf16 v[48:51], v[208:211], v[12:15], v[48:51]\n"
        "v_mfma_f32_16x16x32_bf16 v[52:55], v[212:215], v[12:15], v[52:55]\n"
        "v_mfma_f32_16x16x32_bf16 v[56:59], v[216:219], v[12:15], v[56:59]\n"
        "s_getreg_b32 s88, hwreg(HW_REG_IB_STS, 0, 4)\n"
        "v_mfma_f32_16x16x32_bf16 v[60:63], v[220:223], v[12:15], v[60:63]\n"
        "s_cmp_le_u32 s88, 0\n"
        "s_cselect_b32 s88, s73, s89\n"
        "s_cselect_b32 s73, s89, s73\n"
        "v_mfma_f32_16x16x32_bf16 v[64:67], v[224:227], v[12:15], v[64:67]\n"
        "buffer_store_dword v5, v3, s[40:43], s88 offen sc1\n"
        "v_mfma_f32_16x16x32_bf16 v[68:71], v[228:231], v[12:15], v[68:71]\n"
        "s_waitcnt lgkmcnt(1)\n"
        "ds_read_b128 v[12:15], v0 offset:512\n"
        "v_mfma_f32_16x16x32_bf16 v[48:51], v[232:235], v[16:19], v[48:51]\n"
        "v_mfma_f32_16x16x32_bf16 v[52:55], v[236:239], v[16:19], v[52:55]\n"
        "v_mfma_f32_16x16x32_bf16 v[56:59], v[240:243], v[16:19], v[56:59]\n"
        "s_getreg_b32 s88, hwreg(HW_REG_IB_STS, 0, 4)\n"
        "v_mfma_f32_16x16x32_bf16 v[60:63], v[244:247], v[16:19], v[60:63]\n"
        "s_cmp_le_u32 s88, 1\n"
        "s_cselect_b32 s88, s73, s89\n"
        "s_cselect_b32 s73, s89, s73\n"
        "v_mfma_f32_16x16x32_bf16 v[64:67], v[248:251], v[16:19], v[64:67]\n"
        "buffer_store_dword v5, v3, s[40:43], s88 offen sc1\n"
        "v_mfma_f32_16x16x32_bf16 v[68:71], v[252:255], v[16:19], v[68:71]\n"
        "s_waitcnt lgkmcnt(1)\n"
        "ds_read_b128 v[16:19], v0 offset:1024\n"
        "s_mov_b32 s84, s73\n"
        "s_mov_b32 s73, s60\n"
        "s_branch .Lf1_i0_%=\n"
        ".p2align 5\n"
        ".Lf1_i11_%=:\n"
        "v_mfma_f32_16x16x32_bf16 v[60:63], a[252:255], v[12:15], v[60:63]\n"
        "buffer_load_dword v4, v2, s[40:43], s86 offen sc1\n"
        "v_mfma_f32_16x16x32_bf16 v[64:67], v[80:83], v[12:15], v[64:67]\n"
        "v_rcp_f32 v75, v75\n"
        "v_mfma_f32_16x16x32_bf16 v[68:71], v[84:87], v[12:15], v[68:71]\n"
        "s_waitcnt lgkmcnt(1)\n"
        "ds_read_b128 v[12:15], v0 offset:6656\n"
        "v_mul_f32 v40, v40, v72\n"
        "v_mfma_f32_16x16x32_bf16 v[48:51], v[88:91], v[16:19], v[48:51]\n"
        "v_mul_f32 v41, v41, v73\n"
        "v_mul_f32 v42, v42, v74\n"
        "v_mfma_f32_16x16x32_bf16 v[52:55], v[92:95], v[16:19], v[52:55]\n"
        "v_mul_f32 v43, v43, v75\n"
        "v_cvt_pk_bf16_f32 v44, v40, v41\n"
        "v_mfma_f32_16x16x32_bf16 v[56:59], v[96:99], v[16:19], v[56:59]\n"
        "v_cvt_pk_bf16_f32 v45, v42, v43\n"
        "s_nop 0\n"
        "v_mfma_f32_16x16x32_bf16 v[60:63], v[100:103], v[16:19], v[60:63]\n"
        "v_permlane16_swap_b32 v44, v46\n"
        "v_mfma_f32_16x16x32_bf16 v[64:67], v[104:107], v[16:19], v[64:67]\n"
        "v_permlane16_swap_b32 v45, v47\n"
        "v_mfma_f32_16x16x32_bf16 v[68:71], v[108:111], v[16:19], v[68:71]\n"
        "s_waitcnt lgkmcnt(1)\n"
        "ds_read_b128 v[16:19], v0 offset:7168\n"
        "v_mfma_f32_16x16x32_bf16 v[48:51], v[112:115], v[8:11], v[48:51]\n"
        "buffer_store_dwordx4 v[44:47], v7, s[40:43], s83 offen offset:0 sc1\n"
        "v_mfma_f32_16x16x32_bf16 v[52:55], v[116:119], v[8:11], v[52:55]\n"
        "v_mov_b32 v5, s74\n"
        "v_mfma_f32_16x16x32_bf16 v[56:59], v[120:123], v[8:11], v[56:59]\n"
        "s_add_u32 s74, s74, 1\n"
        "v_mfma_f32_16x16x32_bf16 v[60:63], v[124:127], v[8:11], v[60:63]\n"
        "s_add_u32 s72, s75, s59\n"
        "v_mfma_f32_16x16x32_bf16 v[64:67], v[128:131], v[8:11], v[64:67]\n"
        "s_add_u32 s83, s75, s63\n"
        "v_mfma_f32_16x16x32_bf16 v[68:71], v[132:135], v[8:11], v[68:71]\n"
        "s_waitcnt lgkmcnt(1)\n"
        "ds_read_b128 v[8:11], v0 offset:7680\n"
        "v_mfma_f32_16x16x32_bf16 v[48:51], v[136:139], v[12:15], v[48:51]\n"
        "s_add_u32 s72, s72, 49152\n"
        "v_mfma_f32_16x16x32_bf16 v[52:55], v[140:143], v[12:15], v[52:55]\n"
        "s_add_u32 s83, s83, 4096\n"
        "v_mfma_f32_16x16x32_bf16 v[56:59], v[144:147], v[12:15], v[56:59]\n"
        "v_mfma_f32_16x16x32_bf16 v[60:63], v[148:151], v[12:15], v[60:63]\n"
        "v_mfma_f32_16x16x32_bf16 v[64:67], v[152:155], v[12:15], v[64:67]\n"
        "v_mfma_f32_16x16x32_bf16 v[68:71], v[156:159], v[12:15], v[68:71]\n"
        "s_waitcnt lgkmcnt(1)\n"
        "ds_read_b128 v[12:15], v0 offset:8192\n"
        "v_mfma_f32_16x16x32_bf16 v[48:51], v[160:163], v[16:19], v[48:51]\n"
        "v_mfma_f32_16x16x32_bf16 v[52:55], v[164:167], v[16:19], v[52:55]\n"
        "v_mfma_f32_16x16x32_bf16 v[56:59], v[168:171], v[16:19], v[56:59]\n"
        "v_mfma_f32_16x16x32_bf16 v[60:63], v[172:175], v[16:19], v[60:63]\n"
        "v_mfma_f32_16x16x32_bf16 v[64:67], v[176:179], v[16:19], v[64:67]\n"
        "v_mfma_f32_16x16x32_bf16 v[68:71], v[180:183], v[16:19], v[68:71]\n"
        "s_waitcnt lgkmcnt(1)\n"
        "ds_read_b128 v[16:19], v0 offset:8704\n"
        "v_mfma_f32_16x16x32_bf16 v[48:51], v[184:187], v[8:11], v[48:51]\n"
        "s_waitcnt vmcnt(1)\n"
        "v_cmp_gt_i32 vcc, s70, v4\n"
        "v_mfma_f32_16x16x32_bf16 v[52:55], v[188:191], v[8:11], v[52:55]\n"
        "v_mfma_f32_16x16x32_bf16 v[56:59], v[192:195], v[8:11], v[56:59]\n"
        "s_and_b64 vcc, vcc, 1\n"
        "v_mfma_f32_16x16x32_bf16 v[60:63], v[196:199], v[8:11], v[60:63]\n"
        "s_cbranch_vccnz .Lf1_i12_%=\n"
        "v_mfma_f32_16x16x32_bf16 v[64:67], v[200:203], v[8:11], v[64:67]\n"
        "buffer_load_dwordx4 v1, s[40:43], s71 offen offset:0 sc1 lds\n"
        "v_mfma_f32_16x16x32_bf16 v[68:71], v[204:207], v[8:11], v[68:71]\n"
        "s_waitcnt lgkmcnt(1)\n"
        "v_mfma_f32_16x16x32_bf16 v[48:51], v[208:211], v[12:15], v[48:51]\n"
        "s_getreg_b32 s88, hwreg(HW_REG_IB_STS, 0, 4)\n"
        "buffer_load_dwordx4 v1, s[40:43], s71 offen offset:1024 sc1 lds\n"
        "v_mfma_f32_16x16x32_bf16 v[52:55], v[212:215], v[12:15], v[52:55]\n"
        "s_cmp_le_u32 s88, 1\n"
        "s_cselect_b32 s88, s73, s89\n"
        "s_cselect_b32 s73, s89, s73\n"
        "v_mfma_f32_16x16x32_bf16 v[56:59], v[216:219], v[12:15], v[56:59]\n"
        "buffer_load_dwordx4 v1, s[40:43], s71 offen offset:2048 sc1 lds\n"
        "v_mfma_f32_16x16x32_bf16 v[60:63], v[220:223], v[12:15], v[60:63]\n"
        "buffer_store_dword v5, v3, s[40:43], s88 offen sc1\n"
        "v_mfma_f32_16x16x32_bf16 v[64:67], v[224:227], v[12:15], v[64:67]\n"
        "buffer_load_dwordx4 v1, s[40:43], s71 offen offset:3072 sc1 lds\n"
        "v_mfma_f32_16x16x32_bf16 v[68:71], v[228:231], v[12:15], v[68:71]\n"
        "s_waitcnt lgkmcnt(0)\n"
        "s_add_u32 m0, s78, s85\n"
        "s_add_u32 s69, s69, 1\n"
        "v_mfma_f32_16x16x32_bf16 v[48:51], v[232:235], v[16:19], v[48:51]\n"
        "buffer_load_dwordx4 v1, s[40:43], s81 offen offset:0 sc1 lds\n"
        "v_mfma_f32_16x16x32_bf16 v[52:55], v[236:239], v[16:19], v[52:55]\n"
        "v_mfma_f32_16x16x32_bf16 v[56:59], v[240:243], v[16:19], v[56:59]\n"
        "s_getreg_b32 s88, hwreg(HW_REG_IB_STS, 0, 4)\n"
        "v_mfma_f32_16x16x32_bf16 v[60:63], v[244:247], v[16:19], v[60:63]\n"
        "s_cmp_le_u32 s88, 6\n"
        "s_cselect_b32 s88, s73, s89\n"
        "s_cselect_b32 s73, s89, s73\n"
        "v_mfma_f32_16x16x32_bf16 v[64:67], v[248:251], v[16:19], v[64:67]\n"
        "buffer_store_dword v5, v3, s[40:43], s88 offen sc1\n"
        "v_mfma_f32_16x16x32_bf16 v[68:71], v[252:255], v[16:19], v[68:71]\n"
        "s_mov_b32 s84, s73\n"
        "s_mov_b32 s73, s60\n"
        "v_mul_f32 v72, s80, v48\n"
        "v_mul_f32 v73, s80, v49\n"
        "v_mul_f32 v74, s80, v50\n"
        "v_mul_f32 v75, s80, v51\n"
        "v_exp_f32 v72, v72\n"
        "v_exp_f32 v73, v73\n"
        "v_exp_f32 v74, v74\n"
        "v_exp_f32 v75, v75\n"
        "v_mul_f32 v48, v48, v52\n"
        "v_mul_f32 v49, v49, v53\n"
        "v_mul_f32 v50, v50, v54\n"
        "v_mul_f32 v51, v51, v55\n"
        "v_add_f32 v72, 1.0, v72\n"
        "v_add_f32 v73, 1.0, v73\n"
        "v_add_f32 v74, 1.0, v74\n"
        "v_add_f32 v75, 1.0, v75\n"
        "v_rcp_f32 v72, v72\n"
        "v_rcp_f32 v73, v73\n"
        "v_rcp_f32 v74, v74\n"
        "v_rcp_f32 v75, v75\n"
        "v_mul_f32 v48, v48, v72\n"
        "v_mul_f32 v49, v49, v73\n"
        "v_mul_f32 v50, v50, v74\n"
        "v_mul_f32 v51, v51, v75\n"
        "v_cvt_pk_bf16_f32 v52, v48, v49\n"
        "v_cvt_pk_bf16_f32 v53, v50, v51\n"
        "s_waitcnt vmcnt(7)\n"
        "buffer_store_dword v5, v3, s[40:43], s84 offen sc1\n"
        "s_mov_b32 s84, s89\n"
        "v_mul_f32 v76, s80, v56\n"
        "v_mul_f32 v77, s80, v57\n"
        "v_mul_f32 v78, s80, v58\n"
        "v_mul_f32 v79, s80, v59\n"
        "v_exp_f32 v76, v76\n"
        "v_exp_f32 v77, v77\n"
        "v_exp_f32 v78, v78\n"
        "v_exp_f32 v79, v79\n"
        "v_mul_f32 v56, v56, v60\n"
        "v_mul_f32 v57, v57, v61\n"
        "v_mul_f32 v58, v58, v62\n"
        "v_mul_f32 v59, v59, v63\n"
        "v_add_f32 v76, 1.0, v76\n"
        "v_add_f32 v77, 1.0, v77\n"
        "v_add_f32 v78, 1.0, v78\n"
        "v_add_f32 v79, 1.0, v79\n"
        "v_rcp_f32 v76, v76\n"
        "v_rcp_f32 v77, v77\n"
        "v_rcp_f32 v78, v78\n"
        "v_rcp_f32 v79, v79\n"
        "v_mul_f32 v56, v56, v76\n"
        "v_mul_f32 v57, v57, v77\n"
        "v_mul_f32 v58, v58, v78\n"
        "v_mul_f32 v59, v59, v79\n"
        "v_cvt_pk_bf16_f32 v54, v56, v57\n"
        "v_cvt_pk_bf16_f32 v55, v58, v59\n"
        "buffer_store_dwordx4 v[52:55], v1, s[40:43], s72 offen offset:0 sc1\n"
        "v_mul_f32 v72, s80, v64\n"
        "v_mul_f32 v73, s80, v65\n"
        "v_mul_f32 v74, s80, v66\n"
        "v_mul_f32 v75, s80, v67\n"
        "v_exp_f32 v72, v72\n"
        "v_exp_f32 v73, v73\n"
        "v_exp_f32 v74, v74\n"
        "v_exp_f32 v75, v75\n"
        "v_mul_f32 v64, v64, v68\n"
        "v_mul_f32 v65, v65, v69\n"
        "v_mul_f32 v66, v66, v70\n"
        "v_mul_f32 v67, v67, v71\n"
        "v_add_f32 v72, 1.0, v72\n"
        "v_add_f32 v73, 1.0, v73\n"
        "v_add_f32 v74, 1.0, v74\n"
        "v_add_f32 v75, 1.0, v75\n"
        "v_rcp_f32 v72, v72\n"
        "v_rcp_f32 v73, v73\n"
        "v_rcp_f32 v74, v74\n"
        "v_rcp_f32 v75, v75\n"
        "v_mul_f32 v64, v64, v72\n"
        "v_mul_f32 v65, v65, v73\n"
        "v_mul_f32 v66, v66, v74\n"
        "v_mul_f32 v67, v67, v75\n"
        "v_cvt_pk_bf16_f32 v68, v64, v65\n"
        "v_cvt_pk_bf16_f32 v69, v66, v67\n"
        "s_nop 0\n"
        "v_permlane16_swap_b32 v68, v70\n"
        "v_permlane16_swap_b32 v69, v71\n"
        "buffer_store_dwordx4 v[68:71], v7, s[40:43], s83 offen offset:0 sc1\n"
        "v_mov_b32 v5, s74\n"
        "s_mov_b32 s84, s60\n"
        "buffer_store_dword v5, v3, s[40:43], s89 offen sc1\n"
        "buffer_store_dword v5, v3, s[40:43], s89 offen sc1\n"
        "s_waitcnt vmcnt(6)\n"
        "s_branch .Lf1_go1_%=\n"
        ".p2align 5\n"
        ".Lf1_i12_%=:\n"
        "v_mfma_f32_16x16x32_bf16 v[64:67], v[200:203], v[8:11], v[64:67]\n"
        "buffer_load_dword v4, v2, s[40:43], s86 offen sc1\n"
        "v_mfma_f32_16x16x32_bf16 v[68:71], v[204:207], v[8:11], v[68:71]\n"
        "s_waitcnt lgkmcnt(1)\n"
        "v_mfma_f32_16x16x32_bf16 v[48:51], v[208:211], v[12:15], v[48:51]\n"
        "s_getreg_b32 s88, hwreg(HW_REG_IB_STS, 0, 4)\n"
        "v_mfma_f32_16x16x32_bf16 v[52:55], v[212:215], v[12:15], v[52:55]\n"
        "s_cmp_le_u32 s88, 1\n"
        "s_cselect_b32 s88, s73, s89\n"
        "s_cselect_b32 s73, s89, s73\n"
        "v_mfma_f32_16x16x32_bf16 v[56:59], v[216:219], v[12:15], v[56:59]\n"
        "buffer_store_dword v5, v3, s[40:43], s88 offen sc1\n"
        "v_mfma_f32_16x16x32_bf16 v[60:63], v[220:223], v[12:15], v[60:63]\n"
        "v_mfma_f32_16x16x32_bf16 v[64:67], v[224:227], v[12:15], v[64:67]\n"
        "v_mfma_f32_16x16x32_bf16 v[68:71], v[228:231], v[12:15], v[68:71]\n"
        "s_waitcnt lgkmcnt(0)\n"
        "v_mfma_f32_16x16x32_bf16 v[48:51], v[232:235], v[16:19], v[48:51]\n"
        "v_mfma_f32_16x16x32_bf16 v[52:55], v[236:239], v[16:19], v[52:55]\n"
        "v_mfma_f32_16x16x32_bf16 v[56:59], v[240:243], v[16:19], v[56:59]\n"
        "s_getreg_b32 s88, hwreg(HW_REG_IB_STS, 0, 4)\n"
        "v_mfma_f32_16x16x32_bf16 v[60:63], v[244:247], v[16:19], v[60:63]\n"
        "s_cmp_le_u32 s88, 2\n"
        "s_cselect_b32 s88, s73, s89\n"
        "s_cselect_b32 s73, s89, s73\n"
        "v_mfma_f32_16x16x32_bf16 v[64:67], v[248:251], v[16:19], v[64:67]\n"
        "buffer_store_dword v5, v3, s[40:43], s88 offen sc1\n"
        "v_mfma_f32_16x16x32_bf16 v[68:71], v[252:255], v[16:19], v[68:71]\n"
        "s_mov_b32 s84, s73\n"
        "s_mov_b32 s73, s60\n"
        "v_mul_f32 v72, s80, v48\n"
        "v_mul_f32 v73, s80, v49\n"
        "v_mul_f32 v74, s80, v50\n"
        "v_mul_f32 v75, s80, v51\n"
        "v_exp_f32 v72, v72\n"
        "v_exp_f32 v73, v73\n"
        "v_exp_f32 v74, v74\n"
        "v_exp_f32 v75, v75\n"
        "v_mul_f32 v48, v48, v52\n"
        "v_mul_f32 v49, v49, v53\n"
        "v_mul_f32 v50, v50, v54\n"
        "v_mul_f32 v51, v51, v55\n"
        "v_add_f32 v72, 1.0, v72\n"
        "v_add_f32 v73, 1.0, v73\n"
        "v_add_f32 v74, 1.0, v74\n"
        "v_add_f32 v75, 1.0, v75\n"
        "v_rcp_f32 v72, v72\n"
        "v_rcp_f32 v73, v73\n"
        "v_rcp_f32 v74, v74\n"
        "v_rcp_f32 v75, v75\n"
        "v_mul_f32 v48, v48, v72\n"
        "v_mul_f32 v49, v49, v73\n"
        "v_mul_f32 v50, v50, v74\n"
        "v_mul_f32 v51, v51, v75\n"
        "v_cvt_pk_bf16_f32 v52, v48, v49\n"
        "v_cvt_pk_bf16_f32 v53, v50, v51\n"
        "s_waitcnt vmcnt(3)\n"
        "buffer_store_dword v5, v3, s[40:43], s84 offen sc1\n"
        "s_mov_b32 s84, s89\n"
        "v_mul_f32 v76, s80, v56\n"
        "v_mul_f32 v77, s80, v57\n"
        "v_mul_f32 v78, s80, v58\n"
        "v_mul_f32 v79, s80, v59\n"
        "v_exp_f32 v76, v76\n"
        "v_exp_f32 v77, v77\n"
        "v_exp_f32 v78, v78\n"
        "v_exp_f32 v79, v79\n"
        "v_mul_f32 v56, v56, v60\n"
        "v_mul_f32 v57, v57, v61\n"
        "v_mul_f32 v58, v58, v62\n"
        "v_mul_f32 v59, v59, v63\n"
        "v_add_f32 v76, 1.0, v76\n"
        "v_add_f32 v77, 1.0, v77\n"
        "v_add_f32 v78, 1.0, v78\n"
        "v_add_f32 v79, 1.0, v79\n"
        "v_rcp_f32 v76, v76\n"
        "v_rcp_f32 v77, v77\n"
        "v_rcp_f32 v78, v78\n"
        "v_rcp_f32 v79, v79\n"
        "v_mul_f32 v56, v56, v76\n"
        "v_mul_f32 v57, v57, v77\n"
        "v_mul_f32 v58, v58, v78\n"
        "v_mul_f32 v59, v59, v79\n"
        "v_cvt_pk_bf16_f32 v54, v56, v57\n"
        "v_cvt_pk_bf16_f32 v55, v58, v59\n"
        "buffer_store_dwordx4 v[52:55], v1, s[40:43], s72 offen offset:0 sc1\n"
        "v_mul_f32 v72, s80, v64\n"
        "v_mul_f32 v73, s80, v65\n"
        "v_mul_f32 v74, s80, v66\n"
        "v_mul_f32 v75, s80, v67\n"
        "v_exp_f32 v72, v72\n"
        "v_exp_f32 v73, v73\n"
        "v_exp_f32 v74, v74\n"
        "v_exp_f32 v75, v75\n"
        "v_mul_f32 v64, v64, v68\n"
        "v_mul_f32 v65, v65, v69\n"
        "v_mul_f32 v66, v66, v70\n"
        "v_mul_f32 v67, v67, v71\n"
        "v_add_f32 v72, 1.0, v72\n"
        "v_add_f32 v73, 1.0, v73\n"
        "v_add_f32 v74, 1.0, v74\n"
        "v_add_f32 v75, 1.0, v75\n"
        "v_rcp_f32 v72, v72\n"
        "v_rcp_f32 v73, v73\n"
        "v_rcp_f32 v74, v74\n"
        "v_rcp_f32 v75, v75\n"
        "v_mul_f32 v64, v64, v72\n"
        "v_mul_f32 v65, v65, v73\n"
        "v_mul_f32 v66, v66, v74\n"
        "v_mul_f32 v67, v67, v75\n"
        "v_cvt_pk_bf16_f32 v68, v64, v65\n"
        "v_cvt_pk_bf16_f32 v69, v66, v67\n"
        "s_nop 0\n"
        "v_permlane16_swap_b32 v68, v70\n"
        "v_permlane16_swap_b32 v69, v71\n"
        "buffer_store_dwordx4 v[68:71], v7, s[40:43], s83 offen offset:0 sc1\n"
        "s_waitcnt vmcnt(5)\n"
        "v_cmp_gt_i32 vcc, s70, v4\n"
        "s_nop 0\n"
        "s_and_b64 vcc, vcc, 1\n"
        "s_cbranch_vccnz .Lf1_late1_%=\n"
        "buffer_load_dwordx4 v1, s[40:43], s71 offen offset:0 sc1 lds\n"
        "buffer_load_dwordx4 v1, s[40:43], s71 offen offset:1024 sc1 lds\n"
        "buffer_load_dwordx4 v1, s[40:43], s71 offen offset:2048 sc1 lds\n"
        "buffer_load_dwordx4 v1, s[40:43], s71 offen offset:3072 sc1 lds\n"
        "s_add_u32 m0, s78, s85\n"
        "s_add_u32 s69, s69, 1\n"
        "buffer_load_dwordx4 v1, s[40:43], s81 offen offset:0 sc1 lds\n"
        "s_waitcnt vmcnt(5)\n"
        "v_mov_b32 v5, s74\n"
        "buffer_store_dword v5, v3, s[40:43], s73 offen sc1\n"
        "s_waitcnt vmcnt(1)\n"
        "s_branch .Lf1_go1_%=\n"
        ".p2align 5\n"
        ".Lf1_late1_%=:\n"
        "s_waitcnt vmcnt(0)\n"
        "v_mov_b32_e64 v5, s74\n"
        "buffer_store_dword v5, v3, s[40:43], s73 offen sc1\n"
        "s_cmp_eq_u32 s69, 128\n"
        "s_nop 0\n"
        "s_cbranch_scc0 .Lf1_wait1_%=\n"
        "s_endpgm\n"
        ".p2align 5\n"
        ".Lf1_wait1_%=:\n"
        "s_mov_b32 s64, 0\n"
        ".Lf1_in1_top_%=:\n"
        "buffer_load_dword v4, v2, s[40:43], s86 offen sc1\n"
        "s_waitcnt vmcnt(0)\n"
        "v_cmp_gt_i32_e64 vcc, s70, v4\n"
        "s_nop 0\n"
        "s_and_b64 vcc, vcc, 1\n"
        "s_cbranch_vccz .Lf1_in1_ok_%=\n"
        "s_add_u32 s64, s64, 1\n"
        "s_and_b32 s65, s64, 255\n"
        "s_cbranch_scc1 .Lf1_in1_go_%=\n"
        "s_mov_b32 s65, 49152\n"
        "s_cmp_lt_u32 s64, 1048576\n"
        "s_cbranch_scc1 .Lf1_in1_chk_%=\n"
        "v_mov_b32 v4, 1\n"
        "buffer_store_dword v4, off, s[40:43], s65 sc1\n"
        "s_waitcnt vmcnt(0)\n"
        ".Lf1_in1_chk_%=:\n"
        "buffer_load_dword v4, off, s[40:43], s65 sc1\n"
        "s_waitcnt vmcnt(0)\n"
        "v_readfirstlane_b32 s65, v4\n"
        "s_nop 0\n"
        "s_cmp_lg_u32 s65, 1\n"
        "s_cbranch_scc1 .Lf1_in1_go_%=\n"
        "s_endpgm\n"
        ".p2align 5\n"
        ".Lf1_in1_go_%=:\n"
        "s_branch .Lf1_in1_top_%=\n"
        ".p2align 5\n"
        ".Lf1_in1_ok_%=:\n"
        "buffer_load_dwordx4 v1, s[40:43], s71 offen offset:0 sc1 lds\n"
        "buffer_load_dwordx4 v1, s[40:43], s71 offen offset:1024 sc1 lds\n"
        "buffer_load_dwordx4 v1, s[40:43], s71 offen offset:2048 sc1 lds\n"
        "buffer_load_dwordx4 v1, s[40:43], s71 offen offset:3072 sc1 lds\n"
        "s_add_u32 m0, s78, s85\n"
        "s_add_u32 s69, s69, 1\n"
        "buffer_load_dwordx4 v1, s[40:43], s81 offen offset:0 sc1 lds\n"
        "s_waitcnt vmcnt(0)\n"
        ".Lf1_go1_%=:\n"
        "s_mov_b32 s72, 241172480\n"
        "s_mov_b32 s83, 241172480\n"
        "s_mov_b32 s73, 241172480\n"
        "v_add_u32 v0, s78, v6\n"
        "s_barrier\n"
        "ds_read_b128 v[8:11], v0 offset:0\n"
        "ds_read_b128 v[12:15], v0 offset:512\n"
        "ds_read_b128 v[16:19], v0 offset:1024\n"
        "s_waitcnt lgkmcnt(2)\n"
        "s_branch .Lf1_i0_%=\n"
        ".Lf1_end_%=:\n"
        :: "s"(p.ws), "s"(p.w1), "s"(p.w3), "s"(p.w3), "s"(pair(w, delay)), "s"(pair(x4, pr)), TILE_ARGS(65 * PAGE + spread(B_XN1)),
           "v"(pair(lane == 0 ? flag(F_R3 + 8 * (6 * kap + jj) + w) : OOB, lane < 3 ? lane * 32768 : OOB)), "v"(pair(0, 0)), "v"(pair(0, (g & 1) ? OOB : (g >> 1) * 256 + i * 16)),
           "v"(pair(vo[0], vo[1])), "v"(pair(vo[2], w * 40960 + (4 * g + q) * 96 + 8 * pp))
        : "v0", "v1", "v2", "v3", "v4", "v5", "v6", "v7", "v8", "v9", "v10", "v11", "v12", "v13", "v14", "v15", "v16", "v17", "v18", "v19", "v20", "v21", "v22", "v23", "v24", "v25", "v26", "v27", "v28", "v29", "v30", "v31", "v32", "v33", "v34", "v35", "v36", "v37", "v38", "v39", "v40", "v41", "v42", "v43", "v44", "v45", "v46", "v47", "v48", "v49", "v50", "v51", "v52", "v53", "v54", "v55", "v56", "v57", "v58", "v59", "v60", "v61", "v62", "v63", "v64", "v65", "v66", "v67", "v68", "v69", "v70", "v71", "v72", "v73", "v74", "v75", "v76", "v77", "v78", "v79", "v255", "a255", "s26", "s27", "s28", "s29", "s30", "s31", "s32", "s33", "s34", "s35", "s36", "s37", "s38", "s39", "s40", "s41", "s42", "s43", "s44", "s45", "s46", "s47", "s48", "s49", "s50", "s51", "s52", "s53", "s54", "s55", "s56", "s57", "s58", "s59", "s60", "s61", "s62", "s63", "s64", "s65", "s66", "s67", "s68", "s69", "s70", "s71", "s72", "s73", "s74", "s75", "s76", "s77", "s78", "s79", "s80", "s81", "s82", "s83", "s84", "s85", "s86", "s87", "s88", "s89", "s90", "s91", "s92", "s93", "s94", "s95", "s96", "s97", "s98", "s99", "vcc", "scc", "memory");
    __builtin_unreachable();
}
__device__ __forceinline__ unsigned part_off(unsigned a, unsigned kap, unsigned chunk) {
    const unsigned b = (a & 1) * 4 + kap;
    return (a >> 1) * (9 * 16384) + (chunk < 64 ? b * 16384 + chunk * 128 : 8 * 16384 + b * 1024 + (chunk - 64) * 128);
}
template <bool OP>
__device__ __forceinline__ void role_k(const Params& p, int kap, int nu, int x4, int pr, int delay) {
    GEMM_HEAD;
    unsigned vo[3], vt[3];
#pragma unroll
    for (int n = 0; n < 3; ++n) {
        const unsigned ps = (64 * n + lane) / 6, gg = (ps >> 2) & 3;
        const unsigned col = (192 * nu + 48 * w) * 2 + ((64 * n + lane) % 6) * 16 - 1024 * n;
        vo[n] = ((OP ? 1024 : 1152) * kap + ps) * 1152 + col;
        vt[n] = (1152 * kap + 48 * (gg >> 1) + 32 + 8 * (gg & 1) + 4 * (ps >> 4) + (ps & 3)) * 1152 + col;
    }
    const unsigned tr = w * 40960 + (4 * g + q) * 96;
    const unsigned c0 = 24 * nu + 6 * w;
    const unsigned s01 = part_off(i >> 3, kap, c0 + g) + (i & 7) * 16, s2 = (g & 1) ? OOB : part_off(i >> 3, kap, c0 + 4 + (g >> 1)) + (i & 7) * 16;
    const unsigned fl = nu * 16 + kap * 4 + w, fo4 = lane < 4 ? lane * 16384 : OOB;
#define K_ARGS "v"(pair(0, 0)), "v"(pair(s01, s2)), "v"(pair(vo[0], vo[1])), "v"(pair(vo[2], tr + 16 * pp)), "v"(pair(vt[0], vt[1])), "v"(pair(vt[2], tr + 64 + 8 * pp))
    if constexpr (OP) {
        asm volatile(
        "s_nop 4\n"
        "s_mov_b64 s[40:41], %0\n"
        "s_and_b32 s41, s41, 0xffff\n"
        "s_mov_b32 s42, 236978176\n"
        "s_mov_b32 s43, 0x20000\n"
        "s_mov_b64 s[48:49], %4\n"
        "s_mov_b64 s[56:57], %5\n"
        "s_mov_b64 s[58:59], %6\n"
        "s_mov_b64 s[60:61], %7\n"
        "s_mov_b64 s[62:63], %8\n"
        "v_mov_b64 v[0:1], %9\n"
        "v_mov_b64 v[2:3], %10\n"
        "v_mov_b64 v[4:5], %11\n"
        "v_mov_b64 v[6:7], %12\n"
        "v_mov_b64 v[8:9], %13\n"
        "v_mov_b64 v[10:11], %14\n"
        "v_mov_b64 v[12:13], %15\n"
        "v_mov_b64 v[14:15], %16\n"
        "s_mov_b32 s47, 0x20000\n"
        "s_mul_i32 s79, s48, 40960\n"
        ".Lop_start_%=:\n"
        "s_sub_u32 s49, s49, 1\n"
        "s_cbranch_scc1 .Lop_start2_%=\n"
        "s_sleep 16\n"
        "s_branch .Lop_start_%=\n"
        ".Lop_start2_%=:\n"
        "s_mov_b64 s[44:45], %1\n"
        "s_and_b32 s45, s45, 0xffff\n"
        "s_mov_b32 s46, 4718592\n"
        "s_add_u32 m0, s79, 0\n"
        "s_mov_b32 s65, 0\n"
        "buffer_load_dwordx4 v8, s[44:47], s65 offen lds\n"
        "buffer_load_dwordx4 v9, s[44:47], s65 offen offset:1024 lds\n"
        "buffer_load_dwordx4 v10, s[44:47], s65 offen offset:2048 lds\n"
        "s_add_u32 m0, s79, 3072\n"
        "s_mov_b32 s65, 36864\n"
        "buffer_load_dwordx4 v8, s[44:47], s65 offen lds\n"
        "buffer_load_dwordx4 v9, s[44:47], s65 offen offset:1024 lds\n"
        "buffer_load_dwordx4 v10, s[44:47], s65 offen offset:2048 lds\n"
        "s_waitcnt vmcnt(3)\n"
        "ds_read_b64_tr_b16 a[0:1], v11 offset:0\n"
        "ds_read_b64_tr_b16 a[2:3], v11 offset:1536\n"
        "ds_read_b64_tr_b16 a[4:5], v11 offset:8\n"
        "ds_read_b64_tr_b16 a[6:7], v11 offset:1544\n"
        "ds_read_b64_tr_b16 a[8:9], v15 offset:0\n"
        "ds_read_b64_tr_b16 a[10:11], v15 offset:1536\n"
        "s_waitcnt vmcnt(0)\n"
        "ds_read_b64_tr_b16 a[12:13], v11 offset:3072\n"
        "ds_read_b64_tr_b16 a[14:15], v11 offset:4608\n"
        "ds_read_b64_tr_b16 a[16:17], v11 offset:3080\n"
        "ds_read_b64_tr_b16 a[18:19], v11 offset:4616\n"
        "ds_read_b64_tr_b16 a[20:21], v15 offset:3072\n"
        "ds_read_b64_tr_b16 a[22:23], v15 offset:4608\n"
        "s_waitcnt lgkmcnt(6)\n"
        "s_add_u32 m0, s79, 0\n"
        "s_mov_b32 s65, 73728\n"
        "buffer_load_dwordx4 v8, s[44:47], s65 offen lds\n"
        "buffer_load_dwordx4 v9, s[44:47], s65 offen offset:1024 lds\n"
        "buffer_load_dwordx4 v10, s[44:47], s65 offen offset:2048 lds\n"
        "s_waitcnt vmcnt(0)\n"
        "ds_read_b64_tr_b16 a[24:25], v11 offset:0\n"
        "ds_read_b64_tr_b16 a[26:27], v11 offset:1536\n"
        "ds_read_b64_tr_b16 a[28:29], v11 offset:8\n"
        "ds_read_b64_tr_b16 a[30:31], v11 offset:1544\n"
        "ds_read_b64_tr_b16 a[32:33], v15 offset:0\n"
        "ds_read_b64_tr_b16 a[34:35], v15 offset:1536\n"
        "s_waitcnt lgkmcnt(6)\n"
        "s_add_u32 m0, s79, 3072\n"
        "s_mov_b32 s65, 110592\n"
        "buffer_load_dwordx4 v8, s[44:47], s65 offen lds\n"
        "buffer_load_dwordx4 v9, s[44:47], s65 offen offset:1024 lds\n"
        "buffer_load_dwordx4 v10, s[44:47], s65 offen offset:2048 lds\n"
        "s_waitcnt vmcnt(0)\n"
        "ds_read_b64_tr_b16 a[36:37], v11 offset:3072\n"
        "ds_read_b64_tr_b16 a[38:39], v11 offset:4608\n"
        "ds_read_b64_tr_b16 a[40:41], v11 offset:3080\n"
        "ds_read_b64_tr_b16 a[42:43], v11 offset:4616\n"
        "ds_read_b64_tr_b16 a[44:45], v15 offset:3072\n"
        "ds_read_b64_tr_b16 a[46:47], v15 offset:4608\n"
        "s_waitcnt lgkmcnt(6)\n"
        "s_add_u32 m0, s79, 0\n"
        "s_mov_b32 s65, 147456\n"
        "buffer_load_dwordx4 v8, s[44:47], s65 offen lds\n"
        "buffer_load_dwordx4 v9, s[44:47], s65 offen offset:1024 lds\n"
        "buffer_load_dwordx4 v10, s[44:47], s65 offen offset:2048 lds\n"
        "s_waitcnt vmcnt(0)\n"
        "ds_read_b64_tr_b16 a[48:49], v11 offset:0\n"
        "ds_read_b64_tr_b16 a[50:51], v11 offset:1536\n"
        "ds_read_b64_tr_b16 a[52:53], v11 offset:8\n"
        "ds_read_b64_tr_b16 a[54:55], v11 offset:1544\n"
        "ds_read_b64_tr_b16 a[56:57], v15 offset:0\n"
        "ds_read_b64_tr_b16 a[58:59], v15 offset:1536\n"
        "s_waitcnt lgkmcnt(6)\n"
        "s_add_u32 m0, s79, 3072\n"
        "s_mov_b32 s65, 184320\n"
        "buffer_load_dwordx4 v8, s[44:47], s65 offen lds\n"
        "buffer_load_dwordx4 v9, s[44:47], s65 offen offset:1024 lds\n"
        "buffer_load_dwordx4 v10, s[44:47], s65 offen offset:2048 lds\n"
        "s_waitcnt vmcnt(0)\n"
        "ds_read_b64_tr_b16 a[60:61], v11 offset:3072\n"
        "ds_read_b64_tr_b16 a[62:63], v11 offset:4608\n"
        "ds_read_b64_tr_b16 a[64:65], v11 offset:3080\n"
        "ds_read_b64_tr_b16 a[66:67], v11 offset:4616\n"
        "ds_read_b64_tr_b16 a[68:69], v15 offset:3072\n"
        "ds_read_b64_tr_b16 a[70:71], v15 offset:4608\n"
        "s_waitcnt lgkmcnt(6)\n"
        "s_add_u32 m0, s79, 0\n"
        "s_mov_b32 s65, 221184\n"
        "buffer_load_dwordx4 v8, s[44:47], s65 offen lds\n"
        "buffer_load_dwordx4 v9, s[44:47], s65 offen offset:1024 lds\n"
        "buffer_load_dwordx4 v10, s[44:47], s65 offen offset:2048 lds\n"
        "s_waitcnt vmcnt(0)\n"
        "ds_read_b64_tr_b16 a[72:73], v11 offset:0\n"
        "ds_read_b64_tr_b16 a[74:75], v11 offset:1536\n"
        "ds_read_b64_tr_b16 a[76:77], v11 offset:8\n"
        "ds_read_b64_tr_b16 a[78:79], v11 offset:1544\n"
        "ds_read_b64_tr_b16 a[80:81], v15 offset:0\n"
        "ds_read_b64_tr_b16 a[82:83], v15 offset:1536\n"
        "s_waitcnt lgkmcnt(6)\n"
        "s_add_u32 m0, s79, 3072\n"
        "s_mov_b32 s65, 258048\n"
        "buffer_load_dwordx4 v8, s[44:47], s65 offen lds\n"
        "buffer_load_dwordx4 v9, s[44:47], s65 offen offset:1024 lds\n"
        "buffer_load_dwordx4 v10, s[44:47], s65 offen offset:2048 lds\n"
        "s_waitcnt vmcnt(0)\n"
        "ds_read_b64_tr_b16 a[84:85], v11 offset:3072\n"
        "ds_read_b64_tr_b16 a[86:87], v11 offset:4608\n"
        "ds_read_b64_tr_b16 a[88:89], v11 offset:3080\n"
        "ds_read_b64_tr_b16 a[90:91], v11 offset:4616\n"
        "ds_read_b64_tr_b16 a[92:93], v15 offset:3072\n"
        "ds_read_b64_tr_b16 a[94:95], v15 offset:4608\n"
        "s_waitcnt lgkmcnt(6)\n"
        "s_add_u32 m0, s79, 0\n"
        "s_mov_b32 s65, 294912\n"
        "buffer_load_dwordx4 v8, s[44:47], s65 offen lds\n"
        "buffer_load_dwordx4 v9, s[44:47], s65 offen offset:1024 lds\n"
        "buffer_load_dwordx4 v10, s[44:47], s65 offen offset:2048 lds\n"
        "s_waitcnt vmcnt(0)\n"
        "ds_read_b64_tr_b16 a[96:97], v11 offset:0\n"
        "ds_read_b64_tr_b16 a[98:99], v11 offset:1536\n"
        "ds_read_b64_tr_b16 a[100:101], v11 offset:8\n"
        "ds_read_b64_tr_b16 a[102:103], v11 offset:1544\n"
        "ds_read_b64_tr_b16 a[104:105], v15 offset:0\n"
        "ds_read_b64_tr_b16 a[106:107], v15 offset:1536\n"
        "s_waitcnt lgkmcnt(6)\n"
        "s_add_u32 m0, s79, 3072\n"
        "s_mov_b32 s65, 331776\n"
        "buffer_load_dwordx4 v8, s[44:47], s65 offen lds\n"
        "buffer_load_dwordx4 v9, s[44:47], s65 offen offset:1024 lds\n"
        "buffer_load_dwordx4 v10, s[44:47], s65 offen offset:2048 lds\n"
        "s_waitcnt vmcnt(0)\n"
        "ds_read_b64_tr_b16 a[108:109], v11 offset:3072\n"
        "ds_read_b64_tr_b16 a[110:111], v11 offset:4608\n"
        "ds_read_b64_tr_b16 a[112:113], v11 offset:3080\n"
        "ds_read_b64_tr_b16 a[114:115], v11 offset:4616\n"
        "ds_read_b64_tr_b16 a[116:117], v15 offset:3072\n"
        "ds_read_b64_tr_b16 a[118:119], v15 offset:4608\n"
        "s_waitcnt lgkmcnt(6)\n"
        "s_add_u32 m0, s79, 0\n"
        "s_mov_b32 s65, 368640\n"
        "buffer_load_dwordx4 v8, s[44:47], s65 offen lds\n"
        "buffer_load_dwordx4 v9, s[44:47], s65 offen offset:1024 lds\n"
        "buffer_load_dwordx4 v10, s[44:47], s65 offen offset:2048 lds\n"
        "s_waitcnt vmcnt(0)\n"
        "ds_read_b64_tr_b16 a[120:121], v11 offset:0\n"
        "ds_read_b64_tr_b16 a[122:123], v11 offset:1536\n"
        "ds_read_b64_tr_b16 a[124:125], v11 offset:8\n"
        "ds_read_b64_tr_b16 a[126:127], v11 offset:1544\n"
        "ds_read_b64_tr_b16 a[128:129], v15 offset:0\n"
        "ds_read_b64_tr_b16 a[130:131], v15 offset:1536\n"
        "s_waitcnt lgkmcnt(6)\n"
        "s_add_u32 m0, s79, 3072\n"
        "s_mov_b32 s65, 405504\n"
        "buffer_load_dwordx4 v8, s[44:47], s65 offen lds\n"
        "buffer_load_dwordx4 v9, s[44:47], s65 offen offset:1024 lds\n"
        "buffer_load_dwordx4 v10, s[44:47], s65 offen offset:2048 lds\n"
        "s_waitcnt vmcnt(0)\n"
        "ds_read_b64_tr_b16 a[132:133], v11 offset:3072\n"
        "ds_read_b64_tr_b16 a[134:135], v11 offset:4608\n"
        "ds_read_b64_tr_b16 a[136:137], v11 offset:3080\n"
        "ds_read_b64_tr_b16 a[138:139], v11 offset:4616\n"
        "ds_read_b64_tr_b16 a[140:141], v15 offset:3072\n"
        "ds_read_b64_tr_b16 a[142:143], v15 offset:4608\n"
        "s_waitcnt lgkmcnt(6)\n"
        "s_add_u32 m0, s79, 0\n"
        "s_mov_b32 s65, 442368\n"
        "buffer_load_dwordx4 v8, s[44:47], s65 offen lds\n"
        "buffer_load_dwordx4 v9, s[44:47], s65 offen offset:1024 lds\n"
        "buffer_load_dwordx4 v10, s[44:47], s65 offen offset:2048 lds\n"
        "s_waitcnt vmcnt(0)\n"
        "ds_read_b64_tr_b16 a[144:145], v11 offset:0\n"
        "ds_read_b64_tr_b16 a[146:147], v11 offset:1536\n"
        "ds_read_b64_tr_b16 a[148:149], v11 offset:8\n"
        "ds_read_b64_tr_b16 a[150:151], v11 offset:1544\n"
        "ds_read_b64_tr_b16 a[152:153], v15 offset:0\n"
        "ds_read_b64_tr_b16 a[154:155], v15 offset:1536\n"
        "s_waitcnt lgkmcnt(6)\n"
        "s_add_u32 m0, s79, 3072\n"
        "s_mov_b32 s65, 479232\n"
        "buffer_load_dwordx4 v8, s[44:47], s65 offen lds\n"
        "buffer_load_dwordx4 v9, s[44:47], s65 offen offset:1024 lds\n"
        "buffer_load_dwordx4 v10, s[44:47], s65 offen offset:2048 lds\n"
        "s_waitcnt vmcnt(0)\n"
        "ds_read_b64_tr_b16 a[156:157], v11 offset:3072\n"
        "ds_read_b64_tr_b16 a[158:159], v11 offset:4608\n"
        "ds_read_b64_tr_b16 a[160:161], v11 offset:3080\n"
        "ds_read_b64_tr_b16 a[162:163], v11 offset:4616\n"
        "ds_read_b64_tr_b16 a[164:165], v15 offset:3072\n"
        "ds_read_b64_tr_b16 a[166:167], v15 offset:4608\n"
        "s_waitcnt lgkmcnt(6)\n"
        "s_add_u32 m0, s79, 0\n"
        "s_mov_b32 s65, 516096\n"
        "buffer_load_dwordx4 v8, s[44:47], s65 offen lds\n"
        "buffer_load_dwordx4 v9, s[44:47], s65 offen offset:1024 lds\n"
        "buffer_load_dwordx4 v10, s[44:47], s65 offen offset:2048 lds\n"
        "s_waitcnt vmcnt(0)\n"
        "ds_read_b64_tr_b16 a[168:169], v11 offset:0\n"
        "ds_read_b64_tr_b16 a[170:171], v11 offset:1536\n"
        "ds_read_b64_tr_b16 a[172:173], v11 offset:8\n"
        "ds_read_b64_tr_b16 a[174:175], v11 offset:1544\n"
        "ds_read_b64_tr_b16 a[176:177], v15 offset:0\n"
        "ds_read_b64_tr_b16 a[178:179], v15 offset:1536\n"
        "s_waitcnt lgkmcnt(6)\n"
        "s_add_u32 m0, s79, 3072\n"
        "s_mov_b32 s65, 552960\n"
        "buffer_load_dwordx4 v8, s[44:47], s65 offen lds\n"
        "buffer_load_dwordx4 v9, s[44:47], s65 offen offset:1024 lds\n"
        "buffer_load_dwordx4 v10, s[44:47], s65 offen offset:2048 lds\n"
        "s_waitcnt vmcnt(0)\n"
        "ds_read_b64_tr_b16 a[180:181], v11 offset:3072\n"
        "ds_read_b64_tr_b16 a[182:183], v11 offset:4608\n"
        "ds_read_b64_tr_b16 a[184:185], v11 offset:3080\n"
        "ds_read_b64_tr_b16 a[186:187], v11 offset:4616\n"
        "ds_read_b64_tr_b16 a[188:189], v15 offset:3072\n"
        "ds_read_b64_tr_b16 a[190:191], v15 offset:4608\n"
        "s_waitcnt lgkmcnt(6)\n"
        "s_add_u32 m0, s79, 0\n"
        "s_mov_b32 s65, 589824\n"
        "buffer_load_dwordx4 v8, s[44:47], s65 offen lds\n"
        "buffer_load_dwordx4 v9, s[44:47], s65 offen offset:1024 lds\n"
        "buffer_load_dwordx4 v10, s[44:47], s65 offen offset:2048 lds\n"
        "s_waitcnt vmcnt(0)\n"
        "ds_read_b64_tr_b16 a[192:193], v11 offset:0\n"
        "ds_read_b64_tr_b16 a[194:195], v11 offset:1536\n"
        "ds_read_b64_tr_b16 a[196:197], v11 offset:8\n"
        "ds_read_b64_tr_b16 a[198:199], v11 offset:1544\n"
        "ds_read_b64_tr_b16 a[200:201], v15 offset:0\n"
        "ds_read_b64_tr_b16 a[202:203], v15 offset:1536\n"
        "s_waitcnt lgkmcnt(6)\n"
        "s_add_u32 m0, s79, 3072\n"
        "s_mov_b32 s65, 626688\n"
        "buffer_load_dwordx4 v8, s[44:47], s65 offen lds\n"
        "buffer_load_dwordx4 v9, s[44:47], s65 offen offset:1024 lds\n"
        "buffer_load_dwordx4 v10, s[44:47], s65 offen offset:2048 lds\n"
        "s_waitcnt vmcnt(0)\n"
        "ds_read_b64_tr_b16 a[204:205], v11 offset:3072\n"
        "ds_read_b64_tr_b16 a[206:207], v11 offset:4608\n"
        "ds_read_b64_tr_b16 a[208:209], v11 offset:3080\n"
        "ds_read_b64_tr_b16 a[210:211], v11 offset:4616\n"
        "ds_read_b64_tr_b16 a[212:213], v15 offset:3072\n"
        "ds_read_b64_tr_b16 a[214:215], v15 offset:4608\n"
        "s_waitcnt lgkmcnt(6)\n"
        "s_add_u32 m0, s79, 0\n"
        "s_mov_b32 s65, 663552\n"
        "buffer_load_dwordx4 v8, s[44:47], s65 offen lds\n"
        "buffer_load_dwordx4 v9, s[44:47], s65 offen offset:1024 lds\n"
        "buffer_load_dwordx4 v10, s[44:47], s65 offen offset:2048 lds\n"
        "s_waitcnt vmcnt(0)\n"
        "ds_read_b64_tr_b16 a[216:217], v11 offset:0\n"
        "ds_read_b64_tr_b16 a[218:219], v11 offset:1536\n"
        "ds_read_b64_tr_b16 a[220:221], v11 offset:8\n"
        "ds_read_b64_tr_b16 a[222:223], v11 offset:1544\n"
        "ds_read_b64_tr_b16 a[224:225], v15 offset:0\n"
        "ds_read_b64_tr_b16 a[226:227], v15 offset:1536\n"
        "s_waitcnt lgkmcnt(6)\n"
        "s_add_u32 m0, s79, 3072\n"
        "s_mov_b32 s65, 700416\n"
        "buffer_load_dwordx4 v8, s[44:47], s65 offen lds\n"
        "buffer_load_dwordx4 v9, s[44:47], s65 offen offset:1024 lds\n"
        "buffer_load_dwordx4 v10, s[44:47], s65 offen offset:2048 lds\n"
        "s_waitcnt vmcnt(0)\n"
        "ds_read_b64_tr_b16 a[228:229], v11 offset:3072\n"
        "ds_read_b64_tr_b16 a[230:231], v11 offset:4608\n"
        "ds_read_b64_tr_b16 a[232:233], v11 offset:3080\n"
        "ds_read_b64_tr_b16 a[234:235], v11 offset:4616\n"
        "ds_read_b64_tr_b16 a[236:237], v15 offset:3072\n"
        "ds_read_b64_tr_b16 a[238:239], v15 offset:4608\n"
        "s_waitcnt lgkmcnt(6)\n"
        "s_add_u32 m0, s79, 0\n"
        "s_mov_b32 s65, 737280\n"
        "buffer_load_dwordx4 v8, s[44:47], s65 offen lds\n"
        "buffer_load_dwordx4 v9, s[44:47], s65 offen offset:1024 lds\n"
        "buffer_load_dwordx4 v10, s[44:47], s65 offen offset:2048 lds\n"
        "s_waitcnt vmcnt(0)\n"
        "ds_read_b64_tr_b16 a[240:241], v11 offset:0\n"
        "ds_read_b64_tr_b16 a[242:243], v11 offset:1536\n"
        "ds_read_b64_tr_b16 a[244:245], v11 offset:8\n"
        "ds_read_b64_tr_b16 a[246:247], v11 offset:1544\n"
        "ds_read_b64_tr_b16 a[248:249], v15 offset:0\n"
        "ds_read_b64_tr_b16 a[250:251], v15 offset:1536\n"
        "s_waitcnt lgkmcnt(6)\n"
        "s_add_u32 m0, s79, 3072\n"
        "s_mov_b32 s65, 774144\n"
        "buffer_load_dwordx4 v8, s[44:47], s65 offen lds\n"
        "buffer_load_dwordx4 v9, s[44:47], s65 offen offset:1024 lds\n"
        "buffer_load_dwordx4 v10, s[44:47], s65 offen offset:2048 lds\n"
        "s_waitcnt vmcnt(0)\n"
        "ds_read_b64_tr_b16 a[252:253], v11 offset:3072\n"
        "ds_read_b64_tr_b16 a[254:255], v11 offset:4608\n"
        "ds_read_b64_tr_b16 v[80:81], v11 offset:3080\n"
        "ds_read_b64_tr_b16 v[82:83], v11 offset:4616\n"
        "ds_read_b64_tr_b16 v[84:85], v15 offset:3072\n"
        "ds_read_b64_tr_b16 v[86:87], v15 offset:4608\n"
        "s_waitcnt lgkmcnt(6)\n"
        "s_add_u32 m0, s79, 0\n"
        "s_mov_b32 s65, 811008\n"
        "buffer_load_dwordx4 v8, s[44:47], s65 offen lds\n"
        "buffer_load_dwordx4 v9, s[44:47], s65 offen offset:1024 lds\n"
        "buffer_load_dwordx4 v10, s[44:47], s65 offen offset:2048 lds\n"
        "s_waitcnt vmcnt(0)\n"
        "ds_read_b64_tr_b16 v[88:89], v11 offset:0\n"
        "ds_read_b64_tr_b16 v[90:91], v11 offset:1536\n"
        "ds_read_b64_tr_b16 v[92:93], v11 offset:8\n"
        "ds_read_b64_tr_b16 v[94:95], v11 offset:1544\n"
        "ds_read_b64_tr_b16 v[96:97], v15 offset:0\n"
        "ds_read_b64_tr_b16 v[98:99], v15 offset:1536\n"
        "s_waitcnt lgkmcnt(6)\n"
        "s_add_u32 m0, s79, 3072\n"
        "s_mov_b32 s65, 847872\n"
        "buffer_load_dwordx4 v8, s[44:47], s65 offen lds\n"
        "buffer_load_dwordx4 v9, s[44:47], s65 offen offset:1024 lds\n"
        "buffer_load_dwordx4 v10, s[44:47], s65 offen offset:2048 lds\n"
        "s_waitcnt vmcnt(0)\n"
        "ds_read_b64_tr_b16 v[100:101], v11 offset:3072\n"
        "ds_read_b64_tr_b16 v[102:103], v11 offset:4608\n"
        "ds_read_b64_tr_b16 v[104:105], v11 offset:3080\n"
        "ds_read_b64_tr_b16 v[106:107], v11 offset:4616\n"
        "ds_read_b64_tr_b16 v[108:109], v15 offset:3072\n"
        "ds_read_b64_tr_b16 v[110:111], v15 offset:4608\n"
        "s_waitcnt lgkmcnt(6)\n"
        "s_add_u32 m0, s79, 0\n"
        "s_mov_b32 s65, 884736\n"
        "buffer_load_dwordx4 v8, s[44:47], s65 offen lds\n"
        "buffer_load_dwordx4 v9, s[44:47], s65 offen offset:1024 lds\n"
        "buffer_load_dwordx4 v10, s[44:47], s65 offen offset:2048 lds\n"
        "s_waitcnt vmcnt(0)\n"
        "ds_read_b64_tr_b16 v[112:113], v11 offset:0\n"
        "ds_read_b64_tr_b16 v[114:115], v11 offset:1536\n"
        "ds_read_b64_tr_b16 v[116:117], v11 offset:8\n"
        "ds_read_b64_tr_b16 v[118:119], v11 offset:1544\n"
        "ds_read_b64_tr_b16 v[120:121], v15 offset:0\n"
        "ds_read_b64_tr_b16 v[122:123], v15 offset:1536\n"
        "s_waitcnt lgkmcnt(6)\n"
        "s_add_u32 m0, s79, 3072\n"
        "s_mov_b32 s65, 921600\n"
        "buffer_load_dwordx4 v8, s[44:47], s65 offen lds\n"
        "buffer_load_dwordx4 v9, s[44:47], s65 offen offset:1024 lds\n"
        "buffer_load_dwordx4 v10, s[44:47], s65 offen offset:2048 lds\n"
        "s_waitcnt vmcnt(0)\n"
        "ds_read_b64_tr_b16 v[124:125], v11 offset:3072\n"
        "ds_read_b64_tr_b16 v[126:127], v11 offset:4608\n"
        "ds_read_b64_tr_b16 v[128:129], v11 offset:3080\n"
        "ds_read_b64_tr_b16 v[130:131], v11 offset:4616\n"
        "ds_read_b64_tr_b16 v[132:133], v15 offset:3072\n"
        "ds_read_b64_tr_b16 v[134:135], v15 offset:4608\n"
        "s_waitcnt lgkmcnt(6)\n"
        "s_add_u32 m0, s79, 0\n"
        "s_mov_b32 s65, 958464\n"
        "buffer_load_dwordx4 v8, s[44:47], s65 offen lds\n"
        "buffer_load_dwordx4 v9, s[44:47], s65 offen offset:1024 lds\n"
        "buffer_load_dwordx4 v10, s[44:47], s65 offen offset:2048 lds\n"
        "s_waitcnt vmcnt(0)\n"
        "ds_read_b64_tr_b16 v[136:137], v11 offset:0\n"
        "ds_read_b64_tr_b16 v[138:139], v11 offset:1536\n"
        "ds_read_b64_tr_b16 v[140:141], v11 offset:8\n"
        "ds_read_b64_tr_b16 v[142:143], v11 offset:1544\n"
        "ds_read_b64_tr_b16 v[144:145], v15 offset:0\n"
        "ds_read_b64_tr_b16 v[146:147], v15 offset:1536\n"
        "s_waitcnt lgkmcnt(6)\n"
        "s_add_u32 m0, s79, 3072\n"
        "s_mov_b32 s65, 995328\n"
        "buffer_load_dwordx4 v8, s[44:47], s65 offen lds\n"
        "buffer_load_dwordx4 v9, s[44:47], s65 offen offset:1024 lds\n"
        "buffer_load_dwordx4 v10, s[44:47], s65 offen offset:2048 lds\n"
        "s_waitcnt vmcnt(0)\n"
        "ds_read_b64_tr_b16 v[148:149], v11 offset:3072\n"
        "ds_read_b64_tr_b16 v[150:151], v11 offset:4608\n"
        "ds_read_b64_tr_b16 v[152:153], v11 offset:3080\n"
        "ds_read_b64_tr_b16 v[154:155], v11 offset:4616\n"
        "ds_read_b64_tr_b16 v[156:157], v15 offset:3072\n"
        "ds_read_b64_tr_b16 v[158:159], v15 offset:4608\n"
        "s_waitcnt lgkmcnt(6)\n"
        "s_add_u32 m0, s79, 0\n"
        "s_mov_b32 s65, 1032192\n"
        "buffer_load_dwordx4 v8, s[44:47], s65 offen lds\n"
        "buffer_load_dwordx4 v9, s[44:47], s65 offen offset:1024 lds\n"
        "buffer_load_dwordx4 v10, s[44:47], s65 offen offset:2048 lds\n"
        "s_waitcnt vmcnt(0)\n"
        "ds_read_b64_tr_b16 v[160:161], v11 offset:0\n"
        "ds_read_b64_tr_b16 v[162:163], v11 offset:1536\n"
        "ds_read_b64_tr_b16 v[164:165], v11 offset:8\n"
        "ds_read_b64_tr_b16 v[166:167], v11 offset:1544\n"
        "ds_read_b64_tr_b16 v[168:169], v15 offset:0\n"
        "ds_read_b64_tr_b16 v[170:171], v15 offset:1536\n"
        "s_waitcnt lgkmcnt(6)\n"
        "s_add_u32 m0, s79, 3072\n"
        "s_mov_b32 s65, 1069056\n"
        "buffer_load_dwordx4 v8, s[44:47], s65 offen lds\n"
        "buffer_load_dwordx4 v9, s[44:47], s65 offen offset:1024 lds\n"
        "buffer_load_dwordx4 v10, s[44:47], s65 offen offset:2048 lds\n"
        "s_waitcnt vmcnt(0)\n"
        "ds_read_b64_tr_b16 v[172:173], v11 offset:3072\n"
        "ds_read_b64_tr_b16 v[174:175], v11 offset:4608\n"
        "ds_read_b64_tr_b16 v[176:177], v11 offset:3080\n"
        "ds_read_b64_tr_b16 v[178:179], v11 offset:4616\n"
        "ds_read_b64_tr_b16 v[180:181], v15 offset:3072\n"
        "ds_read_b64_tr_b16 v[182:183], v15 offset:4608\n"
        "s_waitcnt lgkmcnt(6)\n"
        "s_add_u32 m0, s79, 0\n"
        "s_mov_b32 s65, 1105920\n"
        "buffer_load_dwordx4 v8, s[44:47], s65 offen lds\n"
        "buffer_load_dwordx4 v9, s[44:47], s65 offen offset:1024 lds\n"
        "buffer_load_dwordx4 v10, s[44:47], s65 offen offset:2048 lds\n"
        "s_waitcnt vmcnt(0)\n"
        "ds_read_b64_tr_b16 v[184:185], v11 offset:0\n"
        "ds_read_b64_tr_b16 v[186:187], v11 offset:1536\n"
        "ds_read_b64_tr_b16 v[188:189], v11 offset:8\n"
        "ds_read_b64_tr_b16 v[190:191], v11 offset:1544\n"
        "ds_read_b64_tr_b16 v[192:193], v15 offset:0\n"
        "ds_read_b64_tr_b16 v[194:195], v15 offset:1536\n"
        "s_waitcnt lgkmcnt(6)\n"
        "s_add_u32 m0, s79, 3072\n"
        "s_mov_b32 s65, 1142784\n"
        "buffer_load_dwordx4 v8, s[44:47], s65 offen lds\n"
        "buffer_load_dwordx4 v9, s[44:47], s65 offen offset:1024 lds\n"
        "buffer_load_dwordx4 v10, s[44:47], s65 offen offset:2048 lds\n"
        "s_waitcnt vmcnt(0)\n"
        "ds_read_b64_tr_b16 v[196:197], v11 offset:3072\n"
        "ds_read_b64_tr_b16 v[198:199], v11 offset:4608\n"
        "ds_read_b64_tr_b16 v[200:201], v11 offset:3080\n"
        "ds_read_b64_tr_b16 v[202:203], v11 offset:4616\n"
        "ds_read_b64_tr_b16 v[204:205], v15 offset:3072\n"
        "ds_read_b64_tr_b16 v[206:207], v15 offset:4608\n"
        "s_waitcnt lgkmcnt(0)\n"
        "s_getpc_b64 s[44:45]\n"
        ".Lop_pc_%=:\n"
        "s_add_u32 s44, s44, .Lop_code_%=-.Lop_pc_%=\n"
        "s_addc_u32 s45, s45, 0\n"
        "s_and_b32 s45, s45, 0xffff\n"
        "s_and_b32 s44, s44, -4\n"
        "s_mov_b32 s46, .Lop_end_%=-.Lop_code_%=\n"
        "s_mov_b32 s47, 0x20000\n"
        "s_lshl_b32 s65, s48, 10\n"
        "buffer_load_dwordx4 v[24:27], v1, s[44:47], s65 offen\n"
        "s_add_u32 s65, s65, 4096\n"
        "buffer_load_dwordx4 v[28:31], v1, s[44:47], s65 offen\n"
        "s_add_u32 s65, s65, 4096\n"
        "buffer_load_dwordx4 v[32:35], v1, s[44:47], s65 offen\n"
        "s_add_u32 s65, s65, 4096\n"
        "buffer_load_dwordx4 v[36:39], v1, s[44:47], s65 offen\n"
        "s_add_u32 s65, s65, 4096\n"
        "buffer_load_dwordx4 v[40:43], v1, s[44:47], s65 offen\n"
        "s_add_u32 s65, s65, 4096\n"
        "v_lshrrev_b32 v20, 4, v1\n"
        "s_mov_b32 s50, s56\n"
        "v_lshlrev_b32 v22, 7, v20\n"
        "s_sub_u32 s66, 0, s50\n"
        "s_mov_b32 s52, 32768\n"
        "s_mov_b32 s64, 0\n"
        ".Lop_pg0_top_%=:\n"
        "buffer_load_dword v21, v22, s[40:43], s52 offen sc1\n"
        "s_waitcnt vmcnt(0)\n"
        "v_cmp_gt_i32 vcc, 0, v21\n"
        "s_cbranch_vccz .Lop_pg0_ok_%=\n"
        "s_add_u32 s64, s64, 1\n"
        "s_and_b32 s65, s64, 255\n"
        "s_cbranch_scc1 .Lop_pg0_go_%=\n"
        "s_mov_b32 s65, 49152\n"
        "s_cmp_lt_u32 s64, 1048576\n"
        "s_cbranch_scc1 .Lop_pg0_chk_%=\n"
        "v_mov_b32 v21, 1\n"
        "buffer_store_dword v21, off, s[40:43], s65 sc1\n"
        "s_waitcnt vmcnt(0)\n"
        ".Lop_pg0_chk_%=:\n"
        "buffer_load_dword v21, off, s[40:43], s65 sc1\n"
        "s_waitcnt vmcnt(0)\n"
        "v_readfirstlane_b32 s65, v21\n"
        "s_nop 0\n"
        "s_cmp_lg_u32 s65, 1\n"
        "s_cbranch_scc1 .Lop_pg0_go_%=\n"
        "s_endpgm\n"
        ".Lop_pg0_go_%=:\n"
        "s_sleep 2\n"
        "s_branch .Lop_pg0_top_%=\n"
        ".Lop_pg0_ok_%=:\n"
        "v_cmp_eq_u32 s[52:53], 1, v21\n"
        "s_nop 1\n"
        "s_mov_b32 s54, 40960\n"
        "s_mov_b32 s64, 0\n"
        ".Lop_pg1_top_%=:\n"
        "buffer_load_dword v21, v22, s[40:43], s54 offen sc1\n"
        "s_waitcnt vmcnt(0)\n"
        "v_cmp_gt_i32 vcc, 0, v21\n"
        "s_cbranch_vccz .Lop_pg1_ok_%=\n"
        "s_add_u32 s64, s64, 1\n"
        "s_and_b32 s65, s64, 255\n"
        "s_cbranch_scc1 .Lop_pg1_go_%=\n"
        "s_mov_b32 s65, 49152\n"
        "s_cmp_lt_u32 s64, 1048576\n"
        "s_cbranch_scc1 .Lop_pg1_chk_%=\n"
        "v_mov_b32 v21, 1\n"
        "buffer_store_dword v21, off, s[40:43], s65 sc1\n"
        "s_waitcnt vmcnt(0)\n"
        ".Lop_pg1_chk_%=:\n"
        "buffer_load_dword v21, off, s[40:43], s65 sc1\n"
        "s_waitcnt vmcnt(0)\n"
        "v_readfirstlane_b32 s65, v21\n"
        "s_nop 0\n"
        "s_cmp_lg_u32 s65, 1\n"
        "s_cbranch_scc1 .Lop_pg1_go_%=\n"
        "s_endpgm\n"
        ".Lop_pg1_go_%=:\n"
        "s_sleep 2\n"
        "s_branch .Lop_pg1_top_%=\n"
        ".Lop_pg1_ok_%=:\n"
        "v_cmp_eq_u32 s[54:55], 1, v21\n"
        "s_nop 1\n"
        "s_mov_b32 vcc_lo, 0x55555555\n"
        "s_mov_b32 vcc_hi, 0x55555555\n"
        "s_xor_b64 vcc, vcc, s[52:53]\n"
        "s_bcnt1_i32_b64 s65, vcc\n"
        "s_sub_u32 s64, s65, 23\n"
        "s_cmp_lt_u32 s64, 19\n"
        "s_cbranch_scc1 .Lop_rawmap_%=\n"
        "s_cmp_lt_u32 s65, 32\n"
        "s_cselect_b32 s65, 0, -1\n"
        "s_xor_b32 s52, s65, 0x55555555\n"
        "s_xor_b32 s53, s65, 0x55555555\n"
        "s_xor_b32 s54, s65, 0x55555555\n"
        "s_xor_b32 s55, s65, 0xaaaaaaaa\n"
        ".Lop_rawmap_%=:\n"
        "s_xor_b32 s52, s52, s66\n"
        "s_xor_b32 s53, s53, s66\n"
        "s_xor_b32 s54, s54, s66\n"
        "s_xor_b32 s55, s55, s66\n"
        "s_and_b32 s76, s55, 1\n"
        "s_lshl_b32 s76, s76, 13\n"
        "s_add_u32 s60, s60, s76\n"
        "s_barrier\n"
        "v_mov_b32 v48, v6\n"
        "s_mov_b32 s68, -1\n"
        "s_mov_b32 s69, 0\n"
        "s_mov_b32 s78, 0\n"
        "s_mov_b32 s74, -1\n"
        "s_mov_b32 s86, s76\n"
        "s_lshr_b32 s85, s61, 16\n"
        "s_and_b32 s61, s61, 0xffff\n"
        "s_mov_b32 s89, 241172480\n"
        "s_mov_b32 s84, 241172480\n"
        "v_mov_b32 v6, v0\n"
        "s_mov_b32 s80, 0xbfb8aa3b\n"
        "s_lshr_b32 s67, s69, 1\n"
        "s_bitcmp1_b64 s[52:53], s67\n"
        "s_cselect_b32 s65, 8192, 0\n"
        "s_lshl_b32 s77, s67, 21\n"
        "s_add_u32 s77, s77, s65\n"
        "s_add_u32 s71, s77, s58\n"
        "s_add_u32 s78, s78, 32768\n"
        "s_cmp_eq_u32 s78, 98304\n"
        "s_cselect_b32 s78, 0, s78\n"
        "s_add_u32 m0, s78, s61\n"
        "s_lshr_b32 s70, s69, 0\n"
        "s_branch .Lop_wait1_%=\n"
        ".p2align 6\n"
        ".p2align 5\n"
        ".Lop_code_%=:\n"
        ".Lop_i0_%=:\n"
        "v_mfma_f32_16x16x32_bf16 v[24:27], a[0:3], v[8:11], 0\n"
        "buffer_load_dword v4, v2, s[40:43], s86 offen sc1\n"
        "v_mfma_f32_16x16x32_bf16 v[28:31], a[4:7], v[8:11], 0\n"
        "s_add_u32 s71, s77, s58\n"
        "s_add_u32 s71, s71, 4096\n"
        "s_add_u32 s78, s78, 32768\n"
        "v_mfma_f32_16x16x32_bf16 v[32:35], a[8:11], v[8:11], 0\n"
        "s_cmp_eq_u32 s78, 98304\n"
        "s_cselect_b32 s78, 0, s78\n"
        "ds_read_b128 v[8:11], v0 offset:4096\n"
        "v_mfma_f32_16x16x32_bf16 v[24:27], a[12:15], v[12:15], v[24:27]\n"
        "s_add_u32 m0, s78, s61\n"
        "s_lshr_b32 s70, s69, 0\n"
        "v_cvt_pk_bf16_f32 v36, v36, v37\n"
        "v_mfma_f32_16x16x32_bf16 v[28:31], a[16:19], v[12:15], v[28:31]\n"
        "v_cvt_pk_bf16_f32 v37, v38, v39\n"
        "v_cvt_pk_bf16_f32 v38, v40, v41\n"
        "v_mfma_f32_16x16x32_bf16 v[32:35], a[20:23], v[12:15], v[32:35]\n"
        "s_waitcnt lgkmcnt(1)\n"
        "ds_read_b128 v[12:15], v0 offset:5120\n"
        "v_cvt_pk_bf16_f32 v39, v42, v43\n"
        "v_mfma_f32_16x16x32_bf16 v[24:27], a[24:27], v[16:19], v[24:27]\n"
        "buffer_store_dwordx4 v[36:39], v48, s[40:43], s72 offen offset:0 sc1\n"
        "v_mfma_f32_16x16x32_bf16 v[28:31], a[28:31], v[16:19], v[28:31]\n"
        "v_cvt_pk_bf16_f32 v44, v44, v45\n"
        "v_cvt_pk_bf16_f32 v45, v46, v47\n"
        "v_mfma_f32_16x16x32_bf16 v[32:35], a[32:35], v[16:19], v[32:35]\n"
        "s_nop 0\n"
        "ds_read_b128 v[16:19], v0 offset:6144\n"
        "v_mfma_f32_16x16x32_bf16 v[24:27], a[36:39], v[20:23], v[24:27]\n"
        "v_permlane16_swap_b32 v44, v46\n"
        "v_mfma_f32_16x16x32_bf16 v[28:31], a[40:43], v[20:23], v[28:31]\n"
        "v_permlane16_swap_b32 v45, v47\n"
        "v_mfma_f32_16x16x32_bf16 v[32:35], a[44:47], v[20:23], v[32:35]\n"
        "s_waitcnt lgkmcnt(1)\n"
        "ds_read_b128 v[20:23], v0 offset:7168\n"
        "v_mfma_f32_16x16x32_bf16 v[24:27], a[48:51], v[8:11], v[24:27]\n"
        "v_mfma_f32_16x16x32_bf16 v[28:31], a[52:55], v[8:11], v[28:31]\n"
        "buffer_store_dwordx4 v[44:47], v7, s[40:43], s72 offen offset:0 sc1\n"
        "v_mfma_f32_16x16x32_bf16 v[32:35], a[56:59], v[8:11], v[32:35]\n"
        "ds_read_b128 v[8:11], v0 offset:8192\n"
        "v_mfma_f32_16x16x32_bf16 v[24:27], a[60:63], v[12:15], v[24:27]\n"
        "v_mfma_f32_16x16x32_bf16 v[28:31], a[64:67], v[12:15], v[28:31]\n"
        "v_mfma_f32_16x16x32_bf16 v[32:35], a[68:71], v[12:15], v[32:35]\n"
        "s_waitcnt lgkmcnt(1)\n"
        "ds_read_b128 v[12:15], v0 offset:9216\n"
        "v_mfma_f32_16x16x32_bf16 v[24:27], a[72:75], v[16:19], v[24:27]\n"
        "v_mfma_f32_16x16x32_bf16 v[28:31], a[76:79], v[16:19], v[28:31]\n"
        "s_waitcnt vmcnt(2)\n"
        "v_cmp_gt_i32 vcc, s70, v4\n"
        "v_mfma_f32_16x16x32_bf16 v[32:35], a[80:83], v[16:19], v[32:35]\n"
        "ds_read_b128 v[16:19], v0 offset:10240\n"
        "v_mfma_f32_16x16x32_bf16 v[24:27], a[84:87], v[20:23], v[24:27]\n"
        "s_and_b64 vcc, vcc, 3\n"
        "v_mfma_f32_16x16x32_bf16 v[28:31], a[88:91], v[20:23], v[28:31]\n"
        "s_cbranch_vccnz .Lop_i00_%=\n"
        "v_mfma_f32_16x16x32_bf16 v[32:35], a[92:95], v[20:23], v[32:35]\n"
        "s_waitcnt lgkmcnt(1)\n"
        "ds_read_b128 v[20:23], v0 offset:11264\n"
        "v_mfma_f32_16x16x32_bf16 v[24:27], a[96:99], v[8:11], v[24:27]\n"
        "buffer_load_dwordx4 v1, s[40:43], s71 offen offset:0 sc1 lds\n"
        "v_mfma_f32_16x16x32_bf16 v[28:31], a[100:103], v[8:11], v[28:31]\n"
        "v_mfma_f32_16x16x32_bf16 v[32:35], a[104:107], v[8:11], v[32:35]\n"
        "ds_read_b128 v[8:11], v0 offset:12288\n"
        "v_mfma_f32_16x16x32_bf16 v[24:27], a[108:111], v[12:15], v[24:27]\n"
        "buffer_load_dwordx4 v1, s[40:43], s71 offen offset:1024 sc1 lds\n"
        "v_mfma_f32_16x16x32_bf16 v[28:31], a[112:115], v[12:15], v[28:31]\n"
        "v_mfma_f32_16x16x32_bf16 v[32:35], a[116:119], v[12:15], v[32:35]\n"
        "s_waitcnt lgkmcnt(1)\n"
        "ds_read_b128 v[12:15], v0 offset:13312\n"
        "v_mfma_f32_16x16x32_bf16 v[24:27], a[120:123], v[16:19], v[24:27]\n"
        "v_mfma_f32_16x16x32_bf16 v[28:31], a[124:127], v[16:19], v[28:31]\n"
        "buffer_load_dwordx4 v1, s[40:43], s71 offen offset:2048 sc1 lds\n"
        "v_mfma_f32_16x16x32_bf16 v[32:35], a[128:131], v[16:19], v[32:35]\n"
        "ds_read_b128 v[16:19], v0 offset:14336\n"
        "v_mfma_f32_16x16x32_bf16 v[24:27], a[132:135], v[20:23], v[24:27]\n"
        "v_mfma_f32_16x16x32_bf16 v[28:31], a[136:139], v[20:23], v[28:31]\n"
        "v_mfma_f32_16x16x32_bf16 v[32:35], a[140:143], v[20:23], v[32:35]\n"
        "s_waitcnt lgkmcnt(1)\n"
        "ds_read_b128 v[20:23], v0 offset:15360\n"
        "v_mfma_f32_16x16x32_bf16 v[24:27], a[144:147], v[8:11], v[24:27]\n"
        "buffer_load_dwordx4 v1, s[40:43], s71 offen offset:3072 sc1 lds\n"
        "v_mfma_f32_16x16x32_bf16 v[28:31], a[148:151], v[8:11], v[28:31]\n"
        "s_barrier\n"
        "s_add_u32 m0, m0, 16384\n"
        "v_mfma_f32_16x16x32_bf16 v[32:35], a[152:155], v[8:11], v[32:35]\n"
        "s_add_u32 s71, s71, 65536\n"
        "ds_read_b128 v[8:11], v0 offset:16384\n"
        "v_mfma_f32_16x16x32_bf16 v[24:27], a[156:159], v[12:15], v[24:27]\n"
        "buffer_load_dwordx4 v1, s[40:43], s71 offen offset:0 sc1 lds\n"
        "v_mfma_f32_16x16x32_bf16 v[28:31], a[160:163], v[12:15], v[28:31]\n"
        "buffer_store_dword v5, v3, s[40:43], s84 offen sc1\n"
        "v_mfma_f32_16x16x32_bf16 v[32:35], a[164:167], v[12:15], v[32:35]\n"
        "s_waitcnt lgkmcnt(1)\n"
        "ds_read_b128 v[12:15], v0 offset:17408\n"
        "v_mov_b32 v5, s74\n"
        "v_mfma_f32_16x16x32_bf16 v[24:27], a[168:171], v[16:19], v[24:27]\n"
        "s_getreg_b32 s88, hwreg(HW_REG_IB_STS, 0, 4)\n"
        "v_mfma_f32_16x16x32_bf16 v[28:31], a[172:175], v[16:19], v[28:31]\n"
        "buffer_load_dwordx4 v1, s[40:43], s71 offen offset:1024 sc1 lds\n"
        "v_mfma_f32_16x16x32_bf16 v[32:35], a[176:179], v[16:19], v[32:35]\n"
        "ds_read_b128 v[16:19], v0 offset:18432\n"
        "v_mfma_f32_16x16x32_bf16 v[24:27], a[180:183], v[20:23], v[24:27]\n"
        "s_cmp_le_u32 s88, 6\n"
        "s_cselect_b32 s88, s73, s89\n"
        "s_cselect_b32 s73, s89, s73\n"
        "v_mfma_f32_16x16x32_bf16 v[28:31], a[184:187], v[20:23], v[28:31]\n"
        "buffer_store_dword v5, v3, s[40:43], s88 offen sc1\n"
        "v_mfma_f32_16x16x32_bf16 v[32:35], a[188:191], v[20:23], v[32:35]\n"
        "s_waitcnt lgkmcnt(1)\n"
        "ds_read_b128 v[20:23], v0 offset:19456\n"
        "v_mfma_f32_16x16x32_bf16 v[24:27], a[192:195], v[8:11], v[24:27]\n"
        "s_getreg_b32 s88, hwreg(HW_REG_IB_STS, 0, 4)\n"
        "buffer_load_dwordx4 v1, s[40:43], s71 offen offset:2048 sc1 lds\n"
        "v_mfma_f32_16x16x32_bf16 v[28:31], a[196:199], v[8:11], v[28:31]\n"
        "s_cmp_le_u32 s88, 8\n"
        "s_cselect_b32 s88, s73, s89\n"
        "s_cselect_b32 s73, s89, s73\n"
        "v_mfma_f32_16x16x32_bf16 v[32:35], a[200:203], v[8:11], v[32:35]\n"
        "ds_read_b128 v[8:11], v0 offset:20480\n"
        "v_mfma_f32_16x16x32_bf16 v[24:27], a[204:207], v[12:15], v[24:27]\n"
        "buffer_load_dwordx4 v1, s[40:43], s71 offen offset:3072 sc1 lds\n"
        "v_mfma_f32_16x16x32_bf16 v[28:31], a[208:211], v[12:15], v[28:31]\n"
        "s_add_u32 s69, s69, 1\n"
        "buffer_store_dword v5, v3, s[40:43], s88 offen sc1\n"
        "v_mfma_f32_16x16x32_bf16 v[32:35], a[212:215], v[12:15], v[32:35]\n"
        "s_waitcnt lgkmcnt(1)\n"
        "ds_read_b128 v[12:15], v0 offset:21504\n"
        "v_mfma_f32_16x16x32_bf16 v[24:27], a[216:219], v[16:19], v[24:27]\n"
        "s_getreg_b32 s88, hwreg(HW_REG_IB_STS, 0, 4)\n"
        "v_mfma_f32_16x16x32_bf16 v[28:31], a[220:223], v[16:19], v[28:31]\n"
        "s_cmp_le_u32 s88, 11\n"
        "s_cselect_b32 s88, s73, s89\n"
        "s_cselect_b32 s73, s89, s73\n"
        "v_mfma_f32_16x16x32_bf16 v[32:35], a[224:227], v[16:19], v[32:35]\n"
        "ds_read_b128 v[16:19], v0 offset:22528\n"
        "v_mfma_f32_16x16x32_bf16 v[24:27], a[228:231], v[20:23], v[24:27]\n"
        "buffer_store_dword v5, v3, s[40:43], s88 offen sc1\n"
        "v_mfma_f32_16x16x32_bf16 v[28:31], a[232:235], v[20:23], v[28:31]\n"
        "v_mfma_f32_16x16x32_bf16 v[32:35], a[236:239], v[20:23], v[32:35]\n"
        "s_waitcnt lgkmcnt(1)\n"
        "ds_read_b128 v[20:23], v0 offset:23552\n"
        "v_mfma_f32_16x16x32_bf16 v[24:27], a[240:243], v[8:11], v[24:27]\n"
        "v_mfma_f32_16x16x32_bf16 v[28:31], a[244:247], v[8:11], v[28:31]\n"
        "v_mfma_f32_16x16x32_bf16 v[32:35], a[248:251], v[8:11], v[32:35]\n"
        "ds_read_b128 v[8:11], v0 offset:24576\n"
        "v_mfma_f32_16x16x32_bf16 v[24:27], a[252:255], v[12:15], v[24:27]\n"
        "v_mfma_f32_16x16x32_bf16 v[28:31], v[80:83], v[12:15], v[28:31]\n"
        "v_mfma_f32_16x16x32_bf16 v[32:35], v[84:87], v[12:15], v[32:35]\n"
        "s_waitcnt lgkmcnt(1)\n"
        "ds_read_b128 v[12:15], v0 offset:25600\n"
        "v_mfma_f32_16x16x32_bf16 v[24:27], v[88:91], v[16:19], v[24:27]\n"
        "v_mfma_f32_16x16x32_bf16 v[28:31], v[92:95], v[16:19], v[28:31]\n"
        "s_waitcnt vmcnt(12)\n"
        "buffer_store_dword v5, v3, s[40:43], s73 offen sc1\n"
        "v_mfma_f32_16x16x32_bf16 v[32:35], v[96:99], v[16:19], v[32:35]\n"
        "s_mov_b32 s84, s89\n"
        "ds_read_b128 v[16:19], v0 offset:26624\n"
        "s_mov_b32 s73, s60\n"
        "v_mfma_f32_16x16x32_bf16 v[24:27], v[100:103], v[20:23], v[24:27]\n"
        "s_add_u32 s74, s74, 1\n"
        "s_add_u32 s68, s68, 1\n"
        "s_lshr_b32 s66, s68, 1\n"
        "v_mfma_f32_16x16x32_bf16 v[28:31], v[104:107], v[20:23], v[28:31]\n"
        "s_bitcmp1_b32 s54, s66\n"
        "s_cselect_b32 s66, 8192, 0\n"
        "s_lshl_b32 s75, s68, 20\n"
        "v_mfma_f32_16x16x32_bf16 v[32:35], v[108:111], v[20:23], v[32:35]\n"
        "s_waitcnt lgkmcnt(1)\n"
        "ds_read_b128 v[20:23], v0 offset:27648\n"
        "s_add_u32 s75, s75, s66\n"
        "v_mfma_f32_16x16x32_bf16 v[24:27], v[112:115], v[8:11], v[24:27]\n"
        "s_add_u32 s72, s75, s59\n"
        "v_mfma_f32_16x16x32_bf16 v[28:31], v[116:119], v[8:11], v[28:31]\n"
        "v_mfma_f32_16x16x32_bf16 v[32:35], v[120:123], v[8:11], v[32:35]\n"
        "ds_read_b128 v[8:11], v0 offset:28672\n"
        "v_mfma_f32_16x16x32_bf16 v[24:27], v[124:127], v[12:15], v[24:27]\n"
        "v_mfma_f32_16x16x32_bf16 v[28:31], v[128:131], v[12:15], v[28:31]\n"
        "v_mfma_f32_16x16x32_bf16 v[32:35], v[132:135], v[12:15], v[32:35]\n"
        "s_waitcnt lgkmcnt(1)\n"
        "ds_read_b128 v[12:15], v0 offset:29696\n"
        "v_mfma_f32_16x16x32_bf16 v[24:27], v[136:139], v[16:19], v[24:27]\n"
        "v_mfma_f32_16x16x32_bf16 v[28:31], v[140:143], v[16:19], v[28:31]\n"
        "v_mfma_f32_16x16x32_bf16 v[32:35], v[144:147], v[16:19], v[32:35]\n"
        "ds_read_b128 v[16:19], v0 offset:30720\n"
        "v_mfma_f32_16x16x32_bf16 v[24:27], v[148:151], v[20:23], v[24:27]\n"
        "v_mfma_f32_16x16x32_bf16 v[28:31], v[152:155], v[20:23], v[28:31]\n"
        "v_mfma_f32_16x16x32_bf16 v[32:35], v[156:159], v[20:23], v[32:35]\n"
        "s_waitcnt lgkmcnt(1)\n"
        "ds_read_b128 v[20:23], v0 offset:31744\n"
        "v_mfma_f32_16x16x32_bf16 v[24:27], v[160:163], v[8:11], v[24:27]\n"
        "v_mfma_f32_16x16x32_bf16 v[28:31], v[164:167], v[8:11], v[28:31]\n"
        "s_waitcnt vmcnt(9)\n"
        "s_barrier\n"
        "v_add_u32 v0, s78, v6\n"
        "v_mfma_f32_16x16x32_bf16 v[32:35], v[168:171], v[8:11], v[32:35]\n"
        "ds_read_b128 v[8:11], v0 offset:0\n"
        "v_mfma_f32_16x16x32_bf16 v[24:27], v[172:175], v[12:15], v[24:27]\n"
        "v_mfma_f32_16x16x32_bf16 v[28:31], v[176:179], v[12:15], v[28:31]\n"
        "v_mfma_f32_16x16x32_bf16 v[32:35], v[180:183], v[12:15], v[32:35]\n"
        "s_waitcnt lgkmcnt(1)\n"
        "ds_read_b128 v[12:15], v0 offset:1024\n"
        "v_mfma_f32_16x16x32_bf16 v[24:27], v[184:187], v[16:19], v[24:27]\n"
        "v_mfma_f32_16x16x32_bf16 v[28:31], v[188:191], v[16:19], v[28:31]\n"
        "v_mfma_f32_16x16x32_bf16 v[32:35], v[192:195], v[16:19], v[32:35]\n"
        "ds_read_b128 v[16:19], v0 offset:2048\n"
        "v_mfma_f32_16x16x32_bf16 v[24:27], v[196:199], v[20:23], v[24:27]\n"
        "v_mfma_f32_16x16x32_bf16 v[28:31], v[200:203], v[20:23], v[28:31]\n"
        "v_mfma_f32_16x16x32_bf16 v[32:35], v[204:207], v[20:23], v[32:35]\n"
        "s_waitcnt lgkmcnt(1)\n"
        "ds_read_b128 v[20:23], v0 offset:3072\n"
        "s_branch .Lop_i1_%=\n"
        ".p2align 5\n"
        ".Lop_i00_%=:\n"
        "v_mfma_f32_16x16x32_bf16 v[32:35], a[92:95], v[20:23], v[32:35]\n"
        "s_waitcnt lgkmcnt(1)\n"
        "ds_read_b128 v[20:23], v0 offset:11264\n"
        "v_mfma_f32_16x16x32_bf16 v[24:27], a[96:99], v[8:11], v[24:27]\n"
        "buffer_load_dword v4, v2, s[40:43], s86 offen sc1\n"
        "v_mfma_f32_16x16x32_bf16 v[28:31], a[100:103], v[8:11], v[28:31]\n"
        "v_mfma_f32_16x16x32_bf16 v[32:35], a[104:107], v[8:11], v[32:35]\n"
        "ds_read_b128 v[8:11], v0 offset:12288\n"
        "v_mfma_f32_16x16x32_bf16 v[24:27], a[108:111], v[12:15], v[24:27]\n"
        "v_mfma_f32_16x16x32_bf16 v[28:31], a[112:115], v[12:15], v[28:31]\n"
        "v_mfma_f32_16x16x32_bf16 v[32:35], a[116:119], v[12:15], v[32:35]\n"
        "s_waitcnt lgkmcnt(1)\n"
        "ds_read_b128 v[12:15], v0 offset:13312\n"
        "v_mfma_f32_16x16x32_bf16 v[24:27], a[120:123], v[16:19], v[24:27]\n"
        "v_mfma_f32_16x16x32_bf16 v[28:31], a[124:127], v[16:19], v[28:31]\n"
        "v_mfma_f32_16x16x32_bf16 v[32:35], a[128:131], v[16:19], v[32:35]\n"
        "ds_read_b128 v[16:19], v0 offset:14336\n"
        "v_mfma_f32_16x16x32_bf16 v[24:27], a[132:135], v[20:23], v[24:27]\n"
        "v_mfma_f32_16x16x32_bf16 v[28:31], a[136:139], v[20:23], v[28:31]\n"
        "v_mfma_f32_16x16x32_bf16 v[32:35], a[140:143], v[20:23], v[32:35]\n"
        "s_waitcnt lgkmcnt(1)\n"
        "ds_read_b128 v[20:23], v0 offset:15360\n"
        "v_mfma_f32_16x16x32_bf16 v[24:27], a[144:147], v[8:11], v[24:27]\n"
        "v_mfma_f32_16x16x32_bf16 v[28:31], a[148:151], v[8:11], v[28:31]\n"
        "s_barrier\n"
        "v_mfma_f32_16x16x32_bf16 v[32:35], a[152:155], v[8:11], v[32:35]\n"
        "ds_read_b128 v[8:11], v0 offset:16384\n"
        "v_mfma_f32_16x16x32_bf16 v[24:27], a[156:159], v[12:15], v[24:27]\n"
        "buffer_store_dword v5, v3, s[40:43], s84 offen sc1\n"
        "v_mfma_f32_16x16x32_bf16 v[28:31], a[160:163], v[12:15], v[28:31]\n"
        "v_mov_b32 v5, s74\n"
        "v_mfma_f32_16x16x32_bf16 v[32:35], a[164:167], v[12:15], v[32:35]\n"
        "s_waitcnt lgkmcnt(1)\n"
        "ds_read_b128 v[12:15], v0 offset:17408\n"
        "v_mfma_f32_16x16x32_bf16 v[24:27], a[168:171], v[16:19], v[24:27]\n"
        "s_getreg_b32 s88, hwreg(HW_REG_IB_STS, 0, 4)\n"
        "v_mfma_f32_16x16x32_bf16 v[28:31], a[172:175], v[16:19], v[28:31]\n"
        "s_cmp_le_u32 s88, 2\n"
        "s_cselect_b32 s88, s73, s89\n"
        "s_cselect_b32 s73, s89, s73\n"
        "v_mfma_f32_16x16x32_bf16 v[32:35], a[176:179], v[16:19], v[32:35]\n"
        "ds_read_b128 v[16:19], v0 offset:18432\n"
        "v_mfma_f32_16x16x32_bf16 v[24:27], a[180:183], v[20:23], v[24:27]\n"
        "buffer_store_dword v5, v3, s[40:43], s88 offen sc1\n"
        "v_mfma_f32_16x16x32_bf16 v[28:31], a[184:187], v[20:23], v[28:31]\n"
        "s_getreg_b32 s88, hwreg(HW_REG_IB_STS, 0, 4)\n"
        "v_mfma_f32_16x16x32_bf16 v[32:35], a[188:191], v[20:23], v[32:35]\n"
        "s_waitcnt lgkmcnt(1)\n"
        "ds_read_b128 v[20:23], v0 offset:19456\n"
        "v_mfma_f32_16x16x32_bf16 v[24:27], a[192:195], v[8:11], v[24:27]\n"
        "s_cmp_le_u32 s88, 3\n"
        "s_cselect_b32 s88, s73, s89\n"
        "s_cselect_b32 s73, s89, s73\n"
        "v_mfma_f32_16x16x32_bf16 v[28:31], a[196:199], v[8:11], v[28:31]\n"
        "s_waitcnt vmcnt(2)\n"
        "v_cmp_gt_i32 vcc, s70, v4\n"
        "v_mfma_f32_16x16x32_bf16 v[32:35], a[200:203], v[8:11], v[32:35]\n"
        "ds_read_b128 v[8:11], v0 offset:20480\n"
        "v_mfma_f32_16x16x32_bf16 v[24:27], a[204:207], v[12:15], v[24:27]\n"
        "s_and_b64 vcc, vcc, 3\n"
        "v_mfma_f32_16x16x32_bf16 v[28:31], a[208:211], v[12:15], v[28:31]\n"
        "s_cbranch_vccnz .Lop_i01_%=\n"
        "v_mfma_f32_16x16x32_bf16 v[32:35], a[212:215], v[12:15], v[32:35]\n"
        "s_waitcnt lgkmcnt(1)\n"
        "ds_read_b128 v[12:15], v0 offset:21504\n"
        "v_mfma_f32_16x16x32_bf16 v[24:27], a[216:219], v[16:19], v[24:27]\n"
        "buffer_load_dwordx4 v1, s[40:43], s71 offen offset:0 sc1 lds\n"
        "v_mfma_f32_16x16x32_bf16 v[28:31], a[220:223], v[16:19], v[28:31]\n"
        "buffer_store_dword v5, v3, s[40:43], s88 offen sc1\n"
        "v_mfma_f32_16x16x32_bf16 v[32:35], a[224:227], v[16:19], v[32:35]\n"
        "s_getreg_b32 s88, hwreg(HW_REG_IB_STS, 0, 4)\n"
        "ds_read_b128 v[16:19], v0 offset:22528\n"
        "v_mfma_f32_16x16x32_bf16 v[24:27], a[228:231], v[20:23], v[24:27]\n"
        "buffer_load_dwordx4 v1, s[40:43], s71 offen offset:1024 sc1 lds\n"
        "v_mfma_f32_16x16x32_bf16 v[28:31], a[232:235], v[20:23], v[28:31]\n"
        "s_cmp_le_u32 s88, 15\n"
        "s_cselect_b32 s88, s73, s89\n"
        "s_cselect_b32 s73, s89, s73\n"
        "v_mfma_f32_16x16x32_bf16 v[32:35], a[236:239], v[20:23], v[32:35]\n"
        "s_waitcnt lgkmcnt(1)\n"
        "ds_read_b128 v[20:23], v0 offset:23552\n"
        "v_mfma_f32_16x16x32_bf16 v[24:27], a[240:243], v[8:11], v[24:27]\n"
        "buffer_load_dwordx4 v1, s[40:43], s71 offen offset:2048 sc1 lds\n"
        "v_mfma_f32_16x16x32_bf16 v[28:31], a[244:247], v[8:11], v[28:31]\n"
        "buffer_store_dword v5, v3, s[40:43], s88 offen sc1\n"
        "v_mfma_f32_16x16x32_bf16 v[32:35], a[248:251], v[8:11], v[32:35]\n"
        "ds_read_b128 v[8:11], v0 offset:24576\n"
        "v_mfma_f32_16x16x32_bf16 v[24:27], a[252:255], v[12:15], v[24:27]\n"
        "buffer_load_dwordx4 v1, s[40:43], s71 offen offset:3072 sc1 lds\n"
        "v_mfma_f32_16x16x32_bf16 v[28:31], v[80:83], v[12:15], v[28:31]\n"
        "s_add_u32 m0, m0, 16384\n"
        "s_add_u32 s71, s71, 65536\n"
        "v_mfma_f32_16x16x32_bf16 v[32:35], v[84:87], v[12:15], v[32:35]\n"
        "s_waitcnt lgkmcnt(1)\n"
        "ds_read_b128 v[12:15], v0 offset:25600\n"
        "v_mfma_f32_16x16x32_bf16 v[24:27], v[88:91], v[16:19], v[24:27]\n"
        "buffer_load_dwordx4 v1, s[40:43], s71 offen offset:0 sc1 lds\n"
        "v_mfma_f32_16x16x32_bf16 v[28:31], v[92:95], v[16:19], v[28:31]\n"
        "buffer_store_dword v5, v3, s[40:43], s73 offen sc1\n"
        "v_mfma_f32_16x16x32_bf16 v[32:35], v[96:99], v[16:19], v[32:35]\n"
        "s_mov_b32 s84, s89\n"
        "ds_read_b128 v[16:19], v0 offset:26624\n"
        "s_mov_b32 s73, s60\n"
        "v_mfma_f32_16x16x32_bf16 v[24:27], v[100:103], v[20:23], v[24:27]\n"
        "s_add_u32 s74, s74, 1\n"
        "s_add_u32 s68, s68, 1\n"
        "s_lshr_b32 s66, s68, 1\n"
        "v_mfma_f32_16x16x32_bf16 v[28:31], v[104:107], v[20:23], v[28:31]\n"
        "s_bitcmp1_b32 s54, s66\n"
        "s_cselect_b32 s66, 8192, 0\n"
        "s_lshl_b32 s75, s68, 20\n"
        "v_mfma_f32_16x16x32_bf16 v[32:35], v[108:111], v[20:23], v[32:35]\n"
        "s_waitcnt lgkmcnt(1)\n"
        "ds_read_b128 v[20:23], v0 offset:27648\n"
        "s_add_u32 s75, s75, s66\n"
        "v_mfma_f32_16x16x32_bf16 v[24:27], v[112:115], v[8:11], v[24:27]\n"
        "s_add_u32 s72, s75, s59\n"
        "v_mfma_f32_16x16x32_bf16 v[28:31], v[116:119], v[8:11], v[28:31]\n"
        "buffer_load_dwordx4 v1, s[40:43], s71 offen offset:1024 sc1 lds\n"
        "v_mfma_f32_16x16x32_bf16 v[32:35], v[120:123], v[8:11], v[32:35]\n"
        "ds_read_b128 v[8:11], v0 offset:28672\n"
        "v_mfma_f32_16x16x32_bf16 v[24:27], v[124:127], v[12:15], v[24:27]\n"
        "v_mfma_f32_16x16x32_bf16 v[28:31], v[128:131], v[12:15], v[28:31]\n"
        "v_mfma_f32_16x16x32_bf16 v[32:35], v[132:135], v[12:15], v[32:35]\n"
        "s_waitcnt lgkmcnt(1)\n"
        "ds_read_b128 v[12:15], v0 offset:29696\n"
        "v_mfma_f32_16x16x32_bf16 v[24:27], v[136:139], v[16:19], v[24:27]\n"
        "buffer_load_dwordx4 v1, s[40:43], s71 offen offset:2048 sc1 lds\n"
        "v_mfma_f32_16x16x32_bf16 v[28:31], v[140:143], v[16:19], v[28:31]\n"
        "v_mfma_f32_16x16x32_bf16 v[32:35], v[144:147], v[16:19], v[32:35]\n"
        "ds_read_b128 v[16:19], v0 offset:30720\n"
        "v_mfma_f32_16x16x32_bf16 v[24:27], v[148:151], v[20:23], v[24:27]\n"
        "buffer_load_dwordx4 v1, s[40:43], s71 offen offset:3072 sc1 lds\n"
        "v_mfma_f32_16x16x32_bf16 v[28:31], v[152:155], v[20:23], v[28:31]\n"
        "s_add_u32 s69, s69, 1\n"
        "v_mfma_f32_16x16x32_bf16 v[32:35], v[156:159], v[20:23], v[32:35]\n"
        "s_waitcnt lgkmcnt(1)\n"
        "ds_read_b128 v[20:23], v0 offset:31744\n"
        "v_mfma_f32_16x16x32_bf16 v[24:27], v[160:163], v[8:11], v[24:27]\n"
        "v_mfma_f32_16x16x32_bf16 v[28:31], v[164:167], v[8:11], v[28:31]\n"
        "s_waitcnt vmcnt(5)\n"
        "s_barrier\n"
        "v_add_u32 v0, s78, v6\n"
        "v_mfma_f32_16x16x32_bf16 v[32:35], v[168:171], v[8:11], v[32:35]\n"
        "ds_read_b128 v[8:11], v0 offset:0\n"
        "v_mfma_f32_16x16x32_bf16 v[24:27], v[172:175], v[12:15], v[24:27]\n"
        "v_mfma_f32_16x16x32_bf16 v[28:31], v[176:179], v[12:15], v[28:31]\n"
        "v_mfma_f32_16x16x32_bf16 v[32:35], v[180:183], v[12:15], v[32:35]\n"
        "s_waitcnt lgkmcnt(1)\n"
        "ds_read_b128 v[12:15], v0 offset:1024\n"
        "v_mfma_f32_16x16x32_bf16 v[24:27], v[184:187], v[16:19], v[24:27]\n"
        "v_mfma_f32_16x16x32_bf16 v[28:31], v[188:191], v[16:19], v[28:31]\n"
        "v_mfma_f32_16x16x32_bf16 v[32:35], v[192:195], v[16:19], v[32:35]\n"
        "ds_read_b128 v[16:19], v0 offset:2048\n"
        "v_mfma_f32_16x16x32_bf16 v[24:27], v[196:199], v[20:23], v[24:27]\n"
        "v_mfma_f32_16x16x32_bf16 v[28:31], v[200:203], v[20:23], v[28:31]\n"
        "v_mfma_f32_16x16x32_bf16 v[32:35], v[204:207], v[20:23], v[32:35]\n"
        "s_waitcnt lgkmcnt(1)\n"
        "ds_read_b128 v[20:23], v0 offset:3072\n"
        "s_branch .Lop_i1_%=\n"
        ".p2align 5\n"
        ".Lop_i01_%=:\n"
        "v_mfma_f32_16x16x32_bf16 v[32:35], a[212:215], v[12:15], v[32:35]\n"
        "s_waitcnt lgkmcnt(1)\n"
        "ds_read_b128 v[12:15], v0 offset:21504\n"
        "v_mfma_f32_16x16x32_bf16 v[24:27], a[216:219], v[16:19], v[24:27]\n"
        "buffer_load_dword v4, v2, s[40:43], s86 offen sc1\n"
        "v_mfma_f32_16x16x32_bf16 v[28:31], a[220:223], v[16:19], v[28:31]\n"
        "buffer_store_dword v5, v3, s[40:43], s88 offen sc1\n"
        "v_mfma_f32_16x16x32_bf16 v[32:35], a[224:227], v[16:19], v[32:35]\n"
        "s_getreg_b32 s88, hwreg(HW_REG_IB_STS, 0, 4)\n"
        "ds_read_b128 v[16:19], v0 offset:22528\n"
        "v_mfma_f32_16x16x32_bf16 v[24:27], a[228:231], v[20:23], v[24:27]\n"
        "s_cmp_le_u32 s88, 15\n"
        "s_cselect_b32 s88, s73, s89\n"
        "s_cselect_b32 s73, s89, s73\n"
        "v_mfma_f32_16x16x32_bf16 v[28:31], a[232:235], v[20:23], v[28:31]\n"
        "buffer_store_dword v5, v3, s[40:43], s88 offen sc1\n"
        "v_mfma_f32_16x16x32_bf16 v[32:35], a[236:239], v[20:23], v[32:35]\n"
        "s_waitcnt lgkmcnt(1)\n"
        "ds_read_b128 v[20:23], v0 offset:23552\n"
        "v_mfma_f32_16x16x32_bf16 v[24:27], a[240:243], v[8:11], v[24:27]\n"
        "v_mfma_f32_16x16x32_bf16 v[28:31], a[244:247], v[8:11], v[28:31]\n"
        "v_mfma_f32_16x16x32_bf16 v[32:35], a[248:251], v[8:11], v[32:35]\n"
        "ds_read_b128 v[8:11], v0 offset:24576\n"
        "v_mfma_f32_16x16x32_bf16 v[24:27], a[252:255], v[12:15], v[24:27]\n"
        "v_mfma_f32_16x16x32_bf16 v[28:31], v[80:83], v[12:15], v[28:31]\n"
        "v_mfma_f32_16x16x32_bf16 v[32:35], v[84:87], v[12:15], v[32:35]\n"
        "s_waitcnt lgkmcnt(1)\n"
        "ds_read_b128 v[12:15], v0 offset:25600\n"
        "v_mfma_f32_16x16x32_bf16 v[24:27], v[88:91], v[16:19], v[24:27]\n"
        "v_mfma_f32_16x16x32_bf16 v[28:31], v[92:95], v[16:19], v[28:31]\n"
        "buffer_store_dword v5, v3, s[40:43], s73 offen sc1\n"
        "v_mfma_f32_16x16x32_bf16 v[32:35], v[96:99], v[16:19], v[32:35]\n"
        "s_mov_b32 s84, s89\n"
        "ds_read_b128 v[16:19], v0 offset:26624\n"
        "s_mov_b32 s73, s60\n"
        "v_mfma_f32_16x16x32_bf16 v[24:27], v[100:103], v[20:23], v[24:27]\n"
        "s_add_u32 s74, s74, 1\n"
        "s_add_u32 s68, s68, 1\n"
        "s_lshr_b32 s66, s68, 1\n"
        "v_mfma_f32_16x16x32_bf16 v[28:31], v[104:107], v[20:23], v[28:31]\n"
        "s_bitcmp1_b32 s54, s66\n"
        "s_cselect_b32 s66, 8192, 0\n"
        "s_lshl_b32 s75, s68, 20\n"
        "v_mfma_f32_16x16x32_bf16 v[32:35], v[108:111], v[20:23], v[32:35]\n"
        "s_waitcnt lgkmcnt(1)\n"
        "ds_read_b128 v[20:23], v0 offset:27648\n"
        "s_add_u32 s75, s75, s66\n"
        "v_mfma_f32_16x16x32_bf16 v[24:27], v[112:115], v[8:11], v[24:27]\n"
        "s_add_u32 s72, s75, s59\n"
        "v_mfma_f32_16x16x32_bf16 v[28:31], v[116:119], v[8:11], v[28:31]\n"
        "v_mfma_f32_16x16x32_bf16 v[32:35], v[120:123], v[8:11], v[32:35]\n"
        "ds_read_b128 v[8:11], v0 offset:28672\n"
        "v_mfma_f32_16x16x32_bf16 v[24:27], v[124:127], v[12:15], v[24:27]\n"
        "v_mfma_f32_16x16x32_bf16 v[28:31], v[128:131], v[12:15], v[28:31]\n"
        "v_mfma_f32_16x16x32_bf16 v[32:35], v[132:135], v[12:15], v[32:35]\n"
        "s_waitcnt lgkmcnt(1)\n"
        "ds_read_b128 v[12:15], v0 offset:29696\n"
        "v_mfma_f32_16x16x32_bf16 v[24:27], v[136:139], v[16:19], v[24:27]\n"
        "v_mfma_f32_16x16x32_bf16 v[28:31], v[140:143], v[16:19], v[28:31]\n"
        "v_mfma_f32_16x16x32_bf16 v[32:35], v[144:147], v[16:19], v[32:35]\n"
        "ds_read_b128 v[16:19], v0 offset:30720\n"
        "v_mfma_f32_16x16x32_bf16 v[24:27], v[148:151], v[20:23], v[24:27]\n"
        "s_waitcnt vmcnt(3)\n"
        "v_cmp_gt_i32_e64 vcc, s70, v4\n"
        "v_mfma_f32_16x16x32_bf16 v[28:31], v[152:155], v[20:23], v[28:31]\n"
        "s_nop 0\n"
        "v_mfma_f32_16x16x32_bf16 v[32:35], v[156:159], v[20:23], v[32:35]\n"
        "s_waitcnt lgkmcnt(1)\n"
        "ds_read_b128 v[20:23], v0 offset:31744\n"
        "s_and_b64 vcc, vcc, 3\n"
        "v_mfma_f32_16x16x32_bf16 v[24:27], v[160:163], v[8:11], v[24:27]\n"
        "s_cbranch_vccnz .Lop_i02_%=\n"
        "v_mfma_f32_16x16x32_bf16 v[28:31], v[164:167], v[8:11], v[28:31]\n"
        "buffer_load_dwordx4 v1, s[40:43], s71 offen offset:0 sc1 lds\n"
        "v_mfma_f32_16x16x32_bf16 v[32:35], v[168:171], v[8:11], v[32:35]\n"
        "v_mfma_f32_16x16x32_bf16 v[24:27], v[172:175], v[12:15], v[24:27]\n"
        "buffer_load_dwordx4 v1, s[40:43], s71 offen offset:1024 sc1 lds\n"
        "v_mfma_f32_16x16x32_bf16 v[28:31], v[176:179], v[12:15], v[28:31]\n"
        "v_mfma_f32_16x16x32_bf16 v[32:35], v[180:183], v[12:15], v[32:35]\n"
        "s_waitcnt lgkmcnt(0)\n"
        "buffer_load_dwordx4 v1, s[40:43], s71 offen offset:2048 sc1 lds\n"
        "v_mfma_f32_16x16x32_bf16 v[24:27], v[184:187], v[16:19], v[24:27]\n"
        "v_mfma_f32_16x16x32_bf16 v[28:31], v[188:191], v[16:19], v[28:31]\n"
        "buffer_load_dwordx4 v1, s[40:43], s71 offen offset:3072 sc1 lds\n"
        "v_mfma_f32_16x16x32_bf16 v[32:35], v[192:195], v[16:19], v[32:35]\n"
        "s_add_u32 m0, m0, 16384\n"
        "s_add_u32 s71, s71, 65536\n"
        "v_mfma_f32_16x16x32_bf16 v[24:27], v[196:199], v[20:23], v[24:27]\n"
        "buffer_load_dwordx4 v1, s[40:43], s71 offen offset:0 sc1 lds\n"
        "v_mfma_f32_16x16x32_bf16 v[28:31], v[200:203], v[20:23], v[28:31]\n"
        "v_mfma_f32_16x16x32_bf16 v[32:35], v[204:207], v[20:23], v[32:35]\n"
        "buffer_load_dwordx4 v1, s[40:43], s71 offen offset:1024 sc1 lds\n"
        "buffer_load_dwordx4 v1, s[40:43], s71 offen offset:2048 sc1 lds\n"
        "buffer_load_dwordx4 v1, s[40:43], s71 offen offset:3072 sc1 lds\n"
        "s_add_u32 s69, s69, 1\n"
        "s_nop 2\n"
        "v_cvt_pk_bf16_f32 v24, v24, v25\n"
        "v_cvt_pk_bf16_f32 v25, v26, v27\n"
        "v_cvt_pk_bf16_f32 v26, v28, v29\n"
        "v_cvt_pk_bf16_f32 v27, v30, v31\n"
        "buffer_store_dwordx4 v[24:27], v48, s[40:43], s72 offen offset:0 sc1\n"
        "v_cvt_pk_bf16_f32 v32, v32, v33\n"
        "v_cvt_pk_bf16_f32 v33, v34, v35\n"
        "s_nop 0\n"
        "v_permlane16_swap_b32 v32, v34\n"
        "v_permlane16_swap_b32 v33, v35\n"
        "buffer_store_dwordx4 v[32:35], v7, s[40:43], s72 offen offset:0 sc1\n"
        "v_mov_b32 v5, s74\n"
        "s_mov_b32 s84, s60\n"
        "s_waitcnt vmcnt(6)\n"
        "s_branch .Lop_go0_%=\n"
        ".p2align 5\n"
        ".Lop_i02_%=:\n"
        "v_mfma_f32_16x16x32_bf16 v[28:31], v[164:167], v[8:11], v[28:31]\n"
        "buffer_load_dword v4, v2, s[40:43], s86 offen sc1\n"
        "v_mfma_f32_16x16x32_bf16 v[32:35], v[168:171], v[8:11], v[32:35]\n"
        "v_mfma_f32_16x16x32_bf16 v[24:27], v[172:175], v[12:15], v[24:27]\n"
        "v_mfma_f32_16x16x32_bf16 v[28:31], v[176:179], v[12:15], v[28:31]\n"
        "v_mfma_f32_16x16x32_bf16 v[32:35], v[180:183], v[12:15], v[32:35]\n"
        "s_waitcnt lgkmcnt(0)\n"
        "v_mfma_f32_16x16x32_bf16 v[24:27], v[184:187], v[16:19], v[24:27]\n"
        "v_mfma_f32_16x16x32_bf16 v[28:31], v[188:191], v[16:19], v[28:31]\n"
        "v_mfma_f32_16x16x32_bf16 v[32:35], v[192:195], v[16:19], v[32:35]\n"
        "v_mfma_f32_16x16x32_bf16 v[24:27], v[196:199], v[20:23], v[24:27]\n"
        "v_mfma_f32_16x16x32_bf16 v[28:31], v[200:203], v[20:23], v[28:31]\n"
        "v_mfma_f32_16x16x32_bf16 v[32:35], v[204:207], v[20:23], v[32:35]\n"
        "s_nop 2\n"
        "v_cvt_pk_bf16_f32 v24, v24, v25\n"
        "v_cvt_pk_bf16_f32 v25, v26, v27\n"
        "v_cvt_pk_bf16_f32 v26, v28, v29\n"
        "v_cvt_pk_bf16_f32 v27, v30, v31\n"
        "buffer_store_dwordx4 v[24:27], v48, s[40:43], s72 offen offset:0 sc1\n"
        "v_cvt_pk_bf16_f32 v32, v32, v33\n"
        "v_cvt_pk_bf16_f32 v33, v34, v35\n"
        "s_nop 0\n"
        "v_permlane16_swap_b32 v32, v34\n"
        "v_permlane16_swap_b32 v33, v35\n"
        "buffer_store_dwordx4 v[32:35], v7, s[40:43], s72 offen offset:0 sc1\n"
        "s_waitcnt vmcnt(2)\n"
        "v_cmp_gt_i32 vcc, s70, v4\n"
        "s_nop 0\n"
        "s_and_b64 vcc, vcc, 3\n"
        "s_cbranch_vccnz .Lop_late0_%=\n"
        "buffer_load_dwordx4 v1, s[40:43], s71 offen offset:0 sc1 lds\n"
        "buffer_load_dwordx4 v1, s[40:43], s71 offen offset:1024 sc1 lds\n"
        "buffer_load_dwordx4 v1, s[40:43], s71 offen offset:2048 sc1 lds\n"
        "buffer_load_dwordx4 v1, s[40:43], s71 offen offset:3072 sc1 lds\n"
        "s_add_u32 m0, m0, 16384\n"
        "s_add_u32 s71, s71, 65536\n"
        "buffer_load_dwordx4 v1, s[40:43], s71 offen offset:0 sc1 lds\n"
        "buffer_load_dwordx4 v1, s[40:43], s71 offen offset:1024 sc1 lds\n"
        "buffer_load_dwordx4 v1, s[40:43], s71 offen offset:2048 sc1 lds\n"
        "buffer_load_dwordx4 v1, s[40:43], s71 offen offset:3072 sc1 lds\n"
        "s_add_u32 s69, s69, 1\n"
        "s_waitcnt vmcnt(8)\n"
        "v_mov_b32 v5, s74\n"
        "buffer_store_dword v5, v3, s[40:43], s73 offen sc1\n"
        "s_waitcnt vmcnt(5)\n"
        "s_branch .Lop_go0_%=\n"
        ".p2align 5\n"
        ".Lop_late0_%=:\n"
        "s_waitcnt vmcnt(0)\n"
        "v_mov_b32 v5, s74\n"
        "buffer_store_dword v5, v3, s[40:43], s73 offen sc1\n"
        "s_mov_b32 s64, 0\n"
        ".Lop_in0_top_%=:\n"
        "buffer_load_dword v4, v2, s[40:43], s86 offen sc1\n"
        "s_waitcnt vmcnt(0)\n"
        "v_cmp_gt_i32 vcc, s70, v4\n"
        "s_nop 0\n"
        "s_and_b64 vcc, vcc, 3\n"
        "s_cbranch_vccz .Lop_in0_ok_%=\n"
        "s_add_u32 s64, s64, 1\n"
        "s_and_b32 s65, s64, 255\n"
        "s_nop 0\n"
        "s_cbranch_scc1 .Lop_in0_go_%=\n"
        "s_mov_b32 s65, 49152\n"
        "s_cmp_lt_u32 s64, 1048576\n"
        "s_nop 0\n"
        "s_nop 0\n"
        "s_nop 0\n"
        "s_cbranch_scc1 .Lop_in0_chk_%=\n"
        "v_mov_b32 v4, 1\n"
        "buffer_store_dword v4, off, s[40:43], s65 sc1\n"
        "s_waitcnt vmcnt(0)\n"
        ".Lop_in0_chk_%=:\n"
        "buffer_load_dword v4, off, s[40:43], s65 sc1\n"
        "s_waitcnt vmcnt(0)\n"
        "v_readfirstlane_b32 s65, v4\n"
        "s_nop 0\n"
        "s_cmp_lg_u32 s65, 1\n"
        "s_cbranch_scc1 .Lop_in0_go_%=\n"
        "s_endpgm\n"
        ".p2align 5\n"
        ".Lop_in0_go_%=:\n"
        "s_branch .Lop_in0_top_%=\n"
        ".p2align 5\n"
        ".Lop_in0_ok_%=:\n"
        "buffer_load_dwordx4 v1, s[40:43], s71 offen offset:0 sc1 lds\n"
        "buffer_load_dwordx4 v1, s[40:43], s71 offen offset:1024 sc1 lds\n"
        "buffer_load_dwordx4 v1, s[40:43], s71 offen offset:2048 sc1 lds\n"
        "buffer_load_dwordx4 v1, s[40:43], s71 offen offset:3072 sc1 lds\n"
        "s_add_u32 m0, m0, 16384\n"
        "s_add_u32 s71, s71, 65536\n"
        "buffer_load_dwordx4 v1, s[40:43], s71 offen offset:0 sc1 lds\n"
        "buffer_load_dwordx4 v1, s[40:43], s71 offen offset:1024 sc1 lds\n"
        "buffer_load_dwordx4 v1, s[40:43], s71 offen offset:2048 sc1 lds\n"
        "buffer_load_dwordx4 v1, s[40:43], s71 offen offset:3072 sc1 lds\n"
        "s_add_u32 s69, s69, 1\n"
        "s_waitcnt vmcnt(4)\n"
        ".Lop_go0_%=:\n"
        "s_mov_b32 s72, 241172480\n"
        "s_mov_b32 s83, 241172480\n"
        "s_mov_b32 s73, 241172480\n"
        "v_add_u32 v0, s78, v6\n"
        "s_barrier\n"
        "ds_read_b128 v[8:11], v0 offset:0\n"
        "ds_read_b128 v[12:15], v0 offset:1024\n"
        "ds_read_b128 v[16:19], v0 offset:2048\n"
        "ds_read_b128 v[20:23], v0 offset:3072\n"
        "s_waitcnt lgkmcnt(2)\n"
        "s_branch .Lop_i1_%=\n"
        ".p2align 5\n"
        ".Lop_i1_%=:\n"
        "v_mfma_f32_16x16x32_bf16 v[36:39], a[0:3], v[8:11], 0\n"
        "buffer_load_dword v4, v2, s[40:43], s86 offen sc1\n"
        "v_mfma_f32_16x16x32_bf16 v[40:43], a[4:7], v[8:11], 0\n"
        "s_lshr_b32 s67, s69, 1\n"
        "s_bitcmp1_b64 s[52:53], s67\n"
        "s_cselect_b32 s65, 8192, 0\n"
        "v_mfma_f32_16x16x32_bf16 v[44:47], a[8:11], v[8:11], 0\n"
        "s_lshl_b32 s77, s67, 21\n"
        "ds_read_b128 v[8:11], v0 offset:4096\n"
        "s_add_u32 s77, s77, s65\n"
        "v_mfma_f32_16x16x32_bf16 v[36:39], a[12:15], v[12:15], v[36:39]\n"
        "s_add_u32 s71, s77, s58\n"
        "s_add_u32 s78, s78, 32768\n"
        "v_cvt_pk_bf16_f32 v24, v24, v25\n"
        "v_mfma_f32_16x16x32_bf16 v[40:43], a[16:19], v[12:15], v[40:43]\n"
        "s_cmp_eq_u32 s78, 98304\n"
        "s_cselect_b32 s78, 0, s78\n"
        "s_add_u32 m0, s78, s61\n"
        "v_mfma_f32_16x16x32_bf16 v[44:47], a[20:23], v[12:15], v[44:47]\n"
        "s_waitcnt lgkmcnt(1)\n"
        "ds_read_b128 v[12:15], v0 offset:5120\n"
        "s_lshr_b32 s70, s69, 0\n"
        "v_mfma_f32_16x16x32_bf16 v[36:39], a[24:27], v[16:19], v[36:39]\n"
        "v_cvt_pk_bf16_f32 v25, v26, v27\n"
        "v_cvt_pk_bf16_f32 v26, v28, v29\n"
        "v_mfma_f32_16x16x32_bf16 v[40:43], a[28:31], v[16:19], v[40:43]\n"
        "v_cvt_pk_bf16_f32 v27, v30, v31\n"
        "v_mfma_f32_16x16x32_bf16 v[44:47], a[32:35], v[16:19], v[44:47]\n"
        "ds_read_b128 v[16:19], v0 offset:6144\n"
        "v_mfma_f32_16x16x32_bf16 v[36:39], a[36:39], v[20:23], v[36:39]\n"
        "buffer_store_dwordx4 v[24:27], v48, s[40:43], s72 offen offset:0 sc1\n"
        "v_mfma_f32_16x16x32_bf16 v[40:43], a[40:43], v[20:23], v[40:43]\n"
        "v_cvt_pk_bf16_f32 v32, v32, v33\n"
        "v_cvt_pk_bf16_f32 v33, v34, v35\n"
        "v_mfma_f32_16x16x32_bf16 v[44:47], a[44:47], v[20:23], v[44:47]\n"
        "s_waitcnt lgkmcnt(1)\n"
        "ds_read_b128 v[20:23], v0 offset:7168\n"
        "s_nop 0\n"
        "v_mfma_f32_16x16x32_bf16 v[36:39], a[48:51], v[8:11], v[36:39]\n"
        "v_permlane16_swap_b32 v32, v34\n"
        "v_mfma_f32_16x16x32_bf16 v[40:43], a[52:55], v[8:11], v[40:43]\n"
        "v_permlane16_swap_b32 v33, v35\n"
        "v_mfma_f32_16x16x32_bf16 v[44:47], a[56:59], v[8:11], v[44:47]\n"
        "ds_read_b128 v[8:11], v0 offset:8192\n"
        "v_mfma_f32_16x16x32_bf16 v[36:39], a[60:63], v[12:15], v[36:39]\n"
        "v_mfma_f32_16x16x32_bf16 v[40:43], a[64:67], v[12:15], v[40:43]\n"
        "buffer_store_dwordx4 v[32:35], v7, s[40:43], s72 offen offset:0 sc1\n"
        "v_mfma_f32_16x16x32_bf16 v[44:47], a[68:71], v[12:15], v[44:47]\n"
        "s_waitcnt lgkmcnt(1)\n"
        "ds_read_b128 v[12:15], v0 offset:9216\n"
        "v_mfma_f32_16x16x32_bf16 v[36:39], a[72:75], v[16:19], v[36:39]\n"
        "s_nop 0\n"
        "v_mfma_f32_16x16x32_bf16 v[40:43], a[76:79], v[16:19], v[40:43]\n"
        "s_waitcnt vmcnt(2)\n"
        "v_cmp_gt_i32_e64 vcc, s70, v4\n"
        "v_mfma_f32_16x16x32_bf16 v[44:47], a[80:83], v[16:19], v[44:47]\n"
        "s_nop 0\n"
        "ds_read_b128 v[16:19], v0 offset:10240\n"
        "v_mfma_f32_16x16x32_bf16 v[36:39], a[84:87], v[20:23], v[36:39]\n"
        "s_and_b64 vcc, vcc, 3\n"
        "v_mfma_f32_16x16x32_bf16 v[40:43], a[88:91], v[20:23], v[40:43]\n"
        "s_cbranch_vccnz .Lop_i10_%=\n"
        "v_mfma_f32_16x16x32_bf16 v[44:47], a[92:95], v[20:23], v[44:47]\n"
        "s_waitcnt lgkmcnt(1)\n"
        "ds_read_b128 v[20:23], v0 offset:11264\n"
        "v_mfma_f32_16x16x32_bf16 v[36:39], a[96:99], v[8:11], v[36:39]\n"
        "buffer_load_dwordx4 v1, s[40:43], s71 offen offset:0 sc1 lds\n"
        "v_mfma_f32_16x16x32_bf16 v[40:43], a[100:103], v[8:11], v[40:43]\n"
        "v_mfma_f32_16x16x32_bf16 v[44:47], a[104:107], v[8:11], v[44:47]\n"
        "ds_read_b128 v[8:11], v0 offset:12288\n"
        "v_mfma_f32_16x16x32_bf16 v[36:39], a[108:111], v[12:15], v[36:39]\n"
        "buffer_load_dwordx4 v1, s[40:43], s71 offen offset:1024 sc1 lds\n"
        "v_mfma_f32_16x16x32_bf16 v[40:43], a[112:115], v[12:15], v[40:43]\n"
        "v_mfma_f32_16x16x32_bf16 v[44:47], a[116:119], v[12:15], v[44:47]\n"
        "s_waitcnt lgkmcnt(1)\n"
        "ds_read_b128 v[12:15], v0 offset:13312\n"
        "v_mfma_f32_16x16x32_bf16 v[36:39], a[120:123], v[16:19], v[36:39]\n"
        "v_mfma_f32_16x16x32_bf16 v[40:43], a[124:127], v[16:19], v[40:43]\n"
        "buffer_load_dwordx4 v1, s[40:43], s71 offen offset:2048 sc1 lds\n"
        "v_mfma_f32_16x16x32_bf16 v[44:47], a[128:131], v[16:19], v[44:47]\n"
        "ds_read_b128 v[16:19], v0 offset:14336\n"
        "v_mfma_f32_16x16x32_bf16 v[36:39], a[132:135], v[20:23], v[36:39]\n"
        "v_mfma_f32_16x16x32_bf16 v[40:43], a[136:139], v[20:23], v[40:43]\n"
        "v_mfma_f32_16x16x32_bf16 v[44:47], a[140:143], v[20:23], v[44:47]\n"
        "s_waitcnt lgkmcnt(1)\n"
        "ds_read_b128 v[20:23], v0 offset:15360\n"
        "v_mfma_f32_16x16x32_bf16 v[36:39], a[144:147], v[8:11], v[36:39]\n"
        "buffer_load_dwordx4 v1, s[40:43], s71 offen offset:3072 sc1 lds\n"
        "v_mfma_f32_16x16x32_bf16 v[40:43], a[148:151], v[8:11], v[40:43]\n"
        "s_barrier\n"
        "s_add_u32 m0, m0, 16384\n"
        "v_mfma_f32_16x16x32_bf16 v[44:47], a[152:155], v[8:11], v[44:47]\n"
        "s_add_u32 s71, s71, 65536\n"
        "ds_read_b128 v[8:11], v0 offset:16384\n"
        "v_mfma_f32_16x16x32_bf16 v[36:39], a[156:159], v[12:15], v[36:39]\n"
        "buffer_load_dwordx4 v1, s[40:43], s71 offen offset:0 sc1 lds\n"
        "v_mfma_f32_16x16x32_bf16 v[40:43], a[160:163], v[12:15], v[40:43]\n"
        "buffer_store_dword v5, v3, s[40:43], s84 offen sc1\n"
        "v_mfma_f32_16x16x32_bf16 v[44:47], a[164:167], v[12:15], v[44:47]\n"
        "s_waitcnt lgkmcnt(1)\n"
        "ds_read_b128 v[12:15], v0 offset:17408\n"
        "v_mov_b32 v5, s74\n"
        "v_mfma_f32_16x16x32_bf16 v[36:39], a[168:171], v[16:19], v[36:39]\n"
        "s_getreg_b32 s88, hwreg(HW_REG_IB_STS, 0, 4)\n"
        "v_mfma_f32_16x16x32_bf16 v[40:43], a[172:175], v[16:19], v[40:43]\n"
        "buffer_load_dwordx4 v1, s[40:43], s71 offen offset:1024 sc1 lds\n"
        "v_mfma_f32_16x16x32_bf16 v[44:47], a[176:179], v[16:19], v[44:47]\n"
        "ds_read_b128 v[16:19], v0 offset:18432\n"
        "v_mfma_f32_16x16x32_bf16 v[36:39], a[180:183], v[20:23], v[36:39]\n"
        "s_cmp_le_u32 s88, 6\n"
        "s_cselect_b32 s88, s73, s89\n"
        "s_cselect_b32 s73, s89, s73\n"
        "v_mfma_f32_16x16x32_bf16 v[40:43], a[184:187], v[20:23], v[40:43]\n"
        "buffer_store_dword v5, v3, s[40:43], s88 offen sc1\n"
        "v_mfma_f32_16x16x32_bf16 v[44:47], a[188:191], v[20:23], v[44:47]\n"
        "s_waitcnt lgkmcnt(1)\n"
        "ds_read_b128 v[20:23], v0 offset:19456\n"
        "v_mfma_f32_16x16x32_bf16 v[36:39], a[192:195], v[8:11], v[36:39]\n"
        "s_getreg_b32 s88, hwreg(HW_REG_IB_STS, 0, 4)\n"
        "buffer_load_dwordx4 v1, s[40:43], s71 offen offset:2048 sc1 lds\n"
        "v_mfma_f32_16x16x32_bf16 v[40:43], a[196:199], v[8:11], v[40:43]\n"
        "s_cmp_le_u32 s88, 8\n"
        "s_cselect_b32 s88, s73, s89\n"
        "s_cselect_b32 s73, s89, s73\n"
        "v_mfma_f32_16x16x32_bf16 v[44:47], a[200:203], v[8:11], v[44:47]\n"
        "ds_read_b128 v[8:11], v0 offset:20480\n"
        "v_mfma_f32_16x16x32_bf16 v[36:39], a[204:207], v[12:15], v[36:39]\n"
        "buffer_load_dwordx4 v1, s[40:43], s71 offen offset:3072 sc1 lds\n"
        "v_mfma_f32_16x16x32_bf16 v[40:43], a[208:211], v[12:15], v[40:43]\n"
        "s_add_u32 s69, s69, 1\n"
        "buffer_store_dword v5, v3, s[40:43], s88 offen sc1\n"
        "v_mfma_f32_16x16x32_bf16 v[44:47], a[212:215], v[12:15], v[44:47]\n"
        "s_waitcnt lgkmcnt(1)\n"
        "ds_read_b128 v[12:15], v0 offset:21504\n"
        "v_mfma_f32_16x16x32_bf16 v[36:39], a[216:219], v[16:19], v[36:39]\n"
        "v_mfma_f32_16x16x32_bf16 v[40:43], a[220:223], v[16:19], v[40:43]\n"
        "v_mfma_f32_16x16x32_bf16 v[44:47], a[224:227], v[16:19], v[44:47]\n"
        "s_getreg_b32 s88, hwreg(HW_REG_IB_STS, 0, 4)\n"
        "ds_read_b128 v[16:19], v0 offset:22528\n"
        "v_mfma_f32_16x16x32_bf16 v[36:39], a[228:231], v[20:23], v[36:39]\n"
        "s_cmp_le_u32 s88, 11\n"
        "s_cselect_b32 s88, s73, s89\n"
        "s_cselect_b32 s73, s89, s73\n"
        "v_mfma_f32_16x16x32_bf16 v[40:43], a[232:235], v[20:23], v[40:43]\n"
        "buffer_store_dword v5, v3, s[40:43], s88 offen sc1\n"
        "v_mfma_f32_16x16x32_bf16 v[44:47], a[236:239], v[20:23], v[44:47]\n"
        "s_waitcnt lgkmcnt(1)\n"
        "ds_read_b128 v[20:23], v0 offset:23552\n"
        "v_mfma_f32_16x16x32_bf16 v[36:39], a[240:243], v[8:11], v[36:39]\n"
        "v_mfma_f32_16x16x32_bf16 v[40:43], a[244:247], v[8:11], v[40:43]\n"
        "v_mfma_f32_16x16x32_bf16 v[44:47], a[248:251], v[8:11], v[44:47]\n"
        "ds_read_b128 v[8:11], v0 offset:24576\n"
        "v_mfma_f32_16x16x32_bf16 v[36:39], a[252:255], v[12:15], v[36:39]\n"
        "v_mfma_f32_16x16x32_bf16 v[40:43], v[80:83], v[12:15], v[40:43]\n"
        "v_mfma_f32_16x16x32_bf16 v[44:47], v[84:87], v[12:15], v[44:47]\n"
        "s_waitcnt lgkmcnt(1)\n"
        "ds_read_b128 v[12:15], v0 offset:25600\n"
        "v_mfma_f32_16x16x32_bf16 v[36:39], v[88:91], v[16:19], v[36:39]\n"
        "v_mfma_f32_16x16x32_bf16 v[40:43], v[92:95], v[16:19], v[40:43]\n"
        "v_mfma_f32_16x16x32_bf16 v[44:47], v[96:99], v[16:19], v[44:47]\n"
        "ds_read_b128 v[16:19], v0 offset:26624\n"
        "v_mfma_f32_16x16x32_bf16 v[36:39], v[100:103], v[20:23], v[36:39]\n"
        "v_mfma_f32_16x16x32_bf16 v[40:43], v[104:107], v[20:23], v[40:43]\n"
        "s_waitcnt vmcnt(12)\n"
        "buffer_store_dword v5, v3, s[40:43], s73 offen sc1\n"
        "v_mfma_f32_16x16x32_bf16 v[44:47], v[108:111], v[20:23], v[44:47]\n"
        "s_waitcnt lgkmcnt(1)\n"
        "ds_read_b128 v[20:23], v0 offset:27648\n"
        "s_mov_b32 s84, s89\n"
        "v_mfma_f32_16x16x32_bf16 v[36:39], v[112:115], v[8:11], v[36:39]\n"
        "s_mov_b32 s73, s60\n"
        "s_add_u32 s74, s74, 1\n"
        "s_add_u32 s72, s75, s59\n"
        "v_mfma_f32_16x16x32_bf16 v[40:43], v[116:119], v[8:11], v[40:43]\n"
        "s_add_u32 s72, s72, 147456\n"
        "v_mfma_f32_16x16x32_bf16 v[44:47], v[120:123], v[8:11], v[44:47]\n"
        "ds_read_b128 v[8:11], v0 offset:28672\n"
        "v_mfma_f32_16x16x32_bf16 v[36:39], v[124:127], v[12:15], v[36:39]\n"
        "v_mfma_f32_16x16x32_bf16 v[40:43], v[128:131], v[12:15], v[40:43]\n"
        "v_mfma_f32_16x16x32_bf16 v[44:47], v[132:135], v[12:15], v[44:47]\n"
        "s_waitcnt lgkmcnt(1)\n"
        "ds_read_b128 v[12:15], v0 offset:29696\n"
        "v_mfma_f32_16x16x32_bf16 v[36:39], v[136:139], v[16:19], v[36:39]\n"
        "v_mfma_f32_16x16x32_bf16 v[40:43], v[140:143], v[16:19], v[40:43]\n"
        "v_mfma_f32_16x16x32_bf16 v[44:47], v[144:147], v[16:19], v[44:47]\n"
        "ds_read_b128 v[16:19], v0 offset:30720\n"
        "v_mfma_f32_16x16x32_bf16 v[36:39], v[148:151], v[20:23], v[36:39]\n"
        "v_mfma_f32_16x16x32_bf16 v[40:43], v[152:155], v[20:23], v[40:43]\n"
        "v_mfma_f32_16x16x32_bf16 v[44:47], v[156:159], v[20:23], v[44:47]\n"
        "s_waitcnt lgkmcnt(1)\n"
        "ds_read_b128 v[20:23], v0 offset:31744\n"
        "v_mfma_f32_16x16x32_bf16 v[36:39], v[160:163], v[8:11], v[36:39]\n"
        "v_mfma_f32_16x16x32_bf16 v[40:43], v[164:167], v[8:11], v[40:43]\n"
        "s_waitcnt vmcnt(9)\n"
        "s_barrier\n"
        "v_add_u32 v0, s78, v6\n"
        "v_mfma_f32_16x16x32_bf16 v[44:47], v[168:171], v[8:11], v[44:47]\n"
        "ds_read_b128 v[8:11], v0 offset:0\n"
        "v_mfma_f32_16x16x32_bf16 v[36:39], v[172:175], v[12:15], v[36:39]\n"
        "v_mfma_f32_16x16x32_bf16 v[40:43], v[176:179], v[12:15], v[40:43]\n"
        "v_mfma_f32_16x16x32_bf16 v[44:47], v[180:183], v[12:15], v[44:47]\n"
        "s_waitcnt lgkmcnt(1)\n"
        "ds_read_b128 v[12:15], v0 offset:1024\n"
        "v_mfma_f32_16x16x32_bf16 v[36:39], v[184:187], v[16:19], v[36:39]\n"
        "v_mfma_f32_16x16x32_bf16 v[40:43], v[188:191], v[16:19], v[40:43]\n"
        "v_mfma_f32_16x16x32_bf16 v[44:47], v[192:195], v[16:19], v[44:47]\n"
        "ds_read_b128 v[16:19], v0 offset:2048\n"
        "v_mfma_f32_16x16x32_bf16 v[36:39], v[196:199], v[20:23], v[36:39]\n"
        "v_mfma_f32_16x16x32_bf16 v[40:43], v[200:203], v[20:23], v[40:43]\n"
        "v_mfma_f32_16x16x32_bf16 v[44:47], v[204:207], v[20:23], v[44:47]\n"
        "s_waitcnt lgkmcnt(1)\n"
        "ds_read_b128 v[20:23], v0 offset:3072\n"
        "s_branch .Lop_i0_%=\n"
        ".p2align 5\n"
        ".Lop_i10_%=:\n"
        "v_mfma_f32_16x16x32_bf16 v[44:47], a[92:95], v[20:23], v[44:47]\n"
        "s_waitcnt lgkmcnt(1)\n"
        "ds_read_b128 v[20:23], v0 offset:11264\n"
        "v_mfma_f32_16x16x32_bf16 v[36:39], a[96:99], v[8:11], v[36:39]\n"
        "buffer_load_dword v4, v2, s[40:43], s86 offen sc1\n"
        "v_mfma_f32_16x16x32_bf16 v[40:43], a[100:103], v[8:11], v[40:43]\n"
        "v_mfma_f32_16x16x32_bf16 v[44:47], a[104:107], v[8:11], v[44:47]\n"
        "ds_read_b128 v[8:11], v0 offset:12288\n"
        "v_mfma_f32_16x16x32_bf16 v[36:39], a[108:111], v[12:15], v[36:39]\n"
        "v_mfma_f32_16x16x32_bf16 v[40:43], a[112:115], v[12:15], v[40:43]\n"
        "v_mfma_f32_16x16x32_bf16 v[44:47], a[116:119], v[12:15], v[44:47]\n"
        "s_waitcnt lgkmcnt(1)\n"
        "ds_read_b128 v[12:15], v0 offset:13312\n"
        "v_mfma_f32_16x16x32_bf16 v[36:39], a[120:123], v[16:19], v[36:39]\n"
        "v_mfma_f32_16x16x32_bf16 v[40:43], a[124:127], v[16:19], v[40:43]\n"
        "v_mfma_f32_16x16x32_bf16 v[44:47], a[128:131], v[16:19], v[44:47]\n"
        "ds_read_b128 v[16:19], v0 offset:14336\n"
        "v_mfma_f32_16x16x32_bf16 v[36:39], a[132:135], v[20:23], v[36:39]\n"
        "v_mfma_f32_16x16x32_bf16 v[40:43], a[136:139], v[20:23], v[40:43]\n"
        "v_mfma_f32_16x16x32_bf16 v[44:47], a[140:143], v[20:23], v[44:47]\n"
        "s_waitcnt lgkmcnt(1)\n"
        "ds_read_b128 v[20:23], v0 offset:15360\n"
        "v_mfma_f32_16x16x32_bf16 v[36:39], a[144:147], v[8:11], v[36:39]\n"
        "v_mfma_f32_16x16x32_bf16 v[40:43], a[148:151], v[8:11], v[40:43]\n"
        "s_barrier\n"
        "v_mfma_f32_16x16x32_bf16 v[44:47], a[152:155], v[8:11], v[44:47]\n"
        "ds_read_b128 v[8:11], v0 offset:16384\n"
        "v_mfma_f32_16x16x32_bf16 v[36:39], a[156:159], v[12:15], v[36:39]\n"
        "buffer_store_dword v5, v3, s[40:43], s84 offen sc1\n"
        "v_mfma_f32_16x16x32_bf16 v[40:43], a[160:163], v[12:15], v[40:43]\n"
        "v_mov_b32 v5, s74\n"
        "v_mfma_f32_16x16x32_bf16 v[44:47], a[164:167], v[12:15], v[44:47]\n"
        "s_waitcnt lgkmcnt(1)\n"
        "ds_read_b128 v[12:15], v0 offset:17408\n"
        "v_mfma_f32_16x16x32_bf16 v[36:39], a[168:171], v[16:19], v[36:39]\n"
        "s_getreg_b32 s88, hwreg(HW_REG_IB_STS, 0, 4)\n"
        "v_mfma_f32_16x16x32_bf16 v[40:43], a[172:175], v[16:19], v[40:43]\n"
        "s_cmp_le_u32 s88, 2\n"
        "s_cselect_b32 s88, s73, s89\n"
        "s_cselect_b32 s73, s89, s73\n"
        "v_mfma_f32_16x16x32_bf16 v[44:47], a[176:179], v[16:19], v[44:47]\n"
        "ds_read_b128 v[16:19], v0 offset:18432\n"
        "v_mfma_f32_16x16x32_bf16 v[36:39], a[180:183], v[20:23], v[36:39]\n"
        "buffer_store_dword v5, v3, s[40:43], s88 offen sc1\n"
        "v_mfma_f32_16x16x32_bf16 v[40:43], a[184:187], v[20:23], v[40:43]\n"
        "s_nop 0\n"
        "v_mfma_f32_16x16x32_bf16 v[44:47], a[188:191], v[20:23], v[44:47]\n"
        "s_waitcnt lgkmcnt(1)\n"
        "ds_read_b128 v[20:23], v0 offset:19456\n"
        "v_mfma_f32_16x16x32_bf16 v[36:39], a[192:195], v[8:11], v[36:39]\n"
        "s_getreg_b32 s88, hwreg(HW_REG_IB_STS, 0, 4)\n"
        "v_mfma_f32_16x16x32_bf16 v[40:43], a[196:199], v[8:11], v[40:43]\n"
        "s_waitcnt vmcnt(2)\n"
        "v_cmp_gt_i32_e64 vcc, s70, v4\n"
        "v_mfma_f32_16x16x32_bf16 v[44:47], a[200:203], v[8:11], v[44:47]\n"
        "s_nop 0\n"
        "ds_read_b128 v[8:11], v0 offset:20480\n"
        "v_mfma_f32_16x16x32_bf16 v[36:39], a[204:207], v[12:15], v[36:39]\n"
        "s_and_b64 vcc, vcc, 3\n"
        "v_mfma_f32_16x16x32_bf16 v[40:43], a[208:211], v[12:15], v[40:43]\n"
        "s_cbranch_vccnz .Lop_i11_%=\n"
        "v_mfma_f32_16x16x32_bf16 v[44:47], a[212:215], v[12:15], v[44:47]\n"
        "s_waitcnt lgkmcnt(1)\n"
        "ds_read_b128 v[12:15], v0 offset:21504\n"
        "v_mfma_f32_16x16x32_bf16 v[36:39], a[216:219], v[16:19], v[36:39]\n"
        "buffer_load_dwordx4 v1, s[40:43], s71 offen offset:0 sc1 lds\n"
        "v_mfma_f32_16x16x32_bf16 v[40:43], a[220:223], v[16:19], v[40:43]\n"
        "s_cmp_le_u32 s88, 3\n"
        "s_cselect_b32 s88, s73, s89\n"
        "s_cselect_b32 s73, s89, s73\n"
        "v_mfma_f32_16x16x32_bf16 v[44:47], a[224:227], v[16:19], v[44:47]\n"
        "ds_read_b128 v[16:19], v0 offset:22528\n"
        "v_mfma_f32_16x16x32_bf16 v[36:39], a[228:231], v[20:23], v[36:39]\n"
        "buffer_load_dwordx4 v1, s[40:43], s71 offen offset:1024 sc1 lds\n"
        "v_mfma_f32_16x16x32_bf16 v[40:43], a[232:235], v[20:23], v[40:43]\n"
        "buffer_store_dword v5, v3, s[40:43], s88 offen sc1\n"
        "v_mfma_f32_16x16x32_bf16 v[44:47], a[236:239], v[20:23], v[44:47]\n"
        "s_waitcnt lgkmcnt(1)\n"
        "ds_read_b128 v[20:23], v0 offset:23552\n"
        "v_mfma_f32_16x16x32_bf16 v[36:39], a[240:243], v[8:11], v[36:39]\n"
        "s_getreg_b32 s88, hwreg(HW_REG_IB_STS, 0, 4)\n"
        "buffer_load_dwordx4 v1, s[40:43], s71 offen offset:2048 sc1 lds\n"
        "v_mfma_f32_16x16x32_bf16 v[40:43], a[244:247], v[8:11], v[40:43]\n"
        "s_cmp_le_u32 s88, 15\n"
        "s_cselect_b32 s88, s73, s89\n"
        "s_cselect_b32 s73, s89, s73\n"
        "v_mfma_f32_16x16x32_bf16 v[44:47], a[248:251], v[8:11], v[44:47]\n"
        "ds_read_b128 v[8:11], v0 offset:24576\n"
        "v_mfma_f32_16x16x32_bf16 v[36:39], a[252:255], v[12:15], v[36:39]\n"
        "buffer_load_dwordx4 v1, s[40:43], s71 offen offset:3072 sc1 lds\n"
        "v_mfma_f32_16x16x32_bf16 v[40:43], v[80:83], v[12:15], v[40:43]\n"
        "s_add_u32 m0, m0, 16384\n"
        "s_add_u32 s71, s71, 65536\n"
        "v_mfma_f32_16x16x32_bf16 v[44:47], v[84:87], v[12:15], v[44:47]\n"
        "s_waitcnt lgkmcnt(1)\n"
        "ds_read_b128 v[12:15], v0 offset:25600\n"
        "v_mfma_f32_16x16x32_bf16 v[36:39], v[88:91], v[16:19], v[36:39]\n"
        "buffer_load_dwordx4 v1, s[40:43], s71 offen offset:0 sc1 lds\n"
        "v_mfma_f32_16x16x32_bf16 v[40:43], v[92:95], v[16:19], v[40:43]\n"
        "buffer_store_dword v5, v3, s[40:43], s88 offen sc1\n"
        "v_mfma_f32_16x16x32_bf16 v[44:47], v[96:99], v[16:19], v[44:47]\n"
        "ds_read_b128 v[16:19], v0 offset:26624\n"
        "v_mfma_f32_16x16x32_bf16 v[36:39], v[100:103], v[20:23], v[36:39]\n"
        "v_mfma_f32_16x16x32_bf16 v[40:43], v[104:107], v[20:23], v[40:43]\n"
        "buffer_store_dword v5, v3, s[40:43], s73 offen sc1\n"
        "v_mfma_f32_16x16x32_bf16 v[44:47], v[108:111], v[20:23], v[44:47]\n"
        "s_waitcnt lgkmcnt(1)\n"
        "ds_read_b128 v[20:23], v0 offset:27648\n"
        "s_mov_b32 s84, s89\n"
        "v_mfma_f32_16x16x32_bf16 v[36:39], v[112:115], v[8:11], v[36:39]\n"
        "s_mov_b32 s73, s60\n"
        "s_add_u32 s74, s74, 1\n"
        "s_add_u32 s72, s75, s59\n"
        "v_mfma_f32_16x16x32_bf16 v[40:43], v[116:119], v[8:11], v[40:43]\n"
        "s_add_u32 s72, s72, 147456\n"
        "buffer_load_dwordx4 v1, s[40:43], s71 offen offset:1024 sc1 lds\n"
        "v_mfma_f32_16x16x32_bf16 v[44:47], v[120:123], v[8:11], v[44:47]\n"
        "ds_read_b128 v[8:11], v0 offset:28672\n"
        "v_mfma_f32_16x16x32_bf16 v[36:39], v[124:127], v[12:15], v[36:39]\n"
        "v_mfma_f32_16x16x32_bf16 v[40:43], v[128:131], v[12:15], v[40:43]\n"
        "v_mfma_f32_16x16x32_bf16 v[44:47], v[132:135], v[12:15], v[44:47]\n"
        "s_waitcnt lgkmcnt(1)\n"
        "ds_read_b128 v[12:15], v0 offset:29696\n"
        "v_mfma_f32_16x16x32_bf16 v[36:39], v[136:139], v[16:19], v[36:39]\n"
        "buffer_load_dwordx4 v1, s[40:43], s71 offen offset:2048 sc1 lds\n"
        "v_mfma_f32_16x16x32_bf16 v[40:43], v[140:143], v[16:19], v[40:43]\n"
        "v_mfma_f32_16x16x32_bf16 v[44:47], v[144:147], v[16:19], v[44:47]\n"
        "ds_read_b128 v[16:19], v0 offset:30720\n"
        "v_mfma_f32_16x16x32_bf16 v[36:39], v[148:151], v[20:23], v[36:39]\n"
        "buffer_load_dwordx4 v1, s[40:43], s71 offen offset:3072 sc1 lds\n"
        "v_mfma_f32_16x16x32_bf16 v[40:43], v[152:155], v[20:23], v[40:43]\n"
        "s_add_u32 s69, s69, 1\n"
        "v_mfma_f32_16x16x32_bf16 v[44:47], v[156:159], v[20:23], v[44:47]\n"
        "s_waitcnt lgkmcnt(1)\n"
        "ds_read_b128 v[20:23], v0 offset:31744\n"
        "v_mfma_f32_16x16x32_bf16 v[36:39], v[160:163], v[8:11], v[36:39]\n"
        "v_mfma_f32_16x16x32_bf16 v[40:43], v[164:167], v[8:11], v[40:43]\n"
        "s_waitcnt vmcnt(6)\n"
        "s_barrier\n"
        "v_add_u32 v0, s78, v6\n"
        "v_mfma_f32_16x16x32_bf16 v[44:47], v[168:171], v[8:11], v[44:47]\n"
        "ds_read_b128 v[8:11], v0 offset:0\n"
        "v_mfma_f32_16x16x32_bf16 v[36:39], v[172:175], v[12:15], v[36:39]\n"
        "v_mfma_f32_16x16x32_bf16 v[40:43], v[176:179], v[12:15], v[40:43]\n"
        "v_mfma_f32_16x16x32_bf16 v[44:47], v[180:183], v[12:15], v[44:47]\n"
        "s_waitcnt lgkmcnt(1)\n"
        "ds_read_b128 v[12:15], v0 offset:1024\n"
        "v_mfma_f32_16x16x32_bf16 v[36:39], v[184:187], v[16:19], v[36:39]\n"
        "v_mfma_f32_16x16x32_bf16 v[40:43], v[188:191], v[16:19], v[40:43]\n"
        "v_mfma_f32_16x16x32_bf16 v[44:47], v[192:195], v[16:19], v[44:47]\n"
        "ds_read_b128 v[16:19], v0 offset:2048\n"
        "v_mfma_f32_16x16x32_bf16 v[36:39], v[196:199], v[20:23], v[36:39]\n"
        "v_mfma_f32_16x16x32_bf16 v[40:43], v[200:203], v[20:23], v[40:43]\n"
        "v_mfma_f32_16x16x32_bf16 v[44:47], v[204:207], v[20:23], v[44:47]\n"
        "s_waitcnt lgkmcnt(1)\n"
        "ds_read_b128 v[20:23], v0 offset:3072\n"
        "s_branch .Lop_i0_%=\n"
        ".p2align 5\n"
        ".Lop_i11_%=:\n"
        "v_mfma_f32_16x16x32_bf16 v[44:47], a[212:215], v[12:15], v[44:47]\n"
        "s_waitcnt lgkmcnt(1)\n"
        "ds_read_b128 v[12:15], v0 offset:21504\n"
        "v_mfma_f32_16x16x32_bf16 v[36:39], a[216:219], v[16:19], v[36:39]\n"
        "buffer_load_dword v4, v2, s[40:43], s86 offen sc1\n"
        "v_mfma_f32_16x16x32_bf16 v[40:43], a[220:223], v[16:19], v[40:43]\n"
        "s_cmp_le_u32 s88, 3\n"
        "s_cselect_b32 s88, s73, s89\n"
        "s_cselect_b32 s73, s89, s73\n"
        "v_mfma_f32_16x16x32_bf16 v[44:47], a[224:227], v[16:19], v[44:47]\n"
        "ds_read_b128 v[16:19], v0 offset:22528\n"
        "v_mfma_f32_16x16x32_bf16 v[36:39], a[228:231], v[20:23], v[36:39]\n"
        "buffer_store_dword v5, v3, s[40:43], s88 offen sc1\n"
        "v_mfma_f32_16x16x32_bf16 v[40:43], a[232:235], v[20:23], v[40:43]\n"
        "s_getreg_b32 s88, hwreg(HW_REG_IB_STS, 0, 4)\n"
        "v_mfma_f32_16x16x32_bf16 v[44:47], a[236:239], v[20:23], v[44:47]\n"
        "s_waitcnt lgkmcnt(1)\n"
        "ds_read_b128 v[20:23], v0 offset:23552\n"
        "v_mfma_f32_16x16x32_bf16 v[36:39], a[240:243], v[8:11], v[36:39]\n"
        "s_cmp_le_u32 s88, 15\n"
        "s_cselect_b32 s88, s73, s89\n"
        "s_cselect_b32 s73, s89, s73\n"
        "v_mfma_f32_16x16x32_bf16 v[40:43], a[244:247], v[8:11], v[40:43]\n"
        "buffer_store_dword v5, v3, s[40:43], s88 offen sc1\n"
        "v_mfma_f32_16x16x32_bf16 v[44:47], a[248:251], v[8:11], v[44:47]\n"
        "ds_read_b128 v[8:11], v0 offset:24576\n"
        "v_mfma_f32_16x16x32_bf16 v[36:39], a[252:255], v[12:15], v[36:39]\n"
        "v_mfma_f32_16x16x32_bf16 v[40:43], v[80:83], v[12:15], v[40:43]\n"
        "v_mfma_f32_16x16x32_bf16 v[44:47], v[84:87], v[12:15], v[44:47]\n"
        "s_waitcnt lgkmcnt(1)\n"
        "ds_read_b128 v[12:15], v0 offset:25600\n"
        "v_mfma_f32_16x16x32_bf16 v[36:39], v[88:91], v[16:19], v[36:39]\n"
        "v_mfma_f32_16x16x32_bf16 v[40:43], v[92:95], v[16:19], v[40:43]\n"
        "v_mfma_f32_16x16x32_bf16 v[44:47], v[96:99], v[16:19], v[44:47]\n"
        "ds_read_b128 v[16:19], v0 offset:26624\n"
        "v_mfma_f32_16x16x32_bf16 v[36:39], v[100:103], v[20:23], v[36:39]\n"
        "v_mfma_f32_16x16x32_bf16 v[40:43], v[104:107], v[20:23], v[40:43]\n"
        "buffer_store_dword v5, v3, s[40:43], s73 offen sc1\n"
        "v_mfma_f32_16x16x32_bf16 v[44:47], v[108:111], v[20:23], v[44:47]\n"
        "s_waitcnt lgkmcnt(1)\n"
        "ds_read_b128 v[20:23], v0 offset:27648\n"
        "s_mov_b32 s84, s89\n"
        "v_mfma_f32_16x16x32_bf16 v[36:39], v[112:115], v[8:11], v[36:39]\n"
        "s_mov_b32 s73, s60\n"
        "s_add_u32 s74, s74, 1\n"
        "s_add_u32 s72, s75, s59\n"
        "v_mfma_f32_16x16x32_bf16 v[40:43], v[116:119], v[8:11], v[40:43]\n"
        "s_add_u32 s72, s72, 147456\n"
        "v_mfma_f32_16x16x32_bf16 v[44:47], v[120:123], v[8:11], v[44:47]\n"
        "ds_read_b128 v[8:11], v0 offset:28672\n"
        "v_mfma_f32_16x16x32_bf16 v[36:39], v[124:127], v[12:15], v[36:39]\n"
        "v_mfma_f32_16x16x32_bf16 v[40:43], v[128:131], v[12:15], v[40:43]\n"
        "v_mfma_f32_16x16x32_bf16 v[44:47], v[132:135], v[12:15], v[44:47]\n"
        "s_waitcnt lgkmcnt(1)\n"
        "ds_read_b128 v[12:15], v0 offset:29696\n"
        "v_mfma_f32_16x16x32_bf16 v[36:39], v[136:139], v[16:19], v[36:39]\n"
        "v_mfma_f32_16x16x32_bf16 v[40:43], v[140:143], v[16:19], v[40:43]\n"
        "v_mfma_f32_16x16x32_bf16 v[44:47], v[144:147], v[16:19], v[44:47]\n"
        "ds_read_b128 v[16:19], v0 offset:30720\n"
        "v_mfma_f32_16x16x32_bf16 v[36:39], v[148:151], v[20:23], v[36:39]\n"
        "s_waitcnt vmcnt(3)\n"
        "v_cmp_gt_i32 vcc, s70, v4\n"
        "v_mfma_f32_16x16x32_bf16 v[40:43], v[152:155], v[20:23], v[40:43]\n"
        "v_mfma_f32_16x16x32_bf16 v[44:47], v[156:159], v[20:23], v[44:47]\n"
        "s_waitcnt lgkmcnt(1)\n"
        "ds_read_b128 v[20:23], v0 offset:31744\n"
        "s_and_b64 vcc, vcc, 3\n"
        "v_mfma_f32_16x16x32_bf16 v[36:39], v[160:163], v[8:11], v[36:39]\n"
        "s_cbranch_vccnz .Lop_i12_%=\n"
        "v_mfma_f32_16x16x32_bf16 v[40:43], v[164:167], v[8:11], v[40:43]\n"
        "buffer_load_dwordx4 v1, s[40:43], s71 offen offset:0 sc1 lds\n"
        "v_mfma_f32_16x16x32_bf16 v[44:47], v[168:171], v[8:11], v[44:47]\n"
        "v_mfma_f32_16x16x32_bf16 v[36:39], v[172:175], v[12:15], v[36:39]\n"
        "buffer_load_dwordx4 v1, s[40:43], s71 offen offset:1024 sc1 lds\n"
        "v_mfma_f32_16x16x32_bf16 v[40:43], v[176:179], v[12:15], v[40:43]\n"
        "v_mfma_f32_16x16x32_bf16 v[44:47], v[180:183], v[12:15], v[44:47]\n"
        "s_waitcnt lgkmcnt(0)\n"
        "buffer_load_dwordx4 v1, s[40:43], s71 offen offset:2048 sc1 lds\n"
        "v_mfma_f32_16x16x32_bf16 v[36:39], v[184:187], v[16:19], v[36:39]\n"
        "v_mfma_f32_16x16x32_bf16 v[40:43], v[188:191], v[16:19], v[40:43]\n"
        "buffer_load_dwordx4 v1, s[40:43], s71 offen offset:3072 sc1 lds\n"
        "v_mfma_f32_16x16x32_bf16 v[44:47], v[192:195], v[16:19], v[44:47]\n"
        "s_add_u32 m0, m0, 16384\n"
        "s_add_u32 s71, s71, 65536\n"
        "v_mfma_f32_16x16x32_bf16 v[36:39], v[196:199], v[20:23], v[36:39]\n"
        "buffer_load_dwordx4 v1, s[40:43], s71 offen offset:0 sc1 lds\n"
        "v_mfma_f32_16x16x32_bf16 v[40:43], v[200:203], v[20:23], v[40:43]\n"
        "v_mfma_f32_16x16x32_bf16 v[44:47], v[204:207], v[20:23], v[44:47]\n"
        "buffer_load_dwordx4 v1, s[40:43], s71 offen offset:1024 sc1 lds\n"
        "buffer_load_dwordx4 v1, s[40:43], s71 offen offset:2048 sc1 lds\n"
        "buffer_load_dwordx4 v1, s[40:43], s71 offen offset:3072 sc1 lds\n"
        "s_add_u32 s69, s69, 1\n"
        "s_nop 2\n"
        "v_cvt_pk_bf16_f32 v36, v36, v37\n"
        "v_cvt_pk_bf16_f32 v37, v38, v39\n"
        "v_cvt_pk_bf16_f32 v38, v40, v41\n"
        "v_cvt_pk_bf16_f32 v39, v42, v43\n"
        "buffer_store_dwordx4 v[36:39], v48, s[40:43], s72 offen offset:0 sc1\n"
        "v_cvt_pk_bf16_f32 v44, v44, v45\n"
        "v_cvt_pk_bf16_f32 v45, v46, v47\n"
        "s_nop 0\n"
        "v_permlane16_swap_b32 v44, v46\n"
        "v_permlane16_swap_b32 v45, v47\n"
        "buffer_store_dwordx4 v[44:47], v7, s[40:43], s72 offen offset:0 sc1\n"
        "v_mov_b32 v5, s74\n"
        "s_mov_b32 s84, s60\n"
        "s_waitcnt vmcnt(6)\n"
        "s_branch .Lop_go1_%=\n"
        ".p2align 5\n"
        ".Lop_i12_%=:\n"
        "v_mfma_f32_16x16x32_bf16 v[40:43], v[164:167], v[8:11], v[40:43]\n"
        "buffer_load_dword v4, v2, s[40:43], s86 offen sc1\n"
        "v_mfma_f32_16x16x32_bf16 v[44:47], v[168:171], v[8:11], v[44:47]\n"
        "v_mfma_f32_16x16x32_bf16 v[36:39], v[172:175], v[12:15], v[36:39]\n"
        "v_mfma_f32_16x16x32_bf16 v[40:43], v[176:179], v[12:15], v[40:43]\n"
        "v_mfma_f32_16x16x32_bf16 v[44:47], v[180:183], v[12:15], v[44:47]\n"
        "s_waitcnt lgkmcnt(0)\n"
        "v_mfma_f32_16x16x32_bf16 v[36:39], v[184:187], v[16:19], v[36:39]\n"
        "v_mfma_f32_16x16x32_bf16 v[40:43], v[188:191], v[16:19], v[40:43]\n"
        "v_mfma_f32_16x16x32_bf16 v[44:47], v[192:195], v[16:19], v[44:47]\n"
        "v_mfma_f32_16x16x32_bf16 v[36:39], v[196:199], v[20:23], v[36:39]\n"
        "v_mfma_f32_16x16x32_bf16 v[40:43], v[200:203], v[20:23], v[40:43]\n"
        "v_mfma_f32_16x16x32_bf16 v[44:47], v[204:207], v[20:23], v[44:47]\n"
        "s_nop 2\n"
        "v_cvt_pk_bf16_f32 v36, v36, v37\n"
        "v_cvt_pk_bf16_f32 v37, v38, v39\n"
        "v_cvt_pk_bf16_f32 v38, v40, v41\n"
        "v_cvt_pk_bf16_f32 v39, v42, v43\n"
        "buffer_store_dwordx4 v[36:39], v48, s[40:43], s72 offen offset:0 sc1\n"
        "v_cvt_pk_bf16_f32 v44, v44, v45\n"
        "v_cvt_pk_bf16_f32 v45, v46, v47\n"
        "s_nop 0\n"
        "v_permlane16_swap_b32 v44, v46\n"
        "v_permlane16_swap_b32 v45, v47\n"
        "buffer_store_dwordx4 v[44:47], v7, s[40:43], s72 offen offset:0 sc1\n"
        "s_waitcnt vmcnt(2)\n"
        "v_cmp_gt_i32 vcc, s70, v4\n"
        "s_nop 0\n"
        "s_and_b64 vcc, vcc, 3\n"
        "s_cbranch_vccnz .Lop_late1_%=\n"
        "buffer_load_dwordx4 v1, s[40:43], s71 offen offset:0 sc1 lds\n"
        "buffer_load_dwordx4 v1, s[40:43], s71 offen offset:1024 sc1 lds\n"
        "buffer_load_dwordx4 v1, s[40:43], s71 offen offset:2048 sc1 lds\n"
        "buffer_load_dwordx4 v1, s[40:43], s71 offen offset:3072 sc1 lds\n"
        "s_add_u32 m0, m0, 16384\n"
        "s_add_u32 s71, s71, 65536\n"
        "buffer_load_dwordx4 v1, s[40:43], s71 offen offset:0 sc1 lds\n"
        "buffer_load_dwordx4 v1, s[40:43], s71 offen offset:1024 sc1 lds\n"
        "buffer_load_dwordx4 v1, s[40:43], s71 offen offset:2048 sc1 lds\n"
        "buffer_load_dwordx4 v1, s[40:43], s71 offen offset:3072 sc1 lds\n"
        "s_add_u32 s69, s69, 1\n"
        "s_waitcnt vmcnt(8)\n"
        "v_mov_b32 v5, s74\n"
        "buffer_store_dword v5, v3, s[40:43], s73 offen sc1\n"
        "s_waitcnt vmcnt(5)\n"
        "s_branch .Lop_go1_%=\n"
        ".p2align 5\n"
        ".Lop_late1_%=:\n"
        "s_waitcnt vmcnt(0)\n"
        "v_mov_b32_e64 v5, s74\n"
        "buffer_store_dword v5, v3, s[40:43], s73 offen sc1\n"
        "s_cmp_eq_u32 s69, 128\n"
        "s_nop 0\n"
        "s_cbranch_scc0 .Lop_wait1_%=\n"
        "s_endpgm\n"
        ".p2align 5\n"
        ".Lop_wait1_%=:\n"
        "s_mov_b32 s64, 0\n"
        ".Lop_in1_top_%=:\n"
        "buffer_load_dword v4, v2, s[40:43], s86 offen sc1\n"
        "s_waitcnt vmcnt(0)\n"
        "v_cmp_gt_i32_e64 vcc, s70, v4\n"
        "s_nop 0\n"
        "s_and_b64 vcc, vcc, 3\n"
        "s_cbranch_vccz .Lop_in1_ok_%=\n"
        "s_add_u32 s64, s64, 1\n"
        "s_and_b32 s65, s64, 255\n"
        "s_cbranch_scc1 .Lop_in1_go_%=\n"
        "s_mov_b32 s65, 49152\n"
        "s_cmp_lt_u32 s64, 1048576\n"
        "s_cbranch_scc1 .Lop_in1_chk_%=\n"
        "v_mov_b32 v4, 1\n"
        "buffer_store_dword v4, off, s[40:43], s65 sc1\n"
        "s_waitcnt vmcnt(0)\n"
        ".Lop_in1_chk_%=:\n"
        "buffer_load_dword v4, off, s[40:43], s65 sc1\n"
        "s_waitcnt vmcnt(0)\n"
        "v_readfirstlane_b32 s65, v4\n"
        "s_nop 0\n"
        "s_cmp_lg_u32 s65, 1\n"
        "s_cbranch_scc1 .Lop_in1_go_%=\n"
        "s_endpgm\n"
        ".p2align 5\n"
        ".Lop_in1_go_%=:\n"
        "s_branch .Lop_in1_top_%=\n"
        ".p2align 5\n"
        ".Lop_in1_ok_%=:\n"
        "buffer_load_dwordx4 v1, s[40:43], s71 offen offset:0 sc1 lds\n"
        "buffer_load_dwordx4 v1, s[40:43], s71 offen offset:1024 sc1 lds\n"
        "buffer_load_dwordx4 v1, s[40:43], s71 offen offset:2048 sc1 lds\n"
        "buffer_load_dwordx4 v1, s[40:43], s71 offen offset:3072 sc1 lds\n"
        "s_add_u32 m0, m0, 16384\n"
        "s_add_u32 s71, s71, 65536\n"
        "buffer_load_dwordx4 v1, s[40:43], s71 offen offset:0 sc1 lds\n"
        "buffer_load_dwordx4 v1, s[40:43], s71 offen offset:1024 sc1 lds\n"
        "buffer_load_dwordx4 v1, s[40:43], s71 offen offset:2048 sc1 lds\n"
        "buffer_load_dwordx4 v1, s[40:43], s71 offen offset:3072 sc1 lds\n"
        "s_add_u32 s69, s69, 1\n"
        "s_waitcnt vmcnt(4)\n"
        ".Lop_go1_%=:\n"
        "s_mov_b32 s72, 241172480\n"
        "s_mov_b32 s83, 241172480\n"
        "s_mov_b32 s73, 241172480\n"
        "v_add_u32 v0, s78, v6\n"
        "s_barrier\n"
        "ds_read_b128 v[8:11], v0 offset:0\n"
        "ds_read_b128 v[12:15], v0 offset:1024\n"
        "ds_read_b128 v[16:19], v0 offset:2048\n"
        "ds_read_b128 v[20:23], v0 offset:3072\n"
        "s_waitcnt lgkmcnt(2)\n"
        "s_branch .Lop_i0_%=\n"
        ".Lop_end_%=:\n"
            :: "s"(p.ws), "s"(p.Wo), "s"(p.Wo), "s"(p.Wo), "s"(pair(w, delay)), "s"(pair(x4, pr)),
               "s"(pair(PAGE + spread(A_Q) + (8 * kap + w) * 16384, 65 * PAGE + spread(B_P3))), "s"(pair(flag(F_OP + fl), w * 4096)), "s"(pair(0, 0)),
               "v"(pair(lane * 16, lane * 16)), "v"(pair(lane < 2 ? flag(F_AT + (8 * kap + w + 4 * lane) * 3 + nu) : OOB, fo4)), K_ARGS
            : "v0", "v1", "v2", "v3", "v4", "v5", "v6", "v7", "v8", "v9", "v10", "v11", "v12", "v13", "v14", "v15", "v16", "v17", "v18", "v19", "v20", "v21", "v22", "v23", "v24", "v25", "v26", "v27", "v28", "v29", "v30", "v31", "v32", "v33", "v34", "v35", "v36", "v37", "v38", "v39", "v40", "v41", "v42", "v43", "v44", "v45", "v46", "v47", "v48", "v49", "v50", "v51", "v52", "v53", "v54", "v55", "v56", "v57", "v58", "v59", "v60", "v61", "v62", "v63", "v64", "v65", "v66", "v67", "v68", "v69", "v70", "v71", "v72", "v73", "v74", "v75", "v76", "v77", "v78", "v79", "v255", "a255", "s26", "s27", "s28", "s29", "s30", "s31", "s32", "s33", "s34", "s35", "s36", "s37", "s38", "s39", "s40", "s41", "s42", "s43", "s44", "s45", "s46", "s47", "s48", "s49", "s50", "s51", "s52", "s53", "s54", "s55", "s56", "s57", "s58", "s59", "s60", "s61", "s62", "s63", "s64", "s65", "s66", "s67", "s68", "s69", "s70", "s71", "s72", "s73", "s74", "s75", "s76", "s77", "s78", "s79", "s80", "s81", "s82", "s83", "s84", "s85", "s86", "s87", "s88", "s89", "s90", "s91", "s92", "s93", "s94", "s95", "s96", "s97", "s98", "s99", "vcc", "scc", "memory");
    } else {
        const unsigned hb = 65 * PAGE + spread(B_H + kap * 73728);
        asm volatile(
        "s_nop 4\n"
        "s_mov_b64 s[40:41], %0\n"
        "s_and_b32 s41, s41, 0xffff\n"
        "s_mov_b32 s42, 236978176\n"
        "s_mov_b32 s43, 0x20000\n"
        "s_mov_b64 s[48:49], %4\n"
        "s_mov_b64 s[56:57], %5\n"
        "s_mov_b64 s[58:59], %6\n"
        "s_mov_b64 s[60:61], %7\n"
        "s_mov_b64 s[62:63], %8\n"
        "v_mov_b64 v[0:1], %9\n"
        "v_mov_b64 v[2:3], %10\n"
        "v_mov_b64 v[4:5], %11\n"
        "v_mov_b64 v[6:7], %12\n"
        "v_mov_b64 v[8:9], %13\n"
        "v_mov_b64 v[10:11], %14\n"
        "v_mov_b64 v[12:13], %15\n"
        "v_mov_b64 v[14:15], %16\n"
        "s_mov_b32 s47, 0x20000\n"
        "s_mul_i32 s79, s48, 40960\n"
        ".Lf2_start_%=:\n"
        "s_sub_u32 s49, s49, 1\n"
        "s_cbranch_scc1 .Lf2_start2_%=\n"
        "s_sleep 16\n"
        "s_branch .Lf2_start_%=\n"
        ".Lf2_start2_%=:\n"
        "s_mov_b64 s[44:45], %1\n"
        "s_and_b32 s45, s45, 0xffff\n"
        "s_mov_b32 s46, 5308416\n"
        "s_add_u32 m0, s79, 0\n"
        "s_mov_b32 s65, 0\n"
        "buffer_load_dwordx4 v8, s[44:47], s65 offen lds\n"
        "buffer_load_dwordx4 v9, s[44:47], s65 offen offset:1024 lds\n"
        "buffer_load_dwordx4 v10, s[44:47], s65 offen offset:2048 lds\n"
        "s_add_u32 m0, s79, 3072\n"
        "s_mov_b32 s65, 55296\n"
        "buffer_load_dwordx4 v8, s[44:47], s65 offen lds\n"
        "buffer_load_dwordx4 v9, s[44:47], s65 offen offset:1024 lds\n"
        "buffer_load_dwordx4 v10, s[44:47], s65 offen offset:2048 lds\n"
        "s_waitcnt vmcnt(3)\n"
        "ds_read_b64_tr_b16 a[0:1], v11 offset:0\n"
        "ds_read_b64_tr_b16 a[2:3], v11 offset:1536\n"
        "ds_read_b64_tr_b16 a[4:5], v11 offset:8\n"
        "ds_read_b64_tr_b16 a[6:7], v11 offset:1544\n"
        "ds_read_b64_tr_b16 a[8:9], v15 offset:0\n"
        "ds_read_b64_tr_b16 a[10:11], v15 offset:1536\n"
        "s_waitcnt vmcnt(0)\n"
        "ds_read_b64_tr_b16 a[12:13], v11 offset:3072\n"
        "ds_read_b64_tr_b16 a[14:15], v11 offset:4608\n"
        "ds_read_b64_tr_b16 a[16:17], v11 offset:3080\n"
        "ds_read_b64_tr_b16 a[18:19], v11 offset:4616\n"
        "ds_read_b64_tr_b16 a[20:21], v15 offset:3072\n"
        "ds_read_b64_tr_b16 a[22:23], v15 offset:4608\n"
        "s_waitcnt lgkmcnt(6)\n"
        "s_add_u32 m0, s79, 0\n"
        "s_mov_b32 s65, 110592\n"
        "buffer_load_dwordx4 v8, s[44:47], s65 offen lds\n"
        "buffer_load_dwordx4 v9, s[44:47], s65 offen offset:1024 lds\n"
        "buffer_load_dwordx4 v10, s[44:47], s65 offen offset:2048 lds\n"
        "s_waitcnt vmcnt(0)\n"
        "ds_read_b64_tr_b16 a[24:25], v11 offset:0\n"
        "ds_read_b64_tr_b16 a[26:27], v11 offset:1536\n"
        "ds_read_b64_tr_b16 a[28:29], v11 offset:8\n"
        "ds_read_b64_tr_b16 a[30:31], v11 offset:1544\n"
        "ds_read_b64_tr_b16 a[32:33], v15 offset:0\n"
        "ds_read_b64_tr_b16 a[34:35], v15 offset:1536\n"
        "s_waitcnt lgkmcnt(6)\n"
        "s_add_u32 m0, s79, 3072\n"
        "s_mov_b32 s65, 165888\n"
        "buffer_load_dwordx4 v8, s[44:47], s65 offen lds\n"
        "buffer_load_dwordx4 v9, s[44:47], s65 offen offset:1024 lds\n"
        "buffer_load_dwordx4 v10, s[44:47], s65 offen offset:2048 lds\n"
        "s_waitcnt vmcnt(0)\n"
        "ds_read_b64_tr_b16 a[36:37], v11 offset:3072\n"
        "ds_read_b64_tr_b16 a[38:39], v11 offset:4608\n"
        "ds_read_b64_tr_b16 a[40:41], v11 offset:3080\n"
        "ds_read_b64_tr_b16 a[42:43], v11 offset:4616\n"
        "ds_read_b64_tr_b16 a[44:45], v15 offset:3072\n"
        "ds_read_b64_tr_b16 a[46:47], v15 offset:4608\n"
        "s_waitcnt lgkmcnt(6)\n"
        "s_add_u32 m0, s79, 0\n"
        "s_mov_b32 s65, 221184\n"
        "buffer_load_dwordx4 v8, s[44:47], s65 offen lds\n"
        "buffer_load_dwordx4 v9, s[44:47], s65 offen offset:1024 lds\n"
        "buffer_load_dwordx4 v10, s[44:47], s65 offen offset:2048 lds\n"
        "s_waitcnt vmcnt(0)\n"
        "ds_read_b64_tr_b16 a[48:49], v11 offset:0\n"
        "ds_read_b64_tr_b16 a[50:51], v11 offset:1536\n"
        "ds_read_b64_tr_b16 a[52:53], v11 offset:8\n"
        "ds_read_b64_tr_b16 a[54:55], v11 offset:1544\n"
        "ds_read_b64_tr_b16 a[56:57], v15 offset:0\n"
        "ds_read_b64_tr_b16 a[58:59], v15 offset:1536\n"
        "s_waitcnt lgkmcnt(6)\n"
        "s_add_u32 m0, s79, 3072\n"
        "s_mov_b32 s65, 276480\n"
        "buffer_load_dwordx4 v8, s[44:47], s65 offen lds\n"
        "buffer_load_dwordx4 v9, s[44:47], s65 offen offset:1024 lds\n"
        "buffer_load_dwordx4 v10, s[44:47], s65 offen offset:2048 lds\n"
        "s_waitcnt vmcnt(0)\n"
        "ds_read_b64_tr_b16 a[60:61], v11 offset:3072\n"
        "ds_read_b64_tr_b16 a[62:63], v11 offset:4608\n"
        "ds_read_b64_tr_b16 a[64:65], v11 offset:3080\n"
        "ds_read_b64_tr_b16 a[66:67], v11 offset:4616\n"
        "ds_read_b64_tr_b16 a[68:69], v15 offset:3072\n"
        "ds_read_b64_tr_b16 a[70:71], v15 offset:4608\n"
        "s_waitcnt lgkmcnt(6)\n"
        "s_add_u32 m0, s79, 0\n"
        "s_mov_b32 s65, 331776\n"
        "buffer_load_dwordx4 v8, s[44:47], s65 offen lds\n"
        "buffer_load_dwordx4 v9, s[44:47], s65 offen offset:1024 lds\n"
        "buffer_load_dwordx4 v10, s[44:47], s65 offen offset:2048 lds\n"
        "s_waitcnt vmcnt(0)\n"
        "ds_read_b64_tr_b16 a[72:73], v11 offset:0\n"
        "ds_read_b64_tr_b16 a[74:75], v11 offset:1536\n"
        "ds_read_b64_tr_b16 a[76:77], v11 offset:8\n"
        "ds_read_b64_tr_b16 a[78:79], v11 offset:1544\n"
        "ds_read_b64_tr_b16 a[80:81], v15 offset:0\n"
        "ds_read_b64_tr_b16 a[82:83], v15 offset:1536\n"
        "s_waitcnt lgkmcnt(6)\n"
        "s_add_u32 m0, s79, 3072\n"
        "s_mov_b32 s65, 387072\n"
        "buffer_load_dwordx4 v8, s[44:47], s65 offen lds\n"
        "buffer_load_dwordx4 v9, s[44:47], s65 offen offset:1024 lds\n"
        "buffer_load_dwordx4 v10, s[44:47], s65 offen offset:2048 lds\n"
        "s_waitcnt vmcnt(0)\n"
        "ds_read_b64_tr_b16 a[84:85], v11 offset:3072\n"
        "ds_read_b64_tr_b16 a[86:87], v11 offset:4608\n"
        "ds_read_b64_tr_b16 a[88:89], v11 offset:3080\n"
        "ds_read_b64_tr_b16 a[90:91], v11 offset:4616\n"
        "ds_read_b64_tr_b16 a[92:93], v15 offset:3072\n"
        "ds_read_b64_tr_b16 a[94:95], v15 offset:4608\n"
        "s_waitcnt lgkmcnt(6)\n"
        "s_add_u32 m0, s79, 0\n"
        "s_mov_b32 s65, 0\n"
        "buffer_load_dwordx4 v12, s[44:47], s65 offen lds\n"
        "buffer_load_dwordx4 v13, s[44:47], s65 offen offset:1024 lds\n"
        "buffer_load_dwordx4 v14, s[44:47], s65 offen offset:2048 lds\n"
        "s_waitcnt vmcnt(0)\n"
        "ds_read_b64_tr_b16 a[96:97], v11 offset:0\n"
        "ds_read_b64_tr_b16 a[98:99], v11 offset:1536\n"
        "ds_read_b64_tr_b16 a[100:101], v11 offset:8\n"
        "ds_read_b64_tr_b16 a[102:103], v11 offset:1544\n"
        "ds_read_b64_tr_b16 a[104:105], v15 offset:0\n"
        "ds_read_b64_tr_b16 a[106:107], v15 offset:1536\n"
        "s_waitcnt lgkmcnt(6)\n"
        "s_add_u32 m0, s79, 3072\n"
        "s_mov_b32 s65, 110592\n"
        "buffer_load_dwordx4 v12, s[44:47], s65 offen lds\n"
        "buffer_load_dwordx4 v13, s[44:47], s65 offen offset:1024 lds\n"
        "buffer_load_dwordx4 v14, s[44:47], s65 offen offset:2048 lds\n"
        "s_waitcnt vmcnt(0)\n"
        "ds_read_b64_tr_b16 a[108:109], v11 offset:3072\n"
        "ds_read_b64_tr_b16 a[110:111], v11 offset:4608\n"
        "ds_read_b64_tr_b16 a[112:113], v11 offset:3080\n"
        "ds_read_b64_tr_b16 a[114:115], v11 offset:4616\n"
        "ds_read_b64_tr_b16 a[116:117], v15 offset:3072\n"
        "ds_read_b64_tr_b16 a[118:119], v15 offset:4608\n"
        "s_waitcnt lgkmcnt(6)\n"
        "s_add_u32 m0, s79, 0\n"
        "s_mov_b32 s65, 221184\n"
        "buffer_load_dwordx4 v12, s[44:47], s65 offen lds\n"
        "buffer_load_dwordx4 v13, s[44:47], s65 offen offset:1024 lds\n"
        "buffer_load_dwordx4 v14, s[44:47], s65 offen offset:2048 lds\n"
        "s_waitcnt vmcnt(0)\n"
        "ds_read_b64_tr_b16 a[120:121], v11 offset:0\n"
        "ds_read_b64_tr_b16 a[122:123], v11 offset:1536\n"
        "ds_read_b64_tr_b16 a[124:125], v11 offset:8\n"
        "ds_read_b64_tr_b16 a[126:127], v11 offset:1544\n"
        "ds_read_b64_tr_b16 a[128:129], v15 offset:0\n"
        "ds_read_b64_tr_b16 a[130:131], v15 offset:1536\n"
        "s_waitcnt lgkmcnt(6)\n"
        "s_add_u32 m0, s79, 3072\n"
        "s_mov_b32 s65, 331776\n"
        "buffer_load_dwordx4 v12, s[44:47], s65 offen lds\n"
        "buffer_load_dwordx4 v13, s[44:47], s65 offen offset:1024 lds\n"
        "buffer_load_dwordx4 v14, s[44:47], s65 offen offset:2048 lds\n"
        "s_waitcnt vmcnt(0)\n"
        "ds_read_b64_tr_b16 a[132:133], v11 offset:3072\n"
        "ds_read_b64_tr_b16 a[134:135], v11 offset:4608\n"
        "ds_read_b64_tr_b16 a[136:137], v11 offset:3080\n"
        "ds_read_b64_tr_b16 a[138:139], v11 offset:4616\n"
        "ds_read_b64_tr_b16 a[140:141], v15 offset:3072\n"
        "ds_read_b64_tr_b16 a[142:143], v15 offset:4608\n"
        "s_waitcnt lgkmcnt(6)\n"
        "s_add_u32 m0, s79, 0\n"
        "s_mov_b32 s65, 442368\n"
        "buffer_load_dwordx4 v8, s[44:47], s65 offen lds\n"
        "buffer_load_dwordx4 v9, s[44:47], s65 offen offset:1024 lds\n"
        "buffer_load_dwordx4 v10, s[44:47], s65 offen offset:2048 lds\n"
        "s_waitcnt vmcnt(0)\n"
        "ds_read_b64_tr_b16 a[144:145], v11 offset:0\n"
        "ds_read_b64_tr_b16 a[146:147], v11 offset:1536\n"
        "ds_read_b64_tr_b16 a[148:149], v11 offset:8\n"
        "ds_read_b64_tr_b16 a[150:151], v11 offset:1544\n"
        "ds_read_b64_tr_b16 a[152:153], v15 offset:0\n"
        "ds_read_b64_tr_b16 a[154:155], v15 offset:1536\n"
        "s_waitcnt lgkmcnt(6)\n"
        "s_add_u32 m0, s79, 3072\n"
        "s_mov_b32 s65, 497664\n"
        "buffer_load_dwordx4 v8, s[44:47], s65 offen lds\n"
        "buffer_load_dwordx4 v9, s[44:47], s65 offen offset:1024 lds\n"
        "buffer_load_dwordx4 v10, s[44:47], s65 offen offset:2048 lds\n"
        "s_waitcnt vmcnt(0)\n"
        "ds_read_b64_tr_b16 a[156:157], v11 offset:3072\n"
        "ds_read_b64_tr_b16 a[158:159], v11 offset:4608\n"
        "ds_read_b64_tr_b16 a[160:161], v11 offset:3080\n"
        "ds_read_b64_tr_b16 a[162:163], v11 offset:4616\n"
        "ds_read_b64_tr_b16 a[164:165], v15 offset:3072\n"
        "ds_read_b64_tr_b16 a[166:167], v15 offset:4608\n"
        "s_waitcnt lgkmcnt(6)\n"
        "s_add_u32 m0, s79, 0\n"
        "s_mov_b32 s65, 552960\n"
        "buffer_load_dwordx4 v8, s[44:47], s65 offen lds\n"
        "buffer_load_dwordx4 v9, s[44:47], s65 offen offset:1024 lds\n"
        "buffer_load_dwordx4 v10, s[44:47], s65 offen offset:2048 lds\n"
        "s_waitcnt vmcnt(0)\n"
        "ds_read_b64_tr_b16 a[168:169], v11 offset:0\n"
        "ds_read_b64_tr_b16 a[170:171], v11 offset:1536\n"
        "ds_read_b64_tr_b16 a[172:173], v11 offset:8\n"
        "ds_read_b64_tr_b16 a[174:175], v11 offset:1544\n"
        "ds_read_b64_tr_b16 a[176:177], v15 offset:0\n"
        "ds_read_b64_tr_b16 a[178:179], v15 offset:1536\n"
        "s_waitcnt lgkmcnt(6)\n"
        "s_add_u32 m0, s79, 3072\n"
        "s_mov_b32 s65, 608256\n"
        "buffer_load_dwordx4 v8, s[44:47], s65 offen lds\n"
        "buffer_load_dwordx4 v9, s[44:47], s65 offen offset:1024 lds\n"
        "buffer_load_dwordx4 v10, s[44:47], s65 offen offset:2048 lds\n"
        "s_waitcnt vmcnt(0)\n"
        "ds_read_b64_tr_b16 a[180:181], v11 offset:3072\n"
        "ds_read_b64_tr_b16 a[182:183], v11 offset:4608\n"
        "ds_read_b64_tr_b16 a[184:185], v11 offset:3080\n"
        "ds_read_b64_tr_b16 a[186:187], v11 offset:4616\n"
        "ds_read_b64_tr_b16 a[188:189], v15 offset:3072\n"
        "ds_read_b64_tr_b16 a[190:191], v15 offset:4608\n"
        "s_waitcnt lgkmcnt(6)\n"
        "s_add_u32 m0, s79, 0\n"
        "s_mov_b32 s65, 663552\n"
        "buffer_load_dwordx4 v8, s[44:47], s65 offen lds\n"
        "buffer_load_dwordx4 v9, s[44:47], s65 offen offset:1024 lds\n"
        "buffer_load_dwordx4 v10, s[44:47], s65 offen offset:2048 lds\n"
        "s_waitcnt vmcnt(0)\n"
        "ds_read_b64_tr_b16 a[192:193], v11 offset:0\n"
        "ds_read_b64_tr_b16 a[194:195], v11 offset:1536\n"
        "ds_read_b64_tr_b16 a[196:197], v11 offset:8\n"
        "ds_read_b64_tr_b16 a[198:199], v11 offset:1544\n"
        "ds_read_b64_tr_b16 a[200:201], v15 offset:0\n"
        "ds_read_b64_tr_b16 a[202:203], v15 offset:1536\n"
        "s_waitcnt lgkmcnt(6)\n"
        "s_add_u32 m0, s79, 3072\n"
        "s_mov_b32 s65, 718848\n"
        "buffer_load_dwordx4 v8, s[44:47], s65 offen lds\n"
        "buffer_load_dwordx4 v9, s[44:47], s65 offen offset:1024 lds\n"
        "buffer_load_dwordx4 v10, s[44:47], s65 offen offset:2048 lds\n"
        "s_waitcnt vmcnt(0)\n"
        "ds_read_b64_tr_b16 a[204:205], v11 offset:3072\n"
        "ds_read_b64_tr_b16 a[206:207], v11 offset:4608\n"
        "ds_read_b64_tr_b16 a[208:209], v11 offset:3080\n"
        "ds_read_b64_tr_b16 a[210:211], v11 offset:4616\n"
        "ds_read_b64_tr_b16 a[212:213], v15 offset:3072\n"
        "ds_read_b64_tr_b16 a[214:215], v15 offset:4608\n"
        "s_waitcnt lgkmcnt(6)\n"
        "s_add_u32 m0, s79, 0\n"
        "s_mov_b32 s65, 774144\n"
        "buffer_load_dwordx4 v8, s[44:47], s65 offen lds\n"
        "buffer_load_dwordx4 v9, s[44:47], s65 offen offset:1024 lds\n"
        "buffer_load_dwordx4 v10, s[44:47], s65 offen offset:2048 lds\n"
        "s_waitcnt vmcnt(0)\n"
        "ds_read_b64_tr_b16 a[216:217], v11 offset:0\n"
        "ds_read_b64_tr_b16 a[218:219], v11 offset:1536\n"
        "ds_read_b64_tr_b16 a[220:221], v11 offset:8\n"
        "ds_read_b64_tr_b16 a[222:223], v11 offset:1544\n"
        "ds_read_b64_tr_b16 a[224:225], v15 offset:0\n"
        "ds_read_b64_tr_b16 a[226:227], v15 offset:1536\n"
        "s_waitcnt lgkmcnt(6)\n"
        "s_add_u32 m0, s79, 3072\n"
        "s_mov_b32 s65, 829440\n"
        "buffer_load_dwordx4 v8, s[44:47], s65 offen lds\n"
        "buffer_load_dwordx4 v9, s[44:47], s65 offen offset:1024 lds\n"
        "buffer_load_dwordx4 v10, s[44:47], s65 offen offset:2048 lds\n"
        "s_waitcnt vmcnt(0)\n"
        "ds_read_b64_tr_b16 a[228:229], v11 offset:3072\n"
        "ds_read_b64_tr_b16 a[230:231], v11 offset:4608\n"
        "ds_read_b64_tr_b16 a[232:233], v11 offset:3080\n"
        "ds_read_b64_tr_b16 a[234:235], v11 offset:4616\n"
        "ds_read_b64_tr_b16 a[236:237], v15 offset:3072\n"
        "ds_read_b64_tr_b16 a[238:239], v15 offset:4608\n"
        "s_waitcnt lgkmcnt(6)\n"
        "s_add_u32 m0, s79, 0\n"
        "s_mov_b32 s65, 442368\n"
        "buffer_load_dwordx4 v12, s[44:47], s65 offen lds\n"
        "buffer_load_dwordx4 v13, s[44:47], s65 offen offset:1024 lds\n"
        "buffer_load_dwordx4 v14, s[44:47], s65 offen offset:2048 lds\n"
        "s_waitcnt vmcnt(0)\n"
        "ds_read_b64_tr_b16 a[240:241], v11 offset:0\n"
        "ds_read_b64_tr_b16 a[242:243], v11 offset:1536\n"
        "ds_read_b64_tr_b16 a[244:245], v11 offset:8\n"
        "ds_read_b64_tr_b16 a[246:247], v11 offset:1544\n"
        "ds_read_b64_tr_b16 a[248:249], v15 offset:0\n"
        "ds_read_b64_tr_b16 a[250:251], v15 offset:1536\n"
        "s_waitcnt lgkmcnt(6)\n"
        "s_add_u32 m0, s79, 3072\n"
        "s_mov_b32 s65, 552960\n"
        "buffer_load_dwordx4 v12, s[44:47], s65 offen lds\n"
        "buffer_load_dwordx4 v13, s[44:47], s65 offen offset:1024 lds\n"
        "buffer_load_dwordx4 v14, s[44:47], s65 offen offset:2048 lds\n"
        "s_waitcnt vmcnt(0)\n"
        "ds_read_b64_tr_b16 a[252:253], v11 offset:3072\n"
        "ds_read_b64_tr_b16 a[254:255], v11 offset:4608\n"
        "ds_read_b64_tr_b16 v[80:81], v11 offset:3080\n"
        "ds_read_b64_tr_b16 v[82:83], v11 offset:4616\n"
        "ds_read_b64_tr_b16 v[84:85], v15 offset:3072\n"
        "ds_read_b64_tr_b16 v[86:87], v15 offset:4608\n"
        "s_waitcnt lgkmcnt(6)\n"
        "s_add_u32 m0, s79, 0\n"
        "s_mov_b32 s65, 663552\n"
        "buffer_load_dwordx4 v12, s[44:47], s65 offen lds\n"
        "buffer_load_dwordx4 v13, s[44:47], s65 offen offset:1024 lds\n"
        "buffer_load_dwordx4 v14, s[44:47], s65 offen offset:2048 lds\n"
        "s_waitcnt vmcnt(0)\n"
        "ds_read_b64_tr_b16 v[88:89], v11 offset:0\n"
        "ds_read_b64_tr_b16 v[90:91], v11 offset:1536\n"
        "ds_read_b64_tr_b16 v[92:93], v11 offset:8\n"
        "ds_read_b64_tr_b16 v[94:95], v11 offset:1544\n"
        "ds_read_b64_tr_b16 v[96:97], v15 offset:0\n"
        "ds_read_b64_tr_b16 v[98:99], v15 offset:1536\n"
        "s_waitcnt lgkmcnt(6)\n"
        "s_add_u32 m0, s79, 3072\n"
        "s_mov_b32 s65, 774144\n"
        "buffer_load_dwordx4 v12, s[44:47], s65 offen lds\n"
        "buffer_load_dwordx4 v13, s[44:47], s65 offen offset:1024 lds\n"
        "buffer_load_dwordx4 v14, s[44:47], s65 offen offset:2048 lds\n"
        "s_waitcnt vmcnt(0)\n"
        "ds_read_b64_tr_b16 v[100:101], v11 offset:3072\n"
        "ds_read_b64_tr_b16 v[102:103], v11 offset:4608\n"
        "ds_read_b64_tr_b16 v[104:105], v11 offset:3080\n"
        "ds_read_b64_tr_b16 v[106:107], v11 offset:4616\n"
        "ds_read_b64_tr_b16 v[108:109], v15 offset:3072\n"
        "ds_read_b64_tr_b16 v[110:111], v15 offset:4608\n"
        "s_waitcnt lgkmcnt(6)\n"
        "s_add_u32 m0, s79, 0\n"
        "s_mov_b32 s65, 884736\n"
        "buffer_load_dwordx4 v8, s[44:47], s65 offen lds\n"
        "buffer_load_dwordx4 v9, s[44:47], s65 offen offset:1024 lds\n"
        "buffer_load_dwordx4 v10, s[44:47], s65 offen offset:2048 lds\n"
        "s_waitcnt vmcnt(0)\n"
        "ds_read_b64_tr_b16 v[112:113], v11 offset:0\n"
        "ds_read_b64_tr_b16 v[114:115], v11 offset:1536\n"
        "ds_read_b64_tr_b16 v[116:117], v11 offset:8\n"
        "ds_read_b64_tr_b16 v[118:119], v11 offset:1544\n"
        "ds_read_b64_tr_b16 v[120:121], v15 offset:0\n"
        "ds_read_b64_tr_b16 v[122:123], v15 offset:1536\n"
        "s_waitcnt lgkmcnt(6)\n"
        "s_add_u32 m0, s79, 3072\n"
        "s_mov_b32 s65, 940032\n"
        "buffer_load_dwordx4 v8, s[44:47], s65 offen lds\n"
        "buffer_load_dwordx4 v9, s[44:47], s65 offen offset:1024 lds\n"
        "buffer_load_dwordx4 v10, s[44:47], s65 offen offset:2048 lds\n"
        "s_waitcnt vmcnt(0)\n"
        "ds_read_b64_tr_b16 v[124:125], v11 offset:3072\n"
        "ds_read_b64_tr_b16 v[126:127], v11 offset:4608\n"
        "ds_read_b64_tr_b16 v[128:129], v11 offset:3080\n"
        "ds_read_b64_tr_b16 v[130:131], v11 offset:4616\n"
        "ds_read_b64_tr_b16 v[132:133], v15 offset:3072\n"
        "ds_read_b64_tr_b16 v[134:135], v15 offset:4608\n"
        "s_waitcnt lgkmcnt(6)\n"
        "s_add_u32 m0, s79, 0\n"
        "s_mov_b32 s65, 995328\n"
        "buffer_load_dwordx4 v8, s[44:47], s65 offen lds\n"
        "buffer_load_dwordx4 v9, s[44:47], s65 offen offset:1024 lds\n"
        "buffer_load_dwordx4 v10, s[44:47], s65 offen offset:2048 lds\n"
        "s_waitcnt vmcnt(0)\n"
        "ds_read_b64_tr_b16 v[136:137], v11 offset:0\n"
        "ds_read_b64_tr_b16 v[138:139], v11 offset:1536\n"
        "ds_read_b64_tr_b16 v[140:141], v11 offset:8\n"
        "ds_read_b64_tr_b16 v[142:143], v11 offset:1544\n"
        "ds_read_b64_tr_b16 v[144:145], v15 offset:0\n"
        "ds_read_b64_tr_b16 v[146:147], v15 offset:1536\n"
        "s_waitcnt lgkmcnt(6)\n"
        "s_add_u32 m0, s79, 3072\n"
        "s_mov_b32 s65, 1050624\n"
        "buffer_load_dwordx4 v8, s[44:47], s65 offen lds\n"
        "buffer_load_dwordx4 v9, s[44:47], s65 offen offset:1024 lds\n"
        "buffer_load_dwordx4 v10, s[44:47], s65 offen offset:2048 lds\n"
        "s_waitcnt vmcnt(0)\n"
        "ds_read_b64_tr_b16 v[148:149], v11 offset:3072\n"
        "ds_read_b64_tr_b16 v[150:151], v11 offset:4608\n"
        "ds_read_b64_tr_b16 v[152:153], v11 offset:3080\n"
        "ds_read_b64_tr_b16 v[154:155], v11 offset:4616\n"
        "ds_read_b64_tr_b16 v[156:157], v15 offset:3072\n"
        "ds_read_b64_tr_b16 v[158:159], v15 offset:4608\n"
        "s_waitcnt lgkmcnt(6)\n"
        "s_add_u32 m0, s79, 0\n"
        "s_mov_b32 s65, 1105920\n"
        "buffer_load_dwordx4 v8, s[44:47], s65 offen lds\n"
        "buffer_load_dwordx4 v9, s[44:47], s65 offen offset:1024 lds\n"
        "buffer_load_dwordx4 v10, s[44:47], s65 offen offset:2048 lds\n"
        "s_waitcnt vmcnt(0)\n"
        "ds_read_b64_tr_b16 v[160:161], v11 offset:0\n"
        "ds_read_b64_tr_b16 v[162:163], v11 offset:1536\n"
        "ds_read_b64_tr_b16 v[164:165], v11 offset:8\n"
        "ds_read_b64_tr_b16 v[166:167], v11 offset:1544\n"
        "ds_read_b64_tr_b16 v[168:169], v15 offset:0\n"
        "ds_read_b64_tr_b16 v[170:171], v15 offset:1536\n"
        "s_waitcnt lgkmcnt(6)\n"
        "s_add_u32 m0, s79, 3072\n"
        "s_mov_b32 s65, 1161216\n"
        "buffer_load_dwordx4 v8, s[44:47], s65 offen lds\n"
        "buffer_load_dwordx4 v9, s[44:47], s65 offen offset:1024 lds\n"
        "buffer_load_dwordx4 v10, s[44:47], s65 offen offset:2048 lds\n"
        "s_waitcnt vmcnt(0)\n"
        "ds_read_b64_tr_b16 v[172:173], v11 offset:3072\n"
        "ds_read_b64_tr_b16 v[174:175], v11 offset:4608\n"
        "ds_read_b64_tr_b16 v[176:177], v11 offset:3080\n"
        "ds_read_b64_tr_b16 v[178:179], v11 offset:4616\n"
        "ds_read_b64_tr_b16 v[180:181], v15 offset:3072\n"
        "ds_read_b64_tr_b16 v[182:183], v15 offset:4608\n"
        "s_waitcnt lgkmcnt(6)\n"
        "s_add_u32 m0, s79, 0\n"
        "s_mov_b32 s65, 1216512\n"
        "buffer_load_dwordx4 v8, s[44:47], s65 offen lds\n"
        "buffer_load_dwordx4 v9, s[44:47], s65 offen offset:1024 lds\n"
        "buffer_load_dwordx4 v10, s[44:47], s65 offen offset:2048 lds\n"
        "s_waitcnt vmcnt(0)\n"
        "ds_read_b64_tr_b16 v[184:185], v11 offset:0\n"
        "ds_read_b64_tr_b16 v[186:187], v11 offset:1536\n"
        "ds_read_b64_tr_b16 v[188:189], v11 offset:8\n"
        "ds_read_b64_tr_b16 v[190:191], v11 offset:1544\n"
        "ds_read_b64_tr_b16 v[192:193], v15 offset:0\n"
        "ds_read_b64_tr_b16 v[194:195], v15 offset:1536\n"
        "s_waitcnt lgkmcnt(6)\n"
        "s_add_u32 m0, s79, 3072\n"
        "s_mov_b32 s65, 1271808\n"
        "buffer_load_dwordx4 v8, s[44:47], s65 offen lds\n"
        "buffer_load_dwordx4 v9, s[44:47], s65 offen offset:1024 lds\n"
        "buffer_load_dwordx4 v10, s[44:47], s65 offen offset:2048 lds\n"
        "s_waitcnt vmcnt(0)\n"
        "ds_read_b64_tr_b16 v[196:197], v11 offset:3072\n"
        "ds_read_b64_tr_b16 v[198:199], v11 offset:4608\n"
        "ds_read_b64_tr_b16 v[200:201], v11 offset:3080\n"
        "ds_read_b64_tr_b16 v[202:203], v11 offset:4616\n"
        "ds_read_b64_tr_b16 v[204:205], v15 offset:3072\n"
        "ds_read_b64_tr_b16 v[206:207], v15 offset:4608\n"
        "s_waitcnt lgkmcnt(6)\n"
        "s_add_u32 m0, s79, 0\n"
        "s_mov_b32 s65, 884736\n"
        "buffer_load_dwordx4 v12, s[44:47], s65 offen lds\n"
        "buffer_load_dwordx4 v13, s[44:47], s65 offen offset:1024 lds\n"
        "buffer_load_dwordx4 v14, s[44:47], s65 offen offset:2048 lds\n"
        "s_waitcnt vmcnt(0)\n"
        "ds_read_b64_tr_b16 v[208:209], v11 offset:0\n"
        "ds_read_b64_tr_b16 v[210:211], v11 offset:1536\n"
        "ds_read_b64_tr_b16 v[212:213], v11 offset:8\n"
        "ds_read_b64_tr_b16 v[214:215], v11 offset:1544\n"
        "ds_read_b64_tr_b16 v[216:217], v15 offset:0\n"
        "ds_read_b64_tr_b16 v[218:219], v15 offset:1536\n"
        "s_waitcnt lgkmcnt(6)\n"
        "s_add_u32 m0, s79, 3072\n"
        "s_mov_b32 s65, 995328\n"
        "buffer_load_dwordx4 v12, s[44:47], s65 offen lds\n"
        "buffer_load_dwordx4 v13, s[44:47], s65 offen offset:1024 lds\n"
        "buffer_load_dwordx4 v14, s[44:47], s65 offen offset:2048 lds\n"
        "s_waitcnt vmcnt(0)\n"
        "ds_read_b64_tr_b16 v[220:221], v11 offset:3072\n"
        "ds_read_b64_tr_b16 v[222:223], v11 offset:4608\n"
        "ds_read_b64_tr_b16 v[224:225], v11 offset:3080\n"
        "ds_read_b64_tr_b16 v[226:227], v11 offset:4616\n"
        "ds_read_b64_tr_b16 v[228:229], v15 offset:3072\n"
        "ds_read_b64_tr_b16 v[230:231], v15 offset:4608\n"
        "s_waitcnt lgkmcnt(6)\n"
        "s_add_u32 m0, s79, 0\n"
        "s_mov_b32 s65, 1105920\n"
        "buffer_load_dwordx4 v12, s[44:47], s65 offen lds\n"
        "buffer_load_dwordx4 v13, s[44:47], s65 offen offset:1024 lds\n"
        "buffer_load_dwordx4 v14, s[44:47], s65 offen offset:2048 lds\n"
        "s_waitcnt vmcnt(0)\n"
        "ds_read_b64_tr_b16 v[232:233], v11 offset:0\n"
        "ds_read_b64_tr_b16 v[234:235], v11 offset:1536\n"
        "ds_read_b64_tr_b16 v[236:237], v11 offset:8\n"
        "ds_read_b64_tr_b16 v[238:239], v11 offset:1544\n"
        "ds_read_b64_tr_b16 v[240:241], v15 offset:0\n"
        "ds_read_b64_tr_b16 v[242:243], v15 offset:1536\n"
        "s_waitcnt lgkmcnt(6)\n"
        "s_add_u32 m0, s79, 3072\n"
        "s_mov_b32 s65, 1216512\n"
        "buffer_load_dwordx4 v12, s[44:47], s65 offen lds\n"
        "buffer_load_dwordx4 v13, s[44:47], s65 offen offset:1024 lds\n"
        "buffer_load_dwordx4 v14, s[44:47], s65 offen offset:2048 lds\n"
        "s_waitcnt vmcnt(0)\n"
        "ds_read_b64_tr_b16 v[244:245], v11 offset:3072\n"
        "ds_read_b64_tr_b16 v[246:247], v11 offset:4608\n"
        "ds_read_b64_tr_b16 v[248:249], v11 offset:3080\n"
        "ds_read_b64_tr_b16 v[250:251], v11 offset:4616\n"
        "ds_read_b64_tr_b16 v[252:253], v15 offset:3072\n"
        "ds_read_b64_tr_b16 v[254:255], v15 offset:4608\n"
        "s_waitcnt lgkmcnt(0)\n"
        "s_getpc_b64 s[44:45]\n"
        ".Lf2_pc_%=:\n"
        "s_add_u32 s44, s44, .Lf2_code_%=-.Lf2_pc_%=\n"
        "s_addc_u32 s45, s45, 0\n"
        "s_and_b32 s45, s45, 0xffff\n"
        "s_and_b32 s44, s44, -4\n"
        "s_mov_b32 s46, .Lf2_end_%=-.Lf2_code_%=\n"
        "s_mov_b32 s47, 0x20000\n"
        "s_lshl_b32 s65, s48, 10\n"
        "buffer_load_dwordx4 v[24:27], v1, s[44:47], s65 offen\n"
        "s_add_u32 s65, s65, 4096\n"
        "buffer_load_dwordx4 v[28:31], v1, s[44:47], s65 offen\n"
        "s_add_u32 s65, s65, 4096\n"
        "buffer_load_dwordx4 v[32:35], v1, s[44:47], s65 offen\n"
        "s_add_u32 s65, s65, 4096\n"
        "buffer_load_dwordx4 v[36:39], v1, s[44:47], s65 offen\n"
        "s_add_u32 s65, s65, 4096\n"
        "buffer_load_dwordx4 v[40:43], v1, s[44:47], s65 offen\n"
        "s_add_u32 s65, s65, 4096\n"
        "v_lshrrev_b32 v20, 4, v1\n"
        "s_mov_b32 s50, s56\n"
        "v_lshlrev_b32 v22, 7, v20\n"
        "s_sub_u32 s66, 0, s50\n"
        "s_mov_b32 s52, 32768\n"
        "s_mov_b32 s64, 0\n"
        ".Lf2_pg0_top_%=:\n"
        "buffer_load_dword v21, v22, s[40:43], s52 offen sc1\n"
        "s_waitcnt vmcnt(0)\n"
        "v_cmp_gt_i32 vcc, 0, v21\n"
        "s_cbranch_vccz .Lf2_pg0_ok_%=\n"
        "s_add_u32 s64, s64, 1\n"
        "s_and_b32 s65, s64, 255\n"
        "s_cbranch_scc1 .Lf2_pg0_go_%=\n"
        "s_mov_b32 s65, 49152\n"
        "s_cmp_lt_u32 s64, 1048576\n"
        "s_cbranch_scc1 .Lf2_pg0_chk_%=\n"
        "v_mov_b32 v21, 1\n"
        "buffer_store_dword v21, off, s[40:43], s65 sc1\n"
        "s_waitcnt vmcnt(0)\n"
        ".Lf2_pg0_chk_%=:\n"
        "buffer_load_dword v21, off, s[40:43], s65 sc1\n"
        "s_waitcnt vmcnt(0)\n"
        "v_readfirstlane_b32 s65, v21\n"
        "s_nop 0\n"
        "s_cmp_lg_u32 s65, 1\n"
        "s_cbranch_scc1 .Lf2_pg0_go_%=\n"
        "s_endpgm\n"
        ".Lf2_pg0_go_%=:\n"
        "s_sleep 2\n"
        "s_branch .Lf2_pg0_top_%=\n"
        ".Lf2_pg0_ok_%=:\n"
        "v_cmp_eq_u32 s[52:53], 1, v21\n"
        "s_nop 1\n"
        "s_mov_b32 s54, 40960\n"
        "s_mov_b32 s64, 0\n"
        ".Lf2_pg1_top_%=:\n"
        "buffer_load_dword v21, v22, s[40:43], s54 offen sc1\n"
        "s_waitcnt vmcnt(0)\n"
        "v_cmp_gt_i32 vcc, 0, v21\n"
        "s_cbranch_vccz .Lf2_pg1_ok_%=\n"
        "s_add_u32 s64, s64, 1\n"
        "s_and_b32 s65, s64, 255\n"
        "s_cbranch_scc1 .Lf2_pg1_go_%=\n"
        "s_mov_b32 s65, 49152\n"
        "s_cmp_lt_u32 s64, 1048576\n"
        "s_cbranch_scc1 .Lf2_pg1_chk_%=\n"
        "v_mov_b32 v21, 1\n"
        "buffer_store_dword v21, off, s[40:43], s65 sc1\n"
        "s_waitcnt vmcnt(0)\n"
        ".Lf2_pg1_chk_%=:\n"
        "buffer_load_dword v21, off, s[40:43], s65 sc1\n"
        "s_waitcnt vmcnt(0)\n"
        "v_readfirstlane_b32 s65, v21\n"
        "s_nop 0\n"
        "s_cmp_lg_u32 s65, 1\n"
        "s_cbranch_scc1 .Lf2_pg1_go_%=\n"
        "s_endpgm\n"
        ".Lf2_pg1_go_%=:\n"
        "s_sleep 2\n"
        "s_branch .Lf2_pg1_top_%=\n"
        ".Lf2_pg1_ok_%=:\n"
        "v_cmp_eq_u32 s[54:55], 1, v21\n"
        "s_nop 1\n"
        "s_mov_b32 vcc_lo, 0x55555555\n"
        "s_mov_b32 vcc_hi, 0x55555555\n"
        "s_xor_b64 vcc, vcc, s[52:53]\n"
        "s_bcnt1_i32_b64 s65, vcc\n"
        "s_sub_u32 s64, s65, 23\n"
        "s_cmp_lt_u32 s64, 19\n"
        "s_cbranch_scc1 .Lf2_rawmap_%=\n"
        "s_cmp_lt_u32 s65, 32\n"
        "s_cselect_b32 s65, 0, -1\n"
        "s_xor_b32 s52, s65, 0x55555555\n"
        "s_xor_b32 s53, s65, 0x55555555\n"
        "s_xor_b32 s54, s65, 0x55555555\n"
        "s_xor_b32 s55, s65, 0xaaaaaaaa\n"
        ".Lf2_rawmap_%=:\n"
        "s_xor_b32 s52, s52, s66\n"
        "s_xor_b32 s53, s53, s66\n"
        "s_xor_b32 s54, s54, s66\n"
        "s_xor_b32 s55, s55, s66\n"
        "s_and_b32 s76, s55, 1\n"
        "s_lshl_b32 s76, s76, 13\n"
        "s_add_u32 s60, s60, s76\n"
        "s_barrier\n"
        "v_mov_b32 v48, v6\n"
        "s_mov_b32 s68, -1\n"
        "s_mov_b32 s69, 0\n"
        "s_mov_b32 s78, 0\n"
        "s_mov_b32 s74, -1\n"
        "s_mov_b32 s86, s76\n"
        "s_lshr_b32 s85, s61, 16\n"
        "s_and_b32 s61, s61, 0xffff\n"
        "s_mov_b32 s89, 241172480\n"
        "s_mov_b32 s84, 241172480\n"
        "v_mov_b32 v6, v0\n"
        "s_mov_b32 s80, 0xbfb8aa3b\n"
        "s_lshr_b32 s67, s69, 1\n"
        "s_lshr_b32 s65, s67, 1\n"
        "s_bitcmp1_b32 s54, s65\n"
        "s_cselect_b32 s65, 8192, 0\n"
        "s_lshl_b32 s77, s67, 20\n"
        "s_add_u32 s77, s77, s65\n"
        "s_add_u32 s71, s77, s58\n"
        "s_add_u32 s81, s77, s62\n"
        "s_add_u32 s78, s78, 36864\n"
        "s_cmp_eq_u32 s78, 110592\n"
        "s_cselect_b32 s78, 0, s78\n"
        "s_add_u32 m0, s78, s61\n"
        "s_lshr_b32 s70, s69, 0\n"
        "s_branch .Lf2_wait1_%=\n"
        ".p2align 6\n"
        ".p2align 5\n"
        ".Lf2_code_%=:\n"
        ".Lf2_i0_%=:\n"
        "v_mfma_f32_16x16x32_bf16 v[24:27], a[0:3], v[8:11], 0\n"
        "v_mfma_f32_16x16x32_bf16 v[28:31], a[4:7], v[8:11], 0\n"
        "s_add_u32 s71, s77, s58\n"
        "buffer_load_dword v4, v2, s[40:43], s86 offen sc1\n"
        "v_mfma_f32_16x16x32_bf16 v[32:35], a[8:11], v[8:11], 0\n"
        "s_add_u32 s71, s71, 49152\n"
        "ds_read_b128 v[8:11], v0 offset:4096\n"
        "s_add_u32 s81, s77, s62\n"
        "v_mfma_f32_16x16x32_bf16 v[24:27], a[12:15], v[12:15], v[24:27]\n"
        "s_add_u32 s81, s81, 4096\n"
        "s_add_u32 s78, s78, 36864\n"
        "v_cvt_pk_bf16_f32 v36, v36, v37\n"
        "v_mfma_f32_16x16x32_bf16 v[28:31], a[16:19], v[12:15], v[28:31]\n"
        "s_cmp_eq_u32 s78, 110592\n"
        "s_cselect_b32 s78, 0, s78\n"
        "s_add_u32 m0, s78, s61\n"
        "v_mfma_f32_16x16x32_bf16 v[32:35], a[20:23], v[12:15], v[32:35]\n"
        "s_waitcnt lgkmcnt(1)\n"
        "ds_read_b128 v[12:15], v0 offset:5120\n"
        "s_lshr_b32 s70, s69, 0\n"
        "v_mfma_f32_16x16x32_bf16 v[24:27], a[24:27], v[16:19], v[24:27]\n"
        "v_cvt_pk_bf16_f32 v37, v38, v39\n"
        "v_cvt_pk_bf16_f32 v38, v40, v41\n"
        "v_mfma_f32_16x16x32_bf16 v[28:31], a[28:31], v[16:19], v[28:31]\n"
        "v_cvt_pk_bf16_f32 v39, v42, v43\n"
        "v_mfma_f32_16x16x32_bf16 v[32:35], a[32:35], v[16:19], v[32:35]\n"
        "ds_read_b128 v[16:19], v0 offset:6144\n"
        "v_mfma_f32_16x16x32_bf16 v[24:27], a[36:39], v[20:23], v[24:27]\n"
        "buffer_store_dwordx4 v[36:39], v48, s[40:43], s72 offen offset:0 sc1\n"
        "v_mfma_f32_16x16x32_bf16 v[28:31], a[40:43], v[20:23], v[28:31]\n"
        "v_cvt_pk_bf16_f32 v44, v44, v45\n"
        "v_cvt_pk_bf16_f32 v45, v46, v47\n"
        "v_mfma_f32_16x16x32_bf16 v[32:35], a[44:47], v[20:23], v[32:35]\n"
        "s_waitcnt lgkmcnt(1)\n"
        "ds_read_b128 v[20:23], v0 offset:7168\n"
        "s_nop 0\n"
        "v_mfma_f32_16x16x32_bf16 v[24:27], a[48:51], v[8:11], v[24:27]\n"
        "v_permlane16_swap_b32 v44, v46\n"
        "v_mfma_f32_16x16x32_bf16 v[28:31], a[52:55], v[8:11], v[28:31]\n"
        "v_permlane16_swap_b32 v45, v47\n"
        "v_mfma_f32_16x16x32_bf16 v[32:35], a[56:59], v[8:11], v[32:35]\n"
        "ds_read_b128 v[8:11], v0 offset:8192\n"
        "v_mfma_f32_16x16x32_bf16 v[24:27], a[60:63], v[12:15], v[24:27]\n"
        "v_mfma_f32_16x16x32_bf16 v[28:31], a[64:67], v[12:15], v[28:31]\n"
        "buffer_store_dwordx4 v[44:47], v7, s[40:43], s72 offen offset:0 sc1\n"
        "v_mfma_f32_16x16x32_bf16 v[32:35], a[68:71], v[12:15], v[32:35]\n"
        "s_waitcnt lgkmcnt(1)\n"
        "ds_read_b128 v[12:15], v0 offset:9216\n"
        "v_mfma_f32_16x16x32_bf16 v[24:27], a[72:75], v[16:19], v[24:27]\n"
        "v_mfma_f32_16x16x32_bf16 v[28:31], a[76:79], v[16:19], v[28:31]\n"
        "v_mfma_f32_16x16x32_bf16 v[32:35], a[80:83], v[16:19], v[32:35]\n"
        "ds_read_b128 v[16:19], v0 offset:10240\n"
        "v_mfma_f32_16x16x32_bf16 v[24:27], a[84:87], v[20:23], v[24:27]\n"
        "v_mfma_f32_16x16x32_bf16 v[28:31], a[88:91], v[20:23], v[28:31]\n"
        "v_mfma_f32_16x16x32_bf16 v[32:35], a[92:95], v[20:23], v[32:35]\n"
        "s_waitcnt lgkmcnt(1)\n"
        "ds_read_b128 v[20:23], v0 offset:11264\n"
        "v_mfma_f32_16x16x32_bf16 v[24:27], a[96:99], v[8:11], v[24:27]\n"
        "v_mfma_f32_16x16x32_bf16 v[28:31], a[100:103], v[8:11], v[28:31]\n"
        "s_waitcnt vmcnt(3)\n"
        "s_barrier\n"
        "v_mfma_f32_16x16x32_bf16 v[32:35], a[104:107], v[8:11], v[32:35]\n"
        "ds_read_b128 v[8:11], v0 offset:12288\n"
        "v_mfma_f32_16x16x32_bf16 v[24:27], a[108:111], v[12:15], v[24:27]\n"
        "buffer_store_dword v5, v3, s[40:43], s84 offen sc1\n"
        "v_mfma_f32_16x16x32_bf16 v[28:31], a[112:115], v[12:15], v[28:31]\n"
        "v_mov_b32 v5, s74\n"
        "v_mfma_f32_16x16x32_bf16 v[32:35], a[116:119], v[12:15], v[32:35]\n"
        "s_waitcnt lgkmcnt(1)\n"
        "ds_read_b128 v[12:15], v0 offset:13312\n"
        "v_mfma_f32_16x16x32_bf16 v[24:27], a[120:123], v[16:19], v[24:27]\n"
        "v_mfma_f32_16x16x32_bf16 v[28:31], a[124:127], v[16:19], v[28:31]\n"
        "s_waitcnt vmcnt(3)\n"
        "v_cmp_gt_i32 vcc, s70, v4\n"
        "v_mfma_f32_16x16x32_bf16 v[32:35], a[128:131], v[16:19], v[32:35]\n"
        "ds_read_b128 v[16:19], v0 offset:14336\n"
        "v_mfma_f32_16x16x32_bf16 v[24:27], a[132:135], v[20:23], v[24:27]\n"
        "s_and_b64 vcc, vcc, 63\n"
        "v_mfma_f32_16x16x32_bf16 v[28:31], a[136:139], v[20:23], v[28:31]\n"
        "s_cbranch_vccnz .Lf2_i00_%=\n"
        "v_mfma_f32_16x16x32_bf16 v[32:35], a[140:143], v[20:23], v[32:35]\n"
        "s_waitcnt lgkmcnt(1)\n"
        "ds_read_b128 v[20:23], v0 offset:15360\n"
        "v_mfma_f32_16x16x32_bf16 v[24:27], a[144:147], v[8:11], v[24:27]\n"
        "buffer_load_dwordx4 v1, s[40:43], s71 offen offset:0 sc1 lds\n"
        "v_mfma_f32_16x16x32_bf16 v[28:31], a[148:151], v[8:11], v[28:31]\n"
        "v_mfma_f32_16x16x32_bf16 v[32:35], a[152:155], v[8:11], v[32:35]\n"
        "ds_read_b128 v[8:11], v0 offset:16384\n"
        "v_mfma_f32_16x16x32_bf16 v[24:27], a[156:159], v[12:15], v[24:27]\n"
        "buffer_load_dwordx4 v1, s[40:43], s71 offen offset:1024 sc1 lds\n"
        "v_mfma_f32_16x16x32_bf16 v[28:31], a[160:163], v[12:15], v[28:31]\n"
        "s_add_u32 m0, m0, s85\n"
        "s_add_u32 s71, s71, 16384\n"
        "v_mfma_f32_16x16x32_bf16 v[32:35], a[164:167], v[12:15], v[32:35]\n"
        "s_waitcnt lgkmcnt(1)\n"
        "ds_read_b128 v[12:15], v0 offset:17408\n"
        "v_mfma_f32_16x16x32_bf16 v[24:27], a[168:171], v[16:19], v[24:27]\n"
        "s_getreg_b32 s88, hwreg(HW_REG_IB_STS, 0, 4)\n"
        "v_mfma_f32_16x16x32_bf16 v[28:31], a[172:175], v[16:19], v[28:31]\n"
        "buffer_load_dwordx4 v1, s[40:43], s81 offen offset:0 sc1 lds\n"
        "v_mfma_f32_16x16x32_bf16 v[32:35], a[176:179], v[16:19], v[32:35]\n"
        "s_sub_u32 m0, m0, s85\n"
        "ds_read_b128 v[16:19], v0 offset:18432\n"
        "s_add_u32 m0, m0, 12288\n"
        "v_mfma_f32_16x16x32_bf16 v[24:27], a[180:183], v[20:23], v[24:27]\n"
        "s_add_u32 s81, s81, 16384\n"
        "v_mfma_f32_16x16x32_bf16 v[28:31], a[184:187], v[20:23], v[28:31]\n"
        "s_cmp_le_u32 s88, 3\n"
        "s_cselect_b32 s88, s73, s89\n"
        "s_cselect_b32 s73, s89, s73\n"
        "v_mfma_f32_16x16x32_bf16 v[32:35], a[188:191], v[20:23], v[32:35]\n"
        "s_waitcnt lgkmcnt(1)\n"
        "ds_read_b128 v[20:23], v0 offset:19456\n"
        "v_mfma_f32_16x16x32_bf16 v[24:27], a[192:195], v[8:11], v[24:27]\n"
        "buffer_load_dwordx4 v1, s[40:43], s71 offen offset:0 sc1 lds\n"
        "v_mfma_f32_16x16x32_bf16 v[28:31], a[196:199], v[8:11], v[28:31]\n"
        "buffer_store_dword v5, v3, s[40:43], s88 offen sc1\n"
        "v_mfma_f32_16x16x32_bf16 v[32:35], a[200:203], v[8:11], v[32:35]\n"
        "s_getreg_b32 s88, hwreg(HW_REG_IB_STS, 0, 4)\n"
        "ds_read_b128 v[8:11], v0 offset:20480\n"
        "v_mfma_f32_16x16x32_bf16 v[24:27], a[204:207], v[12:15], v[24:27]\n"
        "buffer_load_dwordx4 v1, s[40:43], s71 offen offset:1024 sc1 lds\n"
        "v_mfma_f32_16x16x32_bf16 v[28:31], a[208:211], v[12:15], v[28:31]\n"
        "s_add_u32 m0, m0, s85\n"
        "s_add_u32 s71, s71, 16384\n"
        "v_mfma_f32_16x16x32_bf16 v[32:35], a[212:215], v[12:15], v[32:35]\n"
        "s_waitcnt lgkmcnt(1)\n"
        "ds_read_b128 v[12:15], v0 offset:21504\n"
        "v_mfma_f32_16x16x32_bf16 v[24:27], a[216:219], v[16:19], v[24:27]\n"
        "s_cmp_le_u32 s88, 6\n"
        "s_cselect_b32 s88, s73, s89\n"
        "s_cselect_b32 s73, s89, s73\n"
        "v_mfma_f32_16x16x32_bf16 v[28:31], a[220:223], v[16:19], v[28:31]\n"
        "buffer_load_dwordx4 v1, s[40:43], s81 offen offset:0 sc1 lds\n"
        "v_mfma_f32_16x16x32_bf16 v[32:35], a[224:227], v[16:19], v[32:35]\n"
        "s_sub_u32 m0, m0, s85\n"
        "ds_read_b128 v[16:19], v0 offset:22528\n"
        "s_add_u32 m0, m0, 12288\n"
        "v_mfma_f32_16x16x32_bf16 v[24:27], a[228:231], v[20:23], v[24:27]\n"
        "s_add_u32 s81, s81, 16384\n"
        "buffer_store_dword v5, v3, s[40:43], s88 offen sc1\n"
        "v_mfma_f32_16x16x32_bf16 v[28:31], a[232:235], v[20:23], v[28:31]\n"
        "s_getreg_b32 s88, hwreg(HW_REG_IB_STS, 0, 4)\n"
        "v_mfma_f32_16x16x32_bf16 v[32:35], a[236:239], v[20:23], v[32:35]\n"
        "s_waitcnt lgkmcnt(1)\n"
        "ds_read_b128 v[20:23], v0 offset:23552\n"
        "v_mfma_f32_16x16x32_bf16 v[24:27], a[240:243], v[8:11], v[24:27]\n"
        "buffer_load_dwordx4 v1, s[40:43], s71 offen offset:0 sc1 lds\n"
        "v_mfma_f32_16x16x32_bf16 v[28:31], a[244:247], v[8:11], v[28:31]\n"
        "s_cmp_le_u32 s88, 9\n"
        "s_cselect_b32 s88, s73, s89\n"
        "s_cselect_b32 s73, s89, s73\n"
        "v_mfma_f32_16x16x32_bf16 v[32:35], a[248:251], v[8:11], v[32:35]\n"
        "ds_read_b128 v[8:11], v0 offset:24576\n"
        "v_mfma_f32_16x16x32_bf16 v[24:27], a[252:255], v[12:15], v[24:27]\n"
        "buffer_load_dwordx4 v1, s[40:43], s71 offen offset:1024 sc1 lds\n"
        "v_mfma_f32_16x16x32_bf16 v[28:31], v[80:83], v[12:15], v[28:31]\n"
        "s_add_u32 m0, m0, s85\n"
        "s_add_u32 s71, s71, 16384\n"
        "v_mfma_f32_16x16x32_bf16 v[32:35], v[84:87], v[12:15], v[32:35]\n"
        "s_waitcnt lgkmcnt(1)\n"
        "ds_read_b128 v[12:15], v0 offset:25600\n"
        "v_mfma_f32_16x16x32_bf16 v[24:27], v[88:91], v[16:19], v[24:27]\n"
        "buffer_store_dword v5, v3, s[40:43], s88 offen sc1\n"
        "v_mfma_f32_16x16x32_bf16 v[28:31], v[92:95], v[16:19], v[28:31]\n"
        "buffer_load_dwordx4 v1, s[40:43], s81 offen offset:0 sc1 lds\n"
        "v_mfma_f32_16x16x32_bf16 v[32:35], v[96:99], v[16:19], v[32:35]\n"
        "s_add_u32 s69, s69, 1\n"
        "ds_read_b128 v[16:19], v0 offset:26624\n"
        "v_mfma_f32_16x16x32_bf16 v[24:27], v[100:103], v[20:23], v[24:27]\n"
        "v_mfma_f32_16x16x32_bf16 v[28:31], v[104:107], v[20:23], v[28:31]\n"
        "s_waitcnt vmcnt(13)\n"
        "buffer_store_dword v5, v3, s[40:43], s73 offen sc1\n"
        "v_mfma_f32_16x16x32_bf16 v[32:35], v[108:111], v[20:23], v[32:35]\n"
        "s_waitcnt lgkmcnt(1)\n"
        "ds_read_b128 v[20:23], v0 offset:27648\n"
        "s_mov_b32 s84, s89\n"
        "v_mfma_f32_16x16x32_bf16 v[24:27], v[112:115], v[8:11], v[24:27]\n"
        "s_mov_b32 s73, s60\n"
        "s_add_u32 s74, s74, 1\n"
        "s_add_u32 s68, s68, 1\n"
        "v_mfma_f32_16x16x32_bf16 v[28:31], v[116:119], v[8:11], v[28:31]\n"
        "s_lshr_b32 s66, s68, 2\n"
        "s_add_u32 s66, s66, 1\n"
        "v_mfma_f32_16x16x32_bf16 v[32:35], v[120:123], v[8:11], v[32:35]\n"
        "s_bitcmp1_b32 s55, s66\n"
        "s_cselect_b32 s66, 8192, 0\n"
        "ds_read_b128 v[8:11], v0 offset:28672\n"
        "v_mfma_f32_16x16x32_bf16 v[24:27], v[124:127], v[12:15], v[24:27]\n"
        "s_lshl_b32 s75, s68, 19\n"
        "s_add_u32 s75, s75, s66\n"
        "s_add_u32 s72, s75, s59\n"
        "v_mfma_f32_16x16x32_bf16 v[28:31], v[128:131], v[12:15], v[28:31]\n"
        "v_mfma_f32_16x16x32_bf16 v[32:35], v[132:135], v[12:15], v[32:35]\n"
        "s_waitcnt lgkmcnt(1)\n"
        "ds_read_b128 v[12:15], v0 offset:29696\n"
        "v_mfma_f32_16x16x32_bf16 v[24:27], v[136:139], v[16:19], v[24:27]\n"
        "v_mfma_f32_16x16x32_bf16 v[28:31], v[140:143], v[16:19], v[28:31]\n"
        "v_mfma_f32_16x16x32_bf16 v[32:35], v[144:147], v[16:19], v[32:35]\n"
        "ds_read_b128 v[16:19], v0 offset:30720\n"
        "v_mfma_f32_16x16x32_bf16 v[24:27], v[148:151], v[20:23], v[24:27]\n"
        "v_mfma_f32_16x16x32_bf16 v[28:31], v[152:155], v[20:23], v[28:31]\n"
        "v_mfma_f32_16x16x32_bf16 v[32:35], v[156:159], v[20:23], v[32:35]\n"
        "s_waitcnt lgkmcnt(1)\n"
        "ds_read_b128 v[20:23], v0 offset:31744\n"
        "v_mfma_f32_16x16x32_bf16 v[24:27], v[160:163], v[8:11], v[24:27]\n"
        "v_mfma_f32_16x16x32_bf16 v[28:31], v[164:167], v[8:11], v[28:31]\n"
        "v_mfma_f32_16x16x32_bf16 v[32:35], v[168:171], v[8:11], v[32:35]\n"
        "ds_read_b128 v[8:11], v0 offset:32768\n"
        "v_mfma_f32_16x16x32_bf16 v[24:27], v[172:175], v[12:15], v[24:27]\n"
        "v_mfma_f32_16x16x32_bf16 v[28:31], v[176:179], v[12:15], v[28:31]\n"
        "v_mfma_f32_16x16x32_bf16 v[32:35], v[180:183], v[12:15], v[32:35]\n"
        "s_waitcnt lgkmcnt(1)\n"
        "ds_read_b128 v[12:15], v0 offset:33792\n"
        "v_mfma_f32_16x16x32_bf16 v[24:27], v[184:187], v[16:19], v[24:27]\n"
        "v_mfma_f32_16x16x32_bf16 v[28:31], v[188:191], v[16:19], v[28:31]\n"
        "v_mfma_f32_16x16x32_bf16 v[32:35], v[192:195], v[16:19], v[32:35]\n"
        "ds_read_b128 v[16:19], v0 offset:34816\n"
        "v_mfma_f32_16x16x32_bf16 v[24:27], v[196:199], v[20:23], v[24:27]\n"
        "v_mfma_f32_16x16x32_bf16 v[28:31], v[200:203], v[20:23], v[28:31]\n"
        "v_mfma_f32_16x16x32_bf16 v[32:35], v[204:207], v[20:23], v[32:35]\n"
        "s_waitcnt lgkmcnt(1)\n"
        "ds_read_b128 v[20:23], v0 offset:35840\n"
        "v_mfma_f32_16x16x32_bf16 v[24:27], v[208:211], v[8:11], v[24:27]\n"
        "v_mfma_f32_16x16x32_bf16 v[28:31], v[212:215], v[8:11], v[28:31]\n"
        "s_waitcnt vmcnt(10)\n"
        "s_barrier\n"
        "v_add_u32 v0, s78, v6\n"
        "v_mfma_f32_16x16x32_bf16 v[32:35], v[216:219], v[8:11], v[32:35]\n"
        "ds_read_b128 v[8:11], v0 offset:0\n"
        "v_mfma_f32_16x16x32_bf16 v[24:27], v[220:223], v[12:15], v[24:27]\n"
        "v_mfma_f32_16x16x32_bf16 v[28:31], v[224:227], v[12:15], v[28:31]\n"
        "v_mfma_f32_16x16x32_bf16 v[32:35], v[228:231], v[12:15], v[32:35]\n"
        "s_waitcnt lgkmcnt(1)\n"
        "ds_read_b128 v[12:15], v0 offset:1024\n"
        "v_mfma_f32_16x16x32_bf16 v[24:27], v[232:235], v[16:19], v[24:27]\n"
        "v_mfma_f32_16x16x32_bf16 v[28:31], v[236:239], v[16:19], v[28:31]\n"
        "v_mfma_f32_16x16x32_bf16 v[32:35], v[240:243], v[16:19], v[32:35]\n"
        "ds_read_b128 v[16:19], v0 offset:2048\n"
        "v_mfma_f32_16x16x32_bf16 v[24:27], v[244:247], v[20:23], v[24:27]\n"
        "v_mfma_f32_16x16x32_bf16 v[28:31], v[248:251], v[20:23], v[28:31]\n"
        "v_mfma_f32_16x16x32_bf16 v[32:35], v[252:255], v[20:23], v[32:35]\n"
        "s_waitcnt lgkmcnt(1)\n"
        "ds_read_b128 v[20:23], v0 offset:3072\n"
        "s_branch .Lf2_i1_%=\n"
        ".p2align 5\n"
        ".Lf2_i00_%=:\n"
        "v_mfma_f32_16x16x32_bf16 v[32:35], a[140:143], v[20:23], v[32:35]\n"
        "s_waitcnt lgkmcnt(1)\n"
        "ds_read_b128 v[20:23], v0 offset:15360\n"
        "v_mfma_f32_16x16x32_bf16 v[24:27], a[144:147], v[8:11], v[24:27]\n"
        "buffer_load_dword v4, v2, s[40:43], s86 offen sc1\n"
        "v_mfma_f32_16x16x32_bf16 v[28:31], a[148:151], v[8:11], v[28:31]\n"
        "v_mfma_f32_16x16x32_bf16 v[32:35], a[152:155], v[8:11], v[32:35]\n"
        "ds_read_b128 v[8:11], v0 offset:16384\n"
        "v_mfma_f32_16x16x32_bf16 v[24:27], a[156:159], v[12:15], v[24:27]\n"
        "v_mfma_f32_16x16x32_bf16 v[28:31], a[160:163], v[12:15], v[28:31]\n"
        "s_getreg_b32 s88, hwreg(HW_REG_IB_STS, 0, 4)\n"
        "v_mfma_f32_16x16x32_bf16 v[32:35], a[164:167], v[12:15], v[32:35]\n"
        "s_waitcnt lgkmcnt(1)\n"
        "ds_read_b128 v[12:15], v0 offset:17408\n"
        "v_mfma_f32_16x16x32_bf16 v[24:27], a[168:171], v[16:19], v[24:27]\n"
        "s_cmp_le_u32 s88, 2\n"
        "s_cselect_b32 s88, s73, s89\n"
        "s_cselect_b32 s73, s89, s73\n"
        "v_mfma_f32_16x16x32_bf16 v[28:31], a[172:175], v[16:19], v[28:31]\n"
        "buffer_store_dword v5, v3, s[40:43], s88 offen sc1\n"
        "v_mfma_f32_16x16x32_bf16 v[32:35], a[176:179], v[16:19], v[32:35]\n"
        "ds_read_b128 v[16:19], v0 offset:18432\n"
        "v_mfma_f32_16x16x32_bf16 v[24:27], a[180:183], v[20:23], v[24:27]\n"
        "v_mfma_f32_16x16x32_bf16 v[28:31], a[184:187], v[20:23], v[28:31]\n"
        "v_mfma_f32_16x16x32_bf16 v[32:35], a[188:191], v[20:23], v[32:35]\n"
        "s_waitcnt lgkmcnt(1)\n"
        "ds_read_b128 v[20:23], v0 offset:19456\n"
        "v_mfma_f32_16x16x32_bf16 v[24:27], a[192:195], v[8:11], v[24:27]\n"
        "s_getreg_b32 s88, hwreg(HW_REG_IB_STS, 0, 4)\n"
        "v_mfma_f32_16x16x32_bf16 v[28:31], a[196:199], v[8:11], v[28:31]\n"
        "s_cmp_le_u32 s88, 3\n"
        "s_cselect_b32 s88, s73, s89\n"
        "s_cselect_b32 s73, s89, s73\n"
        "v_mfma_f32_16x16x32_bf16 v[32:35], a[200:203], v[8:11], v[32:35]\n"
        "ds_read_b128 v[8:11], v0 offset:20480\n"
        "v_mfma_f32_16x16x32_bf16 v[24:27], a[204:207], v[12:15], v[24:27]\n"
        "buffer_store_dword v5, v3, s[40:43], s88 offen sc1\n"
        "v_mfma_f32_16x16x32_bf16 v[28:31], a[208:211], v[12:15], v[28:31]\n"
        "v_mfma_f32_16x16x32_bf16 v[32:35], a[212:215], v[12:15], v[32:35]\n"
        "s_waitcnt lgkmcnt(1)\n"
        "ds_read_b128 v[12:15], v0 offset:21504\n"
        "v_mfma_f32_16x16x32_bf16 v[24:27], a[216:219], v[16:19], v[24:27]\n"
        "v_mfma_f32_16x16x32_bf16 v[28:31], a[220:223], v[16:19], v[28:31]\n"
        "v_mfma_f32_16x16x32_bf16 v[32:35], a[224:227], v[16:19], v[32:35]\n"
        "s_getreg_b32 s88, hwreg(HW_REG_IB_STS, 0, 4)\n"
        "ds_read_b128 v[16:19], v0 offset:22528\n"
        "v_mfma_f32_16x16x32_bf16 v[24:27], a[228:231], v[20:23], v[24:27]\n"
        "s_cmp_le_u32 s88, 4\n"
        "s_cselect_b32 s88, s73, s89\n"
        "s_cselect_b32 s73, s89, s73\n"
        "v_mfma_f32_16x16x32_bf16 v[28:31], a[232:235], v[20:23], v[28:31]\n"
        "buffer_store_dword v5, v3, s[40:43], s88 offen sc1\n"
        "v_mfma_f32_16x16x32_bf16 v[32:35], a[236:239], v[20:23], v[32:35]\n"
        "s_waitcnt lgkmcnt(1)\n"
        "ds_read_b128 v[20:23], v0 offset:23552\n"
        "v_mfma_f32_16x16x32_bf16 v[24:27], a[240:243], v[8:11], v[24:27]\n"
        "v_mfma_f32_16x16x32_bf16 v[28:31], a[244:247], v[8:11], v[28:31]\n"
        "s_waitcnt vmcnt(3)\n"
        "v_cmp_gt_i32 vcc, s70, v4\n"
        "v_mfma_f32_16x16x32_bf16 v[32:35], a[248:251], v[8:11], v[32:35]\n"
        "ds_read_b128 v[8:11], v0 offset:24576\n"
        "v_mfma_f32_16x16x32_bf16 v[24:27], a[252:255], v[12:15], v[24:27]\n"
        "s_and_b64 vcc, vcc, 63\n"
        "v_mfma_f32_16x16x32_bf16 v[28:31], v[80:83], v[12:15], v[28:31]\n"
        "s_cbranch_vccnz .Lf2_i01_%=\n"
        "v_mfma_f32_16x16x32_bf16 v[32:35], v[84:87], v[12:15], v[32:35]\n"
        "s_waitcnt lgkmcnt(1)\n"
        "ds_read_b128 v[12:15], v0 offset:25600\n"
        "v_mfma_f32_16x16x32_bf16 v[24:27], v[88:91], v[16:19], v[24:27]\n"
        "buffer_load_dwordx4 v1, s[40:43], s71 offen offset:0 sc1 lds\n"
        "v_mfma_f32_16x16x32_bf16 v[28:31], v[92:95], v[16:19], v[28:31]\n"
        "v_mfma_f32_16x16x32_bf16 v[32:35], v[96:99], v[16:19], v[32:35]\n"
        "ds_read_b128 v[16:19], v0 offset:26624\n"
        "v_mfma_f32_16x16x32_bf16 v[24:27], v[100:103], v[20:23], v[24:27]\n"
        "buffer_load_dwordx4 v1, s[40:43], s71 offen offset:1024 sc1 lds\n"
        "v_mfma_f32_16x16x32_bf16 v[28:31], v[104:107], v[20:23], v[28:31]\n"
        "s_add_u32 m0, m0, s85\n"
        "s_add_u32 s71, s71, 16384\n"
        "v_mfma_f32_16x16x32_bf16 v[32:35], v[108:111], v[20:23], v[32:35]\n"
        "s_waitcnt lgkmcnt(1)\n"
        "ds_read_b128 v[20:23], v0 offset:27648\n"
        "v_mfma_f32_16x16x32_bf16 v[24:27], v[112:115], v[8:11], v[24:27]\n"
        "buffer_load_dwordx4 v1, s[40:43], s81 offen offset:0 sc1 lds\n"
        "v_mfma_f32_16x16x32_bf16 v[28:31], v[116:119], v[8:11], v[28:31]\n"
        "s_sub_u32 m0, m0, s85\n"
        "s_add_u32 m0, m0, 12288\n"
        "s_add_u32 s81, s81, 16384\n"
        "v_mfma_f32_16x16x32_bf16 v[32:35], v[120:123], v[8:11], v[32:35]\n"
        "ds_read_b128 v[8:11], v0 offset:28672\n"
        "v_mfma_f32_16x16x32_bf16 v[24:27], v[124:127], v[12:15], v[24:27]\n"
        "buffer_load_dwordx4 v1, s[40:43], s71 offen offset:0 sc1 lds\n"
        "v_mfma_f32_16x16x32_bf16 v[28:31], v[128:131], v[12:15], v[28:31]\n"
        "buffer_store_dword v5, v3, s[40:43], s73 offen sc1\n"
        "v_mfma_f32_16x16x32_bf16 v[32:35], v[132:135], v[12:15], v[32:35]\n"
        "s_waitcnt lgkmcnt(1)\n"
        "ds_read_b128 v[12:15], v0 offset:29696\n"
        "s_mov_b32 s84, s89\n"
        "v_mfma_f32_16x16x32_bf16 v[24:27], v[136:139], v[16:19], v[24:27]\n"
        "s_mov_b32 s73, s60\n"
        "buffer_load_dwordx4 v1, s[40:43], s71 offen offset:1024 sc1 lds\n"
        "v_mfma_f32_16x16x32_bf16 v[28:31], v[140:143], v[16:19], v[28:31]\n"
        "s_add_u32 m0, m0, s85\n"
        "s_add_u32 s71, s71, 16384\n"
        "s_add_u32 s74, s74, 1\n"
        "v_mfma_f32_16x16x32_bf16 v[32:35], v[144:147], v[16:19], v[32:35]\n"
        "s_add_u32 s68, s68, 1\n"
        "ds_read_b128 v[16:19], v0 offset:30720\n"
        "s_lshr_b32 s66, s68, 2\n"
        "v_mfma_f32_16x16x32_bf16 v[24:27], v[148:151], v[20:23], v[24:27]\n"
        "s_add_u32 s66, s66, 1\n"
        "s_bitcmp1_b32 s55, s66\n"
        "s_cselect_b32 s66, 8192, 0\n"
        "v_mfma_f32_16x16x32_bf16 v[28:31], v[152:155], v[20:23], v[28:31]\n"
        "s_lshl_b32 s75, s68, 19\n"
        "s_add_u32 s75, s75, s66\n"
        "s_add_u32 s72, s75, s59\n"
        "v_mfma_f32_16x16x32_bf16 v[32:35], v[156:159], v[20:23], v[32:35]\n"
        "s_waitcnt lgkmcnt(1)\n"
        "ds_read_b128 v[20:23], v0 offset:31744\n"
        "v_mfma_f32_16x16x32_bf16 v[24:27], v[160:163], v[8:11], v[24:27]\n"
        "v_mfma_f32_16x16x32_bf16 v[28:31], v[164:167], v[8:11], v[28:31]\n"
        "buffer_load_dwordx4 v1, s[40:43], s81 offen offset:0 sc1 lds\n"
        "v_mfma_f32_16x16x32_bf16 v[32:35], v[168:171], v[8:11], v[32:35]\n"
        "s_sub_u32 m0, m0, s85\n"
        "ds_read_b128 v[8:11], v0 offset:32768\n"
        "s_add_u32 m0, m0, 12288\n"
        "v_mfma_f32_16x16x32_bf16 v[24:27], v[172:175], v[12:15], v[24:27]\n"
        "s_add_u32 s81, s81, 16384\n"
        "v_mfma_f32_16x16x32_bf16 v[28:31], v[176:179], v[12:15], v[28:31]\n"
        "v_mfma_f32_16x16x32_bf16 v[32:35], v[180:183], v[12:15], v[32:35]\n"
        "s_waitcnt lgkmcnt(1)\n"
        "ds_read_b128 v[12:15], v0 offset:33792\n"
        "v_mfma_f32_16x16x32_bf16 v[24:27], v[184:187], v[16:19], v[24:27]\n"
        "buffer_load_dwordx4 v1, s[40:43], s71 offen offset:0 sc1 lds\n"
        "v_mfma_f32_16x16x32_bf16 v[28:31], v[188:191], v[16:19], v[28:31]\n"
        "v_mfma_f32_16x16x32_bf16 v[32:35], v[192:195], v[16:19], v[32:35]\n"
        "ds_read_b128 v[16:19], v0 offset:34816\n"
        "v_mfma_f32_16x16x32_bf16 v[24:27], v[196:199], v[20:23], v[24:27]\n"
        "buffer_load_dwordx4 v1, s[40:43], s71 offen offset:1024 sc1 lds\n"
        "v_mfma_f32_16x16x32_bf16 v[28:31], v[200:203], v[20:23], v[28:31]\n"
        "s_add_u32 m0, m0, s85\n"
        "s_add_u32 s71, s71, 16384\n"
        "v_mfma_f32_16x16x32_bf16 v[32:35], v[204:207], v[20:23], v[32:35]\n"
        "s_waitcnt lgkmcnt(1)\n"
        "ds_read_b128 v[20:23], v0 offset:35840\n"
        "v_mfma_f32_16x16x32_bf16 v[24:27], v[208:211], v[8:11], v[24:27]\n"
        "v_mfma_f32_16x16x32_bf16 v[28:31], v[212:215], v[8:11], v[28:31]\n"
        "s_waitcnt vmcnt(6)\n"
        "s_barrier\n"
        "v_add_u32 v0, s78, v6\n"
        "v_mfma_f32_16x16x32_bf16 v[32:35], v[216:219], v[8:11], v[32:35]\n"
        "ds_read_b128 v[8:11], v0 offset:0\n"
        "v_mfma_f32_16x16x32_bf16 v[24:27], v[220:223], v[12:15], v[24:27]\n"
        "buffer_load_dwordx4 v1, s[40:43], s81 offen offset:0 sc1 lds\n"
        "v_mfma_f32_16x16x32_bf16 v[28:31], v[224:227], v[12:15], v[28:31]\n"
        "s_add_u32 s69, s69, 1\n"
        "v_mfma_f32_16x16x32_bf16 v[32:35], v[228:231], v[12:15], v[32:35]\n"
        "s_waitcnt lgkmcnt(1)\n"
        "ds_read_b128 v[12:15], v0 offset:1024\n"
        "v_mfma_f32_16x16x32_bf16 v[24:27], v[232:235], v[16:19], v[24:27]\n"
        "v_mfma_f32_16x16x32_bf16 v[28:31], v[236:239], v[16:19], v[28:31]\n"
        "v_mfma_f32_16x16x32_bf16 v[32:35], v[240:243], v[16:19], v[32:35]\n"
        "ds_read_b128 v[16:19], v0 offset:2048\n"
        "v_mfma_f32_16x16x32_bf16 v[24:27], v[244:247], v[20:23], v[24:27]\n"
        "v_mfma_f32_16x16x32_bf16 v[28:31], v[248:251], v[20:23], v[28:31]\n"
        "v_mfma_f32_16x16x32_bf16 v[32:35], v[252:255], v[20:23], v[32:35]\n"
        "s_waitcnt lgkmcnt(1)\n"
        "ds_read_b128 v[20:23], v0 offset:3072\n"
        "s_branch .Lf2_i1_%=\n"
        ".p2align 5\n"
        ".Lf2_i01_%=:\n"
        "v_mfma_f32_16x16x32_bf16 v[32:35], v[84:87], v[12:15], v[32:35]\n"
        "s_waitcnt lgkmcnt(1)\n"
        "ds_read_b128 v[12:15], v0 offset:25600\n"
        "v_mfma_f32_16x16x32_bf16 v[24:27], v[88:91], v[16:19], v[24:27]\n"
        "buffer_load_dword v4, v2, s[40:43], s86 offen sc1\n"
        "v_mfma_f32_16x16x32_bf16 v[28:31], v[92:95], v[16:19], v[28:31]\n"
        "v_mfma_f32_16x16x32_bf16 v[32:35], v[96:99], v[16:19], v[32:35]\n"
        "ds_read_b128 v[16:19], v0 offset:26624\n"
        "v_mfma_f32_16x16x32_bf16 v[24:27], v[100:103], v[20:23], v[24:27]\n"
        "v_mfma_f32_16x16x32_bf16 v[28:31], v[104:107], v[20:23], v[28:31]\n"
        "buffer_store_dword v5, v3, s[40:43], s73 offen sc1\n"
        "v_mfma_f32_16x16x32_bf16 v[32:35], v[108:111], v[20:23], v[32:35]\n"
        "s_waitcnt lgkmcnt(1)\n"
        "ds_read_b128 v[20:23], v0 offset:27648\n"
        "s_mov_b32 s84, s89\n"
        "v_mfma_f32_16x16x32_bf16 v[24:27], v[112:115], v[8:11], v[24:27]\n"
        "s_mov_b32 s73, s60\n"
        "s_add_u32 s74, s74, 1\n"
        "s_add_u32 s68, s68, 1\n"
        "v_mfma_f32_16x16x32_bf16 v[28:31], v[116:119], v[8:11], v[28:31]\n"
        "s_lshr_b32 s66, s68, 2\n"
        "s_add_u32 s66, s66, 1\n"
        "v_mfma_f32_16x16x32_bf16 v[32:35], v[120:123], v[8:11], v[32:35]\n"
        "s_bitcmp1_b32 s55, s66\n"
        "s_cselect_b32 s66, 8192, 0\n"
        "ds_read_b128 v[8:11], v0 offset:28672\n"
        "v_mfma_f32_16x16x32_bf16 v[24:27], v[124:127], v[12:15], v[24:27]\n"
        "s_lshl_b32 s75, s68, 19\n"
        "s_add_u32 s75, s75, s66\n"
        "s_add_u32 s72, s75, s59\n"
        "v_mfma_f32_16x16x32_bf16 v[28:31], v[128:131], v[12:15], v[28:31]\n"
        "v_mfma_f32_16x16x32_bf16 v[32:35], v[132:135], v[12:15], v[32:35]\n"
        "s_waitcnt lgkmcnt(1)\n"
        "ds_read_b128 v[12:15], v0 offset:29696\n"
        "v_mfma_f32_16x16x32_bf16 v[24:27], v[136:139], v[16:19], v[24:27]\n"
        "v_mfma_f32_16x16x32_bf16 v[28:31], v[140:143], v[16:19], v[28:31]\n"
        "v_mfma_f32_16x16x32_bf16 v[32:35], v[144:147], v[16:19], v[32:35]\n"
        "ds_read_b128 v[16:19], v0 offset:30720\n"
        "v_mfma_f32_16x16x32_bf16 v[24:27], v[148:151], v[20:23], v[24:27]\n"
        "v_mfma_f32_16x16x32_bf16 v[28:31], v[152:155], v[20:23], v[28:31]\n"
        "v_mfma_f32_16x16x32_bf16 v[32:35], v[156:159], v[20:23], v[32:35]\n"
        "s_waitcnt lgkmcnt(1)\n"
        "ds_read_b128 v[20:23], v0 offset:31744\n"
        "v_mfma_f32_16x16x32_bf16 v[24:27], v[160:163], v[8:11], v[24:27]\n"
        "v_mfma_f32_16x16x32_bf16 v[28:31], v[164:167], v[8:11], v[28:31]\n"
        "v_mfma_f32_16x16x32_bf16 v[32:35], v[168:171], v[8:11], v[32:35]\n"
        "ds_read_b128 v[8:11], v0 offset:32768\n"
        "v_mfma_f32_16x16x32_bf16 v[24:27], v[172:175], v[12:15], v[24:27]\n"
        "v_mfma_f32_16x16x32_bf16 v[28:31], v[176:179], v[12:15], v[28:31]\n"
        "v_mfma_f32_16x16x32_bf16 v[32:35], v[180:183], v[12:15], v[32:35]\n"
        "s_waitcnt lgkmcnt(1)\n"
        "ds_read_b128 v[12:15], v0 offset:33792\n"
        "v_mfma_f32_16x16x32_bf16 v[24:27], v[184:187], v[16:19], v[24:27]\n"
        "v_mfma_f32_16x16x32_bf16 v[28:31], v[188:191], v[16:19], v[28:31]\n"
        "v_mfma_f32_16x16x32_bf16 v[32:35], v[192:195], v[16:19], v[32:35]\n"
        "ds_read_b128 v[16:19], v0 offset:34816\n"
        "v_mfma_f32_16x16x32_bf16 v[24:27], v[196:199], v[20:23], v[24:27]\n"
        "s_waitcnt vmcnt(1)\n"
        "v_cmp_gt_i32 vcc, s70, v4\n"
        "v_mfma_f32_16x16x32_bf16 v[28:31], v[200:203], v[20:23], v[28:31]\n"
        "s_nop 0\n"
        "v_mfma_f32_16x16x32_bf16 v[32:35], v[204:207], v[20:23], v[32:35]\n"
        "s_waitcnt lgkmcnt(1)\n"
        "ds_read_b128 v[20:23], v0 offset:35840\n"
        "s_and_b64 vcc, vcc, 63\n"
        "v_mfma_f32_16x16x32_bf16 v[24:27], v[208:211], v[8:11], v[24:27]\n"
        "s_cbranch_vccnz .Lf2_i02_%=\n"
        "v_mfma_f32_16x16x32_bf16 v[28:31], v[212:215], v[8:11], v[28:31]\n"
        "buffer_load_dwordx4 v1, s[40:43], s71 offen offset:0 sc1 lds\n"
        "v_mfma_f32_16x16x32_bf16 v[32:35], v[216:219], v[8:11], v[32:35]\n"
        "v_mfma_f32_16x16x32_bf16 v[24:27], v[220:223], v[12:15], v[24:27]\n"
        "buffer_load_dwordx4 v1, s[40:43], s71 offen offset:1024 sc1 lds\n"
        "v_mfma_f32_16x16x32_bf16 v[28:31], v[224:227], v[12:15], v[28:31]\n"
        "s_add_u32 m0, m0, s85\n"
        "s_add_u32 s71, s71, 16384\n"
        "v_mfma_f32_16x16x32_bf16 v[32:35], v[228:231], v[12:15], v[32:35]\n"
        "s_waitcnt lgkmcnt(0)\n"
        "buffer_load_dwordx4 v1, s[40:43], s81 offen offset:0 sc1 lds\n"
        "v_mfma_f32_16x16x32_bf16 v[24:27], v[232:235], v[16:19], v[24:27]\n"
        "s_sub_u32 m0, m0, s85\n"
        "s_add_u32 m0, m0, 12288\n"
        "s_add_u32 s81, s81, 16384\n"
        "v_mfma_f32_16x16x32_bf16 v[28:31], v[236:239], v[16:19], v[28:31]\n"
        "buffer_load_dwordx4 v1, s[40:43], s71 offen offset:0 sc1 lds\n"
        "v_mfma_f32_16x16x32_bf16 v[32:35], v[240:243], v[16:19], v[32:35]\n"
        "v_mfma_f32_16x16x32_bf16 v[24:27], v[244:247], v[20:23], v[24:27]\n"
        "buffer_load_dwordx4 v1, s[40:43], s71 offen offset:1024 sc1 lds\n"
        "v_mfma_f32_16x16x32_bf16 v[28:31], v[248:251], v[20:23], v[28:31]\n"
        "s_add_u32 m0, m0, s85\n"
        "s_add_u32 s71, s71, 16384\n"
        "v_mfma_f32_16x16x32_bf16 v[32:35], v[252:255], v[20:23], v[32:35]\n"
        "buffer_load_dwordx4 v1, s[40:43], s81 offen offset:0 sc1 lds\n"
        "s_sub_u32 m0, m0, s85\n"
        "s_add_u32 m0, m0, 12288\n"
        "s_add_u32 s81, s81, 16384\n"
        "buffer_load_dwordx4 v1, s[40:43], s71 offen offset:0 sc1 lds\n"
        "buffer_load_dwordx4 v1, s[40:43], s71 offen offset:1024 sc1 lds\n"
        "s_add_u32 m0, m0, s85\n"
        "s_add_u32 s71, s71, 16384\n"
        "buffer_load_dwordx4 v1, s[40:43], s81 offen offset:0 sc1 lds\n"
        "s_add_u32 s69, s69, 1\n"
        "s_nop 2\n"
        "v_cvt_pk_bf16_f32 v24, v24, v25\n"
        "v_cvt_pk_bf16_f32 v25, v26, v27\n"
        "v_cvt_pk_bf16_f32 v26, v28, v29\n"
        "v_cvt_pk_bf16_f32 v27, v30, v31\n"
        "buffer_store_dwordx4 v[24:27], v48, s[40:43], s72 offen offset:0 sc1\n"
        "v_cvt_pk_bf16_f32 v32, v32, v33\n"
        "v_cvt_pk_bf16_f32 v33, v34, v35\n"
        "s_nop 0\n"
        "v_permlane16_swap_b32 v32, v34\n"
        "v_permlane16_swap_b32 v33, v35\n"
        "buffer_store_dwordx4 v[32:35], v7, s[40:43], s72 offen offset:0 sc1\n"
        "v_mov_b32 v5, s74\n"
        "s_mov_b32 s84, s60\n"
        "s_waitcnt vmcnt(8)\n"
        "s_branch .Lf2_go0_%=\n"
        ".p2align 5\n"
        ".Lf2_i02_%=:\n"
        "v_mfma_f32_16x16x32_bf16 v[28:31], v[212:215], v[8:11], v[28:31]\n"
        "buffer_load_dword v4, v2, s[40:43], s86 offen sc1\n"
        "v_mfma_f32_16x16x32_bf16 v[32:35], v[216:219], v[8:11], v[32:35]\n"
        "v_mfma_f32_16x16x32_bf16 v[24:27], v[220:223], v[12:15], v[24:27]\n"
        "v_mfma_f32_16x16x32_bf16 v[28:31], v[224:227], v[12:15], v[28:31]\n"
        "v_mfma_f32_16x16x32_bf16 v[32:35], v[228:231], v[12:15], v[32:35]\n"
        "s_waitcnt lgkmcnt(0)\n"
        "v_mfma_f32_16x16x32_bf16 v[24:27], v[232:235], v[16:19], v[24:27]\n"
        "v_mfma_f32_16x16x32_bf16 v[28:31], v[236:239], v[16:19], v[28:31]\n"
        "v_mfma_f32_16x16x32_bf16 v[32:35], v[240:243], v[16:19], v[32:35]\n"
        "v_mfma_f32_16x16x32_bf16 v[24:27], v[244:247], v[20:23], v[24:27]\n"
        "v_mfma_f32_16x16x32_bf16 v[28:31], v[248:251], v[20:23], v[28:31]\n"
        "v_mfma_f32_16x16x32_bf16 v[32:35], v[252:255], v[20:23], v[32:35]\n"
        "s_nop 2\n"
        "v_cvt_pk_bf16_f32 v24, v24, v25\n"
        "v_cvt_pk_bf16_f32 v25, v26, v27\n"
        "v_cvt_pk_bf16_f32 v26, v28, v29\n"
        "v_cvt_pk_bf16_f32 v27, v30, v31\n"
        "buffer_store_dwordx4 v[24:27], v48, s[40:43], s72 offen offset:0 sc1\n"
        "v_cvt_pk_bf16_f32 v32, v32, v33\n"
        "v_cvt_pk_bf16_f32 v33, v34, v35\n"
        "s_nop 0\n"
        "v_permlane16_swap_b32 v32, v34\n"
        "v_permlane16_swap_b32 v33, v35\n"
        "buffer_store_dwordx4 v[32:35], v7, s[40:43], s72 offen offset:0 sc1\n"
        "s_waitcnt vmcnt(2)\n"
        "v_cmp_gt_i32 vcc, s70, v4\n"
        "s_nop 0\n"
        "s_and_b64 vcc, vcc, 63\n"
        "s_cbranch_vccnz .Lf2_late0_%=\n"
        "buffer_load_dwordx4 v1, s[40:43], s71 offen offset:0 sc1 lds\n"
        "buffer_load_dwordx4 v1, s[40:43], s71 offen offset:1024 sc1 lds\n"
        "s_add_u32 m0, m0, s85\n"
        "s_add_u32 s71, s71, 16384\n"
        "buffer_load_dwordx4 v1, s[40:43], s81 offen offset:0 sc1 lds\n"
        "s_sub_u32 m0, m0, s85\n"
        "s_add_u32 m0, m0, 12288\n"
        "s_add_u32 s81, s81, 16384\n"
        "buffer_load_dwordx4 v1, s[40:43], s71 offen offset:0 sc1 lds\n"
        "buffer_load_dwordx4 v1, s[40:43], s71 offen offset:1024 sc1 lds\n"
        "s_add_u32 m0, m0, s85\n"
        "s_add_u32 s71, s71, 16384\n"
        "buffer_load_dwordx4 v1, s[40:43], s81 offen offset:0 sc1 lds\n"
        "s_sub_u32 m0, m0, s85\n"
        "s_add_u32 m0, m0, 12288\n"
        "s_add_u32 s81, s81, 16384\n"
        "buffer_load_dwordx4 v1, s[40:43], s71 offen offset:0 sc1 lds\n"
        "buffer_load_dwordx4 v1, s[40:43], s71 offen offset:1024 sc1 lds\n"
        "s_add_u32 m0, m0, s85\n"
        "s_add_u32 s71, s71, 16384\n"
        "buffer_load_dwordx4 v1, s[40:43], s81 offen offset:0 sc1 lds\n"
        "s_add_u32 s69, s69, 1\n"
        "s_waitcnt vmcnt(9)\n"
        "v_mov_b32 v5, s74\n"
        "buffer_store_dword v5, v3, s[40:43], s73 offen sc1\n"
        "s_waitcnt vmcnt(7)\n"
        "s_branch .Lf2_go0_%=\n"
        ".p2align 5\n"
        ".Lf2_late0_%=:\n"
        "s_waitcnt vmcnt(0)\n"
        "v_mov_b32 v5, s74\n"
        "buffer_store_dword v5, v3, s[40:43], s73 offen sc1\n"
        "s_mov_b32 s64, 0\n"
        ".Lf2_in0_top_%=:\n"
        "buffer_load_dword v4, v2, s[40:43], s86 offen sc1\n"
        "s_waitcnt vmcnt(0)\n"
        "v_cmp_gt_i32 vcc, s70, v4\n"
        "s_nop 0\n"
        "s_and_b64 vcc, vcc, 63\n"
        "s_cbranch_vccz .Lf2_in0_ok_%=\n"
        "s_add_u32 s64, s64, 1\n"
        "s_and_b32 s65, s64, 255\n"
        "s_nop 0\n"
        "s_cbranch_scc1 .Lf2_in0_go_%=\n"
        "s_mov_b32 s65, 49152\n"
        "s_cmp_lt_u32 s64, 1048576\n"
        "s_nop 0\n"
        "s_nop 0\n"
        "s_nop 0\n"
        "s_cbranch_scc1 .Lf2_in0_chk_%=\n"
        "v_mov_b32 v4, 1\n"
        "buffer_store_dword v4, off, s[40:43], s65 sc1\n"
        "s_waitcnt vmcnt(0)\n"
        ".Lf2_in0_chk_%=:\n"
        "buffer_load_dword v4, off, s[40:43], s65 sc1\n"
        "s_waitcnt vmcnt(0)\n"
        "v_readfirstlane_b32 s65, v4\n"
        "s_nop 0\n"
        "s_cmp_lg_u32 s65, 1\n"
        "s_cbranch_scc1 .Lf2_in0_go_%=\n"
        "s_endpgm\n"
        ".p2align 5\n"
        ".Lf2_in0_go_%=:\n"
        "s_branch .Lf2_in0_top_%=\n"
        ".p2align 5\n"
        ".Lf2_in0_ok_%=:\n"
        "buffer_load_dwordx4 v1, s[40:43], s71 offen offset:0 sc1 lds\n"
        "buffer_load_dwordx4 v1, s[40:43], s71 offen offset:1024 sc1 lds\n"
        "s_add_u32 m0, m0, s85\n"
        "s_add_u32 s71, s71, 16384\n"
        "buffer_load_dwordx4 v1, s[40:43], s81 offen offset:0 sc1 lds\n"
        "s_sub_u32 m0, m0, s85\n"
        "s_add_u32 m0, m0, 12288\n"
        "s_add_u32 s81, s81, 16384\n"
        "buffer_load_dwordx4 v1, s[40:43], s71 offen offset:0 sc1 lds\n"
        "buffer_load_dwordx4 v1, s[40:43], s71 offen offset:1024 sc1 lds\n"
        "s_add_u32 m0, m0, s85\n"
        "s_add_u32 s71, s71, 16384\n"
        "buffer_load_dwordx4 v1, s[40:43], s81 offen offset:0 sc1 lds\n"
        "s_sub_u32 m0, m0, s85\n"
        "s_add_u32 m0, m0, 12288\n"
        "s_add_u32 s81, s81, 16384\n"
        "buffer_load_dwordx4 v1, s[40:43], s71 offen offset:0 sc1 lds\n"
        "buffer_load_dwordx4 v1, s[40:43], s71 offen offset:1024 sc1 lds\n"
        "s_add_u32 m0, m0, s85\n"
        "s_add_u32 s71, s71, 16384\n"
        "buffer_load_dwordx4 v1, s[40:43], s81 offen offset:0 sc1 lds\n"
        "s_add_u32 s69, s69, 1\n"
        "s_waitcnt vmcnt(6)\n"
        ".Lf2_go0_%=:\n"
        "s_mov_b32 s72, 241172480\n"
        "s_mov_b32 s83, 241172480\n"
        "s_mov_b32 s73, 241172480\n"
        "v_add_u32 v0, s78, v6\n"
        "s_barrier\n"
        "ds_read_b128 v[8:11], v0 offset:0\n"
        "ds_read_b128 v[12:15], v0 offset:1024\n"
        "ds_read_b128 v[16:19], v0 offset:2048\n"
        "ds_read_b128 v[20:23], v0 offset:3072\n"
        "s_waitcnt lgkmcnt(2)\n"
        "s_branch .Lf2_i1_%=\n"
        ".p2align 5\n"
        ".Lf2_i1_%=:\n"
        "v_mfma_f32_16x16x32_bf16 v[36:39], a[0:3], v[8:11], 0\n"
        "v_mfma_f32_16x16x32_bf16 v[40:43], a[4:7], v[8:11], 0\n"
        "s_lshr_b32 s67, s69, 1\n"
        "buffer_load_dword v4, v2, s[40:43], s86 offen sc1\n"
        "v_mfma_f32_16x16x32_bf16 v[44:47], a[8:11], v[8:11], 0\n"
        "s_lshr_b32 s65, s67, 1\n"
        "ds_read_b128 v[8:11], v0 offset:4096\n"
        "v_cvt_pk_bf16_f32 v24, v24, v25\n"
        "v_mfma_f32_16x16x32_bf16 v[36:39], a[12:15], v[12:15], v[36:39]\n"
        "s_bitcmp1_b32 s54, s65\n"
        "s_cselect_b32 s65, 8192, 0\n"
        "s_lshl_b32 s77, s67, 20\n"
        "v_mfma_f32_16x16x32_bf16 v[40:43], a[16:19], v[12:15], v[40:43]\n"
        "s_add_u32 s77, s77, s65\n"
        "s_add_u32 s71, s77, s58\n"
        "s_add_u32 s81, s77, s62\n"
        "v_mfma_f32_16x16x32_bf16 v[44:47], a[20:23], v[12:15], v[44:47]\n"
        "s_waitcnt lgkmcnt(1)\n"
        "ds_read_b128 v[12:15], v0 offset:5120\n"
        "s_add_u32 s78, s78, 36864\n"
        "v_mfma_f32_16x16x32_bf16 v[36:39], a[24:27], v[16:19], v[36:39]\n"
        "s_cmp_eq_u32 s78, 110592\n"
        "s_cselect_b32 s78, 0, s78\n"
        "s_add_u32 m0, s78, s61\n"
        "v_mfma_f32_16x16x32_bf16 v[40:43], a[28:31], v[16:19], v[40:43]\n"
        "s_lshr_b32 s70, s69, 0\n"
        "v_cvt_pk_bf16_f32 v25, v26, v27\n"
        "v_cvt_pk_bf16_f32 v26, v28, v29\n"
        "v_mfma_f32_16x16x32_bf16 v[44:47], a[32:35], v[16:19], v[44:47]\n"
        "ds_read_b128 v[16:19], v0 offset:6144\n"
        "v_cvt_pk_bf16_f32 v27, v30, v31\n"
        "v_mfma_f32_16x16x32_bf16 v[36:39], a[36:39], v[20:23], v[36:39]\n"
        "buffer_store_dwordx4 v[24:27], v48, s[40:43], s72 offen offset:0 sc1\n"
        "v_mfma_f32_16x16x32_bf16 v[40:43], a[40:43], v[20:23], v[40:43]\n"
        "v_cvt_pk_bf16_f32 v32, v32, v33\n"
        "v_cvt_pk_bf16_f32 v33, v34, v35\n"
        "v_mfma_f32_16x16x32_bf16 v[44:47], a[44:47], v[20:23], v[44:47]\n"
        "s_waitcnt lgkmcnt(1)\n"
        "ds_read_b128 v[20:23], v0 offset:7168\n"
        "s_nop 0\n"
        "v_mfma_f32_16x16x32_bf16 v[36:39], a[48:51], v[8:11], v[36:39]\n"
        "v_permlane16_swap_b32 v32, v34\n"
        "v_mfma_f32_16x16x32_bf16 v[40:43], a[52:55], v[8:11], v[40:43]\n"
        "v_permlane16_swap_b32 v33, v35\n"
        "v_mfma_f32_16x16x32_bf16 v[44:47], a[56:59], v[8:11], v[44:47]\n"
        "ds_read_b128 v[8:11], v0 offset:8192\n"
        "v_mfma_f32_16x16x32_bf16 v[36:39], a[60:63], v[12:15], v[36:39]\n"
        "v_mfma_f32_16x16x32_bf16 v[40:43], a[64:67], v[12:15], v[40:43]\n"
        "buffer_store_dwordx4 v[32:35], v7, s[40:43], s72 offen offset:0 sc1\n"
        "v_mfma_f32_16x16x32_bf16 v[44:47], a[68:71], v[12:15], v[44:47]\n"
        "s_waitcnt lgkmcnt(1)\n"
        "ds_read_b128 v[12:15], v0 offset:9216\n"
        "v_mfma_f32_16x16x32_bf16 v[36:39], a[72:75], v[16:19], v[36:39]\n"
        "v_mfma_f32_16x16x32_bf16 v[40:43], a[76:79], v[16:19], v[40:43]\n"
        "v_mfma_f32_16x16x32_bf16 v[44:47], a[80:83], v[16:19], v[44:47]\n"
        "ds_read_b128 v[16:19], v0 offset:10240\n"
        "v_mfma_f32_16x16x32_bf16 v[36:39], a[84:87], v[20:23], v[36:39]\n"
        "v_mfma_f32_16x16x32_bf16 v[40:43], a[88:91], v[20:23], v[40:43]\n"
        "v_mfma_f32_16x16x32_bf16 v[44:47], a[92:95], v[20:23], v[44:47]\n"
        "s_waitcnt lgkmcnt(1)\n"
        "ds_read_b128 v[20:23], v0 offset:11264\n"
        "v_mfma_f32_16x16x32_bf16 v[36:39], a[96:99], v[8:11], v[36:39]\n"
        "v_mfma_f32_16x16x32_bf16 v[40:43], a[100:103], v[8:11], v[40:43]\n"
        "s_waitcnt vmcnt(3)\n"
        "s_barrier\n"
        "v_mfma_f32_16x16x32_bf16 v[44:47], a[104:107], v[8:11], v[44:47]\n"
        "ds_read_b128 v[8:11], v0 offset:12288\n"
        "v_mfma_f32_16x16x32_bf16 v[36:39], a[108:111], v[12:15], v[36:39]\n"
        "buffer_store_dword v5, v3, s[40:43], s84 offen sc1\n"
        "v_mfma_f32_16x16x32_bf16 v[40:43], a[112:115], v[12:15], v[40:43]\n"
        "v_mov_b32 v5, s74\n"
        "v_mfma_f32_16x16x32_bf16 v[44:47], a[116:119], v[12:15], v[44:47]\n"
        "s_waitcnt lgkmcnt(1)\n"
        "ds_read_b128 v[12:15], v0 offset:13312\n"
        "v_mfma_f32_16x16x32_bf16 v[36:39], a[120:123], v[16:19], v[36:39]\n"
        "v_mfma_f32_16x16x32_bf16 v[40:43], a[124:127], v[16:19], v[40:43]\n"
        "s_waitcnt vmcnt(3)\n"
        "v_cmp_gt_i32_e64 vcc, s70, v4\n"
        "v_mfma_f32_16x16x32_bf16 v[44:47], a[128:131], v[16:19], v[44:47]\n"
        "s_nop 0\n"
        "ds_read_b128 v[16:19], v0 offset:14336\n"
        "v_mfma_f32_16x16x32_bf16 v[36:39], a[132:135], v[20:23], v[36:39]\n"
        "s_and_b64 vcc, vcc, 63\n"
        "v_mfma_f32_16x16x32_bf16 v[40:43], a[136:139], v[20:23], v[40:43]\n"
        "s_cbranch_vccnz .Lf2_i10_%=\n"
        "v_mfma_f32_16x16x32_bf16 v[44:47], a[140:143], v[20:23], v[44:47]\n"
        "s_waitcnt lgkmcnt(1)\n"
        "ds_read_b128 v[20:23], v0 offset:15360\n"
        "v_mfma_f32_16x16x32_bf16 v[36:39], a[144:147], v[8:11], v[36:39]\n"
        "buffer_load_dwordx4 v1, s[40:43], s71 offen offset:0 sc1 lds\n"
        "v_mfma_f32_16x16x32_bf16 v[40:43], a[148:151], v[8:11], v[40:43]\n"
        "v_mfma_f32_16x16x32_bf16 v[44:47], a[152:155], v[8:11], v[44:47]\n"
        "ds_read_b128 v[8:11], v0 offset:16384\n"
        "v_mfma_f32_16x16x32_bf16 v[36:39], a[156:159], v[12:15], v[36:39]\n"
        "buffer_load_dwordx4 v1, s[40:43], s71 offen offset:1024 sc1 lds\n"
        "v_mfma_f32_16x16x32_bf16 v[40:43], a[160:163], v[12:15], v[40:43]\n"
        "s_add_u32 m0, m0, s85\n"
        "s_add_u32 s71, s71, 16384\n"
        "v_mfma_f32_16x16x32_bf16 v[44:47], a[164:167], v[12:15], v[44:47]\n"
        "s_waitcnt lgkmcnt(1)\n"
        "ds_read_b128 v[12:15], v0 offset:17408\n"
        "v_mfma_f32_16x16x32_bf16 v[36:39], a[168:171], v[16:19], v[36:39]\n"
        "s_getreg_b32 s88, hwreg(HW_REG_IB_STS, 0, 4)\n"
        "v_mfma_f32_16x16x32_bf16 v[40:43], a[172:175], v[16:19], v[40:43]\n"
        "buffer_load_dwordx4 v1, s[40:43], s81 offen offset:0 sc1 lds\n"
        "v_mfma_f32_16x16x32_bf16 v[44:47], a[176:179], v[16:19], v[44:47]\n"
        "s_sub_u32 m0, m0, s85\n"
        "ds_read_b128 v[16:19], v0 offset:18432\n"
        "s_add_u32 m0, m0, 12288\n"
        "v_mfma_f32_16x16x32_bf16 v[36:39], a[180:183], v[20:23], v[36:39]\n"
        "s_add_u32 s81, s81, 16384\n"
        "v_mfma_f32_16x16x32_bf16 v[40:43], a[184:187], v[20:23], v[40:43]\n"
        "s_cmp_le_u32 s88, 3\n"
        "s_cselect_b32 s88, s73, s89\n"
        "s_cselect_b32 s73, s89, s73\n"
        "v_mfma_f32_16x16x32_bf16 v[44:47], a[188:191], v[20:23], v[44:47]\n"
        "s_waitcnt lgkmcnt(1)\n"
        "ds_read_b128 v[20:23], v0 offset:19456\n"
        "v_mfma_f32_16x16x32_bf16 v[36:39], a[192:195], v[8:11], v[36:39]\n"
        "buffer_load_dwordx4 v1, s[40:43], s71 offen offset:0 sc1 lds\n"
        "v_mfma_f32_16x16x32_bf16 v[40:43], a[196:199], v[8:11], v[40:43]\n"
        "buffer_store_dword v5, v3, s[40:43], s88 offen sc1\n"
        "v_mfma_f32_16x16x32_bf16 v[44:47], a[200:203], v[8:11], v[44:47]\n"
        "s_getreg_b32 s88, hwreg(HW_REG_IB_STS, 0, 4)\n"
        "ds_read_b128 v[8:11], v0 offset:20480\n"
        "v_mfma_f32_16x16x32_bf16 v[36:39], a[204:207], v[12:15], v[36:39]\n"
        "buffer_load_dwordx4 v1, s[40:43], s71 offen offset:1024 sc1 lds\n"
        "v_mfma_f32_16x16x32_bf16 v[40:43], a[208:211], v[12:15], v[40:43]\n"
        "s_add_u32 m0, m0, s85\n"
        "s_add_u32 s71, s71, 16384\n"
        "v_mfma_f32_16x16x32_bf16 v[44:47], a[212:215], v[12:15], v[44:47]\n"
        "s_waitcnt lgkmcnt(1)\n"
        "ds_read_b128 v[12:15], v0 offset:21504\n"
        "v_mfma_f32_16x16x32_bf16 v[36:39], a[216:219], v[16:19], v[36:39]\n"
        "s_cmp_le_u32 s88, 6\n"
        "s_cselect_b32 s88, s73, s89\n"
        "s_cselect_b32 s73, s89, s73\n"
        "v_mfma_f32_16x16x32_bf16 v[40:43], a[220:223], v[16:19], v[40:43]\n"
        "buffer_load_dwordx4 v1, s[40:43], s81 offen offset:0 sc1 lds\n"
        "v_mfma_f32_16x16x32_bf16 v[44:47], a[224:227], v[16:19], v[44:47]\n"
        "s_sub_u32 m0, m0, s85\n"
        "ds_read_b128 v[16:19], v0 offset:22528\n"
        "s_add_u32 m0, m0, 12288\n"
        "v_mfma_f32_16x16x32_bf16 v[36:39], a[228:231], v[20:23], v[36:39]\n"
        "s_add_u32 s81, s81, 16384\n"
        "buffer_store_dword v5, v3, s[40:43], s88 offen sc1\n"
        "v_mfma_f32_16x16x32_bf16 v[40:43], a[232:235], v[20:23], v[40:43]\n"
        "s_getreg_b32 s88, hwreg(HW_REG_IB_STS, 0, 4)\n"
        "v_mfma_f32_16x16x32_bf16 v[44:47], a[236:239], v[20:23], v[44:47]\n"
        "s_waitcnt lgkmcnt(1)\n"
        "ds_read_b128 v[20:23], v0 offset:23552\n"
        "v_mfma_f32_16x16x32_bf16 v[36:39], a[240:243], v[8:11], v[36:39]\n"
        "buffer_load_dwordx4 v1, s[40:43], s71 offen offset:0 sc1 lds\n"
        "v_mfma_f32_16x16x32_bf16 v[40:43], a[244:247], v[8:11], v[40:43]\n"
        "s_cmp_le_u32 s88, 9\n"
        "s_cselect_b32 s88, s73, s89\n"
        "s_cselect_b32 s73, s89, s73\n"
        "v_mfma_f32_16x16x32_bf16 v[44:47], a[248:251], v[8:11], v[44:47]\n"
        "ds_read_b128 v[8:11], v0 offset:24576\n"
        "v_mfma_f32_16x16x32_bf16 v[36:39], a[252:255], v[12:15], v[36:39]\n"
        "buffer_load_dwordx4 v1, s[40:43], s71 offen offset:1024 sc1 lds\n"
        "v_mfma_f32_16x16x32_bf16 v[40:43], v[80:83], v[12:15], v[40:43]\n"
        "s_add_u32 m0, m0, s85\n"
        "s_add_u32 s71, s71, 16384\n"
        "v_mfma_f32_16x16x32_bf16 v[44:47], v[84:87], v[12:15], v[44:47]\n"
        "s_waitcnt lgkmcnt(1)\n"
        "ds_read_b128 v[12:15], v0 offset:25600\n"
        "v_mfma_f32_16x16x32_bf16 v[36:39], v[88:91], v[16:19], v[36:39]\n"
        "buffer_store_dword v5, v3, s[40:43], s88 offen sc1\n"
        "v_mfma_f32_16x16x32_bf16 v[40:43], v[92:95], v[16:19], v[40:43]\n"
        "buffer_load_dwordx4 v1, s[40:43], s81 offen offset:0 sc1 lds\n"
        "v_mfma_f32_16x16x32_bf16 v[44:47], v[96:99], v[16:19], v[44:47]\n"
        "s_add_u32 s69, s69, 1\n"
        "ds_read_b128 v[16:19], v0 offset:26624\n"
        "v_mfma_f32_16x16x32_bf16 v[36:39], v[100:103], v[20:23], v[36:39]\n"
        "v_mfma_f32_16x16x32_bf16 v[40:43], v[104:107], v[20:23], v[40:43]\n"
        "s_waitcnt vmcnt(13)\n"
        "buffer_store_dword v5, v3, s[40:43], s73 offen sc1\n"
        "v_mfma_f32_16x16x32_bf16 v[44:47], v[108:111], v[20:23], v[44:47]\n"
        "s_waitcnt lgkmcnt(1)\n"
        "ds_read_b128 v[20:23], v0 offset:27648\n"
        "s_mov_b32 s84, s89\n"
        "v_mfma_f32_16x16x32_bf16 v[36:39], v[112:115], v[8:11], v[36:39]\n"
        "s_mov_b32 s73, s60\n"
        "s_add_u32 s74, s74, 1\n"
        "s_add_u32 s72, s75, s59\n"
        "v_mfma_f32_16x16x32_bf16 v[40:43], v[116:119], v[8:11], v[40:43]\n"
        "s_add_u32 s72, s72, 147456\n"
        "v_mfma_f32_16x16x32_bf16 v[44:47], v[120:123], v[8:11], v[44:47]\n"
        "ds_read_b128 v[8:11], v0 offset:28672\n"
        "v_mfma_f32_16x16x32_bf16 v[36:39], v[124:127], v[12:15], v[36:39]\n"
        "v_mfma_f32_16x16x32_bf16 v[40:43], v[128:131], v[12:15], v[40:43]\n"
        "v_mfma_f32_16x16x32_bf16 v[44:47], v[132:135], v[12:15], v[44:47]\n"
        "s_waitcnt lgkmcnt(1)\n"
        "ds_read_b128 v[12:15], v0 offset:29696\n"
        "v_mfma_f32_16x16x32_bf16 v[36:39], v[136:139], v[16:19], v[36:39]\n"
        "v_mfma_f32_16x16x32_bf16 v[40:43], v[140:143], v[16:19], v[40:43]\n"
        "v_mfma_f32_16x16x32_bf16 v[44:47], v[144:147], v[16:19], v[44:47]\n"
        "ds_read_b128 v[16:19], v0 offset:30720\n"
        "v_mfma_f32_16x16x32_bf16 v[36:39], v[148:151], v[20:23], v[36:39]\n"
        "v_mfma_f32_16x16x32_bf16 v[40:43], v[152:155], v[20:23], v[40:43]\n"
        "v_mfma_f32_16x16x32_bf16 v[44:47], v[156:159], v[20:23], v[44:47]\n"
        "s_waitcnt lgkmcnt(1)\n"
        "ds_read_b128 v[20:23], v0 offset:31744\n"
        "v_mfma_f32_16x16x32_bf16 v[36:39], v[160:163], v[8:11], v[36:39]\n"
        "v_mfma_f32_16x16x32_bf16 v[40:43], v[164:167], v[8:11], v[40:43]\n"
        "v_mfma_f32_16x16x32_bf16 v[44:47], v[168:171], v[8:11], v[44:47]\n"
        "ds_read_b128 v[8:11], v0 offset:32768\n"
        "v_mfma_f32_16x16x32_bf16 v[36:39], v[172:175], v[12:15], v[36:39]\n"
        "v_mfma_f32_16x16x32_bf16 v[40:43], v[176:179], v[12:15], v[40:43]\n"
        "v_mfma_f32_16x16x32_bf16 v[44:47], v[180:183], v[12:15], v[44:47]\n"
        "s_waitcnt lgkmcnt(1)\n"
        "ds_read_b128 v[12:15], v0 offset:33792\n"
        "v_mfma_f32_16x16x32_bf16 v[36:39], v[184:187], v[16:19], v[36:39]\n"
        "v_mfma_f32_16x16x32_bf16 v[40:43], v[188:191], v[16:19], v[40:43]\n"
        "v_mfma_f32_16x16x32_bf16 v[44:47], v[192:195], v[16:19], v[44:47]\n"
        "ds_read_b128 v[16:19], v0 offset:34816\n"
        "v_mfma_f32_16x16x32_bf16 v[36:39], v[196:199], v[20:23], v[36:39]\n"
        "v_mfma_f32_16x16x32_bf16 v[40:43], v[200:203], v[20:23], v[40:43]\n"
        "v_mfma_f32_16x16x32_bf16 v[44:47], v[204:207], v[20:23], v[44:47]\n"
        "s_waitcnt lgkmcnt(1)\n"
        "ds_read_b128 v[20:23], v0 offset:35840\n"
        "v_mfma_f32_16x16x32_bf16 v[36:39], v[208:211], v[8:11], v[36:39]\n"
        "v_mfma_f32_16x16x32_bf16 v[40:43], v[212:215], v[8:11], v[40:43]\n"
        "s_waitcnt vmcnt(10)\n"
        "s_barrier\n"
        "v_add_u32 v0, s78, v6\n"
        "v_mfma_f32_16x16x32_bf16 v[44:47], v[216:219], v[8:11], v[44:47]\n"
        "ds_read_b128 v[8:11], v0 offset:0\n"
        "v_mfma_f32_16x16x32_bf16 v[36:39], v[220:223], v[12:15], v[36:39]\n"
        "v_mfma_f32_16x16x32_bf16 v[40:43], v[224:227], v[12:15], v[40:43]\n"
        "v_mfma_f32_16x16x32_bf16 v[44:47], v[228:231], v[12:15], v[44:47]\n"
        "s_waitcnt lgkmcnt(1)\n"
        "ds_read_b128 v[12:15], v0 offset:1024\n"
        "v_mfma_f32_16x16x32_bf16 v[36:39], v[232:235], v[16:19], v[36:39]\n"
        "v_mfma_f32_16x16x32_bf16 v[40:43], v[236:239], v[16:19], v[40:43]\n"
        "v_mfma_f32_16x16x32_bf16 v[44:47], v[240:243], v[16:19], v[44:47]\n"
        "ds_read_b128 v[16:19], v0 offset:2048\n"
        "v_mfma_f32_16x16x32_bf16 v[36:39], v[244:247], v[20:23], v[36:39]\n"
        "v_mfma_f32_16x16x32_bf16 v[40:43], v[248:251], v[20:23], v[40:43]\n"
        "v_mfma_f32_16x16x32_bf16 v[44:47], v[252:255], v[20:23], v[44:47]\n"
        "s_waitcnt lgkmcnt(1)\n"
        "ds_read_b128 v[20:23], v0 offset:3072\n"
        "s_branch .Lf2_i0_%=\n"
        ".p2align 5\n"
        ".Lf2_i10_%=:\n"
        "v_mfma_f32_16x16x32_bf16 v[44:47], a[140:143], v[20:23], v[44:47]\n"
        "s_waitcnt lgkmcnt(1)\n"
        "ds_read_b128 v[20:23], v0 offset:15360\n"
        "v_mfma_f32_16x16x32_bf16 v[36:39], a[144:147], v[8:11], v[36:39]\n"
        "buffer_load_dword v4, v2, s[40:43], s86 offen sc1\n"
        "v_mfma_f32_16x16x32_bf16 v[40:43], a[148:151], v[8:11], v[40:43]\n"
        "v_mfma_f32_16x16x32_bf16 v[44:47], a[152:155], v[8:11], v[44:47]\n"
        "ds_read_b128 v[8:11], v0 offset:16384\n"
        "v_mfma_f32_16x16x32_bf16 v[36:39], a[156:159], v[12:15], v[36:39]\n"
        "v_mfma_f32_16x16x32_bf16 v[40:43], a[160:163], v[12:15], v[40:43]\n"
        "s_getreg_b32 s88, hwreg(HW_REG_IB_STS, 0, 4)\n"
        "v_mfma_f32_16x16x32_bf16 v[44:47], a[164:167], v[12:15], v[44:47]\n"
        "s_waitcnt lgkmcnt(1)\n"
        "ds_read_b128 v[12:15], v0 offset:17408\n"
        "v_mfma_f32_16x16x32_bf16 v[36:39], a[168:171], v[16:19], v[36:39]\n"
        "s_cmp_le_u32 s88, 2\n"
        "s_cselect_b32 s88, s73, s89\n"
        "s_cselect_b32 s73, s89, s73\n"
        "v_mfma_f32_16x16x32_bf16 v[40:43], a[172:175], v[16:19], v[40:43]\n"
        "buffer_store_dword v5, v3, s[40:43], s88 offen sc1\n"
        "v_mfma_f32_16x16x32_bf16 v[44:47], a[176:179], v[16:19], v[44:47]\n"
        "ds_read_b128 v[16:19], v0 offset:18432\n"
        "v_mfma_f32_16x16x32_bf16 v[36:39], a[180:183], v[20:23], v[36:39]\n"
        "v_mfma_f32_16x16x32_bf16 v[40:43], a[184:187], v[20:23], v[40:43]\n"
        "v_mfma_f32_16x16x32_bf16 v[44:47], a[188:191], v[20:23], v[44:47]\n"
        "s_waitcnt lgkmcnt(1)\n"
        "ds_read_b128 v[20:23], v0 offset:19456\n"
        "v_mfma_f32_16x16x32_bf16 v[36:39], a[192:195], v[8:11], v[36:39]\n"
        "s_getreg_b32 s88, hwreg(HW_REG_IB_STS, 0, 4)\n"
        "v_mfma_f32_16x16x32_bf16 v[40:43], a[196:199], v[8:11], v[40:43]\n"
        "s_cmp_le_u32 s88, 3\n"
        "s_cselect_b32 s88, s73, s89\n"
        "s_cselect_b32 s73, s89, s73\n"
        "v_mfma_f32_16x16x32_bf16 v[44:47], a[200:203], v[8:11], v[44:47]\n"
        "ds_read_b128 v[8:11], v0 offset:20480\n"
        "v_mfma_f32_16x16x32_bf16 v[36:39], a[204:207], v[12:15], v[36:39]\n"
        "buffer_store_dword v5, v3, s[40:43], s88 offen sc1\n"
        "v_mfma_f32_16x16x32_bf16 v[40:43], a[208:211], v[12:15], v[40:43]\n"
        "v_mfma_f32_16x16x32_bf16 v[44:47], a[212:215], v[12:15], v[44:47]\n"
        "s_waitcnt lgkmcnt(1)\n"
        "ds_read_b128 v[12:15], v0 offset:21504\n"
        "v_mfma_f32_16x16x32_bf16 v[36:39], a[216:219], v[16:19], v[36:39]\n"
        "v_mfma_f32_16x16x32_bf16 v[40:43], a[220:223], v[16:19], v[40:43]\n"
        "v_mfma_f32_16x16x32_bf16 v[44:47], a[224:227], v[16:19], v[44:47]\n"
        "s_getreg_b32 s88, hwreg(HW_REG_IB_STS, 0, 4)\n"
        "ds_read_b128 v[16:19], v0 offset:22528\n"
        "v_mfma_f32_16x16x32_bf16 v[36:39], a[228:231], v[20:23], v[36:39]\n"
        "s_cmp_le_u32 s88, 4\n"
        "s_cselect_b32 s88, s73, s89\n"
        "s_cselect_b32 s73, s89, s73\n"
        "v_mfma_f32_16x16x32_bf16 v[40:43], a[232:235], v[20:23], v[40:43]\n"
        "buffer_store_dword v5, v3, s[40:43], s88 offen sc1\n"
        "v_mfma_f32_16x16x32_bf16 v[44:47], a[236:239], v[20:23], v[44:47]\n"
        "s_waitcnt lgkmcnt(1)\n"
        "ds_read_b128 v[20:23], v0 offset:23552\n"
        "v_mfma_f32_16x16x32_bf16 v[36:39], a[240:243], v[8:11], v[36:39]\n"
        "v_mfma_f32_16x16x32_bf16 v[40:43], a[244:247], v[8:11], v[40:43]\n"
        "s_waitcnt vmcnt(3)\n"
        "v_cmp_gt_i32 vcc, s70, v4\n"
        "v_mfma_f32_16x16x32_bf16 v[44:47], a[248:251], v[8:11], v[44:47]\n"
        "ds_read_b128 v[8:11], v0 offset:24576\n"
        "v_mfma_f32_16x16x32_bf16 v[36:39], a[252:255], v[12:15], v[36:39]\n"
        "s_and_b64 vcc, vcc, 63\n"
        "v_mfma_f32_16x16x32_bf16 v[40:43], v[80:83], v[12:15], v[40:43]\n"
        "s_cbranch_vccnz .Lf2_i11_%=\n"
        "v_mfma_f32_16x16x32_bf16 v[44:47], v[84:87], v[12:15], v[44:47]\n"
        "s_waitcnt lgkmcnt(1)\n"
        "ds_read_b128 v[12:15], v0 offset:25600\n"
        "v_mfma_f32_16x16x32_bf16 v[36:39], v[88:91], v[16:19], v[36:39]\n"
        "buffer_load_dwordx4 v1, s[40:43], s71 offen offset:0 sc1 lds\n"
        "v_mfma_f32_16x16x32_bf16 v[40:43], v[92:95], v[16:19], v[40:43]\n"
        "v_mfma_f32_16x16x32_bf16 v[44:47], v[96:99], v[16:19], v[44:47]\n"
        "ds_read_b128 v[16:19], v0 offset:26624\n"
        "v_mfma_f32_16x16x32_bf16 v[36:39], v[100:103], v[20:23], v[36:39]\n"
        "buffer_load_dwordx4 v1, s[40:43], s71 offen offset:1024 sc1 lds\n"
        "v_mfma_f32_16x16x32_bf16 v[40:43], v[104:107], v[20:23], v[40:43]\n"
        "s_add_u32 m0, m0, s85\n"
        "s_add_u32 s71, s71, 16384\n"
        "v_mfma_f32_16x16x32_bf16 v[44:47], v[108:111], v[20:23], v[44:47]\n"
        "s_waitcnt lgkmcnt(1)\n"
        "ds_read_b128 v[20:23], v0 offset:27648\n"
        "v_mfma_f32_16x16x32_bf16 v[36:39], v[112:115], v[8:11], v[36:39]\n"
        "buffer_load_dwordx4 v1, s[40:43], s81 offen offset:0 sc1 lds\n"
        "v_mfma_f32_16x16x32_bf16 v[40:43], v[116:119], v[8:11], v[40:43]\n"
        "s_sub_u32 m0, m0, s85\n"
        "s_add_u32 m0, m0, 12288\n"
        "s_add_u32 s81, s81, 16384\n"
        "v_mfma_f32_16x16x32_bf16 v[44:47], v[120:123], v[8:11], v[44:47]\n"
        "ds_read_b128 v[8:11], v0 offset:28672\n"
        "v_mfma_f32_16x16x32_bf16 v[36:39], v[124:127], v[12:15], v[36:39]\n"
        "buffer_load_dwordx4 v1, s[40:43], s71 offen offset:0 sc1 lds\n"
        "v_mfma_f32_16x16x32_bf16 v[40:43], v[128:131], v[12:15], v[40:43]\n"
        "buffer_store_dword v5, v3, s[40:43], s73 offen sc1\n"
        "v_mfma_f32_16x16x32_bf16 v[44:47], v[132:135], v[12:15], v[44:47]\n"
        "s_waitcnt lgkmcnt(1)\n"
        "ds_read_b128 v[12:15], v0 offset:29696\n"
        "s_mov_b32 s84, s89\n"
        "v_mfma_f32_16x16x32_bf16 v[36:39], v[136:139], v[16:19], v[36:39]\n"
        "s_mov_b32 s73, s60\n"
        "buffer_load_dwordx4 v1, s[40:43], s71 offen offset:1024 sc1 lds\n"
        "v_mfma_f32_16x16x32_bf16 v[40:43], v[140:143], v[16:19], v[40:43]\n"
        "s_add_u32 m0, m0, s85\n"
        "s_add_u32 s71, s71, 16384\n"
        "s_add_u32 s74, s74, 1\n"
        "v_mfma_f32_16x16x32_bf16 v[44:47], v[144:147], v[16:19], v[44:47]\n"
        "s_add_u32 s72, s75, s59\n"
        "ds_read_b128 v[16:19], v0 offset:30720\n"
        "s_add_u32 s72, s72, 147456\n"
        "v_mfma_f32_16x16x32_bf16 v[36:39], v[148:151], v[20:23], v[36:39]\n"
        "v_mfma_f32_16x16x32_bf16 v[40:43], v[152:155], v[20:23], v[40:43]\n"
        "v_mfma_f32_16x16x32_bf16 v[44:47], v[156:159], v[20:23], v[44:47]\n"
        "s_waitcnt lgkmcnt(1)\n"
        "ds_read_b128 v[20:23], v0 offset:31744\n"
        "v_mfma_f32_16x16x32_bf16 v[36:39], v[160:163], v[8:11], v[36:39]\n"
        "v_mfma_f32_16x16x32_bf16 v[40:43], v[164:167], v[8:11], v[40:43]\n"
        "buffer_load_dwordx4 v1, s[40:43], s81 offen offset:0 sc1 lds\n"
        "v_mfma_f32_16x16x32_bf16 v[44:47], v[168:171], v[8:11], v[44:47]\n"
        "s_sub_u32 m0, m0, s85\n"
        "ds_read_b128 v[8:11], v0 offset:32768\n"
        "s_add_u32 m0, m0, 12288\n"
        "v_mfma_f32_16x16x32_bf16 v[36:39], v[172:175], v[12:15], v[36:39]\n"
        "s_add_u32 s81, s81, 16384\n"
        "v_mfma_f32_16x16x32_bf16 v[40:43], v[176:179], v[12:15], v[40:43]\n"
        "v_mfma_f32_16x16x32_bf16 v[44:47], v[180:183], v[12:15], v[44:47]\n"
        "s_waitcnt lgkmcnt(1)\n"
        "ds_read_b128 v[12:15], v0 offset:33792\n"
        "v_mfma_f32_16x16x32_bf16 v[36:39], v[184:187], v[16:19], v[36:39]\n"
        "buffer_load_dwordx4 v1, s[40:43], s71 offen offset:0 sc1 lds\n"
        "v_mfma_f32_16x16x32_bf16 v[40:43], v[188:191], v[16:19], v[40:43]\n"
        "v_mfma_f32_16x16x32_bf16 v[44:47], v[192:195], v[16:19], v[44:47]\n"
        "ds_read_b128 v[16:19], v0 offset:34816\n"
        "v_mfma_f32_16x16x32_bf16 v[36:39], v[196:199], v[20:23], v[36:39]\n"
        "buffer_load_dwordx4 v1, s[40:43], s71 offen offset:1024 sc1 lds\n"
        "v_mfma_f32_16x16x32_bf16 v[40:43], v[200:203], v[20:23], v[40:43]\n"
        "s_add_u32 m0, m0, s85\n"
        "s_add_u32 s71, s71, 16384\n"
        "v_mfma_f32_16x16x32_bf16 v[44:47], v[204:207], v[20:23], v[44:47]\n"
        "s_waitcnt lgkmcnt(1)\n"
        "ds_read_b128 v[20:23], v0 offset:35840\n"
        "v_mfma_f32_16x16x32_bf16 v[36:39], v[208:211], v[8:11], v[36:39]\n"
        "v_mfma_f32_16x16x32_bf16 v[40:43], v[212:215], v[8:11], v[40:43]\n"
        "s_waitcnt vmcnt(6)\n"
        "s_barrier\n"
        "v_add_u32 v0, s78, v6\n"
        "v_mfma_f32_16x16x32_bf16 v[44:47], v[216:219], v[8:11], v[44:47]\n"
        "ds_read_b128 v[8:11], v0 offset:0\n"
        "v_mfma_f32_16x16x32_bf16 v[36:39], v[220:223], v[12:15], v[36:39]\n"
        "buffer_load_dwordx4 v1, s[40:43], s81 offen offset:0 sc1 lds\n"
        "v_mfma_f32_16x16x32_bf16 v[40:43], v[224:227], v[12:15], v[40:43]\n"
        "s_add_u32 s69, s69, 1\n"
        "v_mfma_f32_16x16x32_bf16 v[44:47], v[228:231], v[12:15], v[44:47]\n"
        "s_waitcnt lgkmcnt(1)\n"
        "ds_read_b128 v[12:15], v0 offset:1024\n"
        "v_mfma_f32_16x16x32_bf16 v[36:39], v[232:235], v[16:19], v[36:39]\n"
        "v_mfma_f32_16x16x32_bf16 v[40:43], v[236:239], v[16:19], v[40:43]\n"
        "v_mfma_f32_16x16x32_bf16 v[44:47], v[240:243], v[16:19], v[44:47]\n"
        "ds_read_b128 v[16:19], v0 offset:2048\n"
        "v_mfma_f32_16x16x32_bf16 v[36:39], v[244:247], v[20:23], v[36:39]\n"
        "v_mfma_f32_16x16x32_bf16 v[40:43], v[248:251], v[20:23], v[40:43]\n"
        "v_mfma_f32_16x16x32_bf16 v[44:47], v[252:255], v[20:23], v[44:47]\n"
        "s_waitcnt lgkmcnt(1)\n"
        "ds_read_b128 v[20:23], v0 offset:3072\n"
        "s_branch .Lf2_i0_%=\n"
        ".p2align 5\n"
        ".Lf2_i11_%=:\n"
        "v_mfma_f32_16x16x32_bf16 v[44:47], v[84:87], v[12:15], v[44:47]\n"
        "s_waitcnt lgkmcnt(1)\n"
        "ds_read_b128 v[12:15], v0 offset:25600\n"
        "v_mfma_f32_16x16x32_bf16 v[36:39], v[88:91], v[16:19], v[36:39]\n"
        "buffer_load_dword v4, v2, s[40:43], s86 offen sc1\n"
        "v_mfma_f32_16x16x32_bf16 v[40:43], v[92:95], v[16:19], v[40:43]\n"
        "v_mfma_f32_16x16x32_bf16 v[44:47], v[96:99], v[16:19], v[44:47]\n"
        "ds_read_b128 v[16:19], v0 offset:26624\n"
        "v_mfma_f32_16x16x32_bf16 v[36:39], v[100:103], v[20:23], v[36:39]\n"
        "v_mfma_f32_16x16x32_bf16 v[40:43], v[104:107], v[20:23], v[40:43]\n"
        "buffer_store_dword v5, v3, s[40:43], s73 offen sc1\n"
        "v_mfma_f32_16x16x32_bf16 v[44:47], v[108:111], v[20:23], v[44:47]\n"
        "s_waitcnt lgkmcnt(1)\n"
        "ds_read_b128 v[20:23], v0 offset:27648\n"
        "s_mov_b32 s84, s89\n"
        "v_mfma_f32_16x16x32_bf16 v[36:39], v[112:115], v[8:11], v[36:39]\n"
        "s_mov_b32 s73, s60\n"
        "s_add_u32 s74, s74, 1\n"
        "s_add_u32 s72, s75, s59\n"
        "v_mfma_f32_16x16x32_bf16 v[40:43], v[116:119], v[8:11], v[40:43]\n"
        "s_add_u32 s72, s72, 147456\n"
        "v_mfma_f32_16x16x32_bf16 v[44:47], v[120:123], v[8:11], v[44:47]\n"
        "ds_read_b128 v[8:11], v0 offset:28672\n"
        "v_mfma_f32_16x16x32_bf16 v[36:39], v[124:127], v[12:15], v[36:39]\n"
        "v_mfma_f32_16x16x32_bf16 v[40:43], v[128:131], v[12:15], v[40:43]\n"
        "v_mfma_f32_16x16x32_bf16 v[44:47], v[132:135], v[12:15], v[44:47]\n"
        "s_waitcnt lgkmcnt(1)\n"
        "ds_read_b128 v[12:15], v0 offset:29696\n"
        "v_mfma_f32_16x16x32_bf16 v[36:39], v[136:139], v[16:19], v[36:39]\n"
        "v_mfma_f32_16x16x32_bf16 v[40:43], v[140:143], v[16:19], v[40:43]\n"
        "v_mfma_f32_16x16x32_bf16 v[44:47], v[144:147], v[16:19], v[44:47]\n"
        "ds_read_b128 v[16:19], v0 offset:30720\n"
        "v_mfma_f32_16x16x32_bf16 v[36:39], v[148:151], v[20:23], v[36:39]\n"
        "v_mfma_f32_16x16x32_bf16 v[40:43], v[152:155], v[20:23], v[40:43]\n"
        "v_mfma_f32_16x16x32_bf16 v[44:47], v[156:159], v[20:23], v[44:47]\n"
        "s_waitcnt lgkmcnt(1)\n"
        "ds_read_b128 v[20:23], v0 offset:31744\n"
        "v_mfma_f32_16x16x32_bf16 v[36:39], v[160:163], v[8:11], v[36:39]\n"
        "v_mfma_f32_16x16x32_bf16 v[40:43], v[164:167], v[8:11], v[40:43]\n"
        "v_mfma_f32_16x16x32_bf16 v[44:47], v[168:171], v[8:11], v[44:47]\n"
        "ds_read_b128 v[8:11], v0 offset:32768\n"
        "v_mfma_f32_16x16x32_bf16 v[36:39], v[172:175], v[12:15], v[36:39]\n"
        "v_mfma_f32_16x16x32_bf16 v[40:43], v[176:179], v[12:15], v[40:43]\n"
        "v_mfma_f32_16x16x32_bf16 v[44:47], v[180:183], v[12:15], v[44:47]\n"
        "s_waitcnt lgkmcnt(1)\n"
        "ds_read_b128 v[12:15], v0 offset:33792\n"
        "v_mfma_f32_16x16x32_bf16 v[36:39], v[184:187], v[16:19], v[36:39]\n"
        "v_mfma_f32_16x16x32_bf16 v[40:43], v[188:191], v[16:19], v[40:43]\n"
        "v_mfma_f32_16x16x32_bf16 v[44:47], v[192:195], v[16:19], v[44:47]\n"
        "ds_read_b128 v[16:19], v0 offset:34816\n"
        "v_mfma_f32_16x16x32_bf16 v[36:39], v[196:199], v[20:23], v[36:39]\n"
        "s_waitcnt vmcnt(1)\n"
        "v_cmp_gt_i32 vcc, s70, v4\n"
        "v_mfma_f32_16x16x32_bf16 v[40:43], v[200:203], v[20:23], v[40:43]\n"
        "v_mfma_f32_16x16x32_bf16 v[44:47], v[204:207], v[20:23], v[44:47]\n"
        "s_waitcnt lgkmcnt(1)\n"
        "ds_read_b128 v[20:23], v0 offset:35840\n"
        "s_and_b64 vcc, vcc, 63\n"
        "v_mfma_f32_16x16x32_bf16 v[36:39], v[208:211], v[8:11], v[36:39]\n"
        "s_cbranch_vccnz .Lf2_i12_%=\n"
        "v_mfma_f32_16x16x32_bf16 v[40:43], v[212:215], v[8:11], v[40:43]\n"
        "buffer_load_dwordx4 v1, s[40:43], s71 offen offset:0 sc1 lds\n"
        "v_mfma_f32_16x16x32_bf16 v[44:47], v[216:219], v[8:11], v[44:47]\n"
        "v_mfma_f32_16x16x32_bf16 v[36:39], v[220:223], v[12:15], v[36:39]\n"
        "buffer_load_dwordx4 v1, s[40:43], s71 offen offset:1024 sc1 lds\n"
        "v_mfma_f32_16x16x32_bf16 v[40:43], v[224:227], v[12:15], v[40:43]\n"
        "s_add_u32 m0, m0, s85\n"
        "s_add_u32 s71, s71, 16384\n"
        "v_mfma_f32_16x16x32_bf16 v[44:47], v[228:231], v[12:15], v[44:47]\n"
        "s_waitcnt lgkmcnt(0)\n"
        "buffer_load_dwordx4 v1, s[40:43], s81 offen offset:0 sc1 lds\n"
        "v_mfma_f32_16x16x32_bf16 v[36:39], v[232:235], v[16:19], v[36:39]\n"
        "s_sub_u32 m0, m0, s85\n"
        "s_add_u32 m0, m0, 12288\n"
        "s_add_u32 s81, s81, 16384\n"
        "v_mfma_f32_16x16x32_bf16 v[40:43], v[236:239], v[16:19], v[40:43]\n"
        "buffer_load_dwordx4 v1, s[40:43], s71 offen offset:0 sc1 lds\n"
        "v_mfma_f32_16x16x32_bf16 v[44:47], v[240:243], v[16:19], v[44:47]\n"
        "v_mfma_f32_16x16x32_bf16 v[36:39], v[244:247], v[20:23], v[36:39]\n"
        "buffer_load_dwordx4 v1, s[40:43], s71 offen offset:1024 sc1 lds\n"
        "v_mfma_f32_16x16x32_bf16 v[40:43], v[248:251], v[20:23], v[40:43]\n"
        "s_add_u32 m0, m0, s85\n"
        "s_add_u32 s71, s71, 16384\n"
        "v_mfma_f32_16x16x32_bf16 v[44:47], v[252:255], v[20:23], v[44:47]\n"
        "buffer_load_dwordx4 v1, s[40:43], s81 offen offset:0 sc1 lds\n"
        "s_sub_u32 m0, m0, s85\n"
        "s_add_u32 m0, m0, 12288\n"
        "s_add_u32 s81, s81, 16384\n"
        "buffer_load_dwordx4 v1, s[40:43], s71 offen offset:0 sc1 lds\n"
        "buffer_load_dwordx4 v1, s[40:43], s71 offen offset:1024 sc1 lds\n"
        "s_add_u32 m0, m0, s85\n"
        "s_add_u32 s71, s71, 16384\n"
        "buffer_load_dwordx4 v1, s[40:43], s81 offen offset:0 sc1 lds\n"
        "s_add_u32 s69, s69, 1\n"
        "s_nop 2\n"
        "v_cvt_pk_bf16_f32 v36, v36, v37\n"
        "v_cvt_pk_bf16_f32 v37, v38, v39\n"
        "v_cvt_pk_bf16_f32 v38, v40, v41\n"
        "v_cvt_pk_bf16_f32 v39, v42, v43\n"
        "buffer_store_dwordx4 v[36:39], v48, s[40:43], s72 offen offset:0 sc1\n"
        "v_cvt_pk_bf16_f32 v44, v44, v45\n"
        "v_cvt_pk_bf16_f32 v45, v46, v47\n"
        "s_nop 0\n"
        "v_permlane16_swap_b32 v44, v46\n"
        "v_permlane16_swap_b32 v45, v47\n"
        "buffer_store_dwordx4 v[44:47], v7, s[40:43], s72 offen offset:0 sc1\n"
        "v_mov_b32 v5, s74\n"
        "s_mov_b32 s84, s60\n"
        "s_waitcnt vmcnt(8)\n"
        "s_branch .Lf2_go1_%=\n"
        ".p2align 5\n"
        ".Lf2_i12_%=:\n"
        "v_mfma_f32_16x16x32_bf16 v[40:43], v[212:215], v[8:11], v[40:43]\n"
        "buffer_load_dword v4, v2, s[40:43], s86 offen sc1\n"
        "v_mfma_f32_16x16x32_bf16 v[44:47], v[216:219], v[8:11], v[44:47]\n"
        "v_mfma_f32_16x16x32_bf16 v[36:39], v[220:223], v[12:15], v[36:39]\n"
        "v_mfma_f32_16x16x32_bf16 v[40:43], v[224:227], v[12:15], v[40:43]\n"
        "v_mfma_f32_16x16x32_bf16 v[44:47], v[228:231], v[12:15], v[44:47]\n"
        "s_waitcnt lgkmcnt(0)\n"
        "v_mfma_f32_16x16x32_bf16 v[36:39], v[232:235], v[16:19], v[36:39]\n"
        "v_mfma_f32_16x16x32_bf16 v[40:43], v[236:239], v[16:19], v[40:43]\n"
        "v_mfma_f32_16x16x32_bf16 v[44:47], v[240:243], v[16:19], v[44:47]\n"
        "v_mfma_f32_16x16x32_bf16 v[36:39], v[244:247], v[20:23], v[36:39]\n"
        "v_mfma_f32_16x16x32_bf16 v[40:43], v[248:251], v[20:23], v[40:43]\n"
        "v_mfma_f32_16x16x32_bf16 v[44:47], v[252:255], v[20:23], v[44:47]\n"
        "s_nop 2\n"
        "v_cvt_pk_bf16_f32 v36, v36, v37\n"
        "v_cvt_pk_bf16_f32 v37, v38, v39\n"
        "v_cvt_pk_bf16_f32 v38, v40, v41\n"
        "v_cvt_pk_bf16_f32 v39, v42, v43\n"
        "buffer_store_dwordx4 v[36:39], v48, s[40:43], s72 offen offset:0 sc1\n"
        "v_cvt_pk_bf16_f32 v44, v44, v45\n"
        "v_cvt_pk_bf16_f32 v45, v46, v47\n"
        "s_nop 0\n"
        "v_permlane16_swap_b32 v44, v46\n"
        "v_permlane16_swap_b32 v45, v47\n"
        "buffer_store_dwordx4 v[44:47], v7, s[40:43], s72 offen offset:0 sc1\n"
        "s_waitcnt vmcnt(2)\n"
        "v_cmp_gt_i32 vcc, s70, v4\n"
        "s_nop 0\n"
        "s_and_b64 vcc, vcc, 63\n"
        "s_cbranch_vccnz .Lf2_late1_%=\n"
        "buffer_load_dwordx4 v1, s[40:43], s71 offen offset:0 sc1 lds\n"
        "buffer_load_dwordx4 v1, s[40:43], s71 offen offset:1024 sc1 lds\n"
        "s_add_u32 m0, m0, s85\n"
        "s_add_u32 s71, s71, 16384\n"
        "buffer_load_dwordx4 v1, s[40:43], s81 offen offset:0 sc1 lds\n"
        "s_sub_u32 m0, m0, s85\n"
        "s_add_u32 m0, m0, 12288\n"
        "s_add_u32 s81, s81, 16384\n"
        "buffer_load_dwordx4 v1, s[40:43], s71 offen offset:0 sc1 lds\n"
        "buffer_load_dwordx4 v1, s[40:43], s71 offen offset:1024 sc1 lds\n"
        "s_add_u32 m0, m0, s85\n"
        "s_add_u32 s71, s71, 16384\n"
        "buffer_load_dwordx4 v1, s[40:43], s81 offen offset:0 sc1 lds\n"
        "s_sub_u32 m0, m0, s85\n"
        "s_add_u32 m0, m0, 12288\n"
        "s_add_u32 s81, s81, 16384\n"
        "buffer_load_dwordx4 v1, s[40:43], s71 offen offset:0 sc1 lds\n"
        "buffer_load_dwordx4 v1, s[40:43], s71 offen offset:1024 sc1 lds\n"
        "s_add_u32 m0, m0, s85\n"
        "s_add_u32 s71, s71, 16384\n"
        "buffer_load_dwordx4 v1, s[40:43], s81 offen offset:0 sc1 lds\n"
        "s_add_u32 s69, s69, 1\n"
        "s_waitcnt vmcnt(9)\n"
        "v_mov_b32 v5, s74\n"
        "buffer_store_dword v5, v3, s[40:43], s73 offen sc1\n"
        "s_waitcnt vmcnt(7)\n"
        "s_branch .Lf2_go1_%=\n"
        ".p2align 5\n"
        ".Lf2_late1_%=:\n"
        "s_waitcnt vmcnt(0)\n"
        "v_mov_b32_e64 v5, s74\n"
        "buffer_store_dword v5, v3, s[40:43], s73 offen sc1\n"
        "s_cmp_eq_u32 s69, 128\n"
        "s_nop 0\n"
        "s_cbranch_scc0 .Lf2_wait1_%=\n"
        "s_endpgm\n"
        ".p2align 5\n"
        ".Lf2_wait1_%=:\n"
        "s_mov_b32 s64, 0\n"
        ".Lf2_in1_top_%=:\n"
        "buffer_load_dword v4, v2, s[40:43], s86 offen sc1\n"
        "s_waitcnt vmcnt(0)\n"
        "v_cmp_gt_i32_e64 vcc, s70, v4\n"
        "s_nop 0\n"
        "s_and_b64 vcc, vcc, 63\n"
        "s_cbranch_vccz .Lf2_in1_ok_%=\n"
        "s_add_u32 s64, s64, 1\n"
        "s_and_b32 s65, s64, 255\n"
        "s_cbranch_scc1 .Lf2_in1_go_%=\n"
        "s_mov_b32 s65, 49152\n"
        "s_cmp_lt_u32 s64, 1048576\n"
        "s_cbranch_scc1 .Lf2_in1_chk_%=\n"
        "v_mov_b32 v4, 1\n"
        "buffer_store_dword v4, off, s[40:43], s65 sc1\n"
        "s_waitcnt vmcnt(0)\n"
        ".Lf2_in1_chk_%=:\n"
        "buffer_load_dword v4, off, s[40:43], s65 sc1\n"
        "s_waitcnt vmcnt(0)\n"
        "v_readfirstlane_b32 s65, v4\n"
        "s_nop 0\n"
        "s_cmp_lg_u32 s65, 1\n"
        "s_cbranch_scc1 .Lf2_in1_go_%=\n"
        "s_endpgm\n"
        ".p2align 5\n"
        ".Lf2_in1_go_%=:\n"
        "s_branch .Lf2_in1_top_%=\n"
        ".p2align 5\n"
        ".Lf2_in1_ok_%=:\n"
        "buffer_load_dwordx4 v1, s[40:43], s71 offen offset:0 sc1 lds\n"
        "buffer_load_dwordx4 v1, s[40:43], s71 offen offset:1024 sc1 lds\n"
        "s_add_u32 m0, m0, s85\n"
        "s_add_u32 s71, s71, 16384\n"
        "buffer_load_dwordx4 v1, s[40:43], s81 offen offset:0 sc1 lds\n"
        "s_sub_u32 m0, m0, s85\n"
        "s_add_u32 m0, m0, 12288\n"
        "s_add_u32 s81, s81, 16384\n"
        "buffer_load_dwordx4 v1, s[40:43], s71 offen offset:0 sc1 lds\n"
        "buffer_load_dwordx4 v1, s[40:43], s71 offen offset:1024 sc1 lds\n"
        "s_add_u32 m0, m0, s85\n"
        "s_add_u32 s71, s71, 16384\n"
        "buffer_load_dwordx4 v1, s[40:43], s81 offen offset:0 sc1 lds\n"
        "s_sub_u32 m0, m0, s85\n"
        "s_add_u32 m0, m0, 12288\n"
        "s_add_u32 s81, s81, 16384\n"
        "buffer_load_dwordx4 v1, s[40:43], s71 offen offset:0 sc1 lds\n"
        "buffer_load_dwordx4 v1, s[40:43], s71 offen offset:1024 sc1 lds\n"
        "s_add_u32 m0, m0, s85\n"
        "s_add_u32 s71, s71, 16384\n"
        "buffer_load_dwordx4 v1, s[40:43], s81 offen offset:0 sc1 lds\n"
        "s_add_u32 s69, s69, 1\n"
        "s_waitcnt vmcnt(6)\n"
        ".Lf2_go1_%=:\n"
        "s_mov_b32 s72, 241172480\n"
        "s_mov_b32 s83, 241172480\n"
        "s_mov_b32 s73, 241172480\n"
        "v_add_u32 v0, s78, v6\n"
        "s_barrier\n"
        "ds_read_b128 v[8:11], v0 offset:0\n"
        "ds_read_b128 v[12:15], v0 offset:1024\n"
        "ds_read_b128 v[16:19], v0 offset:2048\n"
        "ds_read_b128 v[20:23], v0 offset:3072\n"
        "s_waitcnt lgkmcnt(2)\n"
        "s_branch .Lf2_i0_%=\n"
        ".Lf2_end_%=:\n"
            :: "s"(p.ws), "s"(p.w2), "s"(p.w2), "s"(p.w2), "s"(pair(w, delay)), "s"(pair(x4, pr)),
               "s"(pair(hb + w * 2048, 97 * PAGE + spread(C_P5))), "s"(pair(flag(F_F2 + fl), (w * 2048) | (8192 - w * 1024) << 16)), "s"(pair(hb + 6 * 16384 + w * 1024, 0)),
               "v"(pair(lane * 16, lane * 16)), "v"(pair(lane < 6 ? flag(F_F1 + 128 * nu + 24 * kap + 8 * (lane >> 1) + 2 * w + (lane & 1)) : OOB, fo4)), K_ARGS
            : "v0", "v1", "v2", "v3", "v4", "v5", "v6", "v7", "v8", "v9", "v10", "v11", "v12", "v13", "v14", "v15", "v16", "v17", "v18", "v19", "v20", "v21", "v22", "v23", "v24", "v25", "v26", "v27", "v28", "v29", "v30", "v31", "v32", "v33", "v34", "v35", "v36", "v37", "v38", "v39", "v40", "v41", "v42", "v43", "v44", "v45", "v46", "v47", "v48", "v49", "v50", "v51", "v52", "v53", "v54", "v55", "v56", "v57", "v58", "v59", "v60", "v61", "v62", "v63", "v64", "v65", "v66", "v67", "v68", "v69", "v70", "v71", "v72", "v73", "v74", "v75", "v76", "v77", "v78", "v79", "v255", "a255", "s26", "s27", "s28", "s29", "s30", "s31", "s32", "s33", "s34", "s35", "s36", "s37", "s38", "s39", "s40", "s41", "s42", "s43", "s44", "s45", "s46", "s47", "s48", "s49", "s50", "s51", "s52", "s53", "s54", "s55", "s56", "s57", "s58", "s59", "s60", "s61", "s62", "s63", "s64", "s65", "s66", "s67", "s68", "s69", "s70", "s71", "s72", "s73", "s74", "s75", "s76", "s77", "s78", "s79", "s80", "s81", "s82", "s83", "s84", "s85", "s86", "s87", "s88", "s89", "s90", "s91", "s92", "s93", "s94", "s95", "s96", "s97", "s98", "s99", "vcc", "scc", "memory");
    }
    __builtin_unreachable();
}

__device__ __forceinline__ void role_attn(const Params& p, int h0, int x4, int pr) {
    const unsigned lane = threadIdx.x & 63, i = lane & 15, g = lane >> 4;
    const int w = rfl(threadIdx.x >> 6), h = h0 + w;
    asm volatile(
        "s_nop 4\n"
        "s_mov_b64 s[40:41], %0\n"
        "s_and_b32 s41, s41, 0xffff\n"
        "s_mov_b32 s42, 236978176\n"
        "s_mov_b32 s43, 0x20000\n"
        "s_mov_b64 s[56:57], %1\n"
        "s_mov_b64 s[58:59], %2\n"
        "s_mov_b64 s[60:61], %3\n"
        "v_mov_b64 v[0:1], %4\n"
        "v_mov_b64 v[2:3], %5\n"
        "v_mov_b64 v[4:5], %6\n"
        "v_cmp_ge_i32 s[76:77], 0, v5\n"
        "v_cmp_ge_i32 s[78:79], -1, v5\n"
        "v_cmp_ge_i32 s[80:81], -2, v5\n"
        "v_cmp_ge_i32 s[82:83], -3, v5\n"
        "s_mov_b32 s84, 0\n"
        "s_mov_b32 s85, -1\n"
        "s_mov_b32 s75, 0x3e0293ee\n"
        "s_getpc_b64 s[44:45]\n"
        ".Lat_pc_%=:\n"
        "s_add_u32 s44, s44, .Lat_code_%=-.Lat_pc_%=\n"
        "s_addc_u32 s45, s45, 0\n"
        "s_and_b32 s45, s45, 0xffff\n"
        "s_and_b32 s44, s44, -4\n"
        "s_mov_b32 s46, .Lat_end_%=-.Lat_code_%=\n"
        "s_mov_b32 s47, 0x20000\n"
        "s_mov_b32 s65, 0\n"
        "buffer_load_dwordx4 v[24:27], v0, s[44:47], s65 offen\n"
        "s_add_u32 s65, s65, 1024\n"
        "buffer_load_dwordx4 v[28:31], v0, s[44:47], s65 offen\n"
        "s_add_u32 s65, s65, 1024\n"
        "buffer_load_dwordx4 v[32:35], v0, s[44:47], s65 offen\n"
        "s_add_u32 s65, s65, 1024\n"
        "buffer_load_dwordx4 v[36:39], v0, s[44:47], s65 offen\n"
        "s_add_u32 s65, s65, 1024\n"
        "buffer_load_dwordx4 v[40:43], v0, s[44:47], s65 offen\n"
        "s_add_u32 s65, s65, 1024\n"
        "v_lshrrev_b32 v12, 4, v0\n"
        "s_mov_b32 s50, s56\n"
        "v_lshlrev_b32 v14, 7, v12\n"
        "s_sub_u32 s66, 0, s50\n"
        "s_mov_b32 s52, 32768\n"
        "s_mov_b32 s64, 0\n"
        ".Lat_pg0_top_%=:\n"
        "buffer_load_dword v13, v14, s[40:43], s52 offen sc1\n"
        "s_waitcnt vmcnt(0)\n"
        "v_cmp_gt_i32 vcc, 0, v13\n"
        "s_cbranch_vccz .Lat_pg0_ok_%=\n"
        "s_add_u32 s64, s64, 1\n"
        "s_and_b32 s65, s64, 255\n"
        "s_cbranch_scc1 .Lat_pg0_go_%=\n"
        "s_mov_b32 s65, 49152\n"
        "s_cmp_lt_u32 s64, 1048576\n"
        "s_cbranch_scc1 .Lat_pg0_chk_%=\n"
        "v_mov_b32 v13, 1\n"
        "buffer_store_dword v13, off, s[40:43], s65 sc1\n"
        "s_waitcnt vmcnt(0)\n"
        ".Lat_pg0_chk_%=:\n"
        "buffer_load_dword v13, off, s[40:43], s65 sc1\n"
        "s_waitcnt vmcnt(0)\n"
        "v_readfirstlane_b32 s65, v13\n"
        "s_nop 0\n"
        "s_cmp_lg_u32 s65, 1\n"
        "s_cbranch_scc1 .Lat_pg0_go_%=\n"
        "s_endpgm\n"
        ".Lat_pg0_go_%=:\n"
        "s_sleep 2\n"
        "s_branch .Lat_pg0_top_%=\n"
        ".Lat_pg0_ok_%=:\n"
        "v_cmp_eq_u32 s[52:53], 1, v13\n"
        "s_nop 1\n"
        "s_mov_b32 s54, 40960\n"
        "s_mov_b32 s64, 0\n"
        ".Lat_pg1_top_%=:\n"
        "buffer_load_dword v13, v14, s[40:43], s54 offen sc1\n"
        "s_waitcnt vmcnt(0)\n"
        "v_cmp_gt_i32 vcc, 0, v13\n"
        "s_cbranch_vccz .Lat_pg1_ok_%=\n"
        "s_add_u32 s64, s64, 1\n"
        "s_and_b32 s65, s64, 255\n"
        "s_cbranch_scc1 .Lat_pg1_go_%=\n"
        "s_mov_b32 s65, 49152\n"
        "s_cmp_lt_u32 s64, 1048576\n"
        "s_cbranch_scc1 .Lat_pg1_chk_%=\n"
        "v_mov_b32 v13, 1\n"
        "buffer_store_dword v13, off, s[40:43], s65 sc1\n"
        "s_waitcnt vmcnt(0)\n"
        ".Lat_pg1_chk_%=:\n"
        "buffer_load_dword v13, off, s[40:43], s65 sc1\n"
        "s_waitcnt vmcnt(0)\n"
        "v_readfirstlane_b32 s65, v13\n"
        "s_nop 0\n"
        "s_cmp_lg_u32 s65, 1\n"
        "s_cbranch_scc1 .Lat_pg1_go_%=\n"
        "s_endpgm\n"
        ".Lat_pg1_go_%=:\n"
        "s_sleep 2\n"
        "s_branch .Lat_pg1_top_%=\n"
        ".Lat_pg1_ok_%=:\n"
        "v_cmp_eq_u32 s[54:55], 1, v13\n"
        "s_nop 1\n"
        "s_mov_b32 vcc_lo, 0x55555555\n"
        "s_mov_b32 vcc_hi, 0x55555555\n"
        "s_xor_b64 vcc, vcc, s[52:53]\n"
        "s_bcnt1_i32_b64 s65, vcc\n"
        "s_sub_u32 s64, s65, 23\n"
        "s_cmp_lt_u32 s64, 19\n"
        "s_cbranch_scc1 .Lat_rawmap_%=\n"
        "s_cmp_lt_u32 s65, 32\n"
        "s_cselect_b32 s65, 0, -1\n"
        "s_xor_b32 s52, s65, 0x55555555\n"
        "s_xor_b32 s53, s65, 0x55555555\n"
        "s_xor_b32 s54, s65, 0x55555555\n"
        "s_xor_b32 s55, s65, 0xaaaaaaaa\n"
        ".Lat_rawmap_%=:\n"
        "s_xor_b32 s52, s52, s66\n"
        "s_xor_b32 s53, s53, s66\n"
        "s_xor_b32 s54, s54, s66\n"
        "s_xor_b32 s55, s55, s66\n"
        "s_and_b32 s71, s55, 1\n"
        "s_lshl_b32 s71, s71, 13\n"
        "s_add_u32 s60, s60, s71\n"
        "v_mov_b64 v[166:167], 0\n"
        "v_mov_b64 v[172:173], 0\n"
        "v_mov_b64 v[176:177], 0\n"
        "v_mov_b64 v[178:179], 0\n"
        "v_mov_b64 v[182:183], 0\n"
        "v_mov_b32 v10, 0xff61b1e6\n"
        "s_mov_b32 s68, 0\n"
        "s_mov_b32 s72, 0\n"
        "s_bitcmp1_b64 s[52:53], s68\n"
        "s_cselect_b32 s65, 8192, 0\n"
        "s_lshl_b32 s70, s68, 21\n"
        "s_add_u32 s70, s70, s65\n"
        ".p2align 6\n"
        ".Lat_code_%=:\n"
        ".Lat_top_%=:\n"
        "s_mov_b32 s69, s70\n"
        "s_lshl_b32 s67, s68, 1\n"
        "s_mov_b32 s64, 0\n"
        "s_mov_b32 s86, s72\n"
        "s_cmp_eq_u32 s72, 7\n"
        "s_nop 0\n"
        "s_nop 0\n"
        "s_nop 0\n"
        "s_cbranch_scc1 .Lat_T_%=\n"
        ".Lat_more_%=:\n"
        "buffer_load_dword v5, v1, s[40:43], s71 offen sc1\n"
        "s_mov_b32 s86, s72\n"
        "s_waitcnt vmcnt(0)\n"
        "v_min_i32_dpp v5, v5, v5 quad_perm:[1,0,3,2] row_mask:0xf bank_mask:0xf\n"
        "s_nop 1\n"
        "v_min_i32_dpp v5, v5, v5 quad_perm:[2,3,0,1] row_mask:0xf bank_mask:0xf\n"
        "s_nop 0\n"
        "v_readlane_b32 s73, v5, 0\n"
        "v_readlane_b32 s74, v5, 4\n"
        "s_nop 0\n"
        "s_bitcmp1_b32 s72, 0\n"
        "s_cbranch_scc1 .Lat_hasK_%=\n"
        "s_add_u32 s65, s67, 1\n"
        "s_cmp_ge_i32 s73, s65\n"
        "s_cbranch_scc0 .Lat_hasK_%=\n"
        "s_add_u32 s65, s69, s58\n"
        "s_add_u32 s65, s65, 524288\n"
        "s_add_u32 s66, s65, 4096\n"
        "buffer_load_dwordx4 v[48:51], v0, s[40:43], s65 offen offset:0 sc1\n"
        "buffer_load_dwordx4 v[52:55], v0, s[40:43], s65 offen offset:1024 sc1\n"
        "buffer_load_dwordx4 v[56:59], v0, s[40:43], s65 offen offset:2048 sc1\n"
        "buffer_load_dwordx4 v[60:63], v0, s[40:43], s65 offen offset:3072 sc1\n"
        "buffer_load_dwordx4 v[64:67], v0, s[40:43], s66 offen offset:0 sc1\n"
        "buffer_load_dwordx4 v[68:71], v0, s[40:43], s66 offen offset:1024 sc1\n"
        "buffer_load_dwordx4 v[72:75], v0, s[40:43], s66 offen offset:2048 sc1\n"
        "buffer_load_dwordx4 v[76:79], v0, s[40:43], s66 offen offset:3072 sc1\n"
        "s_or_b32 s72, s72, 1\n"
        ".Lat_hasK_%=:\n"
        "s_bitcmp1_b32 s72, 1\n"
        "s_cbranch_scc1 .Lat_hasV_%=\n"
        "s_add_u32 s65, s67, 1\n"
        "s_cmp_ge_i32 s73, s65\n"
        "s_nop 0\n"
        "s_cbranch_scc0 .Lat_hasV_%=\n"
        "s_add_u32 s65, s69, s58\n"
        "s_add_u32 s65, s65, 1048576\n"
        "s_add_u32 s66, s65, 4096\n"
        "buffer_load_dwordx4 v[80:83], v0, s[40:43], s65 offen offset:0 sc1\n"
        "buffer_load_dwordx4 v[84:87], v0, s[40:43], s65 offen offset:1024 sc1\n"
        "buffer_load_dwordx4 v[88:91], v0, s[40:43], s65 offen offset:2048 sc1\n"
        "buffer_load_dwordx4 v[92:95], v0, s[40:43], s65 offen offset:3072 sc1\n"
        "buffer_load_dwordx4 v[96:99], v0, s[40:43], s66 offen offset:0 sc1\n"
        "buffer_load_dwordx4 v[100:103], v0, s[40:43], s66 offen offset:1024 sc1\n"
        "buffer_load_dwordx4 v[104:107], v0, s[40:43], s66 offen offset:2048 sc1\n"
        "buffer_load_dwordx4 v[108:111], v0, s[40:43], s66 offen offset:3072 sc1\n"
        "s_or_b32 s72, s72, 2\n"
        ".Lat_hasV_%=:\n"
        "s_bitcmp1_b32 s72, 3\n"
        "s_cbranch_scc1 .Lat_hasM_%=\n"
        "s_cmp_ge_i32 s74, s68\n"
        "s_cbranch_scc0 .Lat_hasM_%=\n"
        "s_add_u32 s65, s69, s59\n"
        "buffer_load_dwordx4 v[112:115], v3, s[40:43], s65 offen offset:0 sc1\n"
        "buffer_load_dwordx4 v[116:119], v3, s[40:43], s65 offen offset:512 sc1\n"
        "buffer_load_dwordx4 v[120:123], v3, s[40:43], s65 offen offset:1024 sc1\n"
        "buffer_load_dwordx4 v[124:127], v3, s[40:43], s65 offen offset:1536 sc1\n"
        "buffer_load_dwordx4 v[128:131], v4, s[40:43], s65 offen offset:2048 sc1\n"
        "buffer_load_dwordx4 v[132:135], v4, s[40:43], s65 offen offset:2560 sc1\n"
        "buffer_load_dwordx4 v[136:139], v4, s[40:43], s65 offen offset:3072 sc1\n"
        "buffer_load_dwordx4 v[140:143], v4, s[40:43], s65 offen offset:3584 sc1\n"
        "s_or_b32 s72, s72, 8\n"
        "s_nop 0\n"
        "s_nop 0\n"
        ".Lat_hasM_%=:\n"
        "s_bitcmp1_b32 s72, 2\n"
        "s_cbranch_scc1 .Lat_hasQ_%=\n"
        "s_add_u32 s65, s67, 1\n"
        "s_cmp_ge_i32 s73, s65\n"
        "s_cbranch_scc0 .Lat_hasQ_%=\n"
        "s_add_u32 s65, s69, s58\n"
        "s_add_u32 s65, s65, 0\n"
        "s_add_u32 s66, s65, 4096\n"
        "buffer_load_dwordx4 v[16:19], v0, s[40:43], s65 offen offset:0 sc1\n"
        "buffer_load_dwordx4 v[20:23], v0, s[40:43], s65 offen offset:1024 sc1\n"
        "buffer_load_dwordx4 v[24:27], v0, s[40:43], s65 offen offset:2048 sc1\n"
        "buffer_load_dwordx4 v[28:31], v0, s[40:43], s65 offen offset:3072 sc1\n"
        "buffer_load_dwordx4 v[32:35], v0, s[40:43], s66 offen offset:0 sc1\n"
        "buffer_load_dwordx4 v[36:39], v0, s[40:43], s66 offen offset:1024 sc1\n"
        "buffer_load_dwordx4 v[40:43], v0, s[40:43], s66 offen offset:2048 sc1\n"
        "buffer_load_dwordx4 v[44:47], v0, s[40:43], s66 offen offset:3072 sc1\n"
        "s_or_b32 s72, s72, 4\n"
        ".Lat_hasQ_%=:\n"
        "s_and_b32 s65, s72, 7\n"
        "s_cmp_eq_u32 s65, 7\n"
        "s_cbranch_scc1 .Lat_T_%=\n"
        "s_add_u32 s64, s64, 1\n"
        "s_and_b32 s65, s64, 255\n"
        "s_nop 0\n"
        "s_cbranch_scc1 .Lat_sp_go_%=\n"
        "s_mov_b32 s65, 49152\n"
        "s_cmp_lt_u32 s64, 1048576\n"
        "s_nop 0\n"
        "s_nop 0\n"
        "s_nop 0\n"
        "s_cbranch_scc1 .Lat_sp_chk_%=\n"
        "v_mov_b32 v6, 1\n"
        "buffer_store_dword v6, off, s[40:43], s65 sc1\n"
        "s_waitcnt vmcnt(0)\n"
        ".Lat_sp_chk_%=:\n"
        "buffer_load_dword v6, off, s[40:43], s65 sc1\n"
        "s_waitcnt vmcnt(0)\n"
        "v_readfirstlane_b32 s65, v6\n"
        "s_nop 0\n"
        "s_cmp_lg_u32 s65, 1\n"
        "s_cbranch_scc1 .Lat_sp_go_%=\n"
        "s_endpgm\n"
        ".p2align 5\n"
        ".Lat_sp_go_%=:\n"
        "s_branch .Lat_more_%=\n"
        ".p2align 5\n"
        ".Lat_T_%=:\n"
        "s_add_u32 s66, s68, 1\n"
        "s_bitcmp1_b64 s[52:53], s66\n"
        "s_cselect_b32 s65, 8192, 0\n"
        "s_lshl_b32 s70, s66, 21\n"
        "s_add_u32 s70, s70, s65\n"
        "s_add_u32 s67, s67, 2\n"
        "s_cmp_eq_u32 s86, 7\n"
        "s_cbranch_scc1 .Lat_T2_%=\n"
        "s_waitcnt vmcnt(0)\n"
        ".Lat_T2_%=:\n"
        "s_cmp_eq_u32 s72, 15\n"
        "s_cbranch_scc1 .Lat_T3_%=\n"
        "buffer_load_dword v5, v1, s[40:43], s71 offen sc1\n"
        ".Lat_T3_%=:\n"
        "s_waitcnt vmcnt(10)\n"
        "v_mfma_f32_16x16x32_bf16 v[144:147], v[48:51], v[16:19], 0\n"
        "v_mfma_f32_16x16x32_bf16 v[152:155], v[52:55], v[20:23], 0\n"
        "v_mfma_f32_16x16x32_bf16 v[148:151], v[48:51], v[20:23], 0\n"
        "v_mfma_f32_16x16x32_bf16 v[144:147], v[56:59], v[24:27], v[144:147]\n"
        "v_mfma_f32_16x16x32_bf16 v[152:155], v[60:63], v[28:31], v[152:155]\n"
        "v_mfma_f32_16x16x32_bf16 v[148:151], v[56:59], v[28:31], v[148:151]\n"
        "v_mfma_f32_16x16x32_bf16 v[144:147], v[64:67], v[32:35], v[144:147]\n"
        "v_mfma_f32_16x16x32_bf16 v[152:155], v[68:71], v[36:39], v[152:155]\n"
        "v_mfma_f32_16x16x32_bf16 v[148:151], v[64:67], v[36:39], v[148:151]\n"
        "v_mfma_f32_16x16x32_bf16 v[144:147], v[72:75], v[40:43], v[144:147]\n"
        "v_mfma_f32_16x16x32_bf16 v[152:155], v[76:79], v[44:47], v[152:155]\n"
        "v_mfma_f32_16x16x32_bf16 v[148:151], v[72:75], v[44:47], v[148:151]\n"
        "v_cndmask_b32 v144, v10, v144, s[76:77]\n"
        "v_cndmask_b32 v145, v10, v145, s[78:79]\n"
        "v_cndmask_b32 v146, v10, v146, s[80:81]\n"
        "v_cndmask_b32 v147, v10, v147, s[82:83]\n"
        "v_cndmask_b32 v152, v10, v152, s[76:77]\n"
        "v_cndmask_b32 v153, v10, v153, s[78:79]\n"
        "v_cndmask_b32 v154, v10, v154, s[80:81]\n"
        "v_cndmask_b32 v155, v10, v155, s[82:83]\n"
        "v_max3_f32 v8, v144, v145, v146\n"
        "v_max3_f32 v9, v152, v153, v154\n"
        "v_max_f32 v8, v8, v147\n"
        "v_max3_f32 v9, v9, v155, v148\n"
        "v_max3_f32 v9, v9, v149, v150\n"
        "v_max_f32 v9, v9, v151\n"
        "v_mul_f32 v144, s75, v144\n"
        "v_mul_f32 v145, s75, v145\n"
        "v_mul_f32 v146, s75, v146\n"
        "v_mul_f32 v147, s75, v147\n"
        "v_mul_f32 v148, s75, v148\n"
        "v_mul_f32 v149, s75, v149\n"
        "v_mul_f32 v150, s75, v150\n"
        "v_mul_f32 v151, s75, v151\n"
        "v_mul_f32 v152, s75, v152\n"
        "v_mul_f32 v153, s75, v153\n"
        "v_mul_f32_e64 v154, s75, v154\n"
        "v_mul_f32_e64 v155, s75, v155\n"
        "s_cmp_eq_u32 s72, 15\n"
        "s_cbranch_scc1 .Lat_hasm_%=\n"
        "s_mov_b32 s64, 0\n"
        ".Lat_wm_%=:\n"
        "s_waitcnt vmcnt(0)\n"
        "v_min_i32_dpp v5, v5, v5 quad_perm:[1,0,3,2] row_mask:0xf bank_mask:0xf\n"
        "s_nop 0\n"
        "s_nop 0\n"
        "v_min_i32_dpp v5, v5, v5 quad_perm:[2,3,0,1] row_mask:0xf bank_mask:0xf\n"
        "s_nop 0\n"
        "v_readlane_b32 s73, v5, 0\n"
        "v_readlane_b32 s74, v5, 4\n"
        "s_nop 0\n"
        "s_cmp_ge_i32 s74, s68\n"
        "s_cbranch_scc1 .Lat_gm_%=\n"
        "s_add_u32 s64, s64, 1\n"
        "s_and_b32 s65, s64, 255\n"
        "s_cbranch_scc1 .Lat_sq_go_%=\n"
        "s_mov_b32 s65, 49152\n"
        "s_cmp_lt_u32 s64, 1048576\n"
        "s_cbranch_scc1 .Lat_sq_chk_%=\n"
        "v_mov_b32 v6, 1\n"
        "buffer_store_dword v6, off, s[40:43], s65 sc1\n"
        "s_waitcnt vmcnt(0)\n"
        ".Lat_sq_chk_%=:\n"
        "buffer_load_dword v6, off, s[40:43], s65 sc1\n"
        "s_waitcnt vmcnt(0)\n"
        "v_readfirstlane_b32 s65, v6\n"
        "s_nop 0\n"
        "s_cmp_lg_u32 s65, 1\n"
        "s_cbranch_scc1 .Lat_sq_go_%=\n"
        "s_endpgm\n"
        ".p2align 5\n"
        ".Lat_sq_go_%=:\n"
        "buffer_load_dword v5, v1, s[40:43], s71 offen sc1\n"
        "s_branch .Lat_wm_%=\n"
        ".p2align 5\n"
        ".Lat_gm_%=:\n"
        "s_add_u32 s65, s69, s59\n"
        "buffer_load_dwordx4 v[112:115], v3, s[40:43], s65 offen offset:0 sc1\n"
        "buffer_load_dwordx4 v[116:119], v3, s[40:43], s65 offen offset:512 sc1\n"
        "buffer_load_dwordx4 v[120:123], v3, s[40:43], s65 offen offset:1024 sc1\n"
        "buffer_load_dwordx4 v[124:127], v3, s[40:43], s65 offen offset:1536 sc1\n"
        "buffer_load_dwordx4 v[128:131], v4, s[40:43], s65 offen offset:2048 sc1\n"
        "buffer_load_dwordx4 v[132:135], v4, s[40:43], s65 offen offset:2560 sc1\n"
        "buffer_load_dwordx4 v[136:139], v4, s[40:43], s65 offen offset:3072 sc1\n"
        "buffer_load_dwordx4 v[140:143], v4, s[40:43], s65 offen offset:3584 sc1\n"
        ".Lat_hasm_%=:\n"
        "s_mov_b32 s72, 0\n"
        "s_add_u32 s87, s70, s58\n"
        "s_add_u32 s87, s87, 524288\n"
        "s_add_u32 s88, s67, 1\n"
        "s_cmp_ge_i32 s73, s88\n"
        "s_cselect_b32 s87, s87, 241172480\n"
        "s_cselect_b32 s88, 1, 0\n"
        "s_or_b32 s72, s72, s88\n"
        "s_add_u32 s88, s87, 4096\n"
        "s_add_u32 s65, s69, s58\n"
        "s_add_u32 s66, s65, 4096\n"
        "s_lshl_b32 s89, s68, 1\n"
        "v_mov_b32 v6, s89\n"
        "s_waitcnt vmcnt(7)\n"
        "v_mfma_f32_16x16x32_bf16 v[156:159], v[112:115], v[16:19], 0\n"
        "s_waitcnt vmcnt(6)\n"
        "v_mfma_f32_16x16x32_bf16 v[156:159], v[116:119], v[24:27], v[156:159]\n"
        "s_waitcnt vmcnt(5)\n"
        "v_mfma_f32_16x16x32_bf16 v[156:159], v[120:123], v[32:35], v[156:159]\n"
        "s_waitcnt vmcnt(4)\n"
        "v_mfma_f32_16x16x32_bf16 v[156:159], v[124:127], v[40:43], v[156:159]\n"
        "v_mfma_f32_16x16x32_bf16 v[160:163], v[112:115], v[20:23], 0\n"
        "v_mfma_f32_16x16x32_bf16 v[160:163], v[116:119], v[28:31], v[160:163]\n"
        "v_mfma_f32_16x16x32_bf16 v[160:163], v[120:123], v[36:39], v[160:163]\n"
        "v_mfma_f32_16x16x32_bf16 v[160:163], v[124:127], v[44:47], v[160:163]\n"
        "v_max3_f32 v8, v8, v156, v157\n"
        "v_max3_f32 v216, v8, v158, v159\n"
        "v_max3_f32 v217, v8, v158, v159\n"
        "v_mul_f32 v156, s75, v156\n"
        "v_mul_f32 v157, s75, v157\n"
        "v_permlane16_swap_b32 v216, v217\n"
        "v_max_f32 v8, v216, v217\n"
        "v_max_f32 v216, v216, v217\n"
        "v_mul_f32 v158, s75, v158\n"
        "v_mul_f32 v159, s75, v159\n"
        "v_permlane32_swap_b32 v8, v216\n"
        "v_max_f32 v8, v8, v216\n"
        "v_mul_f32 v8, s75, v8\n"
        "v_cndmask_b32 v216, v8, -v10, s[84:85]\n"
        "v_sub_f32 v144, v144, v8\n"
        "v_exp_f32 v144, v144\n"
        "v_sub_f32 v145, v145, v8\n"
        "v_exp_f32 v145, v145\n"
        "v_sub_f32 v146, v146, v8\n"
        "v_exp_f32 v146, v146\n"
        "v_sub_f32 v147, v147, v8\n"
        "v_exp_f32 v147, v147\n"
        "v_sub_f32 v156, v156, v216\n"
        "v_exp_f32 v156, v156\n"
        "v_sub_f32 v157, v157, v216\n"
        "v_exp_f32 v157, v157\n"
        "v_sub_f32 v158, v158, v216\n"
        "v_exp_f32 v158, v158\n"
        "v_sub_f32 v159, v159, v216\n"
        "v_exp_f32 v159, v159\n"
        "v_add_f32 v216, v144, v145\n"
        "v_add_f32 v217, v146, v147\n"
        "v_add_f32 v216, v216, v156\n"
        "v_add_f32 v217, v217, v157\n"
        "v_add_f32 v216, v216, v158\n"
        "v_add_f32 v217, v217, v159\n"
        "v_add_f32 v8, v216, v217\n"
        "v_add_f32 v216, v216, v217\n"
        "s_nop 1\n"
        "v_permlane16_swap_b32 v8, v216\n"
        "v_add_f32 v217, v8, v216\n"
        "v_add_f32 v216, v8, v216\n"
        "s_nop 1\n"
        "v_permlane32_swap_b32 v216, v217\n"
        "v_add_f32 v8, v216, v217\n"
        "v_rcp_f32 v8, v8\n"
        "s_nop 0\n"
        "v_mul_f32 v156, v156, v8\n"
        "v_mul_f32 v157, v157, v8\n"
        "v_mul_f32 v158, v158, v8\n"
        "v_mul_f32 v159, v159, v8\n"
        "v_cvt_pk_bf16_f32 v174, v156, v157\n"
        "v_cvt_pk_bf16_f32 v175, v158, v159\n"
        "v_mul_f32 v144, v144, v8\n"
        "v_mul_f32 v145, v145, v8\n"
        "v_cvt_pk_bf16_f32 v164, v144, v145\n"
        "v_mul_f32 v146, v146, v8\n"
        "v_mul_f32 v147, v147, v8\n"
        "v_cvt_pk_bf16_f32 v165, v146, v147\n"
        "s_waitcnt vmcnt(0)\n"
        "s_nop 0\n"
        "v_mfma_f32_16x16x32_bf16 v[184:187], v[128:131], v[174:177], 0\n"
        "v_max3_f32 v9, v9, v160, v161\n"
        "v_max3_f32 v220, v9, v162, v163\n"
        "v_max3_f32 v221, v9, v162, v163\n"
        "v_mfma_f32_16x16x32_bf16 v[188:191], v[128:131], v[172:175], 0\n"
        "v_mul_f32 v160, s75, v160\n"
        "v_mul_f32 v161, s75, v161\n"
        "v_mfma_f32_16x16x32_bf16 v[184:187], v[80:83], v[164:167], v[184:187]\n"
        "v_permlane16_swap_b32 v220, v221\n"
        "v_max_f32 v9, v220, v221\n"
        "v_mfma_f32_16x16x32_bf16 v[188:191], v[84:87], v[164:167], v[188:191]\n"
        "v_max_f32 v220, v220, v221\n"
        "v_mul_f32 v162, s75, v162\n"
        "v_mul_f32 v163, s75, v163\n"
        "v_mfma_f32_16x16x32_bf16 v[192:195], v[132:135], v[174:177], 0\n"
        "v_permlane32_swap_b32 v9, v220\n"
        "v_max_f32 v9, v9, v220\n"
        "v_mfma_f32_16x16x32_bf16 v[196:199], v[132:135], v[172:175], 0\n"
        "v_cvt_pk_bf16_f32 v184, v184, v185\n"
        "v_cvt_pk_bf16_f32 v185, v186, v187\n"
        "v_cvt_pk_bf16_f32 v186, v188, v189\n"
        "v_mfma_f32_16x16x32_bf16 v[192:195], v[88:91], v[164:167], v[192:195]\n"
        "v_cvt_pk_bf16_f32 v187, v190, v191\n"
        "buffer_store_dwordx4 v[184:187], v0, s[40:43], s65 offen offset:0 sc1\n"
        "v_mfma_f32_16x16x32_bf16 v[196:199], v[92:95], v[164:167], v[196:199]\n"
        "v_mul_f32 v9, s75, v9\n"
        "v_cndmask_b32 v220, v9, -v10, s[84:85]\n"
        "v_sub_f32 v148, v148, v9\n"
        "v_mfma_f32_16x16x32_bf16 v[200:203], v[136:139], v[174:177], 0\n"
        "v_exp_f32 v148, v148\n"
        "v_sub_f32 v149, v149, v9\n"
        "v_mfma_f32_16x16x32_bf16 v[204:207], v[136:139], v[172:175], 0\n"
        "v_cvt_pk_bf16_f32 v192, v192, v193\n"
        "v_cvt_pk_bf16_f32 v193, v194, v195\n"
        "v_cvt_pk_bf16_f32 v194, v196, v197\n"
        "v_mfma_f32_16x16x32_bf16 v[200:203], v[96:99], v[164:167], v[200:203]\n"
        "v_cvt_pk_bf16_f32 v195, v198, v199\n"
        "buffer_store_dwordx4 v[192:195], v0, s[40:43], s65 offen offset:1024 sc1\n"
        "v_mfma_f32_16x16x32_bf16 v[204:207], v[100:103], v[164:167], v[204:207]\n"
        "v_exp_f32 v149, v149\n"
        "v_sub_f32 v150, v150, v9\n"
        "v_mfma_f32_16x16x32_bf16 v[208:211], v[140:143], v[174:177], 0\n"
        "v_exp_f32 v150, v150\n"
        "v_sub_f32 v151, v151, v9\n"
        "v_mfma_f32_16x16x32_bf16 v[212:215], v[140:143], v[172:175], 0\n"
        "v_cvt_pk_bf16_f32 v200, v200, v201\n"
        "v_cvt_pk_bf16_f32 v201, v202, v203\n"
        "v_cvt_pk_bf16_f32 v202, v204, v205\n"
        "v_mfma_f32_16x16x32_bf16 v[208:211], v[104:107], v[164:167], v[208:211]\n"
        "v_cvt_pk_bf16_f32 v203, v206, v207\n"
        "buffer_store_dwordx4 v[200:203], v0, s[40:43], s65 offen offset:2048 sc1\n"
        "v_mfma_f32_16x16x32_bf16 v[212:215], v[108:111], v[164:167], v[212:215]\n"
        "v_exp_f32 v151, v151\n"
        "v_sub_f32 v152, v152, v9\n"
        "v_exp_f32 v152, v152\n"
        "v_sub_f32 v153, v153, v9\n"
        "v_exp_f32 v153, v153\n"
        "v_sub_f32 v154, v154, v9\n"
        "v_exp_f32 v154, v154\n"
        "v_sub_f32 v155, v155, v9\n"
        "v_exp_f32 v155, v155\n"
        "v_sub_f32 v160, v160, v220\n"
        "v_cvt_pk_bf16_f32 v208, v208, v209\n"
        "v_cvt_pk_bf16_f32 v209, v210, v211\n"
        "v_cvt_pk_bf16_f32 v210, v212, v213\n"
        "v_cvt_pk_bf16_f32 v211, v214, v215\n"
        "buffer_store_dwordx4 v[208:211], v0, s[40:43], s65 offen offset:3072 sc1\n"
        "v_exp_f32 v160, v160\n"
        "v_sub_f32 v161, v161, v220\n"
        "v_exp_f32 v161, v161\n"
        "v_sub_f32 v162, v162, v220\n"
        "v_exp_f32 v162, v162\n"
        "v_sub_f32 v163, v163, v220\n"
        "buffer_load_dwordx4 v[48:51], v0, s[40:43], s87 offen offset:0 sc1\n"
        "v_exp_f32 v163, v163\n"
        "v_add_f32 v220, v148, v149\n"
        "v_add_f32 v221, v150, v151\n"
        "v_add_f32 v220, v220, v152\n"
        "v_add_f32 v221, v221, v153\n"
        "v_add_f32 v220, v220, v154\n"
        "buffer_load_dwordx4 v[52:55], v0, s[40:43], s87 offen offset:1024 sc1\n"
        "v_add_f32 v221, v221, v155\n"
        "v_add_f32 v220, v220, v160\n"
        "v_add_f32 v221, v221, v161\n"
        "v_add_f32 v220, v220, v162\n"
        "v_add_f32 v221, v221, v163\n"
        "v_add_f32 v9, v220, v221\n"
        "buffer_load_dwordx4 v[56:59], v0, s[40:43], s87 offen offset:2048 sc1\n"
        "v_add_f32 v220, v220, v221\n"
        "s_nop 1\n"
        "v_permlane16_swap_b32 v9, v220\n"
        "v_add_f32 v221, v9, v220\n"
        "v_add_f32 v220, v9, v220\n"
        "s_nop 1\n"
        "buffer_load_dwordx4 v[60:63], v0, s[40:43], s87 offen offset:3072 sc1\n"
        "v_permlane32_swap_b32 v220, v221\n"
        "v_add_f32 v9, v220, v221\n"
        "v_rcp_f32 v9, v9\n"
        "s_nop 0\n"
        "v_mul_f32 v160, v160, v9\n"
        "v_mul_f32 v161, v161, v9\n"
        "buffer_load_dwordx4 v[64:67], v0, s[40:43], s88 offen offset:0 sc1\n"
        "v_mul_f32 v162, v162, v9\n"
        "v_mul_f32 v163, v163, v9\n"
        "v_cvt_pk_bf16_f32 v180, v160, v161\n"
        "v_cvt_pk_bf16_f32 v181, v162, v163\n"
        "v_mul_f32 v148, v148, v9\n"
        "v_mul_f32 v149, v149, v9\n"
        "buffer_load_dwordx4 v[68:71], v0, s[40:43], s88 offen offset:1024 sc1\n"
        "v_cvt_pk_bf16_f32 v168, v148, v149\n"
        "v_mul_f32 v150, v150, v9\n"
        "v_mul_f32 v151, v151, v9\n"
        "v_cvt_pk_bf16_f32 v169, v150, v151\n"
        "v_mul_f32 v152, v152, v9\n"
        "v_mul_f32 v153, v153, v9\n"
        "buffer_load_dwordx4 v[72:75], v0, s[40:43], s88 offen offset:2048 sc1\n"
        "v_cvt_pk_bf16_f32 v170, v152, v153\n"
        "v_mul_f32 v154, v154, v9\n"
        "v_mul_f32 v155, v155, v9\n"
        "v_cvt_pk_bf16_f32 v171, v154, v155\n"
        "s_nop 1\n"
        "v_mfma_f32_16x16x32_bf16 v[184:187], v[128:131], v[180:183], 0\n"
        "buffer_load_dwordx4 v[76:79], v0, s[40:43], s88 offen offset:3072 sc1\n"
        "v_mfma_f32_16x16x32_bf16 v[188:191], v[128:131], v[178:181], 0\n"
        "buffer_load_dword v5, v1, s[40:43], s71 offen sc1\n"
        "v_mfma_f32_16x16x32_bf16 v[184:187], v[80:83], v[168:171], v[184:187]\n"
        "v_mfma_f32_16x16x32_bf16 v[188:191], v[84:87], v[168:171], v[188:191]\n"
        "v_mfma_f32_16x16x32_bf16 v[192:195], v[132:135], v[180:183], 0\n"
        "s_waitcnt vmcnt(9)\n"
        "buffer_store_dword v6, v2, s[40:43], s60 offen sc1\n"
        "v_mfma_f32_16x16x32_bf16 v[196:199], v[132:135], v[178:181], 0\n"
        "v_cvt_pk_bf16_f32 v184, v184, v185\n"
        "v_cvt_pk_bf16_f32 v185, v186, v187\n"
        "v_cvt_pk_bf16_f32 v186, v188, v189\n"
        "v_mfma_f32_16x16x32_bf16 v[192:195], v[88:91], v[168:171], v[192:195]\n"
        "v_cvt_pk_bf16_f32 v187, v190, v191\n"
        "buffer_store_dwordx4 v[184:187], v0, s[40:43], s66 offen offset:0 sc1\n"
        "v_mfma_f32_16x16x32_bf16 v[196:199], v[92:95], v[168:171], v[196:199]\n"
        "v_mfma_f32_16x16x32_bf16 v[200:203], v[136:139], v[180:183], 0\n"
        "v_mfma_f32_16x16x32_bf16 v[204:207], v[136:139], v[178:181], 0\n"
        "v_cvt_pk_bf16_f32 v192, v192, v193\n"
        "v_cvt_pk_bf16_f32 v193, v194, v195\n"
        "v_cvt_pk_bf16_f32 v194, v196, v197\n"
        "v_mfma_f32_16x16x32_bf16 v[200:203], v[96:99], v[168:171], v[200:203]\n"
        "v_cvt_pk_bf16_f32 v195, v198, v199\n"
        "buffer_store_dwordx4 v[192:195], v0, s[40:43], s66 offen offset:1024 sc1\n"
        "v_mfma_f32_16x16x32_bf16 v[204:207], v[100:103], v[168:171], v[204:207]\n"
        "v_mfma_f32_16x16x32_bf16 v[208:211], v[140:143], v[180:183], 0\n"
        "v_mfma_f32_16x16x32_bf16 v[212:215], v[140:143], v[178:181], 0\n"
        "v_cvt_pk_bf16_f32 v200, v200, v201\n"
        "v_cvt_pk_bf16_f32 v201, v202, v203\n"
        "v_cvt_pk_bf16_f32 v202, v204, v205\n"
        "v_mfma_f32_16x16x32_bf16 v[208:211], v[104:107], v[168:171], v[208:211]\n"
        "v_cvt_pk_bf16_f32 v203, v206, v207\n"
        "buffer_store_dwordx4 v[200:203], v0, s[40:43], s66 offen offset:2048 sc1\n"
        "v_mfma_f32_16x16x32_bf16 v[212:215], v[108:111], v[168:171], v[212:215]\n"
        "s_waitcnt vmcnt(4)\n"
        "v_min_i32_dpp v5, v5, v5 quad_perm:[1,0,3,2] row_mask:0xf bank_mask:0xf\n"
        "s_nop 1\n"
        "v_min_i32_dpp v5, v5, v5 quad_perm:[2,3,0,1] row_mask:0xf bank_mask:0xf\n"
        "s_nop 0\n"
        "v_readlane_b32 s73, v5, 0\n"
        "v_readlane_b32 s74, v5, 4\n"
        "s_nop 0\n"
        "s_nop 0\n"
        "v_cvt_pk_bf16_f32 v208, v208, v209\n"
        "v_cvt_pk_bf16_f32 v209, v210, v211\n"
        "v_cvt_pk_bf16_f32 v210, v212, v213\n"
        "v_cvt_pk_bf16_f32 v211, v214, v215\n"
        "buffer_store_dwordx4 v[208:211], v0, s[40:43], s66 offen offset:3072 sc1\n"
        "s_add_u32 s65, s70, s58\n"
        "s_add_u32 s65, s65, 0\n"
        "s_add_u32 s66, s67, 1\n"
        "s_cmp_ge_i32 s73, s66\n"
        "s_cselect_b32 s65, s65, 241172480\n"
        "s_cselect_b32 s66, 4, 0\n"
        "s_or_b32 s72, s72, s66\n"
        "s_add_u32 s66, s65, 4096\n"
        "buffer_load_dwordx4 v[16:19], v0, s[40:43], s65 offen offset:0 sc1\n"
        "buffer_load_dwordx4 v[20:23], v0, s[40:43], s65 offen offset:1024 sc1\n"
        "buffer_load_dwordx4 v[24:27], v0, s[40:43], s65 offen offset:2048 sc1\n"
        "buffer_load_dwordx4 v[28:31], v0, s[40:43], s65 offen offset:3072 sc1\n"
        "buffer_load_dwordx4 v[32:35], v0, s[40:43], s66 offen offset:0 sc1\n"
        "buffer_load_dwordx4 v[36:39], v0, s[40:43], s66 offen offset:1024 sc1\n"
        "buffer_load_dwordx4 v[40:43], v0, s[40:43], s66 offen offset:2048 sc1\n"
        "buffer_load_dwordx4 v[44:47], v0, s[40:43], s66 offen offset:3072 sc1\n"
        "s_add_u32 s65, s70, s58\n"
        "s_add_u32 s65, s65, 1048576\n"
        "s_add_u32 s66, s67, 1\n"
        "s_cmp_ge_i32 s73, s66\n"
        "s_cselect_b32 s65, s65, 241172480\n"
        "s_cselect_b32 s66, 2, 0\n"
        "s_or_b32 s72, s72, s66\n"
        "s_add_u32 s66, s65, 4096\n"
        "v_add_u32 v6, 1, v6\n"
        "s_waitcnt vmcnt(8)\n"
        "buffer_store_dword v6, v2, s[40:43], s60 offen sc1\n"
        "buffer_load_dwordx4 v[80:83], v0, s[40:43], s65 offen offset:0 sc1\n"
        "buffer_load_dwordx4 v[84:87], v0, s[40:43], s65 offen offset:1024 sc1\n"
        "buffer_load_dwordx4 v[88:91], v0, s[40:43], s65 offen offset:2048 sc1\n"
        "buffer_load_dwordx4 v[92:95], v0, s[40:43], s65 offen offset:3072 sc1\n"
        "buffer_load_dwordx4 v[96:99], v0, s[40:43], s66 offen offset:0 sc1\n"
        "buffer_load_dwordx4 v[100:103], v0, s[40:43], s66 offen offset:1024 sc1\n"
        "buffer_load_dwordx4 v[104:107], v0, s[40:43], s66 offen offset:2048 sc1\n"
        "buffer_load_dwordx4 v[108:111], v0, s[40:43], s66 offen offset:3072 sc1\n"
        "s_add_u32 s68, s68, 1\n"
        "s_cmp_lt_u32 s68, 64\n"
        "s_cbranch_scc1 .Lat_top_%=\n"
        "s_cmp_eq_u32 s61, 0\n"
        "s_cbranch_scc0 .Lat_bye_%=\n"
        "s_and_b32 s66, s57, 7\n"
        "s_lshl_b32 s66, s66, 1\n"
        "s_add_u32 s66, s66, s50\n"
        "s_lshl_b32 s66, s66, 7\n"
        "s_add_u32 s66, s66, 51456\n"
        ".Lat_bye_%=:\n"
        "s_endpgm\n"
        ".Lat_end_%=:\n"
        :: "s"(p.ws), "s"(pair(x4, pr)), "s"(pair(PAGE + h * 16384, PAGE + spread(A_KM) + (h >> 1) * 16384 + (h & 1) * 4096)), "s"(pair(0, w)),
           "v"(pair(lane * 16, lane < 8 ? flag(((lane & 4) ? F_CH : F_S1 + 128) + 4 * h + (lane & 3)) : OOB)), "v"(pair(lane < 3 ? flag(F_AT + 3 * h + lane) : OOB, (g * 8 + (i & 7)) * 16)),
           "v"(pair((g & 1) * 256 + i * 16, 4 * g - i))
        : "v0", "v1", "v2", "v3", "v4", "v5", "v6", "v7", "v8", "v9", "v10", "v11", "v12", "v13", "v14", "v15", "v16", "v17", "v18", "v19", "v20", "v21", "v22", "v23", "v24", "v25", "v26", "v27", "v28", "v29", "v30", "v31", "v32", "v33", "v34", "v35", "v36", "v37", "v38", "v39", "v40", "v41", "v42", "v43", "v44", "v45", "v46", "v47", "v48", "v49", "v50", "v51", "v52", "v53", "v54", "v55", "v56", "v57", "v58", "v59", "v60", "v61", "v62", "v63", "v64", "v65", "v66", "v67", "v68", "v69", "v70", "v71", "v72", "v73", "v74", "v75", "v76", "v77", "v78", "v79", "v255", "a255", "s26", "s27", "s28", "s29", "s30", "s31", "s32", "s33", "s34", "s35", "s36", "s37", "s38", "s39", "s40", "s41", "s42", "s43", "s44", "s45", "s46", "s47", "s48", "s49", "s50", "s51", "s52", "s53", "s54", "s55", "s56", "s57", "s58", "s59", "s60", "s61", "s62", "s63", "s64", "s65", "s66", "s67", "s68", "s69", "s70", "s71", "s72", "s73", "s74", "s75", "s76", "s77", "s78", "s79", "s80", "s81", "s82", "s83", "s84", "s85", "s86", "s87", "s88", "s89", "s90", "s91", "s92", "s93", "s94", "s95", "s96", "s97", "s98", "s99", "vcc", "scc", "memory");
    __builtin_unreachable();
}

template <bool R5>
__device__ __forceinline__ void role_red(const Params& p, int a, int x4, int pr, u64 epoch) {
    const unsigned lane = threadIdx.x & 63, i = lane & 15, g = lane >> 4;
    const int w = rfl(threadIdx.x >> 6);
    if (w == 3) return;
    unsigned to[3], po[3], one[4];
#pragma unroll
    for (int n = 0; n < 3; ++n) { const unsigned b = 3 * w + n; to[n] = b < 8 ? a * 16384 + b * 1024 : 65536 + a * 1024; po[n] = part_off(a, 0, 8 * b); }
#pragma unroll
    for (int e = 0; e < 4; ++e) one[e] = ((i & 7) == 2 * e) ? 0x3f80u : (((i & 7) == 2 * e + 1) ? 0x3f800000u : 0u);
    const u64 o0 = pair(one[0], one[1]), o1 = pair(one[2], one[3]);
    const bool lo = i < 8, r0 = g == 2 * (i >> 3), r1 = g == 1 + 2 * (i >> 3);
    const unsigned fin = lane < 16 ? flag((R5 ? F_F2 : F_OP) + a * 64 + w * 16 + lane) : OOB;
    const unsigned fout = (w == 0 && lane < (R5 ? 64 : 48)) ? flag((R5 ? F_XN : F_R3) + 8 * (lane >> 1) + 2 * a + (lane & 1)) : OOB;
#define RED_ARGS :: "s"(p.ws), "s"(p.x), "s"(p.out), "s"(pair(x4 | (a >> 1) << 8, pr)), "s"(pair(to[0], to[1])), "s"(pair(to[2], po[0])), "s"(pair(po[1], po[2])), \
        "v"(pair(lane * 16, fin)), "v"(pair(fout, g * 16384 + i * 16)), "v"(pair(g * (w == 2 ? 1024 : 16384) + i * 16, (8 * a + (lane & 7)) * 1152 + w * 384 + (lane >> 3) * 16)), \
        "v"(pair(w * 64 + (lane & 7) * 8, (lane & 7) * 8)), "v"(lo ? o0 : 0), "v"(lo ? o1 : 0), "v"(lo ? 0 : o0), "v"(lo ? 0 : o1), \
        "v"(r0 ? o0 : 0), "v"(r0 ? o1 : 0), "v"(r1 ? o0 : 0), "v"(r1 ? o1 : 0), "v"(epoch + 8), "v"(pair(a == 0 && threadIdx.x == 0 ? 0u : OOB, a == 3 && threadIdx.x < 18 ? (G_DONE + threadIdx.x) * 128 : OOB))
    if constexpr (R5) {
        asm volatile(
        "s_nop 4\n"
        "s_mov_b64 s[40:41], %0\n"
        "s_and_b32 s41, s41, 0xffff\n"
        "s_mov_b32 s42, 236978176\n"
        "s_mov_b32 s43, 0x20000\n"
        "s_mov_b64 s[44:45], %1\n"
        "s_and_b32 s45, s45, 0xffff\n"
        "s_mov_b32 s46, 2359296\n"
        "s_mov_b32 s47, 0x20000\n"
        "s_mov_b64 s[56:57], %3\n"
        "s_mov_b64 s[58:59], %4\n"
        "s_mov_b64 s[60:61], %5\n"
        "s_mov_b64 s[62:63], %6\n"
        "v_mov_b64 v[0:1], %7\n"
        "v_mov_b64 v[2:3], %8\n"
        "v_mov_b64 v[4:5], %9\n"
        "v_mov_b64 v[6:7], %10\n"
        "v_mov_b64 v[8:9], %11\n"
        "v_mov_b64 v[10:11], %12\n"
        "v_mov_b64 v[12:13], %13\n"
        "v_mov_b64 v[14:15], %14\n"
        "v_mov_b64 v[16:17], %15\n"
        "v_mov_b64 v[18:19], %16\n"
        "v_mov_b64 v[20:21], %17\n"
        "v_mov_b64 v[22:23], %18\n"
        "v_mov_b64 v[24:25], %19\n"
        "v_mov_b64 v[26:27], %20\n"
        "v_mov_b64 v[200:201], v[24:25]\n"
        "v_mov_b64 v[202:203], v[26:27]\n"
        "s_lshr_b32 s74, s56, 8\n"
        "s_and_b32 s56, s56, 1\n"
        "s_mov_b32 s68, 0\n"
        "s_mul_i32 s65, s68, 36864\n"
        "buffer_load_dwordx4 v[80:83], v5, s[44:47], s65 offen offset:0\n"
        "buffer_load_dwordx4 v[84:87], v5, s[44:47], s65 offen offset:128\n"
        "buffer_load_dwordx4 v[88:91], v5, s[44:47], s65 offen offset:256\n"
        "s_getpc_b64 s[76:77]\n"
        ".Lr5_pc_%=:\n"
        "s_add_u32 s76, s76, .Lr5_code_%=-.Lr5_pc_%=\n"
        "s_addc_u32 s77, s77, 0\n"
        "s_and_b32 s77, s77, 0xffff\n"
        "s_and_b32 s76, s76, -4\n"
        "s_mov_b32 s78, .Lr5_end_%=-.Lr5_code_%=\n"
        "s_mov_b32 s79, 0x20000\n"
        "s_mov_b32 s65, 0\n"
        "buffer_load_dwordx4 v[32:35], v0, s[76:79], s65 offen\n"
        "s_add_u32 s65, s65, 1024\n"
        "buffer_load_dwordx4 v[36:39], v0, s[76:79], s65 offen\n"
        "s_add_u32 s65, s65, 1024\n"
        "buffer_load_dwordx4 v[40:43], v0, s[76:79], s65 offen\n"
        "s_add_u32 s65, s65, 1024\n"
        "buffer_load_dwordx4 v[44:47], v0, s[76:79], s65 offen\n"
        "s_add_u32 s65, s65, 1024\n"
        "buffer_load_dwordx4 v[48:51], v0, s[76:79], s65 offen\n"
        "s_add_u32 s65, s65, 1024\n"
        "v_lshrrev_b32 v26, 4, v0\n"
        "s_mov_b32 s50, s56\n"
        "v_lshlrev_b32 v28, 7, v26\n"
        "s_sub_u32 s66, 0, s50\n"
        "s_mov_b32 s52, 32768\n"
        "s_mov_b32 s64, 0\n"
        ".Lr5_pg0_top_%=:\n"
        "buffer_load_dword v27, v28, s[40:43], s52 offen sc1\n"
        "s_waitcnt vmcnt(0)\n"
        "v_cmp_gt_i32 vcc, 0, v27\n"
        "s_cbranch_vccz .Lr5_pg0_ok_%=\n"
        "s_add_u32 s64, s64, 1\n"
        "s_and_b32 s65, s64, 255\n"
        "s_cbranch_scc1 .Lr5_pg0_go_%=\n"
        "s_mov_b32 s65, 49152\n"
        "s_cmp_lt_u32 s64, 1048576\n"
        "s_cbranch_scc1 .Lr5_pg0_chk_%=\n"
        "v_mov_b32 v27, 1\n"
        "buffer_store_dword v27, off, s[40:43], s65 sc1\n"
        "s_waitcnt vmcnt(0)\n"
        ".Lr5_pg0_chk_%=:\n"
        "buffer_load_dword v27, off, s[40:43], s65 sc1\n"
        "s_waitcnt vmcnt(0)\n"
        "v_readfirstlane_b32 s65, v27\n"
        "s_nop 0\n"
        "s_cmp_lg_u32 s65, 1\n"
        "s_cbranch_scc1 .Lr5_pg0_go_%=\n"
        "s_endpgm\n"
        ".Lr5_pg0_go_%=:\n"
        "s_sleep 2\n"
        "s_branch .Lr5_pg0_top_%=\n"
        ".Lr5_pg0_ok_%=:\n"
        "v_cmp_eq_u32 s[52:53], 1, v27\n"
        "s_nop 1\n"
        "s_mov_b32 s54, 40960\n"
        "s_mov_b32 s64, 0\n"
        ".Lr5_pg1_top_%=:\n"
        "buffer_load_dword v27, v28, s[40:43], s54 offen sc1\n"
        "s_waitcnt vmcnt(0)\n"
        "v_cmp_gt_i32 vcc, 0, v27\n"
        "s_cbranch_vccz .Lr5_pg1_ok_%=\n"
        "s_add_u32 s64, s64, 1\n"
        "s_and_b32 s65, s64, 255\n"
        "s_cbranch_scc1 .Lr5_pg1_go_%=\n"
        "s_mov_b32 s65, 49152\n"
        "s_cmp_lt_u32 s64, 1048576\n"
        "s_cbranch_scc1 .Lr5_pg1_chk_%=\n"
        "v_mov_b32 v27, 1\n"
        "buffer_store_dword v27, off, s[40:43], s65 sc1\n"
        "s_waitcnt vmcnt(0)\n"
        ".Lr5_pg1_chk_%=:\n"
        "buffer_load_dword v27, off, s[40:43], s65 sc1\n"
        "s_waitcnt vmcnt(0)\n"
        "v_readfirstlane_b32 s65, v27\n"
        "s_nop 0\n"
        "s_cmp_lg_u32 s65, 1\n"
        "s_cbranch_scc1 .Lr5_pg1_go_%=\n"
        "s_endpgm\n"
        ".Lr5_pg1_go_%=:\n"
        "s_sleep 2\n"
        "s_branch .Lr5_pg1_top_%=\n"
        ".Lr5_pg1_ok_%=:\n"
        "v_cmp_eq_u32 s[54:55], 1, v27\n"
        "s_nop 1\n"
        "s_mov_b32 vcc_lo, 0x55555555\n"
        "s_mov_b32 vcc_hi, 0x55555555\n"
        "s_xor_b64 vcc, vcc, s[52:53]\n"
        "s_bcnt1_i32_b64 s65, vcc\n"
        "s_sub_u32 s64, s65, 23\n"
        "s_cmp_lt_u32 s64, 19\n"
        "s_cbranch_scc1 .Lr5_rawmap_%=\n"
        "s_cmp_lt_u32 s65, 32\n"
        "s_cselect_b32 s65, 0, -1\n"
        "s_xor_b32 s52, s65, 0x55555555\n"
        "s_xor_b32 s53, s65, 0x55555555\n"
        "s_xor_b32 s54, s65, 0x55555555\n"
        "s_xor_b32 s55, s65, 0xaaaaaaaa\n"
        ".Lr5_rawmap_%=:\n"
        "s_xor_b32 s52, s52, s66\n"
        "s_xor_b32 s53, s53, s66\n"
        "s_xor_b32 s54, s54, s66\n"
        "s_xor_b32 s55, s55, s66\n"
        "s_and_b32 s69, s55, 1\n"
        "s_lshl_b32 s69, s69, 13\n"
        "s_xor_b32 s70, s69, 8192\n"
        "s_cmp_eq_u32 s50, 0\n"
        "s_cbranch_scc0 .Lr5_l1_%=\n"
        ".p2align 6\n"
        ".Lr5_code_%=:\n"
        ".Lr5_l0_%=:\n"
        "s_bitcmp1_b64 s[52:53], s68\n"
        "s_cselect_b32 s65, 8192, 0\n"
        "s_lshl_b32 s71, s68, 21\n"
        "s_add_u32 s71, s71, s65\n"
        "s_add_u32 s71, s71, 2097152\n"
        "s_lshr_b32 s65, s68, 2\n"
        "s_add_u32 s65, s65, 1\n"
        "s_bitcmp1_b32 s55, s65\n"
        "s_cselect_b32 s65, 8192, 0\n"
        "s_lshl_b32 s72, s68, 19\n"
        "s_add_u32 s72, s72, s65\n"
        "s_add_u32 s72, s72, 203423744\n"
        "s_add_u32 s76, s72, s61\n"
        "s_add_u32 s79, s71, s58\n"
        "s_add_u32 s79, s79, 1998848\n"
        "s_add_u32 s77, s72, s62\n"
        "s_add_u32 s80, s71, s59\n"
        "s_add_u32 s80, s80, 1998848\n"
        "s_add_u32 s78, s72, s63\n"
        "s_add_u32 s81, s71, s60\n"
        "s_add_u32 s81, s81, 1998848\n"
        "s_lshl_b32 s73, s68, 1\n"
        "s_add_u32 s73, s73, s74\n"
        "s_mov_b32 s64, 0\n"
        ".Lr5_l0w_top_%=:\n"
        "buffer_load_dword v24, v1, s[40:43], s69 offen sc1\n"
        "s_waitcnt vmcnt(0)\n"
        "v_cmp_gt_i32_e64 vcc, s73, v24\n"
        "s_nop 0\n"
        "s_and_b64 vcc, vcc, 0xffff\n"
        "s_nop 0\n"
        "s_cbranch_vccz .Lr5_l0w_ok_%=\n"
        "s_add_u32 s64, s64, 1\n"
        "s_and_b32 s65, s64, 255\n"
        "s_cbranch_scc1 .Lr5_l0w_go_%=\n"
        "s_mov_b32 s65, 49152\n"
        "s_cmp_lt_u32 s64, 1048576\n"
        "s_cbranch_scc1 .Lr5_l0w_chk_%=\n"
        "v_mov_b32 v24, 1\n"
        "buffer_store_dword v24, off, s[40:43], s65 sc1\n"
        "s_waitcnt vmcnt(0)\n"
        ".Lr5_l0w_chk_%=:\n"
        "buffer_load_dword v24, off, s[40:43], s65 sc1\n"
        "s_waitcnt vmcnt(0)\n"
        "v_readfirstlane_b32 s65, v24\n"
        "s_nop 0\n"
        "s_cmp_lg_u32 s65, 1\n"
        "s_cbranch_scc1 .Lr5_l0w_go_%=\n"
        "s_endpgm\n"
        ".p2align 5\n"
        ".Lr5_l0w_go_%=:\n"
        "s_branch .Lr5_l0w_top_%=\n"
        ".p2align 5\n"
        ".Lr5_l0w_ok_%=:\n"
        "buffer_load_dwordx4 v[32:35], v3, s[40:43], s76 offen offset:0 sc1\n"
        "buffer_load_dwordx4 v[40:43], v3, s[40:43], s76 offen offset:512 sc1\n"
        "buffer_load_dwordx4 v[36:39], v3, s[40:43], s76 offen offset:256 sc1\n"
        "buffer_load_dwordx4 v[44:47], v3, s[40:43], s76 offen offset:768 sc1\n"
        "buffer_load_dwordx4 v[80:83], v0, s[40:43], s79 offen sc1\n"
        "buffer_load_dwordx4 v[48:51], v3, s[40:43], s77 offen offset:0 sc1\n"
        "buffer_load_dwordx4 v[56:59], v3, s[40:43], s77 offen offset:512 sc1\n"
        "buffer_load_dwordx4 v[52:55], v3, s[40:43], s77 offen offset:256 sc1\n"
        "buffer_load_dwordx4 v[60:63], v3, s[40:43], s77 offen offset:768 sc1\n"
        "buffer_load_dwordx4 v[84:87], v0, s[40:43], s80 offen sc1\n"
        "buffer_load_dwordx4 v[64:67], v4, s[40:43], s78 offen offset:0 sc1\n"
        "buffer_load_dwordx4 v[72:75], v4, s[40:43], s78 offen offset:512 sc1\n"
        "buffer_load_dwordx4 v[68:71], v4, s[40:43], s78 offen offset:256 sc1\n"
        "buffer_load_dwordx4 v[76:79], v4, s[40:43], s78 offen offset:768 sc1\n"
        "buffer_load_dwordx4 v[88:91], v0, s[40:43], s81 offen sc1\n"
        "s_xor_b32 s71, s71, 8192\n"
        "s_add_u32 s72, s71, 1835008\n"
        "s_add_u32 s71, s71, 1916928\n"
        "s_add_u32 s82, s72, s58\n"
        "s_add_u32 s85, s71, s58\n"
        "s_add_u32 s83, s72, s59\n"
        "s_add_u32 s86, s71, s59\n"
        "s_add_u32 s84, s72, s60\n"
        "s_add_u32 s87, s71, s60\n"
        "s_waitcnt vmcnt(14)\n"
        "v_mfma_f32_16x16x32_bf16 v[92:95], v[8:11], v[32:35], 0\n"
        "s_waitcnt vmcnt(13)\n"
        "v_mfma_f32_16x16x32_bf16 v[92:95], v[12:15], v[40:43], v[92:95]\n"
        "s_waitcnt vmcnt(12)\n"
        "v_mfma_f32_16x16x32_bf16 v[96:99], v[8:11], v[36:39], 0\n"
        "s_waitcnt vmcnt(11)\n"
        "v_mfma_f32_16x16x32_bf16 v[96:99], v[12:15], v[44:47], v[96:99]\n"
        "s_waitcnt vmcnt(10)\n"
        "v_mfma_f32_16x16x32_bf16 v[92:95], v[16:19], v[80:83], v[92:95]\n"
        "v_mfma_f32_16x16x32_bf16 v[96:99], v[20:23], v[80:83], v[96:99]\n"
        "s_waitcnt vmcnt(9)\n"
        "v_mfma_f32_16x16x32_bf16 v[100:103], v[8:11], v[48:51], 0\n"
        "s_waitcnt vmcnt(8)\n"
        "v_mfma_f32_16x16x32_bf16 v[100:103], v[12:15], v[56:59], v[100:103]\n"
        "s_waitcnt vmcnt(7)\n"
        "v_mfma_f32_16x16x32_bf16 v[104:107], v[8:11], v[52:55], 0\n"
        "s_waitcnt vmcnt(6)\n"
        "v_mfma_f32_16x16x32_bf16 v[104:107], v[12:15], v[60:63], v[104:107]\n"
        "s_waitcnt vmcnt(5)\n"
        "v_mfma_f32_16x16x32_bf16 v[100:103], v[16:19], v[84:87], v[100:103]\n"
        "v_mfma_f32_16x16x32_bf16 v[104:107], v[20:23], v[84:87], v[104:107]\n"
        "s_waitcnt vmcnt(4)\n"
        "v_mfma_f32_16x16x32_bf16 v[108:111], v[8:11], v[64:67], 0\n"
        "s_waitcnt vmcnt(3)\n"
        "v_mfma_f32_16x16x32_bf16 v[108:111], v[12:15], v[72:75], v[108:111]\n"
        "s_waitcnt vmcnt(2)\n"
        "v_mfma_f32_16x16x32_bf16 v[112:115], v[8:11], v[68:71], 0\n"
        "s_waitcnt vmcnt(1)\n"
        "v_mfma_f32_16x16x32_bf16 v[112:115], v[12:15], v[76:79], v[112:115]\n"
        "s_waitcnt vmcnt(0)\n"
        "v_mfma_f32_16x16x32_bf16 v[108:111], v[16:19], v[88:91], v[108:111]\n"
        "v_mfma_f32_16x16x32_bf16 v[112:115], v[20:23], v[88:91], v[112:115]\n"
        "s_add_u32 s68, s68, 1\n"
        "s_sub_u32 s68, s68, 1\n"
        "v_pk_add_f32 v[164:165], v[92:93], v[94:95]\n"
        "v_pk_add_f32 v[166:167], v[96:97], v[98:99]\n"
        "v_pk_add_f32 v[168:169], v[100:101], v[102:103]\n"
        "v_pk_add_f32 v[170:171], v[104:105], v[106:107]\n"
        "v_pk_add_f32 v[164:165], v[164:165], v[166:167]\n"
        "v_pk_add_f32 v[168:169], v[168:169], v[170:171]\n"
        "v_pk_add_f32 v[164:165], v[164:165], v[168:169]\n"
        "s_nop 0\n"
        "v_pk_add_f32 v[166:167], v[108:109], v[110:111]\n"
        "v_pk_add_f32 v[168:169], v[112:113], v[114:115]\n"
        "v_pk_add_f32 v[164:165], v[164:165], v[166:167]\n"
        "v_pk_add_f32 v[164:165], v[164:165], v[168:169]\n"
        "v_add_f32 v164, v164, v165\n"
        "v_cvt_pk_bf16_f32 v140, v92, v93\n"
        "v_cvt_pk_bf16_f32 v141, v94, v95\n"
        "v_add_f32_dpp v166, v164, v164 row_ror:8 row_mask:0xf bank_mask:0xf\n"
        "v_add_f32_dpp v167, v164, v164 row_ror:8 row_mask:0xf bank_mask:0xf\n"
        "v_cvt_pk_bf16_f32 v142, v96, v97\n"
        "v_cvt_pk_bf16_f32 v143, v98, v99\n"
        "v_permlane16_swap_b32 v166, v167\n"
        "v_add_f32 v164, v166, v167\n"
        "v_add_f32 v166, v166, v167\n"
        "v_cvt_pk_bf16_f32 v144, v100, v101\n"
        "v_cvt_pk_bf16_f32 v145, v102, v103\n"
        "v_permlane32_swap_b32 v164, v166\n"
        "v_add_f32 v164, v164, v166\n"
        "v_mul_f32 v164, 0x3baaaaab, v164\n"
        "v_pk_add_f32 v[116:117], v[92:93], v[164:165] op_sel_hi:[1,0] neg_lo:[0,1] neg_hi:[0,1]\n"
        "v_pk_add_f32 v[118:119], v[94:95], v[164:165] op_sel_hi:[1,0] neg_lo:[0,1] neg_hi:[0,1]\n"
        "v_pk_add_f32 v[120:121], v[96:97], v[164:165] op_sel_hi:[1,0] neg_lo:[0,1] neg_hi:[0,1]\n"
        "v_pk_add_f32 v[122:123], v[98:99], v[164:165] op_sel_hi:[1,0] neg_lo:[0,1] neg_hi:[0,1]\n"
        "v_pk_add_f32 v[124:125], v[100:101], v[164:165] op_sel_hi:[1,0] neg_lo:[0,1] neg_hi:[0,1]\n"
        "v_pk_add_f32 v[126:127], v[102:103], v[164:165] op_sel_hi:[1,0] neg_lo:[0,1] neg_hi:[0,1]\n"
        "v_pk_add_f32 v[128:129], v[104:105], v[164:165] op_sel_hi:[1,0] neg_lo:[0,1] neg_hi:[0,1]\n"
        "v_pk_add_f32 v[130:131], v[106:107], v[164:165] op_sel_hi:[1,0] neg_lo:[0,1] neg_hi:[0,1]\n"
        "v_pk_add_f32 v[132:133], v[108:109], v[164:165] op_sel_hi:[1,0] neg_lo:[0,1] neg_hi:[0,1]\n"
        "v_pk_add_f32 v[134:135], v[110:111], v[164:165] op_sel_hi:[1,0] neg_lo:[0,1] neg_hi:[0,1]\n"
        "v_pk_add_f32 v[136:137], v[112:113], v[164:165] op_sel_hi:[1,0] neg_lo:[0,1] neg_hi:[0,1]\n"
        "v_pk_add_f32 v[138:139], v[114:115], v[164:165] op_sel_hi:[1,0] neg_lo:[0,1] neg_hi:[0,1]\n"
        "v_pk_mul_f32 v[166:167], v[116:117], v[116:117]\n"
        "v_pk_mul_f32 v[168:169], v[118:119], v[118:119]\n"
        "v_pk_fma_f32 v[166:167], v[120:121], v[120:121], v[166:167]\n"
        "v_pk_fma_f32 v[168:169], v[122:123], v[122:123], v[168:169]\n"
        "v_pk_fma_f32 v[166:167], v[124:125], v[124:125], v[166:167]\n"
        "v_pk_fma_f32 v[168:169], v[126:127], v[126:127], v[168:169]\n"
        "v_pk_fma_f32 v[166:167], v[128:129], v[128:129], v[166:167]\n"
        "v_pk_fma_f32 v[168:169], v[130:131], v[130:131], v[168:169]\n"
        "v_pk_fma_f32 v[166:167], v[132:133], v[132:133], v[166:167]\n"
        "v_pk_fma_f32 v[168:169], v[134:135], v[134:135], v[168:169]\n"
        "v_pk_fma_f32 v[166:167], v[136:137], v[136:137], v[166:167]\n"
        "v_pk_fma_f32 v[168:169], v[138:139], v[138:139], v[168:169]\n"
        "v_pk_add_f32 v[166:167], v[166:167], v[168:169]\n"
        "v_add_f32 v165, v166, v167\n"
        "v_cvt_pk_bf16_f32 v146, v104, v105\n"
        "v_cvt_pk_bf16_f32 v147, v106, v107\n"
        "v_add_f32_dpp v166, v165, v165 row_ror:8 row_mask:0xf bank_mask:0xf\n"
        "v_add_f32_dpp v167, v165, v165 row_ror:8 row_mask:0xf bank_mask:0xf\n"
        "v_cvt_pk_bf16_f32 v148, v108, v109\n"
        "v_cvt_pk_bf16_f32 v149, v110, v111\n"
        "v_permlane16_swap_b32 v166, v167\n"
        "v_add_f32 v165, v166, v167\n"
        "v_add_f32 v166, v166, v167\n"
        "v_cvt_pk_bf16_f32 v150, v112, v113\n"
        "v_cvt_pk_bf16_f32 v151, v114, v115\n"
        "v_permlane32_swap_b32 v165, v166\n"
        "v_add_f32 v165, v165, v166\n"
        "ds_write_b64 v6, v[164:165]\n"
        "v_permlane16_swap_b32 v140, v142\n"
        "v_permlane16_swap_b32 v141, v143\n"
        "v_permlane16_swap_b32 v144, v146\n"
        "v_permlane16_swap_b32 v145, v147\n"
        "s_waitcnt lgkmcnt(0)\n"
        "s_barrier\n"
        "ds_read_b64 v[166:167], v7 offset:0\n"
        "ds_read_b64 v[168:169], v7 offset:64\n"
        "ds_read_b64 v[170:171], v7 offset:128\n"
        "v_permlane16_swap_b32 v148, v150\n"
        "v_permlane16_swap_b32 v149, v151\n"
        "buffer_store_dwordx4 v[140:143], v0, s[40:43], s85 offen sc1\n"
        "buffer_store_dwordx4 v[144:147], v0, s[40:43], s86 offen sc1\n"
        "buffer_store_dwordx4 v[148:151], v0, s[40:43], s87 offen sc1\n"
        "s_waitcnt lgkmcnt(0)\n"
        "v_add_f32 v172, v166, v168\n"
        "v_add_f32 v173, v167, v169\n"
        "v_add_f32 v172, v172, v170\n"
        "v_add_f32 v173, v173, v171\n"
        "v_mul_f32 v172, 0x3eaaaaab, v172\n"
        "v_sub_f32 v166, v166, v172\n"
        "v_sub_f32 v168, v168, v172\n"
        "v_sub_f32 v170, v170, v172\n"
        "v_mul_f32 v166, v166, v166\n"
        "v_fmac_f32 v166, v168, v168\n"
        "v_fmac_f32 v166, v170, v170\n"
        "v_fmamk_f32 v173, v166, 0x43400000, v173\n"
        "v_mul_f32 v173, 0x3ae38e39, v173\n"
        "v_cvt_pk_bf16_f32 v172, v172, v173\n"
        "v_and_b32 v173, 0xffff0000, v172\n"
        "v_lshlrev_b32 v172, 16, v172\n"
        "v_add_f32 v173, 0x3727c5ac, v173\n"
        "v_cvt_pk_bf16_f32 v173, v173, v173\n"
        "v_lshlrev_b32 v173, 16, v173\n"
        "v_sqrt_f32 v173, v173\n"
        "v_sub_f32 v172, v164, v172\n"
        "v_cvt_pk_bf16_f32 v173, v173, v173\n"
        "v_lshlrev_b32 v173, 16, v173\n"
        "v_rcp_f32 v173, v173\n"
        "v_mov_b32 v25, s68\n"
        "v_mul_f32 v172, v172, v173\n"
        "v_pk_fma_f32 v[116:117], v[116:117], v[172:173], v[172:173] op_sel:[0,1,0] op_sel_hi:[1,1,0]\n"
        "v_pk_fma_f32 v[118:119], v[118:119], v[172:173], v[172:173] op_sel:[0,1,0] op_sel_hi:[1,1,0]\n"
        "v_pk_fma_f32 v[120:121], v[120:121], v[172:173], v[172:173] op_sel:[0,1,0] op_sel_hi:[1,1,0]\n"
        "v_pk_fma_f32 v[122:123], v[122:123], v[172:173], v[172:173] op_sel:[0,1,0] op_sel_hi:[1,1,0]\n"
        "v_cvt_pk_bf16_f32 v152, v116, v117\n"
        "v_cvt_pk_bf16_f32 v153, v118, v119\n"
        "v_cvt_pk_bf16_f32 v154, v120, v121\n"
        "v_cvt_pk_bf16_f32 v155, v122, v123\n"
        "s_nop 0\n"
        "v_permlane16_swap_b32 v152, v154\n"
        "v_permlane16_swap_b32 v153, v155\n"
        "buffer_store_dwordx4 v[152:155], v0, s[40:43], s82 offen sc1\n"
        "v_pk_fma_f32 v[124:125], v[124:125], v[172:173], v[172:173] op_sel:[0,1,0] op_sel_hi:[1,1,0]\n"
        "v_pk_fma_f32 v[126:127], v[126:127], v[172:173], v[172:173] op_sel:[0,1,0] op_sel_hi:[1,1,0]\n"
        "v_pk_fma_f32 v[128:129], v[128:129], v[172:173], v[172:173] op_sel:[0,1,0] op_sel_hi:[1,1,0]\n"
        "v_pk_fma_f32 v[130:131], v[130:131], v[172:173], v[172:173] op_sel:[0,1,0] op_sel_hi:[1,1,0]\n"
        "v_cvt_pk_bf16_f32 v156, v124, v125\n"
        "v_cvt_pk_bf16_f32 v157, v126, v127\n"
        "v_cvt_pk_bf16_f32 v158, v128, v129\n"
        "v_cvt_pk_bf16_f32 v159, v130, v131\n"
        "s_nop 0\n"
        "v_permlane16_swap_b32 v156, v158\n"
        "v_permlane16_swap_b32 v157, v159\n"
        "buffer_store_dwordx4 v[156:159], v0, s[40:43], s83 offen sc1\n"
        "v_pk_fma_f32 v[132:133], v[132:133], v[172:173], v[172:173] op_sel:[0,1,0] op_sel_hi:[1,1,0]\n"
        "v_pk_fma_f32 v[134:135], v[134:135], v[172:173], v[172:173] op_sel:[0,1,0] op_sel_hi:[1,1,0]\n"
        "v_pk_fma_f32 v[136:137], v[136:137], v[172:173], v[172:173] op_sel:[0,1,0] op_sel_hi:[1,1,0]\n"
        "v_pk_fma_f32 v[138:139], v[138:139], v[172:173], v[172:173] op_sel:[0,1,0] op_sel_hi:[1,1,0]\n"
        "v_cvt_pk_bf16_f32 v160, v132, v133\n"
        "v_cvt_pk_bf16_f32 v161, v134, v135\n"
        "v_cvt_pk_bf16_f32 v162, v136, v137\n"
        "v_cvt_pk_bf16_f32 v163, v138, v139\n"
        "s_nop 0\n"
        "v_permlane16_swap_b32 v160, v162\n"
        "v_permlane16_swap_b32 v161, v163\n"
        "buffer_store_dwordx4 v[160:163], v0, s[40:43], s84 offen sc1\n"
        "s_waitcnt vmcnt(0)\n"
        "s_barrier\n"
        "buffer_store_dword v25, v2, s[40:43], s70 offen sc1\n"
        "s_add_u32 s68, s68, 1\n"
        "s_cmp_lt_u32 s68, 64\n"
        "s_cbranch_scc1 .Lr5_l0_%=\n"
        "s_endpgm\n"
        ".p2align 5\n"
        ".Lr5_l1_%=:\n"
        "s_bitcmp1_b64 s[52:53], s68\n"
        "s_cselect_b32 s65, 8192, 0\n"
        "s_lshl_b32 s71, s68, 21\n"
        "s_add_u32 s71, s71, s65\n"
        "s_add_u32 s71, s71, 2097152\n"
        "s_xor_b32 s71, s71, 8192\n"
        "s_add_u32 s72, s71, 1835008\n"
        "s_add_u32 s71, s71, 1916928\n"
        "s_add_u32 s82, s72, s58\n"
        "s_add_u32 s85, s71, s58\n"
        "s_add_u32 s83, s72, s59\n"
        "s_add_u32 s86, s71, s59\n"
        "s_add_u32 s84, s72, s60\n"
        "s_add_u32 s87, s71, s60\n"
        "s_waitcnt vmcnt(0)\n"
        "v_mfma_f32_16x16x32_bf16 v[92:95], v[16:19], v[80:83], 0\n"
        "v_mfma_f32_16x16x32_bf16 v[96:99], v[20:23], v[80:83], 0\n"
        "v_mfma_f32_16x16x32_bf16 v[100:103], v[16:19], v[84:87], 0\n"
        "v_mfma_f32_16x16x32_bf16 v[104:107], v[20:23], v[84:87], 0\n"
        "v_mfma_f32_16x16x32_bf16 v[108:111], v[16:19], v[88:91], 0\n"
        "v_mfma_f32_16x16x32_bf16 v[112:115], v[20:23], v[88:91], 0\n"
        "s_add_u32 s68, s68, 1\n"
        "s_mul_i32 s65, s68, 36864\n"
        "buffer_load_dwordx4 v[80:83], v5, s[44:47], s65 offen offset:0\n"
        "buffer_load_dwordx4 v[84:87], v5, s[44:47], s65 offen offset:128\n"
        "buffer_load_dwordx4 v[88:91], v5, s[44:47], s65 offen offset:256\n"
        "s_sub_u32 s68, s68, 1\n"
        "v_pk_add_f32 v[164:165], v[92:93], v[94:95]\n"
        "v_pk_add_f32 v[166:167], v[96:97], v[98:99]\n"
        "v_pk_add_f32 v[168:169], v[100:101], v[102:103]\n"
        "v_pk_add_f32 v[170:171], v[104:105], v[106:107]\n"
        "v_pk_add_f32 v[164:165], v[164:165], v[166:167]\n"
        "v_pk_add_f32 v[168:169], v[168:169], v[170:171]\n"
        "v_pk_add_f32 v[164:165], v[164:165], v[168:169]\n"
        "v_pk_add_f32 v[166:167], v[108:109], v[110:111]\n"
        "v_pk_add_f32 v[168:169], v[112:113], v[114:115]\n"
        "v_pk_add_f32 v[164:165], v[164:165], v[166:167]\n"
        "v_pk_add_f32 v[164:165], v[164:165], v[168:169]\n"
        "v_add_f32 v164, v164, v165\n"
        "s_nop 1\n"
        "v_add_f32_dpp v166, v164, v164 row_ror:8 row_mask:0xf bank_mask:0xf\n"
        "v_add_f32_dpp v167, v164, v164 row_ror:8 row_mask:0xf bank_mask:0xf\n"
        "s_nop 1\n"
        "v_permlane16_swap_b32 v166, v167\n"
        "v_add_f32 v164, v166, v167\n"
        "v_add_f32 v166, v166, v167\n"
        "s_nop 1\n"
        "v_permlane32_swap_b32 v164, v166\n"
        "v_add_f32 v164, v164, v166\n"
        "v_mul_f32 v164, 0x3baaaaab, v164\n"
        "v_pk_add_f32 v[116:117], v[92:93], v[164:165] op_sel_hi:[1,0] neg_lo:[0,1] neg_hi:[0,1]\n"
        "v_pk_add_f32 v[118:119], v[94:95], v[164:165] op_sel_hi:[1,0] neg_lo:[0,1] neg_hi:[0,1]\n"
        "v_pk_add_f32 v[120:121], v[96:97], v[164:165] op_sel_hi:[1,0] neg_lo:[0,1] neg_hi:[0,1]\n"
        "v_pk_add_f32 v[122:123], v[98:99], v[164:165] op_sel_hi:[1,0] neg_lo:[0,1] neg_hi:[0,1]\n"
        "v_pk_add_f32 v[124:125], v[100:101], v[164:165] op_sel_hi:[1,0] neg_lo:[0,1] neg_hi:[0,1]\n"
        "v_pk_add_f32 v[126:127], v[102:103], v[164:165] op_sel_hi:[1,0] neg_lo:[0,1] neg_hi:[0,1]\n"
        "v_pk_add_f32 v[128:129], v[104:105], v[164:165] op_sel_hi:[1,0] neg_lo:[0,1] neg_hi:[0,1]\n"
        "v_pk_add_f32 v[130:131], v[106:107], v[164:165] op_sel_hi:[1,0] neg_lo:[0,1] neg_hi:[0,1]\n"
        "v_pk_add_f32 v[132:133], v[108:109], v[164:165] op_sel_hi:[1,0] neg_lo:[0,1] neg_hi:[0,1]\n"
        "v_pk_add_f32 v[134:135], v[110:111], v[164:165] op_sel_hi:[1,0] neg_lo:[0,1] neg_hi:[0,1]\n"
        "v_pk_add_f32 v[136:137], v[112:113], v[164:165] op_sel_hi:[1,0] neg_lo:[0,1] neg_hi:[0,1]\n"
        "v_pk_add_f32 v[138:139], v[114:115], v[164:165] op_sel_hi:[1,0] neg_lo:[0,1] neg_hi:[0,1]\n"
        "v_pk_mul_f32 v[166:167], v[116:117], v[116:117]\n"
        "v_pk_mul_f32 v[168:169], v[118:119], v[118:119]\n"
        "v_pk_fma_f32 v[166:167], v[120:121], v[120:121], v[166:167]\n"
        "v_pk_fma_f32 v[168:169], v[122:123], v[122:123], v[168:169]\n"
        "v_pk_fma_f32 v[166:167], v[124:125], v[124:125], v[166:167]\n"
        "v_pk_fma_f32 v[168:169], v[126:127], v[126:127], v[168:169]\n"
        "v_pk_fma_f32 v[166:167], v[128:129], v[128:129], v[166:167]\n"
        "v_pk_fma_f32 v[168:169], v[130:131], v[130:131], v[168:169]\n"
        "v_pk_fma_f32 v[166:167], v[132:133], v[132:133], v[166:167]\n"
        "v_pk_fma_f32 v[168:169], v[134:135], v[134:135], v[168:169]\n"
        "v_pk_fma_f32 v[166:167], v[136:137], v[136:137], v[166:167]\n"
        "v_pk_fma_f32 v[168:169], v[138:139], v[138:139], v[168:169]\n"
        "v_pk_add_f32 v[166:167], v[166:167], v[168:169]\n"
        "v_add_f32 v165, v166, v167\n"
        "s_nop 1\n"
        "v_add_f32_dpp v166, v165, v165 row_ror:8 row_mask:0xf bank_mask:0xf\n"
        "v_add_f32_dpp v167, v165, v165 row_ror:8 row_mask:0xf bank_mask:0xf\n"
        "s_nop 1\n"
        "v_permlane16_swap_b32 v166, v167\n"
        "v_add_f32 v165, v166, v167\n"
        "v_add_f32 v166, v166, v167\n"
        "s_nop 1\n"
        "v_permlane32_swap_b32 v165, v166\n"
        "v_add_f32 v165, v165, v166\n"
        "ds_write_b64 v6, v[164:165]\n"
        "s_waitcnt lgkmcnt(0)\n"
        "s_barrier\n"
        "ds_read_b64 v[166:167], v7 offset:0\n"
        "ds_read_b64 v[168:169], v7 offset:64\n"
        "ds_read_b64 v[170:171], v7 offset:128\n"
        "s_waitcnt lgkmcnt(0)\n"
        "v_add_f32 v172, v166, v168\n"
        "v_add_f32 v173, v167, v169\n"
        "v_add_f32 v172, v172, v170\n"
        "v_add_f32 v173, v173, v171\n"
        "v_mul_f32 v172, 0x3eaaaaab, v172\n"
        "v_sub_f32 v166, v166, v172\n"
        "v_sub_f32 v168, v168, v172\n"
        "v_sub_f32 v170, v170, v172\n"
        "v_mul_f32 v166, v166, v166\n"
        "v_fmac_f32 v166, v168, v168\n"
        "v_fmac_f32 v166, v170, v170\n"
        "v_fmamk_f32 v173, v166, 0x43400000, v173\n"
        "v_mul_f32 v173, 0x3ae38e39, v173\n"
        "v_cvt_pk_bf16_f32 v172, v172, v173\n"
        "v_and_b32 v173, 0xffff0000, v172\n"
        "v_lshlrev_b32 v172, 16, v172\n"
        "v_add_f32 v173, 0x3727c5ac, v173\n"
        "v_cvt_pk_bf16_f32 v173, v173, v173\n"
        "v_lshlrev_b32 v173, 16, v173\n"
        "v_sqrt_f32 v173, v173\n"
        "v_sub_f32 v172, v164, v172\n"
        "v_cvt_pk_bf16_f32 v173, v173, v173\n"
        "v_lshlrev_b32 v173, 16, v173\n"
        "v_rcp_f32 v173, v173\n"
        "v_mov_b32 v25, s68\n"
        "v_mul_f32 v172, v172, v173\n"
        "v_pk_fma_f32 v[116:117], v[116:117], v[172:173], v[172:173] op_sel:[0,1,0] op_sel_hi:[1,1,0]\n"
        "v_pk_fma_f32 v[118:119], v[118:119], v[172:173], v[172:173] op_sel:[0,1,0] op_sel_hi:[1,1,0]\n"
        "v_pk_fma_f32 v[120:121], v[120:121], v[172:173], v[172:173] op_sel:[0,1,0] op_sel_hi:[1,1,0]\n"
        "v_pk_fma_f32 v[122:123], v[122:123], v[172:173], v[172:173] op_sel:[0,1,0] op_sel_hi:[1,1,0]\n"
        "v_cvt_pk_bf16_f32 v152, v116, v117\n"
        "v_cvt_pk_bf16_f32 v153, v118, v119\n"
        "v_cvt_pk_bf16_f32 v154, v120, v121\n"
        "v_cvt_pk_bf16_f32 v155, v122, v123\n"
        "s_nop 0\n"
        "v_permlane16_swap_b32 v152, v154\n"
        "v_permlane16_swap_b32 v153, v155\n"
        "buffer_store_dwordx4 v[152:155], v0, s[40:43], s82 offen sc1\n"
        "v_pk_fma_f32 v[124:125], v[124:125], v[172:173], v[172:173] op_sel:[0,1,0] op_sel_hi:[1,1,0]\n"
        "v_pk_fma_f32 v[126:127], v[126:127], v[172:173], v[172:173] op_sel:[0,1,0] op_sel_hi:[1,1,0]\n"
        "v_pk_fma_f32 v[128:129], v[128:129], v[172:173], v[172:173] op_sel:[0,1,0] op_sel_hi:[1,1,0]\n"
        "v_pk_fma_f32 v[130:131], v[130:131], v[172:173], v[172:173] op_sel:[0,1,0] op_sel_hi:[1,1,0]\n"
        "v_cvt_pk_bf16_f32 v156, v124, v125\n"
        "v_cvt_pk_bf16_f32 v157, v126, v127\n"
        "v_cvt_pk_bf16_f32 v158, v128, v129\n"
        "v_cvt_pk_bf16_f32 v159, v130, v131\n"
        "s_nop 0\n"
        "v_permlane16_swap_b32 v156, v158\n"
        "v_permlane16_swap_b32 v157, v159\n"
        "buffer_store_dwordx4 v[156:159], v0, s[40:43], s83 offen sc1\n"
        "v_pk_fma_f32 v[132:133], v[132:133], v[172:173], v[172:173] op_sel:[0,1,0] op_sel_hi:[1,1,0]\n"
        "v_pk_fma_f32 v[134:135], v[134:135], v[172:173], v[172:173] op_sel:[0,1,0] op_sel_hi:[1,1,0]\n"
        "v_pk_fma_f32 v[136:137], v[136:137], v[172:173], v[172:173] op_sel:[0,1,0] op_sel_hi:[1,1,0]\n"
        "v_pk_fma_f32 v[138:139], v[138:139], v[172:173], v[172:173] op_sel:[0,1,0] op_sel_hi:[1,1,0]\n"
        "v_cvt_pk_bf16_f32 v160, v132, v133\n"
        "v_cvt_pk_bf16_f32 v161, v134, v135\n"
        "v_cvt_pk_bf16_f32 v162, v136, v137\n"
        "v_cvt_pk_bf16_f32 v163, v138, v139\n"
        "s_nop 0\n"
        "v_permlane16_swap_b32 v160, v162\n"
        "v_permlane16_swap_b32 v161, v163\n"
        "buffer_store_dwordx4 v[160:163], v0, s[40:43], s84 offen sc1\n"
        "s_waitcnt vmcnt(0)\n"
        "s_barrier\n"
        "buffer_store_dword v25, v2, s[40:43], s70 offen sc1\n"
        "s_add_u32 s68, s68, 1\n"
        "s_cmp_lt_u32 s68, 64\n"
        "s_cbranch_scc1 .Lr5_l1_%=\n"
        "s_mov_b32 s68, 0\n"
        "s_mov_b64 s[44:45], %2\n"
        "s_and_b32 s45, s45, 0xffff\n"
        ".Lr5_fin_%=:\n"
        "s_bitcmp1_b64 s[52:53], s68\n"
        "s_cselect_b32 s65, 8192, 0\n"
        "s_lshl_b32 s71, s68, 21\n"
        "s_add_u32 s71, s71, s65\n"
        "s_add_u32 s71, s71, 2097152\n"
        "s_lshr_b32 s65, s68, 2\n"
        "s_add_u32 s65, s65, 1\n"
        "s_bitcmp1_b32 s55, s65\n"
        "s_cselect_b32 s65, 8192, 0\n"
        "s_lshl_b32 s72, s68, 19\n"
        "s_add_u32 s72, s72, s65\n"
        "s_add_u32 s72, s72, 203423744\n"
        "s_add_u32 s76, s72, s61\n"
        "s_add_u32 s79, s71, s58\n"
        "s_add_u32 s79, s79, 1998848\n"
        "s_add_u32 s77, s72, s62\n"
        "s_add_u32 s80, s71, s59\n"
        "s_add_u32 s80, s80, 1998848\n"
        "s_add_u32 s78, s72, s63\n"
        "s_add_u32 s81, s71, s60\n"
        "s_add_u32 s81, s81, 1998848\n"
        "s_lshl_b32 s73, s68, 1\n"
        "s_add_u32 s73, s73, s74\n"
        "s_mov_b32 s64, 0\n"
        ".Lr5_finw_top_%=:\n"
        "buffer_load_dword v24, v1, s[40:43], s69 offen sc1\n"
        "s_waitcnt vmcnt(0)\n"
        "v_cmp_gt_i32 vcc, s73, v24\n"
        "s_nop 0\n"
        "s_and_b64 vcc, vcc, 0xffff\n"
        "s_cbranch_vccz .Lr5_finw_ok_%=\n"
        "s_add_u32 s64, s64, 1\n"
        "s_and_b32 s65, s64, 255\n"
        "s_nop 0\n"
        "s_cbranch_scc1 .Lr5_finw_go_%=\n"
        "s_mov_b32 s65, 49152\n"
        "s_cmp_lt_u32 s64, 1048576\n"
        "s_nop 0\n"
        "s_nop 0\n"
        "s_nop 0\n"
        "s_cbranch_scc1 .Lr5_finw_chk_%=\n"
        "v_mov_b32 v24, 1\n"
        "buffer_store_dword v24, off, s[40:43], s65 sc1\n"
        "s_waitcnt vmcnt(0)\n"
        ".Lr5_finw_chk_%=:\n"
        "buffer_load_dword v24, off, s[40:43], s65 sc1\n"
        "s_waitcnt vmcnt(0)\n"
        "v_readfirstlane_b32 s65, v24\n"
        "s_nop 0\n"
        "s_cmp_lg_u32 s65, 1\n"
        "s_cbranch_scc1 .Lr5_finw_go_%=\n"
        "s_endpgm\n"
        ".p2align 5\n"
        ".Lr5_finw_go_%=:\n"
        "s_branch .Lr5_finw_top_%=\n"
        ".p2align 5\n"
        ".Lr5_finw_ok_%=:\n"
        "s_cmp_eq_u32 s68, 63\n"
        "s_cselect_b32 s65, 0, 241172480\n"
        "v_mov_b32 v25, 1\n"
        "buffer_store_dword v25, v203, s[40:43], s65 offen sc1\n"
        "buffer_load_dwordx4 v[32:35], v3, s[40:43], s76 offen offset:0 sc1\n"
        "buffer_load_dwordx4 v[40:43], v3, s[40:43], s76 offen offset:512 sc1\n"
        "buffer_load_dwordx4 v[36:39], v3, s[40:43], s76 offen offset:256 sc1\n"
        "buffer_load_dwordx4 v[44:47], v3, s[40:43], s76 offen offset:768 sc1\n"
        "buffer_load_dwordx4 v[80:83], v0, s[40:43], s79 offen sc1\n"
        "buffer_load_dwordx4 v[48:51], v3, s[40:43], s77 offen offset:0 sc1\n"
        "buffer_load_dwordx4 v[56:59], v3, s[40:43], s77 offen offset:512 sc1\n"
        "buffer_load_dwordx4 v[52:55], v3, s[40:43], s77 offen offset:256 sc1\n"
        "buffer_load_dwordx4 v[60:63], v3, s[40:43], s77 offen offset:768 sc1\n"
        "buffer_load_dwordx4 v[84:87], v0, s[40:43], s80 offen sc1\n"
        "buffer_load_dwordx4 v[64:67], v4, s[40:43], s78 offen offset:0 sc1\n"
        "buffer_load_dwordx4 v[72:75], v4, s[40:43], s78 offen offset:512 sc1\n"
        "buffer_load_dwordx4 v[68:71], v4, s[40:43], s78 offen offset:256 sc1\n"
        "buffer_load_dwordx4 v[76:79], v4, s[40:43], s78 offen offset:768 sc1\n"
        "buffer_load_dwordx4 v[88:91], v0, s[40:43], s81 offen sc1\n"
        "s_waitcnt vmcnt(14)\n"
        "v_mfma_f32_16x16x32_bf16 v[92:95], v[8:11], v[32:35], 0\n"
        "s_waitcnt vmcnt(13)\n"
        "v_mfma_f32_16x16x32_bf16 v[92:95], v[12:15], v[40:43], v[92:95]\n"
        "s_waitcnt vmcnt(12)\n"
        "v_mfma_f32_16x16x32_bf16 v[96:99], v[8:11], v[36:39], 0\n"
        "s_waitcnt vmcnt(11)\n"
        "v_mfma_f32_16x16x32_bf16 v[96:99], v[12:15], v[44:47], v[96:99]\n"
        "s_waitcnt vmcnt(10)\n"
        "v_mfma_f32_16x16x32_bf16 v[92:95], v[16:19], v[80:83], v[92:95]\n"
        "v_mfma_f32_16x16x32_bf16 v[96:99], v[20:23], v[80:83], v[96:99]\n"
        "s_waitcnt vmcnt(9)\n"
        "v_mfma_f32_16x16x32_bf16 v[100:103], v[8:11], v[48:51], 0\n"
        "s_waitcnt vmcnt(8)\n"
        "v_mfma_f32_16x16x32_bf16 v[100:103], v[12:15], v[56:59], v[100:103]\n"
        "s_waitcnt vmcnt(7)\n"
        "v_mfma_f32_16x16x32_bf16 v[104:107], v[8:11], v[52:55], 0\n"
        "s_waitcnt vmcnt(6)\n"
        "v_mfma_f32_16x16x32_bf16 v[104:107], v[12:15], v[60:63], v[104:107]\n"
        "s_waitcnt vmcnt(5)\n"
        "v_mfma_f32_16x16x32_bf16 v[100:103], v[16:19], v[84:87], v[100:103]\n"
        "v_mfma_f32_16x16x32_bf16 v[104:107], v[20:23], v[84:87], v[104:107]\n"
        "s_waitcnt vmcnt(4)\n"
        "v_mfma_f32_16x16x32_bf16 v[108:111], v[8:11], v[64:67], 0\n"
        "s_waitcnt vmcnt(3)\n"
        "v_mfma_f32_16x16x32_bf16 v[108:111], v[12:15], v[72:75], v[108:111]\n"
        "s_waitcnt vmcnt(2)\n"
        "v_mfma_f32_16x16x32_bf16 v[112:115], v[8:11], v[68:71], 0\n"
        "s_waitcnt vmcnt(1)\n"
        "v_mfma_f32_16x16x32_bf16 v[112:115], v[12:15], v[76:79], v[112:115]\n"
        "s_waitcnt vmcnt(0)\n"
        "v_mfma_f32_16x16x32_bf16 v[108:111], v[16:19], v[88:91], v[108:111]\n"
        "v_mfma_f32_16x16x32_bf16 v[112:115], v[20:23], v[88:91], v[112:115]\n"
        "s_mul_i32 s65, s68, 36864\n"
        "v_cvt_pk_bf16_f32 v140, v92, v93\n"
        "v_cvt_pk_bf16_f32 v141, v94, v95\n"
        "v_cvt_pk_bf16_f32 v142, v96, v97\n"
        "v_cvt_pk_bf16_f32 v143, v98, v99\n"
        "s_nop 0\n"
        "v_permlane16_swap_b32 v140, v142\n"
        "v_permlane16_swap_b32 v141, v143\n"
        "buffer_store_dwordx4 v[140:143], v5, s[44:47], s65 offen offset:0 sc1\n"
        "v_cvt_pk_bf16_f32 v144, v100, v101\n"
        "v_cvt_pk_bf16_f32 v145, v102, v103\n"
        "v_cvt_pk_bf16_f32 v146, v104, v105\n"
        "v_cvt_pk_bf16_f32 v147, v106, v107\n"
        "s_nop 0\n"
        "v_permlane16_swap_b32 v144, v146\n"
        "v_permlane16_swap_b32 v145, v147\n"
        "buffer_store_dwordx4 v[144:147], v5, s[44:47], s65 offen offset:128 sc1\n"
        "s_nop 0\n"
        "v_cvt_pk_bf16_f32 v148, v108, v109\n"
        "v_cvt_pk_bf16_f32 v149, v110, v111\n"
        "v_cvt_pk_bf16_f32 v150, v112, v113\n"
        "v_cvt_pk_bf16_f32 v151, v114, v115\n"
        "s_nop 0\n"
        "v_permlane16_swap_b32 v148, v150\n"
        "v_permlane16_swap_b32 v149, v151\n"
        "buffer_store_dwordx4 v[148:151], v5, s[44:47], s65 offen offset:256 sc1\n"
        "s_add_u32 s68, s68, 1\n"
        "s_cmp_lt_u32 s68, 64\n"
        "s_cbranch_scc1 .Lr5_fin_%=\n"
        "s_barrier\n"
        "s_mov_b32 s65, 49280\n"
        "buffer_store_dwordx2 v[200:201], v202, s[40:43], s65 offen sc1\n"
        "s_endpgm\n"
        ".Lr5_end_%=:\n"
            RED_ARGS : "v0", "v1", "v2", "v3", "v4", "v5", "v6", "v7", "v8", "v9", "v10", "v11", "v12", "v13", "v14", "v15", "v16", "v17", "v18", "v19", "v20", "v21", "v22", "v23", "v24", "v25", "v26", "v27", "v28", "v29", "v30", "v31", "v32", "v33", "v34", "v35", "v36", "v37", "v38", "v39", "v40", "v41", "v42", "v43", "v44", "v45", "v46", "v47", "v48", "v49", "v50", "v51", "v52", "v53", "v54", "v55", "v56", "v57", "v58", "v59", "v60", "v61", "v62", "v63", "v64", "v65", "v66", "v67", "v68", "v69", "v70", "v71", "v72", "v73", "v74", "v75", "v76", "v77", "v78", "v79", "v255", "a255", "s26", "s27", "s28", "s29", "s30", "s31", "s32", "s33", "s34", "s35", "s36", "s37", "s38", "s39", "s40", "s41", "s42", "s43", "s44", "s45", "s46", "s47", "s48", "s49", "s50", "s51", "s52", "s53", "s54", "s55", "s56", "s57", "s58", "s59", "s60", "s61", "s62", "s63", "s64", "s65", "s66", "s67", "s68", "s69", "s70", "s71", "s72", "s73", "s74", "s75", "s76", "s77", "s78", "s79", "s80", "s81", "s82", "s83", "s84", "s85", "s86", "s87", "s88", "s89", "s90", "s91", "s92", "s93", "s94", "s95", "s96", "s97", "s98", "s99", "vcc", "scc", "memory");
    } else {
        asm volatile(
        "s_nop 4\n"
        "s_mov_b64 s[40:41], %0\n"
        "s_and_b32 s41, s41, 0xffff\n"
        "s_mov_b32 s42, 236978176\n"
        "s_mov_b32 s43, 0x20000\n"
        "s_mov_b64 s[44:45], %1\n"
        "s_and_b32 s45, s45, 0xffff\n"
        "s_mov_b32 s46, 2359296\n"
        "s_mov_b32 s47, 0x20000\n"
        "s_mov_b64 s[56:57], %3\n"
        "s_mov_b64 s[58:59], %4\n"
        "s_mov_b64 s[60:61], %5\n"
        "s_mov_b64 s[62:63], %6\n"
        "v_mov_b64 v[0:1], %7\n"
        "v_mov_b64 v[2:3], %8\n"
        "v_mov_b64 v[4:5], %9\n"
        "v_mov_b64 v[6:7], %10\n"
        "v_mov_b64 v[8:9], %11\n"
        "v_mov_b64 v[10:11], %12\n"
        "v_mov_b64 v[12:13], %13\n"
        "v_mov_b64 v[14:15], %14\n"
        "v_mov_b64 v[16:17], %15\n"
        "v_mov_b64 v[18:19], %16\n"
        "v_mov_b64 v[20:21], %17\n"
        "v_mov_b64 v[22:23], %18\n"
        "v_mov_b64 v[24:25], %19\n"
        "v_mov_b64 v[26:27], %20\n"
        "v_mov_b64 v[200:201], v[24:25]\n"
        "v_mov_b64 v[202:203], v[26:27]\n"
        "s_lshr_b32 s74, s56, 8\n"
        "s_and_b32 s56, s56, 1\n"
        "s_mov_b32 s68, 0\n"
        "s_mul_i32 s65, s68, 36864\n"
        "buffer_load_dwordx4 v[80:83], v5, s[44:47], s65 offen offset:0\n"
        "buffer_load_dwordx4 v[84:87], v5, s[44:47], s65 offen offset:128\n"
        "buffer_load_dwordx4 v[88:91], v5, s[44:47], s65 offen offset:256\n"
        "s_getpc_b64 s[76:77]\n"
        ".Lr3_pc_%=:\n"
        "s_add_u32 s76, s76, .Lr3_code_%=-.Lr3_pc_%=\n"
        "s_addc_u32 s77, s77, 0\n"
        "s_and_b32 s77, s77, 0xffff\n"
        "s_and_b32 s76, s76, -4\n"
        "s_mov_b32 s78, .Lr3_end_%=-.Lr3_code_%=\n"
        "s_mov_b32 s79, 0x20000\n"
        "s_mov_b32 s65, 0\n"
        "buffer_load_dwordx4 v[32:35], v0, s[76:79], s65 offen\n"
        "s_add_u32 s65, s65, 1024\n"
        "buffer_load_dwordx4 v[36:39], v0, s[76:79], s65 offen\n"
        "s_add_u32 s65, s65, 1024\n"
        "buffer_load_dwordx4 v[40:43], v0, s[76:79], s65 offen\n"
        "s_add_u32 s65, s65, 1024\n"
        "buffer_load_dwordx4 v[44:47], v0, s[76:79], s65 offen\n"
        "s_add_u32 s65, s65, 1024\n"
        "buffer_load_dwordx4 v[48:51], v0, s[76:79], s65 offen\n"
        "s_add_u32 s65, s65, 1024\n"
        "v_lshrrev_b32 v26, 4, v0\n"
        "s_mov_b32 s50, s56\n"
        "v_lshlrev_b32 v28, 7, v26\n"
        "s_sub_u32 s66, 0, s50\n"
        "s_mov_b32 s52, 32768\n"
        "s_mov_b32 s64, 0\n"
        ".Lr3_pg0_top_%=:\n"
        "buffer_load_dword v27, v28, s[40:43], s52 offen sc1\n"
        "s_waitcnt vmcnt(0)\n"
        "v_cmp_gt_i32 vcc, 0, v27\n"
        "s_cbranch_vccz .Lr3_pg0_ok_%=\n"
        "s_add_u32 s64, s64, 1\n"
        "s_and_b32 s65, s64, 255\n"
        "s_cbranch_scc1 .Lr3_pg0_go_%=\n"
        "s_mov_b32 s65, 49152\n"
        "s_cmp_lt_u32 s64, 1048576\n"
        "s_cbranch_scc1 .Lr3_pg0_chk_%=\n"
        "v_mov_b32 v27, 1\n"
        "buffer_store_dword v27, off, s[40:43], s65 sc1\n"
        "s_waitcnt vmcnt(0)\n"
        ".Lr3_pg0_chk_%=:\n"
        "buffer_load_dword v27, off, s[40:43], s65 sc1\n"
        "s_waitcnt vmcnt(0)\n"
        "v_readfirstlane_b32 s65, v27\n"
        "s_nop 0\n"
        "s_cmp_lg_u32 s65, 1\n"
        "s_cbranch_scc1 .Lr3_pg0_go_%=\n"
        "s_endpgm\n"
        ".Lr3_pg0_go_%=:\n"
        "s_sleep 2\n"
        "s_branch .Lr3_pg0_top_%=\n"
        ".Lr3_pg0_ok_%=:\n"
        "v_cmp_eq_u32 s[52:53], 1, v27\n"
        "s_nop 1\n"
        "s_mov_b32 s54, 40960\n"
        "s_mov_b32 s64, 0\n"
        ".Lr3_pg1_top_%=:\n"
        "buffer_load_dword v27, v28, s[40:43], s54 offen sc1\n"
        "s_waitcnt vmcnt(0)\n"
        "v_cmp_gt_i32 vcc, 0, v27\n"
        "s_cbranch_vccz .Lr3_pg1_ok_%=\n"
        "s_add_u32 s64, s64, 1\n"
        "s_and_b32 s65, s64, 255\n"
        "s_cbranch_scc1 .Lr3_pg1_go_%=\n"
        "s_mov_b32 s65, 49152\n"
        "s_cmp_lt_u32 s64, 1048576\n"
        "s_cbranch_scc1 .Lr3_pg1_chk_%=\n"
        "v_mov_b32 v27, 1\n"
        "buffer_store_dword v27, off, s[40:43], s65 sc1\n"
        "s_waitcnt vmcnt(0)\n"
        ".Lr3_pg1_chk_%=:\n"
        "buffer_load_dword v27, off, s[40:43], s65 sc1\n"
        "s_waitcnt vmcnt(0)\n"
        "v_readfirstlane_b32 s65, v27\n"
        "s_nop 0\n"
        "s_cmp_lg_u32 s65, 1\n"
        "s_cbranch_scc1 .Lr3_pg1_go_%=\n"
        "s_endpgm\n"
        ".Lr3_pg1_go_%=:\n"
        "s_sleep 2\n"
        "s_branch .Lr3_pg1_top_%=\n"
        ".Lr3_pg1_ok_%=:\n"
        "v_cmp_eq_u32 s[54:55], 1, v27\n"
        "s_nop 1\n"
        "s_mov_b32 vcc_lo, 0x55555555\n"
        "s_mov_b32 vcc_hi, 0x55555555\n"
        "s_xor_b64 vcc, vcc, s[52:53]\n"
        "s_bcnt1_i32_b64 s65, vcc\n"
        "s_sub_u32 s64, s65, 23\n"
        "s_cmp_lt_u32 s64, 19\n"
        "s_cbranch_scc1 .Lr3_rawmap_%=\n"
        "s_cmp_lt_u32 s65, 32\n"
        "s_cselect_b32 s65, 0, -1\n"
        "s_xor_b32 s52, s65, 0x55555555\n"
        "s_xor_b32 s53, s65, 0x55555555\n"
        "s_xor_b32 s54, s65, 0x55555555\n"
        "s_xor_b32 s55, s65, 0xaaaaaaaa\n"
        ".Lr3_rawmap_%=:\n"
        "s_xor_b32 s52, s52, s66\n"
        "s_xor_b32 s53, s53, s66\n"
        "s_xor_b32 s54, s54, s66\n"
        "s_xor_b32 s55, s55, s66\n"
        "s_and_b32 s69, s55, 1\n"
        "s_lshl_b32 s69, s69, 13\n"
        "s_xor_b32 s70, s69, 0\n"
        "s_cmp_eq_u32 s50, 0\n"
        "s_cbranch_scc0 .Lr3_l1_%=\n"
        ".p2align 6\n"
        ".Lr3_code_%=:\n"
        ".Lr3_l0_%=:\n"
        "s_bitcmp1_b64 s[52:53], s68\n"
        "s_cselect_b32 s65, 8192, 0\n"
        "s_lshl_b32 s71, s68, 21\n"
        "s_add_u32 s71, s71, s65\n"
        "s_add_u32 s71, s71, 2097152\n"
        "s_lshr_b32 s65, s68, 1\n"
        "s_bitcmp1_b32 s54, s65\n"
        "s_cselect_b32 s65, 8192, 0\n"
        "s_lshl_b32 s72, s68, 20\n"
        "s_add_u32 s72, s72, s65\n"
        "s_add_u32 s72, s72, 136904704\n"
        "s_add_u32 s76, s72, s61\n"
        "s_add_u32 s79, s71, s58\n"
        "s_add_u32 s79, s79, 1916928\n"
        "s_add_u32 s77, s72, s62\n"
        "s_add_u32 s80, s71, s59\n"
        "s_add_u32 s80, s80, 1916928\n"
        "s_add_u32 s78, s72, s63\n"
        "s_add_u32 s81, s71, s60\n"
        "s_add_u32 s81, s81, 1916928\n"
        "s_lshl_b32 s73, s68, 1\n"
        "s_add_u32 s73, s73, s74\n"
        "s_mov_b32 s64, 0\n"
        ".Lr3_l0w_top_%=:\n"
        "buffer_load_dword v24, v1, s[40:43], s69 offen sc1\n"
        "s_waitcnt vmcnt(0)\n"
        "v_cmp_gt_i32_e64 vcc, s73, v24\n"
        "s_nop 0\n"
        "s_and_b64 vcc, vcc, 0xffff\n"
        "s_nop 0\n"
        "s_nop 0\n"
        "s_cbranch_vccz .Lr3_l0w_ok_%=\n"
        "s_add_u32 s64, s64, 1\n"
        "s_and_b32 s65, s64, 255\n"
        "s_cbranch_scc1 .Lr3_l0w_go_%=\n"
        "s_mov_b32 s65, 49152\n"
        "s_cmp_lt_u32 s64, 1048576\n"
        "s_cbranch_scc1 .Lr3_l0w_chk_%=\n"
        "v_mov_b32 v24, 1\n"
        "buffer_store_dword v24, off, s[40:43], s65 sc1\n"
        "s_waitcnt vmcnt(0)\n"
        ".Lr3_l0w_chk_%=:\n"
        "buffer_load_dword v24, off, s[40:43], s65 sc1\n"
        "s_waitcnt vmcnt(0)\n"
        "v_readfirstlane_b32 s65, v24\n"
        "s_nop 0\n"
        "s_cmp_lg_u32 s65, 1\n"
        "s_cbranch_scc1 .Lr3_l0w_go_%=\n"
        "s_endpgm\n"
        ".p2align 5\n"
        ".Lr3_l0w_go_%=:\n"
        "s_branch .Lr3_l0w_top_%=\n"
        ".p2align 5\n"
        ".Lr3_l0w_ok_%=:\n"
        "buffer_load_dwordx4 v[32:35], v3, s[40:43], s76 offen offset:0 sc1\n"
        "buffer_load_dwordx4 v[40:43], v3, s[40:43], s76 offen offset:512 sc1\n"
        "buffer_load_dwordx4 v[36:39], v3, s[40:43], s76 offen offset:256 sc1\n"
        "buffer_load_dwordx4 v[44:47], v3, s[40:43], s76 offen offset:768 sc1\n"
        "buffer_load_dwordx4 v[48:51], v3, s[40:43], s77 offen offset:0 sc1\n"
        "buffer_load_dwordx4 v[56:59], v3, s[40:43], s77 offen offset:512 sc1\n"
        "buffer_load_dwordx4 v[52:55], v3, s[40:43], s77 offen offset:256 sc1\n"
        "buffer_load_dwordx4 v[60:63], v3, s[40:43], s77 offen offset:768 sc1\n"
        "buffer_load_dwordx4 v[64:67], v4, s[40:43], s78 offen offset:0 sc1\n"
        "buffer_load_dwordx4 v[72:75], v4, s[40:43], s78 offen offset:512 sc1\n"
        "buffer_load_dwordx4 v[68:71], v4, s[40:43], s78 offen offset:256 sc1\n"
        "buffer_load_dwordx4 v[76:79], v4, s[40:43], s78 offen offset:768 sc1\n"
        "s_add_u32 s72, s72, 294912\n"
        "s_add_u32 s71, s71, 1998848\n"
        "s_add_u32 s82, s72, s58\n"
        "s_add_u32 s85, s71, s58\n"
        "s_add_u32 s83, s72, s59\n"
        "s_add_u32 s86, s71, s59\n"
        "s_add_u32 s84, s72, s60\n"
        "s_add_u32 s87, s71, s60\n"
        "s_waitcnt vmcnt(11)\n"
        "v_mfma_f32_16x16x32_bf16 v[92:95], v[8:11], v[32:35], 0\n"
        "s_waitcnt vmcnt(10)\n"
        "v_mfma_f32_16x16x32_bf16 v[92:95], v[12:15], v[40:43], v[92:95]\n"
        "s_waitcnt vmcnt(9)\n"
        "v_mfma_f32_16x16x32_bf16 v[96:99], v[8:11], v[36:39], 0\n"
        "s_waitcnt vmcnt(8)\n"
        "v_mfma_f32_16x16x32_bf16 v[96:99], v[12:15], v[44:47], v[96:99]\n"
        "v_mfma_f32_16x16x32_bf16 v[92:95], v[16:19], v[80:83], v[92:95]\n"
        "v_mfma_f32_16x16x32_bf16 v[96:99], v[20:23], v[80:83], v[96:99]\n"
        "s_waitcnt vmcnt(7)\n"
        "v_mfma_f32_16x16x32_bf16 v[100:103], v[8:11], v[48:51], 0\n"
        "s_waitcnt vmcnt(6)\n"
        "v_mfma_f32_16x16x32_bf16 v[100:103], v[12:15], v[56:59], v[100:103]\n"
        "s_waitcnt vmcnt(5)\n"
        "v_mfma_f32_16x16x32_bf16 v[104:107], v[8:11], v[52:55], 0\n"
        "s_waitcnt vmcnt(4)\n"
        "v_mfma_f32_16x16x32_bf16 v[104:107], v[12:15], v[60:63], v[104:107]\n"
        "v_mfma_f32_16x16x32_bf16 v[100:103], v[16:19], v[84:87], v[100:103]\n"
        "v_mfma_f32_16x16x32_bf16 v[104:107], v[20:23], v[84:87], v[104:107]\n"
        "s_waitcnt vmcnt(3)\n"
        "v_mfma_f32_16x16x32_bf16 v[108:111], v[8:11], v[64:67], 0\n"
        "s_waitcnt vmcnt(2)\n"
        "v_mfma_f32_16x16x32_bf16 v[108:111], v[12:15], v[72:75], v[108:111]\n"
        "s_waitcnt vmcnt(1)\n"
        "v_mfma_f32_16x16x32_bf16 v[112:115], v[8:11], v[68:71], 0\n"
        "s_waitcnt vmcnt(0)\n"
        "v_mfma_f32_16x16x32_bf16 v[112:115], v[12:15], v[76:79], v[112:115]\n"
        "v_mfma_f32_16x16x32_bf16 v[108:111], v[16:19], v[88:91], v[108:111]\n"
        "v_mfma_f32_16x16x32_bf16 v[112:115], v[20:23], v[88:91], v[112:115]\n"
        "s_add_u32 s68, s68, 1\n"
        "s_mul_i32 s65, s68, 36864\n"
        "buffer_load_dwordx4 v[80:83], v5, s[44:47], s65 offen offset:0\n"
        "buffer_load_dwordx4 v[84:87], v5, s[44:47], s65 offen offset:128\n"
        "buffer_load_dwordx4 v[88:91], v5, s[44:47], s65 offen offset:256\n"
        "s_sub_u32 s68, s68, 1\n"
        "v_pk_add_f32 v[164:165], v[92:93], v[94:95]\n"
        "v_pk_add_f32 v[166:167], v[96:97], v[98:99]\n"
        "v_pk_add_f32 v[168:169], v[100:101], v[102:103]\n"
        "v_pk_add_f32 v[170:171], v[104:105], v[106:107]\n"
        "v_pk_add_f32 v[164:165], v[164:165], v[166:167]\n"
        "v_pk_add_f32 v[168:169], v[168:169], v[170:171]\n"
        "v_pk_add_f32 v[164:165], v[164:165], v[168:169]\n"
        "v_pk_add_f32 v[166:167], v[108:109], v[110:111]\n"
        "v_pk_add_f32 v[168:169], v[112:113], v[114:115]\n"
        "v_pk_add_f32 v[164:165], v[164:165], v[166:167]\n"
        "v_pk_add_f32 v[164:165], v[164:165], v[168:169]\n"
        "v_add_f32 v164, v164, v165\n"
        "v_cvt_pk_bf16_f32 v140, v92, v93\n"
        "v_cvt_pk_bf16_f32 v141, v94, v95\n"
        "v_add_f32_dpp v166, v164, v164 row_ror:8 row_mask:0xf bank_mask:0xf\n"
        "v_add_f32_dpp v167, v164, v164 row_ror:8 row_mask:0xf bank_mask:0xf\n"
        "v_cvt_pk_bf16_f32 v142, v96, v97\n"
        "v_cvt_pk_bf16_f32 v143, v98, v99\n"
        "v_permlane16_swap_b32 v166, v167\n"
        "v_add_f32 v164, v166, v167\n"
        "v_add_f32 v166, v166, v167\n"
        "v_cvt_pk_bf16_f32 v144, v100, v101\n"
        "v_cvt_pk_bf16_f32 v145, v102, v103\n"
        "v_permlane32_swap_b32 v164, v166\n"
        "v_add_f32 v164, v164, v166\n"
        "v_mul_f32 v164, 0x3baaaaab, v164\n"
        "v_pk_add_f32 v[116:117], v[92:93], v[164:165] op_sel_hi:[1,0] neg_lo:[0,1] neg_hi:[0,1]\n"
        "v_pk_add_f32 v[118:119], v[94:95], v[164:165] op_sel_hi:[1,0] neg_lo:[0,1] neg_hi:[0,1]\n"
        "v_pk_add_f32 v[120:121], v[96:97], v[164:165] op_sel_hi:[1,0] neg_lo:[0,1] neg_hi:[0,1]\n"
        "v_pk_add_f32 v[122:123], v[98:99], v[164:165] op_sel_hi:[1,0] neg_lo:[0,1] neg_hi:[0,1]\n"
        "v_pk_add_f32 v[124:125], v[100:101], v[164:165] op_sel_hi:[1,0] neg_lo:[0,1] neg_hi:[0,1]\n"
        "v_pk_add_f32 v[126:127], v[102:103], v[164:165] op_sel_hi:[1,0] neg_lo:[0,1] neg_hi:[0,1]\n"
        "v_pk_add_f32 v[128:129], v[104:105], v[164:165] op_sel_hi:[1,0] neg_lo:[0,1] neg_hi:[0,1]\n"
        "v_pk_add_f32 v[130:131], v[106:107], v[164:165] op_sel_hi:[1,0] neg_lo:[0,1] neg_hi:[0,1]\n"
        "v_pk_add_f32 v[132:133], v[108:109], v[164:165] op_sel_hi:[1,0] neg_lo:[0,1] neg_hi:[0,1]\n"
        "v_pk_add_f32 v[134:135], v[110:111], v[164:165] op_sel_hi:[1,0] neg_lo:[0,1] neg_hi:[0,1]\n"
        "v_pk_add_f32 v[136:137], v[112:113], v[164:165] op_sel_hi:[1,0] neg_lo:[0,1] neg_hi:[0,1]\n"
        "v_pk_add_f32 v[138:139], v[114:115], v[164:165] op_sel_hi:[1,0] neg_lo:[0,1] neg_hi:[0,1]\n"
        "v_pk_mul_f32 v[166:167], v[116:117], v[116:117]\n"
        "v_pk_mul_f32 v[168:169], v[118:119], v[118:119]\n"
        "v_pk_fma_f32 v[166:167], v[120:121], v[120:121], v[166:167]\n"
        "v_pk_fma_f32 v[168:169], v[122:123], v[122:123], v[168:169]\n"
        "v_pk_fma_f32 v[166:167], v[124:125], v[124:125], v[166:167]\n"
        "v_pk_fma_f32 v[168:169], v[126:127], v[126:127], v[168:169]\n"
        "v_pk_fma_f32 v[166:167], v[128:129], v[128:129], v[166:167]\n"
        "v_pk_fma_f32 v[168:169], v[130:131], v[130:131], v[168:169]\n"
        "v_pk_fma_f32 v[166:167], v[132:133], v[132:133], v[166:167]\n"
        "v_pk_fma_f32 v[168:169], v[134:135], v[134:135], v[168:169]\n"
        "v_pk_fma_f32 v[166:167], v[136:137], v[136:137], v[166:167]\n"
        "v_pk_fma_f32 v[168:169], v[138:139], v[138:139], v[168:169]\n"
        "v_pk_add_f32 v[166:167], v[166:167], v[168:169]\n"
        "v_add_f32 v165, v166, v167\n"
        "v_cvt_pk_bf16_f32 v146, v104, v105\n"
        "v_cvt_pk_bf16_f32 v147, v106, v107\n"
        "v_add_f32_dpp v166, v165, v165 row_ror:8 row_mask:0xf bank_mask:0xf\n"
        "v_add_f32_dpp v167, v165, v165 row_ror:8 row_mask:0xf bank_mask:0xf\n"
        "v_cvt_pk_bf16_f32 v148, v108, v109\n"
        "v_cvt_pk_bf16_f32 v149, v110, v111\n"
        "v_permlane16_swap_b32 v166, v167\n"
        "v_add_f32 v165, v166, v167\n"
        "v_add_f32 v166, v166, v167\n"
        "v_cvt_pk_bf16_f32 v150, v112, v113\n"
        "v_cvt_pk_bf16_f32 v151, v114, v115\n"
        "v_permlane32_swap_b32 v165, v166\n"
        "v_add_f32 v165, v165, v166\n"
        "ds_write_b64 v6, v[164:165]\n"
        "v_permlane16_swap_b32 v140, v142\n"
        "v_permlane16_swap_b32 v141, v143\n"
        "v_permlane16_swap_b32 v144, v146\n"
        "v_permlane16_swap_b32 v145, v147\n"
        "s_waitcnt lgkmcnt(0)\n"
        "s_barrier\n"
        "ds_read_b64 v[166:167], v7 offset:0\n"
        "ds_read_b64 v[168:169], v7 offset:64\n"
        "ds_read_b64 v[170:171], v7 offset:128\n"
        "v_permlane16_swap_b32 v148, v150\n"
        "v_permlane16_swap_b32 v149, v151\n"
        "buffer_store_dwordx4 v[140:143], v0, s[40:43], s85 offen sc1\n"
        "buffer_store_dwordx4 v[144:147], v0, s[40:43], s86 offen sc1\n"
        "buffer_store_dwordx4 v[148:151], v0, s[40:43], s87 offen sc1\n"
        "s_waitcnt lgkmcnt(0)\n"
        "v_add_f32 v172, v166, v168\n"
        "v_add_f32 v173, v167, v169\n"
        "v_add_f32 v172, v172, v170\n"
        "v_add_f32 v173, v173, v171\n"
        "v_mul_f32 v172, 0x3eaaaaab, v172\n"
        "v_sub_f32 v166, v166, v172\n"
        "v_sub_f32 v168, v168, v172\n"
        "v_sub_f32 v170, v170, v172\n"
        "v_mul_f32 v166, v166, v166\n"
        "v_fmac_f32 v166, v168, v168\n"
        "v_fmac_f32 v166, v170, v170\n"
        "v_fmamk_f32 v173, v166, 0x43400000, v173\n"
        "v_mul_f32 v173, 0x3ae38e39, v173\n"
        "v_cvt_pk_bf16_f32 v172, v172, v173\n"
        "v_and_b32 v173, 0xffff0000, v172\n"
        "v_lshlrev_b32 v172, 16, v172\n"
        "v_add_f32 v173, 0x3727c5ac, v173\n"
        "v_cvt_pk_bf16_f32 v173, v173, v173\n"
        "v_lshlrev_b32 v173, 16, v173\n"
        "v_sqrt_f32 v173, v173\n"
        "v_sub_f32 v172, v164, v172\n"
        "v_cvt_pk_bf16_f32 v173, v173, v173\n"
        "v_lshlrev_b32 v173, 16, v173\n"
        "v_rcp_f32 v173, v173\n"
        "v_mov_b32 v25, s68\n"
        "v_mul_f32 v172, v172, v173\n"
        "v_pk_fma_f32 v[116:117], v[116:117], v[172:173], v[172:173] op_sel:[0,1,0] op_sel_hi:[1,1,0]\n"
        "v_pk_fma_f32 v[118:119], v[118:119], v[172:173], v[172:173] op_sel:[0,1,0] op_sel_hi:[1,1,0]\n"
        "v_pk_fma_f32 v[120:121], v[120:121], v[172:173], v[172:173] op_sel:[0,1,0] op_sel_hi:[1,1,0]\n"
        "v_pk_fma_f32 v[122:123], v[122:123], v[172:173], v[172:173] op_sel:[0,1,0] op_sel_hi:[1,1,0]\n"
        "v_cvt_pk_bf16_f32 v152, v116, v117\n"
        "v_cvt_pk_bf16_f32 v153, v118, v119\n"
        "v_cvt_pk_bf16_f32 v154, v120, v121\n"
        "v_cvt_pk_bf16_f32 v155, v122, v123\n"
        "s_nop 0\n"
        "v_permlane16_swap_b32 v152, v154\n"
        "v_permlane16_swap_b32 v153, v155\n"
        "buffer_store_dwordx4 v[152:155], v0, s[40:43], s82 offen sc1\n"
        "v_pk_fma_f32 v[124:125], v[124:125], v[172:173], v[172:173] op_sel:[0,1,0] op_sel_hi:[1,1,0]\n"
        "v_pk_fma_f32 v[126:127], v[126:127], v[172:173], v[172:173] op_sel:[0,1,0] op_sel_hi:[1,1,0]\n"
        "v_pk_fma_f32 v[128:129], v[128:129], v[172:173], v[172:173] op_sel:[0,1,0] op_sel_hi:[1,1,0]\n"
        "v_pk_fma_f32 v[130:131], v[130:131], v[172:173], v[172:173] op_sel:[0,1,0] op_sel_hi:[1,1,0]\n"
        "v_cvt_pk_bf16_f32 v156, v124, v125\n"
        "v_cvt_pk_bf16_f32 v157, v126, v127\n"
        "v_cvt_pk_bf16_f32 v158, v128, v129\n"
        "v_cvt_pk_bf16_f32 v159, v130, v131\n"
        "s_nop 0\n"
        "v_permlane16_swap_b32 v156, v158\n"
        "v_permlane16_swap_b32 v157, v159\n"
        "buffer_store_dwordx4 v[156:159], v0, s[40:43], s83 offen sc1\n"
        "v_pk_fma_f32 v[132:133], v[132:133], v[172:173], v[172:173] op_sel:[0,1,0] op_sel_hi:[1,1,0]\n"
        "v_pk_fma_f32 v[134:135], v[134:135], v[172:173], v[172:173] op_sel:[0,1,0] op_sel_hi:[1,1,0]\n"
        "v_pk_fma_f32 v[136:137], v[136:137], v[172:173], v[172:173] op_sel:[0,1,0] op_sel_hi:[1,1,0]\n"
        "v_pk_fma_f32 v[138:139], v[138:139], v[172:173], v[172:173] op_sel:[0,1,0] op_sel_hi:[1,1,0]\n"
        "v_cvt_pk_bf16_f32 v160, v132, v133\n"
        "v_cvt_pk_bf16_f32 v161, v134, v135\n"
        "v_cvt_pk_bf16_f32 v162, v136, v137\n"
        "v_cvt_pk_bf16_f32 v163, v138, v139\n"
        "s_nop 0\n"
        "v_permlane16_swap_b32 v160, v162\n"
        "v_permlane16_swap_b32 v161, v163\n"
        "buffer_store_dwordx4 v[160:163], v0, s[40:43], s84 offen sc1\n"
        "s_waitcnt vmcnt(0)\n"
        "s_barrier\n"
        "buffer_store_dword v25, v2, s[40:43], s70 offen sc1\n"
        "s_add_u32 s68, s68, 1\n"
        "s_cmp_lt_u32 s68, 64\n"
        "s_cbranch_scc1 .Lr3_l0_%=\n"
        "s_endpgm\n"
        ".p2align 5\n"
        ".Lr3_l1_%=:\n"
        "s_bitcmp1_b64 s[52:53], s68\n"
        "s_cselect_b32 s65, 8192, 0\n"
        "s_lshl_b32 s71, s68, 21\n"
        "s_add_u32 s71, s71, s65\n"
        "s_add_u32 s71, s71, 2097152\n"
        "s_lshr_b32 s65, s68, 1\n"
        "s_bitcmp1_b32 s54, s65\n"
        "s_cselect_b32 s65, 8192, 0\n"
        "s_lshl_b32 s72, s68, 20\n"
        "s_add_u32 s72, s72, s65\n"
        "s_add_u32 s72, s72, 136904704\n"
        "s_add_u32 s76, s72, s61\n"
        "s_add_u32 s79, s71, s58\n"
        "s_add_u32 s79, s79, 1916928\n"
        "s_add_u32 s77, s72, s62\n"
        "s_add_u32 s80, s71, s59\n"
        "s_add_u32 s80, s80, 1916928\n"
        "s_add_u32 s78, s72, s63\n"
        "s_add_u32 s81, s71, s60\n"
        "s_add_u32 s81, s81, 1916928\n"
        "s_lshl_b32 s73, s68, 1\n"
        "s_add_u32 s73, s73, s74\n"
        "s_mov_b32 s64, 0\n"
        ".Lr3_l1w_top_%=:\n"
        "buffer_load_dword v24, v1, s[40:43], s69 offen sc1\n"
        "s_waitcnt vmcnt(0)\n"
        "v_cmp_gt_i32_e64 vcc, s73, v24\n"
        "s_nop 0\n"
        "s_and_b64 vcc, vcc, 0xffff\n"
        "s_nop 0\n"
        "s_nop 0\n"
        "s_cbranch_vccz .Lr3_l1w_ok_%=\n"
        "s_add_u32 s64, s64, 1\n"
        "s_and_b32 s65, s64, 255\n"
        "s_cbranch_scc1 .Lr3_l1w_go_%=\n"
        "s_mov_b32 s65, 49152\n"
        "s_cmp_lt_u32 s64, 1048576\n"
        "s_cbranch_scc1 .Lr3_l1w_chk_%=\n"
        "v_mov_b32 v24, 1\n"
        "buffer_store_dword v24, off, s[40:43], s65 sc1\n"
        "s_waitcnt vmcnt(0)\n"
        ".Lr3_l1w_chk_%=:\n"
        "buffer_load_dword v24, off, s[40:43], s65 sc1\n"
        "s_waitcnt vmcnt(0)\n"
        "v_readfirstlane_b32 s65, v24\n"
        "s_nop 0\n"
        "s_cmp_lg_u32 s65, 1\n"
        "s_cbranch_scc1 .Lr3_l1w_go_%=\n"
        "s_endpgm\n"
        ".p2align 5\n"
        ".Lr3_l1w_go_%=:\n"
        "s_branch .Lr3_l1w_top_%=\n"
        ".p2align 5\n"
        ".Lr3_l1w_ok_%=:\n"
        "buffer_load_dwordx4 v[32:35], v3, s[40:43], s76 offen offset:0 sc1\n"
        "buffer_load_dwordx4 v[40:43], v3, s[40:43], s76 offen offset:512 sc1\n"
        "buffer_load_dwordx4 v[36:39], v3, s[40:43], s76 offen offset:256 sc1\n"
        "buffer_load_dwordx4 v[44:47], v3, s[40:43], s76 offen offset:768 sc1\n"
        "buffer_load_dwordx4 v[80:83], v0, s[40:43], s79 offen sc1\n"
        "buffer_load_dwordx4 v[48:51], v3, s[40:43], s77 offen offset:0 sc1\n"
        "buffer_load_dwordx4 v[56:59], v3, s[40:43], s77 offen offset:512 sc1\n"
        "buffer_load_dwordx4 v[52:55], v3, s[40:43], s77 offen offset:256 sc1\n"
        "buffer_load_dwordx4 v[60:63], v3, s[40:43], s77 offen offset:768 sc1\n"
        "buffer_load_dwordx4 v[84:87], v0, s[40:43], s80 offen sc1\n"
        "buffer_load_dwordx4 v[64:67], v4, s[40:43], s78 offen offset:0 sc1\n"
        "buffer_load_dwordx4 v[72:75], v4, s[40:43], s78 offen offset:512 sc1\n"
        "buffer_load_dwordx4 v[68:71], v4, s[40:43], s78 offen offset:256 sc1\n"
        "buffer_load_dwordx4 v[76:79], v4, s[40:43], s78 offen offset:768 sc1\n"
        "buffer_load_dwordx4 v[88:91], v0, s[40:43], s81 offen sc1\n"
        "s_add_u32 s72, s72, 294912\n"
        "s_add_u32 s71, s71, 1998848\n"
        "s_add_u32 s82, s72, s58\n"
        "s_add_u32 s85, s71, s58\n"
        "s_add_u32 s83, s72, s59\n"
        "s_add_u32 s86, s71, s59\n"
        "s_add_u32 s84, s72, s60\n"
        "s_add_u32 s87, s71, s60\n"
        "s_waitcnt vmcnt(14)\n"
        "v_mfma_f32_16x16x32_bf16 v[92:95], v[8:11], v[32:35], 0\n"
        "s_waitcnt vmcnt(13)\n"
        "v_mfma_f32_16x16x32_bf16 v[92:95], v[12:15], v[40:43], v[92:95]\n"
        "s_waitcnt vmcnt(12)\n"
        "v_mfma_f32_16x16x32_bf16 v[96:99], v[8:11], v[36:39], 0\n"
        "s_waitcnt vmcnt(11)\n"
        "v_mfma_f32_16x16x32_bf16 v[96:99], v[12:15], v[44:47], v[96:99]\n"
        "s_waitcnt vmcnt(10)\n"
        "v_mfma_f32_16x16x32_bf16 v[92:95], v[16:19], v[80:83], v[92:95]\n"
        "v_mfma_f32_16x16x32_bf16 v[96:99], v[20:23], v[80:83], v[96:99]\n"
        "s_waitcnt vmcnt(9)\n"
        "v_mfma_f32_16x16x32_bf16 v[100:103], v[8:11], v[48:51], 0\n"
        "s_waitcnt vmcnt(8)\n"
        "v_mfma_f32_16x16x32_bf16 v[100:103], v[12:15], v[56:59], v[100:103]\n"
        "s_waitcnt vmcnt(7)\n"
        "v_mfma_f32_16x16x32_bf16 v[104:107], v[8:11], v[52:55], 0\n"
        "s_waitcnt vmcnt(6)\n"
        "v_mfma_f32_16x16x32_bf16 v[104:107], v[12:15], v[60:63], v[104:107]\n"
        "s_waitcnt vmcnt(5)\n"
        "v_mfma_f32_16x16x32_bf16 v[100:103], v[16:19], v[84:87], v[100:103]\n"
        "v_mfma_f32_16x16x32_bf16 v[104:107], v[20:23], v[84:87], v[104:107]\n"
        "s_waitcnt vmcnt(4)\n"
        "v_mfma_f32_16x16x32_bf16 v[108:111], v[8:11], v[64:67], 0\n"
        "s_waitcnt vmcnt(3)\n"
        "v_mfma_f32_16x16x32_bf16 v[108:111], v[12:15], v[72:75], v[108:111]\n"
        "s_waitcnt vmcnt(2)\n"
        "v_mfma_f32_16x16x32_bf16 v[112:115], v[8:11], v[68:71], 0\n"
        "s_waitcnt vmcnt(1)\n"
        "v_mfma_f32_16x16x32_bf16 v[112:115], v[12:15], v[76:79], v[112:115]\n"
        "s_waitcnt vmcnt(0)\n"
        "v_mfma_f32_16x16x32_bf16 v[108:111], v[16:19], v[88:91], v[108:111]\n"
        "v_mfma_f32_16x16x32_bf16 v[112:115], v[20:23], v[88:91], v[112:115]\n"
        "s_add_u32 s68, s68, 1\n"
        "s_sub_u32 s68, s68, 1\n"
        "v_pk_add_f32 v[164:165], v[92:93], v[94:95]\n"
        "v_pk_add_f32 v[166:167], v[96:97], v[98:99]\n"
        "v_pk_add_f32 v[168:169], v[100:101], v[102:103]\n"
        "v_pk_add_f32 v[170:171], v[104:105], v[106:107]\n"
        "v_pk_add_f32 v[164:165], v[164:165], v[166:167]\n"
        "v_pk_add_f32 v[168:169], v[168:169], v[170:171]\n"
        "v_pk_add_f32 v[164:165], v[164:165], v[168:169]\n"
        "s_nop 0\n"
        "v_pk_add_f32 v[166:167], v[108:109], v[110:111]\n"
        "v_pk_add_f32 v[168:169], v[112:113], v[114:115]\n"
        "v_pk_add_f32 v[164:165], v[164:165], v[166:167]\n"
        "v_pk_add_f32 v[164:165], v[164:165], v[168:169]\n"
        "v_add_f32 v164, v164, v165\n"
        "v_cvt_pk_bf16_f32 v140, v92, v93\n"
        "v_cvt_pk_bf16_f32 v141, v94, v95\n"
        "v_add_f32_dpp v166, v164, v164 row_ror:8 row_mask:0xf bank_mask:0xf\n"
        "v_add_f32_dpp v167, v164, v164 row_ror:8 row_mask:0xf bank_mask:0xf\n"
        "v_cvt_pk_bf16_f32 v142, v96, v97\n"
        "v_cvt_pk_bf16_f32 v143, v98, v99\n"
        "v_permlane16_swap_b32 v166, v167\n"
        "v_add_f32 v164, v166, v167\n"
        "v_add_f32 v166, v166, v167\n"
        "v_cvt_pk_bf16_f32 v144, v100, v101\n"
        "v_cvt_pk_bf16_f32 v145, v102, v103\n"
        "v_permlane32_swap_b32 v164, v166\n"
        "v_add_f32 v164, v164, v166\n"
        "v_mul_f32 v164, 0x3baaaaab, v164\n"
        "v_pk_add_f32 v[116:117], v[92:93], v[164:165] op_sel_hi:[1,0] neg_lo:[0,1] neg_hi:[0,1]\n"
        "v_pk_add_f32 v[118:119], v[94:95], v[164:165] op_sel_hi:[1,0] neg_lo:[0,1] neg_hi:[0,1]\n"
        "v_pk_add_f32 v[120:121], v[96:97], v[164:165] op_sel_hi:[1,0] neg_lo:[0,1] neg_hi:[0,1]\n"
        "v_pk_add_f32 v[122:123], v[98:99], v[164:165] op_sel_hi:[1,0] neg_lo:[0,1] neg_hi:[0,1]\n"
        "v_pk_add_f32 v[124:125], v[100:101], v[164:165] op_sel_hi:[1,0] neg_lo:[0,1] neg_hi:[0,1]\n"
        "v_pk_add_f32 v[126:127], v[102:103], v[164:165] op_sel_hi:[1,0] neg_lo:[0,1] neg_hi:[0,1]\n"
        "v_pk_add_f32 v[128:129], v[104:105], v[164:165] op_sel_hi:[1,0] neg_lo:[0,1] neg_hi:[0,1]\n"
        "v_pk_add_f32 v[130:131], v[106:107], v[164:165] op_sel_hi:[1,0] neg_lo:[0,1] neg_hi:[0,1]\n"
        "v_pk_add_f32 v[132:133], v[108:109], v[164:165] op_sel_hi:[1,0] neg_lo:[0,1] neg_hi:[0,1]\n"
        "v_pk_add_f32 v[134:135], v[110:111], v[164:165] op_sel_hi:[1,0] neg_lo:[0,1] neg_hi:[0,1]\n"
        "v_pk_add_f32 v[136:137], v[112:113], v[164:165] op_sel_hi:[1,0] neg_lo:[0,1] neg_hi:[0,1]\n"
        "v_pk_add_f32 v[138:139], v[114:115], v[164:165] op_sel_hi:[1,0] neg_lo:[0,1] neg_hi:[0,1]\n"
        "v_pk_mul_f32 v[166:167], v[116:117], v[116:117]\n"
        "v_pk_mul_f32 v[168:169], v[118:119], v[118:119]\n"
        "v_pk_fma_f32 v[166:167], v[120:121], v[120:121], v[166:167]\n"
        "v_pk_fma_f32 v[168:169], v[122:123], v[122:123], v[168:169]\n"
        "v_pk_fma_f32 v[166:167], v[124:125], v[124:125], v[166:167]\n"
        "v_pk_fma_f32 v[168:169], v[126:127], v[126:127], v[168:169]\n"
        "v_pk_fma_f32 v[166:167], v[128:129], v[128:129], v[166:167]\n"
        "v_pk_fma_f32 v[168:169], v[130:131], v[130:131], v[168:169]\n"
        "v_pk_fma_f32 v[166:167], v[132:133], v[132:133], v[166:167]\n"
        "v_pk_fma_f32 v[168:169], v[134:135], v[134:135], v[168:169]\n"
        "v_pk_fma_f32 v[166:167], v[136:137], v[136:137], v[166:167]\n"
        "v_pk_fma_f32 v[168:169], v[138:139], v[138:139], v[168:169]\n"
        "v_pk_add_f32 v[166:167], v[166:167], v[168:169]\n"
        "v_add_f32 v165, v166, v167\n"
        "v_cvt_pk_bf16_f32 v146, v104, v105\n"
        "v_cvt_pk_bf16_f32 v147, v106, v107\n"
        "v_add_f32_dpp v166, v165, v165 row_ror:8 row_mask:0xf bank_mask:0xf\n"
        "v_add_f32_dpp v167, v165, v165 row_ror:8 row_mask:0xf bank_mask:0xf\n"
        "v_cvt_pk_bf16_f32 v148, v108, v109\n"
        "v_cvt_pk_bf16_f32 v149, v110, v111\n"
        "v_permlane16_swap_b32 v166, v167\n"
        "v_add_f32 v165, v166, v167\n"
        "v_add_f32 v166, v166, v167\n"
        "v_cvt_pk_bf16_f32 v150, v112, v113\n"
        "v_cvt_pk_bf16_f32 v151, v114, v115\n"
        "v_permlane32_swap_b32 v165, v166\n"
        "v_add_f32 v165, v165, v166\n"
        "ds_write_b64 v6, v[164:165]\n"
        "v_permlane16_swap_b32 v140, v142\n"
        "v_permlane16_swap_b32 v141, v143\n"
        "v_permlane16_swap_b32 v144, v146\n"
        "v_permlane16_swap_b32 v145, v147\n"
        "s_waitcnt lgkmcnt(0)\n"
        "s_barrier\n"
        "ds_read_b64 v[166:167], v7 offset:0\n"
        "ds_read_b64 v[168:169], v7 offset:64\n"
        "ds_read_b64 v[170:171], v7 offset:128\n"
        "v_permlane16_swap_b32 v148, v150\n"
        "v_permlane16_swap_b32 v149, v151\n"
        "buffer_store_dwordx4 v[140:143], v0, s[40:43], s85 offen sc1\n"
        "buffer_store_dwordx4 v[144:147], v0, s[40:43], s86 offen sc1\n"
        "buffer_store_dwordx4 v[148:151], v0, s[40:43], s87 offen sc1\n"
        "s_waitcnt lgkmcnt(0)\n"
        "v_add_f32 v172, v166, v168\n"
        "v_add_f32 v173, v167, v169\n"
        "v_add_f32 v172, v172, v170\n"
        "v_add_f32 v173, v173, v171\n"
        "v_mul_f32 v172, 0x3eaaaaab, v172\n"
        "v_sub_f32 v166, v166, v172\n"
        "v_sub_f32 v168, v168, v172\n"
        "v_sub_f32 v170, v170, v172\n"
        "v_mul_f32 v166, v166, v166\n"
        "v_fmac_f32 v166, v168, v168\n"
        "v_fmac_f32 v166, v170, v170\n"
        "v_fmamk_f32 v173, v166, 0x43400000, v173\n"
        "v_mul_f32 v173, 0x3ae38e39, v173\n"
        "v_cvt_pk_bf16_f32 v172, v172, v173\n"
        "v_and_b32 v173, 0xffff0000, v172\n"
        "v_lshlrev_b32 v172, 16, v172\n"
        "v_add_f32 v173, 0x3727c5ac, v173\n"
        "v_cvt_pk_bf16_f32 v173, v173, v173\n"
        "v_lshlrev_b32 v173, 16, v173\n"
        "v_sqrt_f32 v173, v173\n"
        "v_sub_f32 v172, v164, v172\n"
        "v_cvt_pk_bf16_f32 v173, v173, v173\n"
        "v_lshlrev_b32 v173, 16, v173\n"
        "v_rcp_f32 v173, v173\n"
        "v_mov_b32 v25, s68\n"
        "v_mul_f32 v172, v172, v173\n"
        "v_pk_fma_f32 v[116:117], v[116:117], v[172:173], v[172:173] op_sel:[0,1,0] op_sel_hi:[1,1,0]\n"
        "v_pk_fma_f32 v[118:119], v[118:119], v[172:173], v[172:173] op_sel:[0,1,0] op_sel_hi:[1,1,0]\n"
        "v_pk_fma_f32 v[120:121], v[120:121], v[172:173], v[172:173] op_sel:[0,1,0] op_sel_hi:[1,1,0]\n"
        "v_pk_fma_f32 v[122:123], v[122:123], v[172:173], v[172:173] op_sel:[0,1,0] op_sel_hi:[1,1,0]\n"
        "v_cvt_pk_bf16_f32 v152, v116, v117\n"
        "v_cvt_pk_bf16_f32 v153, v118, v119\n"
        "v_cvt_pk_bf16_f32 v154, v120, v121\n"
        "v_cvt_pk_bf16_f32 v155, v122, v123\n"
        "s_nop 0\n"
        "v_permlane16_swap_b32 v152, v154\n"
        "v_permlane16_swap_b32 v153, v155\n"
        "buffer_store_dwordx4 v[152:155], v0, s[40:43], s82 offen sc1\n"
        "v_pk_fma_f32 v[124:125], v[124:125], v[172:173], v[172:173] op_sel:[0,1,0] op_sel_hi:[1,1,0]\n"
        "v_pk_fma_f32 v[126:127], v[126:127], v[172:173], v[172:173] op_sel:[0,1,0] op_sel_hi:[1,1,0]\n"
        "v_pk_fma_f32 v[128:129], v[128:129], v[172:173], v[172:173] op_sel:[0,1,0] op_sel_hi:[1,1,0]\n"
        "v_pk_fma_f32 v[130:131], v[130:131], v[172:173], v[172:173] op_sel:[0,1,0] op_sel_hi:[1,1,0]\n"
        "v_cvt_pk_bf16_f32 v156, v124, v125\n"
        "v_cvt_pk_bf16_f32 v157, v126, v127\n"
        "v_cvt_pk_bf16_f32 v158, v128, v129\n"
        "v_cvt_pk_bf16_f32 v159, v130, v131\n"
        "s_nop 0\n"
        "v_permlane16_swap_b32 v156, v158\n"
        "v_permlane16_swap_b32 v157, v159\n"
        "buffer_store_dwordx4 v[156:159], v0, s[40:43], s83 offen sc1\n"
        "v_pk_fma_f32 v[132:133], v[132:133], v[172:173], v[172:173] op_sel:[0,1,0] op_sel_hi:[1,1,0]\n"
        "v_pk_fma_f32 v[134:135], v[134:135], v[172:173], v[172:173] op_sel:[0,1,0] op_sel_hi:[1,1,0]\n"
        "v_pk_fma_f32 v[136:137], v[136:137], v[172:173], v[172:173] op_sel:[0,1,0] op_sel_hi:[1,1,0]\n"
        "v_pk_fma_f32 v[138:139], v[138:139], v[172:173], v[172:173] op_sel:[0,1,0] op_sel_hi:[1,1,0]\n"
        "v_cvt_pk_bf16_f32 v160, v132, v133\n"
        "v_cvt_pk_bf16_f32 v161, v134, v135\n"
        "v_cvt_pk_bf16_f32 v162, v136, v137\n"
        "v_cvt_pk_bf16_f32 v163, v138, v139\n"
        "s_nop 0\n"
        "v_permlane16_swap_b32 v160, v162\n"
        "v_permlane16_swap_b32 v161, v163\n"
        "buffer_store_dwordx4 v[160:163], v0, s[40:43], s84 offen sc1\n"
        "s_waitcnt vmcnt(0)\n"
        "s_barrier\n"
        "buffer_store_dword v25, v2, s[40:43], s70 offen sc1\n"
        "s_add_u32 s68, s68, 1\n"
        "s_cmp_lt_u32 s68, 64\n"
        "s_cbranch_scc1 .Lr3_l1_%=\n"
        "s_endpgm\n"
        ".Lr3_end_%=:\n"
            RED_ARGS : "v0", "v1", "v2", "v3", "v4", "v5", "v6", "v7", "v8", "v9", "v10", "v11", "v12", "v13", "v14", "v15", "v16", "v17", "v18", "v19", "v20", "v21", "v22", "v23", "v24", "v25", "v26", "v27", "v28", "v29", "v30", "v31", "v32", "v33", "v34", "v35", "v36", "v37", "v38", "v39", "v40", "v41", "v42", "v43", "v44", "v45", "v46", "v47", "v48", "v49", "v50", "v51", "v52", "v53", "v54", "v55", "v56", "v57", "v58", "v59", "v60", "v61", "v62", "v63", "v64", "v65", "v66", "v67", "v68", "v69", "v70", "v71", "v72", "v73", "v74", "v75", "v76", "v77", "v78", "v79", "v255", "a255", "s26", "s27", "s28", "s29", "s30", "s31", "s32", "s33", "s34", "s35", "s36", "s37", "s38", "s39", "s40", "s41", "s42", "s43", "s44", "s45", "s46", "s47", "s48", "s49", "s50", "s51", "s52", "s53", "s54", "s55", "s56", "s57", "s58", "s59", "s60", "s61", "s62", "s63", "s64", "s65", "s66", "s67", "s68", "s69", "s70", "s71", "s72", "s73", "s74", "s75", "s76", "s77", "s78", "s79", "s80", "s81", "s82", "s83", "s84", "s85", "s86", "s87", "s88", "s89", "s90", "s91", "s92", "s93", "s94", "s95", "s96", "s97", "s98", "s99", "vcc", "scc", "memory");
    }
    __builtin_unreachable();
}

__global__ __launch_bounds__(256, 1) void mega_kernel(Params p) {
    extern __shared__ __attribute__((aligned(16))) char smem[];
    const rsrc_t rs = __builtin_amdgcn_make_buffer_rsrc(p.ws, 0, WS_USED, 0x00020000);
    const v2u ev = __builtin_amdgcn_raw_buffer_load_b64(rs, G_EPOCH * 128, 0, 16);
    const int roles = role_tab[(threadIdx.x & 3) * 32 + (blockIdx.x >> 3)];
    if (blockIdx.x == 0) st4(rs, threadIdx.x <= 128 || (threadIdx.x >= G_DONE - G_PG && threadIdx.x < G_DONE - G_PG + 18) ? (G_PG + threadIdx.x) * 128 : OOB, -1);
    const u64 epoch = pair(rfl(ev[0]), rfl(ev[1])), tag = (epoch + 0x9e3779b97f4a7c15ull) & ~7ull;
    unsigned* const srot = reinterpret_cast<unsigned*>(smem + CH_IMG);
    if (blockIdx.x == 0) {
        DRAIN();
        if (threadIdx.x == 0) srot[1] = (unsigned)__builtin_amdgcn_s_memrealtime() + TCAL;
        BAR();
        st16(rs, (G_ROT + threadIdx.x) * 128, 0, v4u{(unsigned)tag | my_xcc(), (unsigned)(tag >> 32), srot[1], 0u});
    }
    if (threadIdx.x == 0) {
        unsigned rot = 8, tc = 0;
#pragma unroll 1
        for (unsigned spins = 0; spins < SPIN_CAP; ++spins) {
            const v4u v = ld16(rs, (G_ROT + blockIdx.x) * 128, 0);
            if ((pair(v[0], v[1]) & ~7ull) == tag) { rot = v[0] & 7; tc = v[2]; break; }
            asm volatile("" ::: "memory");
        }
        srot[0] = rot; srot[2] = tc;
    }
    BAR();
    const unsigned rot = rfl(srot[0]), tcal = rfl(srot[2]);
    if (rot == 8) return;
    BAR();
    const unsigned xb = (blockIdx.x + rot) & 7;
    const int x4 = xb >> 2, half = x4, pr = __builtin_amdgcn_readlane(roles, xb & 3), k = pr >> 2, grp = pr & 3, x = x4 * 4 + grp;
    if (k >= (half ? 8 : 16) && k < 28 && !((p.tune2 >> 60) & 1)) {
        const unsigned w = rfl(threadIdx.x >> 6), first = ((half ? pr - 32 : pr + 16) * 4 + w) * 1024;
        warm(p.Wq, 4718592, first, 512); warm(p.x, 2359296, first, 512); warm(p.Wk, 4718592, first, 512); warm(p.Wo, 4718592, first, 512); warm(p.Wv, 4718592, first, 512);
        if (half == 1) { warm(p.w1, 5308416, first, 320); warm(p.w3, 5308416, first, 320); warm(p.w2, 5308416, first, 320); }
        DRAIN();
        BAR();
    }
    auto dl = [&](int r) __attribute__((always_inline)) -> int { return (int)(p.tune2 >> (6 * (r + 5 * half))) & 63; };
    if (k >= 8 && k < 28) {
        const unsigned until = tcal - TCAL + 50 * dl(k < 16 ? 1 : (k < 22 ? 3 : (k < 25 ? 2 : 4)));
        while (until - (unsigned)__builtin_amdgcn_s_memrealtime() < 4000u) __builtin_amdgcn_s_sleep(8);
    }
    if (k < 8) role_chain(p, grp * 8 + k, x4 | dl(0) << 8, tcal, smem);
    else if (k < 16) role_s1(p, grp * 8 + k - 8, x4, pr, 0);
    else if (k < 22) role_f1(p, grp, k - 16, x4, pr, 0);
    else if (k < 25) role_k<true>(p, grp, k - 22, x4, pr, 0);
    else if (k < 28) role_k<false>(p, grp, k - 25, x4, pr, 0);
    else if (k >= 28) {
        startup(rs, ((k - 28) * 8 + x) * 4 + rfl(threadIdx.x >> 6));
        if (k < 30) role_attn(p, grp * 8 + (k - 28) * 4, x4, pr);
        else if (k == 30) role_red<false>(p, grp, x4, pr, epoch);
        else role_red<true>(p, grp, x4, pr, epoch);
    }
}

}

extern "C" void kernel_launch(void* const* d_in, const int* in_sizes, int n_in, void* d_out, int out_size,
                              void* d_ws, size_t ws_size, hipStream_t stream) {
    if (ws_size < (size_t)WS_USED + 2 * PAGE) return;
    Params p;
    p.x = (const u16*)d_in[0]; p.mem0 = (const u16*)d_in[1];
    p.Wq = (const u16*)d_in[2]; p.Wk = (const u16*)d_in[3]; p.Wv = (const u16*)d_in[4]; p.Wo = (const u16*)d_in[5];
    p.w1 = (const u16*)d_in[8]; p.w3 = (const u16*)d_in[9]; p.w2 = (const u16*)d_in[10];
    p.out = (u16*)d_out;
    p.ws = (char*)d_ws + ((0 - (size_t)d_ws) & (PAGE - 1));
    p.tune0 = (u64)D1 | (u64)D2 << 32; p.tune1 = (u64)D3 | (u64)D4 << 32;
    p.tune2 = (u64)DL0 | (u64)DL1 << 30;
    (void)hipFuncSetAttribute((const void*)mega_kernel, hipFuncAttributeMaxDynamicSharedMemorySize, LDS_BYTES);
    mega_kernel<<<256, 256, LDS_BYTES, stream>>>(p);
}
```
